```python
import torch

def _softmax(x, axis=-1):
    return torch.softmax(x, dim=axis)

def _gelu(x):
    return torch.nn.functional.gelu(x, approximate='tanh')

def _silu(x):
    return torch.nn.functional.silu(x)

def _sigmoid(x):
    return torch.sigmoid(x)

def _layernorm(x, axis=-1, eps=1e-05):
    m = x.mean(dim=axis, keepdim=True)
    v = x.var(dim=axis, keepdim=True, unbiased=False)
    return (x - m) / torch.sqrt(v + eps)

def _scatter_add(tgt, idx, src, axis=0):
    return tgt.clone().index_add_(axis, idx, src)

def _segment_sum(src, seg, n):
    out = torch.zeros((n,) + src.shape[1:], dtype=src.dtype)
    return out.index_add_(0, seg, src)

def _pad(x, pw):
    flat = tuple((v for pair in reversed(pw) for v in pair))
    return torch.nn.functional.pad(x, flat)
kernel_name = 'recurrent_scan__memory_aug_recurrent_mem_tr__glu_gated_glu__prompt'

def setup_inputs():

    _w = lambda a: (lambda t: t.to(torch.bfloat16) if t.is_floating_point() else t)(torch.as_tensor(a))

    def _A_setup():
        B, S, Dm = (1, 2048, 1024)
        H, Dh = (32, 128)
        M, seg = (8, 32)
        seg = min(seg, S)
        S = S // seg * seg
        return {'x': _w((lambda *s: torch.randn(*s, dtype=torch.bfloat16) * 0.05)(B, S, Dm)), 'mem0': _w((lambda *s: torch.randn(*s, dtype=torch.bfloat16) * 0.2)(B, M, Dm)), 'Wq': _w((lambda *s: torch.randn(*s, dtype=torch.bfloat16) * 0.8)(Dm, H, Dh) * Dm ** (-0.5)), 'Wk': _w((lambda *s: torch.randn(*s, dtype=torch.bfloat16) * 0.8)(Dm, H, Dh) * Dm ** (-0.5)), 'Wv': _w((lambda *s: torch.randn(*s, dtype=torch.bfloat16) * 0.2)(Dm, H, Dh) * Dm ** (-0.5)), 'Wo': _w((lambda *s: torch.randn(*s, dtype=torch.bfloat16) * 0.2)(H, Dh, Dm) * (H * Dh) ** (-0.5)), 'seg_len': seg}
    _A_in = _A_setup()

    def _B_setup():
        B, S, D, F = (1, 2048, 1024, 3584)
        return {'x': _w((lambda *s: torch.randn(*s, dtype=torch.bfloat16) * 0.05)(B, S, D)), 'w_gate': _w((lambda *s: torch.randn(*s, dtype=torch.bfloat16) * 0.2)(D, F)), 'w_up': _w((lambda *s: torch.randn(*s, dtype=torch.bfloat16) * 0.1)(D, F)), 'w_down': _w((lambda *s: torch.randn(*s, dtype=torch.bfloat16) * 0.1)(F, D))}
    _B_in = _B_setup()
    return {'A_x': _A_in['x'], 'A_mem0': _A_in['mem0'], 'A_Wq': _A_in['Wq'], 'A_Wk': _A_in['Wk'], 'A_Wv': _A_in['Wv'], 'A_Wo': _A_in['Wo'], 'A_seg_len': _A_in['seg_len'], 'B_x': _B_in['x'], 'B_w_gate': _B_in['w_gate'], 'B_w_up': _B_in['w_up'], 'B_w_down': _B_in['w_down']}

def reference(A_x, A_mem0, A_Wq, A_Wk, A_Wv, A_Wo, A_seg_len, B_x, B_w_gate, B_w_up, B_w_down):

    def _A_ref(x, mem0, Wq, Wk, Wv, Wo, seg_len):
        B, S, Dm = x.shape
        H, Dh = (Wq.shape[1], Wq.shape[2])
        M = mem0.shape[1]
        scale = Dh ** (-0.5)
        n_seg = S // seg_len
        L = M + seg_len
        idx = torch.arange(L)
        mask = (idx[:, None] < M) | (idx[:, None] >= idx[None, :])
        mem = mem0
        outs = []
        for s in range(n_seg):
            xs = x[:, s * seg_len:(s + 1) * seg_len, :].clone()
            h = torch.concatenate([mem, xs], axis=1)
            q = torch.einsum('btd,dhe->bhte', h, Wq)
            k = torch.einsum('btd,dhe->bhte', h, Wk)
            v = torch.einsum('btd,dhe->bhte', h, Wv)
            scores = torch.einsum('bhqe,bhke->bhqk', q, k) * scale
            scores = torch.where(mask[None, None], scores, -1000000000.0)
            p = _softmax(scores, axis=-1)
            o = torch.einsum('bhqk,bhke->bhqe', p, v)
            o = torch.einsum('bhte,hed->btd', o, Wo)
            mem = o[:, :M, :].clone()
            outs.append(o[:, M:, :])
        return torch.concatenate(outs, axis=1)

    def _B_ref(x, w_gate, w_up, w_down):
        g = torch.einsum('bsd,df->bsf', x, w_gate)
        u = torch.einsum('bsd,df->bsf', x, w_up)
        h = _sigmoid(g) * u
        return torch.einsum('bsf,fd->bsd', h, w_down)
    x = A_x
    xn = _layernorm(x, axis=-1)
    s = _A_ref(xn, A_mem0, A_Wq, A_Wk, A_Wv, A_Wo, A_seg_len)
    g = _silu(_B_ref(xn, B_w_gate, B_w_up, B_w_down))
    return x + s * g
```

```cpp
#define RGX 1
#define C_O 140
#define C_P 228
#define C_H 60
#define C_D 0
#pragma clang diagnostic ignored "-Wint-to-pointer-cast"
#pragma clang diagnostic ignored "-Winline-asm"
#include <hip/hip_runtime.h>
#include <hip/hip_bf16.h>
#include <stdint.h>

#define LAS __attribute__((address_space(3)))
#define GAS __attribute__((address_space(1)))
typedef unsigned u32;
typedef unsigned char u8;
typedef unsigned short u16;
typedef unsigned long long u64;
typedef u32 v2u __attribute__((ext_vector_type(2)));
typedef u32 v4u __attribute__((ext_vector_type(4)));
typedef float f32x4 __attribute__((ext_vector_type(4)));
typedef __bf16 bf16x2 __attribute__((ext_vector_type(2)));
typedef __amdgpu_buffer_rsrc_t rsrc_t;

constexpr int NSEG = 64, DM = 1024, NH = 32, FF = 3584;
constexpr int MB = 1 << 20;
constexpr int WS_CT = 0;
constexpr int WS_HB = 49152;
constexpr int HB_M = 0, HB_H = 16384, HB_V = 24576, HB_O = 90112, HB_SLOT = 163840;
constexpr int ZERO_BYTES = 49152 + 49152;
constexpr int WS_DBG = 4 * MB;
constexpr int WS_XF = 6 * MB;
constexpr int WS_TK = 10 * MB;
constexpr int WS_OF = 66 * MB;
constexpr int WS_HF = 84 * MB;
constexpr int WS_GT = 100 * MB;
constexpr int WS_END = 104 * MB;
constexpr int TK_STR = 64 * 24576 + 8192, OF_STR = 524288 + 8192;
constexpr int CT_PAR = 0, CT_TMO = 64, CT_TICK = 128;
constexpr int CT_LN = 1024;
constexpr int CT_P1 = 2048;
constexpr int CT_G1 = 4096;
constexpr int CT_AT = 3072;
constexpr int CT_S = 8192;
#define S64_NF 13
constexpr int OOB = 0x7ffffff0;
constexpr int AUX_SC1 = 16;

struct Params {
    const u16 *x, *mem0, *wq, *wk, *wv, *wo, *wg, *wu, *wd;
    u16* out;
    u8* ws;
};
__device__ __forceinline__ u32 pack2(float a, float b) { bf16x2 v = {(__bf16)a, (__bf16)b}; return __builtin_bit_cast(u32, v); }
__device__ __forceinline__ float bflo(u32 d) { return __builtin_bit_cast(float, d << 16); }
__device__ __forceinline__ float bfhi(u32 d) { return __builtin_bit_cast(float, d & 0xFFFF0000u); }
__device__ __forceinline__ float rbf(float a) { return (float)(__bf16)a; }
__device__ __forceinline__ void st128s(v4u v, rsrc_t rs, int voff) { __builtin_amdgcn_raw_buffer_store_b128(v, rs, voff, 0, AUX_SC1); }
__device__ __forceinline__ void st32s(u32 v, rsrc_t rs, int voff) { __builtin_amdgcn_raw_buffer_store_b32(v, rs, voff, 0, AUX_SC1); }
__device__ __forceinline__ u32 ld32s(rsrc_t rs, int voff) { return __builtin_amdgcn_raw_buffer_load_b32(rs, voff, 0, AUX_SC1); }
__device__ __forceinline__ u32 xcc_id() { return (u32)__builtin_amdgcn_s_getreg(20 | (3 << 11)) & 0xfu; }
__device__ __forceinline__ int rfl(int x) { return __builtin_amdgcn_readfirstlane(x); }
__device__ __forceinline__ v4u mksrd(const void* p, u32 bytes) {
    const u64 a = (u64)p;
    return v4u{(u32)rfl((u32)a), (u32)rfl((u32)(a >> 32) & 0xFFFFu), (u32)rfl(bytes), 0x00020000u};
}
#define CFENCE() asm volatile("" ::: "memory")
#define WAIT_V0() asm volatile("s_waitcnt vmcnt(0)" ::: "memory")
#define BAR() do { asm volatile("s_waitcnt lgkmcnt(0)" ::: "memory"); __builtin_amdgcn_s_barrier(); asm volatile("" ::: "memory"); } while (0)
#define RLXA __ATOMIC_RELAXED, __HIP_MEMORY_SCOPE_AGENT
__device__ __forceinline__ int amap(int off, int par) { return WS_HB + ((off >> 13) << 14) + (off & 8191) + (par << 13); }
#define ASM_P1_QK \
" s_nop 7\n" \
" s_nop 7\n" \
" s_waitcnt vmcnt(0) lgkmcnt(0)\n" \
" s_lshr_b32 s52, s48, 1\n" \
" s_lshl_b32 s52, s52, 13\n" \
" s_and_b32 s53, s48, 1\n" \
" s_lshl_b32 s53, s53, 10\n" \
" s_add_u32 s52, s52, s53\n" \
" s_mov_b32 s54, 0\n" \
" s_lshl_b32 s54, s44, 5\n" \
" v_add_u32 v8, 0x10000, v2\n" \
" v_add_u32 v9, 0x10000, v3\n" \
" s_barrier\n" \
" ds_read_b64_tr_b16 a[0:1], v2 offset:0\n" \
" ds_read_b64_tr_b16 a[2:3], v2 offset:512\n" \
" ds_read_b64_tr_b16 a[128:129], v3 offset:0\n" \
" ds_read_b64_tr_b16 a[130:131], v3 offset:512\n" \
" ds_read_b64_tr_b16 a[4:5], v2 offset:4096\n" \
" ds_read_b64_tr_b16 a[6:7], v2 offset:4608\n" \
" ds_read_b64_tr_b16 a[132:133], v3 offset:4096\n" \
" ds_read_b64_tr_b16 a[134:135], v3 offset:4608\n" \
" s_waitcnt lgkmcnt(0)\n" \
" s_barrier\n" \
" s_add_u32 m0, s52, 0\n" \
" s_nop 0\n" \
" buffer_load_dwordx4 v1, s[40:43], s54 offen nt lds\n" \
" s_add_u32 s54, s54, s44\n" \
" s_add_u32 m0, s52, 2048\n" \
" s_nop 0\n" \
" buffer_load_dwordx4 v1, s[40:43], s54 offen nt lds\n" \
" s_add_u32 s54, s54, s44\n" \
" s_add_u32 m0, s52, 4096\n" \
" s_nop 0\n" \
" buffer_load_dwordx4 v1, s[40:43], s54 offen nt lds\n" \
" s_add_u32 s54, s54, s44\n" \
" s_add_u32 m0, s52, 6144\n" \
" s_nop 0\n" \
" buffer_load_dwordx4 v1, s[40:43], s54 offen nt lds\n" \
" s_add_u32 s54, s54, s44\n" \
" s_barrier\n" \
" ds_read_b64_tr_b16 a[8:9], v2 offset:16384\n" \
" ds_read_b64_tr_b16 a[10:11], v2 offset:16896\n" \
" ds_read_b64_tr_b16 a[136:137], v3 offset:16384\n" \
" ds_read_b64_tr_b16 a[138:139], v3 offset:16896\n" \
" ds_read_b64_tr_b16 a[12:13], v2 offset:20480\n" \
" ds_read_b64_tr_b16 a[14:15], v2 offset:20992\n" \
" ds_read_b64_tr_b16 a[140:141], v3 offset:20480\n" \
" ds_read_b64_tr_b16 a[142:143], v3 offset:20992\n" \
" s_waitcnt lgkmcnt(0)\n" \
" s_barrier\n" \
" s_add_u32 m0, s52, 16384\n" \
" s_nop 0\n" \
" buffer_load_dwordx4 v1, s[40:43], s54 offen nt lds\n" \
" s_add_u32 s54, s54, s44\n" \
" s_add_u32 m0, s52, 18432\n" \
" s_nop 0\n" \
" buffer_load_dwordx4 v1, s[40:43], s54 offen nt lds\n" \
" s_add_u32 s54, s54, s44\n" \
" s_add_u32 m0, s52, 20480\n" \
" s_nop 0\n" \
" buffer_load_dwordx4 v1, s[40:43], s54 offen nt lds\n" \
" s_add_u32 s54, s54, s44\n" \
" s_add_u32 m0, s52, 22528\n" \
" s_nop 0\n" \
" buffer_load_dwordx4 v1, s[40:43], s54 offen nt lds\n" \
" s_add_u32 s54, s54, s44\n" \
" s_barrier\n" \
" ds_read_b64_tr_b16 a[16:17], v2 offset:32768\n" \
" ds_read_b64_tr_b16 a[18:19], v2 offset:33280\n" \
" ds_read_b64_tr_b16 a[144:145], v3 offset:32768\n" \
" ds_read_b64_tr_b16 a[146:147], v3 offset:33280\n" \
" ds_read_b64_tr_b16 a[20:21], v2 offset:36864\n" \
" ds_read_b64_tr_b16 a[22:23], v2 offset:37376\n" \
" ds_read_b64_tr_b16 a[148:149], v3 offset:36864\n" \
" ds_read_b64_tr_b16 a[150:151], v3 offset:37376\n" \
" s_waitcnt lgkmcnt(0)\n" \
" s_barrier\n" \
" s_add_u32 m0, s52, 32768\n" \
" s_nop 0\n" \
" buffer_load_dwordx4 v1, s[40:43], s54 offen nt lds\n" \
" s_add_u32 s54, s54, s44\n" \
" s_add_u32 m0, s52, 34816\n" \
" s_nop 0\n" \
" buffer_load_dwordx4 v1, s[40:43], s54 offen nt lds\n" \
" s_add_u32 s54, s54, s44\n" \
" s_add_u32 m0, s52, 36864\n" \
" s_nop 0\n" \
" buffer_load_dwordx4 v1, s[40:43], s54 offen nt lds\n" \
" s_add_u32 s54, s54, s44\n" \
" s_add_u32 m0, s52, 38912\n" \
" s_nop 0\n" \
" buffer_load_dwordx4 v1, s[40:43], s54 offen nt lds\n" \
" s_add_u32 s54, s54, s44\n" \
" s_barrier\n" \
" ds_read_b64_tr_b16 a[24:25], v2 offset:49152\n" \
" ds_read_b64_tr_b16 a[26:27], v2 offset:49664\n" \
" ds_read_b64_tr_b16 a[152:153], v3 offset:49152\n" \
" ds_read_b64_tr_b16 a[154:155], v3 offset:49664\n" \
" ds_read_b64_tr_b16 a[28:29], v2 offset:53248\n" \
" ds_read_b64_tr_b16 a[30:31], v2 offset:53760\n" \
" ds_read_b64_tr_b16 a[156:157], v3 offset:53248\n" \
" ds_read_b64_tr_b16 a[158:159], v3 offset:53760\n" \
" s_waitcnt lgkmcnt(0)\n" \
" s_barrier\n" \
" s_add_u32 m0, s52, 49152\n" \
" s_nop 0\n" \
" buffer_load_dwordx4 v1, s[40:43], s54 offen nt lds\n" \
" s_add_u32 s54, s54, s44\n" \
" s_add_u32 m0, s52, 51200\n" \
" s_nop 0\n" \
" buffer_load_dwordx4 v1, s[40:43], s54 offen nt lds\n" \
" s_add_u32 s54, s54, s44\n" \
" s_add_u32 m0, s52, 53248\n" \
" s_nop 0\n" \
" buffer_load_dwordx4 v1, s[40:43], s54 offen nt lds\n" \
" s_add_u32 s54, s54, s44\n" \
" s_add_u32 m0, s52, 55296\n" \
" s_nop 0\n" \
" buffer_load_dwordx4 v1, s[40:43], s54 offen nt lds\n" \
" s_add_u32 s54, s54, s44\n" \
" s_barrier\n" \
" ds_read_b64_tr_b16 a[32:33], v8 offset:0\n" \
" ds_read_b64_tr_b16 a[34:35], v8 offset:512\n" \
" ds_read_b64_tr_b16 a[160:161], v9 offset:0\n" \
" ds_read_b64_tr_b16 a[162:163], v9 offset:512\n" \
" ds_read_b64_tr_b16 a[36:37], v8 offset:4096\n" \
" ds_read_b64_tr_b16 a[38:39], v8 offset:4608\n" \
" ds_read_b64_tr_b16 a[164:165], v9 offset:4096\n" \
" ds_read_b64_tr_b16 a[166:167], v9 offset:4608\n" \
" s_waitcnt lgkmcnt(0)\n" \
" s_barrier\n" \
" s_add_u32 m0, s52, 65536\n" \
" s_nop 0\n" \
" buffer_load_dwordx4 v1, s[40:43], s54 offen nt lds\n" \
" s_add_u32 s54, s54, s44\n" \
" s_add_u32 m0, s52, 67584\n" \
" s_nop 0\n" \
" buffer_load_dwordx4 v1, s[40:43], s54 offen nt lds\n" \
" s_add_u32 s54, s54, s44\n" \
" s_add_u32 m0, s52, 69632\n" \
" s_nop 0\n" \
" buffer_load_dwordx4 v1, s[40:43], s54 offen nt lds\n" \
" s_add_u32 s54, s54, s44\n" \
" s_add_u32 m0, s52, 71680\n" \
" s_nop 0\n" \
" buffer_load_dwordx4 v1, s[40:43], s54 offen nt lds\n" \
" s_add_u32 s54, s54, s44\n" \
" s_barrier\n" \
" ds_read_b64_tr_b16 a[40:41], v8 offset:16384\n" \
" ds_read_b64_tr_b16 a[42:43], v8 offset:16896\n" \
" ds_read_b64_tr_b16 a[168:169], v9 offset:16384\n" \
" ds_read_b64_tr_b16 a[170:171], v9 offset:16896\n" \
" ds_read_b64_tr_b16 a[44:45], v8 offset:20480\n" \
" ds_read_b64_tr_b16 a[46:47], v8 offset:20992\n" \
" ds_read_b64_tr_b16 a[172:173], v9 offset:20480\n" \
" ds_read_b64_tr_b16 a[174:175], v9 offset:20992\n" \
" s_waitcnt lgkmcnt(0)\n" \
" s_barrier\n" \
" s_add_u32 m0, s52, 81920\n" \
" s_nop 0\n" \
" buffer_load_dwordx4 v1, s[40:43], s54 offen nt lds\n" \
" s_add_u32 s54, s54, s44\n" \
" s_add_u32 m0, s52, 83968\n" \
" s_nop 0\n" \
" buffer_load_dwordx4 v1, s[40:43], s54 offen nt lds\n" \
" s_add_u32 s54, s54, s44\n" \
" s_add_u32 m0, s52, 86016\n" \
" s_nop 0\n" \
" buffer_load_dwordx4 v1, s[40:43], s54 offen nt lds\n" \
" s_add_u32 s54, s54, s44\n" \
" s_add_u32 m0, s52, 88064\n" \
" s_nop 0\n" \
" buffer_load_dwordx4 v1, s[40:43], s54 offen nt lds\n" \
" s_add_u32 s54, s54, s44\n" \
" s_barrier\n" \
" ds_read_b64_tr_b16 a[48:49], v8 offset:32768\n" \
" ds_read_b64_tr_b16 a[50:51], v8 offset:33280\n" \
" ds_read_b64_tr_b16 a[176:177], v9 offset:32768\n" \
" ds_read_b64_tr_b16 a[178:179], v9 offset:33280\n" \
" ds_read_b64_tr_b16 a[52:53], v8 offset:36864\n" \
" ds_read_b64_tr_b16 a[54:55], v8 offset:37376\n" \
" ds_read_b64_tr_b16 a[180:181], v9 offset:36864\n" \
" ds_read_b64_tr_b16 a[182:183], v9 offset:37376\n" \
" s_waitcnt lgkmcnt(0)\n" \
" s_barrier\n" \
" s_add_u32 m0, s52, 98304\n" \
" s_nop 0\n" \
" buffer_load_dwordx4 v1, s[40:43], s54 offen nt lds\n" \
" s_add_u32 s54, s54, s44\n" \
" s_add_u32 m0, s52, 100352\n" \
" s_nop 0\n" \
" buffer_load_dwordx4 v1, s[40:43], s54 offen nt lds\n" \
" s_add_u32 s54, s54, s44\n" \
" s_add_u32 m0, s52, 102400\n" \
" s_nop 0\n" \
" buffer_load_dwordx4 v1, s[40:43], s54 offen nt lds\n" \
" s_add_u32 s54, s54, s44\n" \
" s_add_u32 m0, s52, 104448\n" \
" s_nop 0\n" \
" buffer_load_dwordx4 v1, s[40:43], s54 offen nt lds\n" \
" s_add_u32 s54, s54, s44\n" \
" s_barrier\n" \
" ds_read_b64_tr_b16 a[56:57], v8 offset:49152\n" \
" ds_read_b64_tr_b16 a[58:59], v8 offset:49664\n" \
" ds_read_b64_tr_b16 a[184:185], v9 offset:49152\n" \
" ds_read_b64_tr_b16 a[186:187], v9 offset:49664\n" \
" ds_read_b64_tr_b16 a[60:61], v8 offset:53248\n" \
" ds_read_b64_tr_b16 a[62:63], v8 offset:53760\n" \
" ds_read_b64_tr_b16 a[188:189], v9 offset:53248\n" \
" ds_read_b64_tr_b16 a[190:191], v9 offset:53760\n" \
" s_waitcnt lgkmcnt(0)\n" \
" s_barrier\n" \
" s_add_u32 m0, s52, 114688\n" \
" s_nop 0\n" \
" buffer_load_dwordx4 v1, s[40:43], s54 offen nt lds\n" \
" s_add_u32 s54, s54, s44\n" \
" s_add_u32 m0, s52, 116736\n" \
" s_nop 0\n" \
" buffer_load_dwordx4 v1, s[40:43], s54 offen nt lds\n" \
" s_add_u32 s54, s54, s44\n" \
" s_add_u32 m0, s52, 118784\n" \
" s_nop 0\n" \
" buffer_load_dwordx4 v1, s[40:43], s54 offen nt lds\n" \
" s_add_u32 s54, s54, s44\n" \
" s_add_u32 m0, s52, 120832\n" \
" s_nop 0\n" \
" buffer_load_dwordx4 v1, s[40:43], s54 offen nt lds\n" \
" s_add_u32 s54, s54, s44\n" \
" s_waitcnt vmcnt(28)\n" \
" s_barrier\n" \
" ds_read_b64_tr_b16 a[64:65], v2 offset:0\n" \
" ds_read_b64_tr_b16 a[66:67], v2 offset:512\n" \
" ds_read_b64_tr_b16 a[192:193], v3 offset:0\n" \
" ds_read_b64_tr_b16 a[194:195], v3 offset:512\n" \
" ds_read_b64_tr_b16 a[68:69], v2 offset:4096\n" \
" ds_read_b64_tr_b16 a[70:71], v2 offset:4608\n" \
" ds_read_b64_tr_b16 a[196:197], v3 offset:4096\n" \
" ds_read_b64_tr_b16 a[198:199], v3 offset:4608\n" \
" s_waitcnt vmcnt(24)\n" \
" s_barrier\n" \
" ds_read_b64_tr_b16 a[72:73], v2 offset:16384\n" \
" ds_read_b64_tr_b16 a[74:75], v2 offset:16896\n" \
" ds_read_b64_tr_b16 a[200:201], v3 offset:16384\n" \
" ds_read_b64_tr_b16 a[202:203], v3 offset:16896\n" \
" ds_read_b64_tr_b16 a[76:77], v2 offset:20480\n" \
" ds_read_b64_tr_b16 a[78:79], v2 offset:20992\n" \
" ds_read_b64_tr_b16 a[204:205], v3 offset:20480\n" \
" ds_read_b64_tr_b16 a[206:207], v3 offset:20992\n" \
" s_waitcnt vmcnt(20)\n" \
" s_barrier\n" \
" ds_read_b64_tr_b16 a[80:81], v2 offset:32768\n" \
" ds_read_b64_tr_b16 a[82:83], v2 offset:33280\n" \
" ds_read_b64_tr_b16 a[208:209], v3 offset:32768\n" \
" ds_read_b64_tr_b16 a[210:211], v3 offset:33280\n" \
" ds_read_b64_tr_b16 a[84:85], v2 offset:36864\n" \
" ds_read_b64_tr_b16 a[86:87], v2 offset:37376\n" \
" ds_read_b64_tr_b16 a[212:213], v3 offset:36864\n" \
" ds_read_b64_tr_b16 a[214:215], v3 offset:37376\n" \
" s_waitcnt vmcnt(16)\n" \
" s_barrier\n" \
" ds_read_b64_tr_b16 a[88:89], v2 offset:49152\n" \
" ds_read_b64_tr_b16 a[90:91], v2 offset:49664\n" \
" ds_read_b64_tr_b16 a[216:217], v3 offset:49152\n" \
" ds_read_b64_tr_b16 a[218:219], v3 offset:49664\n" \
" ds_read_b64_tr_b16 a[92:93], v2 offset:53248\n" \
" ds_read_b64_tr_b16 a[94:95], v2 offset:53760\n" \
" ds_read_b64_tr_b16 a[220:221], v3 offset:53248\n" \
" ds_read_b64_tr_b16 a[222:223], v3 offset:53760\n" \
" s_waitcnt vmcnt(12)\n" \
" s_barrier\n" \
" ds_read_b64_tr_b16 a[96:97], v8 offset:0\n" \
" ds_read_b64_tr_b16 a[98:99], v8 offset:512\n" \
" ds_read_b64_tr_b16 a[224:225], v9 offset:0\n" \
" ds_read_b64_tr_b16 a[226:227], v9 offset:512\n" \
" ds_read_b64_tr_b16 a[100:101], v8 offset:4096\n" \
" ds_read_b64_tr_b16 a[102:103], v8 offset:4608\n" \
" ds_read_b64_tr_b16 a[228:229], v9 offset:4096\n" \
" ds_read_b64_tr_b16 a[230:231], v9 offset:4608\n" \
" s_waitcnt vmcnt(8)\n" \
" s_barrier\n" \
" ds_read_b64_tr_b16 a[104:105], v8 offset:16384\n" \
" ds_read_b64_tr_b16 a[106:107], v8 offset:16896\n" \
" ds_read_b64_tr_b16 a[232:233], v9 offset:16384\n" \
" ds_read_b64_tr_b16 a[234:235], v9 offset:16896\n" \
" ds_read_b64_tr_b16 a[108:109], v8 offset:20480\n" \
" ds_read_b64_tr_b16 a[110:111], v8 offset:20992\n" \
" ds_read_b64_tr_b16 a[236:237], v9 offset:20480\n" \
" ds_read_b64_tr_b16 a[238:239], v9 offset:20992\n" \
" s_waitcnt vmcnt(4)\n" \
" s_barrier\n" \
" ds_read_b64_tr_b16 a[112:113], v8 offset:32768\n" \
" ds_read_b64_tr_b16 a[114:115], v8 offset:33280\n" \
" ds_read_b64_tr_b16 a[240:241], v9 offset:32768\n" \
" ds_read_b64_tr_b16 a[242:243], v9 offset:33280\n" \
" ds_read_b64_tr_b16 a[116:117], v8 offset:36864\n" \
" ds_read_b64_tr_b16 a[118:119], v8 offset:37376\n" \
" ds_read_b64_tr_b16 a[244:245], v9 offset:36864\n" \
" ds_read_b64_tr_b16 a[246:247], v9 offset:37376\n" \
" s_waitcnt vmcnt(0)\n" \
" s_barrier\n" \
" ds_read_b64_tr_b16 a[120:121], v8 offset:49152\n" \
" ds_read_b64_tr_b16 a[122:123], v8 offset:49664\n" \
" ds_read_b64_tr_b16 a[248:249], v9 offset:49152\n" \
" ds_read_b64_tr_b16 a[250:251], v9 offset:49664\n" \
" ds_read_b64_tr_b16 a[124:125], v8 offset:53248\n" \
" ds_read_b64_tr_b16 a[126:127], v8 offset:53760\n" \
" ds_read_b64_tr_b16 a[252:253], v9 offset:53248\n" \
" ds_read_b64_tr_b16 a[254:255], v9 offset:53760\n" \
" s_waitcnt lgkmcnt(0)\n" \
" s_barrier\n" \
" v_and_b32 v14, 48, v0\n" \
" v_lshlrev_b32 v14, 3, v14\n" \
" v_add_u32 v14, 4096, v14\n" \
" s_cmp_eq_u32 s46, 0\n" \
" s_cbranch_scc1 Lp1qk_l0\n" \
" s_branch Lp1qk_l1\n" \
"Lp1qk_lk:\n" \
" s_lshl_b32 s55, s48, 13\n" \
" s_lshl_b32 s56, s46, 16\n" \
" s_lshl_b32 s60, s47, 16\n" \
" s_sub_u32 s60, s60, 0x10000\n" \
" s_mul_i32 s57, s46, s49\n" \
" s_sub_u32 s57, s57, s49\n" \
" s_add_u32 s58, s57, s50\n" \
" v_mov_b32 v7, v5\n" \
" v_mov_b32 v5, 0x7ffffff0\n" \
" v_mov_b32 v10, v0\n" \
" v_mov_b32 v11, v6\n" \
" v_mov_b32 v6, 0x7ffffff0\n" \
" s_mov_b32 s59, s46\n" \
" s_min_u32 s61, s56, s60\n" \
" s_add_u32 m0, s55, 0\n" \
" s_nop 0\n" \
" buffer_load_dwordx4 v4, s[36:39], s61 offen offset:0 sc1 lds\n" \
" buffer_load_dwordx4 v4, s[36:39], s61 offen offset:1024 sc1 lds\n" \
" buffer_load_dwordx4 v4, s[36:39], s61 offen offset:2048 sc1 lds\n" \
" buffer_load_dwordx4 v4, s[36:39], s61 offen offset:3072 sc1 lds\n" \
" s_add_u32 m0, s55, 4096\n" \
" s_add_u32 s61, s61, 4096\n" \
" buffer_load_dwordx4 v4, s[36:39], s61 offen offset:0 sc1 lds\n" \
" buffer_load_dwordx4 v4, s[36:39], s61 offen offset:1024 sc1 lds\n" \
" buffer_load_dwordx4 v4, s[36:39], s61 offen offset:2048 sc1 lds\n" \
" buffer_load_dwordx4 v4, s[36:39], s61 offen offset:3072 sc1 lds\n" \
" s_min_u32 s61, s56, s60\n" \
" s_add_u32 s61, s61, 16384\n" \
" s_add_u32 s56, s56, 0x10000\n" \
" s_add_u32 m0, s55, 32768\n" \
" s_nop 0\n" \
" buffer_load_dwordx4 v4, s[36:39], s61 offen offset:0 sc1 lds\n" \
" buffer_load_dwordx4 v4, s[36:39], s61 offen offset:1024 sc1 lds\n" \
" buffer_load_dwordx4 v4, s[36:39], s61 offen offset:2048 sc1 lds\n" \
" buffer_load_dwordx4 v4, s[36:39], s61 offen offset:3072 sc1 lds\n" \
" s_add_u32 m0, s55, 36864\n" \
" s_add_u32 s61, s61, 4096\n" \
" buffer_load_dwordx4 v4, s[36:39], s61 offen offset:0 sc1 lds\n" \
" buffer_load_dwordx4 v4, s[36:39], s61 offen offset:1024 sc1 lds\n" \
" buffer_load_dwordx4 v4, s[36:39], s61 offen offset:2048 sc1 lds\n" \
" buffer_load_dwordx4 v4, s[36:39], s61 offen offset:3072 sc1 lds\n" \
" s_mov_b32 s70, 0\n" \
" s_mov_b32 s72, 0\n" \
" s_waitcnt vmcnt(0) lgkmcnt(0)\n" \
" s_barrier\n" \
" v_add_u32 v12, 0x10000, v10\n" \
" ds_read_b128 v[16:19], v10 offset:0\n" \
" ds_read_b128 v[20:23], v10 offset:16384\n" \
" ds_read_b128 v[24:27], v10 offset:1024\n" \
" ds_read_b128 v[28:31], v10 offset:17408\n" \
" ds_read_b128 v[32:35], v10 offset:2048\n" \
" ds_read_b128 v[36:39], v10 offset:18432\n" \
" s_waitcnt lgkmcnt(4)\n" \
" s_nop 7\n" \
" s_nop 7\n" \
".p2align 6\n" \
"Lp1qk_loop:\n" \
" v_mfma_f32_16x16x32_bf16 v[48:51], a[0:3], v[16:19], 0\n" \
" s_min_u32 s61, s56, s60\n" \
" s_add_u32 m0, s55, 65536\n" \
" s_nop 0\n" \
" buffer_load_dwordx4 v4, s[36:39], s61 offen offset:0 sc1 lds\n" \
" v_mov_b32_e64 v13, s59\n" \
" v_mfma_f32_16x16x32_bf16 v[52:55], a[0:3], v[20:23], 0\n" \
" ds_read_b128 v[40:43], v10 offset:3072\n" \
" s_nop 0\n" \
" v_mfma_f32_16x16x32_bf16 v[56:59], a[128:131], v[16:19], 0\n" \
" ds_read_b128 v[44:47], v10 offset:19456\n" \
" s_nop 0\n" \
" v_mfma_f32_16x16x32_bf16 v[60:63], a[128:131], v[20:23], 0\n" \
" s_waitcnt lgkmcnt(4)\n" \
" v_mul_f32 v64, s51, v64\n" \
" v_mul_f32_e64 v65, s51, v65\n" \
" v_mfma_f32_16x16x32_bf16 v[48:51], a[4:7], v[24:27], v[48:51]\n" \
" buffer_load_dwordx4 v4, s[36:39], s61 offen offset:1024 sc1 lds\n" \
" v_mfma_f32_16x16x32_bf16 v[52:55], a[4:7], v[28:31], v[52:55]\n" \
" ds_read_b128 v[16:19], v10 offset:4096\n" \
" v_mul_f32_e64 v66, s51, v66\n" \
" v_mfma_f32_16x16x32_bf16 v[56:59], a[132:135], v[24:27], v[56:59]\n" \
" ds_read_b128 v[20:23], v10 offset:20480\n" \
" v_mul_f32_e64 v67, s51, v67\n" \
" v_mfma_f32_16x16x32_bf16 v[60:63], a[132:135], v[28:31], v[60:63]\n" \
" s_waitcnt lgkmcnt(4)\n" \
" v_mul_f32 v72, s51, v72\n" \
" v_mul_f32_e64 v73, s51, v73\n" \
" v_mfma_f32_16x16x32_bf16 v[48:51], a[8:11], v[32:35], v[48:51]\n" \
" v_mul_f32 v74, s51, v74\n" \
" v_mul_f32 v75, s51, v75\n" \
" v_mfma_f32_16x16x32_bf16 v[52:55], a[8:11], v[36:39], v[52:55]\n" \
" ds_read_b128 v[24:27], v10 offset:5120\n" \
" v_cvt_pk_bf16_f32 v80, v64, v65\n" \
" v_mfma_f32_16x16x32_bf16 v[56:59], a[136:139], v[32:35], v[56:59]\n" \
" ds_read_b128 v[28:31], v10 offset:21504\n" \
" v_cvt_pk_bf16_f32 v81, v66, v67\n" \
" v_mfma_f32_16x16x32_bf16 v[60:63], a[136:139], v[36:39], v[60:63]\n" \
" s_waitcnt lgkmcnt(4)\n" \
" v_cvt_pk_bf16_f32 v82, v72, v73\n" \
" v_cvt_pk_bf16_f32 v83, v74, v75\n" \
" v_mfma_f32_16x16x32_bf16 v[48:51], a[12:15], v[40:43], v[48:51]\n" \
" buffer_load_dwordx4 v4, s[36:39], s61 offen offset:2048 sc1 lds\n" \
" v_mfma_f32_16x16x32_bf16 v[52:55], a[12:15], v[44:47], v[52:55]\n" \
" ds_read_b128 v[32:35], v10 offset:6144\n" \
" buffer_store_dwordx4 v[80:83], v5, s[36:39], s57 offen sc1\n" \
" v_mfma_f32_16x16x32_bf16 v[56:59], a[140:143], v[40:43], v[56:59]\n" \
" ds_read_b128 v[36:39], v10 offset:22528\n" \
" v_mul_f32 v68, s51, v68\n" \
" v_mfma_f32_16x16x32_bf16 v[60:63], a[140:143], v[44:47], v[60:63]\n" \
" s_waitcnt lgkmcnt(4)\n" \
" v_mul_f32 v69, s51, v69\n" \
" v_mul_f32_e64 v70, s51, v70\n" \
" v_mfma_f32_16x16x32_bf16 v[48:51], a[16:19], v[16:19], v[48:51]\n" \
" buffer_load_dwordx4 v4, s[36:39], s61 offen offset:3072 sc1 lds\n" \
" v_mfma_f32_16x16x32_bf16 v[52:55], a[16:19], v[20:23], v[52:55]\n" \
" ds_read_b128 v[40:43], v10 offset:7168\n" \
" v_mul_f32_e64 v71, s51, v71\n" \
" v_mfma_f32_16x16x32_bf16 v[56:59], a[144:147], v[16:19], v[56:59]\n" \
" ds_read_b128 v[44:47], v10 offset:23552\n" \
" v_mul_f32_e64 v76, s51, v76\n" \
" v_mfma_f32_16x16x32_bf16 v[60:63], a[144:147], v[20:23], v[60:63]\n" \
" s_waitcnt lgkmcnt(4)\n" \
" v_mul_f32 v77, s51, v77\n" \
" v_mul_f32_e64 v78, s51, v78\n" \
" v_mfma_f32_16x16x32_bf16 v[48:51], a[20:23], v[24:27], v[48:51]\n" \
" v_mul_f32_e64 v79, s51, v79\n" \
" v_cvt_pk_bf16_f32 v84, v68, v69\n" \
" v_mfma_f32_16x16x32_bf16 v[52:55], a[20:23], v[28:31], v[52:55]\n" \
" ds_read_b128 v[16:19], v10 offset:8192\n" \
" v_cvt_pk_bf16_f32 v85, v70, v71\n" \
" v_mfma_f32_16x16x32_bf16 v[56:59], a[148:151], v[24:27], v[56:59]\n" \
" ds_read_b128 v[20:23], v10 offset:24576\n" \
" v_cvt_pk_bf16_f32 v86, v76, v77\n" \
" v_mfma_f32_16x16x32_bf16 v[60:63], a[148:151], v[28:31], v[60:63]\n" \
" s_waitcnt lgkmcnt(4)\n" \
" v_cvt_pk_bf16_f32 v87, v78, v79\n" \
" buffer_store_dwordx4 v[84:87], v5, s[36:39], s58 offen sc1\n" \
" v_mfma_f32_16x16x32_bf16 v[48:51], a[24:27], v[32:35], v[48:51]\n" \
" s_add_u32 m0, s55, 69632\n" \
" s_add_u32 s61, s61, 4096\n" \
" buffer_load_dwordx4 v4, s[36:39], s61 offen offset:0 sc1 lds\n" \
" v_mfma_f32_16x16x32_bf16 v[52:55], a[24:27], v[36:39], v[52:55]\n" \
" ds_read_b128 v[24:27], v10 offset:9216\n" \
" v_mfma_f32_16x16x32_bf16 v[56:59], a[152:155], v[32:35], v[56:59]\n" \
" ds_read_b128 v[28:31], v10 offset:25600\n" \
" v_mfma_f32_16x16x32_bf16 v[60:63], a[152:155], v[36:39], v[60:63]\n" \
" s_waitcnt lgkmcnt(4)\n" \
" v_mfma_f32_16x16x32_bf16 v[48:51], a[28:31], v[40:43], v[48:51]\n" \
" buffer_load_dwordx4 v4, s[36:39], s61 offen offset:1024 sc1 lds\n" \
" v_mfma_f32_16x16x32_bf16 v[52:55], a[28:31], v[44:47], v[52:55]\n" \
" ds_read_b128 v[32:35], v10 offset:10240\n" \
" v_mfma_f32_16x16x32_bf16 v[56:59], a[156:159], v[40:43], v[56:59]\n" \
" ds_read_b128 v[36:39], v10 offset:26624\n" \
" v_mfma_f32_16x16x32_bf16 v[60:63], a[156:159], v[44:47], v[60:63]\n" \
" s_waitcnt lgkmcnt(4)\n" \
" v_mfma_f32_16x16x32_bf16 v[48:51], a[32:35], v[16:19], v[48:51]\n" \
" v_mfma_f32_16x16x32_bf16 v[52:55], a[32:35], v[20:23], v[52:55]\n" \
" ds_read_b128 v[40:43], v10 offset:11264\n" \
" v_mfma_f32_16x16x32_bf16 v[56:59], a[160:163], v[16:19], v[56:59]\n" \
" ds_read_b128 v[44:47], v10 offset:27648\n" \
" v_mfma_f32_16x16x32_bf16 v[60:63], a[160:163], v[20:23], v[60:63]\n" \
" s_waitcnt lgkmcnt(4)\n" \
" v_mfma_f32_16x16x32_bf16 v[48:51], a[36:39], v[24:27], v[48:51]\n" \
" buffer_load_dwordx4 v4, s[36:39], s61 offen offset:2048 sc1 lds\n" \
" v_mfma_f32_16x16x32_bf16 v[52:55], a[36:39], v[28:31], v[52:55]\n" \
" ds_read_b128 v[16:19], v10 offset:12288\n" \
" v_mfma_f32_16x16x32_bf16 v[56:59], a[164:167], v[24:27], v[56:59]\n" \
" ds_read_b128 v[20:23], v10 offset:28672\n" \
" v_mfma_f32_16x16x32_bf16 v[60:63], a[164:167], v[28:31], v[60:63]\n" \
" s_waitcnt lgkmcnt(4)\n" \
" v_mfma_f32_16x16x32_bf16 v[48:51], a[40:43], v[32:35], v[48:51]\n" \
" buffer_load_dwordx4 v4, s[36:39], s61 offen offset:3072 sc1 lds\n" \
" v_mfma_f32_16x16x32_bf16 v[52:55], a[40:43], v[36:39], v[52:55]\n" \
" ds_read_b128 v[24:27], v10 offset:13312\n" \
" v_mfma_f32_16x16x32_bf16 v[56:59], a[168:171], v[32:35], v[56:59]\n" \
" ds_read_b128 v[28:31], v10 offset:29696\n" \
" v_mfma_f32_16x16x32_bf16 v[60:63], a[168:171], v[36:39], v[60:63]\n" \
" s_waitcnt lgkmcnt(4)\n" \
" v_mfma_f32_16x16x32_bf16 v[48:51], a[44:47], v[40:43], v[48:51]\n" \
" v_mfma_f32_16x16x32_bf16 v[52:55], a[44:47], v[44:47], v[52:55]\n" \
" ds_read_b128 v[32:35], v10 offset:14336\n" \
" v_mfma_f32_16x16x32_bf16 v[56:59], a[172:175], v[40:43], v[56:59]\n" \
" ds_read_b128 v[36:39], v10 offset:30720\n" \
" v_mfma_f32_16x16x32_bf16 v[60:63], a[172:175], v[44:47], v[60:63]\n" \
" s_waitcnt lgkmcnt(4)\n" \
" v_mfma_f32_16x16x32_bf16 v[48:51], a[48:51], v[16:19], v[48:51]\n" \
" v_mfma_f32_16x16x32_bf16 v[52:55], a[48:51], v[20:23], v[52:55]\n" \
" ds_read_b128 v[40:43], v10 offset:15360\n" \
" v_mfma_f32_16x16x32_bf16 v[56:59], a[176:179], v[16:19], v[56:59]\n" \
" ds_read_b128 v[44:47], v10 offset:31744\n" \
" v_mfma_f32_16x16x32_bf16 v[60:63], a[176:179], v[20:23], v[60:63]\n" \
" s_waitcnt lgkmcnt(4)\n" \
" v_mfma_f32_16x16x32_bf16 v[48:51], a[52:55], v[24:27], v[48:51]\n" \
" s_waitcnt vmcnt(11)\n" \
" s_barrier\n" \
" v_mfma_f32_16x16x32_bf16 v[52:55], a[52:55], v[28:31], v[52:55]\n" \
" ds_read_b128 v[16:19], v10 offset:32768\n" \
" v_mfma_f32_16x16x32_bf16 v[56:59], a[180:183], v[24:27], v[56:59]\n" \
" ds_read_b128 v[20:23], v10 offset:49152\n" \
" v_mfma_f32_16x16x32_bf16 v[60:63], a[180:183], v[28:31], v[60:63]\n" \
" s_waitcnt lgkmcnt(4)\n" \
" v_mfma_f32_16x16x32_bf16 v[48:51], a[56:59], v[32:35], v[48:51]\n" \
" v_mfma_f32_16x16x32_bf16 v[52:55], a[56:59], v[36:39], v[52:55]\n" \
" ds_read_b128 v[24:27], v10 offset:33792\n" \
" v_mfma_f32_16x16x32_bf16 v[56:59], a[184:187], v[32:35], v[56:59]\n" \
" ds_read_b128 v[28:31], v10 offset:50176\n" \
" v_mfma_f32_16x16x32_bf16 v[60:63], a[184:187], v[36:39], v[60:63]\n" \
" s_waitcnt lgkmcnt(4)\n" \
" v_mfma_f32_16x16x32_bf16 v[48:51], a[60:63], v[40:43], v[48:51]\n" \
" v_mfma_f32_16x16x32_bf16 v[52:55], a[60:63], v[44:47], v[52:55]\n" \
" ds_read_b128 v[32:35], v10 offset:34816\n" \
" v_mfma_f32_16x16x32_bf16 v[56:59], a[188:191], v[40:43], v[56:59]\n" \
" ds_read_b128 v[36:39], v10 offset:51200\n" \
" v_mfma_f32_16x16x32_bf16 v[60:63], a[188:191], v[44:47], v[60:63]\n" \
" s_waitcnt lgkmcnt(4)\n" \
" v_mfma_f32_16x16x32_bf16 v[48:51], a[64:67], v[16:19], v[48:51]\n" \
" s_min_u32 s61, s56, s60\n" \
" s_add_u32 s61, s61, 16384\n" \
" s_add_u32 s56, s56, 0x10000\n" \
" s_add_u32 m0, s55, 98304\n" \
" s_nop 0\n" \
" buffer_load_dwordx4 v4, s[36:39], s61 offen offset:0 sc1 lds\n" \
" v_mfma_f32_16x16x32_bf16 v[52:55], a[64:67], v[20:23], v[52:55]\n" \
" ds_read_b128 v[40:43], v10 offset:35840\n" \
" v_mfma_f32_16x16x32_bf16 v[56:59], a[192:195], v[16:19], v[56:59]\n" \
" ds_read_b128 v[44:47], v10 offset:52224\n" \
" v_mfma_f32_16x16x32_bf16 v[60:63], a[192:195], v[20:23], v[60:63]\n" \
" s_waitcnt lgkmcnt(4)\n" \
" v_mfma_f32_16x16x32_bf16 v[48:51], a[68:71], v[24:27], v[48:51]\n" \
" buffer_load_dwordx4 v4, s[36:39], s61 offen offset:1024 sc1 lds\n" \
" v_mfma_f32_16x16x32_bf16 v[52:55], a[68:71], v[28:31], v[52:55]\n" \
" ds_read_b128 v[16:19], v10 offset:36864\n" \
" v_mfma_f32_16x16x32_bf16 v[56:59], a[196:199], v[24:27], v[56:59]\n" \
" ds_read_b128 v[20:23], v10 offset:53248\n" \
" v_mfma_f32_16x16x32_bf16 v[60:63], a[196:199], v[28:31], v[60:63]\n" \
" s_waitcnt lgkmcnt(4)\n" \
" v_mfma_f32_16x16x32_bf16 v[48:51], a[72:75], v[32:35], v[48:51]\n" \
" v_mfma_f32_16x16x32_bf16 v[52:55], a[72:75], v[36:39], v[52:55]\n" \
" ds_read_b128 v[24:27], v10 offset:37888\n" \
" v_mfma_f32_16x16x32_bf16 v[56:59], a[200:203], v[32:35], v[56:59]\n" \
" ds_read_b128 v[28:31], v10 offset:54272\n" \
" v_mfma_f32_16x16x32_bf16 v[60:63], a[200:203], v[36:39], v[60:63]\n" \
" s_waitcnt lgkmcnt(4)\n" \
" v_mfma_f32_16x16x32_bf16 v[48:51], a[76:79], v[40:43], v[48:51]\n" \
" buffer_load_dwordx4 v4, s[36:39], s61 offen offset:2048 sc1 lds\n" \
" v_mfma_f32_16x16x32_bf16 v[52:55], a[76:79], v[44:47], v[52:55]\n" \
" ds_read_b128 v[32:35], v10 offset:38912\n" \
" v_mfma_f32_16x16x32_bf16 v[56:59], a[204:207], v[40:43], v[56:59]\n" \
" ds_read_b128 v[36:39], v10 offset:55296\n" \
" v_mfma_f32_16x16x32_bf16 v[60:63], a[204:207], v[44:47], v[60:63]\n" \
" s_waitcnt lgkmcnt(4)\n" \
" v_mfma_f32_16x16x32_bf16 v[48:51], a[80:83], v[16:19], v[48:51]\n" \
" buffer_load_dwordx4 v4, s[36:39], s61 offen offset:3072 sc1 lds\n" \
" v_mfma_f32_16x16x32_bf16 v[52:55], a[80:83], v[20:23], v[52:55]\n" \
" ds_read_b128 v[40:43], v10 offset:39936\n" \
" v_mfma_f32_16x16x32_bf16 v[56:59], a[208:211], v[16:19], v[56:59]\n" \
" ds_read_b128 v[44:47], v10 offset:56320\n" \
" v_mfma_f32_16x16x32_bf16 v[60:63], a[208:211], v[20:23], v[60:63]\n" \
" s_waitcnt lgkmcnt(4)\n" \
" v_mfma_f32_16x16x32_bf16 v[48:51], a[84:87], v[24:27], v[48:51]\n" \
" v_mfma_f32_16x16x32_bf16 v[52:55], a[84:87], v[28:31], v[52:55]\n" \
" ds_read_b128 v[16:19], v10 offset:40960\n" \
" v_mfma_f32_16x16x32_bf16 v[56:59], a[212:215], v[24:27], v[56:59]\n" \
" ds_read_b128 v[20:23], v10 offset:57344\n" \
" v_mfma_f32_16x16x32_bf16 v[60:63], a[212:215], v[28:31], v[60:63]\n" \
" s_waitcnt lgkmcnt(4)\n" \
" v_mfma_f32_16x16x32_bf16 v[48:51], a[88:91], v[32:35], v[48:51]\n" \
" s_add_u32 m0, s55, 102400\n" \
" s_add_u32 s61, s61, 4096\n" \
" buffer_load_dwordx4 v4, s[36:39], s61 offen offset:0 sc1 lds\n" \
" v_mfma_f32_16x16x32_bf16 v[52:55], a[88:91], v[36:39], v[52:55]\n" \
" ds_read_b128 v[24:27], v10 offset:41984\n" \
" v_mfma_f32_16x16x32_bf16 v[56:59], a[216:219], v[32:35], v[56:59]\n" \
" ds_read_b128 v[28:31], v10 offset:58368\n" \
" v_mfma_f32_16x16x32_bf16 v[60:63], a[216:219], v[36:39], v[60:63]\n" \
" s_waitcnt lgkmcnt(4)\n" \
" v_mfma_f32_16x16x32_bf16 v[48:51], a[92:95], v[40:43], v[48:51]\n" \
" buffer_load_dwordx4 v4, s[36:39], s61 offen offset:1024 sc1 lds\n" \
" v_mfma_f32_16x16x32_bf16 v[52:55], a[92:95], v[44:47], v[52:55]\n" \
" ds_read_b128 v[32:35], v10 offset:43008\n" \
" v_mfma_f32_16x16x32_bf16 v[56:59], a[220:223], v[40:43], v[56:59]\n" \
" ds_read_b128 v[36:39], v10 offset:59392\n" \
" v_mfma_f32_16x16x32_bf16 v[60:63], a[220:223], v[44:47], v[60:63]\n" \
" s_waitcnt lgkmcnt(4)\n" \
" v_mfma_f32_16x16x32_bf16 v[48:51], a[96:99], v[16:19], v[48:51]\n" \
" v_mfma_f32_16x16x32_bf16 v[52:55], a[96:99], v[20:23], v[52:55]\n" \
" ds_read_b128 v[40:43], v10 offset:44032\n" \
" v_mfma_f32_16x16x32_bf16 v[56:59], a[224:227], v[16:19], v[56:59]\n" \
" ds_read_b128 v[44:47], v10 offset:60416\n" \
" v_mfma_f32_16x16x32_bf16 v[60:63], a[224:227], v[20:23], v[60:63]\n" \
" s_waitcnt lgkmcnt(4)\n" \
" v_mfma_f32_16x16x32_bf16 v[48:51], a[100:103], v[24:27], v[48:51]\n" \
" buffer_load_dwordx4 v4, s[36:39], s61 offen offset:2048 sc1 lds\n" \
" v_mfma_f32_16x16x32_bf16 v[52:55], a[100:103], v[28:31], v[52:55]\n" \
" ds_read_b128 v[16:19], v10 offset:45056\n" \
" v_mfma_f32_16x16x32_bf16 v[56:59], a[228:231], v[24:27], v[56:59]\n" \
" ds_read_b128 v[20:23], v10 offset:61440\n" \
" v_mfma_f32_16x16x32_bf16 v[60:63], a[228:231], v[28:31], v[60:63]\n" \
" s_waitcnt lgkmcnt(4)\n" \
" v_mfma_f32_16x16x32_bf16 v[48:51], a[104:107], v[32:35], v[48:51]\n" \
" buffer_load_dwordx4 v4, s[36:39], s61 offen offset:3072 sc1 lds\n" \
" v_mfma_f32_16x16x32_bf16 v[52:55], a[104:107], v[36:39], v[52:55]\n" \
" ds_read_b128 v[24:27], v10 offset:46080\n" \
" v_mfma_f32_16x16x32_bf16 v[56:59], a[232:235], v[32:35], v[56:59]\n" \
" ds_read_b128 v[28:31], v10 offset:62464\n" \
" v_mfma_f32_16x16x32_bf16 v[60:63], a[232:235], v[36:39], v[60:63]\n" \
" s_waitcnt lgkmcnt(4)\n" \
" v_mfma_f32_16x16x32_bf16 v[48:51], a[108:111], v[40:43], v[48:51]\n" \
" v_mfma_f32_16x16x32_bf16 v[52:55], a[108:111], v[44:47], v[52:55]\n" \
" ds_read_b128 v[32:35], v10 offset:47104\n" \
" v_mfma_f32_16x16x32_bf16 v[56:59], a[236:239], v[40:43], v[56:59]\n" \
" ds_read_b128 v[36:39], v10 offset:63488\n" \
" v_mfma_f32_16x16x32_bf16 v[60:63], a[236:239], v[44:47], v[60:63]\n" \
" s_waitcnt lgkmcnt(4)\n" \
" v_mfma_f32_16x16x32_bf16 v[48:51], a[112:115], v[16:19], v[48:51]\n" \
" v_mfma_f32_16x16x32_bf16 v[52:55], a[112:115], v[20:23], v[52:55]\n" \
" ds_read_b128 v[40:43], v10 offset:48128\n" \
" v_mfma_f32_16x16x32_bf16 v[56:59], a[240:243], v[16:19], v[56:59]\n" \
" ds_read_b128 v[44:47], v10 offset:64512\n" \
" v_mfma_f32_16x16x32_bf16 v[60:63], a[240:243], v[20:23], v[60:63]\n" \
" s_waitcnt lgkmcnt(4)\n" \
" v_mfma_f32_16x16x32_bf16 v[48:51], a[116:119], v[24:27], v[48:51]\n" \
" s_waitcnt vmcnt(8)\n" \
" s_barrier\n" \
" v_mfma_f32_16x16x32_bf16 v[52:55], a[116:119], v[28:31], v[52:55]\n" \
" ds_read_b128 v[16:19], v12 offset:0\n" \
" v_mfma_f32_16x16x32_bf16 v[56:59], a[244:247], v[24:27], v[56:59]\n" \
" ds_read_b128 v[20:23], v12 offset:16384\n" \
" v_mfma_f32_16x16x32_bf16 v[60:63], a[244:247], v[28:31], v[60:63]\n" \
" s_waitcnt lgkmcnt(4)\n" \
" buffer_store_dword v13, v6, s[36:39], 0 offen sc1\n" \
" v_mfma_f32_16x16x32_bf16 v[48:51], a[120:123], v[32:35], v[48:51]\n" \
" v_mfma_f32_16x16x32_bf16 v[52:55], a[120:123], v[36:39], v[52:55]\n" \
" ds_read_b128 v[24:27], v12 offset:1024\n" \
" v_mfma_f32_16x16x32_bf16 v[56:59], a[248:251], v[32:35], v[56:59]\n" \
" ds_read_b128 v[28:31], v12 offset:17408\n" \
" v_mfma_f32_16x16x32_bf16 v[60:63], a[248:251], v[36:39], v[60:63]\n" \
" s_waitcnt lgkmcnt(4)\n" \
" v_mfma_f32_16x16x32_bf16 v[48:51], a[124:127], v[40:43], v[48:51]\n" \
" v_mfma_f32_16x16x32_bf16 v[52:55], a[124:127], v[44:47], v[52:55]\n" \
" ds_read_b128 v[32:35], v12 offset:2048\n" \
" v_mfma_f32_16x16x32_bf16 v[56:59], a[252:255], v[40:43], v[56:59]\n" \
" ds_read_b128 v[36:39], v12 offset:18432\n" \
" v_mfma_f32_16x16x32_bf16 v[60:63], a[252:255], v[44:47], v[60:63]\n" \
" s_waitcnt lgkmcnt(4)\n" \
" s_add_u32 s57, s57, s49\n" \
" s_add_u32 s58, s58, s49\n" \
" s_add_u32 s59, s59, 1\n" \
" v_mov_b32 v5, v7\n" \
" v_mov_b32 v6, v11\n" \
" s_and_b32 s62, s59, -2\n" \
" s_cmp_eq_u32 s62, 12\n" \
" s_cbranch_scc1 Lp1qk_l2\n" \
"Lp1qk_lm:\n" \
" s_cmp_ge_u32 s59, s47\n" \
" s_cbranch_scc1 Lp1qk_out0\n" \
" v_mfma_f32_16x16x32_bf16 v[64:67], a[0:3], v[16:19], 0\n" \
" s_min_u32 s61, s56, s60\n" \
" s_add_u32 m0, s55, 0\n" \
" s_nop 0\n" \
" buffer_load_dwordx4 v4, s[36:39], s61 offen offset:0 sc1 lds\n" \
" v_mov_b32_e64 v13, s59\n" \
" v_mfma_f32_16x16x32_bf16 v[68:71], a[0:3], v[20:23], 0\n" \
" ds_read_b128 v[40:43], v12 offset:3072\n" \
" s_nop 0\n" \
" v_mfma_f32_16x16x32_bf16 v[72:75], a[128:131], v[16:19], 0\n" \
" ds_read_b128 v[44:47], v12 offset:19456\n" \
" s_nop 0\n" \
" v_mfma_f32_16x16x32_bf16 v[76:79], a[128:131], v[20:23], 0\n" \
" s_waitcnt lgkmcnt(4)\n" \
" v_mul_f32 v48, s51, v48\n" \
" v_mul_f32_e64 v49, s51, v49\n" \
" v_mfma_f32_16x16x32_bf16 v[64:67], a[4:7], v[24:27], v[64:67]\n" \
" buffer_load_dwordx4 v4, s[36:39], s61 offen offset:1024 sc1 lds\n" \
" v_mfma_f32_16x16x32_bf16 v[68:71], a[4:7], v[28:31], v[68:71]\n" \
" ds_read_b128 v[16:19], v12 offset:4096\n" \
" v_mul_f32_e64 v50, s51, v50\n" \
" v_mfma_f32_16x16x32_bf16 v[72:75], a[132:135], v[24:27], v[72:75]\n" \
" ds_read_b128 v[20:23], v12 offset:20480\n" \
" v_mul_f32_e64 v51, s51, v51\n" \
" v_mfma_f32_16x16x32_bf16 v[76:79], a[132:135], v[28:31], v[76:79]\n" \
" s_waitcnt lgkmcnt(4)\n" \
" v_mul_f32 v56, s51, v56\n" \
" v_mul_f32_e64 v57, s51, v57\n" \
" v_mfma_f32_16x16x32_bf16 v[64:67], a[8:11], v[32:35], v[64:67]\n" \
" v_mul_f32 v58, s51, v58\n" \
" v_mul_f32 v59, s51, v59\n" \
" v_mfma_f32_16x16x32_bf16 v[68:71], a[8:11], v[36:39], v[68:71]\n" \
" ds_read_b128 v[24:27], v12 offset:5120\n" \
" v_cvt_pk_bf16_f32 v80, v48, v49\n" \
" v_mfma_f32_16x16x32_bf16 v[72:75], a[136:139], v[32:35], v[72:75]\n" \
" ds_read_b128 v[28:31], v12 offset:21504\n" \
" v_cvt_pk_bf16_f32 v81, v50, v51\n" \
" v_mfma_f32_16x16x32_bf16 v[76:79], a[136:139], v[36:39], v[76:79]\n" \
" s_waitcnt lgkmcnt(4)\n" \
" v_cvt_pk_bf16_f32 v82, v56, v57\n" \
" v_cvt_pk_bf16_f32 v83, v58, v59\n" \
" v_mfma_f32_16x16x32_bf16 v[64:67], a[12:15], v[40:43], v[64:67]\n" \
" buffer_load_dwordx4 v4, s[36:39], s61 offen offset:2048 sc1 lds\n" \
" v_mfma_f32_16x16x32_bf16 v[68:71], a[12:15], v[44:47], v[68:71]\n" \
" ds_read_b128 v[32:35], v12 offset:6144\n" \
" buffer_store_dwordx4 v[80:83], v5, s[36:39], s57 offen sc1\n" \
" v_mfma_f32_16x16x32_bf16 v[72:75], a[140:143], v[40:43], v[72:75]\n" \
" ds_read_b128 v[36:39], v12 offset:22528\n" \
" v_mul_f32 v52, s51, v52\n" \
" v_mfma_f32_16x16x32_bf16 v[76:79], a[140:143], v[44:47], v[76:79]\n" \
" s_waitcnt lgkmcnt(4)\n" \
" v_mul_f32 v53, s51, v53\n" \
" v_mul_f32_e64 v54, s51, v54\n" \
" v_mfma_f32_16x16x32_bf16 v[64:67], a[16:19], v[16:19], v[64:67]\n" \
" buffer_load_dwordx4 v4, s[36:39], s61 offen offset:3072 sc1 lds\n" \
" v_mfma_f32_16x16x32_bf16 v[68:71], a[16:19], v[20:23], v[68:71]\n" \
" ds_read_b128 v[40:43], v12 offset:7168\n" \
" v_mul_f32_e64 v55, s51, v55\n" \
" v_mfma_f32_16x16x32_bf16 v[72:75], a[144:147], v[16:19], v[72:75]\n" \
" ds_read_b128 v[44:47], v12 offset:23552\n" \
" v_mul_f32_e64 v60, s51, v60\n" \
" v_mfma_f32_16x16x32_bf16 v[76:79], a[144:147], v[20:23], v[76:79]\n" \
" s_waitcnt lgkmcnt(4)\n" \
" v_mul_f32 v61, s51, v61\n" \
" v_mul_f32_e64 v62, s51, v62\n" \
" v_mfma_f32_16x16x32_bf16 v[64:67], a[20:23], v[24:27], v[64:67]\n" \
" v_mul_f32_e64 v63, s51, v63\n" \
" v_cvt_pk_bf16_f32 v84, v52, v53\n" \
" v_mfma_f32_16x16x32_bf16 v[68:71], a[20:23], v[28:31], v[68:71]\n" \
" ds_read_b128 v[16:19], v12 offset:8192\n" \
" v_cvt_pk_bf16_f32 v85, v54, v55\n" \
" v_mfma_f32_16x16x32_bf16 v[72:75], a[148:151], v[24:27], v[72:75]\n" \
" ds_read_b128 v[20:23], v12 offset:24576\n" \
" v_cvt_pk_bf16_f32 v86, v60, v61\n" \
" v_mfma_f32_16x16x32_bf16 v[76:79], a[148:151], v[28:31], v[76:79]\n" \
" s_waitcnt lgkmcnt(4)\n" \
" v_cvt_pk_bf16_f32 v87, v62, v63\n" \
" buffer_store_dwordx4 v[84:87], v5, s[36:39], s58 offen sc1\n" \
" v_mfma_f32_16x16x32_bf16 v[64:67], a[24:27], v[32:35], v[64:67]\n" \
" s_add_u32 m0, s55, 4096\n" \
" s_add_u32 s61, s61, 4096\n" \
" buffer_load_dwordx4 v4, s[36:39], s61 offen offset:0 sc1 lds\n" \
" v_mfma_f32_16x16x32_bf16 v[68:71], a[24:27], v[36:39], v[68:71]\n" \
" ds_read_b128 v[24:27], v12 offset:9216\n" \
" v_mfma_f32_16x16x32_bf16 v[72:75], a[152:155], v[32:35], v[72:75]\n" \
" ds_read_b128 v[28:31], v12 offset:25600\n" \
" v_mfma_f32_16x16x32_bf16 v[76:79], a[152:155], v[36:39], v[76:79]\n" \
" s_waitcnt lgkmcnt(4)\n" \
" v_mfma_f32_16x16x32_bf16 v[64:67], a[28:31], v[40:43], v[64:67]\n" \
" buffer_load_dwordx4 v4, s[36:39], s61 offen offset:1024 sc1 lds\n" \
" v_mfma_f32_16x16x32_bf16 v[68:71], a[28:31], v[44:47], v[68:71]\n" \
" ds_read_b128 v[32:35], v12 offset:10240\n" \
" v_mfma_f32_16x16x32_bf16 v[72:75], a[156:159], v[40:43], v[72:75]\n" \
" ds_read_b128 v[36:39], v12 offset:26624\n" \
" v_mfma_f32_16x16x32_bf16 v[76:79], a[156:159], v[44:47], v[76:79]\n" \
" s_waitcnt lgkmcnt(4)\n" \
" v_mfma_f32_16x16x32_bf16 v[64:67], a[32:35], v[16:19], v[64:67]\n" \
" v_mfma_f32_16x16x32_bf16 v[68:71], a[32:35], v[20:23], v[68:71]\n" \
" ds_read_b128 v[40:43], v12 offset:11264\n" \
" v_mfma_f32_16x16x32_bf16 v[72:75], a[160:163], v[16:19], v[72:75]\n" \
" ds_read_b128 v[44:47], v12 offset:27648\n" \
" v_mfma_f32_16x16x32_bf16 v[76:79], a[160:163], v[20:23], v[76:79]\n" \
" s_waitcnt lgkmcnt(4)\n" \
" v_mfma_f32_16x16x32_bf16 v[64:67], a[36:39], v[24:27], v[64:67]\n" \
" buffer_load_dwordx4 v4, s[36:39], s61 offen offset:2048 sc1 lds\n" \
" v_mfma_f32_16x16x32_bf16 v[68:71], a[36:39], v[28:31], v[68:71]\n" \
" ds_read_b128 v[16:19], v12 offset:12288\n" \
" v_mfma_f32_16x16x32_bf16 v[72:75], a[164:167], v[24:27], v[72:75]\n" \
" ds_read_b128 v[20:23], v12 offset:28672\n" \
" v_mfma_f32_16x16x32_bf16 v[76:79], a[164:167], v[28:31], v[76:79]\n" \
" s_waitcnt lgkmcnt(4)\n" \
" v_mfma_f32_16x16x32_bf16 v[64:67], a[40:43], v[32:35], v[64:67]\n" \
" buffer_load_dwordx4 v4, s[36:39], s61 offen offset:3072 sc1 lds\n" \
" v_mfma_f32_16x16x32_bf16 v[68:71], a[40:43], v[36:39], v[68:71]\n" \
" ds_read_b128 v[24:27], v12 offset:13312\n" \
" v_mfma_f32_16x16x32_bf16 v[72:75], a[168:171], v[32:35], v[72:75]\n" \
" ds_read_b128 v[28:31], v12 offset:29696\n" \
" v_mfma_f32_16x16x32_bf16 v[76:79], a[168:171], v[36:39], v[76:79]\n" \
" s_waitcnt lgkmcnt(4)\n" \
" v_mfma_f32_16x16x32_bf16 v[64:67], a[44:47], v[40:43], v[64:67]\n" \
" v_mfma_f32_16x16x32_bf16 v[68:71], a[44:47], v[44:47], v[68:71]\n" \
" ds_read_b128 v[32:35], v12 offset:14336\n" \
" v_mfma_f32_16x16x32_bf16 v[72:75], a[172:175], v[40:43], v[72:75]\n" \
" ds_read_b128 v[36:39], v12 offset:30720\n" \
" v_mfma_f32_16x16x32_bf16 v[76:79], a[172:175], v[44:47], v[76:79]\n" \
" s_waitcnt lgkmcnt(4)\n" \
" v_mfma_f32_16x16x32_bf16 v[64:67], a[48:51], v[16:19], v[64:67]\n" \
" v_mfma_f32_16x16x32_bf16 v[68:71], a[48:51], v[20:23], v[68:71]\n" \
" ds_read_b128 v[40:43], v12 offset:15360\n" \
" v_mfma_f32_16x16x32_bf16 v[72:75], a[176:179], v[16:19], v[72:75]\n" \
" ds_read_b128 v[44:47], v12 offset:31744\n" \
" v_mfma_f32_16x16x32_bf16 v[76:79], a[176:179], v[20:23], v[76:79]\n" \
" s_waitcnt lgkmcnt(4)\n" \
" v_mfma_f32_16x16x32_bf16 v[64:67], a[52:55], v[24:27], v[64:67]\n" \
" s_waitcnt vmcnt(11)\n" \
" s_barrier\n" \
" v_mfma_f32_16x16x32_bf16 v[68:71], a[52:55], v[28:31], v[68:71]\n" \
" ds_read_b128 v[16:19], v12 offset:32768\n" \
" v_mfma_f32_16x16x32_bf16 v[72:75], a[180:183], v[24:27], v[72:75]\n" \
" ds_read_b128 v[20:23], v12 offset:49152\n" \
" v_mfma_f32_16x16x32_bf16 v[76:79], a[180:183], v[28:31], v[76:79]\n" \
" s_waitcnt lgkmcnt(4)\n" \
" v_mfma_f32_16x16x32_bf16 v[64:67], a[56:59], v[32:35], v[64:67]\n" \
" v_mfma_f32_16x16x32_bf16 v[68:71], a[56:59], v[36:39], v[68:71]\n" \
" ds_read_b128 v[24:27], v12 offset:33792\n" \
" v_mfma_f32_16x16x32_bf16 v[72:75], a[184:187], v[32:35], v[72:75]\n" \
" ds_read_b128 v[28:31], v12 offset:50176\n" \
" v_mfma_f32_16x16x32_bf16 v[76:79], a[184:187], v[36:39], v[76:79]\n" \
" s_waitcnt lgkmcnt(4)\n" \
" v_mfma_f32_16x16x32_bf16 v[64:67], a[60:63], v[40:43], v[64:67]\n" \
" v_mfma_f32_16x16x32_bf16 v[68:71], a[60:63], v[44:47], v[68:71]\n" \
" ds_read_b128 v[32:35], v12 offset:34816\n" \
" v_mfma_f32_16x16x32_bf16 v[72:75], a[188:191], v[40:43], v[72:75]\n" \
" ds_read_b128 v[36:39], v12 offset:51200\n" \
" v_mfma_f32_16x16x32_bf16 v[76:79], a[188:191], v[44:47], v[76:79]\n" \
" s_waitcnt lgkmcnt(4)\n" \
" v_mfma_f32_16x16x32_bf16 v[64:67], a[64:67], v[16:19], v[64:67]\n" \
" s_min_u32 s61, s56, s60\n" \
" s_add_u32 s61, s61, 16384\n" \
" s_add_u32 s56, s56, 0x10000\n" \
" s_add_u32 m0, s55, 32768\n" \
" s_nop 0\n" \
" buffer_load_dwordx4 v4, s[36:39], s61 offen offset:0 sc1 lds\n" \
" v_mfma_f32_16x16x32_bf16 v[68:71], a[64:67], v[20:23], v[68:71]\n" \
" ds_read_b128 v[40:43], v12 offset:35840\n" \
" v_mfma_f32_16x16x32_bf16 v[72:75], a[192:195], v[16:19], v[72:75]\n" \
" ds_read_b128 v[44:47], v12 offset:52224\n" \
" v_mfma_f32_16x16x32_bf16 v[76:79], a[192:195], v[20:23], v[76:79]\n" \
" s_waitcnt lgkmcnt(4)\n" \
" v_mfma_f32_16x16x32_bf16 v[64:67], a[68:71], v[24:27], v[64:67]\n" \
" buffer_load_dwordx4 v4, s[36:39], s61 offen offset:1024 sc1 lds\n" \
" v_mfma_f32_16x16x32_bf16 v[68:71], a[68:71], v[28:31], v[68:71]\n" \
" ds_read_b128 v[16:19], v12 offset:36864\n" \
" v_mfma_f32_16x16x32_bf16 v[72:75], a[196:199], v[24:27], v[72:75]\n" \
" ds_read_b128 v[20:23], v12 offset:53248\n" \
" v_mfma_f32_16x16x32_bf16 v[76:79], a[196:199], v[28:31], v[76:79]\n" \
" s_waitcnt lgkmcnt(4)\n" \
" v_mfma_f32_16x16x32_bf16 v[64:67], a[72:75], v[32:35], v[64:67]\n" \
" v_mfma_f32_16x16x32_bf16 v[68:71], a[72:75], v[36:39], v[68:71]\n" \
" ds_read_b128 v[24:27], v12 offset:37888\n" \
" v_mfma_f32_16x16x32_bf16 v[72:75], a[200:203], v[32:35], v[72:75]\n" \
" ds_read_b128 v[28:31], v12 offset:54272\n" \
" v_mfma_f32_16x16x32_bf16 v[76:79], a[200:203], v[36:39], v[76:79]\n" \
" s_waitcnt lgkmcnt(4)\n" \
" v_mfma_f32_16x16x32_bf16 v[64:67], a[76:79], v[40:43], v[64:67]\n" \
" buffer_load_dwordx4 v4, s[36:39], s61 offen offset:2048 sc1 lds\n" \
" v_mfma_f32_16x16x32_bf16 v[68:71], a[76:79], v[44:47], v[68:71]\n" \
" ds_read_b128 v[32:35], v12 offset:38912\n" \
" v_mfma_f32_16x16x32_bf16 v[72:75], a[204:207], v[40:43], v[72:75]\n" \
" ds_read_b128 v[36:39], v12 offset:55296\n" \
" v_mfma_f32_16x16x32_bf16 v[76:79], a[204:207], v[44:47], v[76:79]\n" \
" s_waitcnt lgkmcnt(4)\n" \
" v_mfma_f32_16x16x32_bf16 v[64:67], a[80:83], v[16:19], v[64:67]\n" \
" buffer_load_dwordx4 v4, s[36:39], s61 offen offset:3072 sc1 lds\n" \
" v_mfma_f32_16x16x32_bf16 v[68:71], a[80:83], v[20:23], v[68:71]\n" \
" ds_read_b128 v[40:43], v12 offset:39936\n" \
" v_mfma_f32_16x16x32_bf16 v[72:75], a[208:211], v[16:19], v[72:75]\n" \
" ds_read_b128 v[44:47], v12 offset:56320\n" \
" v_mfma_f32_16x16x32_bf16 v[76:79], a[208:211], v[20:23], v[76:79]\n" \
" s_waitcnt lgkmcnt(4)\n" \
" v_mfma_f32_16x16x32_bf16 v[64:67], a[84:87], v[24:27], v[64:67]\n" \
" v_mfma_f32_16x16x32_bf16 v[68:71], a[84:87], v[28:31], v[68:71]\n" \
" ds_read_b128 v[16:19], v12 offset:40960\n" \
" v_mfma_f32_16x16x32_bf16 v[72:75], a[212:215], v[24:27], v[72:75]\n" \
" ds_read_b128 v[20:23], v12 offset:57344\n" \
" v_mfma_f32_16x16x32_bf16 v[76:79], a[212:215], v[28:31], v[76:79]\n" \
" s_waitcnt lgkmcnt(4)\n" \
" v_mfma_f32_16x16x32_bf16 v[64:67], a[88:91], v[32:35], v[64:67]\n" \
" s_add_u32 m0, s55, 36864\n" \
" s_add_u32 s61, s61, 4096\n" \
" buffer_load_dwordx4 v4, s[36:39], s61 offen offset:0 sc1 lds\n" \
" v_mfma_f32_16x16x32_bf16 v[68:71], a[88:91], v[36:39], v[68:71]\n" \
" ds_read_b128 v[24:27], v12 offset:41984\n" \
" v_mfma_f32_16x16x32_bf16 v[72:75], a[216:219], v[32:35], v[72:75]\n" \
" ds_read_b128 v[28:31], v12 offset:58368\n" \
" v_mfma_f32_16x16x32_bf16 v[76:79], a[216:219], v[36:39], v[76:79]\n" \
" s_waitcnt lgkmcnt(4)\n" \
" v_mfma_f32_16x16x32_bf16 v[64:67], a[92:95], v[40:43], v[64:67]\n" \
" buffer_load_dwordx4 v4, s[36:39], s61 offen offset:1024 sc1 lds\n" \
" v_mfma_f32_16x16x32_bf16 v[68:71], a[92:95], v[44:47], v[68:71]\n" \
" ds_read_b128 v[32:35], v12 offset:43008\n" \
" v_mfma_f32_16x16x32_bf16 v[72:75], a[220:223], v[40:43], v[72:75]\n" \
" ds_read_b128 v[36:39], v12 offset:59392\n" \
" v_mfma_f32_16x16x32_bf16 v[76:79], a[220:223], v[44:47], v[76:79]\n" \
" s_waitcnt lgkmcnt(4)\n" \
" v_mfma_f32_16x16x32_bf16 v[64:67], a[96:99], v[16:19], v[64:67]\n" \
" v_mfma_f32_16x16x32_bf16 v[68:71], a[96:99], v[20:23], v[68:71]\n" \
" ds_read_b128 v[40:43], v12 offset:44032\n" \
" v_mfma_f32_16x16x32_bf16 v[72:75], a[224:227], v[16:19], v[72:75]\n" \
" ds_read_b128 v[44:47], v12 offset:60416\n" \
" v_mfma_f32_16x16x32_bf16 v[76:79], a[224:227], v[20:23], v[76:79]\n" \
" s_waitcnt lgkmcnt(4)\n" \
" v_mfma_f32_16x16x32_bf16 v[64:67], a[100:103], v[24:27], v[64:67]\n" \
" buffer_load_dwordx4 v4, s[36:39], s61 offen offset:2048 sc1 lds\n" \
" v_mfma_f32_16x16x32_bf16 v[68:71], a[100:103], v[28:31], v[68:71]\n" \
" ds_read_b128 v[16:19], v12 offset:45056\n" \
" v_mfma_f32_16x16x32_bf16 v[72:75], a[228:231], v[24:27], v[72:75]\n" \
" ds_read_b128 v[20:23], v12 offset:61440\n" \
" v_mfma_f32_16x16x32_bf16 v[76:79], a[228:231], v[28:31], v[76:79]\n" \
" s_waitcnt lgkmcnt(4)\n" \
" v_mfma_f32_16x16x32_bf16 v[64:67], a[104:107], v[32:35], v[64:67]\n" \
" buffer_load_dwordx4 v4, s[36:39], s61 offen offset:3072 sc1 lds\n" \
" v_mfma_f32_16x16x32_bf16 v[68:71], a[104:107], v[36:39], v[68:71]\n" \
" ds_read_b128 v[24:27], v12 offset:46080\n" \
" v_mfma_f32_16x16x32_bf16 v[72:75], a[232:235], v[32:35], v[72:75]\n" \
" ds_read_b128 v[28:31], v12 offset:62464\n" \
" v_mfma_f32_16x16x32_bf16 v[76:79], a[232:235], v[36:39], v[76:79]\n" \
" s_waitcnt lgkmcnt(4)\n" \
" v_mfma_f32_16x16x32_bf16 v[64:67], a[108:111], v[40:43], v[64:67]\n" \
" v_mfma_f32_16x16x32_bf16 v[68:71], a[108:111], v[44:47], v[68:71]\n" \
" ds_read_b128 v[32:35], v12 offset:47104\n" \
" v_mfma_f32_16x16x32_bf16 v[72:75], a[236:239], v[40:43], v[72:75]\n" \
" ds_read_b128 v[36:39], v12 offset:63488\n" \
" v_mfma_f32_16x16x32_bf16 v[76:79], a[236:239], v[44:47], v[76:79]\n" \
" s_waitcnt lgkmcnt(4)\n" \
" v_mfma_f32_16x16x32_bf16 v[64:67], a[112:115], v[16:19], v[64:67]\n" \
" v_mfma_f32_16x16x32_bf16 v[68:71], a[112:115], v[20:23], v[68:71]\n" \
" ds_read_b128 v[40:43], v12 offset:48128\n" \
" v_mfma_f32_16x16x32_bf16 v[72:75], a[240:243], v[16:19], v[72:75]\n" \
" ds_read_b128 v[44:47], v12 offset:64512\n" \
" v_mfma_f32_16x16x32_bf16 v[76:79], a[240:243], v[20:23], v[76:79]\n" \
" s_waitcnt lgkmcnt(4)\n" \
" v_mfma_f32_16x16x32_bf16 v[64:67], a[116:119], v[24:27], v[64:67]\n" \
" s_waitcnt vmcnt(8)\n" \
" s_barrier\n" \
" v_mfma_f32_16x16x32_bf16 v[68:71], a[116:119], v[28:31], v[68:71]\n" \
" ds_read_b128 v[16:19], v10 offset:0\n" \
" v_mfma_f32_16x16x32_bf16 v[72:75], a[244:247], v[24:27], v[72:75]\n" \
" ds_read_b128 v[20:23], v10 offset:16384\n" \
" v_mfma_f32_16x16x32_bf16 v[76:79], a[244:247], v[28:31], v[76:79]\n" \
" s_waitcnt lgkmcnt(4)\n" \
" buffer_store_dword v13, v6, s[36:39], 0 offen sc1\n" \
" v_mfma_f32_16x16x32_bf16 v[64:67], a[120:123], v[32:35], v[64:67]\n" \
" v_mfma_f32_16x16x32_bf16 v[68:71], a[120:123], v[36:39], v[68:71]\n" \
" ds_read_b128 v[24:27], v10 offset:1024\n" \
" v_mfma_f32_16x16x32_bf16 v[72:75], a[248:251], v[32:35], v[72:75]\n" \
" ds_read_b128 v[28:31], v10 offset:17408\n" \
" v_mfma_f32_16x16x32_bf16 v[76:79], a[248:251], v[36:39], v[76:79]\n" \
" s_waitcnt lgkmcnt(4)\n" \
" v_mfma_f32_16x16x32_bf16 v[64:67], a[124:127], v[40:43], v[64:67]\n" \
" v_mfma_f32_16x16x32_bf16 v[68:71], a[124:127], v[44:47], v[68:71]\n" \
" ds_read_b128 v[32:35], v10 offset:2048\n" \
" v_mfma_f32_16x16x32_bf16 v[72:75], a[252:255], v[40:43], v[72:75]\n" \
" ds_read_b128 v[36:39], v10 offset:18432\n" \
" v_mfma_f32_16x16x32_bf16 v[76:79], a[252:255], v[44:47], v[76:79]\n" \
" s_waitcnt lgkmcnt(4)\n" \
" s_add_u32 s57, s57, s49\n" \
" s_add_u32 s58, s58, s49\n" \
" s_add_u32 s59, s59, 1\n" \
" v_mov_b32_e64 v5, v7\n" \
" v_mov_b32_e64 v6, v11\n" \
" s_cmp_ge_u32 s59, s47\n" \
" s_nop 0\n" \
" s_cbranch_scc0 Lp1qk_loop\n" \
"; cold\n" \
"Lp1qk_out1:\n" \
" s_nop 7\n" \
" s_nop 7\n" \
" v_mul_f32 v64, s51, v64\n" \
" v_mul_f32 v65, s51, v65\n" \
" v_mul_f32 v66, s51, v66\n" \
" v_mul_f32 v67, s51, v67\n" \
" v_mul_f32 v72, s51, v72\n" \
" v_mul_f32 v73, s51, v73\n" \
" v_mul_f32 v74, s51, v74\n" \
" v_mul_f32 v75, s51, v75\n" \
" v_cvt_pk_bf16_f32 v80, v64, v65\n" \
" v_cvt_pk_bf16_f32 v81, v66, v67\n" \
" v_cvt_pk_bf16_f32 v82, v72, v73\n" \
" v_cvt_pk_bf16_f32 v83, v74, v75\n" \
" buffer_store_dwordx4 v[80:83], v5, s[36:39], s57 offen sc1\n" \
" v_mul_f32 v68, s51, v68\n" \
" v_mul_f32 v69, s51, v69\n" \
" v_mul_f32 v70, s51, v70\n" \
" v_mul_f32 v71, s51, v71\n" \
" v_mul_f32 v76, s51, v76\n" \
" v_mul_f32 v77, s51, v77\n" \
" v_mul_f32 v78, s51, v78\n" \
" v_mul_f32 v79, s51, v79\n" \
" v_cvt_pk_bf16_f32 v84, v68, v69\n" \
" v_cvt_pk_bf16_f32 v85, v70, v71\n" \
" v_cvt_pk_bf16_f32 v86, v76, v77\n" \
" v_cvt_pk_bf16_f32 v87, v78, v79\n" \
" buffer_store_dwordx4 v[84:87], v5, s[36:39], s58 offen sc1\n" \
" s_branch Lp1qk_end\n" \
"Lp1qk_out0:\n" \
" s_nop 7\n" \
" s_nop 7\n" \
" v_mul_f32 v48, s51, v48\n" \
" v_mul_f32 v49, s51, v49\n" \
" v_mul_f32 v50, s51, v50\n" \
" v_mul_f32 v51, s51, v51\n" \
" v_mul_f32 v56, s51, v56\n" \
" v_mul_f32 v57, s51, v57\n" \
" v_mul_f32 v58, s51, v58\n" \
" v_mul_f32 v59, s51, v59\n" \
" v_cvt_pk_bf16_f32 v80, v48, v49\n" \
" v_cvt_pk_bf16_f32 v81, v50, v51\n" \
" v_cvt_pk_bf16_f32 v82, v56, v57\n" \
" v_cvt_pk_bf16_f32 v83, v58, v59\n" \
" buffer_store_dwordx4 v[80:83], v5, s[36:39], s57 offen sc1\n" \
" v_mul_f32 v52, s51, v52\n" \
" v_mul_f32 v53, s51, v53\n" \
" v_mul_f32 v54, s51, v54\n" \
" v_mul_f32 v55, s51, v55\n" \
" v_mul_f32 v60, s51, v60\n" \
" v_mul_f32 v61, s51, v61\n" \
" v_mul_f32 v62, s51, v62\n" \
" v_mul_f32 v63, s51, v63\n" \
" v_cvt_pk_bf16_f32 v84, v52, v53\n" \
" v_cvt_pk_bf16_f32 v85, v54, v55\n" \
" v_cvt_pk_bf16_f32 v86, v60, v61\n" \
" v_cvt_pk_bf16_f32 v87, v62, v63\n" \
" buffer_store_dwordx4 v[84:87], v5, s[36:39], s58 offen sc1\n" \
"Lp1qk_end:\n" \
" s_waitcnt vmcnt(0) lgkmcnt(0)\n" \
" s_barrier\n" \
" v_mov_b32 v13, s59\n" \
" buffer_store_dword v13, v6, s[36:39], 0 offen sc1\n" \
" s_waitcnt vmcnt(0) lgkmcnt(0)\n" \
" s_branch Lp1qk_x\n" \
"Lp1qk_l0:\n" \
" s_nop 7\n" \
" s_nop 7\n" \
" s_waitcnt vmcnt(0) lgkmcnt(0)\n" \
" s_mov_b32 s62, 0\n" \
"Lp1qk_l0a:\n" \
" buffer_load_dword v15, v14, s[36:39], 0 offen sc1\n" \
" s_waitcnt vmcnt(0)\n" \
" v_cmp_gt_u32 vcc, 32, v15\n" \
" s_nop 4\n" \
" s_and_b64 vcc, vcc, 1\n" \
" s_cbranch_vccz Lp1qk_lk\n" \
" s_sleep 4\n" \
" s_add_u32 s62, s62, 1\n" \
" s_cmp_lt_u32 s62, 0x10000\n" \
" s_cbranch_scc1 Lp1qk_l0a\n" \
" v_mov_b32 v15, 256\n" \
" buffer_store_dword v15, v15, s[36:39], 0 offen sc1\n" \
" s_waitcnt vmcnt(0) lgkmcnt(0)\n" \
" s_branch Lp1qk_lk\n" \
"Lp1qk_l1:\n" \
" s_nop 7\n" \
" s_nop 7\n" \
" s_waitcnt vmcnt(0) lgkmcnt(0)\n" \
" s_mov_b32 s62, 0\n" \
"Lp1qk_l1a:\n" \
" buffer_load_dword v15, v14, s[36:39], 0 offen sc1\n" \
" s_waitcnt vmcnt(0)\n" \
" v_cmp_gt_u32 vcc, 32, v15\n" \
" s_nop 4\n" \
" s_and_b64 vcc, vcc, 15\n" \
" s_cbranch_vccz Lp1qk_lk\n" \
" s_sleep 4\n" \
" s_add_u32 s62, s62, 1\n" \
" s_cmp_lt_u32 s62, 0x10000\n" \
" s_cbranch_scc1 Lp1qk_l1a\n" \
" v_mov_b32 v15, 256\n" \
" buffer_store_dword v15, v15, s[36:39], 0 offen sc1\n" \
" s_waitcnt vmcnt(0) lgkmcnt(0)\n" \
" s_branch Lp1qk_lk\n" \
"Lp1qk_l2:\n" \
" s_nop 7\n" \
" s_nop 7\n" \
" s_waitcnt vmcnt(0) lgkmcnt(0)\n" \
" s_mov_b32 s62, 0\n" \
"Lp1qk_l2a:\n" \
" buffer_load_dword v15, v14, s[36:39], 0 offen sc1\n" \
" s_waitcnt vmcnt(0)\n" \
" v_cmp_gt_u32 vcc, 32, v15\n" \
" s_nop 4\n" \
" s_and_b64 vcc, vcc, 15\n" \
" s_cbranch_vccz Lp1qk_lm\n" \
" s_sleep 4\n" \
" s_add_u32 s62, s62, 1\n" \
" s_cmp_lt_u32 s62, 0x10000\n" \
" s_cbranch_scc1 Lp1qk_l2a\n" \
" v_mov_b32 v15, 256\n" \
" buffer_store_dword v15, v15, s[36:39], 0 offen sc1\n" \
" s_waitcnt vmcnt(0) lgkmcnt(0)\n" \
" s_branch Lp1qk_lm\n" \
"Lp1qk_x:\n"
#define ASM_P1_V \
" s_nop 7\n" \
" s_nop 7\n" \
" s_waitcnt vmcnt(0) lgkmcnt(0)\n" \
" s_lshr_b32 s52, s48, 1\n" \
" s_lshl_b32 s52, s52, 13\n" \
" s_and_b32 s53, s48, 1\n" \
" s_lshl_b32 s53, s53, 10\n" \
" s_add_u32 s52, s52, s53\n" \
" s_mov_b32 s54, 0\n" \
" s_lshl_b32 s54, s44, 5\n" \
" v_add_u32 v8, 0x10000, v2\n" \
" v_add_u32 v9, 0x10000, v3\n" \
" s_barrier\n" \
" ds_read_b64_tr_b16 a[0:1], v2 offset:0\n" \
" ds_read_b64_tr_b16 a[2:3], v2 offset:512\n" \
" ds_read_b64_tr_b16 a[128:129], v3 offset:0\n" \
" ds_read_b64_tr_b16 a[130:131], v3 offset:512\n" \
" ds_read_b64_tr_b16 a[4:5], v2 offset:4096\n" \
" ds_read_b64_tr_b16 a[6:7], v2 offset:4608\n" \
" ds_read_b64_tr_b16 a[132:133], v3 offset:4096\n" \
" ds_read_b64_tr_b16 a[134:135], v3 offset:4608\n" \
" s_waitcnt lgkmcnt(0)\n" \
" s_barrier\n" \
" s_add_u32 m0, s52, 0\n" \
" s_nop 0\n" \
" buffer_load_dwordx4 v1, s[40:43], s54 offen nt lds\n" \
" s_add_u32 s54, s54, s44\n" \
" s_add_u32 m0, s52, 2048\n" \
" s_nop 0\n" \
" buffer_load_dwordx4 v1, s[40:43], s54 offen nt lds\n" \
" s_add_u32 s54, s54, s44\n" \
" s_add_u32 m0, s52, 4096\n" \
" s_nop 0\n" \
" buffer_load_dwordx4 v1, s[40:43], s54 offen nt lds\n" \
" s_add_u32 s54, s54, s44\n" \
" s_add_u32 m0, s52, 6144\n" \
" s_nop 0\n" \
" buffer_load_dwordx4 v1, s[40:43], s54 offen nt lds\n" \
" s_add_u32 s54, s54, s44\n" \
" s_barrier\n" \
" ds_read_b64_tr_b16 a[8:9], v2 offset:16384\n" \
" ds_read_b64_tr_b16 a[10:11], v2 offset:16896\n" \
" ds_read_b64_tr_b16 a[136:137], v3 offset:16384\n" \
" ds_read_b64_tr_b16 a[138:139], v3 offset:16896\n" \
" ds_read_b64_tr_b16 a[12:13], v2 offset:20480\n" \
" ds_read_b64_tr_b16 a[14:15], v2 offset:20992\n" \
" ds_read_b64_tr_b16 a[140:141], v3 offset:20480\n" \
" ds_read_b64_tr_b16 a[142:143], v3 offset:20992\n" \
" s_waitcnt lgkmcnt(0)\n" \
" s_barrier\n" \
" s_add_u32 m0, s52, 16384\n" \
" s_nop 0\n" \
" buffer_load_dwordx4 v1, s[40:43], s54 offen nt lds\n" \
" s_add_u32 s54, s54, s44\n" \
" s_add_u32 m0, s52, 18432\n" \
" s_nop 0\n" \
" buffer_load_dwordx4 v1, s[40:43], s54 offen nt lds\n" \
" s_add_u32 s54, s54, s44\n" \
" s_add_u32 m0, s52, 20480\n" \
" s_nop 0\n" \
" buffer_load_dwordx4 v1, s[40:43], s54 offen nt lds\n" \
" s_add_u32 s54, s54, s44\n" \
" s_add_u32 m0, s52, 22528\n" \
" s_nop 0\n" \
" buffer_load_dwordx4 v1, s[40:43], s54 offen nt lds\n" \
" s_add_u32 s54, s54, s44\n" \
" s_barrier\n" \
" ds_read_b64_tr_b16 a[16:17], v2 offset:32768\n" \
" ds_read_b64_tr_b16 a[18:19], v2 offset:33280\n" \
" ds_read_b64_tr_b16 a[144:145], v3 offset:32768\n" \
" ds_read_b64_tr_b16 a[146:147], v3 offset:33280\n" \
" ds_read_b64_tr_b16 a[20:21], v2 offset:36864\n" \
" ds_read_b64_tr_b16 a[22:23], v2 offset:37376\n" \
" ds_read_b64_tr_b16 a[148:149], v3 offset:36864\n" \
" ds_read_b64_tr_b16 a[150:151], v3 offset:37376\n" \
" s_waitcnt lgkmcnt(0)\n" \
" s_barrier\n" \
" s_add_u32 m0, s52, 32768\n" \
" s_nop 0\n" \
" buffer_load_dwordx4 v1, s[40:43], s54 offen nt lds\n" \
" s_add_u32 s54, s54, s44\n" \
" s_add_u32 m0, s52, 34816\n" \
" s_nop 0\n" \
" buffer_load_dwordx4 v1, s[40:43], s54 offen nt lds\n" \
" s_add_u32 s54, s54, s44\n" \
" s_add_u32 m0, s52, 36864\n" \
" s_nop 0\n" \
" buffer_load_dwordx4 v1, s[40:43], s54 offen nt lds\n" \
" s_add_u32 s54, s54, s44\n" \
" s_add_u32 m0, s52, 38912\n" \
" s_nop 0\n" \
" buffer_load_dwordx4 v1, s[40:43], s54 offen nt lds\n" \
" s_add_u32 s54, s54, s44\n" \
" s_barrier\n" \
" ds_read_b64_tr_b16 a[24:25], v2 offset:49152\n" \
" ds_read_b64_tr_b16 a[26:27], v2 offset:49664\n" \
" ds_read_b64_tr_b16 a[152:153], v3 offset:49152\n" \
" ds_read_b64_tr_b16 a[154:155], v3 offset:49664\n" \
" ds_read_b64_tr_b16 a[28:29], v2 offset:53248\n" \
" ds_read_b64_tr_b16 a[30:31], v2 offset:53760\n" \
" ds_read_b64_tr_b16 a[156:157], v3 offset:53248\n" \
" ds_read_b64_tr_b16 a[158:159], v3 offset:53760\n" \
" s_waitcnt lgkmcnt(0)\n" \
" s_barrier\n" \
" s_add_u32 m0, s52, 49152\n" \
" s_nop 0\n" \
" buffer_load_dwordx4 v1, s[40:43], s54 offen nt lds\n" \
" s_add_u32 s54, s54, s44\n" \
" s_add_u32 m0, s52, 51200\n" \
" s_nop 0\n" \
" buffer_load_dwordx4 v1, s[40:43], s54 offen nt lds\n" \
" s_add_u32 s54, s54, s44\n" \
" s_add_u32 m0, s52, 53248\n" \
" s_nop 0\n" \
" buffer_load_dwordx4 v1, s[40:43], s54 offen nt lds\n" \
" s_add_u32 s54, s54, s44\n" \
" s_add_u32 m0, s52, 55296\n" \
" s_nop 0\n" \
" buffer_load_dwordx4 v1, s[40:43], s54 offen nt lds\n" \
" s_add_u32 s54, s54, s44\n" \
" s_barrier\n" \
" ds_read_b64_tr_b16 a[32:33], v8 offset:0\n" \
" ds_read_b64_tr_b16 a[34:35], v8 offset:512\n" \
" ds_read_b64_tr_b16 a[160:161], v9 offset:0\n" \
" ds_read_b64_tr_b16 a[162:163], v9 offset:512\n" \
" ds_read_b64_tr_b16 a[36:37], v8 offset:4096\n" \
" ds_read_b64_tr_b16 a[38:39], v8 offset:4608\n" \
" ds_read_b64_tr_b16 a[164:165], v9 offset:4096\n" \
" ds_read_b64_tr_b16 a[166:167], v9 offset:4608\n" \
" s_waitcnt lgkmcnt(0)\n" \
" s_barrier\n" \
" s_add_u32 m0, s52, 65536\n" \
" s_nop 0\n" \
" buffer_load_dwordx4 v1, s[40:43], s54 offen nt lds\n" \
" s_add_u32 s54, s54, s44\n" \
" s_add_u32 m0, s52, 67584\n" \
" s_nop 0\n" \
" buffer_load_dwordx4 v1, s[40:43], s54 offen nt lds\n" \
" s_add_u32 s54, s54, s44\n" \
" s_add_u32 m0, s52, 69632\n" \
" s_nop 0\n" \
" buffer_load_dwordx4 v1, s[40:43], s54 offen nt lds\n" \
" s_add_u32 s54, s54, s44\n" \
" s_add_u32 m0, s52, 71680\n" \
" s_nop 0\n" \
" buffer_load_dwordx4 v1, s[40:43], s54 offen nt lds\n" \
" s_add_u32 s54, s54, s44\n" \
" s_barrier\n" \
" ds_read_b64_tr_b16 a[40:41], v8 offset:16384\n" \
" ds_read_b64_tr_b16 a[42:43], v8 offset:16896\n" \
" ds_read_b64_tr_b16 a[168:169], v9 offset:16384\n" \
" ds_read_b64_tr_b16 a[170:171], v9 offset:16896\n" \
" ds_read_b64_tr_b16 a[44:45], v8 offset:20480\n" \
" ds_read_b64_tr_b16 a[46:47], v8 offset:20992\n" \
" ds_read_b64_tr_b16 a[172:173], v9 offset:20480\n" \
" ds_read_b64_tr_b16 a[174:175], v9 offset:20992\n" \
" s_waitcnt lgkmcnt(0)\n" \
" s_barrier\n" \
" s_add_u32 m0, s52, 81920\n" \
" s_nop 0\n" \
" buffer_load_dwordx4 v1, s[40:43], s54 offen nt lds\n" \
" s_add_u32 s54, s54, s44\n" \
" s_add_u32 m0, s52, 83968\n" \
" s_nop 0\n" \
" buffer_load_dwordx4 v1, s[40:43], s54 offen nt lds\n" \
" s_add_u32 s54, s54, s44\n" \
" s_add_u32 m0, s52, 86016\n" \
" s_nop 0\n" \
" buffer_load_dwordx4 v1, s[40:43], s54 offen nt lds\n" \
" s_add_u32 s54, s54, s44\n" \
" s_add_u32 m0, s52, 88064\n" \
" s_nop 0\n" \
" buffer_load_dwordx4 v1, s[40:43], s54 offen nt lds\n" \
" s_add_u32 s54, s54, s44\n" \
" s_barrier\n" \
" ds_read_b64_tr_b16 a[48:49], v8 offset:32768\n" \
" ds_read_b64_tr_b16 a[50:51], v8 offset:33280\n" \
" ds_read_b64_tr_b16 a[176:177], v9 offset:32768\n" \
" ds_read_b64_tr_b16 a[178:179], v9 offset:33280\n" \
" ds_read_b64_tr_b16 a[52:53], v8 offset:36864\n" \
" ds_read_b64_tr_b16 a[54:55], v8 offset:37376\n" \
" ds_read_b64_tr_b16 a[180:181], v9 offset:36864\n" \
" ds_read_b64_tr_b16 a[182:183], v9 offset:37376\n" \
" s_waitcnt lgkmcnt(0)\n" \
" s_barrier\n" \
" s_add_u32 m0, s52, 98304\n" \
" s_nop 0\n" \
" buffer_load_dwordx4 v1, s[40:43], s54 offen nt lds\n" \
" s_add_u32 s54, s54, s44\n" \
" s_add_u32 m0, s52, 100352\n" \
" s_nop 0\n" \
" buffer_load_dwordx4 v1, s[40:43], s54 offen nt lds\n" \
" s_add_u32 s54, s54, s44\n" \
" s_add_u32 m0, s52, 102400\n" \
" s_nop 0\n" \
" buffer_load_dwordx4 v1, s[40:43], s54 offen nt lds\n" \
" s_add_u32 s54, s54, s44\n" \
" s_add_u32 m0, s52, 104448\n" \
" s_nop 0\n" \
" buffer_load_dwordx4 v1, s[40:43], s54 offen nt lds\n" \
" s_add_u32 s54, s54, s44\n" \
" s_barrier\n" \
" ds_read_b64_tr_b16 a[56:57], v8 offset:49152\n" \
" ds_read_b64_tr_b16 a[58:59], v8 offset:49664\n" \
" ds_read_b64_tr_b16 a[184:185], v9 offset:49152\n" \
" ds_read_b64_tr_b16 a[186:187], v9 offset:49664\n" \
" ds_read_b64_tr_b16 a[60:61], v8 offset:53248\n" \
" ds_read_b64_tr_b16 a[62:63], v8 offset:53760\n" \
" ds_read_b64_tr_b16 a[188:189], v9 offset:53248\n" \
" ds_read_b64_tr_b16 a[190:191], v9 offset:53760\n" \
" s_waitcnt lgkmcnt(0)\n" \
" s_barrier\n" \
" s_add_u32 m0, s52, 114688\n" \
" s_nop 0\n" \
" buffer_load_dwordx4 v1, s[40:43], s54 offen nt lds\n" \
" s_add_u32 s54, s54, s44\n" \
" s_add_u32 m0, s52, 116736\n" \
" s_nop 0\n" \
" buffer_load_dwordx4 v1, s[40:43], s54 offen nt lds\n" \
" s_add_u32 s54, s54, s44\n" \
" s_add_u32 m0, s52, 118784\n" \
" s_nop 0\n" \
" buffer_load_dwordx4 v1, s[40:43], s54 offen nt lds\n" \
" s_add_u32 s54, s54, s44\n" \
" s_add_u32 m0, s52, 120832\n" \
" s_nop 0\n" \
" buffer_load_dwordx4 v1, s[40:43], s54 offen nt lds\n" \
" s_add_u32 s54, s54, s44\n" \
" s_waitcnt vmcnt(28)\n" \
" s_barrier\n" \
" ds_read_b64_tr_b16 a[64:65], v2 offset:0\n" \
" ds_read_b64_tr_b16 a[66:67], v2 offset:512\n" \
" ds_read_b64_tr_b16 a[192:193], v3 offset:0\n" \
" ds_read_b64_tr_b16 a[194:195], v3 offset:512\n" \
" ds_read_b64_tr_b16 a[68:69], v2 offset:4096\n" \
" ds_read_b64_tr_b16 a[70:71], v2 offset:4608\n" \
" ds_read_b64_tr_b16 a[196:197], v3 offset:4096\n" \
" ds_read_b64_tr_b16 a[198:199], v3 offset:4608\n" \
" s_waitcnt vmcnt(24)\n" \
" s_barrier\n" \
" ds_read_b64_tr_b16 a[72:73], v2 offset:16384\n" \
" ds_read_b64_tr_b16 a[74:75], v2 offset:16896\n" \
" ds_read_b64_tr_b16 a[200:201], v3 offset:16384\n" \
" ds_read_b64_tr_b16 a[202:203], v3 offset:16896\n" \
" ds_read_b64_tr_b16 a[76:77], v2 offset:20480\n" \
" ds_read_b64_tr_b16 a[78:79], v2 offset:20992\n" \
" ds_read_b64_tr_b16 a[204:205], v3 offset:20480\n" \
" ds_read_b64_tr_b16 a[206:207], v3 offset:20992\n" \
" s_waitcnt vmcnt(20)\n" \
" s_barrier\n" \
" ds_read_b64_tr_b16 a[80:81], v2 offset:32768\n" \
" ds_read_b64_tr_b16 a[82:83], v2 offset:33280\n" \
" ds_read_b64_tr_b16 a[208:209], v3 offset:32768\n" \
" ds_read_b64_tr_b16 a[210:211], v3 offset:33280\n" \
" ds_read_b64_tr_b16 a[84:85], v2 offset:36864\n" \
" ds_read_b64_tr_b16 a[86:87], v2 offset:37376\n" \
" ds_read_b64_tr_b16 a[212:213], v3 offset:36864\n" \
" ds_read_b64_tr_b16 a[214:215], v3 offset:37376\n" \
" s_waitcnt vmcnt(16)\n" \
" s_barrier\n" \
" ds_read_b64_tr_b16 a[88:89], v2 offset:49152\n" \
" ds_read_b64_tr_b16 a[90:91], v2 offset:49664\n" \
" ds_read_b64_tr_b16 a[216:217], v3 offset:49152\n" \
" ds_read_b64_tr_b16 a[218:219], v3 offset:49664\n" \
" ds_read_b64_tr_b16 a[92:93], v2 offset:53248\n" \
" ds_read_b64_tr_b16 a[94:95], v2 offset:53760\n" \
" ds_read_b64_tr_b16 a[220:221], v3 offset:53248\n" \
" ds_read_b64_tr_b16 a[222:223], v3 offset:53760\n" \
" s_waitcnt vmcnt(12)\n" \
" s_barrier\n" \
" ds_read_b64_tr_b16 a[96:97], v8 offset:0\n" \
" ds_read_b64_tr_b16 a[98:99], v8 offset:512\n" \
" ds_read_b64_tr_b16 a[224:225], v9 offset:0\n" \
" ds_read_b64_tr_b16 a[226:227], v9 offset:512\n" \
" ds_read_b64_tr_b16 a[100:101], v8 offset:4096\n" \
" ds_read_b64_tr_b16 a[102:103], v8 offset:4608\n" \
" ds_read_b64_tr_b16 a[228:229], v9 offset:4096\n" \
" ds_read_b64_tr_b16 a[230:231], v9 offset:4608\n" \
" s_waitcnt vmcnt(8)\n" \
" s_barrier\n" \
" ds_read_b64_tr_b16 a[104:105], v8 offset:16384\n" \
" ds_read_b64_tr_b16 a[106:107], v8 offset:16896\n" \
" ds_read_b64_tr_b16 a[232:233], v9 offset:16384\n" \
" ds_read_b64_tr_b16 a[234:235], v9 offset:16896\n" \
" ds_read_b64_tr_b16 a[108:109], v8 offset:20480\n" \
" ds_read_b64_tr_b16 a[110:111], v8 offset:20992\n" \
" ds_read_b64_tr_b16 a[236:237], v9 offset:20480\n" \
" ds_read_b64_tr_b16 a[238:239], v9 offset:20992\n" \
" s_waitcnt vmcnt(4)\n" \
" s_barrier\n" \
" ds_read_b64_tr_b16 a[112:113], v8 offset:32768\n" \
" ds_read_b64_tr_b16 a[114:115], v8 offset:33280\n" \
" ds_read_b64_tr_b16 a[240:241], v9 offset:32768\n" \
" ds_read_b64_tr_b16 a[242:243], v9 offset:33280\n" \
" ds_read_b64_tr_b16 a[116:117], v8 offset:36864\n" \
" ds_read_b64_tr_b16 a[118:119], v8 offset:37376\n" \
" ds_read_b64_tr_b16 a[244:245], v9 offset:36864\n" \
" ds_read_b64_tr_b16 a[246:247], v9 offset:37376\n" \
" s_waitcnt vmcnt(0)\n" \
" s_barrier\n" \
" ds_read_b64_tr_b16 a[120:121], v8 offset:49152\n" \
" ds_read_b64_tr_b16 a[122:123], v8 offset:49664\n" \
" ds_read_b64_tr_b16 a[248:249], v9 offset:49152\n" \
" ds_read_b64_tr_b16 a[250:251], v9 offset:49664\n" \
" ds_read_b64_tr_b16 a[124:125], v8 offset:53248\n" \
" ds_read_b64_tr_b16 a[126:127], v8 offset:53760\n" \
" ds_read_b64_tr_b16 a[252:253], v9 offset:53248\n" \
" ds_read_b64_tr_b16 a[254:255], v9 offset:53760\n" \
" s_waitcnt lgkmcnt(0)\n" \
" s_barrier\n" \
" v_and_b32 v14, 48, v0\n" \
" v_lshlrev_b32 v14, 3, v14\n" \
" v_add_u32 v14, 4096, v14\n" \
" s_cmp_eq_u32 s46, 0\n" \
" s_cbranch_scc1 Lp1v_l0\n" \
" s_branch Lp1v_l1\n" \
"Lp1v_lk:\n" \
" s_lshl_b32 s55, s48, 13\n" \
" s_lshl_b32 s56, s46, 16\n" \
" s_lshl_b32 s60, s47, 16\n" \
" s_sub_u32 s60, s60, 0x10000\n" \
" s_mul_i32 s57, s46, s49\n" \
" s_sub_u32 s57, s57, s49\n" \
" s_add_u32 s58, s57, s50\n" \
" v_mov_b32 v7, v5\n" \
" v_mov_b32 v5, 0x7ffffff0\n" \
" v_mov_b32 v10, v0\n" \
" v_mov_b32 v11, v6\n" \
" v_mov_b32 v6, 0x7ffffff0\n" \
" s_mov_b32 s59, s46\n" \
" s_min_u32 s61, s56, s60\n" \
" s_add_u32 m0, s55, 0\n" \
" s_nop 0\n" \
" buffer_load_dwordx4 v4, s[36:39], s61 offen offset:0 sc1 lds\n" \
" buffer_load_dwordx4 v4, s[36:39], s61 offen offset:1024 sc1 lds\n" \
" buffer_load_dwordx4 v4, s[36:39], s61 offen offset:2048 sc1 lds\n" \
" buffer_load_dwordx4 v4, s[36:39], s61 offen offset:3072 sc1 lds\n" \
" s_add_u32 m0, s55, 4096\n" \
" s_add_u32 s61, s61, 4096\n" \
" buffer_load_dwordx4 v4, s[36:39], s61 offen offset:0 sc1 lds\n" \
" buffer_load_dwordx4 v4, s[36:39], s61 offen offset:1024 sc1 lds\n" \
" buffer_load_dwordx4 v4, s[36:39], s61 offen offset:2048 sc1 lds\n" \
" buffer_load_dwordx4 v4, s[36:39], s61 offen offset:3072 sc1 lds\n" \
" s_min_u32 s61, s56, s60\n" \
" s_add_u32 s61, s61, 16384\n" \
" s_add_u32 s56, s56, 0x10000\n" \
" s_add_u32 m0, s55, 32768\n" \
" s_nop 0\n" \
" buffer_load_dwordx4 v4, s[36:39], s61 offen offset:0 sc1 lds\n" \
" buffer_load_dwordx4 v4, s[36:39], s61 offen offset:1024 sc1 lds\n" \
" buffer_load_dwordx4 v4, s[36:39], s61 offen offset:2048 sc1 lds\n" \
" buffer_load_dwordx4 v4, s[36:39], s61 offen offset:3072 sc1 lds\n" \
" s_add_u32 m0, s55, 36864\n" \
" s_add_u32 s61, s61, 4096\n" \
" buffer_load_dwordx4 v4, s[36:39], s61 offen offset:0 sc1 lds\n" \
" buffer_load_dwordx4 v4, s[36:39], s61 offen offset:1024 sc1 lds\n" \
" buffer_load_dwordx4 v4, s[36:39], s61 offen offset:2048 sc1 lds\n" \
" buffer_load_dwordx4 v4, s[36:39], s61 offen offset:3072 sc1 lds\n" \
" s_mov_b32 s70, 0\n" \
" s_mov_b32 s72, 0\n" \
" s_waitcnt vmcnt(0) lgkmcnt(0)\n" \
" s_barrier\n" \
" v_add_u32 v12, 0x10000, v10\n" \
" ds_read_b128 v[16:19], v10 offset:0\n" \
" ds_read_b128 v[20:23], v10 offset:16384\n" \
" ds_read_b128 v[24:27], v10 offset:1024\n" \
" ds_read_b128 v[28:31], v10 offset:17408\n" \
" ds_read_b128 v[32:35], v10 offset:2048\n" \
" ds_read_b128 v[36:39], v10 offset:18432\n" \
" s_waitcnt lgkmcnt(4)\n" \
" s_nop 7\n" \
" s_nop 7\n" \
".p2align 6\n" \
"Lp1v_loop:\n" \
" v_mfma_f32_16x16x32_bf16 v[48:51], v[16:19], a[0:3], 0\n" \
" s_min_u32 s61, s56, s60\n" \
" s_add_u32 m0, s55, 65536\n" \
" s_nop 0\n" \
" buffer_load_dwordx4 v4, s[36:39], s61 offen offset:0 sc1 lds\n" \
" v_mov_b32_e64 v13, s59\n" \
" v_mfma_f32_16x16x32_bf16 v[52:55], v[20:23], a[0:3], 0\n" \
" ds_read_b128 v[40:43], v10 offset:3072\n" \
" s_nop 0\n" \
" v_mfma_f32_16x16x32_bf16 v[56:59], v[16:19], a[128:131], 0\n" \
" ds_read_b128 v[44:47], v10 offset:19456\n" \
" s_nop 0\n" \
" v_mfma_f32_16x16x32_bf16 v[60:63], v[20:23], a[128:131], 0\n" \
" s_waitcnt lgkmcnt(4)\n" \
" v_cvt_pk_bf16_f32 v80, v64, v65\n" \
" v_cvt_pk_bf16_f32 v81, v66, v67\n" \
" v_mfma_f32_16x16x32_bf16 v[48:51], v[24:27], a[4:7], v[48:51]\n" \
" buffer_load_dwordx4 v4, s[36:39], s61 offen offset:1024 sc1 lds\n" \
" v_mfma_f32_16x16x32_bf16 v[52:55], v[28:31], a[4:7], v[52:55]\n" \
" ds_read_b128 v[16:19], v10 offset:4096\n" \
" v_cvt_pk_bf16_f32 v82, v68, v69\n" \
" v_mfma_f32_16x16x32_bf16 v[56:59], v[24:27], a[132:135], v[56:59]\n" \
" ds_read_b128 v[20:23], v10 offset:20480\n" \
" v_cvt_pk_bf16_f32 v83, v70, v71\n" \
" v_mfma_f32_16x16x32_bf16 v[60:63], v[28:31], a[132:135], v[60:63]\n" \
" s_waitcnt lgkmcnt(4)\n" \
" buffer_store_dwordx4 v[80:83], v5, s[36:39], s57 offen sc1\n" \
" v_cvt_pk_bf16_f32 v84, v72, v73\n" \
" v_mfma_f32_16x16x32_bf16 v[48:51], v[32:35], a[8:11], v[48:51]\n" \
" v_cvt_pk_bf16_f32 v85, v74, v75\n" \
" v_cvt_pk_bf16_f32 v86, v76, v77\n" \
" v_mfma_f32_16x16x32_bf16 v[52:55], v[36:39], a[8:11], v[52:55]\n" \
" ds_read_b128 v[24:27], v10 offset:5120\n" \
" v_cvt_pk_bf16_f32 v87, v78, v79\n" \
" v_mfma_f32_16x16x32_bf16 v[56:59], v[32:35], a[136:139], v[56:59]\n" \
" ds_read_b128 v[28:31], v10 offset:21504\n" \
" buffer_store_dwordx4 v[84:87], v5, s[36:39], s58 offen sc1\n" \
" v_mfma_f32_16x16x32_bf16 v[60:63], v[36:39], a[136:139], v[60:63]\n" \
" s_waitcnt lgkmcnt(4)\n" \
" v_mfma_f32_16x16x32_bf16 v[48:51], v[40:43], a[12:15], v[48:51]\n" \
" buffer_load_dwordx4 v4, s[36:39], s61 offen offset:2048 sc1 lds\n" \
" v_mfma_f32_16x16x32_bf16 v[52:55], v[44:47], a[12:15], v[52:55]\n" \
" ds_read_b128 v[32:35], v10 offset:6144\n" \
" v_mfma_f32_16x16x32_bf16 v[56:59], v[40:43], a[140:143], v[56:59]\n" \
" ds_read_b128 v[36:39], v10 offset:22528\n" \
" v_mfma_f32_16x16x32_bf16 v[60:63], v[44:47], a[140:143], v[60:63]\n" \
" s_waitcnt lgkmcnt(4)\n" \
" v_mfma_f32_16x16x32_bf16 v[48:51], v[16:19], a[16:19], v[48:51]\n" \
" buffer_load_dwordx4 v4, s[36:39], s61 offen offset:3072 sc1 lds\n" \
" v_mfma_f32_16x16x32_bf16 v[52:55], v[20:23], a[16:19], v[52:55]\n" \
" ds_read_b128 v[40:43], v10 offset:7168\n" \
" v_mfma_f32_16x16x32_bf16 v[56:59], v[16:19], a[144:147], v[56:59]\n" \
" ds_read_b128 v[44:47], v10 offset:23552\n" \
" v_mfma_f32_16x16x32_bf16 v[60:63], v[20:23], a[144:147], v[60:63]\n" \
" s_waitcnt lgkmcnt(4)\n" \
" v_mfma_f32_16x16x32_bf16 v[48:51], v[24:27], a[20:23], v[48:51]\n" \
" v_mfma_f32_16x16x32_bf16 v[52:55], v[28:31], a[20:23], v[52:55]\n" \
" ds_read_b128 v[16:19], v10 offset:8192\n" \
" v_mfma_f32_16x16x32_bf16 v[56:59], v[24:27], a[148:151], v[56:59]\n" \
" ds_read_b128 v[20:23], v10 offset:24576\n" \
" v_mfma_f32_16x16x32_bf16 v[60:63], v[28:31], a[148:151], v[60:63]\n" \
" s_waitcnt lgkmcnt(4)\n" \
" v_mfma_f32_16x16x32_bf16 v[48:51], v[32:35], a[24:27], v[48:51]\n" \
" s_add_u32 m0, s55, 69632\n" \
" s_add_u32 s61, s61, 4096\n" \
" buffer_load_dwordx4 v4, s[36:39], s61 offen offset:0 sc1 lds\n" \
" v_mfma_f32_16x16x32_bf16 v[52:55], v[36:39], a[24:27], v[52:55]\n" \
" ds_read_b128 v[24:27], v10 offset:9216\n" \
" v_mfma_f32_16x16x32_bf16 v[56:59], v[32:35], a[152:155], v[56:59]\n" \
" ds_read_b128 v[28:31], v10 offset:25600\n" \
" v_mfma_f32_16x16x32_bf16 v[60:63], v[36:39], a[152:155], v[60:63]\n" \
" s_waitcnt lgkmcnt(4)\n" \
" v_mfma_f32_16x16x32_bf16 v[48:51], v[40:43], a[28:31], v[48:51]\n" \
" buffer_load_dwordx4 v4, s[36:39], s61 offen offset:1024 sc1 lds\n" \
" v_mfma_f32_16x16x32_bf16 v[52:55], v[44:47], a[28:31], v[52:55]\n" \
" ds_read_b128 v[32:35], v10 offset:10240\n" \
" v_mfma_f32_16x16x32_bf16 v[56:59], v[40:43], a[156:159], v[56:59]\n" \
" ds_read_b128 v[36:39], v10 offset:26624\n" \
" v_mfma_f32_16x16x32_bf16 v[60:63], v[44:47], a[156:159], v[60:63]\n" \
" s_waitcnt lgkmcnt(4)\n" \
" v_mfma_f32_16x16x32_bf16 v[48:51], v[16:19], a[32:35], v[48:51]\n" \
" v_mfma_f32_16x16x32_bf16 v[52:55], v[20:23], a[32:35], v[52:55]\n" \
" ds_read_b128 v[40:43], v10 offset:11264\n" \
" v_mfma_f32_16x16x32_bf16 v[56:59], v[16:19], a[160:163], v[56:59]\n" \
" ds_read_b128 v[44:47], v10 offset:27648\n" \
" v_mfma_f32_16x16x32_bf16 v[60:63], v[20:23], a[160:163], v[60:63]\n" \
" s_waitcnt lgkmcnt(4)\n" \
" v_mfma_f32_16x16x32_bf16 v[48:51], v[24:27], a[36:39], v[48:51]\n" \
" buffer_load_dwordx4 v4, s[36:39], s61 offen offset:2048 sc1 lds\n" \
" v_mfma_f32_16x16x32_bf16 v[52:55], v[28:31], a[36:39], v[52:55]\n" \
" ds_read_b128 v[16:19], v10 offset:12288\n" \
" v_mfma_f32_16x16x32_bf16 v[56:59], v[24:27], a[164:167], v[56:59]\n" \
" ds_read_b128 v[20:23], v10 offset:28672\n" \
" v_mfma_f32_16x16x32_bf16 v[60:63], v[28:31], a[164:167], v[60:63]\n" \
" s_waitcnt lgkmcnt(4)\n" \
" v_mfma_f32_16x16x32_bf16 v[48:51], v[32:35], a[40:43], v[48:51]\n" \
" buffer_load_dwordx4 v4, s[36:39], s61 offen offset:3072 sc1 lds\n" \
" v_mfma_f32_16x16x32_bf16 v[52:55], v[36:39], a[40:43], v[52:55]\n" \
" ds_read_b128 v[24:27], v10 offset:13312\n" \
" v_mfma_f32_16x16x32_bf16 v[56:59], v[32:35], a[168:171], v[56:59]\n" \
" ds_read_b128 v[28:31], v10 offset:29696\n" \
" v_mfma_f32_16x16x32_bf16 v[60:63], v[36:39], a[168:171], v[60:63]\n" \
" s_waitcnt lgkmcnt(4)\n" \
" v_mfma_f32_16x16x32_bf16 v[48:51], v[40:43], a[44:47], v[48:51]\n" \
" v_mfma_f32_16x16x32_bf16 v[52:55], v[44:47], a[44:47], v[52:55]\n" \
" ds_read_b128 v[32:35], v10 offset:14336\n" \
" v_mfma_f32_16x16x32_bf16 v[56:59], v[40:43], a[172:175], v[56:59]\n" \
" ds_read_b128 v[36:39], v10 offset:30720\n" \
" v_mfma_f32_16x16x32_bf16 v[60:63], v[44:47], a[172:175], v[60:63]\n" \
" s_waitcnt lgkmcnt(4)\n" \
" v_mfma_f32_16x16x32_bf16 v[48:51], v[16:19], a[48:51], v[48:51]\n" \
" v_mfma_f32_16x16x32_bf16 v[52:55], v[20:23], a[48:51], v[52:55]\n" \
" ds_read_b128 v[40:43], v10 offset:15360\n" \
" v_mfma_f32_16x16x32_bf16 v[56:59], v[16:19], a[176:179], v[56:59]\n" \
" ds_read_b128 v[44:47], v10 offset:31744\n" \
" v_mfma_f32_16x16x32_bf16 v[60:63], v[20:23], a[176:179], v[60:63]\n" \
" s_waitcnt lgkmcnt(4)\n" \
" v_mfma_f32_16x16x32_bf16 v[48:51], v[24:27], a[52:55], v[48:51]\n" \
" s_waitcnt vmcnt(11)\n" \
" s_barrier\n" \
" v_mfma_f32_16x16x32_bf16 v[52:55], v[28:31], a[52:55], v[52:55]\n" \
" ds_read_b128 v[16:19], v10 offset:32768\n" \
" v_mfma_f32_16x16x32_bf16 v[56:59], v[24:27], a[180:183], v[56:59]\n" \
" ds_read_b128 v[20:23], v10 offset:49152\n" \
" v_mfma_f32_16x16x32_bf16 v[60:63], v[28:31], a[180:183], v[60:63]\n" \
" s_waitcnt lgkmcnt(4)\n" \
" v_mfma_f32_16x16x32_bf16 v[48:51], v[32:35], a[56:59], v[48:51]\n" \
" v_mfma_f32_16x16x32_bf16 v[52:55], v[36:39], a[56:59], v[52:55]\n" \
" ds_read_b128 v[24:27], v10 offset:33792\n" \
" v_mfma_f32_16x16x32_bf16 v[56:59], v[32:35], a[184:187], v[56:59]\n" \
" ds_read_b128 v[28:31], v10 offset:50176\n" \
" v_mfma_f32_16x16x32_bf16 v[60:63], v[36:39], a[184:187], v[60:63]\n" \
" s_waitcnt lgkmcnt(4)\n" \
" v_mfma_f32_16x16x32_bf16 v[48:51], v[40:43], a[60:63], v[48:51]\n" \
" v_mfma_f32_16x16x32_bf16 v[52:55], v[44:47], a[60:63], v[52:55]\n" \
" ds_read_b128 v[32:35], v10 offset:34816\n" \
" v_mfma_f32_16x16x32_bf16 v[56:59], v[40:43], a[188:191], v[56:59]\n" \
" ds_read_b128 v[36:39], v10 offset:51200\n" \
" v_mfma_f32_16x16x32_bf16 v[60:63], v[44:47], a[188:191], v[60:63]\n" \
" s_waitcnt lgkmcnt(4)\n" \
" v_mfma_f32_16x16x32_bf16 v[48:51], v[16:19], a[64:67], v[48:51]\n" \
" s_min_u32 s61, s56, s60\n" \
" s_add_u32 s61, s61, 16384\n" \
" s_add_u32 s56, s56, 0x10000\n" \
" s_add_u32 m0, s55, 98304\n" \
" s_nop 0\n" \
" buffer_load_dwordx4 v4, s[36:39], s61 offen offset:0 sc1 lds\n" \
" v_mfma_f32_16x16x32_bf16 v[52:55], v[20:23], a[64:67], v[52:55]\n" \
" ds_read_b128 v[40:43], v10 offset:35840\n" \
" v_mfma_f32_16x16x32_bf16 v[56:59], v[16:19], a[192:195], v[56:59]\n" \
" ds_read_b128 v[44:47], v10 offset:52224\n" \
" v_mfma_f32_16x16x32_bf16 v[60:63], v[20:23], a[192:195], v[60:63]\n" \
" s_waitcnt lgkmcnt(4)\n" \
" v_mfma_f32_16x16x32_bf16 v[48:51], v[24:27], a[68:71], v[48:51]\n" \
" buffer_load_dwordx4 v4, s[36:39], s61 offen offset:1024 sc1 lds\n" \
" v_mfma_f32_16x16x32_bf16 v[52:55], v[28:31], a[68:71], v[52:55]\n" \
" ds_read_b128 v[16:19], v10 offset:36864\n" \
" v_mfma_f32_16x16x32_bf16 v[56:59], v[24:27], a[196:199], v[56:59]\n" \
" ds_read_b128 v[20:23], v10 offset:53248\n" \
" v_mfma_f32_16x16x32_bf16 v[60:63], v[28:31], a[196:199], v[60:63]\n" \
" s_waitcnt lgkmcnt(4)\n" \
" v_mfma_f32_16x16x32_bf16 v[48:51], v[32:35], a[72:75], v[48:51]\n" \
" v_mfma_f32_16x16x32_bf16 v[52:55], v[36:39], a[72:75], v[52:55]\n" \
" ds_read_b128 v[24:27], v10 offset:37888\n" \
" v_mfma_f32_16x16x32_bf16 v[56:59], v[32:35], a[200:203], v[56:59]\n" \
" ds_read_b128 v[28:31], v10 offset:54272\n" \
" v_mfma_f32_16x16x32_bf16 v[60:63], v[36:39], a[200:203], v[60:63]\n" \
" s_waitcnt lgkmcnt(4)\n" \
" v_mfma_f32_16x16x32_bf16 v[48:51], v[40:43], a[76:79], v[48:51]\n" \
" buffer_load_dwordx4 v4, s[36:39], s61 offen offset:2048 sc1 lds\n" \
" v_mfma_f32_16x16x32_bf16 v[52:55], v[44:47], a[76:79], v[52:55]\n" \
" ds_read_b128 v[32:35], v10 offset:38912\n" \
" v_mfma_f32_16x16x32_bf16 v[56:59], v[40:43], a[204:207], v[56:59]\n" \
" ds_read_b128 v[36:39], v10 offset:55296\n" \
" v_mfma_f32_16x16x32_bf16 v[60:63], v[44:47], a[204:207], v[60:63]\n" \
" s_waitcnt lgkmcnt(4)\n" \
" v_mfma_f32_16x16x32_bf16 v[48:51], v[16:19], a[80:83], v[48:51]\n" \
" buffer_load_dwordx4 v4, s[36:39], s61 offen offset:3072 sc1 lds\n" \
" v_mfma_f32_16x16x32_bf16 v[52:55], v[20:23], a[80:83], v[52:55]\n" \
" ds_read_b128 v[40:43], v10 offset:39936\n" \
" v_mfma_f32_16x16x32_bf16 v[56:59], v[16:19], a[208:211], v[56:59]\n" \
" ds_read_b128 v[44:47], v10 offset:56320\n" \
" v_mfma_f32_16x16x32_bf16 v[60:63], v[20:23], a[208:211], v[60:63]\n" \
" s_waitcnt lgkmcnt(4)\n" \
" v_mfma_f32_16x16x32_bf16 v[48:51], v[24:27], a[84:87], v[48:51]\n" \
" v_mfma_f32_16x16x32_bf16 v[52:55], v[28:31], a[84:87], v[52:55]\n" \
" ds_read_b128 v[16:19], v10 offset:40960\n" \
" v_mfma_f32_16x16x32_bf16 v[56:59], v[24:27], a[212:215], v[56:59]\n" \
" ds_read_b128 v[20:23], v10 offset:57344\n" \
" v_mfma_f32_16x16x32_bf16 v[60:63], v[28:31], a[212:215], v[60:63]\n" \
" s_waitcnt lgkmcnt(4)\n" \
" v_mfma_f32_16x16x32_bf16 v[48:51], v[32:35], a[88:91], v[48:51]\n" \
" s_add_u32 m0, s55, 102400\n" \
" s_add_u32 s61, s61, 4096\n" \
" buffer_load_dwordx4 v4, s[36:39], s61 offen offset:0 sc1 lds\n" \
" v_mfma_f32_16x16x32_bf16 v[52:55], v[36:39], a[88:91], v[52:55]\n" \
" ds_read_b128 v[24:27], v10 offset:41984\n" \
" v_mfma_f32_16x16x32_bf16 v[56:59], v[32:35], a[216:219], v[56:59]\n" \
" ds_read_b128 v[28:31], v10 offset:58368\n" \
" v_mfma_f32_16x16x32_bf16 v[60:63], v[36:39], a[216:219], v[60:63]\n" \
" s_waitcnt lgkmcnt(4)\n" \
" v_mfma_f32_16x16x32_bf16 v[48:51], v[40:43], a[92:95], v[48:51]\n" \
" buffer_load_dwordx4 v4, s[36:39], s61 offen offset:1024 sc1 lds\n" \
" v_mfma_f32_16x16x32_bf16 v[52:55], v[44:47], a[92:95], v[52:55]\n" \
" ds_read_b128 v[32:35], v10 offset:43008\n" \
" v_mfma_f32_16x16x32_bf16 v[56:59], v[40:43], a[220:223], v[56:59]\n" \
" ds_read_b128 v[36:39], v10 offset:59392\n" \
" v_mfma_f32_16x16x32_bf16 v[60:63], v[44:47], a[220:223], v[60:63]\n" \
" s_waitcnt lgkmcnt(4)\n" \
" v_mfma_f32_16x16x32_bf16 v[48:51], v[16:19], a[96:99], v[48:51]\n" \
" v_mfma_f32_16x16x32_bf16 v[52:55], v[20:23], a[96:99], v[52:55]\n" \
" ds_read_b128 v[40:43], v10 offset:44032\n" \
" v_mfma_f32_16x16x32_bf16 v[56:59], v[16:19], a[224:227], v[56:59]\n" \
" ds_read_b128 v[44:47], v10 offset:60416\n" \
" v_mfma_f32_16x16x32_bf16 v[60:63], v[20:23], a[224:227], v[60:63]\n" \
" s_waitcnt lgkmcnt(4)\n" \
" v_mfma_f32_16x16x32_bf16 v[48:51], v[24:27], a[100:103], v[48:51]\n" \
" buffer_load_dwordx4 v4, s[36:39], s61 offen offset:2048 sc1 lds\n" \
" v_mfma_f32_16x16x32_bf16 v[52:55], v[28:31], a[100:103], v[52:55]\n" \
" ds_read_b128 v[16:19], v10 offset:45056\n" \
" v_mfma_f32_16x16x32_bf16 v[56:59], v[24:27], a[228:231], v[56:59]\n" \
" ds_read_b128 v[20:23], v10 offset:61440\n" \
" v_mfma_f32_16x16x32_bf16 v[60:63], v[28:31], a[228:231], v[60:63]\n" \
" s_waitcnt lgkmcnt(4)\n" \
" v_mfma_f32_16x16x32_bf16 v[48:51], v[32:35], a[104:107], v[48:51]\n" \
" buffer_load_dwordx4 v4, s[36:39], s61 offen offset:3072 sc1 lds\n" \
" v_mfma_f32_16x16x32_bf16 v[52:55], v[36:39], a[104:107], v[52:55]\n" \
" ds_read_b128 v[24:27], v10 offset:46080\n" \
" v_mfma_f32_16x16x32_bf16 v[56:59], v[32:35], a[232:235], v[56:59]\n" \
" ds_read_b128 v[28:31], v10 offset:62464\n" \
" v_mfma_f32_16x16x32_bf16 v[60:63], v[36:39], a[232:235], v[60:63]\n" \
" s_waitcnt lgkmcnt(4)\n" \
" v_mfma_f32_16x16x32_bf16 v[48:51], v[40:43], a[108:111], v[48:51]\n" \
" v_mfma_f32_16x16x32_bf16 v[52:55], v[44:47], a[108:111], v[52:55]\n" \
" ds_read_b128 v[32:35], v10 offset:47104\n" \
" v_mfma_f32_16x16x32_bf16 v[56:59], v[40:43], a[236:239], v[56:59]\n" \
" ds_read_b128 v[36:39], v10 offset:63488\n" \
" v_mfma_f32_16x16x32_bf16 v[60:63], v[44:47], a[236:239], v[60:63]\n" \
" s_waitcnt lgkmcnt(4)\n" \
" v_mfma_f32_16x16x32_bf16 v[48:51], v[16:19], a[112:115], v[48:51]\n" \
" v_mfma_f32_16x16x32_bf16 v[52:55], v[20:23], a[112:115], v[52:55]\n" \
" ds_read_b128 v[40:43], v10 offset:48128\n" \
" v_mfma_f32_16x16x32_bf16 v[56:59], v[16:19], a[240:243], v[56:59]\n" \
" ds_read_b128 v[44:47], v10 offset:64512\n" \
" v_mfma_f32_16x16x32_bf16 v[60:63], v[20:23], a[240:243], v[60:63]\n" \
" s_waitcnt lgkmcnt(4)\n" \
" v_mfma_f32_16x16x32_bf16 v[48:51], v[24:27], a[116:119], v[48:51]\n" \
" s_waitcnt vmcnt(8)\n" \
" s_barrier\n" \
" v_mfma_f32_16x16x32_bf16 v[52:55], v[28:31], a[116:119], v[52:55]\n" \
" ds_read_b128 v[16:19], v12 offset:0\n" \
" v_mfma_f32_16x16x32_bf16 v[56:59], v[24:27], a[244:247], v[56:59]\n" \
" ds_read_b128 v[20:23], v12 offset:16384\n" \
" v_mfma_f32_16x16x32_bf16 v[60:63], v[28:31], a[244:247], v[60:63]\n" \
" s_waitcnt lgkmcnt(4)\n" \
" buffer_store_dword v13, v6, s[36:39], 0 offen sc1\n" \
" v_mfma_f32_16x16x32_bf16 v[48:51], v[32:35], a[120:123], v[48:51]\n" \
" v_mfma_f32_16x16x32_bf16 v[52:55], v[36:39], a[120:123], v[52:55]\n" \
" ds_read_b128 v[24:27], v12 offset:1024\n" \
" v_mfma_f32_16x16x32_bf16 v[56:59], v[32:35], a[248:251], v[56:59]\n" \
" ds_read_b128 v[28:31], v12 offset:17408\n" \
" v_mfma_f32_16x16x32_bf16 v[60:63], v[36:39], a[248:251], v[60:63]\n" \
" s_waitcnt lgkmcnt(4)\n" \
" v_mfma_f32_16x16x32_bf16 v[48:51], v[40:43], a[124:127], v[48:51]\n" \
" v_mfma_f32_16x16x32_bf16 v[52:55], v[44:47], a[124:127], v[52:55]\n" \
" ds_read_b128 v[32:35], v12 offset:2048\n" \
" v_mfma_f32_16x16x32_bf16 v[56:59], v[40:43], a[252:255], v[56:59]\n" \
" ds_read_b128 v[36:39], v12 offset:18432\n" \
" v_mfma_f32_16x16x32_bf16 v[60:63], v[44:47], a[252:255], v[60:63]\n" \
" s_waitcnt lgkmcnt(4)\n" \
" s_add_u32 s57, s57, s49\n" \
" s_add_u32 s58, s58, s49\n" \
" s_add_u32 s59, s59, 1\n" \
" v_mov_b32 v5, v7\n" \
" v_mov_b32 v6, v11\n" \
" s_and_b32 s62, s59, -2\n" \
" s_cmp_eq_u32 s62, 12\n" \
" s_cbranch_scc1 Lp1v_l2\n" \
"Lp1v_lm:\n" \
" s_cmp_ge_u32 s59, s47\n" \
" s_cbranch_scc1 Lp1v_out0\n" \
" v_mfma_f32_16x16x32_bf16 v[64:67], v[16:19], a[0:3], 0\n" \
" s_min_u32 s61, s56, s60\n" \
" s_add_u32 m0, s55, 0\n" \
" s_nop 0\n" \
" buffer_load_dwordx4 v4, s[36:39], s61 offen offset:0 sc1 lds\n" \
" v_mov_b32 v13, s59\n" \
" v_mfma_f32_16x16x32_bf16 v[68:71], v[20:23], a[0:3], 0\n" \
" ds_read_b128 v[40:43], v12 offset:3072\n" \
" s_nop 0\n" \
" v_mfma_f32_16x16x32_bf16 v[72:75], v[16:19], a[128:131], 0\n" \
" ds_read_b128 v[44:47], v12 offset:19456\n" \
" s_nop 0\n" \
" v_mfma_f32_16x16x32_bf16 v[76:79], v[20:23], a[128:131], 0\n" \
" s_waitcnt lgkmcnt(4)\n" \
" v_cvt_pk_bf16_f32 v80, v48, v49\n" \
" v_cvt_pk_bf16_f32 v81, v50, v51\n" \
" v_mfma_f32_16x16x32_bf16 v[64:67], v[24:27], a[4:7], v[64:67]\n" \
" buffer_load_dwordx4 v4, s[36:39], s61 offen offset:1024 sc1 lds\n" \
" v_mfma_f32_16x16x32_bf16 v[68:71], v[28:31], a[4:7], v[68:71]\n" \
" ds_read_b128 v[16:19], v12 offset:4096\n" \
" v_cvt_pk_bf16_f32 v82, v52, v53\n" \
" v_mfma_f32_16x16x32_bf16 v[72:75], v[24:27], a[132:135], v[72:75]\n" \
" ds_read_b128 v[20:23], v12 offset:20480\n" \
" v_cvt_pk_bf16_f32 v83, v54, v55\n" \
" v_mfma_f32_16x16x32_bf16 v[76:79], v[28:31], a[132:135], v[76:79]\n" \
" s_waitcnt lgkmcnt(4)\n" \
" buffer_store_dwordx4 v[80:83], v5, s[36:39], s57 offen sc1\n" \
" v_cvt_pk_bf16_f32 v84, v56, v57\n" \
" v_mfma_f32_16x16x32_bf16 v[64:67], v[32:35], a[8:11], v[64:67]\n" \
" v_cvt_pk_bf16_f32 v85, v58, v59\n" \
" v_cvt_pk_bf16_f32 v86, v60, v61\n" \
" v_mfma_f32_16x16x32_bf16 v[68:71], v[36:39], a[8:11], v[68:71]\n" \
" ds_read_b128 v[24:27], v12 offset:5120\n" \
" v_cvt_pk_bf16_f32 v87, v62, v63\n" \
" v_mfma_f32_16x16x32_bf16 v[72:75], v[32:35], a[136:139], v[72:75]\n" \
" ds_read_b128 v[28:31], v12 offset:21504\n" \
" buffer_store_dwordx4 v[84:87], v5, s[36:39], s58 offen sc1\n" \
" v_mfma_f32_16x16x32_bf16 v[76:79], v[36:39], a[136:139], v[76:79]\n" \
" s_waitcnt lgkmcnt(4)\n" \
" v_mfma_f32_16x16x32_bf16 v[64:67], v[40:43], a[12:15], v[64:67]\n" \
" buffer_load_dwordx4 v4, s[36:39], s61 offen offset:2048 sc1 lds\n" \
" v_mfma_f32_16x16x32_bf16 v[68:71], v[44:47], a[12:15], v[68:71]\n" \
" ds_read_b128 v[32:35], v12 offset:6144\n" \
" v_mfma_f32_16x16x32_bf16 v[72:75], v[40:43], a[140:143], v[72:75]\n" \
" ds_read_b128 v[36:39], v12 offset:22528\n" \
" v_mfma_f32_16x16x32_bf16 v[76:79], v[44:47], a[140:143], v[76:79]\n" \
" s_waitcnt lgkmcnt(4)\n" \
" v_mfma_f32_16x16x32_bf16 v[64:67], v[16:19], a[16:19], v[64:67]\n" \
" buffer_load_dwordx4 v4, s[36:39], s61 offen offset:3072 sc1 lds\n" \
" v_mfma_f32_16x16x32_bf16 v[68:71], v[20:23], a[16:19], v[68:71]\n" \
" ds_read_b128 v[40:43], v12 offset:7168\n" \
" v_mfma_f32_16x16x32_bf16 v[72:75], v[16:19], a[144:147], v[72:75]\n" \
" ds_read_b128 v[44:47], v12 offset:23552\n" \
" v_mfma_f32_16x16x32_bf16 v[76:79], v[20:23], a[144:147], v[76:79]\n" \
" s_waitcnt lgkmcnt(4)\n" \
" v_mfma_f32_16x16x32_bf16 v[64:67], v[24:27], a[20:23], v[64:67]\n" \
" v_mfma_f32_16x16x32_bf16 v[68:71], v[28:31], a[20:23], v[68:71]\n" \
" ds_read_b128 v[16:19], v12 offset:8192\n" \
" v_mfma_f32_16x16x32_bf16 v[72:75], v[24:27], a[148:151], v[72:75]\n" \
" ds_read_b128 v[20:23], v12 offset:24576\n" \
" v_mfma_f32_16x16x32_bf16 v[76:79], v[28:31], a[148:151], v[76:79]\n" \
" s_waitcnt lgkmcnt(4)\n" \
" v_mfma_f32_16x16x32_bf16 v[64:67], v[32:35], a[24:27], v[64:67]\n" \
" s_add_u32 m0, s55, 4096\n" \
" s_add_u32 s61, s61, 4096\n" \
" buffer_load_dwordx4 v4, s[36:39], s61 offen offset:0 sc1 lds\n" \
" v_mfma_f32_16x16x32_bf16 v[68:71], v[36:39], a[24:27], v[68:71]\n" \
" ds_read_b128 v[24:27], v12 offset:9216\n" \
" v_mfma_f32_16x16x32_bf16 v[72:75], v[32:35], a[152:155], v[72:75]\n" \
" ds_read_b128 v[28:31], v12 offset:25600\n" \
" v_mfma_f32_16x16x32_bf16 v[76:79], v[36:39], a[152:155], v[76:79]\n" \
" s_waitcnt lgkmcnt(4)\n" \
" v_mfma_f32_16x16x32_bf16 v[64:67], v[40:43], a[28:31], v[64:67]\n" \
" buffer_load_dwordx4 v4, s[36:39], s61 offen offset:1024 sc1 lds\n" \
" v_mfma_f32_16x16x32_bf16 v[68:71], v[44:47], a[28:31], v[68:71]\n" \
" ds_read_b128 v[32:35], v12 offset:10240\n" \
" v_mfma_f32_16x16x32_bf16 v[72:75], v[40:43], a[156:159], v[72:75]\n" \
" ds_read_b128 v[36:39], v12 offset:26624\n" \
" v_mfma_f32_16x16x32_bf16 v[76:79], v[44:47], a[156:159], v[76:79]\n" \
" s_waitcnt lgkmcnt(4)\n" \
" v_mfma_f32_16x16x32_bf16 v[64:67], v[16:19], a[32:35], v[64:67]\n" \
" v_mfma_f32_16x16x32_bf16 v[68:71], v[20:23], a[32:35], v[68:71]\n" \
" ds_read_b128 v[40:43], v12 offset:11264\n" \
" v_mfma_f32_16x16x32_bf16 v[72:75], v[16:19], a[160:163], v[72:75]\n" \
" ds_read_b128 v[44:47], v12 offset:27648\n" \
" v_mfma_f32_16x16x32_bf16 v[76:79], v[20:23], a[160:163], v[76:79]\n" \
" s_waitcnt lgkmcnt(4)\n" \
" v_mfma_f32_16x16x32_bf16 v[64:67], v[24:27], a[36:39], v[64:67]\n" \
" buffer_load_dwordx4 v4, s[36:39], s61 offen offset:2048 sc1 lds\n" \
" v_mfma_f32_16x16x32_bf16 v[68:71], v[28:31], a[36:39], v[68:71]\n" \
" ds_read_b128 v[16:19], v12 offset:12288\n" \
" v_mfma_f32_16x16x32_bf16 v[72:75], v[24:27], a[164:167], v[72:75]\n" \
" ds_read_b128 v[20:23], v12 offset:28672\n" \
" v_mfma_f32_16x16x32_bf16 v[76:79], v[28:31], a[164:167], v[76:79]\n" \
" s_waitcnt lgkmcnt(4)\n" \
" v_mfma_f32_16x16x32_bf16 v[64:67], v[32:35], a[40:43], v[64:67]\n" \
" buffer_load_dwordx4 v4, s[36:39], s61 offen offset:3072 sc1 lds\n" \
" v_mfma_f32_16x16x32_bf16 v[68:71], v[36:39], a[40:43], v[68:71]\n" \
" ds_read_b128 v[24:27], v12 offset:13312\n" \
" v_mfma_f32_16x16x32_bf16 v[72:75], v[32:35], a[168:171], v[72:75]\n" \
" ds_read_b128 v[28:31], v12 offset:29696\n" \
" v_mfma_f32_16x16x32_bf16 v[76:79], v[36:39], a[168:171], v[76:79]\n" \
" s_waitcnt lgkmcnt(4)\n" \
" v_mfma_f32_16x16x32_bf16 v[64:67], v[40:43], a[44:47], v[64:67]\n" \
" v_mfma_f32_16x16x32_bf16 v[68:71], v[44:47], a[44:47], v[68:71]\n" \
" ds_read_b128 v[32:35], v12 offset:14336\n" \
" v_mfma_f32_16x16x32_bf16 v[72:75], v[40:43], a[172:175], v[72:75]\n" \
" ds_read_b128 v[36:39], v12 offset:30720\n" \
" v_mfma_f32_16x16x32_bf16 v[76:79], v[44:47], a[172:175], v[76:79]\n" \
" s_waitcnt lgkmcnt(4)\n" \
" v_mfma_f32_16x16x32_bf16 v[64:67], v[16:19], a[48:51], v[64:67]\n" \
" v_mfma_f32_16x16x32_bf16 v[68:71], v[20:23], a[48:51], v[68:71]\n" \
" ds_read_b128 v[40:43], v12 offset:15360\n" \
" v_mfma_f32_16x16x32_bf16 v[72:75], v[16:19], a[176:179], v[72:75]\n" \
" ds_read_b128 v[44:47], v12 offset:31744\n" \
" v_mfma_f32_16x16x32_bf16 v[76:79], v[20:23], a[176:179], v[76:79]\n" \
" s_waitcnt lgkmcnt(4)\n" \
" v_mfma_f32_16x16x32_bf16 v[64:67], v[24:27], a[52:55], v[64:67]\n" \
" s_waitcnt vmcnt(11)\n" \
" s_barrier\n" \
" v_mfma_f32_16x16x32_bf16 v[68:71], v[28:31], a[52:55], v[68:71]\n" \
" ds_read_b128 v[16:19], v12 offset:32768\n" \
" v_mfma_f32_16x16x32_bf16 v[72:75], v[24:27], a[180:183], v[72:75]\n" \
" ds_read_b128 v[20:23], v12 offset:49152\n" \
" v_mfma_f32_16x16x32_bf16 v[76:79], v[28:31], a[180:183], v[76:79]\n" \
" s_waitcnt lgkmcnt(4)\n" \
" v_mfma_f32_16x16x32_bf16 v[64:67], v[32:35], a[56:59], v[64:67]\n" \
" v_mfma_f32_16x16x32_bf16 v[68:71], v[36:39], a[56:59], v[68:71]\n" \
" ds_read_b128 v[24:27], v12 offset:33792\n" \
" v_mfma_f32_16x16x32_bf16 v[72:75], v[32:35], a[184:187], v[72:75]\n" \
" ds_read_b128 v[28:31], v12 offset:50176\n" \
" v_mfma_f32_16x16x32_bf16 v[76:79], v[36:39], a[184:187], v[76:79]\n" \
" s_waitcnt lgkmcnt(4)\n" \
" v_mfma_f32_16x16x32_bf16 v[64:67], v[40:43], a[60:63], v[64:67]\n" \
" v_mfma_f32_16x16x32_bf16 v[68:71], v[44:47], a[60:63], v[68:71]\n" \
" ds_read_b128 v[32:35], v12 offset:34816\n" \
" v_mfma_f32_16x16x32_bf16 v[72:75], v[40:43], a[188:191], v[72:75]\n" \
" ds_read_b128 v[36:39], v12 offset:51200\n" \
" v_mfma_f32_16x16x32_bf16 v[76:79], v[44:47], a[188:191], v[76:79]\n" \
" s_waitcnt lgkmcnt(4)\n" \
" v_mfma_f32_16x16x32_bf16 v[64:67], v[16:19], a[64:67], v[64:67]\n" \
" s_min_u32 s61, s56, s60\n" \
" s_add_u32 s61, s61, 16384\n" \
" s_add_u32 s56, s56, 0x10000\n" \
" s_add_u32 m0, s55, 32768\n" \
" s_nop 0\n" \
" buffer_load_dwordx4 v4, s[36:39], s61 offen offset:0 sc1 lds\n" \
" v_mfma_f32_16x16x32_bf16 v[68:71], v[20:23], a[64:67], v[68:71]\n" \
" ds_read_b128 v[40:43], v12 offset:35840\n" \
" v_mfma_f32_16x16x32_bf16 v[72:75], v[16:19], a[192:195], v[72:75]\n" \
" ds_read_b128 v[44:47], v12 offset:52224\n" \
" v_mfma_f32_16x16x32_bf16 v[76:79], v[20:23], a[192:195], v[76:79]\n" \
" s_waitcnt lgkmcnt(4)\n" \
" v_mfma_f32_16x16x32_bf16 v[64:67], v[24:27], a[68:71], v[64:67]\n" \
" buffer_load_dwordx4 v4, s[36:39], s61 offen offset:1024 sc1 lds\n" \
" v_mfma_f32_16x16x32_bf16 v[68:71], v[28:31], a[68:71], v[68:71]\n" \
" ds_read_b128 v[16:19], v12 offset:36864\n" \
" v_mfma_f32_16x16x32_bf16 v[72:75], v[24:27], a[196:199], v[72:75]\n" \
" ds_read_b128 v[20:23], v12 offset:53248\n" \
" v_mfma_f32_16x16x32_bf16 v[76:79], v[28:31], a[196:199], v[76:79]\n" \
" s_waitcnt lgkmcnt(4)\n" \
" v_mfma_f32_16x16x32_bf16 v[64:67], v[32:35], a[72:75], v[64:67]\n" \
" v_mfma_f32_16x16x32_bf16 v[68:71], v[36:39], a[72:75], v[68:71]\n" \
" ds_read_b128 v[24:27], v12 offset:37888\n" \
" v_mfma_f32_16x16x32_bf16 v[72:75], v[32:35], a[200:203], v[72:75]\n" \
" ds_read_b128 v[28:31], v12 offset:54272\n" \
" v_mfma_f32_16x16x32_bf16 v[76:79], v[36:39], a[200:203], v[76:79]\n" \
" s_waitcnt lgkmcnt(4)\n" \
" v_mfma_f32_16x16x32_bf16 v[64:67], v[40:43], a[76:79], v[64:67]\n" \
" buffer_load_dwordx4 v4, s[36:39], s61 offen offset:2048 sc1 lds\n" \
" v_mfma_f32_16x16x32_bf16 v[68:71], v[44:47], a[76:79], v[68:71]\n" \
" ds_read_b128 v[32:35], v12 offset:38912\n" \
" v_mfma_f32_16x16x32_bf16 v[72:75], v[40:43], a[204:207], v[72:75]\n" \
" ds_read_b128 v[36:39], v12 offset:55296\n" \
" v_mfma_f32_16x16x32_bf16 v[76:79], v[44:47], a[204:207], v[76:79]\n" \
" s_waitcnt lgkmcnt(4)\n" \
" v_mfma_f32_16x16x32_bf16 v[64:67], v[16:19], a[80:83], v[64:67]\n" \
" buffer_load_dwordx4 v4, s[36:39], s61 offen offset:3072 sc1 lds\n" \
" v_mfma_f32_16x16x32_bf16 v[68:71], v[20:23], a[80:83], v[68:71]\n" \
" ds_read_b128 v[40:43], v12 offset:39936\n" \
" v_mfma_f32_16x16x32_bf16 v[72:75], v[16:19], a[208:211], v[72:75]\n" \
" ds_read_b128 v[44:47], v12 offset:56320\n" \
" v_mfma_f32_16x16x32_bf16 v[76:79], v[20:23], a[208:211], v[76:79]\n" \
" s_waitcnt lgkmcnt(4)\n" \
" v_mfma_f32_16x16x32_bf16 v[64:67], v[24:27], a[84:87], v[64:67]\n" \
" v_mfma_f32_16x16x32_bf16 v[68:71], v[28:31], a[84:87], v[68:71]\n" \
" ds_read_b128 v[16:19], v12 offset:40960\n" \
" v_mfma_f32_16x16x32_bf16 v[72:75], v[24:27], a[212:215], v[72:75]\n" \
" ds_read_b128 v[20:23], v12 offset:57344\n" \
" v_mfma_f32_16x16x32_bf16 v[76:79], v[28:31], a[212:215], v[76:79]\n" \
" s_waitcnt lgkmcnt(4)\n" \
" v_mfma_f32_16x16x32_bf16 v[64:67], v[32:35], a[88:91], v[64:67]\n" \
" s_add_u32 m0, s55, 36864\n" \
" s_add_u32 s61, s61, 4096\n" \
" buffer_load_dwordx4 v4, s[36:39], s61 offen offset:0 sc1 lds\n" \
" v_mfma_f32_16x16x32_bf16 v[68:71], v[36:39], a[88:91], v[68:71]\n" \
" ds_read_b128 v[24:27], v12 offset:41984\n" \
" v_mfma_f32_16x16x32_bf16 v[72:75], v[32:35], a[216:219], v[72:75]\n" \
" ds_read_b128 v[28:31], v12 offset:58368\n" \
" v_mfma_f32_16x16x32_bf16 v[76:79], v[36:39], a[216:219], v[76:79]\n" \
" s_waitcnt lgkmcnt(4)\n" \
" v_mfma_f32_16x16x32_bf16 v[64:67], v[40:43], a[92:95], v[64:67]\n" \
" buffer_load_dwordx4 v4, s[36:39], s61 offen offset:1024 sc1 lds\n" \
" v_mfma_f32_16x16x32_bf16 v[68:71], v[44:47], a[92:95], v[68:71]\n" \
" ds_read_b128 v[32:35], v12 offset:43008\n" \
" v_mfma_f32_16x16x32_bf16 v[72:75], v[40:43], a[220:223], v[72:75]\n" \
" ds_read_b128 v[36:39], v12 offset:59392\n" \
" v_mfma_f32_16x16x32_bf16 v[76:79], v[44:47], a[220:223], v[76:79]\n" \
" s_waitcnt lgkmcnt(4)\n" \
" v_mfma_f32_16x16x32_bf16 v[64:67], v[16:19], a[96:99], v[64:67]\n" \
" v_mfma_f32_16x16x32_bf16 v[68:71], v[20:23], a[96:99], v[68:71]\n" \
" ds_read_b128 v[40:43], v12 offset:44032\n" \
" v_mfma_f32_16x16x32_bf16 v[72:75], v[16:19], a[224:227], v[72:75]\n" \
" ds_read_b128 v[44:47], v12 offset:60416\n" \
" v_mfma_f32_16x16x32_bf16 v[76:79], v[20:23], a[224:227], v[76:79]\n" \
" s_waitcnt lgkmcnt(4)\n" \
" v_mfma_f32_16x16x32_bf16 v[64:67], v[24:27], a[100:103], v[64:67]\n" \
" buffer_load_dwordx4 v4, s[36:39], s61 offen offset:2048 sc1 lds\n" \
" v_mfma_f32_16x16x32_bf16 v[68:71], v[28:31], a[100:103], v[68:71]\n" \
" ds_read_b128 v[16:19], v12 offset:45056\n" \
" v_mfma_f32_16x16x32_bf16 v[72:75], v[24:27], a[228:231], v[72:75]\n" \
" ds_read_b128 v[20:23], v12 offset:61440\n" \
" v_mfma_f32_16x16x32_bf16 v[76:79], v[28:31], a[228:231], v[76:79]\n" \
" s_waitcnt lgkmcnt(4)\n" \
" v_mfma_f32_16x16x32_bf16 v[64:67], v[32:35], a[104:107], v[64:67]\n" \
" buffer_load_dwordx4 v4, s[36:39], s61 offen offset:3072 sc1 lds\n" \
" v_mfma_f32_16x16x32_bf16 v[68:71], v[36:39], a[104:107], v[68:71]\n" \
" ds_read_b128 v[24:27], v12 offset:46080\n" \
" v_mfma_f32_16x16x32_bf16 v[72:75], v[32:35], a[232:235], v[72:75]\n" \
" ds_read_b128 v[28:31], v12 offset:62464\n" \
" v_mfma_f32_16x16x32_bf16 v[76:79], v[36:39], a[232:235], v[76:79]\n" \
" s_waitcnt lgkmcnt(4)\n" \
" v_mfma_f32_16x16x32_bf16 v[64:67], v[40:43], a[108:111], v[64:67]\n" \
" v_mfma_f32_16x16x32_bf16 v[68:71], v[44:47], a[108:111], v[68:71]\n" \
" ds_read_b128 v[32:35], v12 offset:47104\n" \
" v_mfma_f32_16x16x32_bf16 v[72:75], v[40:43], a[236:239], v[72:75]\n" \
" ds_read_b128 v[36:39], v12 offset:63488\n" \
" v_mfma_f32_16x16x32_bf16 v[76:79], v[44:47], a[236:239], v[76:79]\n" \
" s_waitcnt lgkmcnt(4)\n" \
" v_mfma_f32_16x16x32_bf16 v[64:67], v[16:19], a[112:115], v[64:67]\n" \
" v_mfma_f32_16x16x32_bf16 v[68:71], v[20:23], a[112:115], v[68:71]\n" \
" ds_read_b128 v[40:43], v12 offset:48128\n" \
" v_mfma_f32_16x16x32_bf16 v[72:75], v[16:19], a[240:243], v[72:75]\n" \
" ds_read_b128 v[44:47], v12 offset:64512\n" \
" v_mfma_f32_16x16x32_bf16 v[76:79], v[20:23], a[240:243], v[76:79]\n" \
" s_waitcnt lgkmcnt(4)\n" \
" v_mfma_f32_16x16x32_bf16 v[64:67], v[24:27], a[116:119], v[64:67]\n" \
" s_waitcnt vmcnt(8)\n" \
" s_barrier\n" \
" v_mfma_f32_16x16x32_bf16 v[68:71], v[28:31], a[116:119], v[68:71]\n" \
" ds_read_b128 v[16:19], v10 offset:0\n" \
" v_mfma_f32_16x16x32_bf16 v[72:75], v[24:27], a[244:247], v[72:75]\n" \
" ds_read_b128 v[20:23], v10 offset:16384\n" \
" v_mfma_f32_16x16x32_bf16 v[76:79], v[28:31], a[244:247], v[76:79]\n" \
" s_waitcnt lgkmcnt(4)\n" \
" buffer_store_dword v13, v6, s[36:39], 0 offen sc1\n" \
" v_mfma_f32_16x16x32_bf16 v[64:67], v[32:35], a[120:123], v[64:67]\n" \
" v_mfma_f32_16x16x32_bf16 v[68:71], v[36:39], a[120:123], v[68:71]\n" \
" ds_read_b128 v[24:27], v10 offset:1024\n" \
" v_mfma_f32_16x16x32_bf16 v[72:75], v[32:35], a[248:251], v[72:75]\n" \
" ds_read_b128 v[28:31], v10 offset:17408\n" \
" v_mfma_f32_16x16x32_bf16 v[76:79], v[36:39], a[248:251], v[76:79]\n" \
" s_waitcnt lgkmcnt(4)\n" \
" v_mfma_f32_16x16x32_bf16 v[64:67], v[40:43], a[124:127], v[64:67]\n" \
" v_mfma_f32_16x16x32_bf16 v[68:71], v[44:47], a[124:127], v[68:71]\n" \
" ds_read_b128 v[32:35], v10 offset:2048\n" \
" v_mfma_f32_16x16x32_bf16 v[72:75], v[40:43], a[252:255], v[72:75]\n" \
" ds_read_b128 v[36:39], v10 offset:18432\n" \
" v_mfma_f32_16x16x32_bf16 v[76:79], v[44:47], a[252:255], v[76:79]\n" \
" s_waitcnt lgkmcnt(4)\n" \
" s_add_u32 s57, s57, s49\n" \
" s_add_u32 s58, s58, s49\n" \
" s_add_u32 s59, s59, 1\n" \
" v_mov_b32 v5, v7\n" \
" v_mov_b32 v6, v11\n" \
" s_cmp_ge_u32 s59, s47\n" \
" s_cbranch_scc0 Lp1v_loop\n" \
"; cold\n" \
"Lp1v_out1:\n" \
" s_nop 7\n" \
" s_nop 7\n" \
" v_cvt_pk_bf16_f32 v80, v64, v65\n" \
" v_cvt_pk_bf16_f32 v81, v66, v67\n" \
" v_cvt_pk_bf16_f32 v82, v68, v69\n" \
" v_cvt_pk_bf16_f32 v83, v70, v71\n" \
" buffer_store_dwordx4 v[80:83], v5, s[36:39], s57 offen sc1\n" \
" v_cvt_pk_bf16_f32 v84, v72, v73\n" \
" v_cvt_pk_bf16_f32 v85, v74, v75\n" \
" v_cvt_pk_bf16_f32 v86, v76, v77\n" \
" v_cvt_pk_bf16_f32 v87, v78, v79\n" \
" buffer_store_dwordx4 v[84:87], v5, s[36:39], s58 offen sc1\n" \
" s_branch Lp1v_end\n" \
"Lp1v_out0:\n" \
" s_nop 7\n" \
" s_nop 7\n" \
" v_cvt_pk_bf16_f32 v80, v48, v49\n" \
" v_cvt_pk_bf16_f32 v81, v50, v51\n" \
" v_cvt_pk_bf16_f32 v82, v52, v53\n" \
" v_cvt_pk_bf16_f32 v83, v54, v55\n" \
" buffer_store_dwordx4 v[80:83], v5, s[36:39], s57 offen sc1\n" \
" v_cvt_pk_bf16_f32 v84, v56, v57\n" \
" v_cvt_pk_bf16_f32 v85, v58, v59\n" \
" v_cvt_pk_bf16_f32 v86, v60, v61\n" \
" v_cvt_pk_bf16_f32 v87, v62, v63\n" \
" buffer_store_dwordx4 v[84:87], v5, s[36:39], s58 offen sc1\n" \
"Lp1v_end:\n" \
" s_waitcnt vmcnt(0) lgkmcnt(0)\n" \
" s_barrier\n" \
" v_mov_b32 v13, s59\n" \
" buffer_store_dword v13, v6, s[36:39], 0 offen sc1\n" \
" s_waitcnt vmcnt(0) lgkmcnt(0)\n" \
" s_branch Lp1v_x\n" \
"Lp1v_l0:\n" \
" s_nop 7\n" \
" s_nop 7\n" \
" s_waitcnt vmcnt(0) lgkmcnt(0)\n" \
" s_mov_b32 s62, 0\n" \
"Lp1v_l0a:\n" \
" buffer_load_dword v15, v14, s[36:39], 0 offen sc1\n" \
" s_waitcnt vmcnt(0)\n" \
" v_cmp_gt_u32 vcc, 32, v15\n" \
" s_nop 4\n" \
" s_and_b64 vcc, vcc, 1\n" \
" s_cbranch_vccz Lp1v_lk\n" \
" s_sleep 4\n" \
" s_add_u32 s62, s62, 1\n" \
" s_cmp_lt_u32 s62, 0x10000\n" \
" s_cbranch_scc1 Lp1v_l0a\n" \
" v_mov_b32 v15, 256\n" \
" buffer_store_dword v15, v15, s[36:39], 0 offen sc1\n" \
" s_waitcnt vmcnt(0) lgkmcnt(0)\n" \
" s_branch Lp1v_lk\n" \
"Lp1v_l1:\n" \
" s_nop 7\n" \
" s_nop 7\n" \
" s_waitcnt vmcnt(0) lgkmcnt(0)\n" \
" s_mov_b32 s62, 0\n" \
"Lp1v_l1a:\n" \
" buffer_load_dword v15, v14, s[36:39], 0 offen sc1\n" \
" s_waitcnt vmcnt(0)\n" \
" v_cmp_gt_u32 vcc, 32, v15\n" \
" s_nop 4\n" \
" s_and_b64 vcc, vcc, 15\n" \
" s_cbranch_vccz Lp1v_lk\n" \
" s_sleep 4\n" \
" s_add_u32 s62, s62, 1\n" \
" s_cmp_lt_u32 s62, 0x10000\n" \
" s_cbranch_scc1 Lp1v_l1a\n" \
" v_mov_b32 v15, 256\n" \
" buffer_store_dword v15, v15, s[36:39], 0 offen sc1\n" \
" s_waitcnt vmcnt(0) lgkmcnt(0)\n" \
" s_branch Lp1v_lk\n" \
"Lp1v_l2:\n" \
" s_nop 7\n" \
" s_nop 7\n" \
" s_waitcnt vmcnt(0) lgkmcnt(0)\n" \
" s_mov_b32 s62, 0\n" \
"Lp1v_l2a:\n" \
" buffer_load_dword v15, v14, s[36:39], 0 offen sc1\n" \
" s_waitcnt vmcnt(0)\n" \
" v_cmp_gt_u32 vcc, 32, v15\n" \
" s_nop 4\n" \
" s_and_b64 vcc, vcc, 15\n" \
" s_cbranch_vccz Lp1v_lm\n" \
" s_sleep 4\n" \
" s_add_u32 s62, s62, 1\n" \
" s_cmp_lt_u32 s62, 0x10000\n" \
" s_cbranch_scc1 Lp1v_l2a\n" \
" v_mov_b32 v15, 256\n" \
" buffer_store_dword v15, v15, s[36:39], 0 offen sc1\n" \
" s_waitcnt vmcnt(0) lgkmcnt(0)\n" \
" s_branch Lp1v_lm\n" \
"Lp1v_x:\n"
#define ASM_P1_G1 \
" s_nop 7\n" \
" s_nop 7\n" \
" s_waitcnt vmcnt(0) lgkmcnt(0)\n" \
" s_lshr_b32 s52, s48, 1\n" \
" s_lshl_b32 s52, s52, 13\n" \
" s_and_b32 s53, s48, 1\n" \
" s_lshl_b32 s53, s53, 10\n" \
" s_add_u32 s52, s52, s53\n" \
" s_mov_b32 s54, 0\n" \
" s_lshl_b32 s54, s44, 5\n" \
" v_add_u32 v8, 0x10000, v2\n" \
" v_add_u32 v9, 0x10000, v3\n" \
" s_barrier\n" \
" ds_read_b64_tr_b16 a[0:1], v2 offset:0\n" \
" ds_read_b64_tr_b16 a[2:3], v2 offset:512\n" \
" ds_read_b64_tr_b16 a[128:129], v3 offset:0\n" \
" ds_read_b64_tr_b16 a[130:131], v3 offset:512\n" \
" ds_read_b64_tr_b16 a[4:5], v2 offset:4096\n" \
" ds_read_b64_tr_b16 a[6:7], v2 offset:4608\n" \
" ds_read_b64_tr_b16 a[132:133], v3 offset:4096\n" \
" ds_read_b64_tr_b16 a[134:135], v3 offset:4608\n" \
" s_waitcnt lgkmcnt(0)\n" \
" s_barrier\n" \
" s_add_u32 m0, s52, 0\n" \
" s_nop 0\n" \
" buffer_load_dwordx4 v1, s[40:43], s54 offen nt lds\n" \
" s_add_u32 s54, s54, s44\n" \
" s_add_u32 m0, s52, 2048\n" \
" s_nop 0\n" \
" buffer_load_dwordx4 v1, s[40:43], s54 offen nt lds\n" \
" s_add_u32 s54, s54, s44\n" \
" s_add_u32 m0, s52, 4096\n" \
" s_nop 0\n" \
" buffer_load_dwordx4 v1, s[40:43], s54 offen nt lds\n" \
" s_add_u32 s54, s54, s44\n" \
" s_add_u32 m0, s52, 6144\n" \
" s_nop 0\n" \
" buffer_load_dwordx4 v1, s[40:43], s54 offen nt lds\n" \
" s_add_u32 s54, s54, s44\n" \
" s_barrier\n" \
" ds_read_b64_tr_b16 a[8:9], v2 offset:16384\n" \
" ds_read_b64_tr_b16 a[10:11], v2 offset:16896\n" \
" ds_read_b64_tr_b16 a[136:137], v3 offset:16384\n" \
" ds_read_b64_tr_b16 a[138:139], v3 offset:16896\n" \
" ds_read_b64_tr_b16 a[12:13], v2 offset:20480\n" \
" ds_read_b64_tr_b16 a[14:15], v2 offset:20992\n" \
" ds_read_b64_tr_b16 a[140:141], v3 offset:20480\n" \
" ds_read_b64_tr_b16 a[142:143], v3 offset:20992\n" \
" s_waitcnt lgkmcnt(0)\n" \
" s_barrier\n" \
" s_add_u32 m0, s52, 16384\n" \
" s_nop 0\n" \
" buffer_load_dwordx4 v1, s[40:43], s54 offen nt lds\n" \
" s_add_u32 s54, s54, s44\n" \
" s_add_u32 m0, s52, 18432\n" \
" s_nop 0\n" \
" buffer_load_dwordx4 v1, s[40:43], s54 offen nt lds\n" \
" s_add_u32 s54, s54, s44\n" \
" s_add_u32 m0, s52, 20480\n" \
" s_nop 0\n" \
" buffer_load_dwordx4 v1, s[40:43], s54 offen nt lds\n" \
" s_add_u32 s54, s54, s44\n" \
" s_add_u32 m0, s52, 22528\n" \
" s_nop 0\n" \
" buffer_load_dwordx4 v1, s[40:43], s54 offen nt lds\n" \
" s_add_u32 s54, s54, s44\n" \
" s_barrier\n" \
" ds_read_b64_tr_b16 a[16:17], v2 offset:32768\n" \
" ds_read_b64_tr_b16 a[18:19], v2 offset:33280\n" \
" ds_read_b64_tr_b16 a[144:145], v3 offset:32768\n" \
" ds_read_b64_tr_b16 a[146:147], v3 offset:33280\n" \
" ds_read_b64_tr_b16 a[20:21], v2 offset:36864\n" \
" ds_read_b64_tr_b16 a[22:23], v2 offset:37376\n" \
" ds_read_b64_tr_b16 a[148:149], v3 offset:36864\n" \
" ds_read_b64_tr_b16 a[150:151], v3 offset:37376\n" \
" s_waitcnt lgkmcnt(0)\n" \
" s_barrier\n" \
" s_add_u32 m0, s52, 32768\n" \
" s_nop 0\n" \
" buffer_load_dwordx4 v1, s[40:43], s54 offen nt lds\n" \
" s_add_u32 s54, s54, s44\n" \
" s_add_u32 m0, s52, 34816\n" \
" s_nop 0\n" \
" buffer_load_dwordx4 v1, s[40:43], s54 offen nt lds\n" \
" s_add_u32 s54, s54, s44\n" \
" s_add_u32 m0, s52, 36864\n" \
" s_nop 0\n" \
" buffer_load_dwordx4 v1, s[40:43], s54 offen nt lds\n" \
" s_add_u32 s54, s54, s44\n" \
" s_add_u32 m0, s52, 38912\n" \
" s_nop 0\n" \
" buffer_load_dwordx4 v1, s[40:43], s54 offen nt lds\n" \
" s_add_u32 s54, s54, s44\n" \
" s_barrier\n" \
" ds_read_b64_tr_b16 a[24:25], v2 offset:49152\n" \
" ds_read_b64_tr_b16 a[26:27], v2 offset:49664\n" \
" ds_read_b64_tr_b16 a[152:153], v3 offset:49152\n" \
" ds_read_b64_tr_b16 a[154:155], v3 offset:49664\n" \
" ds_read_b64_tr_b16 a[28:29], v2 offset:53248\n" \
" ds_read_b64_tr_b16 a[30:31], v2 offset:53760\n" \
" ds_read_b64_tr_b16 a[156:157], v3 offset:53248\n" \
" ds_read_b64_tr_b16 a[158:159], v3 offset:53760\n" \
" s_waitcnt lgkmcnt(0)\n" \
" s_barrier\n" \
" s_add_u32 m0, s52, 49152\n" \
" s_nop 0\n" \
" buffer_load_dwordx4 v1, s[40:43], s54 offen nt lds\n" \
" s_add_u32 s54, s54, s44\n" \
" s_add_u32 m0, s52, 51200\n" \
" s_nop 0\n" \
" buffer_load_dwordx4 v1, s[40:43], s54 offen nt lds\n" \
" s_add_u32 s54, s54, s44\n" \
" s_add_u32 m0, s52, 53248\n" \
" s_nop 0\n" \
" buffer_load_dwordx4 v1, s[40:43], s54 offen nt lds\n" \
" s_add_u32 s54, s54, s44\n" \
" s_add_u32 m0, s52, 55296\n" \
" s_nop 0\n" \
" buffer_load_dwordx4 v1, s[40:43], s54 offen nt lds\n" \
" s_add_u32 s54, s54, s44\n" \
" s_barrier\n" \
" ds_read_b64_tr_b16 a[32:33], v8 offset:0\n" \
" ds_read_b64_tr_b16 a[34:35], v8 offset:512\n" \
" ds_read_b64_tr_b16 a[160:161], v9 offset:0\n" \
" ds_read_b64_tr_b16 a[162:163], v9 offset:512\n" \
" ds_read_b64_tr_b16 a[36:37], v8 offset:4096\n" \
" ds_read_b64_tr_b16 a[38:39], v8 offset:4608\n" \
" ds_read_b64_tr_b16 a[164:165], v9 offset:4096\n" \
" ds_read_b64_tr_b16 a[166:167], v9 offset:4608\n" \
" s_waitcnt lgkmcnt(0)\n" \
" s_barrier\n" \
" s_add_u32 m0, s52, 65536\n" \
" s_nop 0\n" \
" buffer_load_dwordx4 v1, s[40:43], s54 offen nt lds\n" \
" s_add_u32 s54, s54, s44\n" \
" s_add_u32 m0, s52, 67584\n" \
" s_nop 0\n" \
" buffer_load_dwordx4 v1, s[40:43], s54 offen nt lds\n" \
" s_add_u32 s54, s54, s44\n" \
" s_add_u32 m0, s52, 69632\n" \
" s_nop 0\n" \
" buffer_load_dwordx4 v1, s[40:43], s54 offen nt lds\n" \
" s_add_u32 s54, s54, s44\n" \
" s_add_u32 m0, s52, 71680\n" \
" s_nop 0\n" \
" buffer_load_dwordx4 v1, s[40:43], s54 offen nt lds\n" \
" s_add_u32 s54, s54, s44\n" \
" s_barrier\n" \
" ds_read_b64_tr_b16 a[40:41], v8 offset:16384\n" \
" ds_read_b64_tr_b16 a[42:43], v8 offset:16896\n" \
" ds_read_b64_tr_b16 a[168:169], v9 offset:16384\n" \
" ds_read_b64_tr_b16 a[170:171], v9 offset:16896\n" \
" ds_read_b64_tr_b16 a[44:45], v8 offset:20480\n" \
" ds_read_b64_tr_b16 a[46:47], v8 offset:20992\n" \
" ds_read_b64_tr_b16 a[172:173], v9 offset:20480\n" \
" ds_read_b64_tr_b16 a[174:175], v9 offset:20992\n" \
" s_waitcnt lgkmcnt(0)\n" \
" s_barrier\n" \
" s_add_u32 m0, s52, 81920\n" \
" s_nop 0\n" \
" buffer_load_dwordx4 v1, s[40:43], s54 offen nt lds\n" \
" s_add_u32 s54, s54, s44\n" \
" s_add_u32 m0, s52, 83968\n" \
" s_nop 0\n" \
" buffer_load_dwordx4 v1, s[40:43], s54 offen nt lds\n" \
" s_add_u32 s54, s54, s44\n" \
" s_add_u32 m0, s52, 86016\n" \
" s_nop 0\n" \
" buffer_load_dwordx4 v1, s[40:43], s54 offen nt lds\n" \
" s_add_u32 s54, s54, s44\n" \
" s_add_u32 m0, s52, 88064\n" \
" s_nop 0\n" \
" buffer_load_dwordx4 v1, s[40:43], s54 offen nt lds\n" \
" s_add_u32 s54, s54, s44\n" \
" s_barrier\n" \
" ds_read_b64_tr_b16 a[48:49], v8 offset:32768\n" \
" ds_read_b64_tr_b16 a[50:51], v8 offset:33280\n" \
" ds_read_b64_tr_b16 a[176:177], v9 offset:32768\n" \
" ds_read_b64_tr_b16 a[178:179], v9 offset:33280\n" \
" ds_read_b64_tr_b16 a[52:53], v8 offset:36864\n" \
" ds_read_b64_tr_b16 a[54:55], v8 offset:37376\n" \
" ds_read_b64_tr_b16 a[180:181], v9 offset:36864\n" \
" ds_read_b64_tr_b16 a[182:183], v9 offset:37376\n" \
" s_waitcnt lgkmcnt(0)\n" \
" s_barrier\n" \
" s_add_u32 m0, s52, 98304\n" \
" s_nop 0\n" \
" buffer_load_dwordx4 v1, s[40:43], s54 offen nt lds\n" \
" s_add_u32 s54, s54, s44\n" \
" s_add_u32 m0, s52, 100352\n" \
" s_nop 0\n" \
" buffer_load_dwordx4 v1, s[40:43], s54 offen nt lds\n" \
" s_add_u32 s54, s54, s44\n" \
" s_add_u32 m0, s52, 102400\n" \
" s_nop 0\n" \
" buffer_load_dwordx4 v1, s[40:43], s54 offen nt lds\n" \
" s_add_u32 s54, s54, s44\n" \
" s_add_u32 m0, s52, 104448\n" \
" s_nop 0\n" \
" buffer_load_dwordx4 v1, s[40:43], s54 offen nt lds\n" \
" s_add_u32 s54, s54, s44\n" \
" s_barrier\n" \
" ds_read_b64_tr_b16 a[56:57], v8 offset:49152\n" \
" ds_read_b64_tr_b16 a[58:59], v8 offset:49664\n" \
" ds_read_b64_tr_b16 a[184:185], v9 offset:49152\n" \
" ds_read_b64_tr_b16 a[186:187], v9 offset:49664\n" \
" ds_read_b64_tr_b16 a[60:61], v8 offset:53248\n" \
" ds_read_b64_tr_b16 a[62:63], v8 offset:53760\n" \
" ds_read_b64_tr_b16 a[188:189], v9 offset:53248\n" \
" ds_read_b64_tr_b16 a[190:191], v9 offset:53760\n" \
" s_waitcnt lgkmcnt(0)\n" \
" s_barrier\n" \
" s_add_u32 m0, s52, 114688\n" \
" s_nop 0\n" \
" buffer_load_dwordx4 v1, s[40:43], s54 offen nt lds\n" \
" s_add_u32 s54, s54, s44\n" \
" s_add_u32 m0, s52, 116736\n" \
" s_nop 0\n" \
" buffer_load_dwordx4 v1, s[40:43], s54 offen nt lds\n" \
" s_add_u32 s54, s54, s44\n" \
" s_add_u32 m0, s52, 118784\n" \
" s_nop 0\n" \
" buffer_load_dwordx4 v1, s[40:43], s54 offen nt lds\n" \
" s_add_u32 s54, s54, s44\n" \
" s_add_u32 m0, s52, 120832\n" \
" s_nop 0\n" \
" buffer_load_dwordx4 v1, s[40:43], s54 offen nt lds\n" \
" s_add_u32 s54, s54, s44\n" \
" s_waitcnt vmcnt(28)\n" \
" s_barrier\n" \
" ds_read_b64_tr_b16 a[64:65], v2 offset:0\n" \
" ds_read_b64_tr_b16 a[66:67], v2 offset:512\n" \
" ds_read_b64_tr_b16 a[192:193], v3 offset:0\n" \
" ds_read_b64_tr_b16 a[194:195], v3 offset:512\n" \
" ds_read_b64_tr_b16 a[68:69], v2 offset:4096\n" \
" ds_read_b64_tr_b16 a[70:71], v2 offset:4608\n" \
" ds_read_b64_tr_b16 a[196:197], v3 offset:4096\n" \
" ds_read_b64_tr_b16 a[198:199], v3 offset:4608\n" \
" s_waitcnt vmcnt(24)\n" \
" s_barrier\n" \
" ds_read_b64_tr_b16 a[72:73], v2 offset:16384\n" \
" ds_read_b64_tr_b16 a[74:75], v2 offset:16896\n" \
" ds_read_b64_tr_b16 a[200:201], v3 offset:16384\n" \
" ds_read_b64_tr_b16 a[202:203], v3 offset:16896\n" \
" ds_read_b64_tr_b16 a[76:77], v2 offset:20480\n" \
" ds_read_b64_tr_b16 a[78:79], v2 offset:20992\n" \
" ds_read_b64_tr_b16 a[204:205], v3 offset:20480\n" \
" ds_read_b64_tr_b16 a[206:207], v3 offset:20992\n" \
" s_waitcnt vmcnt(20)\n" \
" s_barrier\n" \
" ds_read_b64_tr_b16 a[80:81], v2 offset:32768\n" \
" ds_read_b64_tr_b16 a[82:83], v2 offset:33280\n" \
" ds_read_b64_tr_b16 a[208:209], v3 offset:32768\n" \
" ds_read_b64_tr_b16 a[210:211], v3 offset:33280\n" \
" ds_read_b64_tr_b16 a[84:85], v2 offset:36864\n" \
" ds_read_b64_tr_b16 a[86:87], v2 offset:37376\n" \
" ds_read_b64_tr_b16 a[212:213], v3 offset:36864\n" \
" ds_read_b64_tr_b16 a[214:215], v3 offset:37376\n" \
" s_waitcnt vmcnt(16)\n" \
" s_barrier\n" \
" ds_read_b64_tr_b16 a[88:89], v2 offset:49152\n" \
" ds_read_b64_tr_b16 a[90:91], v2 offset:49664\n" \
" ds_read_b64_tr_b16 a[216:217], v3 offset:49152\n" \
" ds_read_b64_tr_b16 a[218:219], v3 offset:49664\n" \
" ds_read_b64_tr_b16 a[92:93], v2 offset:53248\n" \
" ds_read_b64_tr_b16 a[94:95], v2 offset:53760\n" \
" ds_read_b64_tr_b16 a[220:221], v3 offset:53248\n" \
" ds_read_b64_tr_b16 a[222:223], v3 offset:53760\n" \
" s_waitcnt vmcnt(12)\n" \
" s_barrier\n" \
" ds_read_b64_tr_b16 a[96:97], v8 offset:0\n" \
" ds_read_b64_tr_b16 a[98:99], v8 offset:512\n" \
" ds_read_b64_tr_b16 a[224:225], v9 offset:0\n" \
" ds_read_b64_tr_b16 a[226:227], v9 offset:512\n" \
" ds_read_b64_tr_b16 a[100:101], v8 offset:4096\n" \
" ds_read_b64_tr_b16 a[102:103], v8 offset:4608\n" \
" ds_read_b64_tr_b16 a[228:229], v9 offset:4096\n" \
" ds_read_b64_tr_b16 a[230:231], v9 offset:4608\n" \
" s_waitcnt vmcnt(8)\n" \
" s_barrier\n" \
" ds_read_b64_tr_b16 a[104:105], v8 offset:16384\n" \
" ds_read_b64_tr_b16 a[106:107], v8 offset:16896\n" \
" ds_read_b64_tr_b16 a[232:233], v9 offset:16384\n" \
" ds_read_b64_tr_b16 a[234:235], v9 offset:16896\n" \
" ds_read_b64_tr_b16 a[108:109], v8 offset:20480\n" \
" ds_read_b64_tr_b16 a[110:111], v8 offset:20992\n" \
" ds_read_b64_tr_b16 a[236:237], v9 offset:20480\n" \
" ds_read_b64_tr_b16 a[238:239], v9 offset:20992\n" \
" s_waitcnt vmcnt(4)\n" \
" s_barrier\n" \
" ds_read_b64_tr_b16 a[112:113], v8 offset:32768\n" \
" ds_read_b64_tr_b16 a[114:115], v8 offset:33280\n" \
" ds_read_b64_tr_b16 a[240:241], v9 offset:32768\n" \
" ds_read_b64_tr_b16 a[242:243], v9 offset:33280\n" \
" ds_read_b64_tr_b16 a[116:117], v8 offset:36864\n" \
" ds_read_b64_tr_b16 a[118:119], v8 offset:37376\n" \
" ds_read_b64_tr_b16 a[244:245], v9 offset:36864\n" \
" ds_read_b64_tr_b16 a[246:247], v9 offset:37376\n" \
" s_waitcnt vmcnt(0)\n" \
" s_barrier\n" \
" ds_read_b64_tr_b16 a[120:121], v8 offset:49152\n" \
" ds_read_b64_tr_b16 a[122:123], v8 offset:49664\n" \
" ds_read_b64_tr_b16 a[248:249], v9 offset:49152\n" \
" ds_read_b64_tr_b16 a[250:251], v9 offset:49664\n" \
" ds_read_b64_tr_b16 a[124:125], v8 offset:53248\n" \
" ds_read_b64_tr_b16 a[126:127], v8 offset:53760\n" \
" ds_read_b64_tr_b16 a[252:253], v9 offset:53248\n" \
" ds_read_b64_tr_b16 a[254:255], v9 offset:53760\n" \
" s_waitcnt lgkmcnt(0)\n" \
" s_barrier\n" \
" v_and_b32 v14, 48, v0\n" \
" v_lshlrev_b32 v14, 3, v14\n" \
" v_add_u32 v14, 4096, v14\n" \
" s_cmp_eq_u32 s46, 0\n" \
" s_cbranch_scc1 Lp1g1_l0\n" \
" s_branch Lp1g1_l1\n" \
"Lp1g1_lk:\n" \
" s_lshl_b32 s55, s48, 13\n" \
" s_lshl_b32 s56, s46, 16\n" \
" s_lshl_b32 s60, s47, 16\n" \
" s_sub_u32 s60, s60, 0x10000\n" \
" s_mul_i32 s57, s46, s49\n" \
" s_sub_u32 s57, s57, s49\n" \
" s_add_u32 s58, s57, s50\n" \
" v_mov_b32 v7, v5\n" \
" v_mov_b32 v5, 0x7ffffff0\n" \
" v_mov_b32 v10, v0\n" \
" v_mov_b32 v11, v6\n" \
" v_mov_b32 v6, 0x7ffffff0\n" \
" s_mov_b32 s59, s46\n" \
" s_min_u32 s61, s56, s60\n" \
" s_add_u32 m0, s55, 0\n" \
" s_nop 0\n" \
" buffer_load_dwordx4 v4, s[36:39], s61 offen offset:0 sc1 lds\n" \
" buffer_load_dwordx4 v4, s[36:39], s61 offen offset:1024 sc1 lds\n" \
" buffer_load_dwordx4 v4, s[36:39], s61 offen offset:2048 sc1 lds\n" \
" buffer_load_dwordx4 v4, s[36:39], s61 offen offset:3072 sc1 lds\n" \
" s_add_u32 m0, s55, 4096\n" \
" s_add_u32 s61, s61, 4096\n" \
" buffer_load_dwordx4 v4, s[36:39], s61 offen offset:0 sc1 lds\n" \
" buffer_load_dwordx4 v4, s[36:39], s61 offen offset:1024 sc1 lds\n" \
" buffer_load_dwordx4 v4, s[36:39], s61 offen offset:2048 sc1 lds\n" \
" buffer_load_dwordx4 v4, s[36:39], s61 offen offset:3072 sc1 lds\n" \
" s_min_u32 s61, s56, s60\n" \
" s_add_u32 s61, s61, 16384\n" \
" s_add_u32 s56, s56, 0x10000\n" \
" s_add_u32 m0, s55, 32768\n" \
" s_nop 0\n" \
" buffer_load_dwordx4 v4, s[36:39], s61 offen offset:0 sc1 lds\n" \
" buffer_load_dwordx4 v4, s[36:39], s61 offen offset:1024 sc1 lds\n" \
" buffer_load_dwordx4 v4, s[36:39], s61 offen offset:2048 sc1 lds\n" \
" buffer_load_dwordx4 v4, s[36:39], s61 offen offset:3072 sc1 lds\n" \
" s_add_u32 m0, s55, 36864\n" \
" s_add_u32 s61, s61, 4096\n" \
" buffer_load_dwordx4 v4, s[36:39], s61 offen offset:0 sc1 lds\n" \
" buffer_load_dwordx4 v4, s[36:39], s61 offen offset:1024 sc1 lds\n" \
" buffer_load_dwordx4 v4, s[36:39], s61 offen offset:2048 sc1 lds\n" \
" buffer_load_dwordx4 v4, s[36:39], s61 offen offset:3072 sc1 lds\n" \
" s_mov_b32 s70, 0\n" \
" s_mov_b32 s72, 0\n" \
" s_waitcnt vmcnt(0) lgkmcnt(0)\n" \
" s_barrier\n" \
" v_add_u32 v12, 0x10000, v10\n" \
" ds_read_b128 v[16:19], v10 offset:0\n" \
" ds_read_b128 v[20:23], v10 offset:16384\n" \
" ds_read_b128 v[24:27], v10 offset:1024\n" \
" ds_read_b128 v[28:31], v10 offset:17408\n" \
" ds_read_b128 v[32:35], v10 offset:2048\n" \
" ds_read_b128 v[36:39], v10 offset:18432\n" \
" s_waitcnt lgkmcnt(4)\n" \
" s_nop 7\n" \
" s_nop 7\n" \
".p2align 6\n" \
"Lp1g1_loop:\n" \
" v_mfma_f32_16x16x32_bf16 v[48:51], a[0:3], v[16:19], 0\n" \
" s_min_u32 s61, s56, s60\n" \
" s_add_u32 m0, s55, 65536\n" \
" s_nop 0\n" \
" buffer_load_dwordx4 v4, s[36:39], s61 offen offset:0 sc1 lds\n" \
" v_add_u32_e64 v13, -1, s59\n" \
" v_mfma_f32_16x16x32_bf16 v[52:55], a[0:3], v[20:23], 0\n" \
" ds_read_b128 v[40:43], v10 offset:3072\n" \
" s_nop 0\n" \
" v_mfma_f32_16x16x32_bf16 v[56:59], a[128:131], v[16:19], 0\n" \
" ds_read_b128 v[44:47], v10 offset:19456\n" \
" s_nop 0\n" \
" v_mfma_f32_16x16x32_bf16 v[60:63], a[128:131], v[20:23], 0\n" \
" s_waitcnt lgkmcnt(4)\n" \
" v_mul_f32 v64, 0xbfb8aa3b, v64\n" \
" v_mul_f32 v65, 0xbfb8aa3b, v65\n" \
" v_mfma_f32_16x16x32_bf16 v[48:51], a[4:7], v[24:27], v[48:51]\n" \
" buffer_load_dwordx4 v4, s[36:39], s61 offen offset:1024 sc1 lds\n" \
" v_mfma_f32_16x16x32_bf16 v[52:55], a[4:7], v[28:31], v[52:55]\n" \
" ds_read_b128 v[16:19], v10 offset:4096\n" \
" v_mul_f32 v66, 0xbfb8aa3b, v66\n" \
" v_mfma_f32_16x16x32_bf16 v[56:59], a[132:135], v[24:27], v[56:59]\n" \
" ds_read_b128 v[20:23], v10 offset:20480\n" \
" v_mul_f32 v67, 0xbfb8aa3b, v67\n" \
" v_mfma_f32_16x16x32_bf16 v[60:63], a[132:135], v[28:31], v[60:63]\n" \
" s_waitcnt lgkmcnt(4)\n" \
" v_exp_f32_e64 v64, v64\n" \
" v_mfma_f32_16x16x32_bf16 v[48:51], a[8:11], v[32:35], v[48:51]\n" \
" v_exp_f32_e64 v65, v65\n" \
" v_mfma_f32_16x16x32_bf16 v[52:55], a[8:11], v[36:39], v[52:55]\n" \
" ds_read_b128 v[24:27], v10 offset:5120\n" \
" v_mfma_f32_16x16x32_bf16 v[56:59], a[136:139], v[32:35], v[56:59]\n" \
" ds_read_b128 v[28:31], v10 offset:21504\n" \
" v_mfma_f32_16x16x32_bf16 v[60:63], a[136:139], v[36:39], v[60:63]\n" \
" s_waitcnt lgkmcnt(4)\n" \
" v_exp_f32 v66, v66\n" \
" v_mfma_f32_16x16x32_bf16 v[48:51], a[12:15], v[40:43], v[48:51]\n" \
" buffer_load_dwordx4 v4, s[36:39], s61 offen offset:2048 sc1 lds\n" \
" v_mfma_f32_16x16x32_bf16 v[52:55], a[12:15], v[44:47], v[52:55]\n" \
" ds_read_b128 v[32:35], v10 offset:6144\n" \
" v_mfma_f32_16x16x32_bf16 v[56:59], a[140:143], v[40:43], v[56:59]\n" \
" ds_read_b128 v[36:39], v10 offset:22528\n" \
" v_mfma_f32_16x16x32_bf16 v[60:63], a[140:143], v[44:47], v[60:63]\n" \
" s_waitcnt lgkmcnt(4)\n" \
" v_exp_f32 v67, v67\n" \
" v_mfma_f32_16x16x32_bf16 v[48:51], a[16:19], v[16:19], v[48:51]\n" \
" buffer_load_dwordx4 v4, s[36:39], s61 offen offset:3072 sc1 lds\n" \
" v_mfma_f32_16x16x32_bf16 v[52:55], a[16:19], v[20:23], v[52:55]\n" \
" ds_read_b128 v[40:43], v10 offset:7168\n" \
" v_add_f32_e64 v64, 1.0, v64\n" \
" v_mfma_f32_16x16x32_bf16 v[56:59], a[144:147], v[16:19], v[56:59]\n" \
" ds_read_b128 v[44:47], v10 offset:23552\n" \
" v_add_f32_e64 v65, 1.0, v65\n" \
" v_mfma_f32_16x16x32_bf16 v[60:63], a[144:147], v[20:23], v[60:63]\n" \
" s_waitcnt lgkmcnt(4)\n" \
" v_add_f32 v66, 1.0, v66\n" \
" v_add_f32_e64 v67, 1.0, v67\n" \
" v_mfma_f32_16x16x32_bf16 v[48:51], a[20:23], v[24:27], v[48:51]\n" \
" v_rcp_f32_e64 v64, v64\n" \
" v_mfma_f32_16x16x32_bf16 v[52:55], a[20:23], v[28:31], v[52:55]\n" \
" ds_read_b128 v[16:19], v10 offset:8192\n" \
" v_mfma_f32_16x16x32_bf16 v[56:59], a[148:151], v[24:27], v[56:59]\n" \
" ds_read_b128 v[20:23], v10 offset:24576\n" \
" v_mfma_f32_16x16x32_bf16 v[60:63], a[148:151], v[28:31], v[60:63]\n" \
" s_waitcnt lgkmcnt(4)\n" \
" v_rcp_f32 v65, v65\n" \
" v_mfma_f32_16x16x32_bf16 v[48:51], a[24:27], v[32:35], v[48:51]\n" \
" s_add_u32 m0, s55, 69632\n" \
" s_add_u32 s61, s61, 4096\n" \
" buffer_load_dwordx4 v4, s[36:39], s61 offen offset:0 sc1 lds\n" \
" v_mfma_f32_16x16x32_bf16 v[52:55], a[24:27], v[36:39], v[52:55]\n" \
" ds_read_b128 v[24:27], v10 offset:9216\n" \
" v_mfma_f32_16x16x32_bf16 v[56:59], a[152:155], v[32:35], v[56:59]\n" \
" ds_read_b128 v[28:31], v10 offset:25600\n" \
" v_mfma_f32_16x16x32_bf16 v[60:63], a[152:155], v[36:39], v[60:63]\n" \
" s_waitcnt lgkmcnt(4)\n" \
" v_rcp_f32 v66, v66\n" \
" v_mfma_f32_16x16x32_bf16 v[48:51], a[28:31], v[40:43], v[48:51]\n" \
" buffer_load_dwordx4 v4, s[36:39], s61 offen offset:1024 sc1 lds\n" \
" v_mfma_f32_16x16x32_bf16 v[52:55], a[28:31], v[44:47], v[52:55]\n" \
" ds_read_b128 v[32:35], v10 offset:10240\n" \
" v_mfma_f32_16x16x32_bf16 v[56:59], a[156:159], v[40:43], v[56:59]\n" \
" ds_read_b128 v[36:39], v10 offset:26624\n" \
" v_mfma_f32_16x16x32_bf16 v[60:63], a[156:159], v[44:47], v[60:63]\n" \
" s_waitcnt lgkmcnt(4)\n" \
" v_rcp_f32 v67, v67\n" \
" v_mfma_f32_16x16x32_bf16 v[48:51], a[32:35], v[16:19], v[48:51]\n" \
" v_mul_f32 v64, v64, v72\n" \
" v_mul_f32 v65, v65, v73\n" \
" v_mfma_f32_16x16x32_bf16 v[52:55], a[32:35], v[20:23], v[52:55]\n" \
" ds_read_b128 v[40:43], v10 offset:11264\n" \
" v_mul_f32_e64 v66, v66, v74\n" \
" v_mfma_f32_16x16x32_bf16 v[56:59], a[160:163], v[16:19], v[56:59]\n" \
" ds_read_b128 v[44:47], v10 offset:27648\n" \
" v_mul_f32_e64 v67, v67, v75\n" \
" v_mfma_f32_16x16x32_bf16 v[60:63], a[160:163], v[20:23], v[60:63]\n" \
" s_waitcnt lgkmcnt(4)\n" \
" v_cvt_pk_bf16_f32 v80, v64, v65\n" \
" v_cvt_pk_bf16_f32 v81, v66, v67\n" \
" v_mfma_f32_16x16x32_bf16 v[48:51], a[36:39], v[24:27], v[48:51]\n" \
" buffer_load_dwordx4 v4, s[36:39], s61 offen offset:2048 sc1 lds\n" \
" v_mfma_f32_16x16x32_bf16 v[52:55], a[36:39], v[28:31], v[52:55]\n" \
" ds_read_b128 v[16:19], v10 offset:12288\n" \
" v_mul_f32 v68, 0xbfb8aa3b, v68\n" \
" v_mfma_f32_16x16x32_bf16 v[56:59], a[164:167], v[24:27], v[56:59]\n" \
" ds_read_b128 v[20:23], v10 offset:28672\n" \
" v_mul_f32 v69, 0xbfb8aa3b, v69\n" \
" v_mfma_f32_16x16x32_bf16 v[60:63], a[164:167], v[28:31], v[60:63]\n" \
" s_waitcnt lgkmcnt(4)\n" \
" v_mul_f32 v70, 0xbfb8aa3b, v70\n" \
" v_mul_f32 v71, 0xbfb8aa3b, v71\n" \
" v_mfma_f32_16x16x32_bf16 v[48:51], a[40:43], v[32:35], v[48:51]\n" \
" buffer_load_dwordx4 v4, s[36:39], s61 offen offset:3072 sc1 lds\n" \
" v_mfma_f32_16x16x32_bf16 v[52:55], a[40:43], v[36:39], v[52:55]\n" \
" ds_read_b128 v[24:27], v10 offset:13312\n" \
" v_mfma_f32_16x16x32_bf16 v[56:59], a[168:171], v[32:35], v[56:59]\n" \
" ds_read_b128 v[28:31], v10 offset:29696\n" \
" v_mfma_f32_16x16x32_bf16 v[60:63], a[168:171], v[36:39], v[60:63]\n" \
" s_waitcnt lgkmcnt(4)\n" \
" v_exp_f32 v68, v68\n" \
" v_mfma_f32_16x16x32_bf16 v[48:51], a[44:47], v[40:43], v[48:51]\n" \
" v_exp_f32_e64 v69, v69\n" \
" v_mfma_f32_16x16x32_bf16 v[52:55], a[44:47], v[44:47], v[52:55]\n" \
" ds_read_b128 v[32:35], v10 offset:14336\n" \
" v_mfma_f32_16x16x32_bf16 v[56:59], a[172:175], v[40:43], v[56:59]\n" \
" ds_read_b128 v[36:39], v10 offset:30720\n" \
" v_mfma_f32_16x16x32_bf16 v[60:63], a[172:175], v[44:47], v[60:63]\n" \
" s_waitcnt lgkmcnt(4)\n" \
" v_exp_f32 v70, v70\n" \
" v_mfma_f32_16x16x32_bf16 v[48:51], a[48:51], v[16:19], v[48:51]\n" \
" v_exp_f32_e64 v71, v71\n" \
" v_mfma_f32_16x16x32_bf16 v[52:55], a[48:51], v[20:23], v[52:55]\n" \
" ds_read_b128 v[40:43], v10 offset:15360\n" \
" v_add_f32_e64 v68, 1.0, v68\n" \
" v_mfma_f32_16x16x32_bf16 v[56:59], a[176:179], v[16:19], v[56:59]\n" \
" ds_read_b128 v[44:47], v10 offset:31744\n" \
" v_add_f32_e64 v69, 1.0, v69\n" \
" v_mfma_f32_16x16x32_bf16 v[60:63], a[176:179], v[20:23], v[60:63]\n" \
" s_waitcnt lgkmcnt(4)\n" \
" v_add_f32 v70, 1.0, v70\n" \
" v_add_f32_e64 v71, 1.0, v71\n" \
" v_mfma_f32_16x16x32_bf16 v[48:51], a[52:55], v[24:27], v[48:51]\n" \
" s_waitcnt vmcnt(8)\n" \
" s_barrier\n" \
" v_mfma_f32_16x16x32_bf16 v[52:55], a[52:55], v[28:31], v[52:55]\n" \
" ds_read_b128 v[16:19], v10 offset:32768\n" \
" v_mfma_f32_16x16x32_bf16 v[56:59], a[180:183], v[24:27], v[56:59]\n" \
" ds_read_b128 v[20:23], v10 offset:49152\n" \
" v_mfma_f32_16x16x32_bf16 v[60:63], a[180:183], v[28:31], v[60:63]\n" \
" s_waitcnt lgkmcnt(4)\n" \
" buffer_store_dword v13, v6, s[36:39], 0 offen sc1\n" \
" v_rcp_f32 v68, v68\n" \
" v_mfma_f32_16x16x32_bf16 v[48:51], a[56:59], v[32:35], v[48:51]\n" \
" v_rcp_f32_e64 v69, v69\n" \
" v_mfma_f32_16x16x32_bf16 v[52:55], a[56:59], v[36:39], v[52:55]\n" \
" ds_read_b128 v[24:27], v10 offset:33792\n" \
" v_mfma_f32_16x16x32_bf16 v[56:59], a[184:187], v[32:35], v[56:59]\n" \
" ds_read_b128 v[28:31], v10 offset:50176\n" \
" v_mfma_f32_16x16x32_bf16 v[60:63], a[184:187], v[36:39], v[60:63]\n" \
" s_waitcnt lgkmcnt(4)\n" \
" v_rcp_f32 v70, v70\n" \
" v_mfma_f32_16x16x32_bf16 v[48:51], a[60:63], v[40:43], v[48:51]\n" \
" v_rcp_f32_e64 v71, v71\n" \
" v_mfma_f32_16x16x32_bf16 v[52:55], a[60:63], v[44:47], v[52:55]\n" \
" ds_read_b128 v[32:35], v10 offset:34816\n" \
" v_mul_f32_e64 v68, v68, v76\n" \
" v_mfma_f32_16x16x32_bf16 v[56:59], a[188:191], v[40:43], v[56:59]\n" \
" ds_read_b128 v[36:39], v10 offset:51200\n" \
" v_mul_f32_e64 v69, v69, v77\n" \
" v_mfma_f32_16x16x32_bf16 v[60:63], a[188:191], v[44:47], v[60:63]\n" \
" s_waitcnt lgkmcnt(4)\n" \
" v_mul_f32 v70, v70, v78\n" \
" v_mul_f32_e64 v71, v71, v79\n" \
" v_mfma_f32_16x16x32_bf16 v[48:51], a[64:67], v[16:19], v[48:51]\n" \
" s_min_u32 s61, s56, s60\n" \
" s_add_u32 s61, s61, 16384\n" \
" s_add_u32 s56, s56, 0x10000\n" \
" s_add_u32 m0, s55, 98304\n" \
" s_nop 0\n" \
" buffer_load_dwordx4 v4, s[36:39], s61 offen offset:0 sc1 lds\n" \
" v_mfma_f32_16x16x32_bf16 v[52:55], a[64:67], v[20:23], v[52:55]\n" \
" ds_read_b128 v[40:43], v10 offset:35840\n" \
" v_cvt_pk_bf16_f32 v82, v68, v69\n" \
" v_mfma_f32_16x16x32_bf16 v[56:59], a[192:195], v[16:19], v[56:59]\n" \
" ds_read_b128 v[44:47], v10 offset:52224\n" \
" v_cvt_pk_bf16_f32 v83, v70, v71\n" \
" v_mfma_f32_16x16x32_bf16 v[60:63], a[192:195], v[20:23], v[60:63]\n" \
" s_waitcnt lgkmcnt(4)\n" \
" s_nop 0\n" \
" s_nop 0\n" \
" v_mfma_f32_16x16x32_bf16 v[48:51], a[68:71], v[24:27], v[48:51]\n" \
" buffer_load_dwordx4 v4, s[36:39], s61 offen offset:1024 sc1 lds\n" \
" v_mfma_f32_16x16x32_bf16 v[52:55], a[68:71], v[28:31], v[52:55]\n" \
" ds_read_b128 v[16:19], v10 offset:36864\n" \
" v_permlane16_swap_b32 v80, v82\n" \
" v_mfma_f32_16x16x32_bf16 v[56:59], a[196:199], v[24:27], v[56:59]\n" \
" ds_read_b128 v[20:23], v10 offset:53248\n" \
" v_permlane16_swap_b32 v81, v83\n" \
" v_mfma_f32_16x16x32_bf16 v[60:63], a[196:199], v[28:31], v[60:63]\n" \
" s_waitcnt lgkmcnt(4)\n" \
" buffer_store_dwordx4 v[80:83], v5, s[36:39], s57 offen sc1\n" \
" v_mfma_f32_16x16x32_bf16 v[48:51], a[72:75], v[32:35], v[48:51]\n" \
" v_mfma_f32_16x16x32_bf16 v[52:55], a[72:75], v[36:39], v[52:55]\n" \
" ds_read_b128 v[24:27], v10 offset:37888\n" \
" v_mfma_f32_16x16x32_bf16 v[56:59], a[200:203], v[32:35], v[56:59]\n" \
" ds_read_b128 v[28:31], v10 offset:54272\n" \
" v_mfma_f32_16x16x32_bf16 v[60:63], a[200:203], v[36:39], v[60:63]\n" \
" s_waitcnt lgkmcnt(4)\n" \
" v_mfma_f32_16x16x32_bf16 v[48:51], a[76:79], v[40:43], v[48:51]\n" \
" buffer_load_dwordx4 v4, s[36:39], s61 offen offset:2048 sc1 lds\n" \
" v_mfma_f32_16x16x32_bf16 v[52:55], a[76:79], v[44:47], v[52:55]\n" \
" ds_read_b128 v[32:35], v10 offset:38912\n" \
" v_mfma_f32_16x16x32_bf16 v[56:59], a[204:207], v[40:43], v[56:59]\n" \
" ds_read_b128 v[36:39], v10 offset:55296\n" \
" v_mfma_f32_16x16x32_bf16 v[60:63], a[204:207], v[44:47], v[60:63]\n" \
" s_waitcnt lgkmcnt(4)\n" \
" v_mfma_f32_16x16x32_bf16 v[48:51], a[80:83], v[16:19], v[48:51]\n" \
" buffer_load_dwordx4 v4, s[36:39], s61 offen offset:3072 sc1 lds\n" \
" v_mfma_f32_16x16x32_bf16 v[52:55], a[80:83], v[20:23], v[52:55]\n" \
" ds_read_b128 v[40:43], v10 offset:39936\n" \
" v_mfma_f32_16x16x32_bf16 v[56:59], a[208:211], v[16:19], v[56:59]\n" \
" ds_read_b128 v[44:47], v10 offset:56320\n" \
" v_mfma_f32_16x16x32_bf16 v[60:63], a[208:211], v[20:23], v[60:63]\n" \
" s_waitcnt lgkmcnt(4)\n" \
" v_mfma_f32_16x16x32_bf16 v[48:51], a[84:87], v[24:27], v[48:51]\n" \
" v_mfma_f32_16x16x32_bf16 v[52:55], a[84:87], v[28:31], v[52:55]\n" \
" ds_read_b128 v[16:19], v10 offset:40960\n" \
" v_mfma_f32_16x16x32_bf16 v[56:59], a[212:215], v[24:27], v[56:59]\n" \
" ds_read_b128 v[20:23], v10 offset:57344\n" \
" v_mfma_f32_16x16x32_bf16 v[60:63], a[212:215], v[28:31], v[60:63]\n" \
" s_waitcnt lgkmcnt(4)\n" \
" v_mfma_f32_16x16x32_bf16 v[48:51], a[88:91], v[32:35], v[48:51]\n" \
" s_add_u32 m0, s55, 102400\n" \
" s_add_u32 s61, s61, 4096\n" \
" buffer_load_dwordx4 v4, s[36:39], s61 offen offset:0 sc1 lds\n" \
" v_mfma_f32_16x16x32_bf16 v[52:55], a[88:91], v[36:39], v[52:55]\n" \
" ds_read_b128 v[24:27], v10 offset:41984\n" \
" v_mfma_f32_16x16x32_bf16 v[56:59], a[216:219], v[32:35], v[56:59]\n" \
" ds_read_b128 v[28:31], v10 offset:58368\n" \
" v_mfma_f32_16x16x32_bf16 v[60:63], a[216:219], v[36:39], v[60:63]\n" \
" s_waitcnt lgkmcnt(4)\n" \
" v_mfma_f32_16x16x32_bf16 v[48:51], a[92:95], v[40:43], v[48:51]\n" \
" buffer_load_dwordx4 v4, s[36:39], s61 offen offset:1024 sc1 lds\n" \
" v_mfma_f32_16x16x32_bf16 v[52:55], a[92:95], v[44:47], v[52:55]\n" \
" ds_read_b128 v[32:35], v10 offset:43008\n" \
" v_mfma_f32_16x16x32_bf16 v[56:59], a[220:223], v[40:43], v[56:59]\n" \
" ds_read_b128 v[36:39], v10 offset:59392\n" \
" v_mfma_f32_16x16x32_bf16 v[60:63], a[220:223], v[44:47], v[60:63]\n" \
" s_waitcnt lgkmcnt(4)\n" \
" v_mfma_f32_16x16x32_bf16 v[48:51], a[96:99], v[16:19], v[48:51]\n" \
" v_mfma_f32_16x16x32_bf16 v[52:55], a[96:99], v[20:23], v[52:55]\n" \
" ds_read_b128 v[40:43], v10 offset:44032\n" \
" v_mfma_f32_16x16x32_bf16 v[56:59], a[224:227], v[16:19], v[56:59]\n" \
" ds_read_b128 v[44:47], v10 offset:60416\n" \
" v_mfma_f32_16x16x32_bf16 v[60:63], a[224:227], v[20:23], v[60:63]\n" \
" s_waitcnt lgkmcnt(4)\n" \
" v_mfma_f32_16x16x32_bf16 v[48:51], a[100:103], v[24:27], v[48:51]\n" \
" buffer_load_dwordx4 v4, s[36:39], s61 offen offset:2048 sc1 lds\n" \
" v_mfma_f32_16x16x32_bf16 v[52:55], a[100:103], v[28:31], v[52:55]\n" \
" ds_read_b128 v[16:19], v10 offset:45056\n" \
" v_mfma_f32_16x16x32_bf16 v[56:59], a[228:231], v[24:27], v[56:59]\n" \
" ds_read_b128 v[20:23], v10 offset:61440\n" \
" v_mfma_f32_16x16x32_bf16 v[60:63], a[228:231], v[28:31], v[60:63]\n" \
" s_waitcnt lgkmcnt(4)\n" \
" v_mfma_f32_16x16x32_bf16 v[48:51], a[104:107], v[32:35], v[48:51]\n" \
" buffer_load_dwordx4 v4, s[36:39], s61 offen offset:3072 sc1 lds\n" \
" v_mfma_f32_16x16x32_bf16 v[52:55], a[104:107], v[36:39], v[52:55]\n" \
" ds_read_b128 v[24:27], v10 offset:46080\n" \
" v_mfma_f32_16x16x32_bf16 v[56:59], a[232:235], v[32:35], v[56:59]\n" \
" ds_read_b128 v[28:31], v10 offset:62464\n" \
" v_mfma_f32_16x16x32_bf16 v[60:63], a[232:235], v[36:39], v[60:63]\n" \
" s_waitcnt lgkmcnt(4)\n" \
" v_mfma_f32_16x16x32_bf16 v[48:51], a[108:111], v[40:43], v[48:51]\n" \
" v_mfma_f32_16x16x32_bf16 v[52:55], a[108:111], v[44:47], v[52:55]\n" \
" ds_read_b128 v[32:35], v10 offset:47104\n" \
" v_mfma_f32_16x16x32_bf16 v[56:59], a[236:239], v[40:43], v[56:59]\n" \
" ds_read_b128 v[36:39], v10 offset:63488\n" \
" v_mfma_f32_16x16x32_bf16 v[60:63], a[236:239], v[44:47], v[60:63]\n" \
" s_waitcnt lgkmcnt(4)\n" \
" v_mfma_f32_16x16x32_bf16 v[48:51], a[112:115], v[16:19], v[48:51]\n" \
" v_mfma_f32_16x16x32_bf16 v[52:55], a[112:115], v[20:23], v[52:55]\n" \
" ds_read_b128 v[40:43], v10 offset:48128\n" \
" v_mfma_f32_16x16x32_bf16 v[56:59], a[240:243], v[16:19], v[56:59]\n" \
" ds_read_b128 v[44:47], v10 offset:64512\n" \
" v_mfma_f32_16x16x32_bf16 v[60:63], a[240:243], v[20:23], v[60:63]\n" \
" s_waitcnt lgkmcnt(4)\n" \
" v_mfma_f32_16x16x32_bf16 v[48:51], a[116:119], v[24:27], v[48:51]\n" \
" s_waitcnt vmcnt(10)\n" \
" s_barrier\n" \
" v_mfma_f32_16x16x32_bf16 v[52:55], a[116:119], v[28:31], v[52:55]\n" \
" ds_read_b128 v[16:19], v12 offset:0\n" \
" v_mfma_f32_16x16x32_bf16 v[56:59], a[244:247], v[24:27], v[56:59]\n" \
" ds_read_b128 v[20:23], v12 offset:16384\n" \
" v_mfma_f32_16x16x32_bf16 v[60:63], a[244:247], v[28:31], v[60:63]\n" \
" s_waitcnt lgkmcnt(4)\n" \
" v_mfma_f32_16x16x32_bf16 v[48:51], a[120:123], v[32:35], v[48:51]\n" \
" v_mfma_f32_16x16x32_bf16 v[52:55], a[120:123], v[36:39], v[52:55]\n" \
" ds_read_b128 v[24:27], v12 offset:1024\n" \
" v_mfma_f32_16x16x32_bf16 v[56:59], a[248:251], v[32:35], v[56:59]\n" \
" ds_read_b128 v[28:31], v12 offset:17408\n" \
" v_mfma_f32_16x16x32_bf16 v[60:63], a[248:251], v[36:39], v[60:63]\n" \
" s_waitcnt lgkmcnt(4)\n" \
" v_mfma_f32_16x16x32_bf16 v[48:51], a[124:127], v[40:43], v[48:51]\n" \
" v_mfma_f32_16x16x32_bf16 v[52:55], a[124:127], v[44:47], v[52:55]\n" \
" ds_read_b128 v[32:35], v12 offset:2048\n" \
" v_mfma_f32_16x16x32_bf16 v[56:59], a[252:255], v[40:43], v[56:59]\n" \
" ds_read_b128 v[36:39], v12 offset:18432\n" \
" v_mfma_f32_16x16x32_bf16 v[60:63], a[252:255], v[44:47], v[60:63]\n" \
" s_waitcnt lgkmcnt(4)\n" \
" s_add_u32 s57, s57, s49\n" \
" s_add_u32 s58, s58, s49\n" \
" s_add_u32 s59, s59, 1\n" \
" v_mov_b32_e64 v5, v7\n" \
" v_mov_b32_e64 v6, v11\n" \
" s_and_b32 s62, s59, -2\n" \
" s_cmp_eq_u32 s62, 12\n" \
" s_nop 0\n" \
" s_cbranch_scc1 Lp1g1_l2\n" \
"Lp1g1_lm:\n" \
" s_cmp_ge_u32 s59, s47\n" \
" s_cbranch_scc1 Lp1g1_out0\n" \
" v_mfma_f32_16x16x32_bf16 v[64:67], a[0:3], v[16:19], 0\n" \
" s_min_u32 s61, s56, s60\n" \
" s_add_u32 m0, s55, 0\n" \
" s_nop 0\n" \
" buffer_load_dwordx4 v4, s[36:39], s61 offen offset:0 sc1 lds\n" \
" v_add_u32_e64 v13, -1, s59\n" \
" v_mfma_f32_16x16x32_bf16 v[68:71], a[0:3], v[20:23], 0\n" \
" ds_read_b128 v[40:43], v12 offset:3072\n" \
" s_nop 0\n" \
" v_mfma_f32_16x16x32_bf16 v[72:75], a[128:131], v[16:19], 0\n" \
" ds_read_b128 v[44:47], v12 offset:19456\n" \
" s_nop 0\n" \
" v_mfma_f32_16x16x32_bf16 v[76:79], a[128:131], v[20:23], 0\n" \
" s_waitcnt lgkmcnt(4)\n" \
" v_mul_f32 v48, 0xbfb8aa3b, v48\n" \
" v_mul_f32 v49, 0xbfb8aa3b, v49\n" \
" v_mfma_f32_16x16x32_bf16 v[64:67], a[4:7], v[24:27], v[64:67]\n" \
" buffer_load_dwordx4 v4, s[36:39], s61 offen offset:1024 sc1 lds\n" \
" v_mfma_f32_16x16x32_bf16 v[68:71], a[4:7], v[28:31], v[68:71]\n" \
" ds_read_b128 v[16:19], v12 offset:4096\n" \
" v_mul_f32 v50, 0xbfb8aa3b, v50\n" \
" v_mfma_f32_16x16x32_bf16 v[72:75], a[132:135], v[24:27], v[72:75]\n" \
" ds_read_b128 v[20:23], v12 offset:20480\n" \
" v_mul_f32 v51, 0xbfb8aa3b, v51\n" \
" v_mfma_f32_16x16x32_bf16 v[76:79], a[132:135], v[28:31], v[76:79]\n" \
" s_waitcnt lgkmcnt(4)\n" \
" v_exp_f32_e64 v48, v48\n" \
" v_mfma_f32_16x16x32_bf16 v[64:67], a[8:11], v[32:35], v[64:67]\n" \
" v_exp_f32_e64 v49, v49\n" \
" v_mfma_f32_16x16x32_bf16 v[68:71], a[8:11], v[36:39], v[68:71]\n" \
" ds_read_b128 v[24:27], v12 offset:5120\n" \
" v_mfma_f32_16x16x32_bf16 v[72:75], a[136:139], v[32:35], v[72:75]\n" \
" ds_read_b128 v[28:31], v12 offset:21504\n" \
" v_mfma_f32_16x16x32_bf16 v[76:79], a[136:139], v[36:39], v[76:79]\n" \
" s_waitcnt lgkmcnt(4)\n" \
" v_exp_f32 v50, v50\n" \
" v_mfma_f32_16x16x32_bf16 v[64:67], a[12:15], v[40:43], v[64:67]\n" \
" buffer_load_dwordx4 v4, s[36:39], s61 offen offset:2048 sc1 lds\n" \
" v_mfma_f32_16x16x32_bf16 v[68:71], a[12:15], v[44:47], v[68:71]\n" \
" ds_read_b128 v[32:35], v12 offset:6144\n" \
" v_mfma_f32_16x16x32_bf16 v[72:75], a[140:143], v[40:43], v[72:75]\n" \
" ds_read_b128 v[36:39], v12 offset:22528\n" \
" v_mfma_f32_16x16x32_bf16 v[76:79], a[140:143], v[44:47], v[76:79]\n" \
" s_waitcnt lgkmcnt(4)\n" \
" v_exp_f32 v51, v51\n" \
" v_mfma_f32_16x16x32_bf16 v[64:67], a[16:19], v[16:19], v[64:67]\n" \
" buffer_load_dwordx4 v4, s[36:39], s61 offen offset:3072 sc1 lds\n" \
" v_mfma_f32_16x16x32_bf16 v[68:71], a[16:19], v[20:23], v[68:71]\n" \
" ds_read_b128 v[40:43], v12 offset:7168\n" \
" v_add_f32_e64 v48, 1.0, v48\n" \
" v_mfma_f32_16x16x32_bf16 v[72:75], a[144:147], v[16:19], v[72:75]\n" \
" ds_read_b128 v[44:47], v12 offset:23552\n" \
" v_add_f32_e64 v49, 1.0, v49\n" \
" v_mfma_f32_16x16x32_bf16 v[76:79], a[144:147], v[20:23], v[76:79]\n" \
" s_waitcnt lgkmcnt(4)\n" \
" v_add_f32 v50, 1.0, v50\n" \
" v_add_f32_e64 v51, 1.0, v51\n" \
" v_mfma_f32_16x16x32_bf16 v[64:67], a[20:23], v[24:27], v[64:67]\n" \
" v_rcp_f32_e64 v48, v48\n" \
" v_mfma_f32_16x16x32_bf16 v[68:71], a[20:23], v[28:31], v[68:71]\n" \
" ds_read_b128 v[16:19], v12 offset:8192\n" \
" v_mfma_f32_16x16x32_bf16 v[72:75], a[148:151], v[24:27], v[72:75]\n" \
" ds_read_b128 v[20:23], v12 offset:24576\n" \
" v_mfma_f32_16x16x32_bf16 v[76:79], a[148:151], v[28:31], v[76:79]\n" \
" s_waitcnt lgkmcnt(4)\n" \
" v_rcp_f32 v49, v49\n" \
" v_mfma_f32_16x16x32_bf16 v[64:67], a[24:27], v[32:35], v[64:67]\n" \
" s_add_u32 m0, s55, 4096\n" \
" s_add_u32 s61, s61, 4096\n" \
" buffer_load_dwordx4 v4, s[36:39], s61 offen offset:0 sc1 lds\n" \
" v_mfma_f32_16x16x32_bf16 v[68:71], a[24:27], v[36:39], v[68:71]\n" \
" ds_read_b128 v[24:27], v12 offset:9216\n" \
" v_mfma_f32_16x16x32_bf16 v[72:75], a[152:155], v[32:35], v[72:75]\n" \
" ds_read_b128 v[28:31], v12 offset:25600\n" \
" v_mfma_f32_16x16x32_bf16 v[76:79], a[152:155], v[36:39], v[76:79]\n" \
" s_waitcnt lgkmcnt(4)\n" \
" v_rcp_f32 v50, v50\n" \
" v_mfma_f32_16x16x32_bf16 v[64:67], a[28:31], v[40:43], v[64:67]\n" \
" buffer_load_dwordx4 v4, s[36:39], s61 offen offset:1024 sc1 lds\n" \
" v_mfma_f32_16x16x32_bf16 v[68:71], a[28:31], v[44:47], v[68:71]\n" \
" ds_read_b128 v[32:35], v12 offset:10240\n" \
" v_mfma_f32_16x16x32_bf16 v[72:75], a[156:159], v[40:43], v[72:75]\n" \
" ds_read_b128 v[36:39], v12 offset:26624\n" \
" v_mfma_f32_16x16x32_bf16 v[76:79], a[156:159], v[44:47], v[76:79]\n" \
" s_waitcnt lgkmcnt(4)\n" \
" v_rcp_f32 v51, v51\n" \
" v_mfma_f32_16x16x32_bf16 v[64:67], a[32:35], v[16:19], v[64:67]\n" \
" v_mul_f32 v48, v48, v56\n" \
" v_mul_f32 v49, v49, v57\n" \
" v_mfma_f32_16x16x32_bf16 v[68:71], a[32:35], v[20:23], v[68:71]\n" \
" ds_read_b128 v[40:43], v12 offset:11264\n" \
" v_mul_f32_e64 v50, v50, v58\n" \
" v_mfma_f32_16x16x32_bf16 v[72:75], a[160:163], v[16:19], v[72:75]\n" \
" ds_read_b128 v[44:47], v12 offset:27648\n" \
" v_mul_f32_e64 v51, v51, v59\n" \
" v_mfma_f32_16x16x32_bf16 v[76:79], a[160:163], v[20:23], v[76:79]\n" \
" s_waitcnt lgkmcnt(4)\n" \
" v_cvt_pk_bf16_f32 v80, v48, v49\n" \
" v_cvt_pk_bf16_f32 v81, v50, v51\n" \
" v_mfma_f32_16x16x32_bf16 v[64:67], a[36:39], v[24:27], v[64:67]\n" \
" buffer_load_dwordx4 v4, s[36:39], s61 offen offset:2048 sc1 lds\n" \
" v_mfma_f32_16x16x32_bf16 v[68:71], a[36:39], v[28:31], v[68:71]\n" \
" ds_read_b128 v[16:19], v12 offset:12288\n" \
" v_mul_f32 v52, 0xbfb8aa3b, v52\n" \
" v_mfma_f32_16x16x32_bf16 v[72:75], a[164:167], v[24:27], v[72:75]\n" \
" ds_read_b128 v[20:23], v12 offset:28672\n" \
" v_mul_f32 v53, 0xbfb8aa3b, v53\n" \
" v_mfma_f32_16x16x32_bf16 v[76:79], a[164:167], v[28:31], v[76:79]\n" \
" s_waitcnt lgkmcnt(4)\n" \
" v_mul_f32 v54, 0xbfb8aa3b, v54\n" \
" v_mul_f32 v55, 0xbfb8aa3b, v55\n" \
" v_mfma_f32_16x16x32_bf16 v[64:67], a[40:43], v[32:35], v[64:67]\n" \
" buffer_load_dwordx4 v4, s[36:39], s61 offen offset:3072 sc1 lds\n" \
" v_mfma_f32_16x16x32_bf16 v[68:71], a[40:43], v[36:39], v[68:71]\n" \
" ds_read_b128 v[24:27], v12 offset:13312\n" \
" v_mfma_f32_16x16x32_bf16 v[72:75], a[168:171], v[32:35], v[72:75]\n" \
" ds_read_b128 v[28:31], v12 offset:29696\n" \
" v_mfma_f32_16x16x32_bf16 v[76:79], a[168:171], v[36:39], v[76:79]\n" \
" s_waitcnt lgkmcnt(4)\n" \
" v_exp_f32 v52, v52\n" \
" v_mfma_f32_16x16x32_bf16 v[64:67], a[44:47], v[40:43], v[64:67]\n" \
" v_exp_f32_e64 v53, v53\n" \
" v_mfma_f32_16x16x32_bf16 v[68:71], a[44:47], v[44:47], v[68:71]\n" \
" ds_read_b128 v[32:35], v12 offset:14336\n" \
" v_mfma_f32_16x16x32_bf16 v[72:75], a[172:175], v[40:43], v[72:75]\n" \
" ds_read_b128 v[36:39], v12 offset:30720\n" \
" v_mfma_f32_16x16x32_bf16 v[76:79], a[172:175], v[44:47], v[76:79]\n" \
" s_waitcnt lgkmcnt(4)\n" \
" v_exp_f32 v54, v54\n" \
" v_mfma_f32_16x16x32_bf16 v[64:67], a[48:51], v[16:19], v[64:67]\n" \
" v_exp_f32_e64 v55, v55\n" \
" v_mfma_f32_16x16x32_bf16 v[68:71], a[48:51], v[20:23], v[68:71]\n" \
" ds_read_b128 v[40:43], v12 offset:15360\n" \
" v_add_f32_e64 v52, 1.0, v52\n" \
" v_mfma_f32_16x16x32_bf16 v[72:75], a[176:179], v[16:19], v[72:75]\n" \
" ds_read_b128 v[44:47], v12 offset:31744\n" \
" v_add_f32_e64 v53, 1.0, v53\n" \
" v_mfma_f32_16x16x32_bf16 v[76:79], a[176:179], v[20:23], v[76:79]\n" \
" s_waitcnt lgkmcnt(4)\n" \
" v_add_f32 v54, 1.0, v54\n" \
" v_add_f32_e64 v55, 1.0, v55\n" \
" v_mfma_f32_16x16x32_bf16 v[64:67], a[52:55], v[24:27], v[64:67]\n" \
" s_waitcnt vmcnt(8)\n" \
" s_barrier\n" \
" v_mfma_f32_16x16x32_bf16 v[68:71], a[52:55], v[28:31], v[68:71]\n" \
" ds_read_b128 v[16:19], v12 offset:32768\n" \
" v_mfma_f32_16x16x32_bf16 v[72:75], a[180:183], v[24:27], v[72:75]\n" \
" ds_read_b128 v[20:23], v12 offset:49152\n" \
" v_mfma_f32_16x16x32_bf16 v[76:79], a[180:183], v[28:31], v[76:79]\n" \
" s_waitcnt lgkmcnt(4)\n" \
" buffer_store_dword v13, v6, s[36:39], 0 offen sc1\n" \
" v_rcp_f32 v52, v52\n" \
" v_mfma_f32_16x16x32_bf16 v[64:67], a[56:59], v[32:35], v[64:67]\n" \
" v_rcp_f32_e64 v53, v53\n" \
" v_mfma_f32_16x16x32_bf16 v[68:71], a[56:59], v[36:39], v[68:71]\n" \
" ds_read_b128 v[24:27], v12 offset:33792\n" \
" v_mfma_f32_16x16x32_bf16 v[72:75], a[184:187], v[32:35], v[72:75]\n" \
" ds_read_b128 v[28:31], v12 offset:50176\n" \
" v_mfma_f32_16x16x32_bf16 v[76:79], a[184:187], v[36:39], v[76:79]\n" \
" s_waitcnt lgkmcnt(4)\n" \
" v_rcp_f32 v54, v54\n" \
" v_mfma_f32_16x16x32_bf16 v[64:67], a[60:63], v[40:43], v[64:67]\n" \
" v_rcp_f32_e64 v55, v55\n" \
" v_mfma_f32_16x16x32_bf16 v[68:71], a[60:63], v[44:47], v[68:71]\n" \
" ds_read_b128 v[32:35], v12 offset:34816\n" \
" v_mul_f32_e64 v52, v52, v60\n" \
" v_mfma_f32_16x16x32_bf16 v[72:75], a[188:191], v[40:43], v[72:75]\n" \
" ds_read_b128 v[36:39], v12 offset:51200\n" \
" v_mul_f32_e64 v53, v53, v61\n" \
" v_mfma_f32_16x16x32_bf16 v[76:79], a[188:191], v[44:47], v[76:79]\n" \
" s_waitcnt lgkmcnt(4)\n" \
" v_mul_f32 v54, v54, v62\n" \
" v_mul_f32_e64 v55, v55, v63\n" \
" v_mfma_f32_16x16x32_bf16 v[64:67], a[64:67], v[16:19], v[64:67]\n" \
" s_min_u32 s61, s56, s60\n" \
" s_add_u32 s61, s61, 16384\n" \
" s_add_u32 s56, s56, 0x10000\n" \
" s_add_u32 m0, s55, 32768\n" \
" s_nop 0\n" \
" buffer_load_dwordx4 v4, s[36:39], s61 offen offset:0 sc1 lds\n" \
" v_mfma_f32_16x16x32_bf16 v[68:71], a[64:67], v[20:23], v[68:71]\n" \
" ds_read_b128 v[40:43], v12 offset:35840\n" \
" v_cvt_pk_bf16_f32 v82, v52, v53\n" \
" v_mfma_f32_16x16x32_bf16 v[72:75], a[192:195], v[16:19], v[72:75]\n" \
" ds_read_b128 v[44:47], v12 offset:52224\n" \
" v_cvt_pk_bf16_f32 v83, v54, v55\n" \
" v_mfma_f32_16x16x32_bf16 v[76:79], a[192:195], v[20:23], v[76:79]\n" \
" s_waitcnt lgkmcnt(4)\n" \
" s_nop 0\n" \
" s_nop 0\n" \
" v_mfma_f32_16x16x32_bf16 v[64:67], a[68:71], v[24:27], v[64:67]\n" \
" buffer_load_dwordx4 v4, s[36:39], s61 offen offset:1024 sc1 lds\n" \
" v_mfma_f32_16x16x32_bf16 v[68:71], a[68:71], v[28:31], v[68:71]\n" \
" ds_read_b128 v[16:19], v12 offset:36864\n" \
" v_permlane16_swap_b32 v80, v82\n" \
" v_mfma_f32_16x16x32_bf16 v[72:75], a[196:199], v[24:27], v[72:75]\n" \
" ds_read_b128 v[20:23], v12 offset:53248\n" \
" v_permlane16_swap_b32 v81, v83\n" \
" v_mfma_f32_16x16x32_bf16 v[76:79], a[196:199], v[28:31], v[76:79]\n" \
" s_waitcnt lgkmcnt(4)\n" \
" buffer_store_dwordx4 v[80:83], v5, s[36:39], s57 offen sc1\n" \
" v_mfma_f32_16x16x32_bf16 v[64:67], a[72:75], v[32:35], v[64:67]\n" \
" v_mfma_f32_16x16x32_bf16 v[68:71], a[72:75], v[36:39], v[68:71]\n" \
" ds_read_b128 v[24:27], v12 offset:37888\n" \
" v_mfma_f32_16x16x32_bf16 v[72:75], a[200:203], v[32:35], v[72:75]\n" \
" ds_read_b128 v[28:31], v12 offset:54272\n" \
" v_mfma_f32_16x16x32_bf16 v[76:79], a[200:203], v[36:39], v[76:79]\n" \
" s_waitcnt lgkmcnt(4)\n" \
" v_mfma_f32_16x16x32_bf16 v[64:67], a[76:79], v[40:43], v[64:67]\n" \
" buffer_load_dwordx4 v4, s[36:39], s61 offen offset:2048 sc1 lds\n" \
" v_mfma_f32_16x16x32_bf16 v[68:71], a[76:79], v[44:47], v[68:71]\n" \
" ds_read_b128 v[32:35], v12 offset:38912\n" \
" v_mfma_f32_16x16x32_bf16 v[72:75], a[204:207], v[40:43], v[72:75]\n" \
" ds_read_b128 v[36:39], v12 offset:55296\n" \
" v_mfma_f32_16x16x32_bf16 v[76:79], a[204:207], v[44:47], v[76:79]\n" \
" s_waitcnt lgkmcnt(4)\n" \
" v_mfma_f32_16x16x32_bf16 v[64:67], a[80:83], v[16:19], v[64:67]\n" \
" buffer_load_dwordx4 v4, s[36:39], s61 offen offset:3072 sc1 lds\n" \
" v_mfma_f32_16x16x32_bf16 v[68:71], a[80:83], v[20:23], v[68:71]\n" \
" ds_read_b128 v[40:43], v12 offset:39936\n" \
" v_mfma_f32_16x16x32_bf16 v[72:75], a[208:211], v[16:19], v[72:75]\n" \
" ds_read_b128 v[44:47], v12 offset:56320\n" \
" v_mfma_f32_16x16x32_bf16 v[76:79], a[208:211], v[20:23], v[76:79]\n" \
" s_waitcnt lgkmcnt(4)\n" \
" v_mfma_f32_16x16x32_bf16 v[64:67], a[84:87], v[24:27], v[64:67]\n" \
" v_mfma_f32_16x16x32_bf16 v[68:71], a[84:87], v[28:31], v[68:71]\n" \
" ds_read_b128 v[16:19], v12 offset:40960\n" \
" v_mfma_f32_16x16x32_bf16 v[72:75], a[212:215], v[24:27], v[72:75]\n" \
" ds_read_b128 v[20:23], v12 offset:57344\n" \
" v_mfma_f32_16x16x32_bf16 v[76:79], a[212:215], v[28:31], v[76:79]\n" \
" s_waitcnt lgkmcnt(4)\n" \
" v_mfma_f32_16x16x32_bf16 v[64:67], a[88:91], v[32:35], v[64:67]\n" \
" s_add_u32 m0, s55, 36864\n" \
" s_add_u32 s61, s61, 4096\n" \
" buffer_load_dwordx4 v4, s[36:39], s61 offen offset:0 sc1 lds\n" \
" v_mfma_f32_16x16x32_bf16 v[68:71], a[88:91], v[36:39], v[68:71]\n" \
" ds_read_b128 v[24:27], v12 offset:41984\n" \
" v_mfma_f32_16x16x32_bf16 v[72:75], a[216:219], v[32:35], v[72:75]\n" \
" ds_read_b128 v[28:31], v12 offset:58368\n" \
" v_mfma_f32_16x16x32_bf16 v[76:79], a[216:219], v[36:39], v[76:79]\n" \
" s_waitcnt lgkmcnt(4)\n" \
" v_mfma_f32_16x16x32_bf16 v[64:67], a[92:95], v[40:43], v[64:67]\n" \
" buffer_load_dwordx4 v4, s[36:39], s61 offen offset:1024 sc1 lds\n" \
" v_mfma_f32_16x16x32_bf16 v[68:71], a[92:95], v[44:47], v[68:71]\n" \
" ds_read_b128 v[32:35], v12 offset:43008\n" \
" v_mfma_f32_16x16x32_bf16 v[72:75], a[220:223], v[40:43], v[72:75]\n" \
" ds_read_b128 v[36:39], v12 offset:59392\n" \
" v_mfma_f32_16x16x32_bf16 v[76:79], a[220:223], v[44:47], v[76:79]\n" \
" s_waitcnt lgkmcnt(4)\n" \
" v_mfma_f32_16x16x32_bf16 v[64:67], a[96:99], v[16:19], v[64:67]\n" \
" v_mfma_f32_16x16x32_bf16 v[68:71], a[96:99], v[20:23], v[68:71]\n" \
" ds_read_b128 v[40:43], v12 offset:44032\n" \
" v_mfma_f32_16x16x32_bf16 v[72:75], a[224:227], v[16:19], v[72:75]\n" \
" ds_read_b128 v[44:47], v12 offset:60416\n" \
" v_mfma_f32_16x16x32_bf16 v[76:79], a[224:227], v[20:23], v[76:79]\n" \
" s_waitcnt lgkmcnt(4)\n" \
" v_mfma_f32_16x16x32_bf16 v[64:67], a[100:103], v[24:27], v[64:67]\n" \
" buffer_load_dwordx4 v4, s[36:39], s61 offen offset:2048 sc1 lds\n" \
" v_mfma_f32_16x16x32_bf16 v[68:71], a[100:103], v[28:31], v[68:71]\n" \
" ds_read_b128 v[16:19], v12 offset:45056\n" \
" v_mfma_f32_16x16x32_bf16 v[72:75], a[228:231], v[24:27], v[72:75]\n" \
" ds_read_b128 v[20:23], v12 offset:61440\n" \
" v_mfma_f32_16x16x32_bf16 v[76:79], a[228:231], v[28:31], v[76:79]\n" \
" s_waitcnt lgkmcnt(4)\n" \
" v_mfma_f32_16x16x32_bf16 v[64:67], a[104:107], v[32:35], v[64:67]\n" \
" buffer_load_dwordx4 v4, s[36:39], s61 offen offset:3072 sc1 lds\n" \
" v_mfma_f32_16x16x32_bf16 v[68:71], a[104:107], v[36:39], v[68:71]\n" \
" ds_read_b128 v[24:27], v12 offset:46080\n" \
" v_mfma_f32_16x16x32_bf16 v[72:75], a[232:235], v[32:35], v[72:75]\n" \
" ds_read_b128 v[28:31], v12 offset:62464\n" \
" v_mfma_f32_16x16x32_bf16 v[76:79], a[232:235], v[36:39], v[76:79]\n" \
" s_waitcnt lgkmcnt(4)\n" \
" v_mfma_f32_16x16x32_bf16 v[64:67], a[108:111], v[40:43], v[64:67]\n" \
" v_mfma_f32_16x16x32_bf16 v[68:71], a[108:111], v[44:47], v[68:71]\n" \
" ds_read_b128 v[32:35], v12 offset:47104\n" \
" v_mfma_f32_16x16x32_bf16 v[72:75], a[236:239], v[40:43], v[72:75]\n" \
" ds_read_b128 v[36:39], v12 offset:63488\n" \
" v_mfma_f32_16x16x32_bf16 v[76:79], a[236:239], v[44:47], v[76:79]\n" \
" s_waitcnt lgkmcnt(4)\n" \
" v_mfma_f32_16x16x32_bf16 v[64:67], a[112:115], v[16:19], v[64:67]\n" \
" v_mfma_f32_16x16x32_bf16 v[68:71], a[112:115], v[20:23], v[68:71]\n" \
" ds_read_b128 v[40:43], v12 offset:48128\n" \
" v_mfma_f32_16x16x32_bf16 v[72:75], a[240:243], v[16:19], v[72:75]\n" \
" ds_read_b128 v[44:47], v12 offset:64512\n" \
" v_mfma_f32_16x16x32_bf16 v[76:79], a[240:243], v[20:23], v[76:79]\n" \
" s_waitcnt lgkmcnt(4)\n" \
" v_mfma_f32_16x16x32_bf16 v[64:67], a[116:119], v[24:27], v[64:67]\n" \
" s_waitcnt vmcnt(10)\n" \
" s_barrier\n" \
" v_mfma_f32_16x16x32_bf16 v[68:71], a[116:119], v[28:31], v[68:71]\n" \
" ds_read_b128 v[16:19], v10 offset:0\n" \
" v_mfma_f32_16x16x32_bf16 v[72:75], a[244:247], v[24:27], v[72:75]\n" \
" ds_read_b128 v[20:23], v10 offset:16384\n" \
" v_mfma_f32_16x16x32_bf16 v[76:79], a[244:247], v[28:31], v[76:79]\n" \
" s_waitcnt lgkmcnt(4)\n" \
" v_mfma_f32_16x16x32_bf16 v[64:67], a[120:123], v[32:35], v[64:67]\n" \
" v_mfma_f32_16x16x32_bf16 v[68:71], a[120:123], v[36:39], v[68:71]\n" \
" ds_read_b128 v[24:27], v10 offset:1024\n" \
" v_mfma_f32_16x16x32_bf16 v[72:75], a[248:251], v[32:35], v[72:75]\n" \
" ds_read_b128 v[28:31], v10 offset:17408\n" \
" v_mfma_f32_16x16x32_bf16 v[76:79], a[248:251], v[36:39], v[76:79]\n" \
" s_waitcnt lgkmcnt(4)\n" \
" v_mfma_f32_16x16x32_bf16 v[64:67], a[124:127], v[40:43], v[64:67]\n" \
" v_mfma_f32_16x16x32_bf16 v[68:71], a[124:127], v[44:47], v[68:71]\n" \
" ds_read_b128 v[32:35], v10 offset:2048\n" \
" v_mfma_f32_16x16x32_bf16 v[72:75], a[252:255], v[40:43], v[72:75]\n" \
" ds_read_b128 v[36:39], v10 offset:18432\n" \
" v_mfma_f32_16x16x32_bf16 v[76:79], a[252:255], v[44:47], v[76:79]\n" \
" s_waitcnt lgkmcnt(4)\n" \
" s_add_u32 s57, s57, s49\n" \
" s_add_u32 s58, s58, s49\n" \
" s_add_u32 s59, s59, 1\n" \
" v_mov_b32_e64 v5, v7\n" \
" v_mov_b32_e64 v6, v11\n" \
" s_cmp_ge_u32 s59, s47\n" \
" s_cbranch_scc0 Lp1g1_loop\n" \
"; cold\n" \
"Lp1g1_out1:\n" \
" s_nop 7\n" \
" s_nop 7\n" \
" v_mul_f32 v64, 0xbfb8aa3b, v64\n" \
" v_mul_f32 v65, 0xbfb8aa3b, v65\n" \
" v_mul_f32 v66, 0xbfb8aa3b, v66\n" \
" v_mul_f32 v67, 0xbfb8aa3b, v67\n" \
" v_exp_f32 v64, v64\n" \
" v_exp_f32 v65, v65\n" \
" v_exp_f32 v66, v66\n" \
" v_exp_f32 v67, v67\n" \
" v_add_f32 v64, 1.0, v64\n" \
" v_add_f32 v65, 1.0, v65\n" \
" v_add_f32 v66, 1.0, v66\n" \
" v_add_f32 v67, 1.0, v67\n" \
" v_rcp_f32 v64, v64\n" \
" v_rcp_f32 v65, v65\n" \
" v_rcp_f32 v66, v66\n" \
" v_rcp_f32 v67, v67\n" \
" v_mul_f32 v64, v64, v72\n" \
" v_mul_f32 v65, v65, v73\n" \
" v_mul_f32 v66, v66, v74\n" \
" v_mul_f32 v67, v67, v75\n" \
" v_cvt_pk_bf16_f32 v80, v64, v65\n" \
" v_cvt_pk_bf16_f32 v81, v66, v67\n" \
" v_mul_f32 v68, 0xbfb8aa3b, v68\n" \
" v_mul_f32 v69, 0xbfb8aa3b, v69\n" \
" v_mul_f32 v70, 0xbfb8aa3b, v70\n" \
" v_mul_f32 v71, 0xbfb8aa3b, v71\n" \
" v_exp_f32 v68, v68\n" \
" v_exp_f32 v69, v69\n" \
" v_exp_f32 v70, v70\n" \
" v_exp_f32 v71, v71\n" \
" v_add_f32 v68, 1.0, v68\n" \
" v_add_f32 v69, 1.0, v69\n" \
" v_add_f32 v70, 1.0, v70\n" \
" v_add_f32 v71, 1.0, v71\n" \
" v_rcp_f32 v68, v68\n" \
" v_rcp_f32 v69, v69\n" \
" v_rcp_f32 v70, v70\n" \
" v_rcp_f32 v71, v71\n" \
" v_mul_f32 v68, v68, v76\n" \
" v_mul_f32 v69, v69, v77\n" \
" v_mul_f32 v70, v70, v78\n" \
" v_mul_f32 v71, v71, v79\n" \
" v_cvt_pk_bf16_f32 v82, v68, v69\n" \
" v_cvt_pk_bf16_f32 v83, v70, v71\n" \
" s_nop 0\n" \
" s_nop 0\n" \
" v_permlane16_swap_b32 v80, v82\n" \
" v_permlane16_swap_b32 v81, v83\n" \
" buffer_store_dwordx4 v[80:83], v5, s[36:39], s57 offen sc1\n" \
" s_branch Lp1g1_end\n" \
"Lp1g1_out0:\n" \
" s_nop 7\n" \
" s_nop 7\n" \
" v_mul_f32 v48, 0xbfb8aa3b, v48\n" \
" v_mul_f32 v49, 0xbfb8aa3b, v49\n" \
" v_mul_f32 v50, 0xbfb8aa3b, v50\n" \
" v_mul_f32 v51, 0xbfb8aa3b, v51\n" \
" v_exp_f32 v48, v48\n" \
" v_exp_f32 v49, v49\n" \
" v_exp_f32 v50, v50\n" \
" v_exp_f32 v51, v51\n" \
" v_add_f32 v48, 1.0, v48\n" \
" v_add_f32 v49, 1.0, v49\n" \
" v_add_f32 v50, 1.0, v50\n" \
" v_add_f32 v51, 1.0, v51\n" \
" v_rcp_f32 v48, v48\n" \
" v_rcp_f32 v49, v49\n" \
" v_rcp_f32 v50, v50\n" \
" v_rcp_f32 v51, v51\n" \
" v_mul_f32 v48, v48, v56\n" \
" v_mul_f32 v49, v49, v57\n" \
" v_mul_f32 v50, v50, v58\n" \
" v_mul_f32 v51, v51, v59\n" \
" v_cvt_pk_bf16_f32 v80, v48, v49\n" \
" v_cvt_pk_bf16_f32 v81, v50, v51\n" \
" v_mul_f32 v52, 0xbfb8aa3b, v52\n" \
" v_mul_f32 v53, 0xbfb8aa3b, v53\n" \
" v_mul_f32 v54, 0xbfb8aa3b, v54\n" \
" v_mul_f32 v55, 0xbfb8aa3b, v55\n" \
" v_exp_f32 v52, v52\n" \
" v_exp_f32 v53, v53\n" \
" v_exp_f32 v54, v54\n" \
" v_exp_f32 v55, v55\n" \
" v_add_f32 v52, 1.0, v52\n" \
" v_add_f32 v53, 1.0, v53\n" \
" v_add_f32 v54, 1.0, v54\n" \
" v_add_f32 v55, 1.0, v55\n" \
" v_rcp_f32 v52, v52\n" \
" v_rcp_f32 v53, v53\n" \
" v_rcp_f32 v54, v54\n" \
" v_rcp_f32 v55, v55\n" \
" v_mul_f32 v52, v52, v60\n" \
" v_mul_f32 v53, v53, v61\n" \
" v_mul_f32 v54, v54, v62\n" \
" v_mul_f32 v55, v55, v63\n" \
" v_cvt_pk_bf16_f32 v82, v52, v53\n" \
" v_cvt_pk_bf16_f32 v83, v54, v55\n" \
" s_nop 0\n" \
" s_nop 0\n" \
" v_permlane16_swap_b32 v80, v82\n" \
" v_permlane16_swap_b32 v81, v83\n" \
" buffer_store_dwordx4 v[80:83], v5, s[36:39], s57 offen sc1\n" \
"Lp1g1_end:\n" \
" s_waitcnt vmcnt(0) lgkmcnt(0)\n" \
" s_barrier\n" \
" v_mov_b32 v13, s59\n" \
" buffer_store_dword v13, v6, s[36:39], 0 offen sc1\n" \
" s_waitcnt vmcnt(0) lgkmcnt(0)\n" \
" s_branch Lp1g1_x\n" \
"Lp1g1_l0:\n" \
" s_nop 7\n" \
" s_nop 7\n" \
" s_waitcnt vmcnt(0) lgkmcnt(0)\n" \
" s_mov_b32 s62, 0\n" \
"Lp1g1_l0a:\n" \
" buffer_load_dword v15, v14, s[36:39], 0 offen sc1\n" \
" s_waitcnt vmcnt(0)\n" \
" v_cmp_gt_u32 vcc, 32, v15\n" \
" s_nop 4\n" \
" s_and_b64 vcc, vcc, 1\n" \
" s_cbranch_vccz Lp1g1_lk\n" \
" s_sleep 4\n" \
" s_add_u32 s62, s62, 1\n" \
" s_cmp_lt_u32 s62, 0x10000\n" \
" s_cbranch_scc1 Lp1g1_l0a\n" \
" v_mov_b32 v15, 256\n" \
" buffer_store_dword v15, v15, s[36:39], 0 offen sc1\n" \
" s_waitcnt vmcnt(0) lgkmcnt(0)\n" \
" s_branch Lp1g1_lk\n" \
"Lp1g1_l1:\n" \
" s_nop 7\n" \
" s_nop 7\n" \
" s_waitcnt vmcnt(0) lgkmcnt(0)\n" \
" s_mov_b32 s62, 0\n" \
"Lp1g1_l1a:\n" \
" buffer_load_dword v15, v14, s[36:39], 0 offen sc1\n" \
" s_waitcnt vmcnt(0)\n" \
" v_cmp_gt_u32 vcc, 32, v15\n" \
" s_nop 4\n" \
" s_and_b64 vcc, vcc, 15\n" \
" s_cbranch_vccz Lp1g1_lk\n" \
" s_sleep 4\n" \
" s_add_u32 s62, s62, 1\n" \
" s_cmp_lt_u32 s62, 0x10000\n" \
" s_cbranch_scc1 Lp1g1_l1a\n" \
" v_mov_b32 v15, 256\n" \
" buffer_store_dword v15, v15, s[36:39], 0 offen sc1\n" \
" s_waitcnt vmcnt(0) lgkmcnt(0)\n" \
" s_branch Lp1g1_lk\n" \
"Lp1g1_l2:\n" \
" s_nop 7\n" \
" s_nop 7\n" \
" s_waitcnt vmcnt(0) lgkmcnt(0)\n" \
" s_mov_b32 s62, 0\n" \
"Lp1g1_l2a:\n" \
" buffer_load_dword v15, v14, s[36:39], 0 offen sc1\n" \
" s_waitcnt vmcnt(0)\n" \
" v_cmp_gt_u32 vcc, 32, v15\n" \
" s_nop 4\n" \
" s_and_b64 vcc, vcc, 15\n" \
" s_cbranch_vccz Lp1g1_lm\n" \
" s_sleep 4\n" \
" s_add_u32 s62, s62, 1\n" \
" s_cmp_lt_u32 s62, 0x10000\n" \
" s_cbranch_scc1 Lp1g1_l2a\n" \
" v_mov_b32 v15, 256\n" \
" buffer_store_dword v15, v15, s[36:39], 0 offen sc1\n" \
" s_waitcnt vmcnt(0) lgkmcnt(0)\n" \
" s_branch Lp1g1_lm\n" \
"Lp1g1_x:\n"
#define ASM_P1_HEAD \
" s_nop 7\n" \
" s_nop 7\n" \
" s_lshr_b32 s52, s48, 1\n" \
" s_lshl_b32 s52, s52, 13\n" \
" s_and_b32 s53, s48, 1\n" \
" s_lshl_b32 s53, s53, 10\n" \
" s_add_u32 s52, s52, s53\n" \
" s_mov_b32 s54, 0\n" \
" buffer_load_dwordx4 v[20:23], v16, s[64:67], 0 offen offset:0\n" \
" buffer_load_dwordx4 v[24:27], v16, s[64:67], 0 offen offset:64\n" \
" buffer_load_dwordx4 v[28:31], v16, s[64:67], 0 offen offset:128\n" \
" buffer_load_dwordx4 v[32:35], v16, s[64:67], 0 offen offset:192\n" \
" buffer_load_dwordx4 v[36:39], v16, s[64:67], 0 offen offset:256\n" \
" buffer_load_dwordx4 v[40:43], v16, s[64:67], 0 offen offset:320\n" \
" buffer_load_dwordx4 v[44:47], v16, s[64:67], 0 offen offset:384\n" \
" buffer_load_dwordx4 v[48:51], v16, s[64:67], 0 offen offset:448\n" \
" s_add_u32 m0, s52, 0\n" \
" s_nop 0\n" \
" buffer_load_dwordx4 v1, s[40:43], s54 offen nt lds\n" \
" s_add_u32 s54, s54, s44\n" \
" s_add_u32 m0, s52, 2048\n" \
" s_nop 0\n" \
" buffer_load_dwordx4 v1, s[40:43], s54 offen nt lds\n" \
" s_add_u32 s54, s54, s44\n" \
" s_add_u32 m0, s52, 4096\n" \
" s_nop 0\n" \
" buffer_load_dwordx4 v1, s[40:43], s54 offen nt lds\n" \
" s_add_u32 s54, s54, s44\n" \
" s_add_u32 m0, s52, 6144\n" \
" s_nop 0\n" \
" buffer_load_dwordx4 v1, s[40:43], s54 offen nt lds\n" \
" s_add_u32 s54, s54, s44\n" \
" s_add_u32 m0, s52, 16384\n" \
" s_nop 0\n" \
" buffer_load_dwordx4 v1, s[40:43], s54 offen nt lds\n" \
" s_add_u32 s54, s54, s44\n" \
" s_add_u32 m0, s52, 18432\n" \
" s_nop 0\n" \
" buffer_load_dwordx4 v1, s[40:43], s54 offen nt lds\n" \
" s_add_u32 s54, s54, s44\n" \
" s_add_u32 m0, s52, 20480\n" \
" s_nop 0\n" \
" buffer_load_dwordx4 v1, s[40:43], s54 offen nt lds\n" \
" s_add_u32 s54, s54, s44\n" \
" s_add_u32 m0, s52, 22528\n" \
" s_nop 0\n" \
" buffer_load_dwordx4 v1, s[40:43], s54 offen nt lds\n" \
" s_add_u32 s54, s54, s44\n" \
" s_add_u32 m0, s52, 32768\n" \
" s_nop 0\n" \
" buffer_load_dwordx4 v1, s[40:43], s54 offen nt lds\n" \
" s_add_u32 s54, s54, s44\n" \
" s_add_u32 m0, s52, 34816\n" \
" s_nop 0\n" \
" buffer_load_dwordx4 v1, s[40:43], s54 offen nt lds\n" \
" s_add_u32 s54, s54, s44\n" \
" s_add_u32 m0, s52, 36864\n" \
" s_nop 0\n" \
" buffer_load_dwordx4 v1, s[40:43], s54 offen nt lds\n" \
" s_add_u32 s54, s54, s44\n" \
" s_add_u32 m0, s52, 38912\n" \
" s_nop 0\n" \
" buffer_load_dwordx4 v1, s[40:43], s54 offen nt lds\n" \
" s_add_u32 s54, s54, s44\n" \
" s_add_u32 m0, s52, 49152\n" \
" s_nop 0\n" \
" buffer_load_dwordx4 v1, s[40:43], s54 offen nt lds\n" \
" s_add_u32 s54, s54, s44\n" \
" s_add_u32 m0, s52, 51200\n" \
" s_nop 0\n" \
" buffer_load_dwordx4 v1, s[40:43], s54 offen nt lds\n" \
" s_add_u32 s54, s54, s44\n" \
" s_add_u32 m0, s52, 53248\n" \
" s_nop 0\n" \
" buffer_load_dwordx4 v1, s[40:43], s54 offen nt lds\n" \
" s_add_u32 s54, s54, s44\n" \
" s_add_u32 m0, s52, 55296\n" \
" s_nop 0\n" \
" buffer_load_dwordx4 v1, s[40:43], s54 offen nt lds\n" \
" s_add_u32 s54, s54, s44\n" \
" s_add_u32 m0, s52, 65536\n" \
" s_nop 0\n" \
" buffer_load_dwordx4 v1, s[40:43], s54 offen nt lds\n" \
" s_add_u32 s54, s54, s44\n" \
" s_add_u32 m0, s52, 67584\n" \
" s_nop 0\n" \
" buffer_load_dwordx4 v1, s[40:43], s54 offen nt lds\n" \
" s_add_u32 s54, s54, s44\n" \
" s_add_u32 m0, s52, 69632\n" \
" s_nop 0\n" \
" buffer_load_dwordx4 v1, s[40:43], s54 offen nt lds\n" \
" s_add_u32 s54, s54, s44\n" \
" s_add_u32 m0, s52, 71680\n" \
" s_nop 0\n" \
" buffer_load_dwordx4 v1, s[40:43], s54 offen nt lds\n" \
" s_add_u32 s54, s54, s44\n" \
" s_add_u32 m0, s52, 81920\n" \
" s_nop 0\n" \
" buffer_load_dwordx4 v1, s[40:43], s54 offen nt lds\n" \
" s_add_u32 s54, s54, s44\n" \
" s_add_u32 m0, s52, 83968\n" \
" s_nop 0\n" \
" buffer_load_dwordx4 v1, s[40:43], s54 offen nt lds\n" \
" s_add_u32 s54, s54, s44\n" \
" s_add_u32 m0, s52, 86016\n" \
" s_nop 0\n" \
" buffer_load_dwordx4 v1, s[40:43], s54 offen nt lds\n" \
" s_add_u32 s54, s54, s44\n" \
" s_add_u32 m0, s52, 88064\n" \
" s_nop 0\n" \
" buffer_load_dwordx4 v1, s[40:43], s54 offen nt lds\n" \
" s_add_u32 s54, s54, s44\n" \
" s_add_u32 m0, s52, 98304\n" \
" s_nop 0\n" \
" buffer_load_dwordx4 v1, s[40:43], s54 offen nt lds\n" \
" s_add_u32 s54, s54, s44\n" \
" s_add_u32 m0, s52, 100352\n" \
" s_nop 0\n" \
" buffer_load_dwordx4 v1, s[40:43], s54 offen nt lds\n" \
" s_add_u32 s54, s54, s44\n" \
" s_add_u32 m0, s52, 102400\n" \
" s_nop 0\n" \
" buffer_load_dwordx4 v1, s[40:43], s54 offen nt lds\n" \
" s_add_u32 s54, s54, s44\n" \
" s_add_u32 m0, s52, 104448\n" \
" s_nop 0\n" \
" buffer_load_dwordx4 v1, s[40:43], s54 offen nt lds\n" \
" s_add_u32 s54, s54, s44\n" \
" s_add_u32 m0, s52, 114688\n" \
" s_nop 0\n" \
" buffer_load_dwordx4 v1, s[40:43], s54 offen nt lds\n" \
" s_add_u32 s54, s54, s44\n" \
" s_add_u32 m0, s52, 116736\n" \
" s_nop 0\n" \
" buffer_load_dwordx4 v1, s[40:43], s54 offen nt lds\n" \
" s_add_u32 s54, s54, s44\n" \
" s_add_u32 m0, s52, 118784\n" \
" s_nop 0\n" \
" buffer_load_dwordx4 v1, s[40:43], s54 offen nt lds\n" \
" s_add_u32 s54, s54, s44\n" \
" s_add_u32 m0, s52, 120832\n" \
" s_nop 0\n" \
" buffer_load_dwordx4 v1, s[40:43], s54 offen nt lds\n" \
" s_add_u32 s54, s54, s44\n" \
" s_waitcnt vmcnt(32)\n"
#define CLOB_P1 \
"a0", "a1", "a2", "a3", "a4", "a5", "a6", "a7", "a8", "a9", "a10", "a11", "a12", "a13", "a14", "a15", "a16", "a17", "a18", "a19", "a20", "a21", "a22", "a23", "a24", "a25", "a26", "a27", "a28", "a29", "a30", "a31", "a32", "a33", "a34", "a35", "a36", "a37", "a38", "a39", "a40", "a41", "a42", "a43", "a44", "a45", "a46", "a47", "a48", "a49", "a50", "a51", "a52", "a53", "a54", "a55", "a56", "a57", "a58", "a59", "a60", "a61", "a62", "a63", "a64", "a65", "a66", "a67", "a68", "a69", "a70", "a71", "a72", "a73", "a74", "a75", "a76", "a77", "a78", "a79", "a80", "a81", "a82", "a83", "a84", "a85", "a86", "a87", "a88", "a89", "a90", "a91", "a92", "a93", "a94", "a95", "a96", "a97", "a98", "a99", "a100", "a101", "a102", "a103", "a104", "a105", "a106", "a107", "a108", "a109", "a110", "a111", "a112", "a113", "a114", "a115", "a116", "a117", "a118", "a119", "a120", "a121", "a122", "a123", "a124", "a125", "a126", "a127", "a128", "a129", "a130", "a131", "a132", "a133", "a134", "a135", "a136", "a137", "a138", "a139", "a140", "a141", "a142", "a143", "a144", "a145", "a146", "a147", "a148", "a149", "a150", "a151", "a152", "a153", "a154", "a155", "a156", "a157", "a158", "a159", "a160", "a161", "a162", "a163", "a164", "a165", "a166", "a167", "a168", "a169", "a170", "a171", "a172", "a173", "a174", "a175", "a176", "a177", "a178", "a179", "a180", "a181", "a182", "a183", "a184", "a185", "a186", "a187", "a188", "a189", "a190", "a191", "a192", "a193", "a194", "a195", "a196", "a197", "a198", "a199", "a200", "a201", "a202", "a203", "a204", "a205", "a206", "a207", "a208", "a209", "a210", "a211", "a212", "a213", "a214", "a215", "a216", "a217", "a218", "a219", "a220", "a221", "a222", "a223", "a224", "a225", "a226", "a227", "a228", "a229", "a230", "a231", "a232", "a233", "a234", "a235", "a236", "a237", "a238", "a239", "a240", "a241", "a242", "a243", "a244", "a245", "a246", "a247", "a248", "a249", "a250", "a251", "a252", "a253", "a254", "a255", "s52", "s53", "s54", "s55", "s56", "s57", "s58", "s59", "s60", "s61", "s62", "v7", "v8", "v9", "v10", "v11", "v12", "v13", "v14", "v15", "v16", "v17", "v18", "v19", "v20", "v21", "v22", "v23", "v24", "v25", "v26", "v27", "v28", "v29", "v30", "v31", "v32", "v33", "v34", "v35", "v36", "v37", "v38", "v39", "v40", "v41", "v42", "v43", "v44", "v45", "v46", "v47", "v48", "v49", "v50", "v51", "v52", "v53", "v54", "v55", "v56", "v57", "v58", "v59", "v60", "v61", "v62", "v63", "v64", "v65", "v66", "v67", "v68", "v69", "v70", "v71", "v72", "v73", "v74", "v75", "v76", "v77", "v78", "v79", "v80", "v81", "v82", "v83", "v84", "v85", "v86", "v87", "vcc", "scc", "memory"
#define ASM_QK \
" s_nop 7\n" \
" s_nop 7\n" \
" v_add_u32 v36, 0x10000, v34\n" \
" v_add_u32 v38, 0x20000, v34\n" \
" v_add_u32 v37, 0x10000, v35\n" \
" v_add_u32 v39, 0x20000, v35\n" \
" s_lshl_b32 s62, s48, 12\n" \
" s_add_u32 m0, s62, 147456\n" \
" s_mov_b32 s54, 524288\n" \
" buffer_load_dwordx4 v32, s[40:43], s54 offen nt lds\n" \
" s_add_u32 m0, s62, 148480\n" \
" s_mov_b32 s54, 524288\n" \
" buffer_load_dwordx4 v33, s[40:43], s54 offen nt lds\n" \
" s_add_u32 m0, s62, 149504\n" \
" s_mov_b32 s54, 655360\n" \
" buffer_load_dwordx4 v32, s[40:43], s54 offen nt lds\n" \
" s_add_u32 m0, s62, 150528\n" \
" s_mov_b32 s54, 655360\n" \
" buffer_load_dwordx4 v33, s[40:43], s54 offen nt lds\n" \
" s_add_u32 m0, s62, 131072\n" \
" s_mov_b32 s54, 786432\n" \
" buffer_load_dwordx4 v32, s[40:43], s54 offen nt lds\n" \
" s_add_u32 m0, s62, 132096\n" \
" s_mov_b32 s54, 786432\n" \
" buffer_load_dwordx4 v33, s[40:43], s54 offen nt lds\n" \
" s_add_u32 m0, s62, 133120\n" \
" s_mov_b32 s54, 917504\n" \
" buffer_load_dwordx4 v32, s[40:43], s54 offen nt lds\n" \
" s_add_u32 m0, s62, 134144\n" \
" s_mov_b32 s54, 917504\n" \
" buffer_load_dwordx4 v33, s[40:43], s54 offen nt lds\n" \
" s_add_u32 m0, s62, 114688\n" \
" s_mov_b32 s54, 1572864\n" \
" buffer_load_dwordx4 v32, s[40:43], s54 offen nt lds\n" \
" s_add_u32 m0, s62, 115712\n" \
" s_mov_b32 s54, 1572864\n" \
" buffer_load_dwordx4 v33, s[40:43], s54 offen nt lds\n" \
" s_add_u32 m0, s62, 116736\n" \
" s_mov_b32 s54, 1703936\n" \
" buffer_load_dwordx4 v32, s[40:43], s54 offen nt lds\n" \
" s_add_u32 m0, s62, 117760\n" \
" s_mov_b32 s54, 1703936\n" \
" buffer_load_dwordx4 v33, s[40:43], s54 offen nt lds\n" \
" s_add_u32 m0, s62, 98304\n" \
" s_mov_b32 s54, 1835008\n" \
" buffer_load_dwordx4 v32, s[40:43], s54 offen nt lds\n" \
" s_add_u32 m0, s62, 99328\n" \
" s_mov_b32 s54, 1835008\n" \
" buffer_load_dwordx4 v33, s[40:43], s54 offen nt lds\n" \
" s_add_u32 m0, s62, 100352\n" \
" s_mov_b32 s54, 1966080\n" \
" buffer_load_dwordx4 v32, s[40:43], s54 offen nt lds\n" \
" s_add_u32 m0, s62, 101376\n" \
" s_mov_b32 s54, 1966080\n" \
" buffer_load_dwordx4 v33, s[40:43], s54 offen nt lds\n" \
" s_add_u32 m0, s62, 81920\n" \
" s_mov_b32 s54, 2621440\n" \
" buffer_load_dwordx4 v32, s[40:43], s54 offen nt lds\n" \
" s_add_u32 m0, s62, 82944\n" \
" s_mov_b32 s54, 2621440\n" \
" buffer_load_dwordx4 v33, s[40:43], s54 offen nt lds\n" \
" s_add_u32 m0, s62, 83968\n" \
" s_mov_b32 s54, 2752512\n" \
" buffer_load_dwordx4 v32, s[40:43], s54 offen nt lds\n" \
" s_add_u32 m0, s62, 84992\n" \
" s_mov_b32 s54, 2752512\n" \
" buffer_load_dwordx4 v33, s[40:43], s54 offen nt lds\n" \
" s_add_u32 m0, s62, 65536\n" \
" s_mov_b32 s54, 2883584\n" \
" buffer_load_dwordx4 v32, s[40:43], s54 offen nt lds\n" \
" s_add_u32 m0, s62, 66560\n" \
" s_mov_b32 s54, 2883584\n" \
" buffer_load_dwordx4 v33, s[40:43], s54 offen nt lds\n" \
" s_add_u32 m0, s62, 67584\n" \
" s_mov_b32 s54, 3014656\n" \
" buffer_load_dwordx4 v32, s[40:43], s54 offen nt lds\n" \
" s_add_u32 m0, s62, 68608\n" \
" s_mov_b32 s54, 3014656\n" \
" buffer_load_dwordx4 v33, s[40:43], s54 offen nt lds\n" \
" s_add_u32 m0, s62, 49152\n" \
" s_mov_b32 s54, 3670016\n" \
" buffer_load_dwordx4 v32, s[40:43], s54 offen nt lds\n" \
" s_add_u32 m0, s62, 50176\n" \
" s_mov_b32 s54, 3670016\n" \
" buffer_load_dwordx4 v33, s[40:43], s54 offen nt lds\n" \
" s_add_u32 m0, s62, 51200\n" \
" s_mov_b32 s54, 3801088\n" \
" buffer_load_dwordx4 v32, s[40:43], s54 offen nt lds\n" \
" s_add_u32 m0, s62, 52224\n" \
" s_mov_b32 s54, 3801088\n" \
" buffer_load_dwordx4 v33, s[40:43], s54 offen nt lds\n" \
" s_add_u32 m0, s62, 32768\n" \
" s_mov_b32 s54, 3932160\n" \
" buffer_load_dwordx4 v32, s[40:43], s54 offen nt lds\n" \
" s_add_u32 m0, s62, 33792\n" \
" s_mov_b32 s54, 3932160\n" \
" buffer_load_dwordx4 v33, s[40:43], s54 offen nt lds\n" \
" s_add_u32 m0, s62, 34816\n" \
" s_mov_b32 s54, 4063232\n" \
" buffer_load_dwordx4 v32, s[40:43], s54 offen nt lds\n" \
" s_add_u32 m0, s62, 35840\n" \
" s_mov_b32 s54, 4063232\n" \
" buffer_load_dwordx4 v33, s[40:43], s54 offen nt lds\n" \
" s_add_u32 m0, s62, 16384\n" \
" s_mov_b32 s54, 4718592\n" \
" buffer_load_dwordx4 v32, s[40:43], s54 offen nt lds\n" \
" s_add_u32 m0, s62, 17408\n" \
" s_mov_b32 s54, 4718592\n" \
" buffer_load_dwordx4 v33, s[40:43], s54 offen nt lds\n" \
" s_add_u32 m0, s62, 18432\n" \
" s_mov_b32 s54, 4849664\n" \
" buffer_load_dwordx4 v32, s[40:43], s54 offen nt lds\n" \
" s_add_u32 m0, s62, 19456\n" \
" s_mov_b32 s54, 4849664\n" \
" buffer_load_dwordx4 v33, s[40:43], s54 offen nt lds\n" \
" s_add_u32 m0, s62, 0\n" \
" s_mov_b32 s54, 4980736\n" \
" buffer_load_dwordx4 v32, s[40:43], s54 offen nt lds\n" \
" s_add_u32 m0, s62, 1024\n" \
" s_mov_b32 s54, 4980736\n" \
" buffer_load_dwordx4 v33, s[40:43], s54 offen nt lds\n" \
" s_add_u32 m0, s62, 2048\n" \
" s_mov_b32 s54, 5111808\n" \
" buffer_load_dwordx4 v32, s[40:43], s54 offen nt lds\n" \
" s_add_u32 m0, s62, 3072\n" \
" s_mov_b32 s54, 5111808\n" \
" buffer_load_dwordx4 v33, s[40:43], s54 offen nt lds\n" \
" s_waitcnt vmcnt(32)\n" \
" s_barrier\n" \
" ds_read_b64_tr_b16 a[16:17], v38 offset:16384\n" \
" ds_read_b64_tr_b16 a[18:19], v38 offset:16896\n" \
" ds_read_b64_tr_b16 a[24:25], v39 offset:16384\n" \
" ds_read_b64_tr_b16 a[26:27], v39 offset:16896\n" \
" ds_read_b64_tr_b16 v[112:113], v38 offset:24576\n" \
" ds_read_b64_tr_b16 v[114:115], v38 offset:25088\n" \
" ds_read_b64_tr_b16 v[120:121], v39 offset:24576\n" \
" ds_read_b64_tr_b16 v[122:123], v39 offset:25088\n" \
" ds_read_b64_tr_b16 a[20:21], v38 offset:0\n" \
" ds_read_b64_tr_b16 a[22:23], v38 offset:512\n" \
" ds_read_b64_tr_b16 a[28:29], v39 offset:0\n" \
" ds_read_b64_tr_b16 a[30:31], v39 offset:512\n" \
" ds_read_b64_tr_b16 v[116:117], v38 offset:8192\n" \
" ds_read_b64_tr_b16 v[118:119], v38 offset:8704\n" \
" ds_read_b64_tr_b16 v[124:125], v39 offset:8192\n" \
" ds_read_b64_tr_b16 v[126:127], v39 offset:8704\n" \
" s_waitcnt lgkmcnt(0)\n" \
" s_barrier\n" \
" s_add_u32 m0, s62, 147456\n" \
" s_mov_b32 s54, 5767168\n" \
" buffer_load_dwordx4 v32, s[40:43], s54 offen nt lds\n" \
" s_add_u32 m0, s62, 148480\n" \
" s_mov_b32 s54, 5767168\n" \
" buffer_load_dwordx4 v33, s[40:43], s54 offen nt lds\n" \
" s_add_u32 m0, s62, 149504\n" \
" s_mov_b32 s54, 5898240\n" \
" buffer_load_dwordx4 v32, s[40:43], s54 offen nt lds\n" \
" s_add_u32 m0, s62, 150528\n" \
" s_mov_b32 s54, 5898240\n" \
" buffer_load_dwordx4 v33, s[40:43], s54 offen nt lds\n" \
" s_add_u32 m0, s62, 131072\n" \
" s_mov_b32 s54, 6029312\n" \
" buffer_load_dwordx4 v32, s[40:43], s54 offen nt lds\n" \
" s_add_u32 m0, s62, 132096\n" \
" s_mov_b32 s54, 6029312\n" \
" buffer_load_dwordx4 v33, s[40:43], s54 offen nt lds\n" \
" s_add_u32 m0, s62, 133120\n" \
" s_mov_b32 s54, 6160384\n" \
" buffer_load_dwordx4 v32, s[40:43], s54 offen nt lds\n" \
" s_add_u32 m0, s62, 134144\n" \
" s_mov_b32 s54, 6160384\n" \
" buffer_load_dwordx4 v33, s[40:43], s54 offen nt lds\n" \
" s_waitcnt vmcnt(32)\n" \
" s_barrier\n" \
" ds_read_b64_tr_b16 a[48:49], v36 offset:49152\n" \
" ds_read_b64_tr_b16 a[50:51], v36 offset:49664\n" \
" ds_read_b64_tr_b16 a[56:57], v37 offset:49152\n" \
" ds_read_b64_tr_b16 a[58:59], v37 offset:49664\n" \
" ds_read_b64_tr_b16 v[128:129], v36 offset:57344\n" \
" ds_read_b64_tr_b16 v[130:131], v36 offset:57856\n" \
" ds_read_b64_tr_b16 v[136:137], v37 offset:57344\n" \
" ds_read_b64_tr_b16 v[138:139], v37 offset:57856\n" \
" ds_read_b64_tr_b16 a[52:53], v36 offset:32768\n" \
" ds_read_b64_tr_b16 a[54:55], v36 offset:33280\n" \
" ds_read_b64_tr_b16 a[60:61], v37 offset:32768\n" \
" ds_read_b64_tr_b16 a[62:63], v37 offset:33280\n" \
" ds_read_b64_tr_b16 v[132:133], v36 offset:40960\n" \
" ds_read_b64_tr_b16 v[134:135], v36 offset:41472\n" \
" ds_read_b64_tr_b16 v[140:141], v37 offset:40960\n" \
" ds_read_b64_tr_b16 v[142:143], v37 offset:41472\n" \
" s_waitcnt lgkmcnt(0)\n" \
" s_barrier\n" \
" s_add_u32 m0, s62, 114688\n" \
" s_mov_b32 s54, 6815744\n" \
" buffer_load_dwordx4 v32, s[40:43], s54 offen nt lds\n" \
" s_add_u32 m0, s62, 115712\n" \
" s_mov_b32 s54, 6815744\n" \
" buffer_load_dwordx4 v33, s[40:43], s54 offen nt lds\n" \
" s_add_u32 m0, s62, 116736\n" \
" s_mov_b32 s54, 6946816\n" \
" buffer_load_dwordx4 v32, s[40:43], s54 offen nt lds\n" \
" s_add_u32 m0, s62, 117760\n" \
" s_mov_b32 s54, 6946816\n" \
" buffer_load_dwordx4 v33, s[40:43], s54 offen nt lds\n" \
" s_add_u32 m0, s62, 98304\n" \
" s_mov_b32 s54, 7077888\n" \
" buffer_load_dwordx4 v32, s[40:43], s54 offen nt lds\n" \
" s_add_u32 m0, s62, 99328\n" \
" s_mov_b32 s54, 7077888\n" \
" buffer_load_dwordx4 v33, s[40:43], s54 offen nt lds\n" \
" s_add_u32 m0, s62, 100352\n" \
" s_mov_b32 s54, 7208960\n" \
" buffer_load_dwordx4 v32, s[40:43], s54 offen nt lds\n" \
" s_add_u32 m0, s62, 101376\n" \
" s_mov_b32 s54, 7208960\n" \
" buffer_load_dwordx4 v33, s[40:43], s54 offen nt lds\n" \
" s_waitcnt vmcnt(32)\n" \
" s_barrier\n" \
" ds_read_b64_tr_b16 a[80:81], v36 offset:16384\n" \
" ds_read_b64_tr_b16 a[82:83], v36 offset:16896\n" \
" ds_read_b64_tr_b16 a[88:89], v37 offset:16384\n" \
" ds_read_b64_tr_b16 a[90:91], v37 offset:16896\n" \
" ds_read_b64_tr_b16 v[144:145], v36 offset:24576\n" \
" ds_read_b64_tr_b16 v[146:147], v36 offset:25088\n" \
" ds_read_b64_tr_b16 v[152:153], v37 offset:24576\n" \
" ds_read_b64_tr_b16 v[154:155], v37 offset:25088\n" \
" ds_read_b64_tr_b16 a[84:85], v36 offset:0\n" \
" ds_read_b64_tr_b16 a[86:87], v36 offset:512\n" \
" ds_read_b64_tr_b16 a[92:93], v37 offset:0\n" \
" ds_read_b64_tr_b16 a[94:95], v37 offset:512\n" \
" ds_read_b64_tr_b16 v[148:149], v36 offset:8192\n" \
" ds_read_b64_tr_b16 v[150:151], v36 offset:8704\n" \
" ds_read_b64_tr_b16 v[156:157], v37 offset:8192\n" \
" ds_read_b64_tr_b16 v[158:159], v37 offset:8704\n" \
" s_waitcnt lgkmcnt(0)\n" \
" s_barrier\n" \
" s_add_u32 m0, s62, 81920\n" \
" s_mov_b32 s54, 7340032\n" \
" buffer_load_dwordx4 v32, s[40:43], s54 offen nt lds\n" \
" s_add_u32 m0, s62, 82944\n" \
" s_mov_b32 s54, 7340032\n" \
" buffer_load_dwordx4 v33, s[40:43], s54 offen nt lds\n" \
" s_add_u32 m0, s62, 83968\n" \
" s_mov_b32 s54, 7471104\n" \
" buffer_load_dwordx4 v32, s[40:43], s54 offen nt lds\n" \
" s_add_u32 m0, s62, 84992\n" \
" s_mov_b32 s54, 7471104\n" \
" buffer_load_dwordx4 v33, s[40:43], s54 offen nt lds\n" \
" s_add_u32 m0, s62, 65536\n" \
" s_mov_b32 s54, 7602176\n" \
" buffer_load_dwordx4 v32, s[40:43], s54 offen nt lds\n" \
" s_add_u32 m0, s62, 66560\n" \
" s_mov_b32 s54, 7602176\n" \
" buffer_load_dwordx4 v33, s[40:43], s54 offen nt lds\n" \
" s_add_u32 m0, s62, 67584\n" \
" s_mov_b32 s54, 7733248\n" \
" buffer_load_dwordx4 v32, s[40:43], s54 offen nt lds\n" \
" s_add_u32 m0, s62, 68608\n" \
" s_mov_b32 s54, 7733248\n" \
" buffer_load_dwordx4 v33, s[40:43], s54 offen nt lds\n" \
" s_waitcnt vmcnt(32)\n" \
" s_barrier\n" \
" ds_read_b64_tr_b16 a[112:113], v34 offset:49152\n" \
" ds_read_b64_tr_b16 a[114:115], v34 offset:49664\n" \
" ds_read_b64_tr_b16 a[120:121], v35 offset:49152\n" \
" ds_read_b64_tr_b16 a[122:123], v35 offset:49664\n" \
" ds_read_b64_tr_b16 v[160:161], v34 offset:57344\n" \
" ds_read_b64_tr_b16 v[162:163], v34 offset:57856\n" \
" ds_read_b64_tr_b16 v[168:169], v35 offset:57344\n" \
" ds_read_b64_tr_b16 v[170:171], v35 offset:57856\n" \
" ds_read_b64_tr_b16 a[116:117], v34 offset:32768\n" \
" ds_read_b64_tr_b16 a[118:119], v34 offset:33280\n" \
" ds_read_b64_tr_b16 a[124:125], v35 offset:32768\n" \
" ds_read_b64_tr_b16 a[126:127], v35 offset:33280\n" \
" ds_read_b64_tr_b16 v[164:165], v34 offset:40960\n" \
" ds_read_b64_tr_b16 v[166:167], v34 offset:41472\n" \
" ds_read_b64_tr_b16 v[172:173], v35 offset:40960\n" \
" ds_read_b64_tr_b16 v[174:175], v35 offset:41472\n" \
" s_waitcnt lgkmcnt(0)\n" \
" s_barrier\n" \
" s_add_u32 m0, s62, 49152\n" \
" s_mov_b32 s54, 7864320\n" \
" buffer_load_dwordx4 v32, s[40:43], s54 offen nt lds\n" \
" s_add_u32 m0, s62, 50176\n" \
" s_mov_b32 s54, 7864320\n" \
" buffer_load_dwordx4 v33, s[40:43], s54 offen nt lds\n" \
" s_add_u32 m0, s62, 51200\n" \
" s_mov_b32 s54, 7995392\n" \
" buffer_load_dwordx4 v32, s[40:43], s54 offen nt lds\n" \
" s_add_u32 m0, s62, 52224\n" \
" s_mov_b32 s54, 7995392\n" \
" buffer_load_dwordx4 v33, s[40:43], s54 offen nt lds\n" \
" s_add_u32 m0, s62, 32768\n" \
" s_mov_b32 s54, 8126464\n" \
" buffer_load_dwordx4 v32, s[40:43], s54 offen nt lds\n" \
" s_add_u32 m0, s62, 33792\n" \
" s_mov_b32 s54, 8126464\n" \
" buffer_load_dwordx4 v33, s[40:43], s54 offen nt lds\n" \
" s_add_u32 m0, s62, 34816\n" \
" s_mov_b32 s54, 8257536\n" \
" buffer_load_dwordx4 v32, s[40:43], s54 offen nt lds\n" \
" s_add_u32 m0, s62, 35840\n" \
" s_mov_b32 s54, 8257536\n" \
" buffer_load_dwordx4 v33, s[40:43], s54 offen nt lds\n" \
" s_waitcnt vmcnt(32)\n" \
" s_barrier\n" \
" ds_read_b64_tr_b16 a[144:145], v34 offset:16384\n" \
" ds_read_b64_tr_b16 a[146:147], v34 offset:16896\n" \
" ds_read_b64_tr_b16 a[152:153], v35 offset:16384\n" \
" ds_read_b64_tr_b16 a[154:155], v35 offset:16896\n" \
" ds_read_b64_tr_b16 v[176:177], v34 offset:24576\n" \
" ds_read_b64_tr_b16 v[178:179], v34 offset:25088\n" \
" ds_read_b64_tr_b16 v[184:185], v35 offset:24576\n" \
" ds_read_b64_tr_b16 v[186:187], v35 offset:25088\n" \
" ds_read_b64_tr_b16 a[148:149], v34 offset:0\n" \
" ds_read_b64_tr_b16 a[150:151], v34 offset:512\n" \
" ds_read_b64_tr_b16 a[156:157], v35 offset:0\n" \
" ds_read_b64_tr_b16 a[158:159], v35 offset:512\n" \
" ds_read_b64_tr_b16 v[180:181], v34 offset:8192\n" \
" ds_read_b64_tr_b16 v[182:183], v34 offset:8704\n" \
" ds_read_b64_tr_b16 v[188:189], v35 offset:8192\n" \
" ds_read_b64_tr_b16 v[190:191], v35 offset:8704\n" \
" s_waitcnt lgkmcnt(0)\n" \
" s_barrier\n" \
" s_add_u32 m0, s62, 0\n" \
" s_mov_b32 s54, 0\n" \
" buffer_load_dwordx4 v32, s[40:43], s54 offen nt lds\n" \
" s_add_u32 m0, s62, 1024\n" \
" s_mov_b32 s54, 0\n" \
" buffer_load_dwordx4 v33, s[40:43], s54 offen nt lds\n" \
" s_add_u32 m0, s62, 2048\n" \
" s_mov_b32 s54, 131072\n" \
" buffer_load_dwordx4 v32, s[40:43], s54 offen nt lds\n" \
" s_add_u32 m0, s62, 3072\n" \
" s_mov_b32 s54, 131072\n" \
" buffer_load_dwordx4 v33, s[40:43], s54 offen nt lds\n" \
" s_add_u32 m0, s62, 16384\n" \
" s_mov_b32 s54, 262144\n" \
" buffer_load_dwordx4 v32, s[40:43], s54 offen nt lds\n" \
" s_add_u32 m0, s62, 17408\n" \
" s_mov_b32 s54, 262144\n" \
" buffer_load_dwordx4 v33, s[40:43], s54 offen nt lds\n" \
" s_add_u32 m0, s62, 18432\n" \
" s_mov_b32 s54, 393216\n" \
" buffer_load_dwordx4 v32, s[40:43], s54 offen nt lds\n" \
" s_add_u32 m0, s62, 19456\n" \
" s_mov_b32 s54, 393216\n" \
" buffer_load_dwordx4 v33, s[40:43], s54 offen nt lds\n" \
" s_waitcnt vmcnt(32)\n" \
" s_barrier\n" \
" ds_read_b64_tr_b16 a[176:177], v38 offset:16384\n" \
" ds_read_b64_tr_b16 a[178:179], v38 offset:16896\n" \
" ds_read_b64_tr_b16 a[184:185], v39 offset:16384\n" \
" ds_read_b64_tr_b16 a[186:187], v39 offset:16896\n" \
" ds_read_b64_tr_b16 v[192:193], v38 offset:24576\n" \
" ds_read_b64_tr_b16 v[194:195], v38 offset:25088\n" \
" ds_read_b64_tr_b16 v[200:201], v39 offset:24576\n" \
" ds_read_b64_tr_b16 v[202:203], v39 offset:25088\n" \
" ds_read_b64_tr_b16 a[180:181], v38 offset:0\n" \
" ds_read_b64_tr_b16 a[182:183], v38 offset:512\n" \
" ds_read_b64_tr_b16 a[188:189], v39 offset:0\n" \
" ds_read_b64_tr_b16 a[190:191], v39 offset:512\n" \
" ds_read_b64_tr_b16 v[196:197], v38 offset:8192\n" \
" ds_read_b64_tr_b16 v[198:199], v38 offset:8704\n" \
" ds_read_b64_tr_b16 v[204:205], v39 offset:8192\n" \
" ds_read_b64_tr_b16 v[206:207], v39 offset:8704\n" \
" s_waitcnt lgkmcnt(0)\n" \
" s_barrier\n" \
" s_waitcnt vmcnt(24)\n" \
" s_barrier\n" \
" ds_read_b64_tr_b16 a[208:209], v36 offset:49152\n" \
" ds_read_b64_tr_b16 a[210:211], v36 offset:49664\n" \
" ds_read_b64_tr_b16 a[216:217], v37 offset:49152\n" \
" ds_read_b64_tr_b16 a[218:219], v37 offset:49664\n" \
" ds_read_b64_tr_b16 v[208:209], v36 offset:57344\n" \
" ds_read_b64_tr_b16 v[210:211], v36 offset:57856\n" \
" ds_read_b64_tr_b16 v[216:217], v37 offset:57344\n" \
" ds_read_b64_tr_b16 v[218:219], v37 offset:57856\n" \
" ds_read_b64_tr_b16 a[212:213], v36 offset:32768\n" \
" ds_read_b64_tr_b16 a[214:215], v36 offset:33280\n" \
" ds_read_b64_tr_b16 a[220:221], v37 offset:32768\n" \
" ds_read_b64_tr_b16 a[222:223], v37 offset:33280\n" \
" ds_read_b64_tr_b16 v[212:213], v36 offset:40960\n" \
" ds_read_b64_tr_b16 v[214:215], v36 offset:41472\n" \
" ds_read_b64_tr_b16 v[220:221], v37 offset:40960\n" \
" ds_read_b64_tr_b16 v[222:223], v37 offset:41472\n" \
" s_waitcnt lgkmcnt(0)\n" \
" s_barrier\n" \
" s_add_u32 m0, s62, 98304\n" \
" s_mov_b32 s54, 1048576\n" \
" buffer_load_dwordx4 v32, s[40:43], s54 offen nt lds\n" \
" s_add_u32 m0, s62, 99328\n" \
" s_mov_b32 s54, 1048576\n" \
" buffer_load_dwordx4 v33, s[40:43], s54 offen nt lds\n" \
" s_add_u32 m0, s62, 100352\n" \
" s_mov_b32 s54, 1179648\n" \
" buffer_load_dwordx4 v32, s[40:43], s54 offen nt lds\n" \
" s_add_u32 m0, s62, 101376\n" \
" s_mov_b32 s54, 1179648\n" \
" buffer_load_dwordx4 v33, s[40:43], s54 offen nt lds\n" \
" s_add_u32 m0, s62, 147456\n" \
" s_mov_b32 s54, 1310720\n" \
" buffer_load_dwordx4 v32, s[40:43], s54 offen nt lds\n" \
" s_add_u32 m0, s62, 148480\n" \
" s_mov_b32 s54, 1310720\n" \
" buffer_load_dwordx4 v33, s[40:43], s54 offen nt lds\n" \
" s_add_u32 m0, s62, 149504\n" \
" s_mov_b32 s54, 1441792\n" \
" buffer_load_dwordx4 v32, s[40:43], s54 offen nt lds\n" \
" s_add_u32 m0, s62, 150528\n" \
" s_mov_b32 s54, 1441792\n" \
" buffer_load_dwordx4 v33, s[40:43], s54 offen nt lds\n" \
" s_waitcnt vmcnt(24)\n" \
" s_barrier\n" \
" ds_read_b64_tr_b16 a[224:225], v36 offset:16384\n" \
" ds_read_b64_tr_b16 a[226:227], v36 offset:16896\n" \
" ds_read_b64_tr_b16 a[232:233], v37 offset:16384\n" \
" ds_read_b64_tr_b16 a[234:235], v37 offset:16896\n" \
" ds_read_b64_tr_b16 v[224:225], v36 offset:24576\n" \
" ds_read_b64_tr_b16 v[226:227], v36 offset:25088\n" \
" ds_read_b64_tr_b16 v[232:233], v37 offset:24576\n" \
" ds_read_b64_tr_b16 v[234:235], v37 offset:25088\n" \
" ds_read_b64_tr_b16 a[228:229], v36 offset:0\n" \
" ds_read_b64_tr_b16 a[230:231], v36 offset:512\n" \
" ds_read_b64_tr_b16 a[236:237], v37 offset:0\n" \
" ds_read_b64_tr_b16 a[238:239], v37 offset:512\n" \
" ds_read_b64_tr_b16 v[228:229], v36 offset:8192\n" \
" ds_read_b64_tr_b16 v[230:231], v36 offset:8704\n" \
" ds_read_b64_tr_b16 v[236:237], v37 offset:8192\n" \
" ds_read_b64_tr_b16 v[238:239], v37 offset:8704\n" \
" s_waitcnt lgkmcnt(0)\n" \
" s_barrier\n" \
" s_add_u32 m0, s62, 65536\n" \
" s_mov_b32 s54, 2097152\n" \
" buffer_load_dwordx4 v32, s[40:43], s54 offen nt lds\n" \
" s_add_u32 m0, s62, 66560\n" \
" s_mov_b32 s54, 2097152\n" \
" buffer_load_dwordx4 v33, s[40:43], s54 offen nt lds\n" \
" s_add_u32 m0, s62, 67584\n" \
" s_mov_b32 s54, 2228224\n" \
" buffer_load_dwordx4 v32, s[40:43], s54 offen nt lds\n" \
" s_add_u32 m0, s62, 68608\n" \
" s_mov_b32 s54, 2228224\n" \
" buffer_load_dwordx4 v33, s[40:43], s54 offen nt lds\n" \
" s_add_u32 m0, s62, 131072\n" \
" s_mov_b32 s54, 2359296\n" \
" buffer_load_dwordx4 v32, s[40:43], s54 offen nt lds\n" \
" s_add_u32 m0, s62, 132096\n" \
" s_mov_b32 s54, 2359296\n" \
" buffer_load_dwordx4 v33, s[40:43], s54 offen nt lds\n" \
" s_add_u32 m0, s62, 133120\n" \
" s_mov_b32 s54, 2490368\n" \
" buffer_load_dwordx4 v32, s[40:43], s54 offen nt lds\n" \
" s_add_u32 m0, s62, 134144\n" \
" s_mov_b32 s54, 2490368\n" \
" buffer_load_dwordx4 v33, s[40:43], s54 offen nt lds\n" \
" s_add_u32 m0, s62, 81920\n" \
" s_mov_b32 s54, 3145728\n" \
" buffer_load_dwordx4 v32, s[40:43], s54 offen nt lds\n" \
" s_add_u32 m0, s62, 82944\n" \
" s_mov_b32 s54, 3145728\n" \
" buffer_load_dwordx4 v33, s[40:43], s54 offen nt lds\n" \
" s_add_u32 m0, s62, 83968\n" \
" s_mov_b32 s54, 3276800\n" \
" buffer_load_dwordx4 v32, s[40:43], s54 offen nt lds\n" \
" s_add_u32 m0, s62, 84992\n" \
" s_mov_b32 s54, 3276800\n" \
" buffer_load_dwordx4 v33, s[40:43], s54 offen nt lds\n" \
" s_add_u32 m0, s62, 114688\n" \
" s_mov_b32 s54, 3407872\n" \
" buffer_load_dwordx4 v32, s[40:43], s54 offen nt lds\n" \
" s_add_u32 m0, s62, 115712\n" \
" s_mov_b32 s54, 3407872\n" \
" buffer_load_dwordx4 v33, s[40:43], s54 offen nt lds\n" \
" s_add_u32 m0, s62, 116736\n" \
" s_mov_b32 s54, 3538944\n" \
" buffer_load_dwordx4 v32, s[40:43], s54 offen nt lds\n" \
" s_add_u32 m0, s62, 117760\n" \
" s_mov_b32 s54, 3538944\n" \
" buffer_load_dwordx4 v33, s[40:43], s54 offen nt lds\n" \
" s_waitcnt vmcnt(32)\n" \
" s_barrier\n" \
" ds_read_b64_tr_b16 a[240:241], v34 offset:49152\n" \
" ds_read_b64_tr_b16 a[242:243], v34 offset:49664\n" \
" ds_read_b64_tr_b16 a[248:249], v35 offset:49152\n" \
" ds_read_b64_tr_b16 a[250:251], v35 offset:49664\n" \
" ds_read_b64_tr_b16 v[240:241], v34 offset:57344\n" \
" ds_read_b64_tr_b16 v[242:243], v34 offset:57856\n" \
" ds_read_b64_tr_b16 v[248:249], v35 offset:57344\n" \
" ds_read_b64_tr_b16 v[250:251], v35 offset:57856\n" \
" ds_read_b64_tr_b16 a[244:245], v34 offset:32768\n" \
" ds_read_b64_tr_b16 a[246:247], v34 offset:33280\n" \
" ds_read_b64_tr_b16 a[252:253], v35 offset:32768\n" \
" ds_read_b64_tr_b16 a[254:255], v35 offset:33280\n" \
" ds_read_b64_tr_b16 v[244:245], v34 offset:40960\n" \
" ds_read_b64_tr_b16 v[246:247], v34 offset:41472\n" \
" ds_read_b64_tr_b16 v[252:253], v35 offset:40960\n" \
" ds_read_b64_tr_b16 v[254:255], v35 offset:41472\n" \
" s_waitcnt lgkmcnt(0)\n" \
" s_barrier\n" \
" s_add_u32 m0, s62, 32768\n" \
" s_mov_b32 s54, 4194304\n" \
" buffer_load_dwordx4 v32, s[40:43], s54 offen nt lds\n" \
" s_add_u32 m0, s62, 33792\n" \
" s_mov_b32 s54, 4194304\n" \
" buffer_load_dwordx4 v33, s[40:43], s54 offen nt lds\n" \
" s_add_u32 m0, s62, 34816\n" \
" s_mov_b32 s54, 4325376\n" \
" buffer_load_dwordx4 v32, s[40:43], s54 offen nt lds\n" \
" s_add_u32 m0, s62, 35840\n" \
" s_mov_b32 s54, 4325376\n" \
" buffer_load_dwordx4 v33, s[40:43], s54 offen nt lds\n" \
" s_add_u32 m0, s62, 49152\n" \
" s_mov_b32 s54, 4456448\n" \
" buffer_load_dwordx4 v32, s[40:43], s54 offen nt lds\n" \
" s_add_u32 m0, s62, 50176\n" \
" s_mov_b32 s54, 4456448\n" \
" buffer_load_dwordx4 v33, s[40:43], s54 offen nt lds\n" \
" s_add_u32 m0, s62, 51200\n" \
" s_mov_b32 s54, 4587520\n" \
" buffer_load_dwordx4 v32, s[40:43], s54 offen nt lds\n" \
" s_add_u32 m0, s62, 52224\n" \
" s_mov_b32 s54, 4587520\n" \
" buffer_load_dwordx4 v33, s[40:43], s54 offen nt lds\n" \
" s_waitcnt vmcnt(32)\n" \
" s_barrier\n" \
" ds_read_b64_tr_b16 a[0:1], v34 offset:0\n" \
" ds_read_b64_tr_b16 a[2:3], v34 offset:512\n" \
" ds_read_b64_tr_b16 a[8:9], v35 offset:0\n" \
" ds_read_b64_tr_b16 a[10:11], v35 offset:512\n" \
" ds_read_b64_tr_b16 v[48:49], v34 offset:8192\n" \
" ds_read_b64_tr_b16 v[50:51], v34 offset:8704\n" \
" ds_read_b64_tr_b16 v[56:57], v35 offset:8192\n" \
" ds_read_b64_tr_b16 v[58:59], v35 offset:8704\n" \
" ds_read_b64_tr_b16 a[4:5], v34 offset:16384\n" \
" ds_read_b64_tr_b16 a[6:7], v34 offset:16896\n" \
" ds_read_b64_tr_b16 a[12:13], v35 offset:16384\n" \
" ds_read_b64_tr_b16 a[14:15], v35 offset:16896\n" \
" ds_read_b64_tr_b16 v[52:53], v34 offset:24576\n" \
" ds_read_b64_tr_b16 v[54:55], v34 offset:25088\n" \
" ds_read_b64_tr_b16 v[60:61], v35 offset:24576\n" \
" ds_read_b64_tr_b16 v[62:63], v35 offset:25088\n" \
" s_waitcnt lgkmcnt(0)\n" \
" s_barrier\n" \
" ds_write_b128 v1, v[48:51] offset:0\n" \
" ds_write_b128 v1, v[52:55] offset:1024\n" \
" ds_write_b128 v1, v[56:59] offset:2048\n" \
" ds_write_b128 v1, v[60:63] offset:3072\n" \
" s_add_u32 m0, s62, 16384\n" \
" s_mov_b32 s54, 5242880\n" \
" buffer_load_dwordx4 v32, s[40:43], s54 offen nt lds\n" \
" s_add_u32 m0, s62, 17408\n" \
" s_mov_b32 s54, 5242880\n" \
" buffer_load_dwordx4 v33, s[40:43], s54 offen nt lds\n" \
" s_add_u32 m0, s62, 18432\n" \
" s_mov_b32 s54, 5373952\n" \
" buffer_load_dwordx4 v32, s[40:43], s54 offen nt lds\n" \
" s_add_u32 m0, s62, 19456\n" \
" s_mov_b32 s54, 5373952\n" \
" buffer_load_dwordx4 v33, s[40:43], s54 offen nt lds\n" \
" s_waitcnt vmcnt(28)\n" \
" s_barrier\n" \
" ds_read_b64_tr_b16 a[32:33], v36 offset:32768\n" \
" ds_read_b64_tr_b16 a[34:35], v36 offset:33280\n" \
" ds_read_b64_tr_b16 a[40:41], v37 offset:32768\n" \
" ds_read_b64_tr_b16 a[42:43], v37 offset:33280\n" \
" ds_read_b64_tr_b16 v[48:49], v36 offset:40960\n" \
" ds_read_b64_tr_b16 v[50:51], v36 offset:41472\n" \
" ds_read_b64_tr_b16 v[56:57], v37 offset:40960\n" \
" ds_read_b64_tr_b16 v[58:59], v37 offset:41472\n" \
" ds_read_b64_tr_b16 a[36:37], v38 offset:16384\n" \
" ds_read_b64_tr_b16 a[38:39], v38 offset:16896\n" \
" ds_read_b64_tr_b16 a[44:45], v39 offset:16384\n" \
" ds_read_b64_tr_b16 a[46:47], v39 offset:16896\n" \
" ds_read_b64_tr_b16 v[52:53], v38 offset:24576\n" \
" ds_read_b64_tr_b16 v[54:55], v38 offset:25088\n" \
" ds_read_b64_tr_b16 v[60:61], v39 offset:24576\n" \
" ds_read_b64_tr_b16 v[62:63], v39 offset:25088\n" \
" s_waitcnt lgkmcnt(0)\n" \
" s_barrier\n" \
" ds_write_b128 v2, v[48:51] offset:32768\n" \
" ds_write_b128 v2, v[52:55] offset:33792\n" \
" ds_write_b128 v2, v[56:59] offset:34816\n" \
" ds_write_b128 v2, v[60:63] offset:35840\n" \
" s_add_u32 m0, s62, 147456\n" \
" s_mov_b32 s54, 5505024\n" \
" buffer_load_dwordx4 v32, s[40:43], s54 offen nt lds\n" \
" s_add_u32 m0, s62, 148480\n" \
" s_mov_b32 s54, 5505024\n" \
" buffer_load_dwordx4 v33, s[40:43], s54 offen nt lds\n" \
" s_add_u32 m0, s62, 149504\n" \
" s_mov_b32 s54, 5636096\n" \
" buffer_load_dwordx4 v32, s[40:43], s54 offen nt lds\n" \
" s_add_u32 m0, s62, 150528\n" \
" s_mov_b32 s54, 5636096\n" \
" buffer_load_dwordx4 v33, s[40:43], s54 offen nt lds\n" \
" s_waitcnt vmcnt(24)\n" \
" s_barrier\n" \
" ds_read_b64_tr_b16 a[64:65], v36 offset:0\n" \
" ds_read_b64_tr_b16 a[66:67], v36 offset:512\n" \
" ds_read_b64_tr_b16 a[72:73], v37 offset:0\n" \
" ds_read_b64_tr_b16 a[74:75], v37 offset:512\n" \
" ds_read_b64_tr_b16 v[48:49], v36 offset:8192\n" \
" ds_read_b64_tr_b16 v[50:51], v36 offset:8704\n" \
" ds_read_b64_tr_b16 v[56:57], v37 offset:8192\n" \
" ds_read_b64_tr_b16 v[58:59], v37 offset:8704\n" \
" ds_read_b64_tr_b16 a[68:69], v38 offset:0\n" \
" ds_read_b64_tr_b16 a[70:71], v38 offset:512\n" \
" ds_read_b64_tr_b16 a[76:77], v39 offset:0\n" \
" ds_read_b64_tr_b16 a[78:79], v39 offset:512\n" \
" ds_read_b64_tr_b16 v[52:53], v38 offset:8192\n" \
" ds_read_b64_tr_b16 v[54:55], v38 offset:8704\n" \
" ds_read_b64_tr_b16 v[60:61], v39 offset:8192\n" \
" ds_read_b64_tr_b16 v[62:63], v39 offset:8704\n" \
" s_waitcnt lgkmcnt(0)\n" \
" s_barrier\n" \
" ds_write_b128 v2, v[48:51] offset:0\n" \
" ds_write_b128 v2, v[52:55] offset:1024\n" \
" ds_write_b128 v2, v[56:59] offset:2048\n" \
" ds_write_b128 v2, v[60:63] offset:3072\n" \
" s_waitcnt vmcnt(16)\n" \
" s_barrier\n" \
" ds_read_b64_tr_b16 a[96:97], v36 offset:16384\n" \
" ds_read_b64_tr_b16 a[98:99], v36 offset:16896\n" \
" ds_read_b64_tr_b16 a[104:105], v37 offset:16384\n" \
" ds_read_b64_tr_b16 a[106:107], v37 offset:16896\n" \
" ds_read_b64_tr_b16 v[48:49], v36 offset:24576\n" \
" ds_read_b64_tr_b16 v[50:51], v36 offset:25088\n" \
" ds_read_b64_tr_b16 v[56:57], v37 offset:24576\n" \
" ds_read_b64_tr_b16 v[58:59], v37 offset:25088\n" \
" ds_read_b64_tr_b16 a[100:101], v36 offset:49152\n" \
" ds_read_b64_tr_b16 a[102:103], v36 offset:49664\n" \
" ds_read_b64_tr_b16 a[108:109], v37 offset:49152\n" \
" ds_read_b64_tr_b16 a[110:111], v37 offset:49664\n" \
" ds_read_b64_tr_b16 v[52:53], v36 offset:57344\n" \
" ds_read_b64_tr_b16 v[54:55], v36 offset:57856\n" \
" ds_read_b64_tr_b16 v[60:61], v37 offset:57344\n" \
" ds_read_b64_tr_b16 v[62:63], v37 offset:57856\n" \
" s_waitcnt lgkmcnt(0)\n" \
" s_barrier\n" \
" ds_write_b128 v2, v[48:51] offset:16384\n" \
" ds_write_b128 v2, v[52:55] offset:17408\n" \
" ds_write_b128 v2, v[56:59] offset:18432\n" \
" ds_write_b128 v2, v[60:63] offset:19456\n" \
" s_waitcnt vmcnt(8)\n" \
" s_barrier\n" \
" ds_read_b64_tr_b16 a[128:129], v34 offset:32768\n" \
" ds_read_b64_tr_b16 a[130:131], v34 offset:33280\n" \
" ds_read_b64_tr_b16 a[136:137], v35 offset:32768\n" \
" ds_read_b64_tr_b16 a[138:139], v35 offset:33280\n" \
" ds_read_b64_tr_b16 v[48:49], v34 offset:40960\n" \
" ds_read_b64_tr_b16 v[50:51], v34 offset:41472\n" \
" ds_read_b64_tr_b16 v[56:57], v35 offset:40960\n" \
" ds_read_b64_tr_b16 v[58:59], v35 offset:41472\n" \
" ds_read_b64_tr_b16 a[132:133], v34 offset:49152\n" \
" ds_read_b64_tr_b16 a[134:135], v34 offset:49664\n" \
" ds_read_b64_tr_b16 a[140:141], v35 offset:49152\n" \
" ds_read_b64_tr_b16 a[142:143], v35 offset:49664\n" \
" ds_read_b64_tr_b16 v[52:53], v34 offset:57344\n" \
" ds_read_b64_tr_b16 v[54:55], v34 offset:57856\n" \
" ds_read_b64_tr_b16 v[60:61], v35 offset:57344\n" \
" ds_read_b64_tr_b16 v[62:63], v35 offset:57856\n" \
" s_waitcnt lgkmcnt(0)\n" \
" s_barrier\n" \
" ds_write_b128 v1, v[48:51] offset:32768\n" \
" ds_write_b128 v1, v[52:55] offset:33792\n" \
" ds_write_b128 v1, v[56:59] offset:34816\n" \
" ds_write_b128 v1, v[60:63] offset:35840\n" \
" s_add_u32 m0, s62, 49152\n" \
" s_mov_b32 s54, 6291456\n" \
" buffer_load_dwordx4 v32, s[40:43], s54 offen nt lds\n" \
" s_add_u32 m0, s62, 50176\n" \
" s_mov_b32 s54, 6291456\n" \
" buffer_load_dwordx4 v33, s[40:43], s54 offen nt lds\n" \
" s_add_u32 m0, s62, 51200\n" \
" s_mov_b32 s54, 6422528\n" \
" buffer_load_dwordx4 v32, s[40:43], s54 offen nt lds\n" \
" s_add_u32 m0, s62, 52224\n" \
" s_mov_b32 s54, 6422528\n" \
" buffer_load_dwordx4 v33, s[40:43], s54 offen nt lds\n" \
" s_add_u32 m0, s62, 131072\n" \
" s_mov_b32 s54, 6553600\n" \
" buffer_load_dwordx4 v32, s[40:43], s54 offen nt lds\n" \
" s_add_u32 m0, s62, 132096\n" \
" s_mov_b32 s54, 6553600\n" \
" buffer_load_dwordx4 v33, s[40:43], s54 offen nt lds\n" \
" s_add_u32 m0, s62, 133120\n" \
" s_mov_b32 s54, 6684672\n" \
" buffer_load_dwordx4 v32, s[40:43], s54 offen nt lds\n" \
" s_add_u32 m0, s62, 134144\n" \
" s_mov_b32 s54, 6684672\n" \
" buffer_load_dwordx4 v33, s[40:43], s54 offen nt lds\n" \
" s_waitcnt vmcnt(8)\n" \
" s_barrier\n" \
" ds_read_b64_tr_b16 a[160:161], v34 offset:16384\n" \
" ds_read_b64_tr_b16 a[162:163], v34 offset:16896\n" \
" ds_read_b64_tr_b16 a[168:169], v35 offset:16384\n" \
" ds_read_b64_tr_b16 a[170:171], v35 offset:16896\n" \
" ds_read_b64_tr_b16 v[48:49], v34 offset:24576\n" \
" ds_read_b64_tr_b16 v[50:51], v34 offset:25088\n" \
" ds_read_b64_tr_b16 v[56:57], v35 offset:24576\n" \
" ds_read_b64_tr_b16 v[58:59], v35 offset:25088\n" \
" ds_read_b64_tr_b16 a[164:165], v38 offset:16384\n" \
" ds_read_b64_tr_b16 a[166:167], v38 offset:16896\n" \
" ds_read_b64_tr_b16 a[172:173], v39 offset:16384\n" \
" ds_read_b64_tr_b16 a[174:175], v39 offset:16896\n" \
" ds_read_b64_tr_b16 v[52:53], v38 offset:24576\n" \
" ds_read_b64_tr_b16 v[54:55], v38 offset:25088\n" \
" ds_read_b64_tr_b16 v[60:61], v39 offset:24576\n" \
" ds_read_b64_tr_b16 v[62:63], v39 offset:25088\n" \
" s_waitcnt lgkmcnt(0)\n" \
" s_barrier\n" \
" ds_write_b128 v1, v[48:51] offset:16384\n" \
" ds_write_b128 v1, v[52:55] offset:17408\n" \
" ds_write_b128 v1, v[56:59] offset:18432\n" \
" ds_write_b128 v1, v[60:63] offset:19456\n" \
" s_waitcnt vmcnt(0)\n" \
" s_barrier\n" \
" ds_read_b64_tr_b16 a[192:193], v34 offset:49152\n" \
" ds_read_b64_tr_b16 a[194:195], v34 offset:49664\n" \
" ds_read_b64_tr_b16 a[200:201], v35 offset:49152\n" \
" ds_read_b64_tr_b16 a[202:203], v35 offset:49664\n" \
" ds_read_b64_tr_b16 v[48:49], v34 offset:57344\n" \
" ds_read_b64_tr_b16 v[50:51], v34 offset:57856\n" \
" ds_read_b64_tr_b16 v[56:57], v35 offset:57344\n" \
" ds_read_b64_tr_b16 v[58:59], v35 offset:57856\n" \
" ds_read_b64_tr_b16 a[196:197], v38 offset:0\n" \
" ds_read_b64_tr_b16 a[198:199], v38 offset:512\n" \
" ds_read_b64_tr_b16 a[204:205], v39 offset:0\n" \
" ds_read_b64_tr_b16 a[206:207], v39 offset:512\n" \
" ds_read_b64_tr_b16 v[52:53], v38 offset:8192\n" \
" ds_read_b64_tr_b16 v[54:55], v38 offset:8704\n" \
" ds_read_b64_tr_b16 v[60:61], v39 offset:8192\n" \
" ds_read_b64_tr_b16 v[62:63], v39 offset:8704\n" \
" s_waitcnt lgkmcnt(0)\n" \
" s_barrier\n" \
" ds_write_b128 v1, v[48:51] offset:49152\n" \
" ds_write_b128 v1, v[52:55] offset:50176\n" \
" ds_write_b128 v1, v[56:59] offset:51200\n" \
" ds_write_b128 v1, v[60:63] offset:52224\n" \
" s_waitcnt vmcnt(0) lgkmcnt(0)\n" \
" s_barrier\n" \
" s_lshl_b32 s62, s48, 11\n" \
" s_add_u32 s63, s63, s62\n" \
" s_mov_b32 s49, 0\n" \
" s_mov_b32 s52, 0\n" \
" s_mov_b32 s81, 0\n" \
" s_mov_b32 s88, 0\n" \
" s_mov_b32 s95, 0x7ffffff0\n" \
" s_mov_b32 s98, 0x3e0293ee\n" \
" s_mov_b32 s99, 0x3e0293ee\n" \
" v_mov_b32 v16, v13\n" \
" v_mov_b32 v20, 0x3f803f80\n" \
" v_mov_b32 v17, v13\n" \
" v_mov_b32 v21, 0x3f803f80\n" \
" v_mov_b32 v18, v13\n" \
" v_mov_b32 v22, 0x3f803f80\n" \
" v_mov_b32 v19, v13\n" \
" v_mov_b32 v23, 0x3f803f80\n" \
" v_mov_b32 v24, 0\n" \
" v_mov_b32 v25, 0\n" \
" v_mov_b32 v26, 0\n" \
" v_mov_b32 v27, 0\n" \
" v_mov_b32 v28, 0\n" \
" v_mov_b32 v29, 0\n" \
" v_cmp_le_i32_e64 s[64:65], 0, v14\n" \
" v_cmp_le_i32_e64 s[66:67], 1, v14\n" \
" v_cmp_le_i32_e64 s[68:69], 2, v14\n" \
" v_cmp_le_i32_e64 s[70:71], 3, v14\n" \
" v_cmp_le_i32_e64 s[72:73], 16, v14\n" \
" v_cmp_le_i32_e64 s[74:75], 17, v14\n" \
" v_cmp_le_i32_e64 s[76:77], 18, v14\n" \
" v_cmp_le_i32_e64 s[78:79], 19, v14\n" \
" s_mov_b32 s82, 0\n" \
" s_mov_b32 s83, -1\n" \
" buffer_load_dword v13, v24, s[36:39], 0 offen sc1\n" \
" s_waitcnt vmcnt(0)\n" \
" v_cmp_eq_u32 vcc, 0, v13\n" \
" s_nop 0\n" \
" s_cbranch_vccnz Lq_p_cold\n" \
"Lq_p_ok:\n" \
" v_readfirstlane_b32 s54, v13\n" \
" s_nop 0\n" \
" s_and_b32 s54, s54, 1\n" \
" s_lshl_b32 s54, s54, 13\n" \
" v_add_u32 v4, s54, v4\n" \
" v_add_u32 v7, s54, v7\n" \
" v_add_u32 v8, s54, v8\n" \
" v_add_u32 v9, s54, v9\n" \
" s_add_u32 s94, s94, s54\n" \
" s_mov_b32 s54, 0\n" \
" buffer_store_dwordx4 v[24:27], v7, s[36:39], s54 offen sc1\n" \
" s_mov_b32 s54, 327680\n" \
" buffer_store_dwordx4 v[24:27], v7, s[36:39], s54 offen sc1\n" \
" s_add_u32 s54, s54, s94\n" \
" buffer_store_dword v24, v10, s[36:39], s54 offen sc1\n" \
" s_mov_b32 s54, 655360\n" \
" buffer_store_dwordx4 v[24:27], v7, s[36:39], s54 offen sc1\n" \
" s_add_u32 s54, s54, s94\n" \
" buffer_store_dword v24, v10, s[36:39], s54 offen sc1\n" \
" s_mov_b32 s54, 983040\n" \
" buffer_store_dwordx4 v[24:27], v7, s[36:39], s54 offen sc1\n" \
" s_add_u32 s54, s54, s94\n" \
" buffer_store_dword v24, v10, s[36:39], s54 offen sc1\n" \
" buffer_load_dword v13, v15, s[36:39], s80 offen sc1\n" \
" s_waitcnt vmcnt(0)\n" \
" v_cmp_ge_u32_e64 s[96:97], s81, v13\n" \
" s_nop 4\n" \
" s_cmp_lg_u64 s[96:97], 0\n" \
" s_cbranch_scc1 Lq_f0_cold\n" \
"Lq_f0_ok:\n" \
" s_add_u32 m0, s62, 131072\n" \
" s_add_u32 s54, s63, 10371072\n" \
" buffer_load_dwordx4 v3, s[36:39], s54 offen offset:0 sc1 lds\n" \
" buffer_load_dwordx4 v3, s[36:39], s54 offen offset:1024 sc1 lds\n" \
" s_add_u32 m0, s62, 139264\n" \
" s_add_u32 s54, s63, 10379264\n" \
" buffer_load_dwordx4 v3, s[36:39], s54 offen offset:0 sc1 lds\n" \
" buffer_load_dwordx4 v3, s[36:39], s54 offen offset:1024 sc1 lds\n" \
" s_add_u32 m0, s62, 147456\n" \
" s_add_u32 s54, s63, 10387456\n" \
" buffer_load_dwordx4 v3, s[36:39], s54 offen offset:0 sc1 lds\n" \
" buffer_load_dwordx4 v3, s[36:39], s54 offen offset:1024 sc1 lds\n" \
" s_memrealtime s[56:57]\n" \
" s_waitcnt lgkmcnt(0)\n" \
" s_mov_b32 s50, s56\n" \
" s_waitcnt vmcnt(0) lgkmcnt(0)\n" \
" s_nop 7\n" \
" s_nop 7\n" \
".p2align 6\n" \
"Lq_loop:\n" \
" s_add_u32 s54, s49, 0\n" \
" s_and_b32 s54, s54, 3\n" \
" s_mul_i32 s58, s54, 327680\n" \
" s_add_u32 s54, s49, 1\n" \
" s_and_b32 s54, s54, 3\n" \
" s_mul_i32 s59, s54, 327680\n" \
" s_add_u32 s54, s49, 2\n" \
" s_and_b32 s54, s54, 3\n" \
" s_mul_i32 s60, s54, 327680\n" \
" s_add_u32 s54, s49, 3\n" \
" s_and_b32 s54, s54, 3\n" \
" s_mul_i32 s61, s54, 327680\n" \
" ds_read_b128 v[32:35], v1 offset:0\n" \
" ds_read_b128 v[36:39], v1 offset:1024\n" \
" ds_read_b128 v[40:43], v1 offset:2048\n" \
" ds_read_b128 v[44:47], v1 offset:3072\n" \
" ds_read_b128 v[48:51], v2 offset:32768\n" \
" ds_read_b128 v[52:55], v2 offset:33792\n" \
" ds_read_b128 v[56:59], v2 offset:34816\n" \
" ds_read_b128 v[60:63], v2 offset:35840\n" \
" v_mov_b64 v[80:81], 0\n" \
" v_mov_b64 v[82:83], 0\n" \
" v_mov_b64 v[84:85], 0\n" \
" v_mov_b64 v[86:87], 0\n" \
" v_mov_b64 v[88:89], 0\n" \
" v_mov_b64 v[90:91], 0\n" \
" v_mov_b64 v[92:93], 0\n" \
" v_mov_b64 v[94:95], 0\n" \
" v_mov_b32 v13, -1\n" \
" v_add_u32 v14, s62, v3\n" \
" v_xor_b32 v31, 0x400, v11\n" \
" s_waitcnt lgkmcnt(0)\n" \
" s_lshl_b32 s55, s48, 10\n" \
" s_add_u32 s55, s55, 114688\n" \
" s_add_u32 s89, s58, 4096\n" \
" s_add_u32 s90, s58, 16384\n" \
" s_add_u32 s91, s58, 20480\n" \
" s_add_u32 s92, s58, s94\n" \
" s_add_u32 s93, s60, s94\n" \
" s_add_u32 s81, s49, 1\n" \
" s_min_u32 s81, s81, 63\n" \
" s_mov_b32 m0, s55\n" \
"Lq_w:\n" \
" s_memrealtime s[56:57]\n" \
" s_waitcnt lgkmcnt(0)\n" \
" s_sub_u32 s54, s56, s50\n" \
" s_cmp_gt_u32 s54, 0xfffffc00\n" \
" s_cbranch_scc1 Lq_w\n" \
"Lq_go:\n" \
" buffer_load_dwordx4 v4, s[36:39], s58 offen sc1 lds\n" \
" s_nop 7\n" \
" s_nop 7\n" \
" s_nop 7\n" \
" s_add_u32 m0, s55, 4096\n" \
" s_nop 0\n" \
" buffer_load_dwordx4 v4, s[36:39], s89 offen sc1 lds\n" \
" s_nop 7\n" \
" s_nop 7\n" \
" s_add_u32 m0, s55, 8192\n" \
" s_nop 0\n" \
" buffer_load_dwordx4 v4, s[36:39], s90 offen sc1 lds\n" \
" s_nop 7\n" \
" s_add_u32 m0, s55, 12288\n" \
" s_nop 0\n" \
" buffer_load_dwordx4 v4, s[36:39], s91 offen sc1 lds\n" \
" s_cmp_lt_u32 s54, 6\n" \
" s_cselect_b32 s51, s50, s56\n" \
" s_max_u32 s51, s51, 1\n" \
" v_mov_b32 v30, s51\n" \
" s_waitcnt vmcnt(3)\n" \
" s_barrier\n" \
" ds_read_b128 v[64:67], v3 offset:0\n" \
" ds_read_b128 v[68:71], v3 offset:1024\n" \
" ds_read_b128 v[72:75], v3 offset:2048\n" \
" ds_read_b128 v[76:79], v3 offset:3072\n" \
" s_waitcnt lgkmcnt(3)\n" \
" v_smfmac_f32_16x16x64_bf16 v[80:83], v[64:67], a[0:7], v12\n" \
" v_min3_u32 v13, v13, v64, v66\n" \
" v_smfmac_f32_16x16x64_bf16 v[84:87], v[64:67], a[8:15], v12\n" \
" v_smfmac_f32_16x16x64_bf16 v[88:91], v[64:67], v[32:39], v12\n" \
" v_smfmac_f32_16x16x64_bf16 v[92:95], v[64:67], v[40:47], v12\n" \
" s_waitcnt lgkmcnt(2)\n" \
" v_smfmac_f32_16x16x64_bf16 v[80:83], v[68:71], a[16:23], v12\n" \
" v_min3_u32 v13, v13, v68, v70\n" \
" v_smfmac_f32_16x16x64_bf16 v[84:87], v[68:71], a[24:31], v12\n" \
" ds_read_b128 v[32:35], v2 offset:0\n" \
" v_smfmac_f32_16x16x64_bf16 v[88:91], v[68:71], v[112:119], v12\n" \
" ds_read_b128 v[36:39], v2 offset:1024\n" \
" v_smfmac_f32_16x16x64_bf16 v[92:95], v[68:71], v[120:127], v12\n" \
" s_waitcnt vmcnt(2)\n" \
" s_barrier\n" \
" ds_read_b128 v[64:67], v3 offset:4096\n" \
" s_waitcnt lgkmcnt(4)\n" \
" v_smfmac_f32_16x16x64_bf16 v[80:83], v[72:75], a[32:39], v12\n" \
" ds_read_b128 v[68:71], v3 offset:5120\n" \
" v_min3_u32 v13, v13, v72, v74\n" \
" v_smfmac_f32_16x16x64_bf16 v[84:87], v[72:75], a[40:47], v12\n" \
" ds_read_b128 v[40:43], v2 offset:2048\n" \
" v_smfmac_f32_16x16x64_bf16 v[88:91], v[72:75], v[48:55], v12\n" \
" ds_read_b128 v[44:47], v2 offset:3072\n" \
" v_smfmac_f32_16x16x64_bf16 v[92:95], v[72:75], v[56:63], v12\n" \
" ds_read_b128 v[72:75], v3 offset:6144\n" \
" s_waitcnt lgkmcnt(7)\n" \
" v_smfmac_f32_16x16x64_bf16 v[80:83], v[76:79], a[48:55], v12\n" \
" v_min3_u32 v13, v13, v76, v78\n" \
" v_smfmac_f32_16x16x64_bf16 v[84:87], v[76:79], a[56:63], v12\n" \
" ds_read_b128 v[48:51], v2 offset:16384\n" \
" v_cmp_eq_u32 vcc, 0, v13\n" \
" v_smfmac_f32_16x16x64_bf16 v[88:91], v[76:79], v[128:135], v12\n" \
" ds_read_b128 v[52:55], v2 offset:17408\n" \
" v_smfmac_f32_16x16x64_bf16 v[92:95], v[76:79], v[136:143], v12\n" \
" ds_read_b128 v[76:79], v3 offset:7168\n" \
" s_waitcnt lgkmcnt(7)\n" \
" v_smfmac_f32_16x16x64_bf16 v[80:83], v[64:67], a[64:71], v12\n" \
" s_nop 0\n" \
" s_nop 0\n" \
" s_nop 0\n" \
" s_cbranch_vccnz Lq_retry\n" \
" v_min3_u32 v13, v13, v64, v66\n" \
" v_smfmac_f32_16x16x64_bf16 v[84:87], v[64:67], a[72:79], v12\n" \
" ds_read_b128 v[56:59], v2 offset:18432\n" \
" v_smfmac_f32_16x16x64_bf16 v[88:91], v[64:67], v[32:39], v12\n" \
" s_waitcnt vmcnt(0)\n" \
" s_barrier\n" \
" ds_read_b128 v[60:63], v2 offset:19456\n" \
" s_waitcnt lgkmcnt(6)\n" \
" v_smfmac_f32_16x16x64_bf16 v[92:95], v[64:67], v[40:47], v12\n" \
" ds_read_b128 v[64:67], v3 offset:8192\n" \
" v_smfmac_f32_16x16x64_bf16 v[80:83], v[68:71], a[80:87], v12\n" \
" buffer_store_dword v30, v10, s[36:39], s92 offen sc1\n" \
" v_min3_u32 v13, v13, v68, v70\n" \
" v_smfmac_f32_16x16x64_bf16 v[84:87], v[68:71], a[88:95], v12\n" \
" ds_read_b128 v[32:35], v1 offset:32768\n" \
" v_smfmac_f32_16x16x64_bf16 v[88:91], v[68:71], v[144:151], v12\n" \
" ds_read_b128 v[36:39], v1 offset:33792\n" \
" v_smfmac_f32_16x16x64_bf16 v[92:95], v[68:71], v[152:159], v12\n" \
" ds_read_b128 v[68:71], v3 offset:9216\n" \
" s_waitcnt lgkmcnt(9)\n" \
" v_smfmac_f32_16x16x64_bf16 v[80:83], v[72:75], a[96:103], v12\n" \
" buffer_store_dwordx4 v[96:99], v11, s[36:39], s95 offen sc1\n" \
" v_min3_u32 v13, v13, v72, v74\n" \
" v_smfmac_f32_16x16x64_bf16 v[84:87], v[72:75], a[104:111], v12\n" \
" ds_read_b128 v[40:43], v1 offset:34816\n" \
" s_waitcnt lgkmcnt(8)\n" \
" v_smfmac_f32_16x16x64_bf16 v[88:91], v[72:75], v[48:55], v12\n" \
" ds_read_b128 v[44:47], v1 offset:35840\n" \
" s_waitcnt lgkmcnt(6)\n" \
" v_smfmac_f32_16x16x64_bf16 v[92:95], v[72:75], v[56:63], v12\n" \
" ds_read_b128 v[72:75], v3 offset:10240\n" \
" v_smfmac_f32_16x16x64_bf16 v[80:83], v[76:79], a[112:119], v12\n" \
" v_min3_u32 v13, v13, v76, v78\n" \
" v_smfmac_f32_16x16x64_bf16 v[84:87], v[76:79], a[120:127], v12\n" \
" ds_read_b128 v[48:51], v1 offset:16384\n" \
" v_smfmac_f32_16x16x64_bf16 v[88:91], v[76:79], v[160:167], v12\n" \
" ds_read_b128 v[52:55], v1 offset:17408\n" \
" v_smfmac_f32_16x16x64_bf16 v[92:95], v[76:79], v[168:175], v12\n" \
" ds_read_b128 v[76:79], v3 offset:11264\n" \
" s_waitcnt lgkmcnt(9)\n" \
" v_smfmac_f32_16x16x64_bf16 v[80:83], v[64:67], a[128:135], v12\n" \
" v_min3_u32 v13, v13, v64, v66\n" \
" v_smfmac_f32_16x16x64_bf16 v[84:87], v[64:67], a[136:143], v12\n" \
" ds_read_b128 v[56:59], v1 offset:18432\n" \
" s_waitcnt lgkmcnt(8)\n" \
" v_smfmac_f32_16x16x64_bf16 v[88:91], v[64:67], v[32:39], v12\n" \
" ds_read_b128 v[60:63], v1 offset:19456\n" \
" s_waitcnt lgkmcnt(6)\n" \
" v_smfmac_f32_16x16x64_bf16 v[92:95], v[64:67], v[40:47], v12\n" \
" ds_read_b128 v[64:67], v3 offset:12288\n" \
" v_smfmac_f32_16x16x64_bf16 v[80:83], v[68:71], a[144:151], v12\n" \
" v_min3_u32 v13, v13, v68, v70\n" \
" v_smfmac_f32_16x16x64_bf16 v[84:87], v[68:71], a[152:159], v12\n" \
" ds_read_b128 v[32:35], v1 offset:49152\n" \
" v_smfmac_f32_16x16x64_bf16 v[88:91], v[68:71], v[176:183], v12\n" \
" ds_read_b128 v[36:39], v1 offset:50176\n" \
" v_smfmac_f32_16x16x64_bf16 v[92:95], v[68:71], v[184:191], v12\n" \
" ds_read_b128 v[68:71], v3 offset:13312\n" \
" s_waitcnt lgkmcnt(9)\n" \
" v_smfmac_f32_16x16x64_bf16 v[80:83], v[72:75], a[160:167], v12\n" \
" buffer_store_dwordx4 v[100:103], v31, s[36:39], s95 offen sc1\n" \
" v_min3_u32 v13, v13, v72, v74\n" \
" v_smfmac_f32_16x16x64_bf16 v[84:87], v[72:75], a[168:175], v12\n" \
" ds_read_b128 v[40:43], v1 offset:51200\n" \
" s_waitcnt lgkmcnt(8)\n" \
" v_smfmac_f32_16x16x64_bf16 v[88:91], v[72:75], v[48:55], v12\n" \
" ds_read_b128 v[44:47], v1 offset:52224\n" \
" s_waitcnt lgkmcnt(6)\n" \
" v_smfmac_f32_16x16x64_bf16 v[92:95], v[72:75], v[56:63], v12\n" \
" ds_read_b128 v[72:75], v3 offset:14336\n" \
" v_smfmac_f32_16x16x64_bf16 v[80:83], v[76:79], a[176:183], v12\n" \
" s_mov_b32 s95, 0x7ffffff0\n" \
" buffer_load_dword v31, v15, s[36:39], s80 offen sc1\n" \
" v_min3_u32 v13, v13, v76, v78\n" \
" v_smfmac_f32_16x16x64_bf16 v[84:87], v[76:79], a[184:191], v12\n" \
" ds_read_b128 v[48:51], v3 offset:20480\n" \
" v_smfmac_f32_16x16x64_bf16 v[88:91], v[76:79], v[192:199], v12\n" \
" ds_read_b128 v[52:55], v3 offset:21504\n" \
" v_smfmac_f32_16x16x64_bf16 v[92:95], v[76:79], v[200:207], v12\n" \
" ds_read_b128 v[76:79], v3 offset:15360\n" \
" s_waitcnt lgkmcnt(9)\n" \
" v_smfmac_f32_16x16x64_bf16 v[80:83], v[64:67], a[192:199], v12\n" \
" v_min3_u32 v13, v13, v64, v66\n" \
" v_smfmac_f32_16x16x64_bf16 v[84:87], v[64:67], a[200:207], v12\n" \
" ds_read_b128 v[56:59], v3 offset:22528\n" \
" s_waitcnt lgkmcnt(8)\n" \
" v_smfmac_f32_16x16x64_bf16 v[88:91], v[64:67], v[32:39], v12\n" \
" ds_read_b128 v[60:63], v3 offset:23552\n" \
" s_waitcnt lgkmcnt(6)\n" \
" v_smfmac_f32_16x16x64_bf16 v[92:95], v[64:67], v[40:47], v12\n" \
" v_smfmac_f32_16x16x64_bf16 v[80:83], v[68:71], a[208:215], v12\n" \
" ds_read_b128 v[96:99], v14 offset:32768\n" \
" v_min3_u32 v13, v13, v68, v70\n" \
" v_smfmac_f32_16x16x64_bf16 v[84:87], v[68:71], a[216:223], v12\n" \
" ds_read_b128 v[32:35], v3 offset:16384\n" \
" v_smfmac_f32_16x16x64_bf16 v[88:91], v[68:71], v[208:215], v12\n" \
" ds_read_b128 v[36:39], v3 offset:17408\n" \
" v_smfmac_f32_16x16x64_bf16 v[92:95], v[68:71], v[216:223], v12\n" \
" s_waitcnt lgkmcnt(8)\n" \
" v_smfmac_f32_16x16x64_bf16 v[80:83], v[72:75], a[224:231], v12\n" \
" ds_read_b128 v[100:103], v14 offset:33792\n" \
" v_min3_u32 v13, v13, v72, v74\n" \
" v_smfmac_f32_16x16x64_bf16 v[84:87], v[72:75], a[232:239], v12\n" \
" ds_read_b128 v[40:43], v3 offset:18432\n" \
" s_waitcnt lgkmcnt(7)\n" \
" v_smfmac_f32_16x16x64_bf16 v[80:83], v[76:79], a[240:247], v12\n" \
" ds_read_b128 v[44:47], v3 offset:19456\n" \
" v_min3_u32 v13, v13, v76, v78\n" \
" v_smfmac_f32_16x16x64_bf16 v[84:87], v[76:79], a[248:255], v12\n" \
" v_cmp_eq_u32_e64 vcc, 0, v13\n" \
" v_smfmac_f32_16x16x64_bf16 v[88:91], v[72:75], v[224:231], v12\n" \
" v_smfmac_f32_16x16x64_bf16 v[92:95], v[72:75], v[232:239], v12\n" \
" v_smfmac_f32_16x16x64_bf16 v[88:91], v[76:79], v[240:247], v12\n" \
" s_cbranch_vccnz Lq_retry\n" \
" v_smfmac_f32_16x16x64_bf16 v[92:95], v[76:79], v[248:255], v12\n" \
" v_pk_add_f32 v[80:81], v[80:81], v[82:83]\n" \
" v_pk_add_f32 v[84:85], v[84:85], v[86:87]\n" \
" v_pk_mul_f32 v[80:81], v[80:81], s[98:99]\n" \
" v_pk_mul_f32 v[84:85], v[84:85], s[98:99]\n" \
" v_cvt_pk_bf16_f32 v80, v80, v81\n" \
" v_cvt_pk_bf16_f32 v84, v84, v85\n" \
" ds_write2_b32 v5, v80, v84 offset1:128\n" \
" v_pk_add_f32 v[88:89], v[88:89], v[90:91]\n" \
" v_pk_add_f32 v[92:93], v[92:93], v[94:95]\n" \
" v_cvt_pk_bf16_f32 v88, v88, v89\n" \
" v_cvt_pk_bf16_f32 v92, v92, v93\n" \
" ds_write2_b32 v0, v88, v92 offset1:128\n" \
" buffer_load_dwordx4 v[104:107], v8, s[36:39], s58 offen sc1\n" \
" s_cmp_eq_u32 s49, 63\n" \
" s_waitcnt lgkmcnt(0)\n" \
" s_barrier\n" \
" ds_read_b64_tr_b16 v[64:65], v6 offset:0\n" \
" ds_read_b64_tr_b16 v[66:67], v6 offset:512\n" \
" ds_read_b64_tr_b16 v[68:69], v6 offset:1024\n" \
" ds_read_b64_tr_b16 v[70:71], v6 offset:1536\n" \
" ds_read_b64_tr_b16 v[72:73], v6 offset:2048\n" \
" ds_read_b64_tr_b16 v[74:75], v6 offset:2560\n" \
" ds_read_b64_tr_b16 v[76:77], v6 offset:3072\n" \
" ds_read_b64_tr_b16 v[78:79], v6 offset:3584\n" \
" s_cbranch_scc1 Lq_last\n" \
" s_waitcnt lgkmcnt(6)\n" \
" v_mfma_f32_16x16x32_bf16 v[88:91], v[64:67], v[64:67], v[16:19]\n" \
" v_mfma_f32_16x16x32_bf16 v[80:83], v[32:35], v[64:67], 0\n" \
" s_waitcnt lgkmcnt(4)\n" \
" v_mfma_f32_16x16x32_bf16 v[88:91], v[68:71], v[68:71], v[88:91]\n" \
" v_mfma_f32_16x16x32_bf16 v[80:83], v[36:39], v[68:71], v[80:83]\n" \
" s_waitcnt lgkmcnt(2)\n" \
" v_mfma_f32_16x16x32_bf16 v[88:91], v[72:75], v[72:75], v[88:91]\n" \
" v_mfma_f32_16x16x32_bf16 v[80:83], v[40:43], v[72:75], v[80:83]\n" \
" s_waitcnt lgkmcnt(0)\n" \
" v_mfma_f32_16x16x32_bf16 v[88:91], v[76:79], v[76:79], v[88:91]\n" \
" v_mfma_f32_16x16x32_bf16 v[80:83], v[44:47], v[76:79], v[80:83]\n" \
" v_mfma_f32_16x16x32_bf16 v[84:87], v[48:51], v[64:67], 0\n" \
" v_mfma_f32_16x16x32_bf16 v[84:87], v[52:55], v[68:71], v[84:87]\n" \
" v_max3_f32 v13, v88, v89, v90\n" \
" v_mfma_f32_16x16x32_bf16 v[84:87], v[56:59], v[72:75], v[84:87]\n" \
" v_max3_f32 v14, v91, v80, v81\n" \
" v_mfma_f32_16x16x32_bf16 v[84:87], v[60:63], v[76:79], v[84:87]\n" \
" v_max3_f32 v13, v13, v82, v83\n" \
" s_nop 6\n" \
" v_max3_f32 v14, v14, v84, v85\n" \
" v_max3_f32 v13, v13, v86, v87\n" \
" v_max_f32 v13, v13, v14\n" \
" v_max_f32 v14, v13, v14\n" \
" s_nop 1\n" \
" v_permlane16_swap_b32 v13, v14\n" \
" v_max_f32 v13, v13, v14\n" \
" v_max_f32 v14, v13, v14\n" \
" s_nop 1\n" \
" v_permlane32_swap_b32 v13, v14\n" \
" v_max_f32_e64 v14, v13, v14\n" \
" v_pk_add_f32 v[80:81], v[80:81], v[14:15] op_sel_hi:[1,0] neg_lo:[0,1] neg_hi:[0,1]\n" \
" v_pk_add_f32 v[82:83], v[82:83], v[14:15] op_sel_hi:[1,0] neg_lo:[0,1] neg_hi:[0,1]\n" \
" v_pk_add_f32 v[84:85], v[84:85], v[14:15] op_sel_hi:[1,0] neg_lo:[0,1] neg_hi:[0,1]\n" \
" v_pk_add_f32 v[86:87], v[86:87], v[14:15] op_sel_hi:[1,0] neg_lo:[0,1] neg_hi:[0,1]\n" \
" v_pk_add_f32 v[88:89], v[88:89], v[14:15] op_sel_hi:[1,0] neg_lo:[0,1] neg_hi:[0,1]\n" \
" v_pk_add_f32 v[90:91], v[90:91], v[14:15] op_sel_hi:[1,0] neg_lo:[0,1] neg_hi:[0,1]\n" \
" v_exp_f32 v80, v80\n" \
" v_exp_f32 v81, v81\n" \
" v_exp_f32 v82, v82\n" \
" v_exp_f32 v83, v83\n" \
" v_exp_f32 v84, v84\n" \
" v_exp_f32 v85, v85\n" \
" v_exp_f32 v86, v86\n" \
" v_exp_f32 v87, v87\n" \
" v_exp_f32_e64 v88, v88\n" \
" v_cvt_pk_bf16_f32 v80, v80, v81\n" \
" v_cvt_pk_bf16_f32 v81, v82, v83\n" \
" v_cvt_pk_bf16_f32 v82, v84, v85\n" \
" v_cvt_pk_bf16_f32 v83, v86, v87\n" \
" v_exp_f32 v89, v89\n" \
" s_nop 0\n" \
" v_mfma_f32_16x16x32_bf16 v[84:87], v[96:99], v[80:83], 0\n" \
" v_exp_f32_e64 v90, v90\n" \
" v_mfma_f32_16x16x32_bf16 v[92:95], v[100:103], v[80:83], 0\n" \
" v_exp_f32_e64 v91, v91\n" \
" v_mfma_f32_16x16x32_bf16 v[108:111], v[20:23], v[80:83], 0\n" \
" v_cvt_pk_bf16_f32 v26, v88, v89\n" \
" v_cvt_pk_bf16_f32 v27, v90, v91\n" \
" s_nop 1\n" \
" v_mfma_f32_16x16x32_bf16 v[108:111], v[20:23], v[26:29], v[108:111]\n" \
" s_waitcnt vmcnt(0)\n" \
" v_mfma_f32_16x16x32_bf16 v[84:87], v[104:107], v[26:29], v[84:87]\n" \
" v_mfma_f32_16x16x32_bf16 v[92:95], v[104:107], v[24:27], v[92:95]\n" \
" v_min_u32 v13, v104, v106\n" \
" v_cmp_eq_u32_e64 vcc, 0, v13\n" \
" v_rcp_f32_e64 v14, v108\n" \
" s_and_b64 vcc, vcc, s[82:83]\n" \
" s_cbranch_vccnz Lq_vr\n" \
"Lq_vr2:\n" \
" v_pk_mul_f32 v[84:85], v[84:85], v[14:15] op_sel_hi:[1,0]\n" \
" v_pk_mul_f32 v[86:87], v[86:87], v[14:15] op_sel_hi:[1,0]\n" \
" v_cvt_pk_bf16_f32 v80, v84, v85\n" \
" v_cvt_pk_bf16_f32 v81, v86, v87\n" \
" v_max_u32 v80, 1, v80\n" \
" v_pk_mul_f32 v[92:93], v[92:93], v[14:15] op_sel_hi:[1,0]\n" \
" v_pk_mul_f32 v[94:95], v[94:95], v[14:15] op_sel_hi:[1,0]\n" \
" v_cvt_pk_bf16_f32 v82, v92, v93\n" \
" v_cvt_pk_bf16_f32 v83, v94, v95\n" \
" v_max_u32_e64 v82, 1, v82\n" \
" buffer_store_dwordx4 v[80:83], v7, s[36:39], s58 offen sc1\n" \
"Lq_tok:\n" \
" s_and_b32 s54, s48, 1\n" \
" s_lshl_b32 s54, s54, 12\n" \
" v_add_u32_e64 v14, s54, v3\n" \
" ds_read_b128 v[96:99], v14 offset:24576\n" \
" ds_read_b128 v[100:103], v14 offset:25600\n" \
" ds_read_b128 v[92:95], v14 offset:26624\n" \
" v_cmp_ge_u32_e64 s[96:97], s81, v31\n" \
" v_xor_b32 v31, 512, v8\n" \
" buffer_load_dwordx4 v[108:111], v31, s[36:39], s58 offen sc1\n" \
" buffer_load_dword v30, v9, s[36:39], s58 offen sc1\n" \
" v_mov_b64 v[26:27], 0\n" \
" buffer_store_dwordx4 v[24:27], v7, s[36:39], s60 offen sc1\n" \
" buffer_store_dword v24, v10, s[36:39], s93 offen sc1\n" \
" s_waitcnt lgkmcnt(2)\n" \
" v_mfma_f32_16x16x32_bf16 v[88:91], v[64:67], v[96:99], v[16:19]\n" \
" v_mfma_f32_16x16x32_bf16 v[80:83], v[32:35], v[96:99], 0\n" \
" v_mfma_f32_16x16x32_bf16 v[84:87], v[48:51], v[96:99], 0\n" \
" ds_read_b128 v[96:99], v14 offset:27648\n" \
" s_waitcnt lgkmcnt(2)\n" \
" v_mfma_f32_16x16x32_bf16 v[88:91], v[68:71], v[100:103], v[88:91]\n" \
" v_mfma_f32_16x16x32_bf16 v[80:83], v[36:39], v[100:103], v[80:83]\n" \
" v_mfma_f32_16x16x32_bf16 v[84:87], v[52:55], v[100:103], v[84:87]\n" \
" s_waitcnt lgkmcnt(1)\n" \
" v_mfma_f32_16x16x32_bf16 v[88:91], v[72:75], v[92:95], v[88:91]\n" \
" v_mfma_f32_16x16x32_bf16 v[80:83], v[40:43], v[92:95], v[80:83]\n" \
" v_mfma_f32_16x16x32_bf16 v[84:87], v[56:59], v[92:95], v[84:87]\n" \
" s_waitcnt lgkmcnt(0)\n" \
" v_mfma_f32_16x16x32_bf16 v[88:91], v[76:79], v[96:99], v[88:91]\n" \
" v_mfma_f32_16x16x32_bf16 v[80:83], v[44:47], v[96:99], v[80:83]\n" \
" v_mfma_f32_16x16x32_bf16 v[84:87], v[60:63], v[96:99], v[84:87]\n" \
" v_add_u32 v14, s62, v3\n" \
" v_xor_b32 v31, 0x800, v14\n" \
" ds_read_b128 v[32:35], v14 offset:32768\n" \
" ds_read_b128 v[36:39], v14 offset:33792\n" \
" ds_read_b128 v[40:43], v31 offset:32768\n" \
" ds_read_b128 v[44:47], v31 offset:33792\n" \
" v_mov_b32 v13, 0xceabdc6e\n" \
" v_cndmask_b32_e64 v80, v13, v80, s[64:65]\n" \
" v_cndmask_b32_e64 v81, v13, v81, s[66:67]\n" \
" v_cndmask_b32_e64 v82, v13, v82, s[68:69]\n" \
" v_cndmask_b32_e64 v83, v13, v83, s[70:71]\n" \
" v_cndmask_b32_e64 v84, v13, v84, s[72:73]\n" \
" v_cndmask_b32_e64 v85, v13, v85, s[74:75]\n" \
" v_cndmask_b32_e64 v86, v13, v86, s[76:77]\n" \
" v_cndmask_b32_e64 v87, v13, v87, s[78:79]\n" \
" s_cmp_lg_u64 s[96:97], 0\n" \
" s_cbranch_scc1 Lq_f_cold\n" \
"Lq_f_ok:\n" \
" s_cmp_lt_u32 s49, 63\n" \
" s_cselect_b32 s54, 24576, 0\n" \
" s_add_u32 s63, s63, s54\n" \
" s_waitcnt lgkmcnt(0)\n" \
" s_barrier\n" \
" s_add_u32 m0, s62, 131072\n" \
" s_add_u32 s54, s63, 10371072\n" \
" buffer_load_dwordx4 v3, s[36:39], s54 offen offset:0 sc1 lds\n" \
" buffer_load_dwordx4 v3, s[36:39], s54 offen offset:1024 sc1 lds\n" \
" s_add_u32 m0, s62, 139264\n" \
" s_add_u32 s54, s63, 10379264\n" \
" buffer_load_dwordx4 v3, s[36:39], s54 offen offset:0 sc1 lds\n" \
" buffer_load_dwordx4 v3, s[36:39], s54 offen offset:1024 sc1 lds\n" \
" s_add_u32 m0, s62, 147456\n" \
" s_add_u32 s54, s63, 10387456\n" \
" buffer_load_dwordx4 v3, s[36:39], s54 offen offset:0 sc1 lds\n" \
" buffer_load_dwordx4 v3, s[36:39], s54 offen offset:1024 sc1 lds\n" \
" v_mov_b32_e64 v31, s49\n" \
" buffer_store_dword v31, v10, s[36:39], s87 offen sc1\n" \
" v_max3_f32 v13, v88, v89, v90\n" \
" v_max3_f32 v14, v91, v80, v81\n" \
" v_max3_f32 v13, v13, v82, v83\n" \
" v_max3_f32 v14, v14, v84, v85\n" \
" v_max3_f32 v13, v13, v86, v87\n" \
" v_max_f32 v13, v13, v14\n" \
" v_max_f32 v14, v13, v14\n" \
" s_nop 1\n" \
" v_permlane16_swap_b32 v13, v14\n" \
" v_max_f32 v13, v13, v14\n" \
" v_max_f32 v14, v13, v14\n" \
" s_nop 1\n" \
" v_permlane32_swap_b32 v13, v14\n" \
" v_max_f32_e64 v14, v13, v14\n" \
" v_pk_add_f32 v[80:81], v[80:81], v[14:15] op_sel_hi:[1,0] neg_lo:[0,1] neg_hi:[0,1]\n" \
" v_pk_add_f32 v[82:83], v[82:83], v[14:15] op_sel_hi:[1,0] neg_lo:[0,1] neg_hi:[0,1]\n" \
" v_pk_add_f32 v[84:85], v[84:85], v[14:15] op_sel_hi:[1,0] neg_lo:[0,1] neg_hi:[0,1]\n" \
" v_pk_add_f32 v[86:87], v[86:87], v[14:15] op_sel_hi:[1,0] neg_lo:[0,1] neg_hi:[0,1]\n" \
" v_pk_add_f32 v[88:89], v[88:89], v[14:15] op_sel_hi:[1,0] neg_lo:[0,1] neg_hi:[0,1]\n" \
" v_pk_add_f32 v[90:91], v[90:91], v[14:15] op_sel_hi:[1,0] neg_lo:[0,1] neg_hi:[0,1]\n" \
" v_exp_f32 v80, v80\n" \
" v_exp_f32 v81, v81\n" \
" v_exp_f32 v82, v82\n" \
" v_exp_f32 v83, v83\n" \
" v_exp_f32 v84, v84\n" \
" v_exp_f32 v85, v85\n" \
" v_exp_f32 v86, v86\n" \
" v_exp_f32 v87, v87\n" \
" v_exp_f32_e64 v88, v88\n" \
" v_cvt_pk_bf16_f32 v80, v80, v81\n" \
" v_cvt_pk_bf16_f32 v81, v82, v83\n" \
" v_cvt_pk_bf16_f32 v82, v84, v85\n" \
" v_cvt_pk_bf16_f32 v83, v86, v87\n" \
" v_exp_f32 v89, v89\n" \
" s_nop 0\n" \
" v_mfma_f32_16x16x32_bf16 v[48:51], v[32:35], v[80:83], 0\n" \
" v_exp_f32_e64 v90, v90\n" \
" v_mfma_f32_16x16x32_bf16 v[52:55], v[36:39], v[80:83], 0\n" \
" v_exp_f32_e64 v91, v91\n" \
" v_mfma_f32_16x16x32_bf16 v[56:59], v[40:43], v[80:83], 0\n" \
" v_cvt_pk_bf16_f32 v26, v88, v89\n" \
" v_cvt_pk_bf16_f32 v27, v90, v91\n" \
" v_mfma_f32_16x16x32_bf16 v[60:63], v[44:47], v[80:83], 0\n" \
" v_mfma_f32_16x16x32_bf16 v[92:95], v[20:23], v[80:83], 0\n" \
" s_waitcnt vmcnt(10)\n" \
" v_min3_u32 v13, v104, v106, v108\n" \
" v_min3_u32 v13, v13, v110, v110\n" \
" v_cmp_eq_u32 vcc, 0, v13\n" \
" s_nop 0\n" \
" s_and_b64 vcc, vcc, s[82:83]\n" \
" s_cbranch_vccnz Lq_v2_cold\n" \
"Lq_v2_ok:\n" \
" v_mfma_f32_16x16x32_bf16 v[92:95], v[20:23], v[26:29], v[92:95]\n" \
" v_mfma_f32_16x16x32_bf16 v[48:51], v[104:107], v[26:29], v[48:51]\n" \
" v_mfma_f32_16x16x32_bf16 v[52:55], v[104:107], v[24:27], v[52:55]\n" \
" v_mfma_f32_16x16x32_bf16 v[56:59], v[108:111], v[26:29], v[56:59]\n" \
" v_mfma_f32_16x16x32_bf16 v[60:63], v[108:111], v[24:27], v[60:63]\n" \
" s_lshl_b32 s95, s49, 13\n" \
" v_rcp_f32 v14, v92\n" \
" s_nop 0\n" \
" v_pk_mul_f32 v[48:49], v[48:49], v[14:15] op_sel_hi:[1,0]\n" \
" v_pk_mul_f32 v[50:51], v[50:51], v[14:15] op_sel_hi:[1,0]\n" \
" v_pk_mul_f32 v[52:53], v[52:53], v[14:15] op_sel_hi:[1,0]\n" \
" v_pk_mul_f32 v[54:55], v[54:55], v[14:15] op_sel_hi:[1,0]\n" \
" v_cvt_pk_bf16_f32 v96, v48, v49\n" \
" v_cvt_pk_bf16_f32 v97, v50, v51\n" \
" v_cvt_pk_bf16_f32 v98, v52, v53\n" \
" v_cvt_pk_bf16_f32 v99, v54, v55\n" \
" v_pk_mul_f32 v[56:57], v[56:57], v[14:15] op_sel_hi:[1,0]\n" \
" v_pk_mul_f32 v[58:59], v[58:59], v[14:15] op_sel_hi:[1,0]\n" \
" v_pk_mul_f32 v[60:61], v[60:61], v[14:15] op_sel_hi:[1,0]\n" \
" v_pk_mul_f32 v[62:63], v[62:63], v[14:15] op_sel_hi:[1,0]\n" \
" v_cvt_pk_bf16_f32 v100, v56, v57\n" \
" v_cvt_pk_bf16_f32 v101, v58, v59\n" \
" v_cvt_pk_bf16_f32 v102, v60, v61\n" \
" v_cvt_pk_bf16_f32 v103, v62, v63\n" \
" s_cmp_eq_u32 s49, 63\n" \
" s_cbranch_scc1 Lq_end\n" \
" s_waitcnt vmcnt(9)\n" \
" v_cmp_eq_u32_e64 vcc, 0, v30\n" \
" s_nop 0\n" \
" s_nop 0\n" \
" s_nop 0\n" \
" s_cbranch_vccnz Lq_h_cold\n" \
"Lq_h_ok:\n" \
" v_subrev_u32 v30, s51, v30\n" \
" s_nop 1\n" \
" v_max_i32_dpp v30, v30, v30 row_ror:8 row_mask:0xf bank_mask:0xf\n" \
" s_nop 1\n" \
" v_max_i32_dpp v30, v30, v30 row_ror:4 row_mask:0xf bank_mask:0xf\n" \
" s_nop 1\n" \
" v_max_i32_dpp v30, v30, v30 row_ror:2 row_mask:0xf bank_mask:0xf\n" \
" s_nop 1\n" \
" v_max_i32_dpp v30, v30, v30 row_ror:1 row_mask:0xf bank_mask:0xf\n" \
" s_nop 0\n" \
" v_readlane_b32 s54, v30, 0\n" \
" v_readlane_b32 s55, v30, 16\n" \
" s_nop 0\n" \
" s_max_i32 s55, s55, s54\n" \
" s_add_u32 s55, s55, s51\n" \
" s_add_u32 s50, s55, s45\n" \
" s_add_u32 s49, s49, 1\n" \
" s_branch Lq_loop\n" \
"; cold\n" \
"Lq_p_cold:\n" \
" s_nop 7\n" \
" s_nop 7\n" \
" s_waitcnt vmcnt(0) lgkmcnt(0)\n" \
" s_mov_b32 s53, 0\n" \
"Lq_p_again:\n" \
" s_cmp_lg_u32 s52, 0\n" \
" s_cbranch_scc1 Lq_p_ok\n" \
" s_sleep 8\n" \
" buffer_load_dword v13, v24, s[36:39], 0 offen sc1\n" \
" s_waitcnt vmcnt(0)\n" \
" v_cmp_eq_u32 vcc, 0, v13\n" \
" s_nop 4\n" \
" s_cbranch_vccz Lq_p_ok\n" \
" s_add_u32 s53, s53, 1\n" \
" s_cmp_lt_u32 s53, 32768\n" \
" s_cbranch_scc1 Lq_p_again\n" \
" s_mov_b32 s52, 1\n" \
" v_mov_b32 v14, 256\n" \
" v_mov_b32 v15, 1\n" \
" buffer_store_dword v15, v14, s[36:39], 0 offen sc1\n" \
" s_waitcnt vmcnt(0) lgkmcnt(0)\n" \
" s_branch Lq_p_ok\n" \
"Lq_f0_cold:\n" \
" s_nop 7\n" \
" s_nop 7\n" \
" s_waitcnt vmcnt(0) lgkmcnt(0)\n" \
" s_mov_b32 s53, 0\n" \
"Lq_f0_again:\n" \
" s_cmp_lg_u32 s52, 0\n" \
" s_cbranch_scc1 Lq_f0_ok\n" \
" s_sleep 4\n" \
" buffer_load_dword v13, v15, s[36:39], s80 offen sc1\n" \
" s_waitcnt vmcnt(0)\n" \
" v_cmp_ge_u32_e64 s[96:97], s81, v13\n" \
" s_nop 4\n" \
" s_cmp_eq_u64 s[96:97], 0\n" \
" s_cbranch_scc1 Lq_f0_ok\n" \
" s_add_u32 s53, s53, 1\n" \
" s_cmp_lt_u32 s53, 32768\n" \
" s_cbranch_scc1 Lq_f0_again\n" \
" s_mov_b32 s52, 1\n" \
" v_mov_b32 v13, 256\n" \
" v_mov_b32 v14, 1\n" \
" buffer_store_dword v14, v13, s[36:39], 0 offen sc1\n" \
" s_waitcnt vmcnt(0) lgkmcnt(0)\n" \
" s_branch Lq_f0_ok\n" \
"Lq_vr:\n" \
" s_nop 7\n" \
" s_nop 7\n" \
" s_waitcnt vmcnt(0) lgkmcnt(0)\n" \
" buffer_load_dwordx4 v[104:107], v8, s[36:39], s58 offen sc1\n" \
" s_waitcnt vmcnt(0)\n" \
" v_min3_u32 v13, v104, v106, v106\n" \
" v_cmp_eq_u32 vcc, 0, v13\n" \
" s_nop 0\n" \
" s_and_b64 vcc, vcc, s[82:83]\n" \
" s_cbranch_vccnz Lq_v_cold\n" \
"Lq_v_ok:\n" \
" v_mfma_f32_16x16x32_bf16 v[84:87], v[96:99], v[80:83], 0\n" \
" v_mfma_f32_16x16x32_bf16 v[92:95], v[100:103], v[80:83], 0\n" \
" v_mfma_f32_16x16x32_bf16 v[84:87], v[104:107], v[26:29], v[84:87]\n" \
" v_mfma_f32_16x16x32_bf16 v[92:95], v[104:107], v[24:27], v[92:95]\n" \
" s_nop 7\n" \
" s_nop 7\n" \
" s_branch Lq_vr2\n" \
"Lq_v_cold:\n" \
" s_nop 7\n" \
" s_nop 7\n" \
" s_waitcnt vmcnt(0) lgkmcnt(0)\n" \
" s_mov_b32 s53, 0\n" \
"Lq_v_again:\n" \
" s_cmp_lg_u32 s52, 0\n" \
" s_cbranch_scc1 Lq_v_ok\n" \
" s_sleep 1\n" \
" buffer_load_dwordx4 v[104:107], v8, s[36:39], s58 offen sc1\n" \
" s_waitcnt vmcnt(0)\n" \
" v_min3_u32 v13, v104, v106, v106\n" \
" v_cmp_eq_u32 vcc, 0, v13\n" \
" s_nop 0\n" \
" s_and_b64 vcc, vcc, s[82:83]\n" \
" s_nop 4\n" \
" s_cbranch_vccz Lq_v_ok\n" \
" s_add_u32 s53, s53, 1\n" \
" s_cmp_lt_u32 s53, 32768\n" \
" s_cbranch_scc1 Lq_v_again\n" \
" s_mov_b32 s52, 1\n" \
" v_mov_b32 v13, 256\n" \
" v_mov_b32 v14, 1\n" \
" buffer_store_dword v14, v13, s[36:39], 0 offen sc1\n" \
" s_waitcnt vmcnt(0) lgkmcnt(0)\n" \
" s_branch Lq_v_ok\n" \
"Lq_f_cold:\n" \
" s_nop 7\n" \
" s_nop 7\n" \
" s_waitcnt vmcnt(0) lgkmcnt(0)\n" \
" s_mov_b32 s53, 0\n" \
"Lq_f_again:\n" \
" s_cmp_lg_u32 s52, 0\n" \
" s_cbranch_scc1 Lq_f_ok\n" \
" s_sleep 4\n" \
" buffer_load_dword v13, v15, s[36:39], s80 offen sc1\n" \
" s_waitcnt vmcnt(0)\n" \
" v_cmp_ge_u32_e64 s[96:97], s81, v13\n" \
" s_nop 4\n" \
" s_cmp_eq_u64 s[96:97], 0\n" \
" s_cbranch_scc1 Lq_f_ok\n" \
" s_add_u32 s53, s53, 1\n" \
" s_cmp_lt_u32 s53, 32768\n" \
" s_cbranch_scc1 Lq_f_again\n" \
" s_mov_b32 s52, 1\n" \
" v_mov_b32 v13, 256\n" \
" v_mov_b32 v14, 1\n" \
" buffer_store_dword v14, v13, s[36:39], 0 offen sc1\n" \
" s_waitcnt vmcnt(0) lgkmcnt(0)\n" \
" s_branch Lq_f_ok\n" \
"Lq_v2_cold:\n" \
" s_nop 7\n" \
" s_nop 7\n" \
" s_waitcnt vmcnt(0) lgkmcnt(0)\n" \
" s_mov_b32 s53, 0\n" \
"Lq_v2_again:\n" \
" s_cmp_lg_u32 s52, 0\n" \
" s_cbranch_scc1 Lq_v2_ok\n" \
" s_sleep 1\n" \
" v_xor_b32 v31, 512, v8\n" \
" buffer_load_dwordx4 v[104:107], v8, s[36:39], s58 offen sc1\n" \
" buffer_load_dwordx4 v[108:111], v31, s[36:39], s58 offen sc1\n" \
" s_waitcnt vmcnt(0)\n" \
" v_min3_u32 v13, v104, v106, v108\n" \
" v_min3_u32 v13, v13, v110, v110\n" \
" v_cmp_eq_u32 vcc, 0, v13\n" \
" s_nop 0\n" \
" s_and_b64 vcc, vcc, s[82:83]\n" \
" s_nop 4\n" \
" s_cbranch_vccz Lq_v2_ok\n" \
" s_add_u32 s53, s53, 1\n" \
" s_cmp_lt_u32 s53, 32768\n" \
" s_cbranch_scc1 Lq_v2_again\n" \
" s_mov_b32 s52, 1\n" \
" v_mov_b32 v13, 256\n" \
" v_mov_b32 v14, 1\n" \
" buffer_store_dword v14, v13, s[36:39], 0 offen sc1\n" \
" s_waitcnt vmcnt(0) lgkmcnt(0)\n" \
" s_branch Lq_v2_ok\n" \
"Lq_h_cold:\n" \
" s_nop 7\n" \
" s_nop 7\n" \
" s_waitcnt vmcnt(0) lgkmcnt(0)\n" \
" s_mov_b32 s53, 0\n" \
"Lq_h_again:\n" \
" s_cmp_lg_u32 s52, 0\n" \
" s_cbranch_scc1 Lq_h_ok\n" \
" s_sleep 1\n" \
" buffer_load_dword v30, v9, s[36:39], s58 offen sc1\n" \
" s_waitcnt vmcnt(0)\n" \
" v_cmp_eq_u32 vcc, 0, v30\n" \
" s_nop 4\n" \
" s_cbranch_vccz Lq_h_ok\n" \
" s_add_u32 s53, s53, 1\n" \
" s_cmp_lt_u32 s53, 32768\n" \
" s_cbranch_scc1 Lq_h_again\n" \
" s_mov_b32 s52, 1\n" \
" v_mov_b32 v13, 256\n" \
" v_mov_b32 v14, 1\n" \
" buffer_store_dword v14, v13, s[36:39], 0 offen sc1\n" \
" s_waitcnt vmcnt(0) lgkmcnt(0)\n" \
" s_branch Lq_h_ok\n" \
"Lq_retry:\n" \
" s_nop 7\n" \
" s_nop 7\n" \
" s_waitcnt vmcnt(0) lgkmcnt(0)\n" \
" s_barrier\n" \
" s_add_u32 s88, s88, 1\n" \
" s_cmp_lt_u32 s88, 32768\n" \
" s_cbranch_scc1 Lq_r1\n" \
" s_mov_b32 s52, 1\n" \
" v_mov_b32 v13, 256\n" \
" v_mov_b32 v14, 1\n" \
" buffer_store_dword v14, v13, s[36:39], 0 offen sc1\n" \
" s_waitcnt vmcnt(0) lgkmcnt(0)\n" \
" s_endpgm\n" \
"Lq_r1:\n" \
" ds_read_b128 v[32:35], v1 offset:0\n" \
" ds_read_b128 v[36:39], v1 offset:1024\n" \
" ds_read_b128 v[40:43], v1 offset:2048\n" \
" ds_read_b128 v[44:47], v1 offset:3072\n" \
" ds_read_b128 v[48:51], v2 offset:32768\n" \
" ds_read_b128 v[52:55], v2 offset:33792\n" \
" ds_read_b128 v[56:59], v2 offset:34816\n" \
" ds_read_b128 v[60:63], v2 offset:35840\n" \
" v_mov_b64 v[80:81], 0\n" \
" v_mov_b64 v[82:83], 0\n" \
" v_mov_b64 v[84:85], 0\n" \
" v_mov_b64 v[86:87], 0\n" \
" v_mov_b64 v[88:89], 0\n" \
" v_mov_b64 v[90:91], 0\n" \
" v_mov_b64 v[92:93], 0\n" \
" v_mov_b64 v[94:95], 0\n" \
" v_mov_b32 v13, -1\n" \
" v_add_u32 v14, s62, v3\n" \
" v_xor_b32 v31, 0x400, v11\n" \
" s_waitcnt lgkmcnt(0)\n" \
" s_cmp_lg_u32 s49, 0\n" \
" s_cbranch_scc1 Lq_r2\n" \
" s_sleep 8\n" \
"Lq_r2:\n" \
" s_memrealtime s[56:57]\n" \
" s_mov_b32 s54, 100\n" \
" s_mov_b32 m0, s55\n" \
" s_waitcnt vmcnt(0) lgkmcnt(0)\n" \
" s_branch Lq_go\n" \
"Lq_last:\n" \
" s_waitcnt vmcnt(0) lgkmcnt(0)\n" \
" s_nop 7\n" \
" s_nop 7\n" \
" s_branch Lq_tok\n" \
"Lq_end:\n" \
" s_nop 7\n" \
" s_nop 7\n" \
" v_xor_b32 v31, 0x400, v11\n" \
" s_add_u32 s49, s49, 1\n" \
" buffer_store_dwordx4 v[96:99], v11, s[36:39], s95 offen sc1\n" \
" buffer_store_dwordx4 v[100:103], v31, s[36:39], s95 offen sc1\n" \
" s_waitcnt vmcnt(0) lgkmcnt(0)\n" \
" s_barrier\n" \
" v_mov_b32 v31, 64\n" \
" buffer_store_dword v31, v10, s[36:39], s87 offen sc1\n" \
" s_waitcnt vmcnt(0) lgkmcnt(0)\n" \
" s_endpgm\n"
#define ASM_VO \
" s_nop 7\n" \
" s_nop 7\n" \
" buffer_load_dword v20, v13, s[68:71], 0 offen offset:0\n" \
" buffer_load_dword v21, v13, s[68:71], 0 offen offset:8\n" \
" buffer_load_dword v22, v13, s[68:71], 0 offen offset:16\n" \
" buffer_load_dword v23, v13, s[68:71], 0 offen offset:24\n" \
" s_mul_i32 s62, s48, 40960\n" \
" s_add_u32 m0, s62, 0\n" \
" s_mov_b32 s54, 0\n" \
" buffer_load_dwordx4 v1, s[40:43], s54 offen nt lds\n" \
" s_add_u32 m0, s62, 1024\n" \
" s_mov_b32 s54, 65536\n" \
" buffer_load_dwordx4 v1, s[40:43], s54 offen nt lds\n" \
" s_add_u32 m0, s62, 2048\n" \
" s_mov_b32 s54, 131072\n" \
" buffer_load_dwordx4 v1, s[40:43], s54 offen nt lds\n" \
" s_add_u32 m0, s62, 3072\n" \
" s_mov_b32 s54, 196608\n" \
" buffer_load_dwordx4 v1, s[40:43], s54 offen nt lds\n" \
" s_add_u32 m0, s62, 4096\n" \
" s_mov_b32 s54, 262144\n" \
" buffer_load_dwordx4 v1, s[40:43], s54 offen nt lds\n" \
" s_add_u32 m0, s62, 5120\n" \
" s_mov_b32 s54, 327680\n" \
" buffer_load_dwordx4 v1, s[40:43], s54 offen nt lds\n" \
" s_add_u32 m0, s62, 6144\n" \
" s_mov_b32 s54, 393216\n" \
" buffer_load_dwordx4 v1, s[40:43], s54 offen nt lds\n" \
" s_add_u32 m0, s62, 7168\n" \
" s_mov_b32 s54, 458752\n" \
" buffer_load_dwordx4 v1, s[40:43], s54 offen nt lds\n" \
" s_add_u32 m0, s62, 8192\n" \
" s_mov_b32 s54, 524288\n" \
" buffer_load_dwordx4 v1, s[40:43], s54 offen nt lds\n" \
" s_add_u32 m0, s62, 9216\n" \
" s_mov_b32 s54, 589824\n" \
" buffer_load_dwordx4 v1, s[40:43], s54 offen nt lds\n" \
" s_add_u32 m0, s62, 10240\n" \
" s_mov_b32 s54, 655360\n" \
" buffer_load_dwordx4 v1, s[40:43], s54 offen nt lds\n" \
" s_add_u32 m0, s62, 11264\n" \
" s_mov_b32 s54, 720896\n" \
" buffer_load_dwordx4 v1, s[40:43], s54 offen nt lds\n" \
" s_add_u32 m0, s62, 12288\n" \
" s_mov_b32 s54, 786432\n" \
" buffer_load_dwordx4 v1, s[40:43], s54 offen nt lds\n" \
" s_add_u32 m0, s62, 13312\n" \
" s_mov_b32 s54, 851968\n" \
" buffer_load_dwordx4 v1, s[40:43], s54 offen nt lds\n" \
" s_add_u32 m0, s62, 14336\n" \
" s_mov_b32 s54, 917504\n" \
" buffer_load_dwordx4 v1, s[40:43], s54 offen nt lds\n" \
" s_add_u32 m0, s62, 15360\n" \
" s_mov_b32 s54, 983040\n" \
" buffer_load_dwordx4 v1, s[40:43], s54 offen nt lds\n" \
" s_add_u32 m0, s62, 16384\n" \
" s_mov_b32 s54, 1048576\n" \
" buffer_load_dwordx4 v1, s[40:43], s54 offen nt lds\n" \
" s_add_u32 m0, s62, 17408\n" \
" s_mov_b32 s54, 1114112\n" \
" buffer_load_dwordx4 v1, s[40:43], s54 offen nt lds\n" \
" s_add_u32 m0, s62, 18432\n" \
" s_mov_b32 s54, 1179648\n" \
" buffer_load_dwordx4 v1, s[40:43], s54 offen nt lds\n" \
" s_add_u32 m0, s62, 19456\n" \
" s_mov_b32 s54, 1245184\n" \
" buffer_load_dwordx4 v1, s[40:43], s54 offen nt lds\n" \
" s_add_u32 m0, s62, 20480\n" \
" s_mov_b32 s54, 1310720\n" \
" buffer_load_dwordx4 v1, s[40:43], s54 offen nt lds\n" \
" s_add_u32 m0, s62, 21504\n" \
" s_mov_b32 s54, 1376256\n" \
" buffer_load_dwordx4 v1, s[40:43], s54 offen nt lds\n" \
" s_add_u32 m0, s62, 22528\n" \
" s_mov_b32 s54, 1441792\n" \
" buffer_load_dwordx4 v1, s[40:43], s54 offen nt lds\n" \
" s_add_u32 m0, s62, 23552\n" \
" s_mov_b32 s54, 1507328\n" \
" buffer_load_dwordx4 v1, s[40:43], s54 offen nt lds\n" \
" s_add_u32 m0, s62, 24576\n" \
" s_mov_b32 s54, 1572864\n" \
" buffer_load_dwordx4 v1, s[40:43], s54 offen nt lds\n" \
" s_add_u32 m0, s62, 25600\n" \
" s_mov_b32 s54, 1638400\n" \
" buffer_load_dwordx4 v1, s[40:43], s54 offen nt lds\n" \
" s_add_u32 m0, s62, 26624\n" \
" s_mov_b32 s54, 1703936\n" \
" buffer_load_dwordx4 v1, s[40:43], s54 offen nt lds\n" \
" s_add_u32 m0, s62, 27648\n" \
" s_mov_b32 s54, 1769472\n" \
" buffer_load_dwordx4 v1, s[40:43], s54 offen nt lds\n" \
" s_add_u32 m0, s62, 28672\n" \
" s_mov_b32 s54, 1835008\n" \
" buffer_load_dwordx4 v1, s[40:43], s54 offen nt lds\n" \
" s_add_u32 m0, s62, 29696\n" \
" s_mov_b32 s54, 1900544\n" \
" buffer_load_dwordx4 v1, s[40:43], s54 offen nt lds\n" \
" s_add_u32 m0, s62, 30720\n" \
" s_mov_b32 s54, 1966080\n" \
" buffer_load_dwordx4 v1, s[40:43], s54 offen nt lds\n" \
" s_add_u32 m0, s62, 31744\n" \
" s_mov_b32 s54, 2031616\n" \
" buffer_load_dwordx4 v1, s[40:43], s54 offen nt lds\n" \
" s_add_u32 m0, s62, 32768\n" \
" s_mov_b32 s54, 0\n" \
" buffer_load_dwordx4 v2, s[64:67], s54 offen lds\n" \
" s_add_u32 m0, s62, 33792\n" \
" s_mov_b32 s54, 32768\n" \
" buffer_load_dwordx4 v2, s[64:67], s54 offen lds\n" \
" s_add_u32 m0, s62, 34816\n" \
" s_mov_b32 s54, 65536\n" \
" buffer_load_dwordx4 v2, s[64:67], s54 offen lds\n" \
" s_add_u32 m0, s62, 35840\n" \
" s_mov_b32 s54, 98304\n" \
" buffer_load_dwordx4 v2, s[64:67], s54 offen lds\n" \
" s_add_u32 m0, s62, 36864\n" \
" s_mov_b32 s54, 131072\n" \
" buffer_load_dwordx4 v2, s[64:67], s54 offen lds\n" \
" s_add_u32 m0, s62, 37888\n" \
" s_mov_b32 s54, 163840\n" \
" buffer_load_dwordx4 v2, s[64:67], s54 offen lds\n" \
" s_add_u32 m0, s62, 38912\n" \
" s_mov_b32 s54, 196608\n" \
" buffer_load_dwordx4 v2, s[64:67], s54 offen lds\n" \
" s_add_u32 m0, s62, 39936\n" \
" s_mov_b32 s54, 229376\n" \
" buffer_load_dwordx4 v2, s[64:67], s54 offen lds\n" \
" s_waitcnt vmcnt(36)\n" \
" ds_read_b64_tr_b16 v[128:129], v3 offset:0\n" \
" ds_read_b64_tr_b16 v[130:131], v3 offset:512\n" \
" ds_read_b64_tr_b16 v[136:137], v3 offset:32\n" \
" ds_read_b64_tr_b16 v[138:139], v3 offset:544\n" \
" ds_read_b64_tr_b16 v[144:145], v3 offset:64\n" \
" ds_read_b64_tr_b16 v[146:147], v3 offset:576\n" \
" ds_read_b64_tr_b16 v[152:153], v3 offset:96\n" \
" ds_read_b64_tr_b16 v[154:155], v3 offset:608\n" \
" s_waitcnt lgkmcnt(0)\n" \
" s_add_u32 m0, s62, 0\n" \
" s_mov_b32 s54, 262144\n" \
" buffer_load_dwordx4 v2, s[64:67], s54 offen lds\n" \
" s_add_u32 m0, s62, 1024\n" \
" s_mov_b32 s54, 294912\n" \
" buffer_load_dwordx4 v2, s[64:67], s54 offen lds\n" \
" s_add_u32 m0, s62, 2048\n" \
" s_mov_b32 s54, 327680\n" \
" buffer_load_dwordx4 v2, s[64:67], s54 offen lds\n" \
" s_add_u32 m0, s62, 3072\n" \
" s_mov_b32 s54, 360448\n" \
" buffer_load_dwordx4 v2, s[64:67], s54 offen lds\n" \
" s_waitcnt vmcnt(36)\n" \
" ds_read_b64_tr_b16 v[132:133], v3 offset:4096\n" \
" ds_read_b64_tr_b16 v[134:135], v3 offset:4608\n" \
" ds_read_b64_tr_b16 v[140:141], v3 offset:4128\n" \
" ds_read_b64_tr_b16 v[142:143], v3 offset:4640\n" \
" ds_read_b64_tr_b16 v[148:149], v3 offset:4160\n" \
" ds_read_b64_tr_b16 v[150:151], v3 offset:4672\n" \
" ds_read_b64_tr_b16 v[156:157], v3 offset:4192\n" \
" ds_read_b64_tr_b16 v[158:159], v3 offset:4704\n" \
" s_waitcnt lgkmcnt(0)\n" \
" s_add_u32 m0, s62, 4096\n" \
" s_mov_b32 s54, 393216\n" \
" buffer_load_dwordx4 v2, s[64:67], s54 offen lds\n" \
" s_add_u32 m0, s62, 5120\n" \
" s_mov_b32 s54, 425984\n" \
" buffer_load_dwordx4 v2, s[64:67], s54 offen lds\n" \
" s_add_u32 m0, s62, 6144\n" \
" s_mov_b32 s54, 458752\n" \
" buffer_load_dwordx4 v2, s[64:67], s54 offen lds\n" \
" s_add_u32 m0, s62, 7168\n" \
" s_mov_b32 s54, 491520\n" \
" buffer_load_dwordx4 v2, s[64:67], s54 offen lds\n" \
" s_waitcnt vmcnt(36)\n" \
" ds_read_b64_tr_b16 v[160:161], v3 offset:8192\n" \
" ds_read_b64_tr_b16 v[162:163], v3 offset:8704\n" \
" ds_read_b64_tr_b16 v[168:169], v3 offset:8224\n" \
" ds_read_b64_tr_b16 v[170:171], v3 offset:8736\n" \
" ds_read_b64_tr_b16 v[176:177], v3 offset:8256\n" \
" ds_read_b64_tr_b16 v[178:179], v3 offset:8768\n" \
" ds_read_b64_tr_b16 v[184:185], v3 offset:8288\n" \
" ds_read_b64_tr_b16 v[186:187], v3 offset:8800\n" \
" s_waitcnt lgkmcnt(0)\n" \
" s_add_u32 m0, s62, 8192\n" \
" s_mov_b32 s54, 524288\n" \
" buffer_load_dwordx4 v2, s[64:67], s54 offen lds\n" \
" s_add_u32 m0, s62, 9216\n" \
" s_mov_b32 s54, 557056\n" \
" buffer_load_dwordx4 v2, s[64:67], s54 offen lds\n" \
" s_add_u32 m0, s62, 10240\n" \
" s_mov_b32 s54, 589824\n" \
" buffer_load_dwordx4 v2, s[64:67], s54 offen lds\n" \
" s_add_u32 m0, s62, 11264\n" \
" s_mov_b32 s54, 622592\n" \
" buffer_load_dwordx4 v2, s[64:67], s54 offen lds\n" \
" s_waitcnt vmcnt(36)\n" \
" ds_read_b64_tr_b16 v[164:165], v3 offset:12288\n" \
" ds_read_b64_tr_b16 v[166:167], v3 offset:12800\n" \
" ds_read_b64_tr_b16 v[172:173], v3 offset:12320\n" \
" ds_read_b64_tr_b16 v[174:175], v3 offset:12832\n" \
" ds_read_b64_tr_b16 v[180:181], v3 offset:12352\n" \
" ds_read_b64_tr_b16 v[182:183], v3 offset:12864\n" \
" ds_read_b64_tr_b16 v[188:189], v3 offset:12384\n" \
" ds_read_b64_tr_b16 v[190:191], v3 offset:12896\n" \
" s_waitcnt lgkmcnt(0)\n" \
" s_add_u32 m0, s62, 12288\n" \
" s_mov_b32 s54, 655360\n" \
" buffer_load_dwordx4 v2, s[64:67], s54 offen lds\n" \
" s_add_u32 m0, s62, 13312\n" \
" s_mov_b32 s54, 688128\n" \
" buffer_load_dwordx4 v2, s[64:67], s54 offen lds\n" \
" s_add_u32 m0, s62, 14336\n" \
" s_mov_b32 s54, 720896\n" \
" buffer_load_dwordx4 v2, s[64:67], s54 offen lds\n" \
" s_add_u32 m0, s62, 15360\n" \
" s_mov_b32 s54, 753664\n" \
" buffer_load_dwordx4 v2, s[64:67], s54 offen lds\n" \
" s_waitcnt vmcnt(36)\n" \
" ds_read_b64_tr_b16 v[192:193], v3 offset:16384\n" \
" ds_read_b64_tr_b16 v[194:195], v3 offset:16896\n" \
" ds_read_b64_tr_b16 v[200:201], v3 offset:16416\n" \
" ds_read_b64_tr_b16 v[202:203], v3 offset:16928\n" \
" ds_read_b64_tr_b16 v[208:209], v3 offset:16448\n" \
" ds_read_b64_tr_b16 v[210:211], v3 offset:16960\n" \
" ds_read_b64_tr_b16 v[216:217], v3 offset:16480\n" \
" ds_read_b64_tr_b16 v[218:219], v3 offset:16992\n" \
" s_waitcnt lgkmcnt(0)\n" \
" s_add_u32 m0, s62, 16384\n" \
" s_mov_b32 s54, 786432\n" \
" buffer_load_dwordx4 v2, s[64:67], s54 offen lds\n" \
" s_add_u32 m0, s62, 17408\n" \
" s_mov_b32 s54, 819200\n" \
" buffer_load_dwordx4 v2, s[64:67], s54 offen lds\n" \
" s_add_u32 m0, s62, 18432\n" \
" s_mov_b32 s54, 851968\n" \
" buffer_load_dwordx4 v2, s[64:67], s54 offen lds\n" \
" s_add_u32 m0, s62, 19456\n" \
" s_mov_b32 s54, 884736\n" \
" buffer_load_dwordx4 v2, s[64:67], s54 offen lds\n" \
" s_waitcnt vmcnt(36)\n" \
" ds_read_b64_tr_b16 v[196:197], v3 offset:20480\n" \
" ds_read_b64_tr_b16 v[198:199], v3 offset:20992\n" \
" ds_read_b64_tr_b16 v[204:205], v3 offset:20512\n" \
" ds_read_b64_tr_b16 v[206:207], v3 offset:21024\n" \
" ds_read_b64_tr_b16 v[212:213], v3 offset:20544\n" \
" ds_read_b64_tr_b16 v[214:215], v3 offset:21056\n" \
" ds_read_b64_tr_b16 v[220:221], v3 offset:20576\n" \
" ds_read_b64_tr_b16 v[222:223], v3 offset:21088\n" \
" s_waitcnt lgkmcnt(0)\n" \
" s_add_u32 m0, s62, 20480\n" \
" s_mov_b32 s54, 917504\n" \
" buffer_load_dwordx4 v2, s[64:67], s54 offen lds\n" \
" s_add_u32 m0, s62, 21504\n" \
" s_mov_b32 s54, 950272\n" \
" buffer_load_dwordx4 v2, s[64:67], s54 offen lds\n" \
" s_add_u32 m0, s62, 22528\n" \
" s_mov_b32 s54, 983040\n" \
" buffer_load_dwordx4 v2, s[64:67], s54 offen lds\n" \
" s_add_u32 m0, s62, 23552\n" \
" s_mov_b32 s54, 1015808\n" \
" buffer_load_dwordx4 v2, s[64:67], s54 offen lds\n" \
" s_waitcnt vmcnt(36)\n" \
" ds_read_b64_tr_b16 v[224:225], v3 offset:24576\n" \
" ds_read_b64_tr_b16 v[226:227], v3 offset:25088\n" \
" ds_read_b64_tr_b16 v[232:233], v3 offset:24608\n" \
" ds_read_b64_tr_b16 v[234:235], v3 offset:25120\n" \
" ds_read_b64_tr_b16 v[240:241], v3 offset:24640\n" \
" ds_read_b64_tr_b16 v[242:243], v3 offset:25152\n" \
" ds_read_b64_tr_b16 v[248:249], v3 offset:24672\n" \
" ds_read_b64_tr_b16 v[250:251], v3 offset:25184\n" \
" s_waitcnt lgkmcnt(0)\n" \
" s_add_u32 m0, s62, 24576\n" \
" s_mov_b32 s54, 1048576\n" \
" buffer_load_dwordx4 v2, s[64:67], s54 offen lds\n" \
" s_add_u32 m0, s62, 25600\n" \
" s_mov_b32 s54, 1081344\n" \
" buffer_load_dwordx4 v2, s[64:67], s54 offen lds\n" \
" s_add_u32 m0, s62, 26624\n" \
" s_mov_b32 s54, 1114112\n" \
" buffer_load_dwordx4 v2, s[64:67], s54 offen lds\n" \
" s_add_u32 m0, s62, 27648\n" \
" s_mov_b32 s54, 1146880\n" \
" buffer_load_dwordx4 v2, s[64:67], s54 offen lds\n" \
" s_waitcnt vmcnt(36)\n" \
" ds_read_b64_tr_b16 v[228:229], v3 offset:28672\n" \
" ds_read_b64_tr_b16 v[230:231], v3 offset:29184\n" \
" ds_read_b64_tr_b16 v[236:237], v3 offset:28704\n" \
" ds_read_b64_tr_b16 v[238:239], v3 offset:29216\n" \
" ds_read_b64_tr_b16 v[244:245], v3 offset:28736\n" \
" ds_read_b64_tr_b16 v[246:247], v3 offset:29248\n" \
" ds_read_b64_tr_b16 v[252:253], v3 offset:28768\n" \
" ds_read_b64_tr_b16 v[254:255], v3 offset:29280\n" \
" s_waitcnt lgkmcnt(0)\n" \
" s_add_u32 m0, s62, 28672\n" \
" s_mov_b32 s54, 1179648\n" \
" buffer_load_dwordx4 v2, s[64:67], s54 offen lds\n" \
" s_add_u32 m0, s62, 29696\n" \
" s_mov_b32 s54, 1212416\n" \
" buffer_load_dwordx4 v2, s[64:67], s54 offen lds\n" \
" s_add_u32 m0, s62, 30720\n" \
" s_mov_b32 s54, 1245184\n" \
" buffer_load_dwordx4 v2, s[64:67], s54 offen lds\n" \
" s_add_u32 m0, s62, 31744\n" \
" s_mov_b32 s54, 1277952\n" \
" buffer_load_dwordx4 v2, s[64:67], s54 offen lds\n" \
" s_waitcnt vmcnt(36)\n" \
" ds_read_b64_tr_b16 a[0:1], v4 offset:32768\n" \
" ds_read_b64_tr_b16 a[2:3], v4 offset:32896\n" \
" ds_read_b64_tr_b16 a[4:5], v4 offset:34816\n" \
" ds_read_b64_tr_b16 a[6:7], v4 offset:34944\n" \
" ds_read_b64_tr_b16 a[8:9], v4 offset:32800\n" \
" ds_read_b64_tr_b16 a[10:11], v4 offset:32928\n" \
" ds_read_b64_tr_b16 a[12:13], v4 offset:34848\n" \
" ds_read_b64_tr_b16 a[14:15], v4 offset:34976\n" \
" s_waitcnt lgkmcnt(0)\n" \
" s_add_u32 m0, s62, 32768\n" \
" s_mov_b32 s54, 1310720\n" \
" buffer_load_dwordx4 v2, s[64:67], s54 offen lds\n" \
" s_add_u32 m0, s62, 33792\n" \
" s_mov_b32 s54, 1343488\n" \
" buffer_load_dwordx4 v2, s[64:67], s54 offen lds\n" \
" s_add_u32 m0, s62, 34816\n" \
" s_mov_b32 s54, 1376256\n" \
" buffer_load_dwordx4 v2, s[64:67], s54 offen lds\n" \
" s_add_u32 m0, s62, 35840\n" \
" s_mov_b32 s54, 1409024\n" \
" buffer_load_dwordx4 v2, s[64:67], s54 offen lds\n" \
" s_waitcnt vmcnt(36)\n" \
" ds_read_b64_tr_b16 a[16:17], v4 offset:36864\n" \
" ds_read_b64_tr_b16 a[18:19], v4 offset:36992\n" \
" ds_read_b64_tr_b16 a[20:21], v4 offset:38912\n" \
" ds_read_b64_tr_b16 a[22:23], v4 offset:39040\n" \
" ds_read_b64_tr_b16 a[24:25], v4 offset:36896\n" \
" ds_read_b64_tr_b16 a[26:27], v4 offset:37024\n" \
" ds_read_b64_tr_b16 a[28:29], v4 offset:38944\n" \
" ds_read_b64_tr_b16 a[30:31], v4 offset:39072\n" \
" s_waitcnt lgkmcnt(0)\n" \
" s_add_u32 m0, s62, 36864\n" \
" s_mov_b32 s54, 1441792\n" \
" buffer_load_dwordx4 v2, s[64:67], s54 offen lds\n" \
" s_add_u32 m0, s62, 37888\n" \
" s_mov_b32 s54, 1474560\n" \
" buffer_load_dwordx4 v2, s[64:67], s54 offen lds\n" \
" s_add_u32 m0, s62, 38912\n" \
" s_mov_b32 s54, 1507328\n" \
" buffer_load_dwordx4 v2, s[64:67], s54 offen lds\n" \
" s_add_u32 m0, s62, 39936\n" \
" s_mov_b32 s54, 1540096\n" \
" buffer_load_dwordx4 v2, s[64:67], s54 offen lds\n" \
" s_waitcnt vmcnt(36)\n" \
" ds_read_b64_tr_b16 a[32:33], v4 offset:0\n" \
" ds_read_b64_tr_b16 a[34:35], v4 offset:128\n" \
" ds_read_b64_tr_b16 a[36:37], v4 offset:2048\n" \
" ds_read_b64_tr_b16 a[38:39], v4 offset:2176\n" \
" ds_read_b64_tr_b16 a[40:41], v4 offset:32\n" \
" ds_read_b64_tr_b16 a[42:43], v4 offset:160\n" \
" ds_read_b64_tr_b16 a[44:45], v4 offset:2080\n" \
" ds_read_b64_tr_b16 a[46:47], v4 offset:2208\n" \
" s_waitcnt lgkmcnt(0)\n" \
" s_add_u32 m0, s62, 0\n" \
" s_mov_b32 s54, 1572864\n" \
" buffer_load_dwordx4 v2, s[64:67], s54 offen lds\n" \
" s_add_u32 m0, s62, 1024\n" \
" s_mov_b32 s54, 1605632\n" \
" buffer_load_dwordx4 v2, s[64:67], s54 offen lds\n" \
" s_add_u32 m0, s62, 2048\n" \
" s_mov_b32 s54, 1638400\n" \
" buffer_load_dwordx4 v2, s[64:67], s54 offen lds\n" \
" s_add_u32 m0, s62, 3072\n" \
" s_mov_b32 s54, 1671168\n" \
" buffer_load_dwordx4 v2, s[64:67], s54 offen lds\n" \
" s_waitcnt vmcnt(36)\n" \
" ds_read_b64_tr_b16 a[48:49], v4 offset:4096\n" \
" ds_read_b64_tr_b16 a[50:51], v4 offset:4224\n" \
" ds_read_b64_tr_b16 a[52:53], v4 offset:6144\n" \
" ds_read_b64_tr_b16 a[54:55], v4 offset:6272\n" \
" ds_read_b64_tr_b16 a[56:57], v4 offset:4128\n" \
" ds_read_b64_tr_b16 a[58:59], v4 offset:4256\n" \
" ds_read_b64_tr_b16 a[60:61], v4 offset:6176\n" \
" ds_read_b64_tr_b16 a[62:63], v4 offset:6304\n" \
" s_waitcnt lgkmcnt(0)\n" \
" s_add_u32 m0, s62, 4096\n" \
" s_mov_b32 s54, 1703936\n" \
" buffer_load_dwordx4 v2, s[64:67], s54 offen lds\n" \
" s_add_u32 m0, s62, 5120\n" \
" s_mov_b32 s54, 1736704\n" \
" buffer_load_dwordx4 v2, s[64:67], s54 offen lds\n" \
" s_add_u32 m0, s62, 6144\n" \
" s_mov_b32 s54, 1769472\n" \
" buffer_load_dwordx4 v2, s[64:67], s54 offen lds\n" \
" s_add_u32 m0, s62, 7168\n" \
" s_mov_b32 s54, 1802240\n" \
" buffer_load_dwordx4 v2, s[64:67], s54 offen lds\n" \
" s_waitcnt vmcnt(36)\n" \
" ds_read_b64_tr_b16 a[64:65], v4 offset:8192\n" \
" ds_read_b64_tr_b16 a[66:67], v4 offset:8320\n" \
" ds_read_b64_tr_b16 a[68:69], v4 offset:10240\n" \
" ds_read_b64_tr_b16 a[70:71], v4 offset:10368\n" \
" ds_read_b64_tr_b16 a[72:73], v4 offset:8224\n" \
" ds_read_b64_tr_b16 a[74:75], v4 offset:8352\n" \
" ds_read_b64_tr_b16 a[76:77], v4 offset:10272\n" \
" ds_read_b64_tr_b16 a[78:79], v4 offset:10400\n" \
" s_waitcnt lgkmcnt(0)\n" \
" s_add_u32 m0, s62, 8192\n" \
" s_mov_b32 s54, 1835008\n" \
" buffer_load_dwordx4 v2, s[64:67], s54 offen lds\n" \
" s_add_u32 m0, s62, 9216\n" \
" s_mov_b32 s54, 1867776\n" \
" buffer_load_dwordx4 v2, s[64:67], s54 offen lds\n" \
" s_add_u32 m0, s62, 10240\n" \
" s_mov_b32 s54, 1900544\n" \
" buffer_load_dwordx4 v2, s[64:67], s54 offen lds\n" \
" s_add_u32 m0, s62, 11264\n" \
" s_mov_b32 s54, 1933312\n" \
" buffer_load_dwordx4 v2, s[64:67], s54 offen lds\n" \
" s_waitcnt vmcnt(36)\n" \
" ds_read_b64_tr_b16 a[80:81], v4 offset:12288\n" \
" ds_read_b64_tr_b16 a[82:83], v4 offset:12416\n" \
" ds_read_b64_tr_b16 a[84:85], v4 offset:14336\n" \
" ds_read_b64_tr_b16 a[86:87], v4 offset:14464\n" \
" ds_read_b64_tr_b16 a[88:89], v4 offset:12320\n" \
" ds_read_b64_tr_b16 a[90:91], v4 offset:12448\n" \
" ds_read_b64_tr_b16 a[92:93], v4 offset:14368\n" \
" ds_read_b64_tr_b16 a[94:95], v4 offset:14496\n" \
" s_waitcnt lgkmcnt(0)\n" \
" s_add_u32 m0, s62, 12288\n" \
" s_mov_b32 s54, 1966080\n" \
" buffer_load_dwordx4 v2, s[64:67], s54 offen lds\n" \
" s_add_u32 m0, s62, 13312\n" \
" s_mov_b32 s54, 1998848\n" \
" buffer_load_dwordx4 v2, s[64:67], s54 offen lds\n" \
" s_add_u32 m0, s62, 14336\n" \
" s_mov_b32 s54, 2031616\n" \
" buffer_load_dwordx4 v2, s[64:67], s54 offen lds\n" \
" s_add_u32 m0, s62, 15360\n" \
" s_mov_b32 s54, 2064384\n" \
" buffer_load_dwordx4 v2, s[64:67], s54 offen lds\n" \
" s_waitcnt vmcnt(36)\n" \
" ds_read_b64_tr_b16 a[96:97], v4 offset:16384\n" \
" ds_read_b64_tr_b16 a[98:99], v4 offset:16512\n" \
" ds_read_b64_tr_b16 a[100:101], v4 offset:18432\n" \
" ds_read_b64_tr_b16 a[102:103], v4 offset:18560\n" \
" ds_read_b64_tr_b16 a[104:105], v4 offset:16416\n" \
" ds_read_b64_tr_b16 a[106:107], v4 offset:16544\n" \
" ds_read_b64_tr_b16 a[108:109], v4 offset:18464\n" \
" ds_read_b64_tr_b16 a[110:111], v4 offset:18592\n" \
" s_waitcnt vmcnt(32)\n" \
" ds_read_b64_tr_b16 a[112:113], v4 offset:20480\n" \
" ds_read_b64_tr_b16 a[114:115], v4 offset:20608\n" \
" ds_read_b64_tr_b16 a[116:117], v4 offset:22528\n" \
" ds_read_b64_tr_b16 a[118:119], v4 offset:22656\n" \
" ds_read_b64_tr_b16 a[120:121], v4 offset:20512\n" \
" ds_read_b64_tr_b16 a[122:123], v4 offset:20640\n" \
" ds_read_b64_tr_b16 a[124:125], v4 offset:22560\n" \
" ds_read_b64_tr_b16 a[126:127], v4 offset:22688\n" \
" s_waitcnt vmcnt(28)\n" \
" ds_read_b64_tr_b16 a[128:129], v4 offset:24576\n" \
" ds_read_b64_tr_b16 a[130:131], v4 offset:24704\n" \
" ds_read_b64_tr_b16 a[132:133], v4 offset:26624\n" \
" ds_read_b64_tr_b16 a[134:135], v4 offset:26752\n" \
" ds_read_b64_tr_b16 a[136:137], v4 offset:24608\n" \
" ds_read_b64_tr_b16 a[138:139], v4 offset:24736\n" \
" ds_read_b64_tr_b16 a[140:141], v4 offset:26656\n" \
" ds_read_b64_tr_b16 a[142:143], v4 offset:26784\n" \
" s_waitcnt vmcnt(24)\n" \
" ds_read_b64_tr_b16 a[144:145], v4 offset:28672\n" \
" ds_read_b64_tr_b16 a[146:147], v4 offset:28800\n" \
" ds_read_b64_tr_b16 a[148:149], v4 offset:30720\n" \
" ds_read_b64_tr_b16 a[150:151], v4 offset:30848\n" \
" ds_read_b64_tr_b16 a[152:153], v4 offset:28704\n" \
" ds_read_b64_tr_b16 a[154:155], v4 offset:28832\n" \
" ds_read_b64_tr_b16 a[156:157], v4 offset:30752\n" \
" ds_read_b64_tr_b16 a[158:159], v4 offset:30880\n" \
" s_waitcnt vmcnt(20)\n" \
" ds_read_b64_tr_b16 a[160:161], v4 offset:32768\n" \
" ds_read_b64_tr_b16 a[162:163], v4 offset:32896\n" \
" ds_read_b64_tr_b16 a[164:165], v4 offset:34816\n" \
" ds_read_b64_tr_b16 a[166:167], v4 offset:34944\n" \
" ds_read_b64_tr_b16 a[168:169], v4 offset:32800\n" \
" ds_read_b64_tr_b16 a[170:171], v4 offset:32928\n" \
" ds_read_b64_tr_b16 a[172:173], v4 offset:34848\n" \
" ds_read_b64_tr_b16 a[174:175], v4 offset:34976\n" \
" s_waitcnt vmcnt(16)\n" \
" ds_read_b64_tr_b16 a[176:177], v4 offset:36864\n" \
" ds_read_b64_tr_b16 a[178:179], v4 offset:36992\n" \
" ds_read_b64_tr_b16 a[180:181], v4 offset:38912\n" \
" ds_read_b64_tr_b16 a[182:183], v4 offset:39040\n" \
" ds_read_b64_tr_b16 a[184:185], v4 offset:36896\n" \
" ds_read_b64_tr_b16 a[186:187], v4 offset:37024\n" \
" ds_read_b64_tr_b16 a[188:189], v4 offset:38944\n" \
" ds_read_b64_tr_b16 a[190:191], v4 offset:39072\n" \
" s_waitcnt vmcnt(12)\n" \
" ds_read_b64_tr_b16 a[192:193], v4 offset:0\n" \
" ds_read_b64_tr_b16 a[194:195], v4 offset:128\n" \
" ds_read_b64_tr_b16 a[196:197], v4 offset:2048\n" \
" ds_read_b64_tr_b16 a[198:199], v4 offset:2176\n" \
" ds_read_b64_tr_b16 a[200:201], v4 offset:32\n" \
" ds_read_b64_tr_b16 a[202:203], v4 offset:160\n" \
" ds_read_b64_tr_b16 a[204:205], v4 offset:2080\n" \
" ds_read_b64_tr_b16 a[206:207], v4 offset:2208\n" \
" s_waitcnt vmcnt(8)\n" \
" ds_read_b64_tr_b16 a[208:209], v4 offset:4096\n" \
" ds_read_b64_tr_b16 a[210:211], v4 offset:4224\n" \
" ds_read_b64_tr_b16 a[212:213], v4 offset:6144\n" \
" ds_read_b64_tr_b16 a[214:215], v4 offset:6272\n" \
" ds_read_b64_tr_b16 a[216:217], v4 offset:4128\n" \
" ds_read_b64_tr_b16 a[218:219], v4 offset:4256\n" \
" ds_read_b64_tr_b16 a[220:221], v4 offset:6176\n" \
" ds_read_b64_tr_b16 a[222:223], v4 offset:6304\n" \
" s_waitcnt vmcnt(4)\n" \
" ds_read_b64_tr_b16 a[224:225], v4 offset:8192\n" \
" ds_read_b64_tr_b16 a[226:227], v4 offset:8320\n" \
" ds_read_b64_tr_b16 a[228:229], v4 offset:10240\n" \
" ds_read_b64_tr_b16 a[230:231], v4 offset:10368\n" \
" ds_read_b64_tr_b16 a[232:233], v4 offset:8224\n" \
" ds_read_b64_tr_b16 a[234:235], v4 offset:8352\n" \
" ds_read_b64_tr_b16 a[236:237], v4 offset:10272\n" \
" ds_read_b64_tr_b16 a[238:239], v4 offset:10400\n" \
" s_waitcnt vmcnt(0)\n" \
" ds_read_b64_tr_b16 a[240:241], v4 offset:12288\n" \
" ds_read_b64_tr_b16 a[242:243], v4 offset:12416\n" \
" ds_read_b64_tr_b16 a[244:245], v4 offset:14336\n" \
" ds_read_b64_tr_b16 a[246:247], v4 offset:14464\n" \
" ds_read_b64_tr_b16 a[248:249], v4 offset:12320\n" \
" ds_read_b64_tr_b16 a[250:251], v4 offset:12448\n" \
" ds_read_b64_tr_b16 a[252:253], v4 offset:14368\n" \
" ds_read_b64_tr_b16 a[254:255], v4 offset:14496\n" \
" s_waitcnt vmcnt(0) lgkmcnt(0)\n" \
" s_barrier\n" \
" s_mov_b32 s49, 0\n" \
" s_mov_b32 s52, 0\n" \
" v_mov_b32 v16, 0\n" \
" v_max_u32 v20, 1, v20\n" \
" v_mov_b32 v17, 0\n" \
" v_max_u32 v21, 1, v21\n" \
" v_mov_b32 v18, 0\n" \
" v_max_u32 v22, 1, v22\n" \
" v_mov_b32 v19, 0\n" \
" v_max_u32 v23, 1, v23\n" \
" s_mov_b32 s72, 0\n" \
" s_mov_b32 s73, -1\n" \
" buffer_load_dword v30, v16, s[36:39], 0 offen sc1\n" \
" s_waitcnt vmcnt(0)\n" \
" v_cmp_eq_u32 vcc, 0, v30\n" \
" s_nop 0\n" \
" s_cbranch_vccnz Lv_p_cold\n" \
"Lv_p_ok:\n" \
" v_readfirstlane_b32 s54, v30\n" \
" s_nop 0\n" \
" s_and_b32 s54, s54, 1\n" \
" s_lshl_b32 s54, s54, 13\n" \
" v_add_u32 v5, s54, v5\n" \
" v_add_u32 v6, s54, v6\n" \
" v_add_u32 v7, s54, v7\n" \
" v_add_u32 v8, s54, v8\n" \
" v_add_u32 v9, s54, v9\n" \
" s_mov_b32 s54, 0\n" \
" buffer_store_dwordx4 v[16:19], v7, s[36:39], s54 offen sc1\n" \
" s_mov_b32 s54, 327680\n" \
" buffer_store_dwordx4 v[16:19], v7, s[36:39], s54 offen sc1\n" \
" buffer_store_dwordx4 v[16:19], v8, s[36:39], s54 offen sc1\n" \
" s_mov_b32 s54, 655360\n" \
" buffer_store_dwordx4 v[16:19], v7, s[36:39], s54 offen sc1\n" \
" buffer_store_dwordx4 v[16:19], v8, s[36:39], s54 offen sc1\n" \
" s_mov_b32 s54, 983040\n" \
" buffer_store_dwordx4 v[16:19], v7, s[36:39], s54 offen sc1\n" \
" buffer_store_dwordx4 v[16:19], v8, s[36:39], s54 offen sc1\n" \
" s_waitcnt vmcnt(0) lgkmcnt(0)\n" \
" s_barrier\n" \
" buffer_store_dwordx4 v[20:23], v8, s[36:39], 0 offen sc1\n" \
" s_memrealtime s[56:57]\n" \
" s_waitcnt lgkmcnt(0)\n" \
" s_mov_b32 s50, s56\n" \
" s_waitcnt vmcnt(0) lgkmcnt(0)\n" \
" v_lshlrev_b32 v31, 1, v0\n" \
" s_nop 7\n" \
" s_nop 7\n" \
".p2align 6\n" \
"Lv_loop:\n" \
" s_add_u32 s54, s49, 0\n" \
" s_and_b32 s54, s54, 3\n" \
" s_mul_i32 s58, s54, 327680\n" \
" s_add_u32 s54, s49, 1\n" \
" s_and_b32 s54, s54, 3\n" \
" s_mul_i32 s59, s54, 327680\n" \
" s_add_u32 s54, s49, 2\n" \
" s_and_b32 s54, s54, 3\n" \
" s_mul_i32 s60, s54, 327680\n" \
" s_add_u32 s54, s49, 3\n" \
" s_and_b32 s54, s54, 3\n" \
" s_mul_i32 s61, s54, 327680\n" \
" v_mov_b64 v[80:81], 0\n" \
" v_mov_b64 v[82:83], 0\n" \
" v_mov_b64 v[84:85], 0\n" \
" v_mov_b64 v[86:87], 0\n" \
" v_mov_b64 v[88:89], 0\n" \
" v_mov_b64 v[90:91], 0\n" \
" v_mov_b64 v[92:93], 0\n" \
" v_mov_b64 v[94:95], 0\n" \
" s_add_u32 s55, s50, s74\n" \
" v_mov_b32 v13, -1\n" \
"Lv_w:\n" \
" s_memrealtime s[56:57]\n" \
" s_waitcnt lgkmcnt(0)\n" \
" s_sub_u32 s54, s56, s55\n" \
" s_cmp_gt_u32 s54, 0xfffffc00\n" \
" s_cbranch_scc1 Lv_w\n" \
" buffer_load_dwordx4 v[64:67], v5, s[36:39], s58 offen offset:0 sc1\n" \
" s_nop 10\n" \
" buffer_load_dwordx4 v[68:71], v5, s[36:39], s58 offen offset:1024 sc1\n" \
" s_nop 10\n" \
" buffer_load_dwordx4 v[72:75], v5, s[36:39], s58 offen offset:2048 sc1\n" \
" s_nop 10\n" \
" buffer_load_dwordx4 v[76:79], v5, s[36:39], s58 offen offset:3072 sc1\n" \
" s_mov_b32 s51, s50\n" \
"Lv_x:\n" \
" s_waitcnt vmcnt(3)\n" \
" v_smfmac_f32_16x16x64_bf16 v[80:83], v[64:67], v[128:135], v12\n" \
" v_min3_u32 v13, v13, v64, v66\n" \
" v_smfmac_f32_16x16x64_bf16 v[84:87], v[64:67], v[136:143], v12\n" \
" v_smfmac_f32_16x16x64_bf16 v[88:91], v[64:67], v[144:151], v12\n" \
" v_smfmac_f32_16x16x64_bf16 v[92:95], v[64:67], v[152:159], v12\n" \
" s_waitcnt vmcnt(2)\n" \
" v_smfmac_f32_16x16x64_bf16 v[80:83], v[68:71], v[160:167], v12\n" \
" v_min3_u32 v13, v13, v68, v70\n" \
" v_smfmac_f32_16x16x64_bf16 v[84:87], v[68:71], v[168:175], v12\n" \
" v_smfmac_f32_16x16x64_bf16 v[88:91], v[68:71], v[176:183], v12\n" \
" v_smfmac_f32_16x16x64_bf16 v[92:95], v[68:71], v[184:191], v12\n" \
" s_waitcnt vmcnt(1)\n" \
" v_smfmac_f32_16x16x64_bf16 v[80:83], v[72:75], v[192:199], v12\n" \
" v_min3_u32 v13, v13, v72, v74\n" \
" v_smfmac_f32_16x16x64_bf16 v[84:87], v[72:75], v[200:207], v12\n" \
" v_smfmac_f32_16x16x64_bf16 v[88:91], v[72:75], v[208:215], v12\n" \
" v_smfmac_f32_16x16x64_bf16 v[92:95], v[72:75], v[216:223], v12\n" \
" s_waitcnt vmcnt(0)\n" \
" v_smfmac_f32_16x16x64_bf16 v[80:83], v[76:79], v[224:231], v12\n" \
" v_min3_u32 v13, v13, v76, v78\n" \
" v_smfmac_f32_16x16x64_bf16 v[84:87], v[76:79], v[232:239], v12\n" \
" v_cmp_eq_u32 vcc, 0, v13\n" \
" v_smfmac_f32_16x16x64_bf16 v[88:91], v[76:79], v[240:247], v12\n" \
" v_smfmac_f32_16x16x64_bf16 v[92:95], v[76:79], v[248:255], v12\n" \
" s_cbranch_vccnz Lv_mretry\n" \
"Lv_x2:\n" \
" v_pk_add_f32 v[80:81], v[80:81], v[82:83]\n" \
" v_pk_add_f32 v[84:85], v[84:85], v[86:87]\n" \
" s_nop 0\n" \
" v_pk_add_f32 v[88:89], v[88:89], v[90:91]\n" \
" s_nop 2\n" \
" v_pk_add_f32 v[92:93], v[92:93], v[94:95]\n" \
" ds_write2_b64 v10, v[80:81], v[84:85] offset0:0 offset1:64\n" \
" ds_write2_b64 v10, v[88:89], v[92:93] offset0:128 offset1:192\n" \
" s_waitcnt lgkmcnt(0)\n" \
" s_barrier\n" \
" ds_read_b128 v[64:67], v11 offset:0\n" \
" ds_read_b128 v[68:71], v11 offset:2048\n" \
" ds_read_b128 v[72:75], v11 offset:4096\n" \
" ds_read_b128 v[76:79], v11 offset:6144\n" \
" s_waitcnt lgkmcnt(2)\n" \
" v_pk_add_f32 v[64:65], v[64:65], v[68:69]\n" \
" v_pk_add_f32 v[66:67], v[66:67], v[70:71]\n" \
" s_waitcnt lgkmcnt(0)\n" \
" v_pk_add_f32 v[72:73], v[72:73], v[76:77]\n" \
" v_pk_add_f32 v[74:75], v[74:75], v[78:79]\n" \
" v_pk_add_f32 v[64:65], v[64:65], v[72:73]\n" \
" v_pk_add_f32 v[66:67], v[66:67], v[74:75]\n" \
" v_cvt_pk_bf16_f32 v68, v64, v65\n" \
" v_cvt_pk_bf16_f32 v69, v66, v67\n" \
" v_cvt_pk_bf16_f32 v71, v66, v67\n" \
" v_max_u32 v70, 1, v68\n" \
" v_max_u32 v68, 1, v68\n" \
" v_permlane32_swap_b32 v69, v71\n" \
" s_nop 0\n" \
" v_permlane32_swap_b32 v68, v70\n" \
" buffer_store_dwordx4 v[68:71], v7, s[36:39], s58 offen sc1\n" \
" s_cmp_eq_u32 s49, 63\n" \
" s_nop 0\n" \
" s_cbranch_scc1 Lv_end\n" \
" buffer_store_dwordx4 v[16:19], v7, s[36:39], s60 offen sc1\n" \
" buffer_store_dwordx4 v[16:19], v8, s[36:39], s61 offen sc1\n" \
" v_mov_b64 v[80:81], 0\n" \
" v_mov_b64 v[82:83], 0\n" \
" v_mov_b64 v[84:85], 0\n" \
" v_mov_b64 v[86:87], 0\n" \
" v_mov_b64 v[88:89], 0\n" \
" v_mov_b64 v[90:91], 0\n" \
" v_mov_b64 v[92:93], 0\n" \
" v_mov_b64 v[94:95], 0\n" \
" s_add_u32 s55, s50, s46\n" \
"Lv_wh:\n" \
" s_memrealtime s[56:57]\n" \
" s_waitcnt lgkmcnt(0)\n" \
" s_sub_u32 s54, s56, s55\n" \
" s_cmp_gt_u32 s54, 0xfffffc00\n" \
" s_cbranch_scc1 Lv_wh\n" \
" buffer_load_dword v30, v9, s[36:39], s58 offen sc1\n" \
" s_waitcnt vmcnt(0)\n" \
" v_cmp_eq_u32 vcc, 0, v30\n" \
" s_nop 0\n" \
" s_cbranch_vccnz Lv_h_cold\n" \
"Lv_h_ok:\n" \
" v_subrev_u32 v30, s51, v30\n" \
" s_nop 1\n" \
" v_max_i32_dpp v30, v30, v30 row_ror:8 row_mask:0xf bank_mask:0xf\n" \
" s_nop 1\n" \
" v_max_i32_dpp v30, v30, v30 row_ror:4 row_mask:0xf bank_mask:0xf\n" \
" s_nop 1\n" \
" v_max_i32_dpp v30, v30, v30 row_ror:2 row_mask:0xf bank_mask:0xf\n" \
" s_nop 1\n" \
" v_max_i32_dpp v30, v30, v30 row_ror:1 row_mask:0xf bank_mask:0xf\n" \
" s_nop 0\n" \
" v_readlane_b32 s54, v30, 0\n" \
" v_readlane_b32 s55, v30, 16\n" \
" s_nop 0\n" \
" s_max_i32 s55, s55, s54\n" \
" s_add_u32 s55, s55, s51\n" \
" s_add_u32 s50, s55, s45\n" \
" s_add_u32 s55, s55, s44\n" \
" s_add_u32 s63, s58, 4096\n" \
" v_mov_b32 v13, -1\n" \
"Lv_wo:\n" \
" s_memrealtime s[56:57]\n" \
" s_waitcnt lgkmcnt(0)\n" \
" s_sub_u32 s54, s56, s55\n" \
" s_cmp_gt_u32 s54, 0xfffffc00\n" \
" s_cbranch_scc1 Lv_wo\n" \
" buffer_load_dwordx4 v[32:35], v6, s[36:39], s58 offen offset:0 sc1\n" \
" s_nop 10\n" \
" buffer_load_dwordx4 v[36:39], v6, s[36:39], s58 offen offset:1024 sc1\n" \
" s_nop 10\n" \
" buffer_load_dwordx4 v[40:43], v6, s[36:39], s58 offen offset:2048 sc1\n" \
" s_nop 10\n" \
" buffer_load_dwordx4 v[44:47], v6, s[36:39], s58 offen offset:3072 sc1\n" \
" s_nop 10\n" \
" buffer_load_dwordx4 v[48:51], v6, s[36:39], s63 offen offset:0 sc1\n" \
" s_nop 10\n" \
" buffer_load_dwordx4 v[52:55], v6, s[36:39], s63 offen offset:1024 sc1\n" \
" s_nop 10\n" \
" buffer_load_dwordx4 v[56:59], v6, s[36:39], s63 offen offset:2048 sc1\n" \
" s_nop 10\n" \
" buffer_load_dwordx4 v[60:63], v6, s[36:39], s63 offen offset:3072 sc1\n" \
"Lv_y:\n" \
" s_waitcnt vmcnt(7)\n" \
" v_smfmac_f32_16x16x64_bf16 v[80:83], v[32:35], a[0:7], v12\n" \
" v_min3_u32 v13, v13, v32, v34\n" \
" v_smfmac_f32_16x16x64_bf16 v[84:87], v[32:35], a[8:15], v12\n" \
" v_smfmac_f32_16x16x64_bf16 v[88:91], v[32:35], a[16:23], v12\n" \
" v_smfmac_f32_16x16x64_bf16 v[92:95], v[32:35], a[24:31], v12\n" \
" s_waitcnt vmcnt(6)\n" \
" v_smfmac_f32_16x16x64_bf16 v[80:83], v[36:39], a[32:39], v12\n" \
" v_min3_u32 v13, v13, v36, v38\n" \
" v_smfmac_f32_16x16x64_bf16 v[84:87], v[36:39], a[40:47], v12\n" \
" v_smfmac_f32_16x16x64_bf16 v[88:91], v[36:39], a[48:55], v12\n" \
" v_smfmac_f32_16x16x64_bf16 v[92:95], v[36:39], a[56:63], v12\n" \
" s_waitcnt vmcnt(5)\n" \
" v_smfmac_f32_16x16x64_bf16 v[80:83], v[40:43], a[64:71], v12\n" \
" v_min3_u32 v13, v13, v40, v42\n" \
" v_smfmac_f32_16x16x64_bf16 v[84:87], v[40:43], a[72:79], v12\n" \
" v_smfmac_f32_16x16x64_bf16 v[88:91], v[40:43], a[80:87], v12\n" \
" v_smfmac_f32_16x16x64_bf16 v[92:95], v[40:43], a[88:95], v12\n" \
" s_waitcnt vmcnt(4)\n" \
" v_smfmac_f32_16x16x64_bf16 v[80:83], v[44:47], a[96:103], v12\n" \
" v_min3_u32 v13, v13, v44, v46\n" \
" v_smfmac_f32_16x16x64_bf16 v[84:87], v[44:47], a[104:111], v12\n" \
" v_smfmac_f32_16x16x64_bf16 v[88:91], v[44:47], a[112:119], v12\n" \
" v_smfmac_f32_16x16x64_bf16 v[92:95], v[44:47], a[120:127], v12\n" \
" s_waitcnt vmcnt(3)\n" \
" v_smfmac_f32_16x16x64_bf16 v[80:83], v[48:51], a[128:135], v12\n" \
" v_min3_u32 v13, v13, v48, v50\n" \
" v_smfmac_f32_16x16x64_bf16 v[84:87], v[48:51], a[136:143], v12\n" \
" v_smfmac_f32_16x16x64_bf16 v[88:91], v[48:51], a[144:151], v12\n" \
" v_smfmac_f32_16x16x64_bf16 v[92:95], v[48:51], a[152:159], v12\n" \
" s_waitcnt vmcnt(2)\n" \
" v_smfmac_f32_16x16x64_bf16 v[80:83], v[52:55], a[160:167], v12\n" \
" v_min3_u32 v13, v13, v52, v54\n" \
" v_smfmac_f32_16x16x64_bf16 v[84:87], v[52:55], a[168:175], v12\n" \
" v_smfmac_f32_16x16x64_bf16 v[88:91], v[52:55], a[176:183], v12\n" \
" v_smfmac_f32_16x16x64_bf16 v[92:95], v[52:55], a[184:191], v12\n" \
" s_waitcnt vmcnt(1)\n" \
" v_smfmac_f32_16x16x64_bf16 v[80:83], v[56:59], a[192:199], v12\n" \
" v_min3_u32 v13, v13, v56, v58\n" \
" v_smfmac_f32_16x16x64_bf16 v[84:87], v[56:59], a[200:207], v12\n" \
" v_smfmac_f32_16x16x64_bf16 v[88:91], v[56:59], a[208:215], v12\n" \
" v_smfmac_f32_16x16x64_bf16 v[92:95], v[56:59], a[216:223], v12\n" \
" s_waitcnt vmcnt(0)\n" \
" v_smfmac_f32_16x16x64_bf16 v[80:83], v[60:63], a[224:231], v12\n" \
" v_min3_u32 v13, v13, v60, v62\n" \
" v_smfmac_f32_16x16x64_bf16 v[84:87], v[60:63], a[232:239], v12\n" \
" v_cmp_eq_u32 vcc, 0, v13\n" \
" v_smfmac_f32_16x16x64_bf16 v[88:91], v[60:63], a[240:247], v12\n" \
" v_smfmac_f32_16x16x64_bf16 v[92:95], v[60:63], a[248:255], v12\n" \
" s_cbranch_vccnz Lv_retry\n" \
"Lv_y2:\n" \
" v_pk_add_f32 v[80:81], v[80:81], v[82:83]\n" \
" v_pk_add_f32 v[84:85], v[84:85], v[86:87]\n" \
" s_nop 0\n" \
" v_pk_add_f32 v[88:89], v[88:89], v[90:91]\n" \
" s_nop 2\n" \
" v_pk_add_f32 v[92:93], v[92:93], v[94:95]\n" \
" v_cndmask_b32_e64 v80, v80, v88, s[72:73]\n" \
" v_cndmask_b32_e64 v81, v81, v89, s[72:73]\n" \
" v_cndmask_b32_e64 v84, v84, v92, s[72:73]\n" \
" v_cndmask_b32_e64 v85, v85, v93, s[72:73]\n" \
" s_nop 0\n" \
" v_permlane32_swap_b32 v80, v84\n" \
" v_permlane32_swap_b32 v81, v85\n" \
" v_pk_add_f32 v[80:81], v[80:81], v[84:85]\n" \
" ds_write2_b32 v15, v80, v81 offset1:32\n" \
" s_cmp_lg_u32 s48, 0\n" \
" s_waitcnt lgkmcnt(0)\n" \
" s_barrier\n" \
" s_cbranch_scc1 Lv_skip\n" \
" ds_read_b128 v[64:67], v31 offset:8192\n" \
" ds_read_b128 v[68:71], v31 offset:8208\n" \
" s_waitcnt lgkmcnt(1)\n" \
" v_pk_add_f32 v[64:65], v[64:65], v[66:67]\n" \
" s_waitcnt lgkmcnt(0)\n" \
" v_pk_add_f32 v[68:69], v[68:69], v[70:71]\n" \
" v_pk_add_f32 v[64:65], v[64:65], v[68:69]\n" \
" v_cvt_pk_bf16_f32 v72, v64, v65\n" \
" v_max_u32 v72, 1, v72\n" \
" s_nop 1\n" \
" v_mov_b32_dpp v73, v72 quad_perm:[1,1,1,1] row_mask:0xf bank_mask:0xf\n" \
" v_mov_b32_dpp v74, v72 quad_perm:[2,2,2,2] row_mask:0xf bank_mask:0xf\n" \
" v_mov_b32_dpp v75, v72 quad_perm:[3,3,3,3] row_mask:0xf bank_mask:0xf\n" \
"Lv_skip:\n" \
" buffer_store_dwordx4 v[72:75], v8, s[36:39], s59 offen sc1\n" \
" s_add_u32 s49, s49, 1\n" \
" s_branch Lv_loop\n" \
"; cold\n" \
"Lv_p_cold:\n" \
" s_nop 7\n" \
" s_nop 7\n" \
" s_waitcnt vmcnt(0) lgkmcnt(0)\n" \
" s_mov_b32 s53, 0\n" \
"Lv_p_again:\n" \
" s_cmp_lg_u32 s52, 0\n" \
" s_cbranch_scc1 Lv_p_ok\n" \
" s_sleep 8\n" \
" buffer_load_dword v30, v16, s[36:39], 0 offen sc1\n" \
" s_waitcnt vmcnt(0)\n" \
" v_cmp_eq_u32 vcc, 0, v30\n" \
" s_nop 4\n" \
" s_cbranch_vccz Lv_p_ok\n" \
" s_add_u32 s53, s53, 1\n" \
" s_cmp_lt_u32 s53, 32768\n" \
" s_cbranch_scc1 Lv_p_again\n" \
" s_mov_b32 s52, 1\n" \
" v_mov_b32 v31, 256\n" \
" v_mov_b32 v32, 1\n" \
" buffer_store_dword v32, v31, s[36:39], 0 offen sc1\n" \
" s_waitcnt vmcnt(0) lgkmcnt(0)\n" \
" s_branch Lv_p_ok\n" \
"Lv_h_cold:\n" \
" s_nop 7\n" \
" s_nop 7\n" \
" s_waitcnt vmcnt(0) lgkmcnt(0)\n" \
" s_mov_b32 s53, 0\n" \
"Lv_h_again:\n" \
" s_cmp_lg_u32 s52, 0\n" \
" s_cbranch_scc1 Lv_h_ok\n" \
" s_sleep 2\n" \
" buffer_load_dword v30, v9, s[36:39], s58 offen sc1\n" \
" s_waitcnt vmcnt(0)\n" \
" v_cmp_eq_u32 vcc, 0, v30\n" \
" s_nop 4\n" \
" s_cbranch_vccz Lv_h_ok\n" \
" s_add_u32 s53, s53, 1\n" \
" s_cmp_lt_u32 s53, 32768\n" \
" s_cbranch_scc1 Lv_h_again\n" \
" s_mov_b32 s52, 1\n" \
" v_mov_b32 v13, 256\n" \
" v_mov_b32 v14, 1\n" \
" buffer_store_dword v14, v13, s[36:39], 0 offen sc1\n" \
" s_waitcnt vmcnt(0) lgkmcnt(0)\n" \
" s_branch Lv_h_ok\n" \
"Lv_retry:\n" \
" s_nop 7\n" \
" s_nop 7\n" \
" s_waitcnt vmcnt(0) lgkmcnt(0)\n" \
" s_cmp_lg_u32 s52, 0\n" \
" s_cbranch_scc1 Lv_y2\n" \
" buffer_load_dwordx4 v[32:35], v6, s[36:39], s58 offen offset:0 sc1\n" \
" buffer_load_dwordx4 v[36:39], v6, s[36:39], s58 offen offset:1024 sc1\n" \
" buffer_load_dwordx4 v[40:43], v6, s[36:39], s58 offen offset:2048 sc1\n" \
" buffer_load_dwordx4 v[44:47], v6, s[36:39], s58 offen offset:3072 sc1\n" \
" buffer_load_dwordx4 v[48:51], v6, s[36:39], s63 offen offset:0 sc1\n" \
" buffer_load_dwordx4 v[52:55], v6, s[36:39], s63 offen offset:1024 sc1\n" \
" buffer_load_dwordx4 v[56:59], v6, s[36:39], s63 offen offset:2048 sc1\n" \
" buffer_load_dwordx4 v[60:63], v6, s[36:39], s63 offen offset:3072 sc1\n" \
" s_waitcnt vmcnt(6)\n" \
" v_min3_u32 v13, v32, v34, v36\n" \
" s_waitcnt vmcnt(5)\n" \
" v_min3_u32 v13, v13, v38, v40\n" \
" s_waitcnt vmcnt(4)\n" \
" v_min3_u32 v13, v13, v42, v44\n" \
" s_waitcnt vmcnt(3)\n" \
" v_min3_u32 v13, v13, v46, v48\n" \
" s_waitcnt vmcnt(2)\n" \
" v_min3_u32 v13, v13, v50, v52\n" \
" s_waitcnt vmcnt(1)\n" \
" v_min3_u32 v13, v13, v54, v56\n" \
" s_waitcnt vmcnt(0)\n" \
" v_min3_u32 v13, v13, v58, v60\n" \
" v_min3_u32 v13, v13, v62, v62\n" \
" v_cmp_eq_u32 vcc, 0, v13\n" \
" s_nop 0\n" \
" s_cbranch_vccnz Lv_o_cold\n" \
"Lv_o_ok:\n" \
" v_mov_b64 v[80:81], 0\n" \
" v_mov_b64 v[82:83], 0\n" \
" v_mov_b64 v[84:85], 0\n" \
" v_mov_b64 v[86:87], 0\n" \
" v_mov_b64 v[88:89], 0\n" \
" v_mov_b64 v[90:91], 0\n" \
" v_mov_b64 v[92:93], 0\n" \
" v_mov_b64 v[94:95], 0\n" \
" v_mov_b32 v13, -1\n" \
" s_nop 7\n" \
" s_nop 7\n" \
" s_branch Lv_y\n" \
"Lv_mretry:\n" \
" s_nop 7\n" \
" s_nop 7\n" \
" s_waitcnt vmcnt(0) lgkmcnt(0)\n" \
" s_cmp_lg_u32 s52, 0\n" \
" s_cbranch_scc1 Lv_x2\n" \
" buffer_load_dwordx4 v[64:67], v5, s[36:39], s58 offen offset:0 sc1\n" \
" buffer_load_dwordx4 v[68:71], v5, s[36:39], s58 offen offset:1024 sc1\n" \
" buffer_load_dwordx4 v[72:75], v5, s[36:39], s58 offen offset:2048 sc1\n" \
" buffer_load_dwordx4 v[76:79], v5, s[36:39], s58 offen offset:3072 sc1\n" \
" s_waitcnt vmcnt(2)\n" \
" v_min3_u32 v13, v64, v66, v68\n" \
" s_waitcnt vmcnt(1)\n" \
" v_min3_u32 v13, v13, v70, v72\n" \
" s_waitcnt vmcnt(0)\n" \
" v_min3_u32 v13, v13, v74, v76\n" \
" v_min3_u32 v13, v13, v78, v78\n" \
" v_cmp_eq_u32 vcc, 0, v13\n" \
" s_nop 0\n" \
" s_cbranch_vccnz Lv_m_cold\n" \
"Lv_m_ok:\n" \
" v_mov_b64 v[80:81], 0\n" \
" v_mov_b64 v[82:83], 0\n" \
" v_mov_b64 v[84:85], 0\n" \
" v_mov_b64 v[86:87], 0\n" \
" v_mov_b64 v[88:89], 0\n" \
" v_mov_b64 v[90:91], 0\n" \
" v_mov_b64 v[92:93], 0\n" \
" v_mov_b64 v[94:95], 0\n" \
" v_mov_b32 v13, -1\n" \
" s_nop 7\n" \
" s_nop 7\n" \
" s_branch Lv_x\n" \
"Lv_o_cold:\n" \
" s_nop 7\n" \
" s_nop 7\n" \
" s_waitcnt vmcnt(0) lgkmcnt(0)\n" \
" s_mov_b32 s53, 0\n" \
"Lv_o_again:\n" \
" s_cmp_lg_u32 s52, 0\n" \
" s_cbranch_scc1 Lv_o_ok\n" \
" s_sleep 1\n" \
" buffer_load_dwordx4 v[32:35], v6, s[36:39], s58 offen offset:0 sc1\n" \
" buffer_load_dwordx4 v[36:39], v6, s[36:39], s58 offen offset:1024 sc1\n" \
" buffer_load_dwordx4 v[40:43], v6, s[36:39], s58 offen offset:2048 sc1\n" \
" buffer_load_dwordx4 v[44:47], v6, s[36:39], s58 offen offset:3072 sc1\n" \
" buffer_load_dwordx4 v[48:51], v6, s[36:39], s63 offen offset:0 sc1\n" \
" buffer_load_dwordx4 v[52:55], v6, s[36:39], s63 offen offset:1024 sc1\n" \
" buffer_load_dwordx4 v[56:59], v6, s[36:39], s63 offen offset:2048 sc1\n" \
" buffer_load_dwordx4 v[60:63], v6, s[36:39], s63 offen offset:3072 sc1\n" \
" s_waitcnt vmcnt(6)\n" \
" v_min3_u32 v13, v32, v34, v36\n" \
" s_waitcnt vmcnt(5)\n" \
" v_min3_u32 v13, v13, v38, v40\n" \
" s_waitcnt vmcnt(4)\n" \
" v_min3_u32 v13, v13, v42, v44\n" \
" s_waitcnt vmcnt(3)\n" \
" v_min3_u32 v13, v13, v46, v48\n" \
" s_waitcnt vmcnt(2)\n" \
" v_min3_u32 v13, v13, v50, v52\n" \
" s_waitcnt vmcnt(1)\n" \
" v_min3_u32 v13, v13, v54, v56\n" \
" s_waitcnt vmcnt(0)\n" \
" v_min3_u32 v13, v13, v58, v60\n" \
" v_min3_u32 v13, v13, v62, v62\n" \
" v_cmp_eq_u32 vcc, 0, v13\n" \
" s_nop 4\n" \
" s_cbranch_vccz Lv_o_ok\n" \
" s_add_u32 s53, s53, 1\n" \
" s_cmp_lt_u32 s53, 32768\n" \
" s_cbranch_scc1 Lv_o_again\n" \
" s_mov_b32 s52, 1\n" \
" v_mov_b32 v13, 256\n" \
" v_mov_b32 v14, 1\n" \
" buffer_store_dword v14, v13, s[36:39], 0 offen sc1\n" \
" s_waitcnt vmcnt(0) lgkmcnt(0)\n" \
" s_branch Lv_o_ok\n" \
"Lv_m_cold:\n" \
" s_nop 7\n" \
" s_nop 7\n" \
" s_waitcnt vmcnt(0) lgkmcnt(0)\n" \
" s_mov_b32 s53, 0\n" \
"Lv_m_again:\n" \
" s_cmp_lg_u32 s52, 0\n" \
" s_cbranch_scc1 Lv_m_ok\n" \
" s_sleep 2\n" \
" buffer_load_dwordx4 v[64:67], v5, s[36:39], s58 offen offset:0 sc1\n" \
" buffer_load_dwordx4 v[68:71], v5, s[36:39], s58 offen offset:1024 sc1\n" \
" buffer_load_dwordx4 v[72:75], v5, s[36:39], s58 offen offset:2048 sc1\n" \
" buffer_load_dwordx4 v[76:79], v5, s[36:39], s58 offen offset:3072 sc1\n" \
" s_waitcnt vmcnt(2)\n" \
" v_min3_u32 v13, v64, v66, v68\n" \
" s_waitcnt vmcnt(1)\n" \
" v_min3_u32 v13, v13, v70, v72\n" \
" s_waitcnt vmcnt(0)\n" \
" v_min3_u32 v13, v13, v74, v76\n" \
" v_min3_u32 v13, v13, v78, v78\n" \
" v_cmp_eq_u32 vcc, 0, v13\n" \
" s_nop 4\n" \
" s_cbranch_vccz Lv_m_ok\n" \
" s_add_u32 s53, s53, 1\n" \
" s_cmp_lt_u32 s53, 32768\n" \
" s_cbranch_scc1 Lv_m_again\n" \
" s_mov_b32 s52, 1\n" \
" v_mov_b32 v13, 256\n" \
" v_mov_b32 v14, 1\n" \
" buffer_store_dword v14, v13, s[36:39], 0 offen sc1\n" \
" s_waitcnt vmcnt(0) lgkmcnt(0)\n" \
" s_branch Lv_m_ok\n" \
"Lv_end:\n" \
" s_nop 7\n" \
" s_nop 7\n" \
" s_waitcnt vmcnt(0) lgkmcnt(0)\n" \
" s_endpgm\n"
#define ASM_S64_WO \
" s_nop 7\n" \
" s_nop 7\n" \
" s_lshl_b32 s52, s48, 15\n" \
" s_lshl_b32 s53, s48, 13\n" \
" s_add_u32 s53, s53, 98304\n" \
" s_sub_u32 s54, s53, s52\n" \
" v_add_u32 v9, s54, v2\n" \
" s_add_u32 m0, s52, 0\n" \
" s_mov_b32 s54, 0\n" \
" buffer_load_dwordx4 v1, s[40:43], s54 offen lds\n" \
" s_add_u32 m0, s52, 1024\n" \
" s_mov_b32 s54, 16384\n" \
" buffer_load_dwordx4 v1, s[40:43], s54 offen lds\n" \
" s_add_u32 m0, s52, 2048\n" \
" s_mov_b32 s54, 32768\n" \
" buffer_load_dwordx4 v1, s[40:43], s54 offen lds\n" \
" s_add_u32 m0, s52, 3072\n" \
" s_mov_b32 s54, 49152\n" \
" buffer_load_dwordx4 v1, s[40:43], s54 offen lds\n" \
" s_add_u32 m0, s52, 4096\n" \
" s_mov_b32 s54, 65536\n" \
" buffer_load_dwordx4 v1, s[40:43], s54 offen lds\n" \
" s_add_u32 m0, s52, 5120\n" \
" s_mov_b32 s54, 81920\n" \
" buffer_load_dwordx4 v1, s[40:43], s54 offen lds\n" \
" s_add_u32 m0, s52, 6144\n" \
" s_mov_b32 s54, 98304\n" \
" buffer_load_dwordx4 v1, s[40:43], s54 offen lds\n" \
" s_add_u32 m0, s52, 7168\n" \
" s_mov_b32 s54, 114688\n" \
" buffer_load_dwordx4 v1, s[40:43], s54 offen lds\n" \
" s_add_u32 m0, s52, 8192\n" \
" s_mov_b32 s54, 131072\n" \
" buffer_load_dwordx4 v1, s[40:43], s54 offen lds\n" \
" s_add_u32 m0, s52, 9216\n" \
" s_mov_b32 s54, 147456\n" \
" buffer_load_dwordx4 v1, s[40:43], s54 offen lds\n" \
" s_add_u32 m0, s52, 10240\n" \
" s_mov_b32 s54, 163840\n" \
" buffer_load_dwordx4 v1, s[40:43], s54 offen lds\n" \
" s_add_u32 m0, s52, 11264\n" \
" s_mov_b32 s54, 180224\n" \
" buffer_load_dwordx4 v1, s[40:43], s54 offen lds\n" \
" s_add_u32 m0, s52, 12288\n" \
" s_mov_b32 s54, 196608\n" \
" buffer_load_dwordx4 v1, s[40:43], s54 offen lds\n" \
" s_add_u32 m0, s52, 13312\n" \
" s_mov_b32 s54, 212992\n" \
" buffer_load_dwordx4 v1, s[40:43], s54 offen lds\n" \
" s_add_u32 m0, s52, 14336\n" \
" s_mov_b32 s54, 229376\n" \
" buffer_load_dwordx4 v1, s[40:43], s54 offen lds\n" \
" s_add_u32 m0, s52, 15360\n" \
" s_mov_b32 s54, 245760\n" \
" buffer_load_dwordx4 v1, s[40:43], s54 offen lds\n" \
" s_add_u32 m0, s52, 16384\n" \
" s_mov_b32 s54, 262144\n" \
" buffer_load_dwordx4 v1, s[40:43], s54 offen lds\n" \
" s_add_u32 m0, s52, 17408\n" \
" s_mov_b32 s54, 278528\n" \
" buffer_load_dwordx4 v1, s[40:43], s54 offen lds\n" \
" s_add_u32 m0, s52, 18432\n" \
" s_mov_b32 s54, 294912\n" \
" buffer_load_dwordx4 v1, s[40:43], s54 offen lds\n" \
" s_add_u32 m0, s52, 19456\n" \
" s_mov_b32 s54, 311296\n" \
" buffer_load_dwordx4 v1, s[40:43], s54 offen lds\n" \
" s_add_u32 m0, s52, 20480\n" \
" s_mov_b32 s54, 327680\n" \
" buffer_load_dwordx4 v1, s[40:43], s54 offen lds\n" \
" s_add_u32 m0, s52, 21504\n" \
" s_mov_b32 s54, 344064\n" \
" buffer_load_dwordx4 v1, s[40:43], s54 offen lds\n" \
" s_add_u32 m0, s52, 22528\n" \
" s_mov_b32 s54, 360448\n" \
" buffer_load_dwordx4 v1, s[40:43], s54 offen lds\n" \
" s_add_u32 m0, s52, 23552\n" \
" s_mov_b32 s54, 376832\n" \
" buffer_load_dwordx4 v1, s[40:43], s54 offen lds\n" \
" s_add_u32 m0, s52, 24576\n" \
" s_mov_b32 s54, 393216\n" \
" buffer_load_dwordx4 v1, s[40:43], s54 offen lds\n" \
" s_add_u32 m0, s52, 25600\n" \
" s_mov_b32 s54, 409600\n" \
" buffer_load_dwordx4 v1, s[40:43], s54 offen lds\n" \
" s_add_u32 m0, s52, 26624\n" \
" s_mov_b32 s54, 425984\n" \
" buffer_load_dwordx4 v1, s[40:43], s54 offen lds\n" \
" s_add_u32 m0, s52, 27648\n" \
" s_mov_b32 s54, 442368\n" \
" buffer_load_dwordx4 v1, s[40:43], s54 offen lds\n" \
" s_add_u32 m0, s52, 28672\n" \
" s_mov_b32 s54, 458752\n" \
" buffer_load_dwordx4 v1, s[40:43], s54 offen lds\n" \
" s_add_u32 m0, s52, 29696\n" \
" s_mov_b32 s54, 475136\n" \
" buffer_load_dwordx4 v1, s[40:43], s54 offen lds\n" \
" s_add_u32 m0, s52, 30720\n" \
" s_mov_b32 s54, 491520\n" \
" buffer_load_dwordx4 v1, s[40:43], s54 offen lds\n" \
" s_add_u32 m0, s52, 31744\n" \
" s_mov_b32 s54, 507904\n" \
" buffer_load_dwordx4 v1, s[40:43], s54 offen lds\n" \
" s_add_u32 m0, s53, 32768\n" \
" s_mov_b32 s54, 524288\n" \
" buffer_load_dwordx4 v1, s[40:43], s54 offen lds\n" \
" s_add_u32 m0, s53, 33792\n" \
" s_mov_b32 s54, 540672\n" \
" buffer_load_dwordx4 v1, s[40:43], s54 offen lds\n" \
" s_add_u32 m0, s53, 34816\n" \
" s_mov_b32 s54, 557056\n" \
" buffer_load_dwordx4 v1, s[40:43], s54 offen lds\n" \
" s_add_u32 m0, s53, 35840\n" \
" s_mov_b32 s54, 573440\n" \
" buffer_load_dwordx4 v1, s[40:43], s54 offen lds\n" \
" s_add_u32 m0, s53, 36864\n" \
" s_mov_b32 s54, 589824\n" \
" buffer_load_dwordx4 v1, s[40:43], s54 offen lds\n" \
" s_add_u32 m0, s53, 37888\n" \
" s_mov_b32 s54, 606208\n" \
" buffer_load_dwordx4 v1, s[40:43], s54 offen lds\n" \
" s_add_u32 m0, s53, 38912\n" \
" s_mov_b32 s54, 622592\n" \
" buffer_load_dwordx4 v1, s[40:43], s54 offen lds\n" \
" s_add_u32 m0, s53, 39936\n" \
" s_mov_b32 s54, 638976\n" \
" buffer_load_dwordx4 v1, s[40:43], s54 offen lds\n" \
" s_waitcnt vmcnt(36)\n" \
" ds_read_b64_tr_b16 a[0:1], v2 offset:0\n" \
" ds_read_b64_tr_b16 a[2:3], v2 offset:2048\n" \
" ds_read_b64_tr_b16 a[128:129], v2 offset:32\n" \
" ds_read_b64_tr_b16 a[130:131], v2 offset:2080\n" \
" ds_read_b64_tr_b16 v[128:129], v2 offset:64\n" \
" ds_read_b64_tr_b16 v[130:131], v2 offset:2112\n" \
" ds_read_b64_tr_b16 v[16:17], v2 offset:96\n" \
" ds_read_b64_tr_b16 v[18:19], v2 offset:2144\n" \
" s_waitcnt lgkmcnt(0)\n" \
" s_add_u32 m0, s52, 0\n" \
" s_mov_b32 s54, 655360\n" \
" buffer_load_dwordx4 v1, s[40:43], s54 offen lds\n" \
" s_add_u32 m0, s52, 1024\n" \
" s_mov_b32 s54, 671744\n" \
" buffer_load_dwordx4 v1, s[40:43], s54 offen lds\n" \
" s_add_u32 m0, s52, 2048\n" \
" s_mov_b32 s54, 688128\n" \
" buffer_load_dwordx4 v1, s[40:43], s54 offen lds\n" \
" s_add_u32 m0, s52, 3072\n" \
" s_mov_b32 s54, 704512\n" \
" buffer_load_dwordx4 v1, s[40:43], s54 offen lds\n" \
" s_waitcnt vmcnt(36)\n" \
" ds_read_b64_tr_b16 a[4:5], v2 offset:4096\n" \
" ds_read_b64_tr_b16 a[6:7], v2 offset:6144\n" \
" ds_read_b64_tr_b16 a[132:133], v2 offset:4128\n" \
" ds_read_b64_tr_b16 a[134:135], v2 offset:6176\n" \
" ds_read_b64_tr_b16 v[132:133], v2 offset:4160\n" \
" ds_read_b64_tr_b16 v[134:135], v2 offset:6208\n" \
" ds_read_b64_tr_b16 v[20:21], v2 offset:4192\n" \
" ds_read_b64_tr_b16 v[22:23], v2 offset:6240\n" \
" s_waitcnt lgkmcnt(0)\n" \
" s_add_u32 m0, s52, 4096\n" \
" s_mov_b32 s54, 720896\n" \
" buffer_load_dwordx4 v1, s[40:43], s54 offen lds\n" \
" s_add_u32 m0, s52, 5120\n" \
" s_mov_b32 s54, 737280\n" \
" buffer_load_dwordx4 v1, s[40:43], s54 offen lds\n" \
" s_add_u32 m0, s52, 6144\n" \
" s_mov_b32 s54, 753664\n" \
" buffer_load_dwordx4 v1, s[40:43], s54 offen lds\n" \
" s_add_u32 m0, s52, 7168\n" \
" s_mov_b32 s54, 770048\n" \
" buffer_load_dwordx4 v1, s[40:43], s54 offen lds\n" \
" s_waitcnt vmcnt(36)\n" \
" ds_read_b64_tr_b16 a[8:9], v2 offset:8192\n" \
" ds_read_b64_tr_b16 a[10:11], v2 offset:10240\n" \
" ds_read_b64_tr_b16 a[136:137], v2 offset:8224\n" \
" ds_read_b64_tr_b16 a[138:139], v2 offset:10272\n" \
" ds_read_b64_tr_b16 v[136:137], v2 offset:8256\n" \
" ds_read_b64_tr_b16 v[138:139], v2 offset:10304\n" \
" ds_read_b64_tr_b16 v[24:25], v2 offset:8288\n" \
" ds_read_b64_tr_b16 v[26:27], v2 offset:10336\n" \
" s_waitcnt lgkmcnt(0)\n" \
" s_add_u32 m0, s52, 8192\n" \
" s_mov_b32 s54, 786432\n" \
" buffer_load_dwordx4 v1, s[40:43], s54 offen lds\n" \
" s_add_u32 m0, s52, 9216\n" \
" s_mov_b32 s54, 802816\n" \
" buffer_load_dwordx4 v1, s[40:43], s54 offen lds\n" \
" s_add_u32 m0, s52, 10240\n" \
" s_mov_b32 s54, 819200\n" \
" buffer_load_dwordx4 v1, s[40:43], s54 offen lds\n" \
" s_add_u32 m0, s52, 11264\n" \
" s_mov_b32 s54, 835584\n" \
" buffer_load_dwordx4 v1, s[40:43], s54 offen lds\n" \
" s_waitcnt vmcnt(36)\n" \
" ds_read_b64_tr_b16 a[12:13], v2 offset:12288\n" \
" ds_read_b64_tr_b16 a[14:15], v2 offset:14336\n" \
" ds_read_b64_tr_b16 a[140:141], v2 offset:12320\n" \
" ds_read_b64_tr_b16 a[142:143], v2 offset:14368\n" \
" ds_read_b64_tr_b16 v[140:141], v2 offset:12352\n" \
" ds_read_b64_tr_b16 v[142:143], v2 offset:14400\n" \
" ds_read_b64_tr_b16 v[28:29], v2 offset:12384\n" \
" ds_read_b64_tr_b16 v[30:31], v2 offset:14432\n" \
" s_waitcnt lgkmcnt(0)\n" \
" s_add_u32 m0, s52, 12288\n" \
" s_mov_b32 s54, 851968\n" \
" buffer_load_dwordx4 v1, s[40:43], s54 offen lds\n" \
" s_add_u32 m0, s52, 13312\n" \
" s_mov_b32 s54, 868352\n" \
" buffer_load_dwordx4 v1, s[40:43], s54 offen lds\n" \
" s_add_u32 m0, s52, 14336\n" \
" s_mov_b32 s54, 884736\n" \
" buffer_load_dwordx4 v1, s[40:43], s54 offen lds\n" \
" s_add_u32 m0, s52, 15360\n" \
" s_mov_b32 s54, 901120\n" \
" buffer_load_dwordx4 v1, s[40:43], s54 offen lds\n" \
" s_waitcnt vmcnt(36)\n" \
" ds_read_b64_tr_b16 a[16:17], v2 offset:16384\n" \
" ds_read_b64_tr_b16 a[18:19], v2 offset:18432\n" \
" ds_read_b64_tr_b16 a[144:145], v2 offset:16416\n" \
" ds_read_b64_tr_b16 a[146:147], v2 offset:18464\n" \
" ds_read_b64_tr_b16 v[144:145], v2 offset:16448\n" \
" ds_read_b64_tr_b16 v[146:147], v2 offset:18496\n" \
" ds_read_b64_tr_b16 v[32:33], v2 offset:16480\n" \
" ds_read_b64_tr_b16 v[34:35], v2 offset:18528\n" \
" s_waitcnt lgkmcnt(0)\n" \
" s_add_u32 m0, s52, 16384\n" \
" s_mov_b32 s54, 917504\n" \
" buffer_load_dwordx4 v1, s[40:43], s54 offen lds\n" \
" s_add_u32 m0, s52, 17408\n" \
" s_mov_b32 s54, 933888\n" \
" buffer_load_dwordx4 v1, s[40:43], s54 offen lds\n" \
" s_add_u32 m0, s52, 18432\n" \
" s_mov_b32 s54, 950272\n" \
" buffer_load_dwordx4 v1, s[40:43], s54 offen lds\n" \
" s_add_u32 m0, s52, 19456\n" \
" s_mov_b32 s54, 966656\n" \
" buffer_load_dwordx4 v1, s[40:43], s54 offen lds\n" \
" s_waitcnt vmcnt(36)\n" \
" ds_read_b64_tr_b16 a[20:21], v2 offset:20480\n" \
" ds_read_b64_tr_b16 a[22:23], v2 offset:22528\n" \
" ds_read_b64_tr_b16 a[148:149], v2 offset:20512\n" \
" ds_read_b64_tr_b16 a[150:151], v2 offset:22560\n" \
" ds_read_b64_tr_b16 v[148:149], v2 offset:20544\n" \
" ds_read_b64_tr_b16 v[150:151], v2 offset:22592\n" \
" ds_read_b64_tr_b16 v[36:37], v2 offset:20576\n" \
" ds_read_b64_tr_b16 v[38:39], v2 offset:22624\n" \
" s_waitcnt lgkmcnt(0)\n" \
" s_add_u32 m0, s52, 20480\n" \
" s_mov_b32 s54, 983040\n" \
" buffer_load_dwordx4 v1, s[40:43], s54 offen lds\n" \
" s_add_u32 m0, s52, 21504\n" \
" s_mov_b32 s54, 999424\n" \
" buffer_load_dwordx4 v1, s[40:43], s54 offen lds\n" \
" s_add_u32 m0, s52, 22528\n" \
" s_mov_b32 s54, 1015808\n" \
" buffer_load_dwordx4 v1, s[40:43], s54 offen lds\n" \
" s_add_u32 m0, s52, 23552\n" \
" s_mov_b32 s54, 1032192\n" \
" buffer_load_dwordx4 v1, s[40:43], s54 offen lds\n" \
" s_waitcnt vmcnt(36)\n" \
" ds_read_b64_tr_b16 a[24:25], v2 offset:24576\n" \
" ds_read_b64_tr_b16 a[26:27], v2 offset:26624\n" \
" ds_read_b64_tr_b16 a[152:153], v2 offset:24608\n" \
" ds_read_b64_tr_b16 a[154:155], v2 offset:26656\n" \
" ds_read_b64_tr_b16 v[152:153], v2 offset:24640\n" \
" ds_read_b64_tr_b16 v[154:155], v2 offset:26688\n" \
" ds_read_b64_tr_b16 v[40:41], v2 offset:24672\n" \
" ds_read_b64_tr_b16 v[42:43], v2 offset:26720\n" \
" s_waitcnt lgkmcnt(0)\n" \
" s_add_u32 m0, s52, 24576\n" \
" s_mov_b32 s54, 1048576\n" \
" buffer_load_dwordx4 v1, s[40:43], s54 offen lds\n" \
" s_add_u32 m0, s52, 25600\n" \
" s_mov_b32 s54, 1064960\n" \
" buffer_load_dwordx4 v1, s[40:43], s54 offen lds\n" \
" s_add_u32 m0, s52, 26624\n" \
" s_mov_b32 s54, 1081344\n" \
" buffer_load_dwordx4 v1, s[40:43], s54 offen lds\n" \
" s_add_u32 m0, s52, 27648\n" \
" s_mov_b32 s54, 1097728\n" \
" buffer_load_dwordx4 v1, s[40:43], s54 offen lds\n" \
" s_waitcnt vmcnt(36)\n" \
" ds_read_b64_tr_b16 a[28:29], v2 offset:28672\n" \
" ds_read_b64_tr_b16 a[30:31], v2 offset:30720\n" \
" ds_read_b64_tr_b16 a[156:157], v2 offset:28704\n" \
" ds_read_b64_tr_b16 a[158:159], v2 offset:30752\n" \
" ds_read_b64_tr_b16 v[156:157], v2 offset:28736\n" \
" ds_read_b64_tr_b16 v[158:159], v2 offset:30784\n" \
" ds_read_b64_tr_b16 v[44:45], v2 offset:28768\n" \
" ds_read_b64_tr_b16 v[46:47], v2 offset:30816\n" \
" s_waitcnt lgkmcnt(0)\n" \
" s_add_u32 m0, s52, 28672\n" \
" s_mov_b32 s54, 1114112\n" \
" buffer_load_dwordx4 v1, s[40:43], s54 offen lds\n" \
" s_add_u32 m0, s52, 29696\n" \
" s_mov_b32 s54, 1130496\n" \
" buffer_load_dwordx4 v1, s[40:43], s54 offen lds\n" \
" s_add_u32 m0, s52, 30720\n" \
" s_mov_b32 s54, 1146880\n" \
" buffer_load_dwordx4 v1, s[40:43], s54 offen lds\n" \
" s_add_u32 m0, s52, 31744\n" \
" s_mov_b32 s54, 1163264\n" \
" buffer_load_dwordx4 v1, s[40:43], s54 offen lds\n" \
" s_waitcnt vmcnt(36)\n" \
" ds_read_b64_tr_b16 a[32:33], v9 offset:32768\n" \
" ds_read_b64_tr_b16 a[34:35], v9 offset:34816\n" \
" ds_read_b64_tr_b16 a[160:161], v9 offset:32800\n" \
" ds_read_b64_tr_b16 a[162:163], v9 offset:34848\n" \
" ds_read_b64_tr_b16 v[160:161], v9 offset:32832\n" \
" ds_read_b64_tr_b16 v[162:163], v9 offset:34880\n" \
" ds_read_b64_tr_b16 v[48:49], v9 offset:32864\n" \
" ds_read_b64_tr_b16 v[50:51], v9 offset:34912\n" \
" s_waitcnt lgkmcnt(0)\n" \
" s_add_u32 m0, s53, 32768\n" \
" s_mov_b32 s54, 1179648\n" \
" buffer_load_dwordx4 v1, s[40:43], s54 offen lds\n" \
" s_add_u32 m0, s53, 33792\n" \
" s_mov_b32 s54, 1196032\n" \
" buffer_load_dwordx4 v1, s[40:43], s54 offen lds\n" \
" s_add_u32 m0, s53, 34816\n" \
" s_mov_b32 s54, 1212416\n" \
" buffer_load_dwordx4 v1, s[40:43], s54 offen lds\n" \
" s_add_u32 m0, s53, 35840\n" \
" s_mov_b32 s54, 1228800\n" \
" buffer_load_dwordx4 v1, s[40:43], s54 offen lds\n" \
" s_waitcnt vmcnt(36)\n" \
" ds_read_b64_tr_b16 a[36:37], v9 offset:36864\n" \
" ds_read_b64_tr_b16 a[38:39], v9 offset:38912\n" \
" ds_read_b64_tr_b16 a[164:165], v9 offset:36896\n" \
" ds_read_b64_tr_b16 a[166:167], v9 offset:38944\n" \
" ds_read_b64_tr_b16 v[164:165], v9 offset:36928\n" \
" ds_read_b64_tr_b16 v[166:167], v9 offset:38976\n" \
" ds_read_b64_tr_b16 v[52:53], v9 offset:36960\n" \
" ds_read_b64_tr_b16 v[54:55], v9 offset:39008\n" \
" s_waitcnt lgkmcnt(0)\n" \
" s_add_u32 m0, s53, 36864\n" \
" s_mov_b32 s54, 1245184\n" \
" buffer_load_dwordx4 v1, s[40:43], s54 offen lds\n" \
" s_add_u32 m0, s53, 37888\n" \
" s_mov_b32 s54, 1261568\n" \
" buffer_load_dwordx4 v1, s[40:43], s54 offen lds\n" \
" s_add_u32 m0, s53, 38912\n" \
" s_mov_b32 s54, 1277952\n" \
" buffer_load_dwordx4 v1, s[40:43], s54 offen lds\n" \
" s_add_u32 m0, s53, 39936\n" \
" s_mov_b32 s54, 1294336\n" \
" buffer_load_dwordx4 v1, s[40:43], s54 offen lds\n" \
" s_waitcnt vmcnt(36)\n" \
" ds_read_b64_tr_b16 a[40:41], v2 offset:0\n" \
" ds_read_b64_tr_b16 a[42:43], v2 offset:2048\n" \
" ds_read_b64_tr_b16 a[168:169], v2 offset:32\n" \
" ds_read_b64_tr_b16 a[170:171], v2 offset:2080\n" \
" ds_read_b64_tr_b16 v[168:169], v2 offset:64\n" \
" ds_read_b64_tr_b16 v[170:171], v2 offset:2112\n" \
" ds_read_b64_tr_b16 v[56:57], v2 offset:96\n" \
" ds_read_b64_tr_b16 v[58:59], v2 offset:2144\n" \
" s_waitcnt lgkmcnt(0)\n" \
" s_add_u32 m0, s52, 0\n" \
" s_mov_b32 s54, 1310720\n" \
" buffer_load_dwordx4 v1, s[40:43], s54 offen lds\n" \
" s_add_u32 m0, s52, 1024\n" \
" s_mov_b32 s54, 1327104\n" \
" buffer_load_dwordx4 v1, s[40:43], s54 offen lds\n" \
" s_add_u32 m0, s52, 2048\n" \
" s_mov_b32 s54, 1343488\n" \
" buffer_load_dwordx4 v1, s[40:43], s54 offen lds\n" \
" s_add_u32 m0, s52, 3072\n" \
" s_mov_b32 s54, 1359872\n" \
" buffer_load_dwordx4 v1, s[40:43], s54 offen lds\n" \
" s_waitcnt vmcnt(36)\n" \
" ds_read_b64_tr_b16 a[44:45], v2 offset:4096\n" \
" ds_read_b64_tr_b16 a[46:47], v2 offset:6144\n" \
" ds_read_b64_tr_b16 a[172:173], v2 offset:4128\n" \
" ds_read_b64_tr_b16 a[174:175], v2 offset:6176\n" \
" ds_read_b64_tr_b16 v[172:173], v2 offset:4160\n" \
" ds_read_b64_tr_b16 v[174:175], v2 offset:6208\n" \
" ds_read_b64_tr_b16 v[60:61], v2 offset:4192\n" \
" ds_read_b64_tr_b16 v[62:63], v2 offset:6240\n" \
" s_waitcnt lgkmcnt(0)\n" \
" s_add_u32 m0, s52, 4096\n" \
" s_mov_b32 s54, 1376256\n" \
" buffer_load_dwordx4 v1, s[40:43], s54 offen lds\n" \
" s_add_u32 m0, s52, 5120\n" \
" s_mov_b32 s54, 1392640\n" \
" buffer_load_dwordx4 v1, s[40:43], s54 offen lds\n" \
" s_add_u32 m0, s52, 6144\n" \
" s_mov_b32 s54, 1409024\n" \
" buffer_load_dwordx4 v1, s[40:43], s54 offen lds\n" \
" s_add_u32 m0, s52, 7168\n" \
" s_mov_b32 s54, 1425408\n" \
" buffer_load_dwordx4 v1, s[40:43], s54 offen lds\n" \
" s_waitcnt vmcnt(36)\n" \
" ds_read_b64_tr_b16 a[48:49], v2 offset:8192\n" \
" ds_read_b64_tr_b16 a[50:51], v2 offset:10240\n" \
" ds_read_b64_tr_b16 a[176:177], v2 offset:8224\n" \
" ds_read_b64_tr_b16 a[178:179], v2 offset:10272\n" \
" ds_read_b64_tr_b16 v[176:177], v2 offset:8256\n" \
" ds_read_b64_tr_b16 v[178:179], v2 offset:10304\n" \
" ds_read_b64_tr_b16 v[64:65], v2 offset:8288\n" \
" ds_read_b64_tr_b16 v[66:67], v2 offset:10336\n" \
" s_waitcnt lgkmcnt(0)\n" \
" s_add_u32 m0, s52, 8192\n" \
" s_mov_b32 s54, 1441792\n" \
" buffer_load_dwordx4 v1, s[40:43], s54 offen lds\n" \
" s_add_u32 m0, s52, 9216\n" \
" s_mov_b32 s54, 1458176\n" \
" buffer_load_dwordx4 v1, s[40:43], s54 offen lds\n" \
" s_add_u32 m0, s52, 10240\n" \
" s_mov_b32 s54, 1474560\n" \
" buffer_load_dwordx4 v1, s[40:43], s54 offen lds\n" \
" s_add_u32 m0, s52, 11264\n" \
" s_mov_b32 s54, 1490944\n" \
" buffer_load_dwordx4 v1, s[40:43], s54 offen lds\n" \
" s_waitcnt vmcnt(36)\n" \
" ds_read_b64_tr_b16 a[52:53], v2 offset:12288\n" \
" ds_read_b64_tr_b16 a[54:55], v2 offset:14336\n" \
" ds_read_b64_tr_b16 a[180:181], v2 offset:12320\n" \
" ds_read_b64_tr_b16 a[182:183], v2 offset:14368\n" \
" ds_read_b64_tr_b16 v[180:181], v2 offset:12352\n" \
" ds_read_b64_tr_b16 v[182:183], v2 offset:14400\n" \
" ds_read_b64_tr_b16 v[68:69], v2 offset:12384\n" \
" ds_read_b64_tr_b16 v[70:71], v2 offset:14432\n" \
" s_waitcnt lgkmcnt(0)\n" \
" s_add_u32 m0, s52, 12288\n" \
" s_mov_b32 s54, 1507328\n" \
" buffer_load_dwordx4 v1, s[40:43], s54 offen lds\n" \
" s_add_u32 m0, s52, 13312\n" \
" s_mov_b32 s54, 1523712\n" \
" buffer_load_dwordx4 v1, s[40:43], s54 offen lds\n" \
" s_add_u32 m0, s52, 14336\n" \
" s_mov_b32 s54, 1540096\n" \
" buffer_load_dwordx4 v1, s[40:43], s54 offen lds\n" \
" s_add_u32 m0, s52, 15360\n" \
" s_mov_b32 s54, 1556480\n" \
" buffer_load_dwordx4 v1, s[40:43], s54 offen lds\n" \
" s_waitcnt vmcnt(36)\n" \
" ds_read_b64_tr_b16 a[56:57], v2 offset:16384\n" \
" ds_read_b64_tr_b16 a[58:59], v2 offset:18432\n" \
" ds_read_b64_tr_b16 a[184:185], v2 offset:16416\n" \
" ds_read_b64_tr_b16 a[186:187], v2 offset:18464\n" \
" ds_read_b64_tr_b16 v[184:185], v2 offset:16448\n" \
" ds_read_b64_tr_b16 v[186:187], v2 offset:18496\n" \
" ds_read_b64_tr_b16 v[72:73], v2 offset:16480\n" \
" ds_read_b64_tr_b16 v[74:75], v2 offset:18528\n" \
" s_waitcnt lgkmcnt(0)\n" \
" s_add_u32 m0, s52, 16384\n" \
" s_mov_b32 s54, 1572864\n" \
" buffer_load_dwordx4 v1, s[40:43], s54 offen lds\n" \
" s_add_u32 m0, s52, 17408\n" \
" s_mov_b32 s54, 1589248\n" \
" buffer_load_dwordx4 v1, s[40:43], s54 offen lds\n" \
" s_add_u32 m0, s52, 18432\n" \
" s_mov_b32 s54, 1605632\n" \
" buffer_load_dwordx4 v1, s[40:43], s54 offen lds\n" \
" s_add_u32 m0, s52, 19456\n" \
" s_mov_b32 s54, 1622016\n" \
" buffer_load_dwordx4 v1, s[40:43], s54 offen lds\n" \
" s_waitcnt vmcnt(36)\n" \
" ds_read_b64_tr_b16 a[60:61], v2 offset:20480\n" \
" ds_read_b64_tr_b16 a[62:63], v2 offset:22528\n" \
" ds_read_b64_tr_b16 a[188:189], v2 offset:20512\n" \
" ds_read_b64_tr_b16 a[190:191], v2 offset:22560\n" \
" ds_read_b64_tr_b16 v[188:189], v2 offset:20544\n" \
" ds_read_b64_tr_b16 v[190:191], v2 offset:22592\n" \
" ds_read_b64_tr_b16 v[76:77], v2 offset:20576\n" \
" ds_read_b64_tr_b16 v[78:79], v2 offset:22624\n" \
" s_waitcnt lgkmcnt(0)\n" \
" s_add_u32 m0, s52, 20480\n" \
" s_mov_b32 s54, 1638400\n" \
" buffer_load_dwordx4 v1, s[40:43], s54 offen lds\n" \
" s_add_u32 m0, s52, 21504\n" \
" s_mov_b32 s54, 1654784\n" \
" buffer_load_dwordx4 v1, s[40:43], s54 offen lds\n" \
" s_add_u32 m0, s52, 22528\n" \
" s_mov_b32 s54, 1671168\n" \
" buffer_load_dwordx4 v1, s[40:43], s54 offen lds\n" \
" s_add_u32 m0, s52, 23552\n" \
" s_mov_b32 s54, 1687552\n" \
" buffer_load_dwordx4 v1, s[40:43], s54 offen lds\n" \
" s_waitcnt vmcnt(36)\n" \
" ds_read_b64_tr_b16 a[64:65], v2 offset:24576\n" \
" ds_read_b64_tr_b16 a[66:67], v2 offset:26624\n" \
" ds_read_b64_tr_b16 a[192:193], v2 offset:24608\n" \
" ds_read_b64_tr_b16 a[194:195], v2 offset:26656\n" \
" ds_read_b64_tr_b16 v[192:193], v2 offset:24640\n" \
" ds_read_b64_tr_b16 v[194:195], v2 offset:26688\n" \
" ds_read_b64_tr_b16 v[80:81], v2 offset:24672\n" \
" ds_read_b64_tr_b16 v[82:83], v2 offset:26720\n" \
" s_waitcnt lgkmcnt(0)\n" \
" s_add_u32 m0, s52, 24576\n" \
" s_mov_b32 s54, 1703936\n" \
" buffer_load_dwordx4 v1, s[40:43], s54 offen lds\n" \
" s_add_u32 m0, s52, 25600\n" \
" s_mov_b32 s54, 1720320\n" \
" buffer_load_dwordx4 v1, s[40:43], s54 offen lds\n" \
" s_add_u32 m0, s52, 26624\n" \
" s_mov_b32 s54, 1736704\n" \
" buffer_load_dwordx4 v1, s[40:43], s54 offen lds\n" \
" s_add_u32 m0, s52, 27648\n" \
" s_mov_b32 s54, 1753088\n" \
" buffer_load_dwordx4 v1, s[40:43], s54 offen lds\n" \
" s_waitcnt vmcnt(36)\n" \
" ds_read_b64_tr_b16 a[68:69], v2 offset:28672\n" \
" ds_read_b64_tr_b16 a[70:71], v2 offset:30720\n" \
" ds_read_b64_tr_b16 a[196:197], v2 offset:28704\n" \
" ds_read_b64_tr_b16 a[198:199], v2 offset:30752\n" \
" ds_read_b64_tr_b16 v[196:197], v2 offset:28736\n" \
" ds_read_b64_tr_b16 v[198:199], v2 offset:30784\n" \
" ds_read_b64_tr_b16 v[84:85], v2 offset:28768\n" \
" ds_read_b64_tr_b16 v[86:87], v2 offset:30816\n" \
" s_waitcnt lgkmcnt(0)\n" \
" s_add_u32 m0, s52, 28672\n" \
" s_mov_b32 s54, 1769472\n" \
" buffer_load_dwordx4 v1, s[40:43], s54 offen lds\n" \
" s_add_u32 m0, s52, 29696\n" \
" s_mov_b32 s54, 1785856\n" \
" buffer_load_dwordx4 v1, s[40:43], s54 offen lds\n" \
" s_add_u32 m0, s52, 30720\n" \
" s_mov_b32 s54, 1802240\n" \
" buffer_load_dwordx4 v1, s[40:43], s54 offen lds\n" \
" s_add_u32 m0, s52, 31744\n" \
" s_mov_b32 s54, 1818624\n" \
" buffer_load_dwordx4 v1, s[40:43], s54 offen lds\n" \
" s_waitcnt vmcnt(36)\n" \
" ds_read_b64_tr_b16 a[72:73], v9 offset:32768\n" \
" ds_read_b64_tr_b16 a[74:75], v9 offset:34816\n" \
" ds_read_b64_tr_b16 a[200:201], v9 offset:32800\n" \
" ds_read_b64_tr_b16 a[202:203], v9 offset:34848\n" \
" ds_read_b64_tr_b16 v[200:201], v9 offset:32832\n" \
" ds_read_b64_tr_b16 v[202:203], v9 offset:34880\n" \
" ds_read_b64_tr_b16 v[88:89], v9 offset:32864\n" \
" ds_read_b64_tr_b16 v[90:91], v9 offset:34912\n" \
" s_waitcnt lgkmcnt(0)\n" \
" s_add_u32 m0, s53, 32768\n" \
" s_mov_b32 s54, 1835008\n" \
" buffer_load_dwordx4 v1, s[40:43], s54 offen lds\n" \
" s_add_u32 m0, s53, 33792\n" \
" s_mov_b32 s54, 1851392\n" \
" buffer_load_dwordx4 v1, s[40:43], s54 offen lds\n" \
" s_add_u32 m0, s53, 34816\n" \
" s_mov_b32 s54, 1867776\n" \
" buffer_load_dwordx4 v1, s[40:43], s54 offen lds\n" \
" s_add_u32 m0, s53, 35840\n" \
" s_mov_b32 s54, 1884160\n" \
" buffer_load_dwordx4 v1, s[40:43], s54 offen lds\n" \
" s_waitcnt vmcnt(36)\n" \
" ds_read_b64_tr_b16 a[76:77], v9 offset:36864\n" \
" ds_read_b64_tr_b16 a[78:79], v9 offset:38912\n" \
" ds_read_b64_tr_b16 a[204:205], v9 offset:36896\n" \
" ds_read_b64_tr_b16 a[206:207], v9 offset:38944\n" \
" ds_read_b64_tr_b16 v[204:205], v9 offset:36928\n" \
" ds_read_b64_tr_b16 v[206:207], v9 offset:38976\n" \
" ds_read_b64_tr_b16 v[92:93], v9 offset:36960\n" \
" ds_read_b64_tr_b16 v[94:95], v9 offset:39008\n" \
" s_waitcnt lgkmcnt(0)\n" \
" s_add_u32 m0, s53, 36864\n" \
" s_mov_b32 s54, 1900544\n" \
" buffer_load_dwordx4 v1, s[40:43], s54 offen lds\n" \
" s_add_u32 m0, s53, 37888\n" \
" s_mov_b32 s54, 1916928\n" \
" buffer_load_dwordx4 v1, s[40:43], s54 offen lds\n" \
" s_add_u32 m0, s53, 38912\n" \
" s_mov_b32 s54, 1933312\n" \
" buffer_load_dwordx4 v1, s[40:43], s54 offen lds\n" \
" s_add_u32 m0, s53, 39936\n" \
" s_mov_b32 s54, 1949696\n" \
" buffer_load_dwordx4 v1, s[40:43], s54 offen lds\n" \
" s_waitcnt vmcnt(36)\n" \
" ds_read_b64_tr_b16 a[80:81], v2 offset:0\n" \
" ds_read_b64_tr_b16 a[82:83], v2 offset:2048\n" \
" ds_read_b64_tr_b16 a[208:209], v2 offset:32\n" \
" ds_read_b64_tr_b16 a[210:211], v2 offset:2080\n" \
" ds_read_b64_tr_b16 v[208:209], v2 offset:64\n" \
" ds_read_b64_tr_b16 v[210:211], v2 offset:2112\n" \
" ds_read_b64_tr_b16 v[96:97], v2 offset:96\n" \
" ds_read_b64_tr_b16 v[98:99], v2 offset:2144\n" \
" s_waitcnt lgkmcnt(0)\n" \
" s_add_u32 m0, s52, 0\n" \
" s_mov_b32 s54, 1966080\n" \
" buffer_load_dwordx4 v1, s[40:43], s54 offen lds\n" \
" s_add_u32 m0, s52, 1024\n" \
" s_mov_b32 s54, 1982464\n" \
" buffer_load_dwordx4 v1, s[40:43], s54 offen lds\n" \
" s_add_u32 m0, s52, 2048\n" \
" s_mov_b32 s54, 1998848\n" \
" buffer_load_dwordx4 v1, s[40:43], s54 offen lds\n" \
" s_add_u32 m0, s52, 3072\n" \
" s_mov_b32 s54, 2015232\n" \
" buffer_load_dwordx4 v1, s[40:43], s54 offen lds\n" \
" s_waitcnt vmcnt(36)\n" \
" ds_read_b64_tr_b16 a[84:85], v2 offset:4096\n" \
" ds_read_b64_tr_b16 a[86:87], v2 offset:6144\n" \
" ds_read_b64_tr_b16 a[212:213], v2 offset:4128\n" \
" ds_read_b64_tr_b16 a[214:215], v2 offset:6176\n" \
" ds_read_b64_tr_b16 v[212:213], v2 offset:4160\n" \
" ds_read_b64_tr_b16 v[214:215], v2 offset:6208\n" \
" ds_read_b64_tr_b16 v[100:101], v2 offset:4192\n" \
" ds_read_b64_tr_b16 v[102:103], v2 offset:6240\n" \
" s_waitcnt lgkmcnt(0)\n" \
" s_add_u32 m0, s52, 4096\n" \
" s_mov_b32 s54, 2031616\n" \
" buffer_load_dwordx4 v1, s[40:43], s54 offen lds\n" \
" s_add_u32 m0, s52, 5120\n" \
" s_mov_b32 s54, 2048000\n" \
" buffer_load_dwordx4 v1, s[40:43], s54 offen lds\n" \
" s_add_u32 m0, s52, 6144\n" \
" s_mov_b32 s54, 2064384\n" \
" buffer_load_dwordx4 v1, s[40:43], s54 offen lds\n" \
" s_add_u32 m0, s52, 7168\n" \
" s_mov_b32 s54, 2080768\n" \
" buffer_load_dwordx4 v1, s[40:43], s54 offen lds\n" \
" s_waitcnt vmcnt(36)\n" \
" ds_read_b64_tr_b16 a[88:89], v2 offset:8192\n" \
" ds_read_b64_tr_b16 a[90:91], v2 offset:10240\n" \
" ds_read_b64_tr_b16 a[216:217], v2 offset:8224\n" \
" ds_read_b64_tr_b16 a[218:219], v2 offset:10272\n" \
" ds_read_b64_tr_b16 v[216:217], v2 offset:8256\n" \
" ds_read_b64_tr_b16 v[218:219], v2 offset:10304\n" \
" ds_read_b64_tr_b16 v[104:105], v2 offset:8288\n" \
" ds_read_b64_tr_b16 v[106:107], v2 offset:10336\n" \
" s_waitcnt lgkmcnt(0)\n" \
" ds_write_b128 v0, v[16:19] offset:8192\n" \
" ds_write_b128 v0, v[20:23] offset:9216\n" \
" ds_write_b128 v0, v[24:27] offset:10240\n" \
" ds_write_b128 v0, v[28:31] offset:11264\n" \
" s_waitcnt vmcnt(32)\n" \
" ds_read_b64_tr_b16 a[92:93], v2 offset:12288\n" \
" ds_read_b64_tr_b16 a[94:95], v2 offset:14336\n" \
" ds_read_b64_tr_b16 a[220:221], v2 offset:12320\n" \
" ds_read_b64_tr_b16 a[222:223], v2 offset:14368\n" \
" ds_read_b64_tr_b16 v[220:221], v2 offset:12352\n" \
" ds_read_b64_tr_b16 v[222:223], v2 offset:14400\n" \
" ds_read_b64_tr_b16 v[108:109], v2 offset:12384\n" \
" ds_read_b64_tr_b16 v[110:111], v2 offset:14432\n" \
" s_waitcnt lgkmcnt(0)\n" \
" ds_write_b128 v0, v[32:35] offset:12288\n" \
" ds_write_b128 v0, v[36:39] offset:13312\n" \
" ds_write_b128 v0, v[40:43] offset:14336\n" \
" ds_write_b128 v0, v[44:47] offset:15360\n" \
" s_waitcnt vmcnt(28)\n" \
" ds_read_b64_tr_b16 a[96:97], v2 offset:16384\n" \
" ds_read_b64_tr_b16 a[98:99], v2 offset:18432\n" \
" ds_read_b64_tr_b16 a[224:225], v2 offset:16416\n" \
" ds_read_b64_tr_b16 a[226:227], v2 offset:18464\n" \
" ds_read_b64_tr_b16 v[224:225], v2 offset:16448\n" \
" ds_read_b64_tr_b16 v[226:227], v2 offset:18496\n" \
" ds_read_b64_tr_b16 v[16:17], v2 offset:16480\n" \
" ds_read_b64_tr_b16 v[18:19], v2 offset:18528\n" \
" s_waitcnt lgkmcnt(0)\n" \
" ds_write_b128 v0, v[48:51] offset:16384\n" \
" ds_write_b128 v0, v[52:55] offset:17408\n" \
" ds_write_b128 v0, v[56:59] offset:18432\n" \
" ds_write_b128 v0, v[60:63] offset:19456\n" \
" s_waitcnt vmcnt(24)\n" \
" ds_read_b64_tr_b16 a[100:101], v2 offset:20480\n" \
" ds_read_b64_tr_b16 a[102:103], v2 offset:22528\n" \
" ds_read_b64_tr_b16 a[228:229], v2 offset:20512\n" \
" ds_read_b64_tr_b16 a[230:231], v2 offset:22560\n" \
" ds_read_b64_tr_b16 v[228:229], v2 offset:20544\n" \
" ds_read_b64_tr_b16 v[230:231], v2 offset:22592\n" \
" ds_read_b64_tr_b16 v[20:21], v2 offset:20576\n" \
" ds_read_b64_tr_b16 v[22:23], v2 offset:22624\n" \
" s_waitcnt lgkmcnt(0)\n" \
" ds_write_b128 v0, v[64:67] offset:20480\n" \
" ds_write_b128 v0, v[68:71] offset:21504\n" \
" ds_write_b128 v0, v[72:75] offset:22528\n" \
" ds_write_b128 v0, v[76:79] offset:23552\n" \
" s_waitcnt vmcnt(20)\n" \
" ds_read_b64_tr_b16 a[104:105], v2 offset:24576\n" \
" ds_read_b64_tr_b16 a[106:107], v2 offset:26624\n" \
" ds_read_b64_tr_b16 a[232:233], v2 offset:24608\n" \
" ds_read_b64_tr_b16 a[234:235], v2 offset:26656\n" \
" ds_read_b64_tr_b16 v[232:233], v2 offset:24640\n" \
" ds_read_b64_tr_b16 v[234:235], v2 offset:26688\n" \
" ds_read_b64_tr_b16 v[24:25], v2 offset:24672\n" \
" ds_read_b64_tr_b16 v[26:27], v2 offset:26720\n" \
" s_waitcnt lgkmcnt(0)\n" \
" ds_write_b128 v0, v[80:83] offset:24576\n" \
" ds_write_b128 v0, v[84:87] offset:25600\n" \
" ds_write_b128 v0, v[88:91] offset:26624\n" \
" ds_write_b128 v0, v[92:95] offset:27648\n" \
" s_waitcnt vmcnt(16)\n" \
" ds_read_b64_tr_b16 a[108:109], v2 offset:28672\n" \
" ds_read_b64_tr_b16 a[110:111], v2 offset:30720\n" \
" ds_read_b64_tr_b16 a[236:237], v2 offset:28704\n" \
" ds_read_b64_tr_b16 a[238:239], v2 offset:30752\n" \
" ds_read_b64_tr_b16 v[236:237], v2 offset:28736\n" \
" ds_read_b64_tr_b16 v[238:239], v2 offset:30784\n" \
" ds_read_b64_tr_b16 v[28:29], v2 offset:28768\n" \
" ds_read_b64_tr_b16 v[30:31], v2 offset:30816\n" \
" s_waitcnt lgkmcnt(0)\n" \
" ds_write_b128 v0, v[96:99] offset:28672\n" \
" ds_write_b128 v0, v[100:103] offset:29696\n" \
" ds_write_b128 v0, v[104:107] offset:30720\n" \
" ds_write_b128 v0, v[108:111] offset:31744\n" \
" s_waitcnt vmcnt(12)\n" \
" ds_read_b64_tr_b16 a[112:113], v9 offset:32768\n" \
" ds_read_b64_tr_b16 a[114:115], v9 offset:34816\n" \
" ds_read_b64_tr_b16 a[240:241], v9 offset:32800\n" \
" ds_read_b64_tr_b16 a[242:243], v9 offset:34848\n" \
" ds_read_b64_tr_b16 v[240:241], v9 offset:32832\n" \
" ds_read_b64_tr_b16 v[242:243], v9 offset:34880\n" \
" ds_read_b64_tr_b16 v[32:33], v9 offset:32864\n" \
" ds_read_b64_tr_b16 v[34:35], v9 offset:34912\n" \
" s_waitcnt lgkmcnt(0)\n" \
" s_waitcnt vmcnt(8)\n" \
" ds_read_b64_tr_b16 a[116:117], v9 offset:36864\n" \
" ds_read_b64_tr_b16 a[118:119], v9 offset:38912\n" \
" ds_read_b64_tr_b16 a[244:245], v9 offset:36896\n" \
" ds_read_b64_tr_b16 a[246:247], v9 offset:38944\n" \
" ds_read_b64_tr_b16 v[244:245], v9 offset:36928\n" \
" ds_read_b64_tr_b16 v[246:247], v9 offset:38976\n" \
" ds_read_b64_tr_b16 v[36:37], v9 offset:36960\n" \
" ds_read_b64_tr_b16 v[38:39], v9 offset:39008\n" \
" s_waitcnt lgkmcnt(0)\n" \
" s_waitcnt vmcnt(4)\n" \
" ds_read_b64_tr_b16 a[120:121], v2 offset:0\n" \
" ds_read_b64_tr_b16 a[122:123], v2 offset:2048\n" \
" ds_read_b64_tr_b16 a[248:249], v2 offset:32\n" \
" ds_read_b64_tr_b16 a[250:251], v2 offset:2080\n" \
" ds_read_b64_tr_b16 v[248:249], v2 offset:64\n" \
" ds_read_b64_tr_b16 v[250:251], v2 offset:2112\n" \
" ds_read_b64_tr_b16 v[40:41], v2 offset:96\n" \
" ds_read_b64_tr_b16 v[42:43], v2 offset:2144\n" \
" s_waitcnt lgkmcnt(0)\n" \
" ds_write_b128 v0, v[16:19] offset:0\n" \
" ds_write_b128 v0, v[20:23] offset:1024\n" \
" ds_write_b128 v0, v[24:27] offset:2048\n" \
" ds_write_b128 v0, v[28:31] offset:3072\n" \
" s_waitcnt vmcnt(0)\n" \
" ds_read_b64_tr_b16 a[124:125], v2 offset:4096\n" \
" ds_read_b64_tr_b16 a[126:127], v2 offset:6144\n" \
" ds_read_b64_tr_b16 a[252:253], v2 offset:4128\n" \
" ds_read_b64_tr_b16 a[254:255], v2 offset:6176\n" \
" ds_read_b64_tr_b16 v[252:253], v2 offset:4160\n" \
" ds_read_b64_tr_b16 v[254:255], v2 offset:6208\n" \
" ds_read_b64_tr_b16 v[44:45], v2 offset:4192\n" \
" ds_read_b64_tr_b16 v[46:47], v2 offset:6240\n" \
" s_waitcnt lgkmcnt(0)\n" \
" ds_write_b128 v0, v[32:35] offset:4096\n" \
" ds_write_b128 v0, v[36:39] offset:5120\n" \
" ds_write_b128 v0, v[40:43] offset:6144\n" \
" ds_write_b128 v0, v[44:47] offset:7168\n" \
" s_waitcnt vmcnt(0) lgkmcnt(0)\n" \
" s_barrier\n" \
" s_mov_b32 s55, 0\n" \
" s_mov_b32 s60, 0\n" \
" v_mov_b32 v10, 0\n" \
"Lswo_prime:\n" \
" s_lshr_b32 s59, s44, 1\n" \
" s_add_u32 s59, s59, 1\n" \
" s_mul_i32 s56, s44, 4096\n" \
" buffer_load_dword v11, v4, s[36:39], 0 offen sc1\n" \
" s_waitcnt vmcnt(0)\n" \
" v_cmp_gt_u32_e64 s[62:63], s59, v11\n" \
" s_nop 4\n" \
" s_cmp_lg_u64 s[62:63], 0\n" \
" s_cbranch_scc1 Lswo_w\n" \
"Lswo_wk:\n" \
" s_mov_b32 s76, 0x7ffffff0\n" \
" s_mov_b32 s77, 0x7ffffff0\n" \
" s_mov_b32 s79, 0\n" \
" s_min_u32 s80, s55, s47\n" \
" s_mov_b32 s81, 1\n" \
" s_mov_b32 s84, 0\n" \
" s_waitcnt vmcnt(0) lgkmcnt(0)\n" \
" s_add_u32 s57, s56, 0\n" \
" buffer_load_dwordx4 v[16:19], v3, s[36:39], s57 offen offset:0 sc1\n" \
" buffer_load_dwordx4 v[20:23], v3, s[36:39], s57 offen offset:1024 sc1\n" \
" buffer_load_dwordx4 v[24:27], v3, s[36:39], s57 offen offset:2048 sc1\n" \
" buffer_load_dwordx4 v[28:31], v3, s[36:39], s57 offen offset:3072 sc1\n" \
" s_add_u32 s57, s56, 532480\n" \
" buffer_load_dwordx4 v[32:35], v3, s[36:39], s57 offen offset:0 sc1\n" \
" buffer_load_dwordx4 v[36:39], v3, s[36:39], s57 offen offset:1024 sc1\n" \
" buffer_load_dwordx4 v[40:43], v3, s[36:39], s57 offen offset:2048 sc1\n" \
" buffer_load_dwordx4 v[44:47], v3, s[36:39], s57 offen offset:3072 sc1\n" \
" s_add_u32 s57, s56, 1064960\n" \
" buffer_load_dwordx4 v[48:51], v3, s[36:39], s57 offen offset:0 sc1\n" \
" buffer_load_dwordx4 v[52:55], v3, s[36:39], s57 offen offset:1024 sc1\n" \
" buffer_load_dwordx4 v[56:59], v3, s[36:39], s57 offen offset:2048 sc1\n" \
" buffer_load_dwordx4 v[60:63], v3, s[36:39], s57 offen offset:3072 sc1\n" \
" s_add_u32 s57, s56, 1597440\n" \
" buffer_load_dwordx4 v[64:67], v3, s[36:39], s57 offen offset:0 sc1\n" \
" buffer_load_dwordx4 v[68:71], v3, s[36:39], s57 offen offset:1024 sc1\n" \
" buffer_load_dwordx4 v[72:75], v3, s[36:39], s57 offen offset:2048 sc1\n" \
" buffer_load_dwordx4 v[76:79], v3, s[36:39], s57 offen offset:3072 sc1\n" \
" ds_read_b128 v[80:83], v0 offset:8192\n" \
" ds_read_b128 v[84:87], v0 offset:9216\n" \
" ds_read_b128 v[88:91], v0 offset:10240\n" \
" s_waitcnt lgkmcnt(0)\n" \
" v_mov_b32 v13, 0x7ffffff0\n" \
" buffer_store_dword v13, v13, s[36:39], 0 offen\n" \
" buffer_store_dword v13, v13, s[36:39], 0 offen\n" \
" buffer_store_dword v13, v13, s[36:39], 0 offen\n" \
" s_waitcnt vmcnt(18)\n" \
" s_nop 7\n" \
" s_nop 7\n" \
".p2align 6\n" \
"Lswo_loop:\n" \
" v_mfma_f32_16x16x32_bf16 v[96:99], a[0:3], v[16:19], 0\n" \
" ds_write_b128 v7, v[104:107] offset:2048\n" \
" v_mfma_f32_16x16x32_bf16 v[100:103], a[128:131], v[16:19], 0\n" \
" ds_write_b128 v7, v[108:111] offset:3072\n" \
" ds_read_b128 v[92:95], v0 offset:11264\n" \
" v_mfma_f32_16x16x32_bf16 v[104:107], v[128:131], v[16:19], 0\n" \
" s_waitcnt vmcnt(17) lgkmcnt(7)\n" \
" v_mfma_f32_16x16x32_bf16 v[108:111], v[80:83], v[16:19], 0\n" \
" s_add_u32 s57, s56, 2129920\n" \
" buffer_load_dwordx4 v[16:19], v3, s[36:39], s57 offen offset:0 sc1\n" \
" v_mfma_f32_16x16x32_bf16 v[96:99], a[4:7], v[20:23], v[96:99]\n" \
" v_cmp_gt_u32 vcc, s79, v9\n" \
" v_mfma_f32_16x16x32_bf16 v[100:103], a[132:135], v[20:23], v[100:103]\n" \
" ds_read_b128 v[80:83], v0 offset:12288\n" \
" v_mfma_f32_16x16x32_bf16 v[104:107], v[132:135], v[20:23], v[104:107]\n" \
" s_waitcnt vmcnt(17) lgkmcnt(7)\n" \
" s_nop 0\n" \
" s_nop 0\n" \
" s_nop 0\n" \
" s_cbranch_vccnz Lswo_p\n" \
"Lswo_pk:\n" \
" v_mfma_f32_16x16x32_bf16 v[108:111], v[84:87], v[20:23], v[108:111]\n" \
" buffer_load_dwordx4 v[20:23], v3, s[36:39], s57 offen offset:1024 sc1\n" \
" v_mfma_f32_16x16x32_bf16 v[96:99], a[8:11], v[24:27], v[96:99]\n" \
" s_waitcnt lgkmcnt(2)\n" \
" s_cmp_lg_u32 s81, 0\n" \
" s_cbranch_scc1 Lswo_nb\n" \
" s_barrier\n" \
"Lswo_nb:\n" \
" v_mfma_f32_16x16x32_bf16 v[100:103], a[136:139], v[24:27], v[100:103]\n" \
" ds_read_b128 v[84:87], v0 offset:13312\n" \
" v_xor_b32 v7, s84, v7\n" \
" v_mfma_f32_16x16x32_bf16 v[104:107], v[136:139], v[24:27], v[104:107]\n" \
" s_waitcnt vmcnt(17)\n" \
" buffer_load_dwordx2 v[14:15], v6, s[36:39], s77 offen sc1\n" \
" v_mfma_f32_16x16x32_bf16 v[108:111], v[88:91], v[24:27], v[108:111]\n" \
" buffer_load_dwordx4 v[24:27], v3, s[36:39], s57 offen offset:2048 sc1\n" \
" v_mfma_f32_16x16x32_bf16 v[96:99], a[12:15], v[28:31], v[96:99]\n" \
" buffer_load_dwordx2 v[12:13], v5, s[64:67], s77 offen\n" \
" ds_read_b128 v[112:115], v8 offset:0\n" \
" v_mfma_f32_16x16x32_bf16 v[100:103], a[140:143], v[28:31], v[100:103]\n" \
" ds_read_b128 v[88:91], v0 offset:14336\n" \
" v_mfma_f32_16x16x32_bf16 v[104:107], v[140:143], v[28:31], v[104:107]\n" \
" s_waitcnt vmcnt(19) lgkmcnt(4)\n" \
" v_mfma_f32_16x16x32_bf16 v[108:111], v[92:95], v[28:31], v[108:111]\n" \
" buffer_load_dwordx4 v[28:31], v3, s[36:39], s57 offen offset:3072 sc1\n" \
" v_mfma_f32_16x16x32_bf16 v[96:99], a[16:19], v[32:35], v[96:99]\n" \
" ds_read_b128 v[116:119], v8 offset:4096\n" \
" s_add_u32 s82, s44, s45\n" \
" s_lshr_b32 s59, s82, 1\n" \
" v_mfma_f32_16x16x32_bf16 v[100:103], a[144:147], v[32:35], v[100:103]\n" \
" ds_read_b128 v[92:95], v0 offset:15360\n" \
" v_mfma_f32_16x16x32_bf16 v[104:107], v[144:147], v[32:35], v[104:107]\n" \
" s_waitcnt vmcnt(19) lgkmcnt(5)\n" \
" s_add_u32 s59, s59, 1\n" \
" v_mfma_f32_16x16x32_bf16 v[108:111], v[80:83], v[32:35], v[108:111]\n" \
" s_add_u32 s57, s56, 2662400\n" \
" buffer_load_dwordx4 v[32:35], v3, s[36:39], s57 offen offset:0 sc1\n" \
" v_mfma_f32_16x16x32_bf16 v[96:99], a[20:23], v[36:39], v[96:99]\n" \
" ds_read_b128 v[120:123], v8 offset:8192\n" \
" v_mfma_f32_16x16x32_bf16 v[100:103], a[148:151], v[36:39], v[100:103]\n" \
" ds_read_b128 v[80:83], v0 offset:16384\n" \
" v_mfma_f32_16x16x32_bf16 v[104:107], v[148:151], v[36:39], v[104:107]\n" \
" s_waitcnt vmcnt(18) lgkmcnt(6)\n" \
" v_mfma_f32_16x16x32_bf16 v[108:111], v[84:87], v[36:39], v[108:111]\n" \
" buffer_load_dwordx4 v[36:39], v3, s[36:39], s57 offen offset:1024 sc1\n" \
" v_mfma_f32_16x16x32_bf16 v[96:99], a[24:27], v[40:43], v[96:99]\n" \
" ds_read_b128 v[124:127], v8 offset:12288\n" \
" v_mfma_f32_16x16x32_bf16 v[100:103], a[152:155], v[40:43], v[100:103]\n" \
" ds_read_b128 v[84:87], v0 offset:17408\n" \
" v_mfma_f32_16x16x32_bf16 v[104:107], v[152:155], v[40:43], v[104:107]\n" \
" s_waitcnt vmcnt(18) lgkmcnt(6)\n" \
" v_mfma_f32_16x16x32_bf16 v[108:111], v[88:91], v[40:43], v[108:111]\n" \
" buffer_load_dwordx4 v[40:43], v3, s[36:39], s57 offen offset:2048 sc1\n" \
" v_mfma_f32_16x16x32_bf16 v[96:99], a[28:31], v[44:47], v[96:99]\n" \
" v_mfma_f32_16x16x32_bf16 v[100:103], a[156:159], v[44:47], v[100:103]\n" \
" ds_read_b128 v[88:91], v0 offset:18432\n" \
" v_mfma_f32_16x16x32_bf16 v[104:107], v[156:159], v[44:47], v[104:107]\n" \
" s_waitcnt vmcnt(18) lgkmcnt(5)\n" \
" v_add_f32 v112, v112, v116\n" \
" v_mfma_f32_16x16x32_bf16 v[108:111], v[92:95], v[44:47], v[108:111]\n" \
" buffer_load_dwordx4 v[44:47], v3, s[36:39], s57 offen offset:3072 sc1\n" \
" v_add_f32_e64 v113, v113, v117\n" \
" v_mfma_f32_16x16x32_bf16 v[96:99], a[32:35], v[48:51], v[96:99]\n" \
" v_add_f32 v114, v114, v118\n" \
" v_add_f32 v115, v115, v119\n" \
" v_mfma_f32_16x16x32_bf16 v[100:103], a[160:163], v[48:51], v[100:103]\n" \
" ds_read_b128 v[92:95], v0 offset:19456\n" \
" s_waitcnt lgkmcnt(3)\n" \
" v_add_f32 v120, v120, v124\n" \
" v_mfma_f32_16x16x32_bf16 v[104:107], v[160:163], v[48:51], v[104:107]\n" \
" s_waitcnt vmcnt(18)\n" \
" v_add_f32 v121, v121, v125\n" \
" v_mfma_f32_16x16x32_bf16 v[108:111], v[80:83], v[48:51], v[108:111]\n" \
" s_add_u32 s57, s56, 3194880\n" \
" buffer_load_dwordx4 v[48:51], v3, s[36:39], s57 offen offset:0 sc1\n" \
" v_add_f32_e64 v122, v122, v126\n" \
" v_mfma_f32_16x16x32_bf16 v[96:99], a[36:39], v[52:55], v[96:99]\n" \
" v_add_f32 v123, v123, v127\n" \
" v_add_f32 v112, v112, v120\n" \
" v_mfma_f32_16x16x32_bf16 v[100:103], a[164:167], v[52:55], v[100:103]\n" \
" ds_read_b128 v[80:83], v0 offset:20480\n" \
" v_add_f32_e64 v113, v113, v121\n" \
" v_mfma_f32_16x16x32_bf16 v[104:107], v[164:167], v[52:55], v[104:107]\n" \
" s_waitcnt vmcnt(18) lgkmcnt(3)\n" \
" v_add_f32 v114, v114, v122\n" \
" v_mfma_f32_16x16x32_bf16 v[108:111], v[84:87], v[52:55], v[108:111]\n" \
" buffer_load_dwordx4 v[52:55], v3, s[36:39], s57 offen offset:1024 sc1\n" \
" v_add_f32_e64 v115, v115, v123\n" \
" v_mfma_f32_16x16x32_bf16 v[96:99], a[40:43], v[56:59], v[96:99]\n" \
" v_xor_b32_e64 v8, s84, v8\n" \
" v_cvt_pk_bf16_f32 v116, v112, v113\n" \
" v_mfma_f32_16x16x32_bf16 v[100:103], a[168:171], v[56:59], v[100:103]\n" \
" ds_read_b128 v[84:87], v0 offset:21504\n" \
" v_cvt_pk_bf16_f32 v117, v114, v115\n" \
" v_mfma_f32_16x16x32_bf16 v[104:107], v[168:171], v[56:59], v[104:107]\n" \
" s_waitcnt vmcnt(18) lgkmcnt(3)\n" \
" v_mfma_f32_16x16x32_bf16 v[108:111], v[88:91], v[56:59], v[108:111]\n" \
" buffer_load_dwordx4 v[56:59], v3, s[36:39], s57 offen offset:2048 sc1\n" \
" buffer_store_dwordx2 v[116:117], v6, s[36:39], s76 offen sc1\n" \
" v_mfma_f32_16x16x32_bf16 v[96:99], a[44:47], v[60:63], v[96:99]\n" \
" v_mfma_f32_16x16x32_bf16 v[100:103], a[172:175], v[60:63], v[100:103]\n" \
" ds_read_b128 v[88:91], v0 offset:22528\n" \
" v_mfma_f32_16x16x32_bf16 v[104:107], v[172:175], v[60:63], v[104:107]\n" \
" s_waitcnt vmcnt(19) lgkmcnt(3)\n" \
" v_mfma_f32_16x16x32_bf16 v[108:111], v[92:95], v[60:63], v[108:111]\n" \
" buffer_load_dwordx4 v[60:63], v3, s[36:39], s57 offen offset:3072 sc1\n" \
" v_mfma_f32_16x16x32_bf16 v[96:99], a[48:51], v[64:67], v[96:99]\n" \
" v_mfma_f32_16x16x32_bf16 v[100:103], a[176:179], v[64:67], v[100:103]\n" \
" ds_read_b128 v[92:95], v0 offset:23552\n" \
" v_mfma_f32_16x16x32_bf16 v[104:107], v[176:179], v[64:67], v[104:107]\n" \
" s_waitcnt vmcnt(17) lgkmcnt(3)\n" \
" v_mfma_f32_16x16x32_bf16 v[108:111], v[80:83], v[64:67], v[108:111]\n" \
" s_add_u32 s57, s56, 3727360\n" \
" buffer_load_dwordx4 v[64:67], v3, s[36:39], s57 offen offset:0 sc1\n" \
" v_mfma_f32_16x16x32_bf16 v[96:99], a[52:55], v[68:71], v[96:99]\n" \
" v_cmp_gt_u32_e64 s[62:63], s59, v11\n" \
" v_mfma_f32_16x16x32_bf16 v[100:103], a[180:183], v[68:71], v[100:103]\n" \
" ds_read_b128 v[80:83], v0 offset:24576\n" \
" v_mfma_f32_16x16x32_bf16 v[104:107], v[180:183], v[68:71], v[104:107]\n" \
" s_waitcnt vmcnt(17) lgkmcnt(3)\n" \
" v_mfma_f32_16x16x32_bf16 v[108:111], v[84:87], v[68:71], v[108:111]\n" \
" buffer_load_dwordx4 v[68:71], v3, s[36:39], s57 offen offset:1024 sc1\n" \
" v_mfma_f32_16x16x32_bf16 v[96:99], a[56:59], v[72:75], v[96:99]\n" \
" s_add_u32 s54, s55, 1\n" \
" s_cmp_lt_u32 s54, s46\n" \
" v_mfma_f32_16x16x32_bf16 v[100:103], a[184:187], v[72:75], v[100:103]\n" \
" ds_read_b128 v[84:87], v0 offset:25600\n" \
" s_cselect_b64 s[62:63], s[62:63], -1\n" \
" v_mfma_f32_16x16x32_bf16 v[104:107], v[184:187], v[72:75], v[104:107]\n" \
" s_waitcnt vmcnt(17) lgkmcnt(3)\n" \
" s_mul_i32 s83, s82, 4096\n" \
" v_mfma_f32_16x16x32_bf16 v[108:111], v[88:91], v[72:75], v[108:111]\n" \
" buffer_load_dwordx4 v[72:75], v3, s[36:39], s57 offen offset:2048 sc1\n" \
" v_mfma_f32_16x16x32_bf16 v[96:99], a[60:63], v[76:79], v[96:99]\n" \
" s_cmp_lg_u64 s[62:63], 0\n" \
" s_cselect_b32 s81, 1, 0\n" \
" v_mfma_f32_16x16x32_bf16 v[100:103], a[188:191], v[76:79], v[100:103]\n" \
" ds_read_b128 v[88:91], v0 offset:26624\n" \
" s_cselect_b32 s83, 0x70000000, s83\n" \
" v_mfma_f32_16x16x32_bf16 v[104:107], v[188:191], v[76:79], v[104:107]\n" \
" s_waitcnt vmcnt(17) lgkmcnt(3)\n" \
" v_mfma_f32_16x16x32_bf16 v[108:111], v[92:95], v[76:79], v[108:111]\n" \
" buffer_load_dwordx4 v[76:79], v3, s[36:39], s57 offen offset:3072 sc1\n" \
" v_mfma_f32_16x16x32_bf16 v[96:99], a[64:67], v[16:19], v[96:99]\n" \
" v_mfma_f32_16x16x32_bf16 v[100:103], a[192:195], v[16:19], v[100:103]\n" \
" ds_read_b128 v[92:95], v0 offset:27648\n" \
" v_mfma_f32_16x16x32_bf16 v[104:107], v[192:195], v[16:19], v[104:107]\n" \
" s_waitcnt vmcnt(17) lgkmcnt(3)\n" \
" s_mov_b32 s56, s83\n" \
" v_mfma_f32_16x16x32_bf16 v[108:111], v[80:83], v[16:19], v[108:111]\n" \
" s_add_u32 s57, s56, 0\n" \
" buffer_load_dwordx4 v[16:19], v3, s[36:39], s57 offen offset:0 sc1\n" \
" v_mfma_f32_16x16x32_bf16 v[96:99], a[68:71], v[20:23], v[96:99]\n" \
" buffer_load_dword v9, v10, s[36:39], s50 offen sc1\n" \
" v_mfma_f32_16x16x32_bf16 v[100:103], a[196:199], v[20:23], v[100:103]\n" \
" ds_read_b128 v[80:83], v0 offset:28672\n" \
" v_mfma_f32_16x16x32_bf16 v[104:107], v[196:199], v[20:23], v[104:107]\n" \
" s_waitcnt vmcnt(17) lgkmcnt(3)\n" \
" v_mfma_f32_16x16x32_bf16 v[108:111], v[84:87], v[20:23], v[108:111]\n" \
" buffer_load_dwordx4 v[20:23], v3, s[36:39], s57 offen offset:1024 sc1\n" \
" v_mfma_f32_16x16x32_bf16 v[96:99], a[72:75], v[24:27], v[96:99]\n" \
" v_mfma_f32_16x16x32_bf16 v[100:103], a[200:203], v[24:27], v[100:103]\n" \
" ds_read_b128 v[84:87], v0 offset:29696\n" \
" v_mfma_f32_16x16x32_bf16 v[104:107], v[200:203], v[24:27], v[104:107]\n" \
" s_waitcnt vmcnt(16) lgkmcnt(3)\n" \
" v_mfma_f32_16x16x32_bf16 v[108:111], v[88:91], v[24:27], v[108:111]\n" \
" buffer_load_dwordx4 v[24:27], v3, s[36:39], s57 offen offset:2048 sc1\n" \
" v_mfma_f32_16x16x32_bf16 v[96:99], a[76:79], v[28:31], v[96:99]\n" \
" v_lshlrev_b32_e64 v120, 16, v14\n" \
" v_and_b32 v121, 0xffff0000, v14\n" \
" v_mfma_f32_16x16x32_bf16 v[100:103], a[204:207], v[28:31], v[100:103]\n" \
" ds_read_b128 v[88:91], v0 offset:30720\n" \
" v_lshlrev_b32_e64 v124, 16, v12\n" \
" v_mfma_f32_16x16x32_bf16 v[104:107], v[204:207], v[28:31], v[104:107]\n" \
" s_waitcnt vmcnt(16) lgkmcnt(3)\n" \
" v_and_b32 v125, 0xffff0000, v12\n" \
" v_mfma_f32_16x16x32_bf16 v[108:111], v[92:95], v[28:31], v[108:111]\n" \
" buffer_load_dwordx4 v[28:31], v3, s[36:39], s57 offen offset:3072 sc1\n" \
" v_lshlrev_b32 v122, 16, v15\n" \
" v_mfma_f32_16x16x32_bf16 v[96:99], a[80:83], v[32:35], v[96:99]\n" \
" v_and_b32 v123, 0xffff0000, v15\n" \
" v_lshlrev_b32_e64 v126, 16, v13\n" \
" v_mfma_f32_16x16x32_bf16 v[100:103], a[208:211], v[32:35], v[100:103]\n" \
" ds_read_b128 v[92:95], v0 offset:31744\n" \
" v_and_b32 v127, 0xffff0000, v13\n" \
" v_mfma_f32_16x16x32_bf16 v[104:107], v[208:211], v[32:35], v[104:107]\n" \
" s_waitcnt vmcnt(16) lgkmcnt(3)\n" \
" v_fmac_f32 v124, v112, v120\n" \
" v_mfma_f32_16x16x32_bf16 v[108:111], v[80:83], v[32:35], v[108:111]\n" \
" s_add_u32 s57, s56, 532480\n" \
" buffer_load_dwordx4 v[32:35], v3, s[36:39], s57 offen offset:0 sc1\n" \
" v_fmac_f32_e64 v125, v113, v121\n" \
" v_mfma_f32_16x16x32_bf16 v[96:99], a[84:87], v[36:39], v[96:99]\n" \
" v_fmac_f32 v126, v114, v122\n" \
" v_fmac_f32 v127, v115, v123\n" \
" v_mfma_f32_16x16x32_bf16 v[100:103], a[212:215], v[36:39], v[100:103]\n" \
" ds_read_b128 v[80:83], v0 offset:0\n" \
" v_cvt_pk_bf16_f32 v118, v124, v125\n" \
" v_mfma_f32_16x16x32_bf16 v[104:107], v[212:215], v[36:39], v[104:107]\n" \
" s_waitcnt vmcnt(16) lgkmcnt(3)\n" \
" v_cvt_pk_bf16_f32 v119, v126, v127\n" \
" v_mfma_f32_16x16x32_bf16 v[108:111], v[84:87], v[36:39], v[108:111]\n" \
" buffer_load_dwordx4 v[36:39], v3, s[36:39], s57 offen offset:1024 sc1\n" \
" v_mfma_f32_16x16x32_bf16 v[96:99], a[88:91], v[40:43], v[96:99]\n" \
" buffer_store_dwordx2 v[118:119], v5, s[68:71], s77 offen\n" \
" v_mfma_f32_16x16x32_bf16 v[100:103], a[216:219], v[40:43], v[100:103]\n" \
" ds_read_b128 v[84:87], v0 offset:1024\n" \
" v_mfma_f32_16x16x32_bf16 v[104:107], v[216:219], v[40:43], v[104:107]\n" \
" s_waitcnt vmcnt(17) lgkmcnt(3)\n" \
" v_mfma_f32_16x16x32_bf16 v[108:111], v[88:91], v[40:43], v[108:111]\n" \
" buffer_load_dwordx4 v[40:43], v3, s[36:39], s57 offen offset:2048 sc1\n" \
" v_mfma_f32_16x16x32_bf16 v[96:99], a[92:95], v[44:47], v[96:99]\n" \
" v_mfma_f32_16x16x32_bf16 v[100:103], a[220:223], v[44:47], v[100:103]\n" \
" ds_read_b128 v[88:91], v0 offset:2048\n" \
" v_mfma_f32_16x16x32_bf16 v[104:107], v[220:223], v[44:47], v[104:107]\n" \
" s_waitcnt vmcnt(17) lgkmcnt(3)\n" \
" v_mfma_f32_16x16x32_bf16 v[108:111], v[92:95], v[44:47], v[108:111]\n" \
" buffer_load_dwordx4 v[44:47], v3, s[36:39], s57 offen offset:3072 sc1\n" \
" v_mfma_f32_16x16x32_bf16 v[96:99], a[96:99], v[48:51], v[96:99]\n" \
" v_mfma_f32_16x16x32_bf16 v[100:103], a[224:227], v[48:51], v[100:103]\n" \
" ds_read_b128 v[92:95], v0 offset:3072\n" \
" v_mfma_f32_16x16x32_bf16 v[104:107], v[224:227], v[48:51], v[104:107]\n" \
" s_waitcnt vmcnt(17) lgkmcnt(3)\n" \
" v_mfma_f32_16x16x32_bf16 v[108:111], v[80:83], v[48:51], v[108:111]\n" \
" s_add_u32 s57, s56, 1064960\n" \
" buffer_load_dwordx4 v[48:51], v3, s[36:39], s57 offen offset:0 sc1\n" \
" v_mfma_f32_16x16x32_bf16 v[96:99], a[100:103], v[52:55], v[96:99]\n" \
" v_mfma_f32_16x16x32_bf16 v[100:103], a[228:231], v[52:55], v[100:103]\n" \
" ds_read_b128 v[80:83], v0 offset:4096\n" \
" v_mfma_f32_16x16x32_bf16 v[104:107], v[228:231], v[52:55], v[104:107]\n" \
" s_waitcnt vmcnt(17) lgkmcnt(3)\n" \
" v_mfma_f32_16x16x32_bf16 v[108:111], v[84:87], v[52:55], v[108:111]\n" \
" buffer_load_dwordx4 v[52:55], v3, s[36:39], s57 offen offset:1024 sc1\n" \
" v_mfma_f32_16x16x32_bf16 v[96:99], a[104:107], v[56:59], v[96:99]\n" \
" v_mfma_f32_16x16x32_bf16 v[100:103], a[232:235], v[56:59], v[100:103]\n" \
" ds_read_b128 v[84:87], v0 offset:5120\n" \
" v_mfma_f32_16x16x32_bf16 v[104:107], v[232:235], v[56:59], v[104:107]\n" \
" s_waitcnt vmcnt(16) lgkmcnt(3)\n" \
" v_mfma_f32_16x16x32_bf16 v[108:111], v[88:91], v[56:59], v[108:111]\n" \
" buffer_load_dwordx4 v[56:59], v3, s[36:39], s57 offen offset:2048 sc1\n" \
" v_mfma_f32_16x16x32_bf16 v[96:99], a[108:111], v[60:63], v[96:99]\n" \
" v_mfma_f32_16x16x32_bf16 v[100:103], a[236:239], v[60:63], v[100:103]\n" \
" ds_read_b128 v[88:91], v0 offset:6144\n" \
" v_mfma_f32_16x16x32_bf16 v[104:107], v[236:239], v[60:63], v[104:107]\n" \
" s_waitcnt vmcnt(16) lgkmcnt(3)\n" \
" v_mfma_f32_16x16x32_bf16 v[108:111], v[92:95], v[60:63], v[108:111]\n" \
" buffer_load_dwordx4 v[60:63], v3, s[36:39], s57 offen offset:3072 sc1\n" \
" v_mfma_f32_16x16x32_bf16 v[96:99], a[112:115], v[64:67], v[96:99]\n" \
" v_mfma_f32_16x16x32_bf16 v[100:103], a[240:243], v[64:67], v[100:103]\n" \
" ds_read_b128 v[92:95], v0 offset:7168\n" \
" v_mfma_f32_16x16x32_bf16 v[104:107], v[240:243], v[64:67], v[104:107]\n" \
" s_waitcnt vmcnt(16) lgkmcnt(3)\n" \
" s_mov_b32 s84, 0x4000\n" \
" v_mfma_f32_16x16x32_bf16 v[108:111], v[80:83], v[64:67], v[108:111]\n" \
" s_add_u32 s57, s56, 1597440\n" \
" buffer_load_dwordx4 v[64:67], v3, s[36:39], s57 offen offset:0 sc1\n" \
" v_mfma_f32_16x16x32_bf16 v[96:99], a[116:119], v[68:71], v[96:99]\n" \
" buffer_load_dword v11, v4, s[36:39], 0 offen sc1\n" \
" v_mfma_f32_16x16x32_bf16 v[100:103], a[244:247], v[68:71], v[100:103]\n" \
" ds_read_b128 v[80:83], v0 offset:8192\n" \
" v_mfma_f32_16x16x32_bf16 v[104:107], v[244:247], v[68:71], v[104:107]\n" \
" s_waitcnt vmcnt(17) lgkmcnt(3)\n" \
" v_mov_b32 v13, s80\n" \
" buffer_store_dword v13, v10, s[36:39], s49 offen sc1\n" \
" v_mfma_f32_16x16x32_bf16 v[108:111], v[84:87], v[68:71], v[108:111]\n" \
" buffer_load_dwordx4 v[68:71], v3, s[36:39], s57 offen offset:1024 sc1\n" \
" v_mfma_f32_16x16x32_bf16 v[96:99], a[120:123], v[72:75], v[96:99]\n" \
" s_lshl_b32 s78, s44, 15\n" \
" s_cmp_lt_u32 s55, s47\n" \
" v_mfma_f32_16x16x32_bf16 v[100:103], a[248:251], v[72:75], v[100:103]\n" \
" ds_read_b128 v[84:87], v0 offset:9216\n" \
" s_cselect_b32 s76, s78, 0x7ffffff0\n" \
" s_cselect_b32 s77, 0x7ffffff0, s78\n" \
" v_mfma_f32_16x16x32_bf16 v[104:107], v[248:251], v[72:75], v[104:107]\n" \
" s_waitcnt vmcnt(18) lgkmcnt(3)\n" \
" s_cselect_b32 s79, s46, s55\n" \
" s_sub_u32 s79, s46, s79\n" \
" v_mfma_f32_16x16x32_bf16 v[108:111], v[88:91], v[72:75], v[108:111]\n" \
" buffer_load_dwordx4 v[72:75], v3, s[36:39], s57 offen offset:2048 sc1\n" \
" v_mfma_f32_16x16x32_bf16 v[96:99], a[124:127], v[76:79], v[96:99]\n" \
" s_add_u32 s80, s55, 1\n" \
" s_min_u32 s80, s80, s47\n" \
" v_mfma_f32_16x16x32_bf16 v[100:103], a[252:255], v[76:79], v[100:103]\n" \
" ds_read_b128 v[88:91], v0 offset:10240\n" \
" s_mov_b32 s44, s82\n" \
" s_add_u32 s55, s55, 1\n" \
" v_mfma_f32_16x16x32_bf16 v[104:107], v[252:255], v[76:79], v[104:107]\n" \
" s_waitcnt vmcnt(18) lgkmcnt(3)\n" \
" ds_write_b128 v7, v[96:99] offset:0\n" \
" v_mfma_f32_16x16x32_bf16 v[108:111], v[92:95], v[76:79], v[108:111]\n" \
" buffer_load_dwordx4 v[76:79], v3, s[36:39], s57 offen offset:3072 sc1\n" \
" ds_write_b128 v7, v[100:103] offset:1024\n" \
" s_cmp_eq_u32 s81, 0\n" \
" s_nop 0\n" \
" s_nop 0\n" \
" s_nop 0\n" \
" s_cbranch_scc1 Lswo_loop\n" \
"; cold\n" \
" s_nop 7\n" \
" s_nop 7\n" \
" s_waitcnt vmcnt(0) lgkmcnt(0)\n" \
" ds_write_b128 v7, v[104:107] offset:2048\n" \
" ds_write_b128 v7, v[108:111] offset:3072\n" \
" s_waitcnt lgkmcnt(0)\n" \
" s_barrier\n" \
" v_cmp_gt_u32 vcc, s79, v9\n" \
" s_nop 4\n" \
" s_cbranch_vccnz Lswo_q\n" \
"Lswo_qk:\n" \
" buffer_load_dwordx2 v[14:15], v6, s[36:39], s77 offen sc1\n" \
" buffer_load_dwordx2 v[12:13], v5, s[64:67], s77 offen\n" \
" ds_read_b128 v[112:115], v8 offset:0\n" \
" ds_read_b128 v[116:119], v8 offset:4096\n" \
" ds_read_b128 v[120:123], v8 offset:8192\n" \
" ds_read_b128 v[124:127], v8 offset:12288\n" \
" s_waitcnt lgkmcnt(2)\n" \
" v_add_f32 v112, v112, v116\n" \
" v_add_f32 v113, v113, v117\n" \
" v_add_f32 v114, v114, v118\n" \
" v_add_f32 v115, v115, v119\n" \
" s_waitcnt lgkmcnt(0)\n" \
" v_add_f32 v120, v120, v124\n" \
" v_add_f32 v121, v121, v125\n" \
" v_add_f32 v122, v122, v126\n" \
" v_add_f32 v123, v123, v127\n" \
" v_add_f32 v112, v112, v120\n" \
" v_add_f32 v113, v113, v121\n" \
" v_add_f32 v114, v114, v122\n" \
" v_add_f32 v115, v115, v123\n" \
" v_xor_b32 v8, s84, v8\n" \
" v_cvt_pk_bf16_f32 v116, v112, v113\n" \
" v_cvt_pk_bf16_f32 v117, v114, v115\n" \
" buffer_store_dwordx2 v[116:117], v6, s[36:39], s76 offen sc1\n" \
" s_waitcnt vmcnt(2)\n" \
" v_lshlrev_b32 v120, 16, v14\n" \
" v_and_b32 v121, 0xffff0000, v14\n" \
" s_waitcnt vmcnt(1)\n" \
" v_lshlrev_b32 v124, 16, v12\n" \
" v_and_b32 v125, 0xffff0000, v12\n" \
" v_lshlrev_b32 v122, 16, v15\n" \
" v_and_b32 v123, 0xffff0000, v15\n" \
" v_lshlrev_b32 v126, 16, v13\n" \
" v_and_b32 v127, 0xffff0000, v13\n" \
" v_fmac_f32 v124, v112, v120\n" \
" v_fmac_f32 v125, v113, v121\n" \
" v_fmac_f32 v126, v114, v122\n" \
" v_fmac_f32 v127, v115, v123\n" \
" v_cvt_pk_bf16_f32 v118, v124, v125\n" \
" v_cvt_pk_bf16_f32 v119, v126, v127\n" \
" buffer_store_dwordx2 v[118:119], v5, s[68:71], s77 offen\n" \
" s_waitcnt vmcnt(1)\n" \
" v_mov_b32 v13, s80\n" \
" buffer_store_dword v13, v10, s[36:39], s49 offen sc1\n" \
" v_xor_b32 v7, 0x4000, v7\n" \
" s_waitcnt vmcnt(0) lgkmcnt(0)\n" \
" s_cmp_lt_u32 s55, s46\n" \
" s_cbranch_scc1 Lswo_prime\n" \
" s_branch Lswo_end\n" \
"Lswo_w:\n" \
" s_nop 7\n" \
" s_nop 7\n" \
" s_waitcnt vmcnt(0) lgkmcnt(0)\n" \
" s_mov_b32 s61, 0\n" \
"Lswo_wa:\n" \
" s_cmp_lg_u32 s60, 0\n" \
" s_cbranch_scc1 Lswo_wk\n" \
" buffer_load_dword v11, v4, s[36:39], 0 offen sc1\n" \
" s_waitcnt vmcnt(0)\n" \
" v_cmp_gt_u32_e64 s[62:63], s59, v11\n" \
" s_nop 4\n" \
" s_cmp_eq_u64 s[62:63], 0\n" \
" s_cbranch_scc1 Lswo_wk\n" \
" s_sleep 2\n" \
" s_add_u32 s61, s61, 1\n" \
" s_cmp_lt_u32 s61, 65536\n" \
" s_cbranch_scc1 Lswo_wa\n" \
" s_mov_b32 s60, 1\n" \
" v_mov_b32 v11, 256\n" \
" v_mov_b32 v12, 1\n" \
" buffer_store_dword v12, v11, s[36:39], 0 offen sc1\n" \
" s_waitcnt vmcnt(0) lgkmcnt(0)\n" \
" s_branch Lswo_wk\n" \
"Lswo_p:\n" \
" s_nop 7\n" \
" s_nop 7\n" \
" s_waitcnt vmcnt(0) lgkmcnt(0)\n" \
" s_mov_b32 s61, 0\n" \
"Lswo_pa:\n" \
" s_cmp_lg_u32 s60, 0\n" \
" s_cbranch_scc1 Lswo_pk\n" \
" buffer_load_dword v9, v10, s[36:39], s50 offen sc1\n" \
" s_waitcnt vmcnt(0)\n" \
" v_cmp_gt_u32 vcc, s79, v9\n" \
" s_nop 4\n" \
" s_cbranch_vccz Lswo_pk\n" \
" s_sleep 2\n" \
" s_add_u32 s61, s61, 1\n" \
" s_cmp_lt_u32 s61, 65536\n" \
" s_cbranch_scc1 Lswo_pa\n" \
" s_mov_b32 s60, 1\n" \
" v_mov_b32 v11, 256\n" \
" v_mov_b32 v12, 1\n" \
" buffer_store_dword v12, v11, s[36:39], 0 offen sc1\n" \
" s_waitcnt vmcnt(0) lgkmcnt(0)\n" \
" s_branch Lswo_pk\n" \
"Lswo_q:\n" \
" s_nop 7\n" \
" s_nop 7\n" \
" s_waitcnt vmcnt(0) lgkmcnt(0)\n" \
" s_mov_b32 s61, 0\n" \
"Lswo_qa:\n" \
" s_cmp_lg_u32 s60, 0\n" \
" s_cbranch_scc1 Lswo_qk\n" \
" buffer_load_dword v9, v10, s[36:39], s50 offen sc1\n" \
" s_waitcnt vmcnt(0)\n" \
" v_cmp_gt_u32 vcc, s79, v9\n" \
" s_nop 4\n" \
" s_cbranch_vccz Lswo_qk\n" \
" s_sleep 2\n" \
" s_add_u32 s61, s61, 1\n" \
" s_cmp_lt_u32 s61, 65536\n" \
" s_cbranch_scc1 Lswo_qa\n" \
" s_mov_b32 s60, 1\n" \
" v_mov_b32 v11, 256\n" \
" v_mov_b32 v12, 1\n" \
" buffer_store_dword v12, v11, s[36:39], 0 offen sc1\n" \
" s_waitcnt vmcnt(0) lgkmcnt(0)\n" \
" s_branch Lswo_qk\n" \
"Lswo_end:\n" \
" s_endpgm\n"
#define ASM_S64_G2 \
" s_nop 7\n" \
" s_nop 7\n" \
" s_lshl_b32 s52, s48, 15\n" \
" s_lshl_b32 s53, s48, 13\n" \
" s_add_u32 s53, s53, 98304\n" \
" s_sub_u32 s54, s53, s52\n" \
" v_add_u32 v9, s54, v2\n" \
" s_add_u32 m0, s52, 0\n" \
" s_mov_b32 s54, 0\n" \
" buffer_load_dwordx4 v1, s[40:43], s54 offen lds\n" \
" s_add_u32 m0, s52, 1024\n" \
" s_mov_b32 s54, 16384\n" \
" buffer_load_dwordx4 v1, s[40:43], s54 offen lds\n" \
" s_add_u32 m0, s52, 2048\n" \
" s_mov_b32 s54, 32768\n" \
" buffer_load_dwordx4 v1, s[40:43], s54 offen lds\n" \
" s_add_u32 m0, s52, 3072\n" \
" s_mov_b32 s54, 49152\n" \
" buffer_load_dwordx4 v1, s[40:43], s54 offen lds\n" \
" s_add_u32 m0, s52, 4096\n" \
" s_mov_b32 s54, 65536\n" \
" buffer_load_dwordx4 v1, s[40:43], s54 offen lds\n" \
" s_add_u32 m0, s52, 5120\n" \
" s_mov_b32 s54, 81920\n" \
" buffer_load_dwordx4 v1, s[40:43], s54 offen lds\n" \
" s_add_u32 m0, s52, 6144\n" \
" s_mov_b32 s54, 98304\n" \
" buffer_load_dwordx4 v1, s[40:43], s54 offen lds\n" \
" s_add_u32 m0, s52, 7168\n" \
" s_mov_b32 s54, 114688\n" \
" buffer_load_dwordx4 v1, s[40:43], s54 offen lds\n" \
" s_add_u32 m0, s52, 8192\n" \
" s_mov_b32 s54, 131072\n" \
" buffer_load_dwordx4 v1, s[40:43], s54 offen lds\n" \
" s_add_u32 m0, s52, 9216\n" \
" s_mov_b32 s54, 147456\n" \
" buffer_load_dwordx4 v1, s[40:43], s54 offen lds\n" \
" s_add_u32 m0, s52, 10240\n" \
" s_mov_b32 s54, 163840\n" \
" buffer_load_dwordx4 v1, s[40:43], s54 offen lds\n" \
" s_add_u32 m0, s52, 11264\n" \
" s_mov_b32 s54, 180224\n" \
" buffer_load_dwordx4 v1, s[40:43], s54 offen lds\n" \
" s_add_u32 m0, s52, 12288\n" \
" s_mov_b32 s54, 196608\n" \
" buffer_load_dwordx4 v1, s[40:43], s54 offen lds\n" \
" s_add_u32 m0, s52, 13312\n" \
" s_mov_b32 s54, 212992\n" \
" buffer_load_dwordx4 v1, s[40:43], s54 offen lds\n" \
" s_add_u32 m0, s52, 14336\n" \
" s_mov_b32 s54, 229376\n" \
" buffer_load_dwordx4 v1, s[40:43], s54 offen lds\n" \
" s_add_u32 m0, s52, 15360\n" \
" s_mov_b32 s54, 245760\n" \
" buffer_load_dwordx4 v1, s[40:43], s54 offen lds\n" \
" s_add_u32 m0, s52, 16384\n" \
" s_mov_b32 s54, 262144\n" \
" buffer_load_dwordx4 v1, s[40:43], s54 offen lds\n" \
" s_add_u32 m0, s52, 17408\n" \
" s_mov_b32 s54, 278528\n" \
" buffer_load_dwordx4 v1, s[40:43], s54 offen lds\n" \
" s_add_u32 m0, s52, 18432\n" \
" s_mov_b32 s54, 294912\n" \
" buffer_load_dwordx4 v1, s[40:43], s54 offen lds\n" \
" s_add_u32 m0, s52, 19456\n" \
" s_mov_b32 s54, 311296\n" \
" buffer_load_dwordx4 v1, s[40:43], s54 offen lds\n" \
" s_add_u32 m0, s52, 20480\n" \
" s_mov_b32 s54, 327680\n" \
" buffer_load_dwordx4 v1, s[40:43], s54 offen lds\n" \
" s_add_u32 m0, s52, 21504\n" \
" s_mov_b32 s54, 344064\n" \
" buffer_load_dwordx4 v1, s[40:43], s54 offen lds\n" \
" s_add_u32 m0, s52, 22528\n" \
" s_mov_b32 s54, 360448\n" \
" buffer_load_dwordx4 v1, s[40:43], s54 offen lds\n" \
" s_add_u32 m0, s52, 23552\n" \
" s_mov_b32 s54, 376832\n" \
" buffer_load_dwordx4 v1, s[40:43], s54 offen lds\n" \
" s_add_u32 m0, s52, 24576\n" \
" s_mov_b32 s54, 393216\n" \
" buffer_load_dwordx4 v1, s[40:43], s54 offen lds\n" \
" s_add_u32 m0, s52, 25600\n" \
" s_mov_b32 s54, 409600\n" \
" buffer_load_dwordx4 v1, s[40:43], s54 offen lds\n" \
" s_add_u32 m0, s52, 26624\n" \
" s_mov_b32 s54, 425984\n" \
" buffer_load_dwordx4 v1, s[40:43], s54 offen lds\n" \
" s_add_u32 m0, s52, 27648\n" \
" s_mov_b32 s54, 442368\n" \
" buffer_load_dwordx4 v1, s[40:43], s54 offen lds\n" \
" s_add_u32 m0, s52, 28672\n" \
" s_mov_b32 s54, 458752\n" \
" buffer_load_dwordx4 v1, s[40:43], s54 offen lds\n" \
" s_add_u32 m0, s52, 29696\n" \
" s_mov_b32 s54, 475136\n" \
" buffer_load_dwordx4 v1, s[40:43], s54 offen lds\n" \
" s_add_u32 m0, s52, 30720\n" \
" s_mov_b32 s54, 491520\n" \
" buffer_load_dwordx4 v1, s[40:43], s54 offen lds\n" \
" s_add_u32 m0, s52, 31744\n" \
" s_mov_b32 s54, 507904\n" \
" buffer_load_dwordx4 v1, s[40:43], s54 offen lds\n" \
" s_add_u32 m0, s53, 32768\n" \
" s_mov_b32 s54, 524288\n" \
" buffer_load_dwordx4 v1, s[40:43], s54 offen lds\n" \
" s_add_u32 m0, s53, 33792\n" \
" s_mov_b32 s54, 540672\n" \
" buffer_load_dwordx4 v1, s[40:43], s54 offen lds\n" \
" s_add_u32 m0, s53, 34816\n" \
" s_mov_b32 s54, 557056\n" \
" buffer_load_dwordx4 v1, s[40:43], s54 offen lds\n" \
" s_add_u32 m0, s53, 35840\n" \
" s_mov_b32 s54, 573440\n" \
" buffer_load_dwordx4 v1, s[40:43], s54 offen lds\n" \
" s_add_u32 m0, s53, 36864\n" \
" s_mov_b32 s54, 589824\n" \
" buffer_load_dwordx4 v1, s[40:43], s54 offen lds\n" \
" s_add_u32 m0, s53, 37888\n" \
" s_mov_b32 s54, 606208\n" \
" buffer_load_dwordx4 v1, s[40:43], s54 offen lds\n" \
" s_add_u32 m0, s53, 38912\n" \
" s_mov_b32 s54, 622592\n" \
" buffer_load_dwordx4 v1, s[40:43], s54 offen lds\n" \
" s_add_u32 m0, s53, 39936\n" \
" s_mov_b32 s54, 638976\n" \
" buffer_load_dwordx4 v1, s[40:43], s54 offen lds\n" \
" s_waitcnt vmcnt(36)\n" \
" ds_read_b64_tr_b16 a[0:1], v2 offset:0\n" \
" ds_read_b64_tr_b16 a[2:3], v2 offset:512\n" \
" ds_read_b64_tr_b16 a[128:129], v2 offset:32\n" \
" ds_read_b64_tr_b16 a[130:131], v2 offset:544\n" \
" ds_read_b64_tr_b16 v[128:129], v2 offset:64\n" \
" ds_read_b64_tr_b16 v[130:131], v2 offset:576\n" \
" ds_read_b64_tr_b16 v[16:17], v2 offset:96\n" \
" ds_read_b64_tr_b16 v[18:19], v2 offset:608\n" \
" s_waitcnt lgkmcnt(0)\n" \
" s_add_u32 m0, s52, 0\n" \
" s_mov_b32 s54, 655360\n" \
" buffer_load_dwordx4 v1, s[40:43], s54 offen lds\n" \
" s_add_u32 m0, s52, 1024\n" \
" s_mov_b32 s54, 671744\n" \
" buffer_load_dwordx4 v1, s[40:43], s54 offen lds\n" \
" s_add_u32 m0, s52, 2048\n" \
" s_mov_b32 s54, 688128\n" \
" buffer_load_dwordx4 v1, s[40:43], s54 offen lds\n" \
" s_add_u32 m0, s52, 3072\n" \
" s_mov_b32 s54, 704512\n" \
" buffer_load_dwordx4 v1, s[40:43], s54 offen lds\n" \
" s_waitcnt vmcnt(36)\n" \
" ds_read_b64_tr_b16 a[4:5], v2 offset:4096\n" \
" ds_read_b64_tr_b16 a[6:7], v2 offset:4608\n" \
" ds_read_b64_tr_b16 a[132:133], v2 offset:4128\n" \
" ds_read_b64_tr_b16 a[134:135], v2 offset:4640\n" \
" ds_read_b64_tr_b16 v[132:133], v2 offset:4160\n" \
" ds_read_b64_tr_b16 v[134:135], v2 offset:4672\n" \
" ds_read_b64_tr_b16 v[20:21], v2 offset:4192\n" \
" ds_read_b64_tr_b16 v[22:23], v2 offset:4704\n" \
" s_waitcnt lgkmcnt(0)\n" \
" s_add_u32 m0, s52, 4096\n" \
" s_mov_b32 s54, 720896\n" \
" buffer_load_dwordx4 v1, s[40:43], s54 offen lds\n" \
" s_add_u32 m0, s52, 5120\n" \
" s_mov_b32 s54, 737280\n" \
" buffer_load_dwordx4 v1, s[40:43], s54 offen lds\n" \
" s_add_u32 m0, s52, 6144\n" \
" s_mov_b32 s54, 753664\n" \
" buffer_load_dwordx4 v1, s[40:43], s54 offen lds\n" \
" s_add_u32 m0, s52, 7168\n" \
" s_mov_b32 s54, 770048\n" \
" buffer_load_dwordx4 v1, s[40:43], s54 offen lds\n" \
" s_waitcnt vmcnt(36)\n" \
" ds_read_b64_tr_b16 a[8:9], v2 offset:8192\n" \
" ds_read_b64_tr_b16 a[10:11], v2 offset:8704\n" \
" ds_read_b64_tr_b16 a[136:137], v2 offset:8224\n" \
" ds_read_b64_tr_b16 a[138:139], v2 offset:8736\n" \
" ds_read_b64_tr_b16 v[136:137], v2 offset:8256\n" \
" ds_read_b64_tr_b16 v[138:139], v2 offset:8768\n" \
" ds_read_b64_tr_b16 v[24:25], v2 offset:8288\n" \
" ds_read_b64_tr_b16 v[26:27], v2 offset:8800\n" \
" s_waitcnt lgkmcnt(0)\n" \
" s_add_u32 m0, s52, 8192\n" \
" s_mov_b32 s54, 786432\n" \
" buffer_load_dwordx4 v1, s[40:43], s54 offen lds\n" \
" s_add_u32 m0, s52, 9216\n" \
" s_mov_b32 s54, 802816\n" \
" buffer_load_dwordx4 v1, s[40:43], s54 offen lds\n" \
" s_add_u32 m0, s52, 10240\n" \
" s_mov_b32 s54, 819200\n" \
" buffer_load_dwordx4 v1, s[40:43], s54 offen lds\n" \
" s_add_u32 m0, s52, 11264\n" \
" s_mov_b32 s54, 835584\n" \
" buffer_load_dwordx4 v1, s[40:43], s54 offen lds\n" \
" s_waitcnt vmcnt(36)\n" \
" ds_read_b64_tr_b16 a[12:13], v2 offset:12288\n" \
" ds_read_b64_tr_b16 a[14:15], v2 offset:12800\n" \
" ds_read_b64_tr_b16 a[140:141], v2 offset:12320\n" \
" ds_read_b64_tr_b16 a[142:143], v2 offset:12832\n" \
" ds_read_b64_tr_b16 v[140:141], v2 offset:12352\n" \
" ds_read_b64_tr_b16 v[142:143], v2 offset:12864\n" \
" ds_read_b64_tr_b16 v[28:29], v2 offset:12384\n" \
" ds_read_b64_tr_b16 v[30:31], v2 offset:12896\n" \
" s_waitcnt lgkmcnt(0)\n" \
" s_add_u32 m0, s52, 12288\n" \
" s_mov_b32 s54, 851968\n" \
" buffer_load_dwordx4 v1, s[40:43], s54 offen lds\n" \
" s_add_u32 m0, s52, 13312\n" \
" s_mov_b32 s54, 868352\n" \
" buffer_load_dwordx4 v1, s[40:43], s54 offen lds\n" \
" s_add_u32 m0, s52, 14336\n" \
" s_mov_b32 s54, 884736\n" \
" buffer_load_dwordx4 v1, s[40:43], s54 offen lds\n" \
" s_add_u32 m0, s52, 15360\n" \
" s_mov_b32 s54, 901120\n" \
" buffer_load_dwordx4 v1, s[40:43], s54 offen lds\n" \
" s_waitcnt vmcnt(36)\n" \
" ds_read_b64_tr_b16 a[16:17], v2 offset:16384\n" \
" ds_read_b64_tr_b16 a[18:19], v2 offset:16896\n" \
" ds_read_b64_tr_b16 a[144:145], v2 offset:16416\n" \
" ds_read_b64_tr_b16 a[146:147], v2 offset:16928\n" \
" ds_read_b64_tr_b16 v[144:145], v2 offset:16448\n" \
" ds_read_b64_tr_b16 v[146:147], v2 offset:16960\n" \
" ds_read_b64_tr_b16 v[32:33], v2 offset:16480\n" \
" ds_read_b64_tr_b16 v[34:35], v2 offset:16992\n" \
" s_waitcnt lgkmcnt(0)\n" \
" s_add_u32 m0, s52, 16384\n" \
" s_mov_b32 s54, 917504\n" \
" buffer_load_dwordx4 v1, s[40:43], s54 offen lds\n" \
" s_add_u32 m0, s52, 17408\n" \
" s_mov_b32 s54, 933888\n" \
" buffer_load_dwordx4 v1, s[40:43], s54 offen lds\n" \
" s_add_u32 m0, s52, 18432\n" \
" s_mov_b32 s54, 950272\n" \
" buffer_load_dwordx4 v1, s[40:43], s54 offen lds\n" \
" s_add_u32 m0, s52, 19456\n" \
" s_mov_b32 s54, 966656\n" \
" buffer_load_dwordx4 v1, s[40:43], s54 offen lds\n" \
" s_waitcnt vmcnt(36)\n" \
" ds_read_b64_tr_b16 a[20:21], v2 offset:20480\n" \
" ds_read_b64_tr_b16 a[22:23], v2 offset:20992\n" \
" ds_read_b64_tr_b16 a[148:149], v2 offset:20512\n" \
" ds_read_b64_tr_b16 a[150:151], v2 offset:21024\n" \
" ds_read_b64_tr_b16 v[148:149], v2 offset:20544\n" \
" ds_read_b64_tr_b16 v[150:151], v2 offset:21056\n" \
" ds_read_b64_tr_b16 v[36:37], v2 offset:20576\n" \
" ds_read_b64_tr_b16 v[38:39], v2 offset:21088\n" \
" s_waitcnt lgkmcnt(0)\n" \
" s_add_u32 m0, s52, 20480\n" \
" s_mov_b32 s54, 983040\n" \
" buffer_load_dwordx4 v1, s[40:43], s54 offen lds\n" \
" s_add_u32 m0, s52, 21504\n" \
" s_mov_b32 s54, 999424\n" \
" buffer_load_dwordx4 v1, s[40:43], s54 offen lds\n" \
" s_add_u32 m0, s52, 22528\n" \
" s_mov_b32 s54, 1015808\n" \
" buffer_load_dwordx4 v1, s[40:43], s54 offen lds\n" \
" s_add_u32 m0, s52, 23552\n" \
" s_mov_b32 s54, 1032192\n" \
" buffer_load_dwordx4 v1, s[40:43], s54 offen lds\n" \
" s_waitcnt vmcnt(36)\n" \
" ds_read_b64_tr_b16 a[24:25], v2 offset:24576\n" \
" ds_read_b64_tr_b16 a[26:27], v2 offset:25088\n" \
" ds_read_b64_tr_b16 a[152:153], v2 offset:24608\n" \
" ds_read_b64_tr_b16 a[154:155], v2 offset:25120\n" \
" ds_read_b64_tr_b16 v[152:153], v2 offset:24640\n" \
" ds_read_b64_tr_b16 v[154:155], v2 offset:25152\n" \
" ds_read_b64_tr_b16 v[40:41], v2 offset:24672\n" \
" ds_read_b64_tr_b16 v[42:43], v2 offset:25184\n" \
" s_waitcnt lgkmcnt(0)\n" \
" s_add_u32 m0, s52, 24576\n" \
" s_mov_b32 s54, 1048576\n" \
" buffer_load_dwordx4 v1, s[40:43], s54 offen lds\n" \
" s_add_u32 m0, s52, 25600\n" \
" s_mov_b32 s54, 1064960\n" \
" buffer_load_dwordx4 v1, s[40:43], s54 offen lds\n" \
" s_add_u32 m0, s52, 26624\n" \
" s_mov_b32 s54, 1081344\n" \
" buffer_load_dwordx4 v1, s[40:43], s54 offen lds\n" \
" s_add_u32 m0, s52, 27648\n" \
" s_mov_b32 s54, 1097728\n" \
" buffer_load_dwordx4 v1, s[40:43], s54 offen lds\n" \
" s_waitcnt vmcnt(36)\n" \
" ds_read_b64_tr_b16 a[28:29], v2 offset:28672\n" \
" ds_read_b64_tr_b16 a[30:31], v2 offset:29184\n" \
" ds_read_b64_tr_b16 a[156:157], v2 offset:28704\n" \
" ds_read_b64_tr_b16 a[158:159], v2 offset:29216\n" \
" ds_read_b64_tr_b16 v[156:157], v2 offset:28736\n" \
" ds_read_b64_tr_b16 v[158:159], v2 offset:29248\n" \
" ds_read_b64_tr_b16 v[44:45], v2 offset:28768\n" \
" ds_read_b64_tr_b16 v[46:47], v2 offset:29280\n" \
" s_waitcnt lgkmcnt(0)\n" \
" s_add_u32 m0, s52, 28672\n" \
" s_mov_b32 s54, 1114112\n" \
" buffer_load_dwordx4 v1, s[40:43], s54 offen lds\n" \
" s_add_u32 m0, s52, 29696\n" \
" s_mov_b32 s54, 1130496\n" \
" buffer_load_dwordx4 v1, s[40:43], s54 offen lds\n" \
" s_add_u32 m0, s52, 30720\n" \
" s_mov_b32 s54, 1146880\n" \
" buffer_load_dwordx4 v1, s[40:43], s54 offen lds\n" \
" s_add_u32 m0, s52, 31744\n" \
" s_mov_b32 s54, 1163264\n" \
" buffer_load_dwordx4 v1, s[40:43], s54 offen lds\n" \
" s_waitcnt vmcnt(36)\n" \
" ds_read_b64_tr_b16 a[32:33], v9 offset:32768\n" \
" ds_read_b64_tr_b16 a[34:35], v9 offset:33280\n" \
" ds_read_b64_tr_b16 a[160:161], v9 offset:32800\n" \
" ds_read_b64_tr_b16 a[162:163], v9 offset:33312\n" \
" ds_read_b64_tr_b16 v[160:161], v9 offset:32832\n" \
" ds_read_b64_tr_b16 v[162:163], v9 offset:33344\n" \
" ds_read_b64_tr_b16 v[48:49], v9 offset:32864\n" \
" ds_read_b64_tr_b16 v[50:51], v9 offset:33376\n" \
" s_waitcnt lgkmcnt(0)\n" \
" s_add_u32 m0, s53, 32768\n" \
" s_mov_b32 s54, 1179648\n" \
" buffer_load_dwordx4 v1, s[40:43], s54 offen lds\n" \
" s_add_u32 m0, s53, 33792\n" \
" s_mov_b32 s54, 1196032\n" \
" buffer_load_dwordx4 v1, s[40:43], s54 offen lds\n" \
" s_add_u32 m0, s53, 34816\n" \
" s_mov_b32 s54, 1212416\n" \
" buffer_load_dwordx4 v1, s[40:43], s54 offen lds\n" \
" s_add_u32 m0, s53, 35840\n" \
" s_mov_b32 s54, 1228800\n" \
" buffer_load_dwordx4 v1, s[40:43], s54 offen lds\n" \
" s_waitcnt vmcnt(36)\n" \
" ds_read_b64_tr_b16 a[36:37], v9 offset:36864\n" \
" ds_read_b64_tr_b16 a[38:39], v9 offset:37376\n" \
" ds_read_b64_tr_b16 a[164:165], v9 offset:36896\n" \
" ds_read_b64_tr_b16 a[166:167], v9 offset:37408\n" \
" ds_read_b64_tr_b16 v[164:165], v9 offset:36928\n" \
" ds_read_b64_tr_b16 v[166:167], v9 offset:37440\n" \
" ds_read_b64_tr_b16 v[52:53], v9 offset:36960\n" \
" ds_read_b64_tr_b16 v[54:55], v9 offset:37472\n" \
" s_waitcnt lgkmcnt(0)\n" \
" s_add_u32 m0, s53, 36864\n" \
" s_mov_b32 s54, 1245184\n" \
" buffer_load_dwordx4 v1, s[40:43], s54 offen lds\n" \
" s_add_u32 m0, s53, 37888\n" \
" s_mov_b32 s54, 1261568\n" \
" buffer_load_dwordx4 v1, s[40:43], s54 offen lds\n" \
" s_add_u32 m0, s53, 38912\n" \
" s_mov_b32 s54, 1277952\n" \
" buffer_load_dwordx4 v1, s[40:43], s54 offen lds\n" \
" s_add_u32 m0, s53, 39936\n" \
" s_mov_b32 s54, 1294336\n" \
" buffer_load_dwordx4 v1, s[40:43], s54 offen lds\n" \
" s_waitcnt vmcnt(36)\n" \
" ds_read_b64_tr_b16 a[40:41], v2 offset:0\n" \
" ds_read_b64_tr_b16 a[42:43], v2 offset:512\n" \
" ds_read_b64_tr_b16 a[168:169], v2 offset:32\n" \
" ds_read_b64_tr_b16 a[170:171], v2 offset:544\n" \
" ds_read_b64_tr_b16 v[168:169], v2 offset:64\n" \
" ds_read_b64_tr_b16 v[170:171], v2 offset:576\n" \
" ds_read_b64_tr_b16 v[56:57], v2 offset:96\n" \
" ds_read_b64_tr_b16 v[58:59], v2 offset:608\n" \
" s_waitcnt lgkmcnt(0)\n" \
" s_add_u32 m0, s52, 0\n" \
" s_mov_b32 s54, 1310720\n" \
" buffer_load_dwordx4 v1, s[40:43], s54 offen lds\n" \
" s_add_u32 m0, s52, 1024\n" \
" s_mov_b32 s54, 1327104\n" \
" buffer_load_dwordx4 v1, s[40:43], s54 offen lds\n" \
" s_add_u32 m0, s52, 2048\n" \
" s_mov_b32 s54, 1343488\n" \
" buffer_load_dwordx4 v1, s[40:43], s54 offen lds\n" \
" s_add_u32 m0, s52, 3072\n" \
" s_mov_b32 s54, 1359872\n" \
" buffer_load_dwordx4 v1, s[40:43], s54 offen lds\n" \
" s_waitcnt vmcnt(36)\n" \
" ds_read_b64_tr_b16 a[44:45], v2 offset:4096\n" \
" ds_read_b64_tr_b16 a[46:47], v2 offset:4608\n" \
" ds_read_b64_tr_b16 a[172:173], v2 offset:4128\n" \
" ds_read_b64_tr_b16 a[174:175], v2 offset:4640\n" \
" ds_read_b64_tr_b16 v[172:173], v2 offset:4160\n" \
" ds_read_b64_tr_b16 v[174:175], v2 offset:4672\n" \
" ds_read_b64_tr_b16 v[60:61], v2 offset:4192\n" \
" ds_read_b64_tr_b16 v[62:63], v2 offset:4704\n" \
" s_waitcnt lgkmcnt(0)\n" \
" s_add_u32 m0, s52, 4096\n" \
" s_mov_b32 s54, 1376256\n" \
" buffer_load_dwordx4 v1, s[40:43], s54 offen lds\n" \
" s_add_u32 m0, s52, 5120\n" \
" s_mov_b32 s54, 1392640\n" \
" buffer_load_dwordx4 v1, s[40:43], s54 offen lds\n" \
" s_add_u32 m0, s52, 6144\n" \
" s_mov_b32 s54, 1409024\n" \
" buffer_load_dwordx4 v1, s[40:43], s54 offen lds\n" \
" s_add_u32 m0, s52, 7168\n" \
" s_mov_b32 s54, 1425408\n" \
" buffer_load_dwordx4 v1, s[40:43], s54 offen lds\n" \
" s_waitcnt vmcnt(36)\n" \
" ds_read_b64_tr_b16 a[48:49], v2 offset:8192\n" \
" ds_read_b64_tr_b16 a[50:51], v2 offset:8704\n" \
" ds_read_b64_tr_b16 a[176:177], v2 offset:8224\n" \
" ds_read_b64_tr_b16 a[178:179], v2 offset:8736\n" \
" ds_read_b64_tr_b16 v[176:177], v2 offset:8256\n" \
" ds_read_b64_tr_b16 v[178:179], v2 offset:8768\n" \
" ds_read_b64_tr_b16 v[64:65], v2 offset:8288\n" \
" ds_read_b64_tr_b16 v[66:67], v2 offset:8800\n" \
" s_waitcnt lgkmcnt(0)\n" \
" s_add_u32 m0, s52, 8192\n" \
" s_mov_b32 s54, 1441792\n" \
" buffer_load_dwordx4 v1, s[40:43], s54 offen lds\n" \
" s_add_u32 m0, s52, 9216\n" \
" s_mov_b32 s54, 1458176\n" \
" buffer_load_dwordx4 v1, s[40:43], s54 offen lds\n" \
" s_add_u32 m0, s52, 10240\n" \
" s_mov_b32 s54, 1474560\n" \
" buffer_load_dwordx4 v1, s[40:43], s54 offen lds\n" \
" s_add_u32 m0, s52, 11264\n" \
" s_mov_b32 s54, 1490944\n" \
" buffer_load_dwordx4 v1, s[40:43], s54 offen lds\n" \
" s_waitcnt vmcnt(36)\n" \
" ds_read_b64_tr_b16 a[52:53], v2 offset:12288\n" \
" ds_read_b64_tr_b16 a[54:55], v2 offset:12800\n" \
" ds_read_b64_tr_b16 a[180:181], v2 offset:12320\n" \
" ds_read_b64_tr_b16 a[182:183], v2 offset:12832\n" \
" ds_read_b64_tr_b16 v[180:181], v2 offset:12352\n" \
" ds_read_b64_tr_b16 v[182:183], v2 offset:12864\n" \
" ds_read_b64_tr_b16 v[68:69], v2 offset:12384\n" \
" ds_read_b64_tr_b16 v[70:71], v2 offset:12896\n" \
" s_waitcnt lgkmcnt(0)\n" \
" s_add_u32 m0, s52, 12288\n" \
" s_mov_b32 s54, 1507328\n" \
" buffer_load_dwordx4 v1, s[40:43], s54 offen lds\n" \
" s_add_u32 m0, s52, 13312\n" \
" s_mov_b32 s54, 1523712\n" \
" buffer_load_dwordx4 v1, s[40:43], s54 offen lds\n" \
" s_add_u32 m0, s52, 14336\n" \
" s_mov_b32 s54, 1540096\n" \
" buffer_load_dwordx4 v1, s[40:43], s54 offen lds\n" \
" s_add_u32 m0, s52, 15360\n" \
" s_mov_b32 s54, 1556480\n" \
" buffer_load_dwordx4 v1, s[40:43], s54 offen lds\n" \
" s_waitcnt vmcnt(36)\n" \
" ds_read_b64_tr_b16 a[56:57], v2 offset:16384\n" \
" ds_read_b64_tr_b16 a[58:59], v2 offset:16896\n" \
" ds_read_b64_tr_b16 a[184:185], v2 offset:16416\n" \
" ds_read_b64_tr_b16 a[186:187], v2 offset:16928\n" \
" ds_read_b64_tr_b16 v[184:185], v2 offset:16448\n" \
" ds_read_b64_tr_b16 v[186:187], v2 offset:16960\n" \
" ds_read_b64_tr_b16 v[72:73], v2 offset:16480\n" \
" ds_read_b64_tr_b16 v[74:75], v2 offset:16992\n" \
" s_waitcnt lgkmcnt(0)\n" \
" s_add_u32 m0, s52, 16384\n" \
" s_mov_b32 s54, 1572864\n" \
" buffer_load_dwordx4 v1, s[40:43], s54 offen lds\n" \
" s_add_u32 m0, s52, 17408\n" \
" s_mov_b32 s54, 1589248\n" \
" buffer_load_dwordx4 v1, s[40:43], s54 offen lds\n" \
" s_add_u32 m0, s52, 18432\n" \
" s_mov_b32 s54, 1605632\n" \
" buffer_load_dwordx4 v1, s[40:43], s54 offen lds\n" \
" s_add_u32 m0, s52, 19456\n" \
" s_mov_b32 s54, 1622016\n" \
" buffer_load_dwordx4 v1, s[40:43], s54 offen lds\n" \
" s_waitcnt vmcnt(36)\n" \
" ds_read_b64_tr_b16 a[60:61], v2 offset:20480\n" \
" ds_read_b64_tr_b16 a[62:63], v2 offset:20992\n" \
" ds_read_b64_tr_b16 a[188:189], v2 offset:20512\n" \
" ds_read_b64_tr_b16 a[190:191], v2 offset:21024\n" \
" ds_read_b64_tr_b16 v[188:189], v2 offset:20544\n" \
" ds_read_b64_tr_b16 v[190:191], v2 offset:21056\n" \
" ds_read_b64_tr_b16 v[76:77], v2 offset:20576\n" \
" ds_read_b64_tr_b16 v[78:79], v2 offset:21088\n" \
" s_waitcnt lgkmcnt(0)\n" \
" s_add_u32 m0, s52, 20480\n" \
" s_mov_b32 s54, 1638400\n" \
" buffer_load_dwordx4 v1, s[40:43], s54 offen lds\n" \
" s_add_u32 m0, s52, 21504\n" \
" s_mov_b32 s54, 1654784\n" \
" buffer_load_dwordx4 v1, s[40:43], s54 offen lds\n" \
" s_add_u32 m0, s52, 22528\n" \
" s_mov_b32 s54, 1671168\n" \
" buffer_load_dwordx4 v1, s[40:43], s54 offen lds\n" \
" s_add_u32 m0, s52, 23552\n" \
" s_mov_b32 s54, 1687552\n" \
" buffer_load_dwordx4 v1, s[40:43], s54 offen lds\n" \
" s_waitcnt vmcnt(36)\n" \
" ds_read_b64_tr_b16 a[64:65], v2 offset:24576\n" \
" ds_read_b64_tr_b16 a[66:67], v2 offset:25088\n" \
" ds_read_b64_tr_b16 a[192:193], v2 offset:24608\n" \
" ds_read_b64_tr_b16 a[194:195], v2 offset:25120\n" \
" ds_read_b64_tr_b16 v[192:193], v2 offset:24640\n" \
" ds_read_b64_tr_b16 v[194:195], v2 offset:25152\n" \
" ds_read_b64_tr_b16 v[80:81], v2 offset:24672\n" \
" ds_read_b64_tr_b16 v[82:83], v2 offset:25184\n" \
" s_waitcnt lgkmcnt(0)\n" \
" s_add_u32 m0, s52, 24576\n" \
" s_mov_b32 s54, 1703936\n" \
" buffer_load_dwordx4 v1, s[40:43], s54 offen lds\n" \
" s_add_u32 m0, s52, 25600\n" \
" s_mov_b32 s54, 1720320\n" \
" buffer_load_dwordx4 v1, s[40:43], s54 offen lds\n" \
" s_add_u32 m0, s52, 26624\n" \
" s_mov_b32 s54, 1736704\n" \
" buffer_load_dwordx4 v1, s[40:43], s54 offen lds\n" \
" s_add_u32 m0, s52, 27648\n" \
" s_mov_b32 s54, 1753088\n" \
" buffer_load_dwordx4 v1, s[40:43], s54 offen lds\n" \
" s_waitcnt vmcnt(36)\n" \
" ds_read_b64_tr_b16 a[68:69], v2 offset:28672\n" \
" ds_read_b64_tr_b16 a[70:71], v2 offset:29184\n" \
" ds_read_b64_tr_b16 a[196:197], v2 offset:28704\n" \
" ds_read_b64_tr_b16 a[198:199], v2 offset:29216\n" \
" ds_read_b64_tr_b16 v[196:197], v2 offset:28736\n" \
" ds_read_b64_tr_b16 v[198:199], v2 offset:29248\n" \
" ds_read_b64_tr_b16 v[84:85], v2 offset:28768\n" \
" ds_read_b64_tr_b16 v[86:87], v2 offset:29280\n" \
" s_waitcnt lgkmcnt(0)\n" \
" s_add_u32 m0, s52, 28672\n" \
" s_mov_b32 s54, 1769472\n" \
" buffer_load_dwordx4 v1, s[40:43], s54 offen lds\n" \
" s_add_u32 m0, s52, 29696\n" \
" s_mov_b32 s54, 1785856\n" \
" buffer_load_dwordx4 v1, s[40:43], s54 offen lds\n" \
" s_add_u32 m0, s52, 30720\n" \
" s_mov_b32 s54, 1802240\n" \
" buffer_load_dwordx4 v1, s[40:43], s54 offen lds\n" \
" s_add_u32 m0, s52, 31744\n" \
" s_mov_b32 s54, 1818624\n" \
" buffer_load_dwordx4 v1, s[40:43], s54 offen lds\n" \
" s_waitcnt vmcnt(36)\n" \
" ds_read_b64_tr_b16 a[72:73], v9 offset:32768\n" \
" ds_read_b64_tr_b16 a[74:75], v9 offset:33280\n" \
" ds_read_b64_tr_b16 a[200:201], v9 offset:32800\n" \
" ds_read_b64_tr_b16 a[202:203], v9 offset:33312\n" \
" ds_read_b64_tr_b16 v[200:201], v9 offset:32832\n" \
" ds_read_b64_tr_b16 v[202:203], v9 offset:33344\n" \
" ds_read_b64_tr_b16 v[88:89], v9 offset:32864\n" \
" ds_read_b64_tr_b16 v[90:91], v9 offset:33376\n" \
" s_waitcnt lgkmcnt(0)\n" \
" s_waitcnt vmcnt(32)\n" \
" ds_read_b64_tr_b16 a[76:77], v9 offset:36864\n" \
" ds_read_b64_tr_b16 a[78:79], v9 offset:37376\n" \
" ds_read_b64_tr_b16 a[204:205], v9 offset:36896\n" \
" ds_read_b64_tr_b16 a[206:207], v9 offset:37408\n" \
" ds_read_b64_tr_b16 v[204:205], v9 offset:36928\n" \
" ds_read_b64_tr_b16 v[206:207], v9 offset:37440\n" \
" ds_read_b64_tr_b16 v[92:93], v9 offset:36960\n" \
" ds_read_b64_tr_b16 v[94:95], v9 offset:37472\n" \
" s_waitcnt lgkmcnt(0)\n" \
" s_waitcnt vmcnt(28)\n" \
" ds_read_b64_tr_b16 a[80:81], v2 offset:0\n" \
" ds_read_b64_tr_b16 a[82:83], v2 offset:512\n" \
" ds_read_b64_tr_b16 a[208:209], v2 offset:32\n" \
" ds_read_b64_tr_b16 a[210:211], v2 offset:544\n" \
" ds_read_b64_tr_b16 v[208:209], v2 offset:64\n" \
" ds_read_b64_tr_b16 v[210:211], v2 offset:576\n" \
" ds_read_b64_tr_b16 v[96:97], v2 offset:96\n" \
" ds_read_b64_tr_b16 v[98:99], v2 offset:608\n" \
" s_waitcnt lgkmcnt(0)\n" \
" ds_write_b128 v0, v[16:19] offset:0\n" \
" ds_write_b128 v0, v[20:23] offset:1024\n" \
" ds_write_b128 v0, v[24:27] offset:2048\n" \
" ds_write_b128 v0, v[28:31] offset:3072\n" \
" s_waitcnt vmcnt(24)\n" \
" ds_read_b64_tr_b16 a[84:85], v2 offset:4096\n" \
" ds_read_b64_tr_b16 a[86:87], v2 offset:4608\n" \
" ds_read_b64_tr_b16 a[212:213], v2 offset:4128\n" \
" ds_read_b64_tr_b16 a[214:215], v2 offset:4640\n" \
" ds_read_b64_tr_b16 v[212:213], v2 offset:4160\n" \
" ds_read_b64_tr_b16 v[214:215], v2 offset:4672\n" \
" ds_read_b64_tr_b16 v[100:101], v2 offset:4192\n" \
" ds_read_b64_tr_b16 v[102:103], v2 offset:4704\n" \
" s_waitcnt lgkmcnt(0)\n" \
" ds_write_b128 v0, v[32:35] offset:4096\n" \
" ds_write_b128 v0, v[36:39] offset:5120\n" \
" ds_write_b128 v0, v[40:43] offset:6144\n" \
" ds_write_b128 v0, v[44:47] offset:7168\n" \
" s_waitcnt vmcnt(20)\n" \
" ds_read_b64_tr_b16 a[88:89], v2 offset:8192\n" \
" ds_read_b64_tr_b16 a[90:91], v2 offset:8704\n" \
" ds_read_b64_tr_b16 a[216:217], v2 offset:8224\n" \
" ds_read_b64_tr_b16 a[218:219], v2 offset:8736\n" \
" ds_read_b64_tr_b16 v[216:217], v2 offset:8256\n" \
" ds_read_b64_tr_b16 v[218:219], v2 offset:8768\n" \
" ds_read_b64_tr_b16 v[104:105], v2 offset:8288\n" \
" ds_read_b64_tr_b16 v[106:107], v2 offset:8800\n" \
" s_waitcnt lgkmcnt(0)\n" \
" ds_write_b128 v0, v[48:51] offset:8192\n" \
" ds_write_b128 v0, v[52:55] offset:9216\n" \
" ds_write_b128 v0, v[56:59] offset:10240\n" \
" ds_write_b128 v0, v[60:63] offset:11264\n" \
" s_waitcnt vmcnt(16)\n" \
" ds_read_b64_tr_b16 a[92:93], v2 offset:12288\n" \
" ds_read_b64_tr_b16 a[94:95], v2 offset:12800\n" \
" ds_read_b64_tr_b16 a[220:221], v2 offset:12320\n" \
" ds_read_b64_tr_b16 a[222:223], v2 offset:12832\n" \
" ds_read_b64_tr_b16 v[220:221], v2 offset:12352\n" \
" ds_read_b64_tr_b16 v[222:223], v2 offset:12864\n" \
" ds_read_b64_tr_b16 v[108:109], v2 offset:12384\n" \
" ds_read_b64_tr_b16 v[110:111], v2 offset:12896\n" \
" s_waitcnt lgkmcnt(0)\n" \
" ds_write_b128 v0, v[64:67] offset:12288\n" \
" ds_write_b128 v0, v[68:71] offset:13312\n" \
" ds_write_b128 v0, v[72:75] offset:14336\n" \
" ds_write_b128 v0, v[76:79] offset:15360\n" \
" s_waitcnt vmcnt(12)\n" \
" ds_read_b64_tr_b16 a[96:97], v2 offset:16384\n" \
" ds_read_b64_tr_b16 a[98:99], v2 offset:16896\n" \
" ds_read_b64_tr_b16 a[224:225], v2 offset:16416\n" \
" ds_read_b64_tr_b16 a[226:227], v2 offset:16928\n" \
" ds_read_b64_tr_b16 v[224:225], v2 offset:16448\n" \
" ds_read_b64_tr_b16 v[226:227], v2 offset:16960\n" \
" ds_read_b64_tr_b16 v[16:17], v2 offset:16480\n" \
" ds_read_b64_tr_b16 v[18:19], v2 offset:16992\n" \
" s_waitcnt lgkmcnt(0)\n" \
" ds_write_b128 v0, v[80:83] offset:16384\n" \
" ds_write_b128 v0, v[84:87] offset:17408\n" \
" ds_write_b128 v0, v[88:91] offset:18432\n" \
" ds_write_b128 v0, v[92:95] offset:19456\n" \
" s_waitcnt vmcnt(8)\n" \
" ds_read_b64_tr_b16 a[100:101], v2 offset:20480\n" \
" ds_read_b64_tr_b16 a[102:103], v2 offset:20992\n" \
" ds_read_b64_tr_b16 a[228:229], v2 offset:20512\n" \
" ds_read_b64_tr_b16 a[230:231], v2 offset:21024\n" \
" ds_read_b64_tr_b16 v[228:229], v2 offset:20544\n" \
" ds_read_b64_tr_b16 v[230:231], v2 offset:21056\n" \
" ds_read_b64_tr_b16 v[20:21], v2 offset:20576\n" \
" ds_read_b64_tr_b16 v[22:23], v2 offset:21088\n" \
" s_waitcnt lgkmcnt(0)\n" \
" ds_write_b128 v0, v[96:99] offset:20480\n" \
" ds_write_b128 v0, v[100:103] offset:21504\n" \
" ds_write_b128 v0, v[104:107] offset:22528\n" \
" ds_write_b128 v0, v[108:111] offset:23552\n" \
" s_waitcnt vmcnt(4)\n" \
" ds_read_b64_tr_b16 a[104:105], v2 offset:24576\n" \
" ds_read_b64_tr_b16 a[106:107], v2 offset:25088\n" \
" ds_read_b64_tr_b16 a[232:233], v2 offset:24608\n" \
" ds_read_b64_tr_b16 a[234:235], v2 offset:25120\n" \
" ds_read_b64_tr_b16 v[232:233], v2 offset:24640\n" \
" ds_read_b64_tr_b16 v[234:235], v2 offset:25152\n" \
" ds_read_b64_tr_b16 v[24:25], v2 offset:24672\n" \
" ds_read_b64_tr_b16 v[26:27], v2 offset:25184\n" \
" s_waitcnt lgkmcnt(0)\n" \
" ds_write_b128 v0, v[16:19] offset:24576\n" \
" ds_write_b128 v0, v[20:23] offset:25600\n" \
" ds_write_b128 v0, v[24:27] offset:26624\n" \
" s_waitcnt vmcnt(0)\n" \
" ds_read_b64_tr_b16 a[108:109], v2 offset:28672\n" \
" ds_read_b64_tr_b16 a[110:111], v2 offset:29184\n" \
" ds_read_b64_tr_b16 a[236:237], v2 offset:28704\n" \
" ds_read_b64_tr_b16 a[238:239], v2 offset:29216\n" \
" ds_read_b64_tr_b16 v[236:237], v2 offset:28736\n" \
" ds_read_b64_tr_b16 v[238:239], v2 offset:29248\n" \
" ds_read_b64_tr_b16 v[28:29], v2 offset:28768\n" \
" ds_read_b64_tr_b16 v[30:31], v2 offset:29280\n" \
" s_waitcnt lgkmcnt(0)\n" \
" ds_write_b128 v0, v[28:31] offset:28672\n" \
" s_waitcnt vmcnt(0) lgkmcnt(0)\n" \
" s_barrier\n" \
" s_mov_b32 s55, 0\n" \
" s_mov_b32 s60, 0\n" \
" v_mov_b32 v10, 0\n" \
"Lsg2_prime:\n" \
" s_lshr_b32 s59, s44, 1\n" \
" s_add_u32 s59, s59, 1\n" \
" s_mul_i32 s56, s44, 114688\n" \
" buffer_load_dword v11, v4, s[36:39], 0 offen sc1\n" \
" s_waitcnt vmcnt(0)\n" \
" v_cmp_gt_u32_e64 s[62:63], s59, v11\n" \
" s_nop 4\n" \
" s_cmp_lg_u64 s[62:63], 0\n" \
" s_cbranch_scc1 Lsg2_w\n" \
"Lsg2_wk:\n" \
" s_mov_b32 s76, 0x7ffffff0\n" \
" s_mov_b32 s77, 0x7ffffff0\n" \
" s_mov_b32 s79, 0\n" \
" s_min_u32 s80, s55, s47\n" \
" s_mov_b32 s81, 1\n" \
" s_mov_b32 s84, 0\n" \
" s_waitcnt vmcnt(0) lgkmcnt(0)\n" \
" s_add_u32 s57, s56, 0\n" \
" buffer_load_dwordx4 v[16:19], v3, s[36:39], s57 offen offset:0 sc1\n" \
" buffer_load_dwordx4 v[20:23], v3, s[36:39], s57 offen offset:1024 sc1\n" \
" buffer_load_dwordx4 v[24:27], v3, s[36:39], s57 offen offset:2048 sc1\n" \
" buffer_load_dwordx4 v[28:31], v3, s[36:39], s57 offen offset:3072 sc1\n" \
" s_add_u32 s57, s56, 4096\n" \
" buffer_load_dwordx4 v[32:35], v3, s[36:39], s57 offen offset:0 sc1\n" \
" buffer_load_dwordx4 v[36:39], v3, s[36:39], s57 offen offset:1024 sc1\n" \
" buffer_load_dwordx4 v[40:43], v3, s[36:39], s57 offen offset:2048 sc1\n" \
" buffer_load_dwordx4 v[44:47], v3, s[36:39], s57 offen offset:3072 sc1\n" \
" s_add_u32 s57, s56, 8192\n" \
" buffer_load_dwordx4 v[48:51], v3, s[36:39], s57 offen offset:0 sc1\n" \
" buffer_load_dwordx4 v[52:55], v3, s[36:39], s57 offen offset:1024 sc1\n" \
" buffer_load_dwordx4 v[56:59], v3, s[36:39], s57 offen offset:2048 sc1\n" \
" buffer_load_dwordx4 v[60:63], v3, s[36:39], s57 offen offset:3072 sc1\n" \
" s_add_u32 s57, s56, 12288\n" \
" buffer_load_dwordx4 v[64:67], v3, s[36:39], s57 offen offset:0 sc1\n" \
" buffer_load_dwordx4 v[68:71], v3, s[36:39], s57 offen offset:1024 sc1\n" \
" ds_read_b128 v[80:83], v0 offset:0\n" \
" ds_read_b128 v[84:87], v0 offset:1024\n" \
" ds_read_b128 v[88:91], v0 offset:2048\n" \
" s_waitcnt lgkmcnt(0)\n" \
" v_mov_b32 v13, 0x7ffffff0\n" \
" buffer_store_dword v13, v13, s[36:39], 0 offen\n" \
" buffer_store_dword v13, v13, s[36:39], 0 offen\n" \
" buffer_store_dword v13, v13, s[36:39], 0 offen\n" \
" s_waitcnt vmcnt(16)\n" \
" s_nop 7\n" \
" s_nop 7\n" \
".p2align 6\n" \
"Lsg2_loop:\n" \
" v_mfma_f32_16x16x32_bf16 v[96:99], a[0:3], v[16:19], 0\n" \
" ds_write_b128 v7, v[104:107] offset:2048\n" \
" v_mfma_f32_16x16x32_bf16 v[100:103], a[128:131], v[16:19], 0\n" \
" ds_write_b128 v7, v[108:111] offset:3072\n" \
" ds_read_b128 v[92:95], v0 offset:3072\n" \
" v_mfma_f32_16x16x32_bf16 v[104:107], v[128:131], v[16:19], 0\n" \
" s_waitcnt vmcnt(15) lgkmcnt(7)\n" \
" v_mfma_f32_16x16x32_bf16 v[108:111], v[80:83], v[16:19], 0\n" \
" buffer_load_dwordx4 v[16:19], v3, s[36:39], s57 offen offset:2048 sc1\n" \
" v_mfma_f32_16x16x32_bf16 v[96:99], a[4:7], v[20:23], v[96:99]\n" \
" v_cmp_gt_u32 vcc, s79, v9\n" \
" v_mfma_f32_16x16x32_bf16 v[100:103], a[132:135], v[20:23], v[100:103]\n" \
" ds_read_b128 v[80:83], v0 offset:4096\n" \
" v_mfma_f32_16x16x32_bf16 v[104:107], v[132:135], v[20:23], v[104:107]\n" \
" s_waitcnt vmcnt(15) lgkmcnt(7)\n" \
" s_cbranch_vccnz Lsg2_p\n" \
"Lsg2_pk:\n" \
" v_mfma_f32_16x16x32_bf16 v[108:111], v[84:87], v[20:23], v[108:111]\n" \
" buffer_load_dwordx4 v[20:23], v3, s[36:39], s57 offen offset:3072 sc1\n" \
" v_mfma_f32_16x16x32_bf16 v[96:99], a[8:11], v[24:27], v[96:99]\n" \
" s_waitcnt lgkmcnt(2)\n" \
" s_cmp_lg_u32 s81, 0\n" \
" s_cbranch_scc1 Lsg2_nb\n" \
" s_barrier\n" \
"Lsg2_nb:\n" \
" v_mfma_f32_16x16x32_bf16 v[100:103], a[136:139], v[24:27], v[100:103]\n" \
" ds_read_b128 v[84:87], v0 offset:5120\n" \
" v_xor_b32_e64 v7, s84, v7\n" \
" v_mfma_f32_16x16x32_bf16 v[104:107], v[136:139], v[24:27], v[104:107]\n" \
" s_waitcnt vmcnt(15)\n" \
" buffer_load_dwordx2 v[14:15], v6, s[36:39], s77 offen sc1\n" \
" v_mfma_f32_16x16x32_bf16 v[108:111], v[88:91], v[24:27], v[108:111]\n" \
" s_add_u32 s57, s56, 16384\n" \
" buffer_load_dwordx4 v[24:27], v3, s[36:39], s57 offen offset:0 sc1\n" \
" v_mfma_f32_16x16x32_bf16 v[96:99], a[12:15], v[28:31], v[96:99]\n" \
" buffer_load_dwordx2 v[12:13], v5, s[64:67], s77 offen\n" \
" ds_read_b128 v[112:115], v8 offset:0\n" \
" v_mfma_f32_16x16x32_bf16 v[100:103], a[140:143], v[28:31], v[100:103]\n" \
" ds_read_b128 v[88:91], v0 offset:6144\n" \
" v_mfma_f32_16x16x32_bf16 v[104:107], v[140:143], v[28:31], v[104:107]\n" \
" s_waitcnt vmcnt(17) lgkmcnt(4)\n" \
" v_mfma_f32_16x16x32_bf16 v[108:111], v[92:95], v[28:31], v[108:111]\n" \
" buffer_load_dwordx4 v[28:31], v3, s[36:39], s57 offen offset:1024 sc1\n" \
" v_mfma_f32_16x16x32_bf16 v[96:99], a[16:19], v[32:35], v[96:99]\n" \
" ds_read_b128 v[116:119], v8 offset:4096\n" \
" s_add_u32 s82, s44, s45\n" \
" s_lshr_b32 s59, s82, 1\n" \
" v_mfma_f32_16x16x32_bf16 v[100:103], a[144:147], v[32:35], v[100:103]\n" \
" ds_read_b128 v[92:95], v0 offset:7168\n" \
" v_mfma_f32_16x16x32_bf16 v[104:107], v[144:147], v[32:35], v[104:107]\n" \
" s_waitcnt vmcnt(17) lgkmcnt(5)\n" \
" s_add_u32 s59, s59, 1\n" \
" v_mfma_f32_16x16x32_bf16 v[108:111], v[80:83], v[32:35], v[108:111]\n" \
" buffer_load_dwordx4 v[32:35], v3, s[36:39], s57 offen offset:2048 sc1\n" \
" v_mfma_f32_16x16x32_bf16 v[96:99], a[20:23], v[36:39], v[96:99]\n" \
" ds_read_b128 v[120:123], v8 offset:8192\n" \
" v_mfma_f32_16x16x32_bf16 v[100:103], a[148:151], v[36:39], v[100:103]\n" \
" ds_read_b128 v[80:83], v0 offset:8192\n" \
" v_mfma_f32_16x16x32_bf16 v[104:107], v[148:151], v[36:39], v[104:107]\n" \
" s_waitcnt vmcnt(16) lgkmcnt(6)\n" \
" v_mfma_f32_16x16x32_bf16 v[108:111], v[84:87], v[36:39], v[108:111]\n" \
" buffer_load_dwordx4 v[36:39], v3, s[36:39], s57 offen offset:3072 sc1\n" \
" v_mfma_f32_16x16x32_bf16 v[96:99], a[24:27], v[40:43], v[96:99]\n" \
" ds_read_b128 v[124:127], v8 offset:12288\n" \
" v_mfma_f32_16x16x32_bf16 v[100:103], a[152:155], v[40:43], v[100:103]\n" \
" ds_read_b128 v[84:87], v0 offset:9216\n" \
" v_mfma_f32_16x16x32_bf16 v[104:107], v[152:155], v[40:43], v[104:107]\n" \
" s_waitcnt vmcnt(16) lgkmcnt(6)\n" \
" v_mfma_f32_16x16x32_bf16 v[108:111], v[88:91], v[40:43], v[108:111]\n" \
" s_add_u32 s57, s56, 20480\n" \
" buffer_load_dwordx4 v[40:43], v3, s[36:39], s57 offen offset:0 sc1\n" \
" v_mfma_f32_16x16x32_bf16 v[96:99], a[28:31], v[44:47], v[96:99]\n" \
" v_mfma_f32_16x16x32_bf16 v[100:103], a[156:159], v[44:47], v[100:103]\n" \
" ds_read_b128 v[88:91], v0 offset:10240\n" \
" v_mfma_f32_16x16x32_bf16 v[104:107], v[156:159], v[44:47], v[104:107]\n" \
" s_waitcnt vmcnt(16) lgkmcnt(5)\n" \
" v_add_f32 v112, v112, v116\n" \
" v_mfma_f32_16x16x32_bf16 v[108:111], v[92:95], v[44:47], v[108:111]\n" \
" buffer_load_dwordx4 v[44:47], v3, s[36:39], s57 offen offset:1024 sc1\n" \
" v_add_f32_e64 v113, v113, v117\n" \
" v_mfma_f32_16x16x32_bf16 v[96:99], a[32:35], v[48:51], v[96:99]\n" \
" v_add_f32 v114, v114, v118\n" \
" v_add_f32 v115, v115, v119\n" \
" v_mfma_f32_16x16x32_bf16 v[100:103], a[160:163], v[48:51], v[100:103]\n" \
" ds_read_b128 v[92:95], v0 offset:11264\n" \
" s_waitcnt lgkmcnt(3)\n" \
" v_add_f32 v120, v120, v124\n" \
" v_mfma_f32_16x16x32_bf16 v[104:107], v[160:163], v[48:51], v[104:107]\n" \
" s_waitcnt vmcnt(16)\n" \
" v_add_f32 v121, v121, v125\n" \
" v_mfma_f32_16x16x32_bf16 v[108:111], v[80:83], v[48:51], v[108:111]\n" \
" buffer_load_dwordx4 v[48:51], v3, s[36:39], s57 offen offset:2048 sc1\n" \
" v_add_f32_e64 v122, v122, v126\n" \
" v_mfma_f32_16x16x32_bf16 v[96:99], a[36:39], v[52:55], v[96:99]\n" \
" v_add_f32 v123, v123, v127\n" \
" v_add_f32 v112, v112, v120\n" \
" v_mfma_f32_16x16x32_bf16 v[100:103], a[164:167], v[52:55], v[100:103]\n" \
" ds_read_b128 v[80:83], v0 offset:12288\n" \
" v_add_f32_e64 v113, v113, v121\n" \
" v_mfma_f32_16x16x32_bf16 v[104:107], v[164:167], v[52:55], v[104:107]\n" \
" s_waitcnt vmcnt(16) lgkmcnt(3)\n" \
" v_add_f32 v114, v114, v122\n" \
" v_mfma_f32_16x16x32_bf16 v[108:111], v[84:87], v[52:55], v[108:111]\n" \
" buffer_load_dwordx4 v[52:55], v3, s[36:39], s57 offen offset:3072 sc1\n" \
" v_add_f32_e64 v115, v115, v123\n" \
" v_mfma_f32_16x16x32_bf16 v[96:99], a[40:43], v[56:59], v[96:99]\n" \
" v_xor_b32_e64 v8, s84, v8\n" \
" v_mul_f32 v116, 0xbfb8aa3b, v112\n" \
" v_mfma_f32_16x16x32_bf16 v[100:103], a[168:171], v[56:59], v[100:103]\n" \
" ds_read_b128 v[84:87], v0 offset:13312\n" \
" v_mul_f32 v117, 0xbfb8aa3b, v113\n" \
" v_mfma_f32_16x16x32_bf16 v[104:107], v[168:171], v[56:59], v[104:107]\n" \
" s_waitcnt vmcnt(14) lgkmcnt(3)\n" \
" v_mul_f32 v118, 0xbfb8aa3b, v114\n" \
" v_mfma_f32_16x16x32_bf16 v[108:111], v[88:91], v[56:59], v[108:111]\n" \
" s_add_u32 s57, s56, 24576\n" \
" buffer_load_dwordx4 v[56:59], v3, s[36:39], s57 offen offset:0 sc1\n" \
" v_mul_f32 v119, 0xbfb8aa3b, v115\n" \
" v_mfma_f32_16x16x32_bf16 v[96:99], a[44:47], v[60:63], v[96:99]\n" \
" v_cmp_gt_u32_e64 s[62:63], s59, v11\n" \
" v_exp_f32 v116, v116\n" \
" v_mfma_f32_16x16x32_bf16 v[100:103], a[172:175], v[60:63], v[100:103]\n" \
" ds_read_b128 v[88:91], v0 offset:14336\n" \
" v_exp_f32_e64 v117, v117\n" \
" v_mfma_f32_16x16x32_bf16 v[104:107], v[172:175], v[60:63], v[104:107]\n" \
" s_waitcnt vmcnt(14) lgkmcnt(3)\n" \
" v_exp_f32 v118, v118\n" \
" v_mfma_f32_16x16x32_bf16 v[108:111], v[92:95], v[60:63], v[108:111]\n" \
" buffer_load_dwordx4 v[60:63], v3, s[36:39], s57 offen offset:1024 sc1\n" \
" v_exp_f32_e64 v119, v119\n" \
" v_mfma_f32_16x16x32_bf16 v[96:99], a[48:51], v[64:67], v[96:99]\n" \
" s_add_u32 s54, s55, 1\n" \
" s_cmp_lt_u32 s54, s46\n" \
" v_add_f32 v116, 1.0, v116\n" \
" v_add_f32 v117, 1.0, v117\n" \
" v_mfma_f32_16x16x32_bf16 v[100:103], a[176:179], v[64:67], v[100:103]\n" \
" ds_read_b128 v[92:95], v0 offset:15360\n" \
" s_cselect_b64 s[62:63], s[62:63], -1\n" \
" v_add_f32 v118, 1.0, v118\n" \
" v_mfma_f32_16x16x32_bf16 v[104:107], v[176:179], v[64:67], v[104:107]\n" \
" s_waitcnt vmcnt(14) lgkmcnt(3)\n" \
" s_mul_i32 s83, s82, 114688\n" \
" v_add_f32 v119, 1.0, v119\n" \
" v_mfma_f32_16x16x32_bf16 v[108:111], v[80:83], v[64:67], v[108:111]\n" \
" buffer_load_dwordx4 v[64:67], v3, s[36:39], s57 offen offset:2048 sc1\n" \
" v_rcp_f32_e64 v116, v116\n" \
" v_mfma_f32_16x16x32_bf16 v[96:99], a[52:55], v[68:71], v[96:99]\n" \
" s_cmp_lg_u64 s[62:63], 0\n" \
" s_cselect_b32 s81, 1, 0\n" \
" v_rcp_f32_e64 v117, v117\n" \
" v_mfma_f32_16x16x32_bf16 v[100:103], a[180:183], v[68:71], v[100:103]\n" \
" ds_read_b128 v[80:83], v0 offset:16384\n" \
" s_cselect_b32 s83, 0x70000000, s83\n" \
" v_rcp_f32_e64 v118, v118\n" \
" v_mfma_f32_16x16x32_bf16 v[104:107], v[180:183], v[68:71], v[104:107]\n" \
" s_waitcnt vmcnt(14) lgkmcnt(3)\n" \
" v_rcp_f32 v119, v119\n" \
" v_mfma_f32_16x16x32_bf16 v[108:111], v[84:87], v[68:71], v[108:111]\n" \
" buffer_load_dwordx4 v[68:71], v3, s[36:39], s57 offen offset:3072 sc1\n" \
" v_mul_f32_e64 v112, v112, v116\n" \
" v_mfma_f32_16x16x32_bf16 v[96:99], a[56:59], v[16:19], v[96:99]\n" \
" v_mul_f32 v113, v113, v117\n" \
" v_mul_f32 v114, v114, v118\n" \
" v_mfma_f32_16x16x32_bf16 v[100:103], a[184:187], v[16:19], v[100:103]\n" \
" ds_read_b128 v[84:87], v0 offset:17408\n" \
" v_mul_f32_e64 v115, v115, v119\n" \
" v_mfma_f32_16x16x32_bf16 v[104:107], v[184:187], v[16:19], v[104:107]\n" \
" s_waitcnt vmcnt(14) lgkmcnt(3)\n" \
" s_mov_b32 s56, s83\n" \
" v_cvt_pk_bf16_f32 v116, v112, v113\n" \
" v_mfma_f32_16x16x32_bf16 v[108:111], v[88:91], v[16:19], v[108:111]\n" \
" s_add_u32 s57, s56, 0\n" \
" buffer_load_dwordx4 v[16:19], v3, s[36:39], s57 offen offset:0 sc1\n" \
" v_cvt_pk_bf16_f32 v117, v114, v115\n" \
" v_mfma_f32_16x16x32_bf16 v[96:99], a[60:63], v[20:23], v[96:99]\n" \
" buffer_load_dword v9, v10, s[36:39], s50 offen sc1\n" \
" v_mfma_f32_16x16x32_bf16 v[100:103], a[188:191], v[20:23], v[100:103]\n" \
" ds_read_b128 v[88:91], v0 offset:18432\n" \
" buffer_store_dwordx2 v[116:117], v6, s[36:39], s76 offen sc1\n" \
" v_mfma_f32_16x16x32_bf16 v[104:107], v[188:191], v[20:23], v[104:107]\n" \
" s_waitcnt vmcnt(15) lgkmcnt(3)\n" \
" v_mfma_f32_16x16x32_bf16 v[108:111], v[92:95], v[20:23], v[108:111]\n" \
" buffer_load_dwordx4 v[20:23], v3, s[36:39], s57 offen offset:1024 sc1\n" \
" v_mfma_f32_16x16x32_bf16 v[96:99], a[64:67], v[24:27], v[96:99]\n" \
" v_mfma_f32_16x16x32_bf16 v[100:103], a[192:195], v[24:27], v[100:103]\n" \
" ds_read_b128 v[92:95], v0 offset:19456\n" \
" v_mfma_f32_16x16x32_bf16 v[104:107], v[192:195], v[24:27], v[104:107]\n" \
" s_waitcnt vmcnt(14) lgkmcnt(3)\n" \
" v_mfma_f32_16x16x32_bf16 v[108:111], v[80:83], v[24:27], v[108:111]\n" \
" buffer_load_dwordx4 v[24:27], v3, s[36:39], s57 offen offset:2048 sc1\n" \
" v_mfma_f32_16x16x32_bf16 v[96:99], a[68:71], v[28:31], v[96:99]\n" \
" v_lshlrev_b32 v120, 16, v14\n" \
" v_and_b32 v121, 0xffff0000, v14\n" \
" v_mfma_f32_16x16x32_bf16 v[100:103], a[196:199], v[28:31], v[100:103]\n" \
" ds_read_b128 v[80:83], v0 offset:20480\n" \
" v_lshlrev_b32_e64 v124, 16, v12\n" \
" v_mfma_f32_16x16x32_bf16 v[104:107], v[196:199], v[28:31], v[104:107]\n" \
" s_waitcnt vmcnt(14) lgkmcnt(3)\n" \
" v_and_b32 v125, 0xffff0000, v12\n" \
" v_mfma_f32_16x16x32_bf16 v[108:111], v[84:87], v[28:31], v[108:111]\n" \
" buffer_load_dwordx4 v[28:31], v3, s[36:39], s57 offen offset:3072 sc1\n" \
" v_lshlrev_b32 v122, 16, v15\n" \
" v_mfma_f32_16x16x32_bf16 v[96:99], a[72:75], v[32:35], v[96:99]\n" \
" v_and_b32 v123, 0xffff0000, v15\n" \
" v_lshlrev_b32_e64 v126, 16, v13\n" \
" v_mfma_f32_16x16x32_bf16 v[100:103], a[200:203], v[32:35], v[100:103]\n" \
" ds_read_b128 v[84:87], v0 offset:21504\n" \
" v_and_b32 v127, 0xffff0000, v13\n" \
" v_mfma_f32_16x16x32_bf16 v[104:107], v[200:203], v[32:35], v[104:107]\n" \
" s_waitcnt vmcnt(14) lgkmcnt(3)\n" \
" v_fmac_f32 v124, v112, v120\n" \
" v_mfma_f32_16x16x32_bf16 v[108:111], v[88:91], v[32:35], v[108:111]\n" \
" s_add_u32 s57, s56, 4096\n" \
" buffer_load_dwordx4 v[32:35], v3, s[36:39], s57 offen offset:0 sc1\n" \
" v_fmac_f32_e64 v125, v113, v121\n" \
" v_mfma_f32_16x16x32_bf16 v[96:99], a[76:79], v[36:39], v[96:99]\n" \
" v_fmac_f32 v126, v114, v122\n" \
" v_fmac_f32 v127, v115, v123\n" \
" v_mfma_f32_16x16x32_bf16 v[100:103], a[204:207], v[36:39], v[100:103]\n" \
" ds_read_b128 v[88:91], v0 offset:22528\n" \
" v_cvt_pk_bf16_f32 v118, v124, v125\n" \
" v_mfma_f32_16x16x32_bf16 v[104:107], v[204:207], v[36:39], v[104:107]\n" \
" s_waitcnt vmcnt(14) lgkmcnt(3)\n" \
" v_cvt_pk_bf16_f32 v119, v126, v127\n" \
" v_mfma_f32_16x16x32_bf16 v[108:111], v[92:95], v[36:39], v[108:111]\n" \
" buffer_load_dwordx4 v[36:39], v3, s[36:39], s57 offen offset:1024 sc1\n" \
" v_mfma_f32_16x16x32_bf16 v[96:99], a[80:83], v[40:43], v[96:99]\n" \
" buffer_store_dwordx2 v[118:119], v5, s[68:71], s77 offen\n" \
" v_mfma_f32_16x16x32_bf16 v[100:103], a[208:211], v[40:43], v[100:103]\n" \
" ds_read_b128 v[92:95], v0 offset:23552\n" \
" v_mfma_f32_16x16x32_bf16 v[104:107], v[208:211], v[40:43], v[104:107]\n" \
" s_waitcnt vmcnt(15) lgkmcnt(3)\n" \
" v_mfma_f32_16x16x32_bf16 v[108:111], v[80:83], v[40:43], v[108:111]\n" \
" buffer_load_dwordx4 v[40:43], v3, s[36:39], s57 offen offset:2048 sc1\n" \
" v_mfma_f32_16x16x32_bf16 v[96:99], a[84:87], v[44:47], v[96:99]\n" \
" v_mfma_f32_16x16x32_bf16 v[100:103], a[212:215], v[44:47], v[100:103]\n" \
" ds_read_b128 v[80:83], v0 offset:24576\n" \
" v_mfma_f32_16x16x32_bf16 v[104:107], v[212:215], v[44:47], v[104:107]\n" \
" s_waitcnt vmcnt(15) lgkmcnt(3)\n" \
" v_mfma_f32_16x16x32_bf16 v[108:111], v[84:87], v[44:47], v[108:111]\n" \
" buffer_load_dwordx4 v[44:47], v3, s[36:39], s57 offen offset:3072 sc1\n" \
" v_mfma_f32_16x16x32_bf16 v[96:99], a[88:91], v[48:51], v[96:99]\n" \
" v_mfma_f32_16x16x32_bf16 v[100:103], a[216:219], v[48:51], v[100:103]\n" \
" ds_read_b128 v[84:87], v0 offset:25600\n" \
" v_mfma_f32_16x16x32_bf16 v[104:107], v[216:219], v[48:51], v[104:107]\n" \
" s_waitcnt vmcnt(15) lgkmcnt(3)\n" \
" v_mfma_f32_16x16x32_bf16 v[108:111], v[88:91], v[48:51], v[108:111]\n" \
" s_add_u32 s57, s56, 8192\n" \
" buffer_load_dwordx4 v[48:51], v3, s[36:39], s57 offen offset:0 sc1\n" \
" v_mfma_f32_16x16x32_bf16 v[96:99], a[92:95], v[52:55], v[96:99]\n" \
" v_mfma_f32_16x16x32_bf16 v[100:103], a[220:223], v[52:55], v[100:103]\n" \
" ds_read_b128 v[88:91], v0 offset:26624\n" \
" v_mfma_f32_16x16x32_bf16 v[104:107], v[220:223], v[52:55], v[104:107]\n" \
" s_waitcnt vmcnt(15) lgkmcnt(3)\n" \
" v_mfma_f32_16x16x32_bf16 v[108:111], v[92:95], v[52:55], v[108:111]\n" \
" buffer_load_dwordx4 v[52:55], v3, s[36:39], s57 offen offset:1024 sc1\n" \
" v_mfma_f32_16x16x32_bf16 v[96:99], a[96:99], v[56:59], v[96:99]\n" \
" v_mfma_f32_16x16x32_bf16 v[100:103], a[224:227], v[56:59], v[100:103]\n" \
" ds_read_b128 v[92:95], v0 offset:28672\n" \
" v_mfma_f32_16x16x32_bf16 v[104:107], v[224:227], v[56:59], v[104:107]\n" \
" s_waitcnt vmcnt(15) lgkmcnt(3)\n" \
" s_mov_b32 s84, 0x4000\n" \
" v_mfma_f32_16x16x32_bf16 v[108:111], v[80:83], v[56:59], v[108:111]\n" \
" buffer_load_dwordx4 v[56:59], v3, s[36:39], s57 offen offset:2048 sc1\n" \
" v_mfma_f32_16x16x32_bf16 v[96:99], a[100:103], v[60:63], v[96:99]\n" \
" buffer_load_dword v11, v4, s[36:39], 0 offen sc1\n" \
" v_mfma_f32_16x16x32_bf16 v[100:103], a[228:231], v[60:63], v[100:103]\n" \
" ds_read_b128 v[80:83], v0 offset:0\n" \
" v_mfma_f32_16x16x32_bf16 v[104:107], v[228:231], v[60:63], v[104:107]\n" \
" s_waitcnt vmcnt(16) lgkmcnt(3)\n" \
" s_waitcnt vmcnt(12)\n" \
" v_mov_b32_e64 v13, s80\n" \
" buffer_store_dword v13, v10, s[36:39], s49 offen sc1\n" \
" v_mfma_f32_16x16x32_bf16 v[108:111], v[84:87], v[60:63], v[108:111]\n" \
" buffer_load_dwordx4 v[60:63], v3, s[36:39], s57 offen offset:3072 sc1\n" \
" v_mfma_f32_16x16x32_bf16 v[96:99], a[104:107], v[64:67], v[96:99]\n" \
" s_lshl_b32 s78, s44, 15\n" \
" s_cmp_lt_u32 s55, s47\n" \
" v_mfma_f32_16x16x32_bf16 v[100:103], a[232:235], v[64:67], v[100:103]\n" \
" ds_read_b128 v[84:87], v0 offset:1024\n" \
" s_cselect_b32 s76, s78, 0x7ffffff0\n" \
" s_cselect_b32 s77, 0x7ffffff0, s78\n" \
" v_mfma_f32_16x16x32_bf16 v[104:107], v[232:235], v[64:67], v[104:107]\n" \
" s_waitcnt lgkmcnt(3)\n" \
" s_cselect_b32 s79, s46, s55\n" \
" s_sub_u32 s79, s46, s79\n" \
" v_mfma_f32_16x16x32_bf16 v[108:111], v[88:91], v[64:67], v[108:111]\n" \
" s_add_u32 s57, s56, 12288\n" \
" buffer_load_dwordx4 v[64:67], v3, s[36:39], s57 offen offset:0 sc1\n" \
" v_mfma_f32_16x16x32_bf16 v[96:99], a[108:111], v[68:71], v[96:99]\n" \
" s_add_u32 s80, s55, 1\n" \
" s_min_u32 s80, s80, s47\n" \
" v_mfma_f32_16x16x32_bf16 v[100:103], a[236:239], v[68:71], v[100:103]\n" \
" ds_read_b128 v[88:91], v0 offset:2048\n" \
" s_mov_b32 s44, s82\n" \
" s_add_u32 s55, s55, 1\n" \
" v_mfma_f32_16x16x32_bf16 v[104:107], v[236:239], v[68:71], v[104:107]\n" \
" s_waitcnt lgkmcnt(3)\n" \
" ds_write_b128 v7, v[96:99] offset:0\n" \
" v_mfma_f32_16x16x32_bf16 v[108:111], v[92:95], v[68:71], v[108:111]\n" \
" buffer_load_dwordx4 v[68:71], v3, s[36:39], s57 offen offset:1024 sc1\n" \
" ds_write_b128 v7, v[100:103] offset:1024\n" \
" s_cmp_eq_u32 s81, 0\n" \
" s_nop 0\n" \
" s_nop 0\n" \
" s_nop 0\n" \
" s_cbranch_scc1 Lsg2_loop\n" \
"; cold\n" \
" s_nop 7\n" \
" s_nop 7\n" \
" s_waitcnt vmcnt(0) lgkmcnt(0)\n" \
" ds_write_b128 v7, v[104:107] offset:2048\n" \
" ds_write_b128 v7, v[108:111] offset:3072\n" \
" s_waitcnt lgkmcnt(0)\n" \
" s_barrier\n" \
" v_cmp_gt_u32 vcc, s79, v9\n" \
" s_nop 4\n" \
" s_cbranch_vccnz Lsg2_q\n" \
"Lsg2_qk:\n" \
" buffer_load_dwordx2 v[14:15], v6, s[36:39], s77 offen sc1\n" \
" buffer_load_dwordx2 v[12:13], v5, s[64:67], s77 offen\n" \
" ds_read_b128 v[112:115], v8 offset:0\n" \
" ds_read_b128 v[116:119], v8 offset:4096\n" \
" ds_read_b128 v[120:123], v8 offset:8192\n" \
" ds_read_b128 v[124:127], v8 offset:12288\n" \
" s_waitcnt lgkmcnt(2)\n" \
" v_add_f32 v112, v112, v116\n" \
" v_add_f32 v113, v113, v117\n" \
" v_add_f32 v114, v114, v118\n" \
" v_add_f32 v115, v115, v119\n" \
" s_waitcnt lgkmcnt(0)\n" \
" v_add_f32 v120, v120, v124\n" \
" v_add_f32 v121, v121, v125\n" \
" v_add_f32 v122, v122, v126\n" \
" v_add_f32 v123, v123, v127\n" \
" v_add_f32 v112, v112, v120\n" \
" v_add_f32 v113, v113, v121\n" \
" v_add_f32 v114, v114, v122\n" \
" v_add_f32 v115, v115, v123\n" \
" v_xor_b32 v8, s84, v8\n" \
" v_mul_f32 v116, 0xbfb8aa3b, v112\n" \
" v_mul_f32 v117, 0xbfb8aa3b, v113\n" \
" v_mul_f32 v118, 0xbfb8aa3b, v114\n" \
" v_mul_f32 v119, 0xbfb8aa3b, v115\n" \
" v_exp_f32 v116, v116\n" \
" v_exp_f32 v117, v117\n" \
" v_exp_f32 v118, v118\n" \
" v_exp_f32 v119, v119\n" \
" v_add_f32 v116, 1.0, v116\n" \
" v_add_f32 v117, 1.0, v117\n" \
" v_add_f32 v118, 1.0, v118\n" \
" v_add_f32 v119, 1.0, v119\n" \
" v_rcp_f32 v116, v116\n" \
" v_rcp_f32 v117, v117\n" \
" v_rcp_f32 v118, v118\n" \
" v_rcp_f32 v119, v119\n" \
" v_mul_f32 v112, v112, v116\n" \
" v_mul_f32 v113, v113, v117\n" \
" v_mul_f32 v114, v114, v118\n" \
" v_mul_f32 v115, v115, v119\n" \
" v_cvt_pk_bf16_f32 v116, v112, v113\n" \
" v_cvt_pk_bf16_f32 v117, v114, v115\n" \
" buffer_store_dwordx2 v[116:117], v6, s[36:39], s76 offen sc1\n" \
" s_waitcnt vmcnt(2)\n" \
" v_lshlrev_b32 v120, 16, v14\n" \
" v_and_b32 v121, 0xffff0000, v14\n" \
" s_waitcnt vmcnt(1)\n" \
" v_lshlrev_b32 v124, 16, v12\n" \
" v_and_b32 v125, 0xffff0000, v12\n" \
" v_lshlrev_b32 v122, 16, v15\n" \
" v_and_b32 v123, 0xffff0000, v15\n" \
" v_lshlrev_b32 v126, 16, v13\n" \
" v_and_b32 v127, 0xffff0000, v13\n" \
" v_fmac_f32 v124, v112, v120\n" \
" v_fmac_f32 v125, v113, v121\n" \
" v_fmac_f32 v126, v114, v122\n" \
" v_fmac_f32 v127, v115, v123\n" \
" v_cvt_pk_bf16_f32 v118, v124, v125\n" \
" v_cvt_pk_bf16_f32 v119, v126, v127\n" \
" buffer_store_dwordx2 v[118:119], v5, s[68:71], s77 offen\n" \
" s_waitcnt vmcnt(1)\n" \
" v_mov_b32 v13, s80\n" \
" buffer_store_dword v13, v10, s[36:39], s49 offen sc1\n" \
" v_xor_b32 v7, 0x4000, v7\n" \
" s_waitcnt vmcnt(0) lgkmcnt(0)\n" \
" s_cmp_lt_u32 s55, s46\n" \
" s_cbranch_scc1 Lsg2_prime\n" \
" s_branch Lsg2_end\n" \
"Lsg2_w:\n" \
" s_nop 7\n" \
" s_nop 7\n" \
" s_waitcnt vmcnt(0) lgkmcnt(0)\n" \
" s_mov_b32 s61, 0\n" \
"Lsg2_wa:\n" \
" s_cmp_lg_u32 s60, 0\n" \
" s_cbranch_scc1 Lsg2_wk\n" \
" buffer_load_dword v11, v4, s[36:39], 0 offen sc1\n" \
" s_waitcnt vmcnt(0)\n" \
" v_cmp_gt_u32_e64 s[62:63], s59, v11\n" \
" s_nop 4\n" \
" s_cmp_eq_u64 s[62:63], 0\n" \
" s_cbranch_scc1 Lsg2_wk\n" \
" s_sleep 2\n" \
" s_add_u32 s61, s61, 1\n" \
" s_cmp_lt_u32 s61, 65536\n" \
" s_cbranch_scc1 Lsg2_wa\n" \
" s_mov_b32 s60, 1\n" \
" v_mov_b32 v11, 256\n" \
" v_mov_b32 v12, 1\n" \
" buffer_store_dword v12, v11, s[36:39], 0 offen sc1\n" \
" s_waitcnt vmcnt(0) lgkmcnt(0)\n" \
" s_branch Lsg2_wk\n" \
"Lsg2_p:\n" \
" s_nop 7\n" \
" s_nop 7\n" \
" s_waitcnt vmcnt(0) lgkmcnt(0)\n" \
" s_mov_b32 s61, 0\n" \
"Lsg2_pa:\n" \
" s_cmp_lg_u32 s60, 0\n" \
" s_cbranch_scc1 Lsg2_pk\n" \
" buffer_load_dword v9, v10, s[36:39], s50 offen sc1\n" \
" s_waitcnt vmcnt(0)\n" \
" v_cmp_gt_u32 vcc, s79, v9\n" \
" s_nop 4\n" \
" s_cbranch_vccz Lsg2_pk\n" \
" s_sleep 2\n" \
" s_add_u32 s61, s61, 1\n" \
" s_cmp_lt_u32 s61, 65536\n" \
" s_cbranch_scc1 Lsg2_pa\n" \
" s_mov_b32 s60, 1\n" \
" v_mov_b32 v11, 256\n" \
" v_mov_b32 v12, 1\n" \
" buffer_store_dword v12, v11, s[36:39], 0 offen sc1\n" \
" s_waitcnt vmcnt(0) lgkmcnt(0)\n" \
" s_branch Lsg2_pk\n" \
"Lsg2_q:\n" \
" s_nop 7\n" \
" s_nop 7\n" \
" s_waitcnt vmcnt(0) lgkmcnt(0)\n" \
" s_mov_b32 s61, 0\n" \
"Lsg2_qa:\n" \
" s_cmp_lg_u32 s60, 0\n" \
" s_cbranch_scc1 Lsg2_qk\n" \
" buffer_load_dword v9, v10, s[36:39], s50 offen sc1\n" \
" s_waitcnt vmcnt(0)\n" \
" v_cmp_gt_u32 vcc, s79, v9\n" \
" s_nop 4\n" \
" s_cbranch_vccz Lsg2_qk\n" \
" s_sleep 2\n" \
" s_add_u32 s61, s61, 1\n" \
" s_cmp_lt_u32 s61, 65536\n" \
" s_cbranch_scc1 Lsg2_qa\n" \
" s_mov_b32 s60, 1\n" \
" v_mov_b32 v11, 256\n" \
" v_mov_b32 v12, 1\n" \
" buffer_store_dword v12, v11, s[36:39], 0 offen sc1\n" \
" s_waitcnt vmcnt(0) lgkmcnt(0)\n" \
" s_branch Lsg2_qk\n" \
"Lsg2_end:\n" \
" s_endpgm\n"

typedef u32 v32u __attribute__((ext_vector_type(32)));
typedef float f32x2 __attribute__((ext_vector_type(2)));
__device__ __forceinline__ float xsum(float s) {
    auto a = __builtin_amdgcn_permlane16_swap(__builtin_bit_cast(u32, s), __builtin_bit_cast(u32, s), false, false);
    s = __builtin_bit_cast(float, (u32)a[0]) + __builtin_bit_cast(float, (u32)a[1]);
    auto b = __builtin_amdgcn_permlane32_swap(__builtin_bit_cast(u32, s), __builtin_bit_cast(u32, s), false, false);
    return __builtin_bit_cast(float, (u32)b[0]) + __builtin_bit_cast(float, (u32)b[1]);
}
__device__ __forceinline__ void ln_tile(const Params& p, const v32u xv, int rt, int w, int lane) {
    const int n = lane & 15, g = lane >> 4;
    const rsrc_t rs = __builtin_amdgcn_make_buffer_rsrc(p.ws, 0, WS_END, 0x00020000);
    LAS float* red = (LAS float*)(uintptr_t)(136 * 1024);
    f32x2 xf[32], s2 = {0.f, 0.f};
#pragma unroll
    for (int i = 0; i < 32; ++i) { xf[i] = f32x2{bflo(xv[i]), bfhi(xv[i])}; s2 += xf[i]; }
    const float s = xsum(s2.x + s2.y);
    if (g == 0) red[n * 4 + w] = s;
    BAR();
    f32x4 t = *(LAS f32x4*)(red + n * 4);
    const float mean = ((t.x + t.y) + (t.z + t.w)) * (1.0f / DM);
    f32x2 q2 = {0.f, 0.f};
#pragma unroll
    for (int i = 0; i < 32; ++i) { const f32x2 a = xf[i] - mean; q2 += a * a; }
    const float q = xsum(q2.x + q2.y);
    if (g == 0) red[64 + n * 4 + w] = q;
    BAR();
    t = *(LAS f32x4*)(red + 64 + n * 4);
    const float var = ((t.x + t.y) + (t.z + t.w)) * (1.0f / DM);
    const float mb = rbf(mean);
    const float rd = 1.0f / rbf(sqrtf(rbf(rbf(var) + 1e-5f)));
#pragma unroll
    for (int i = 0; i < 8; ++i) {
        v4u o;
#pragma unroll
        for (int e = 0; e < 4; ++e) { const f32x2 a = xf[4 * i + e] - mb; const u32 d = pack2(a.x, a.y); const f32x2 y = f32x2{bflo(d), bfhi(d)} * rd; o[e] = pack2(y.x, y.y); }
        st128s(o, rs, WS_XF + rt * 32768 + (w * 8 + i) * 1024 + lane * 16);
    }
    WAIT_V0();
    __builtin_amdgcn_s_barrier(); CFENCE();
    if (w == 0 && lane == 0) __hip_atomic_fetch_add((GAS u32*)(p.ws + WS_CT) + CT_LN + (rt >> 5) * 32, 1u, RLXA);
}
__device__ __forceinline__ void p1_role(const Params& p, int b, int s0, int s1, int id, int w) {
    const int lane = __builtin_amdgcn_mbcnt_hi(~0u, __builtin_amdgcn_mbcnt_lo(~0u, 0u)), n = lane & 15, g = lane >> 4;
    const bool g1 = b >= 96;
    const int h = b & 31, kind = b >> 5;
    const int stride = g1 ? FF * 2 : 8192;
    const u16* src = g1 ? ((w >> 1) ? p.wu : p.wg) + (b - 96) * 64 : (kind == 0 ? p.wk : (kind == 1 ? p.wv : p.wq)) + h * 128 + (w >> 1) * 64;
    const v4u rs = mksrd(p.ws, WS_END), rw = mksrd(src, 1023 * stride + 128);
    const int v0 = lane * 16;
    const int v1 = (8 * (w & 1) + (lane >> 3)) * stride + ((lane & 7) ^ ((((lane >> 4) & 1) | ((w & 1) << 1)) << 1)) * 16;
    const int swz = ((n >> 3) | ((g & 1) << 1)), rowo = (8 * g + (n >> 2)) * 128 + (n & 3) * 8;
    const int v2 = (g1 ? 0 : (w >> 1) * 8192) + rowo + (((g1 ? w : 2 * (w & 1)) ^ swz) * 32);
    const int v3 = (g1 ? 8192 : (w >> 1) * 8192) + rowo + (((g1 ? w : 2 * (w & 1) + 1) ^ swz) * 32);
    const int v4 = WS_XF + (w >> 1) * 32768 + (w & 1) * 8192 + lane * 16;
    int v5, ostr, od;
    if (g1) { v5 = WS_HF + (g & 1) * (112 * 1024) + ((b - 96) * 2 + (w >> 1)) * 1024 + (2 * (w & 1) + (g >> 1)) * 256 + n * 16; ostr = 2 * 112 * 1024; od = 0; }
    else if (kind == 1) { v5 = WS_TK + h * TK_STR + 16384 + w * 2048 + lane * 16; ostr = 24576; od = 1024; }
    else { v5 = WS_TK + h * TK_STR + (kind == 0 ? 0 : 8192) + w * 1024 + lane * 16; ostr = 24576; od = 4096; }
    const int v6 = (w == 0 && lane == 0) ? WS_CT + (g1 ? CT_G1 + (b - 96) * 32 : CT_P1 + h * 32 + kind * 4) * 4 : OOB;
#define P1_IN "{v0}"(v0), "{v1}"(v1), "{v2}"(v2), "{v3}"(v3), "{v4}"(v4), "{s[36:39]}"(rs), "{s[40:43]}"(rw), "{s44}"(16 * stride), "{s46}"(s0), "{s47}"(s1), "{s48}"(w), "{s49}"(ostr), "{s50}"(od), "{s51}"(rfl(kind == 2 ? 0x3e0293ee : 0x3f800000))
    {
        const int rt = ((b & 3) << 5) | ((b >> 2) & 31);
        const v4u rx = mksrd(p.x, b < 128 ? 2048 * 2048 : 0);
        v32u xv;
        asm volatile(ASM_P1_HEAD : "={v[20:51]}"(xv) : "{v1}"(v1), "{v16}"((rt * 16 + n) * 2048 + w * 512 + g * 16), "{s[40:43]}"(rw), "{s[64:67]}"(rx), "{s44}"(16 * stride), "{s48}"(w) : "s52", "s53", "s54", "scc", "memory");
        if (b < 128) ln_tile(p, xv, rt, w, lane);
    }
    int a5 = v5, a6 = v6; u32 tc, tr;
    if (g1) asm volatile(ASM_P1_G1 : "+{v5}"(a5), "+{v6}"(a6), "={s70}"(tc), "={s72}"(tr) : P1_IN : CLOB_P1);
    else if (kind == 1) asm volatile(ASM_P1_V : "+{v5}"(a5), "+{v6}"(a6), "={s70}"(tc), "={s72}"(tr) : P1_IN : CLOB_P1);
    else asm volatile(ASM_P1_QK : "+{v5}"(a5), "+{v6}"(a6), "={s70}"(tc), "={s72}"(tr) : P1_IN : CLOB_P1);
}

#define TUNEV(i, c) (c)
__device__ __forceinline__ int mlane(int n, int g) { return (g >> 1) * 512 + (n >> 3) * 256 + (g & 1) * 128 + (n & 7) * 16; }
__device__ __forceinline__ void qk_role(const Params& p, int h) {
    const int lane = __builtin_amdgcn_mbcnt_hi(~0u, __builtin_amdgcn_mbcnt_lo(~0u, 0u)), w = rfl(threadIdx.x >> 6), n = lane & 15, g = lane >> 4;
    const v4u rs = mksrd(p.ws, WS_END), rw = mksrd(((w >> 1) ? p.wk : p.wq) + h * 128 + (w & 1) * 64, 1023 * 8192 + 128);
    const int l16 = lane * 16, v1 = l16 + w * 4096, v2 = v1 + 65536, v3 = l16 + 112 * 1024;
    const int v4 = amap(HB_M + w * 1024 + mlane(n, g), 0);
    const int v5 = 152 * 1024 + (32 * w + n) * 32 + ((g >> 1) ^ (n >> 2)) * 8 + (g & 1) * 4, v0 = v5 ^ 16;
    const int v6 = 152 * 1024 + (4 * g + (n >> 2)) * 32 + ((n & 3) ^ g) * 8;
    const int v15 = min(l16, 32);
    const int v7 = n < 8 ? amap(HB_O + (n >> 2) * 32768 + h * 1024 + w * 256 + (g >> 1) * 128 + ((n >> 1) & 1) * 64 + (g & 1) * 32 + (n & 1) * 16, 0) : OOB;
    const int v8 = g >= 2 ? amap(HB_V + h * 2048 + w * 512 + (g - 2) * 256 + n * 16, 0) : OOB;
    const int v9 = amap(HB_H + (lane & 31) * 128, 0);
    const int v10 = (w == 0 && lane == 0) ? 0 : OOB;
    const int v11 = WS_OF + h * OF_STR + (w & 1) * 4096 + w * 1024 + l16;
    const int v12 = (n & 2) ? 0xEEEE : 0x4444;
    const int v13 = g < 2 ? 0xFF800000 : 0;
    const int v14 = 16 * (w & 1) + n - 4 * g;
    const int r8 = lane >> 3;
    const int v32 = r8 * 8192 + ((lane & 7) ^ (((r8 >> 1) & 1) << 1)) * 16, v33 = (8 + r8) * 8192 + ((lane & 7) ^ ((((r8 >> 1) & 1) | 2) << 1)) * 16;
    const int swz = (n >> 3) | ((g & 1) << 1), rowo = (w >> 1) * 4096 + (8 * g + (n >> 2)) * 128 + (n & 3) * 8;
    const int v34 = rowo + ((2 * (w & 1)) ^ swz) * 32, v35 = rowo + ((2 * (w & 1) + 1) ^ swz) * 32;
    asm volatile(ASM_QK :: "{v0}"(v0), "{v1}"(v1), "{v2}"(v2), "{v3}"(v3), "{v4}"(v4), "{v5}"(v5), "{v6}"(v6), "{v7}"(v7), "{v8}"(v8), "{v9}"(v9), "{v10}"(v10), "{v11}"(v11), "{v12}"(v12),
        "{v13}"(v13), "{v14}"(v14), "{v15}"(v15), "{v32}"(v32), "{v33}"(v33), "{v34}"(v34), "{v35}"(v35), "{s[36:39]}"(rs), "{s[40:43]}"(rw), "{s45}"(TUNEV(1, C_P)), "{s47}"(WS_DBG + 131072 + (h * 4 + w) * 2048), "{s48}"(w),
        "{s63}"(h * TK_STR), "{s80}"(WS_CT + (CT_P1 + h * 32) * 4), "{s87}"(WS_CT + (CT_AT + h * 32) * 4), "{s94}"(amap(HB_H + h * 128, 0)) : "v255", "a255", "s99", "memory");
    __builtin_unreachable();
}
__device__ __forceinline__ void vo_role(const Params& p, int c) {
    const int lane = __builtin_amdgcn_mbcnt_hi(~0u, __builtin_amdgcn_mbcnt_lo(~0u, 0u)), w = rfl(threadIdx.x >> 6), n = lane & 15, g = lane >> 4;
    const int rg = RGX ? c >> 5 : c & 1, cs = RGX ? c & 31 : c >> 1;
    const v4u rs = mksrd(p.ws, WS_END), rw = mksrd(p.wv + (size_t)(256 * w) * 4096 + c * 64, 255 * 8192 + 128), ro = mksrd(p.wo + (size_t)(1024 * w) * 1024 + cs * 32, 1023 * 2048 + 64), rm = mksrd(p.mem0, 16384);
    const int v0 = lane * 16, v1 = (lane >> 3) * 8192 + (lane & 7) * 16, v2 = (lane >> 2) * 2048 + (lane & 3) * 16;
    const int v3 = w * 40960 + (8 * g + (n >> 2)) * 128 + (n & 3) * 8;
    const int v4 = w * 40960 + (16 * (g & 1) + 8 * (g >> 1) + 4 * (n >> 3) + ((n >> 2) & 1)) * 64 + (n & 3) * 8;
    const int v5 = amap(HB_M + w * 4096 + mlane(n, g), 0);
    const int v6 = amap(HB_O + rg * 32768 + w * 8192 + (2 * (n >> 3) + (g >> 1)) * 256 + (g & 1) * 128 + (n & 7) * 16, 0);
    const int v7 = (w < 2 && lane < 32) ? amap(HB_V + c * 1024 + w * 512 + v0, 0) : OOB;
    const int v8 = (w == 0 && (lane & 3) == 0) ? amap(HB_M + (cs * 2 + rg) * 256 + lane * 4, 0) : OOB;
    const int v9 = amap(HB_H + (lane & 31) * 128, 0);
    const int v10 = w * 2048 + (g >> 1) * 256 + n * 16 + (g & 1) * 8, v11 = (w & 1) * 1024 + v0;
    const int v12 = (n & 2) ? 0xEEEE : 0x4444;
    const int v13 = (4 * rg + 2 * ((lane >> 4) & 1) + ((lane >> 2) & 1)) * 2048 + (32 * cs + 16 * (lane >> 5) + 2 * ((lane >> 3) & 1)) * 2;
    const int v15 = 8192 + (g * 16 + ((n >> 1) & 1) * 8 + (n >> 2)) * 32 + w * 8 + (n & 1) * 4;
    asm volatile(ASM_VO :: "{v0}"(v0), "{v1}"(v1), "{v2}"(v2), "{v3}"(v3), "{v4}"(v4), "{v5}"(v5), "{v6}"(v6), "{v7}"(v7), "{v8}"(v8), "{v9}"(v9), "{v10}"(v10), "{v11}"(v11), "{v12}"(v12),
        "{v13}"(v13), "{v15}"(v15), "{s[36:39]}"(rs), "{s[40:43]}"(rw), "{s[64:67]}"(ro), "{s[68:71]}"(rm), "{s44}"(TUNEV(0, C_O)), "{s45}"(TUNEV(1, C_P)), "{s46}"(TUNEV(2, C_H)), "{s74}"(TUNEV(3, C_D)),
        "{s47}"(WS_DBG + 131072 + ((32 + c) * 4 + w) * 2048), "{s48}"(w) : "v255", "a255", "s99", "memory");
    __builtin_unreachable();
}
__device__ __forceinline__ void probe_span(const Params& p) {
    const rsrc_t rs = __builtin_amdgcn_make_buffer_rsrc(p.ws, 0, WS_END, 0x00020000);
    const int lane = threadIdx.x & 63, base = 2 * MB - 32768;
    u32 best0 = 0xFFFFFFFFu, best1 = 0xFFFFFFFFu;
#pragma unroll 1
    for (int k = 0; k < 10; ++k) {
        u64 t0, t1;
        asm volatile("s_memtime %0\n\ts_waitcnt lgkmcnt(0)" : "=s"(t0) :: "memory");
        st32s(0u, rs, base + (k & 1) * 8192 + ((k >> 2) & 3) * 2048 + ((k >> 1) & 1) * 16384 + (lane & 31) * 4);
        WAIT_V0();
        asm volatile("s_memtime %0\n\ts_waitcnt lgkmcnt(0)" : "=s"(t1) :: "memory");
        if (k & 1) best1 = min(best1, (u32)(t1 - t0)); else best0 = min(best0, (u32)(t1 - t0));
    }
    if (lane == 0) st32s(2u + ((best1 < best0 ? 1u : 0u) ^ (xcc_id() >= 4 ? 1u : 0u)), rs, WS_CT + CT_PAR * 4);
}

__device__ __forceinline__ void s64_role(const Params& p, int kind, int cb, int r, int id, int w) {
    const int lane = __builtin_amdgcn_mbcnt_hi(~0u, __builtin_amdgcn_mbcnt_lo(~0u, 0u)), n = lane & 15, g = lane >> 4;
    const int kr = kind ? 896 : 1024;
    const v4u rs = mksrd(p.ws, WS_END), rw = mksrd((kind ? p.wd : p.wo) + (size_t)(kr * w) * 1024 + cb * 64, (kr - 1) * 2048 + 128), rx = mksrd(p.x, 2048 * 2048), ro = mksrd(p.out, 2048 * 2048);
    const int v0 = lane * 16 + w * 32768, v1 = (lane >> 3) * 2048 + (lane & 7) * 16;
    const int v2 = w * 32768 + ((kind ? 8 : 4) * g + (n >> 2)) * 128 + (n & 3) * 8;
    const int v3 = (kind ? WS_HF + w * 28 * 1024 : WS_OF + 8 * w * OF_STR) + lane * 16;
    const int v4 = WS_CT + (kind ? CT_G1 + (14 * w + min(n, 13)) * 32 : CT_AT + (8 * w + (n & 7)) * 32) * 4;
    const int v5 = n * 2048 + cb * 128 + w * 32 + g * 8, v6 = WS_GT + cb * 2048 + w * 512 + lane * 8;
    int v7 = 131072 + w * 4096 + lane * 16, v8 = 131072 + w * 1024 + lane * 16;
    const int nt = r < 3 ? 26 : 25, nf = kind ? nt - S64_NF : S64_NF;
    int rt = kind ? r + 5 * (nt - 1) : r;
    const int fl = WS_CT + (CT_S + ((cb * 5 + r) * 4 + w) * 4) * 4;
#define S64_IO "{v7}"(v7), "{v8}"(v8), "{s44}"(rt), "{v0}"(v0), "{v1}"(v1), "{v2}"(v2), "{v3}"(v3), "{v4}"(v4), "{v5}"(v5), "{v6}"(v6), "{s[36:39]}"(rs), "{s[40:43]}"(rw), "{s[64:67]}"(rx), "{s[68:71]}"(ro), \
        "{s45}"(kind ? -5 : 5), "{s46}"(nt), "{s47}"(nf), "{s48}"(w), "{s49}"(fl + kind * 8192), "{s50}"(fl + (1 - kind) * 8192), "{s51}"(w ? OOB : WS_DBG + id * 128)
    if (kind) asm volatile(ASM_S64_G2 :: S64_IO : "v255", "a255", "s99", "memory");
    else asm volatile(ASM_S64_WO :: S64_IO : "v255", "a255", "s99", "memory");
    __builtin_unreachable();
}
#define MAIN_BODY \
if (id < 32) qk_role(p, id); \
    else if (id < 96) { if (id == 95) { if (wv_ == 0) probe_span(p); __builtin_amdgcn_s_barrier(); } vo_role(p, id - 32); } \
    const int gi = id - 96, gx = gi >> 5, gr = gi & 31; \
    if (gi < 152) p1_role(p, gi, 0, 64, id, wv_); \
    STAMP(2); \
    { const int rep = gi >> 4; s64_role(p, rep < 4 || rep == 8 ? 1 : 0, gi & 15, rep < 4 ? rep : (rep < 8 ? rep - 4 : 4), id, wv_); }
constexpr int LDS_BYTES = 160 * 1024;
__device__ __forceinline__ int take_role(const Params& p, LAS u32* L) {
    if (threadIdx.x == 0) {
        GAS u32* tk = (GAS u32*)(p.ws + WS_CT) + CT_TICK;
        const u32 x = xcc_id() & 7u;
        const u32 t = __hip_atomic_fetch_add(tk + 64 * x, 1u, RLXA);
        u32 id = 32 * x + t;
        if (t >= 32u) {
            u32 o = __hip_atomic_fetch_add(tk + 64 * 8, 1u, RLXA);
            for (u32 spins = 0; spins < (1u << 20); ++spins) {
                u32 tot = 0;
                for (int k = 0; k < 8; ++k) tot += __hip_atomic_load(tk + 64 * k, RLXA);
                if (tot >= 256u) break;
                __builtin_amdgcn_s_sleep(8);
            }
            id = 255u;
            for (int k = 0; k < 8; ++k) {
                const u32 ck = __hip_atomic_load(tk + 64 * k, RLXA);
                const u32 fr = ck < 32u ? 32u - ck : 0u;
                if (o < fr) { id = 32 * k + ck + o; break; }
                o -= fr;
            }
        }
        *L = id;
    }
    BAR();
    const int id = rfl(*L);
    BAR();
    return id;
}
#define STAMP(i) do {} while (0)
__global__ void __launch_bounds__(256, 1) k_main(Params p) {
    extern __shared__ __attribute__((aligned(16))) u8 smem[];
    const int id = take_role(p, (LAS u32*)smem);
    const int wv_ = rfl(threadIdx.x >> 6);
    STAMP(0);
    MAIN_BODY
    STAMP(7);
}
__global__ void __launch_bounds__(256) k_zero(v4u* w) { w[blockIdx.x * 256 + threadIdx.x] = v4u{0u, 0u, 0u, 0u}; }

extern "C" void kernel_launch(void* const* d_in, const int* in_sizes, int n_in, void* d_out, int out_size, void* d_ws, size_t ws_size, hipStream_t stream) {
    Params p;
    p.x = (const u16*)d_in[0]; p.mem0 = (const u16*)d_in[1]; p.wq = (const u16*)d_in[2]; p.wk = (const u16*)d_in[3]; p.wv = (const u16*)d_in[4]; p.wo = (const u16*)d_in[5];
    p.wg = (const u16*)d_in[8]; p.wu = (const u16*)d_in[9]; p.wd = (const u16*)d_in[10];
    p.out = (u16*)d_out;
    p.ws = (u8*)(((uintptr_t)d_ws + (2u * MB - 1)) & ~(uintptr_t)(2u * MB - 1));
    (void)hipFuncSetAttribute((const void*)k_main, hipFuncAttributeMaxDynamicSharedMemorySize, LDS_BYTES);
    hipLaunchKernelGGL(k_zero, dim3(ZERO_BYTES / 4096), dim3(256), 0, stream, (v4u*)p.ws);
    hipLaunchKernelGGL(k_main, dim3(256), dim3(256), LDS_BYTES, stream, p);
}
```

```cpp
#pragma clang diagnostic ignored "-Wint-to-pointer-cast"
#pragma clang diagnostic ignored "-Winline-asm"
#include <hip/hip_runtime.h>
#include <hip/hip_bf16.h>
#include <stdint.h>

#define LAS __attribute__((address_space(3)))
#define GAS __attribute__((address_space(1)))
typedef unsigned u32;
typedef unsigned char u8;
typedef unsigned short u16;
typedef unsigned long long u64;
typedef u32 v2u __attribute__((ext_vector_type(2)));
typedef u32 v4u __attribute__((ext_vector_type(4)));
typedef float f32x4 __attribute__((ext_vector_type(4)));
typedef __bf16 bf16x2 __attribute__((ext_vector_type(2)));
typedef __amdgpu_buffer_rsrc_t rsrc_t;

constexpr int NSEG = 64, DM = 1024, NH = 32, FF = 3584;
constexpr int MB = 1 << 20;
constexpr int WS_CT = 0;
constexpr int WS_HB = 65536;
constexpr int HB_M = 0, HB_H = 16384, HB_V = 24576, HB_O = 90112, HB_SLOT = 163840;
constexpr int ZERO_BYTES = 65536 + 49152;
constexpr int WS_DBG = 4 * MB;
constexpr int WS_XF = 6 * MB;
constexpr int WS_TK = 10 * MB;
constexpr int WS_OF = 66 * MB;
constexpr int WS_HF = 84 * MB;
constexpr int WS_GT = 100 * MB;
constexpr int WS_END = 108 * MB;
constexpr int TK_STR = 64 * 24576 + 8192, OF_STR = 524288 + 8192;
constexpr int CT_PAR = 0, CT_TMO = 64, CT_TICK = 256;
constexpr int CT_LN = 1024;
constexpr int CT_P1 = 2048;
constexpr int CT_G1 = 4096;
constexpr int CT_AT = 3072;
constexpr int CT_S = 8192;
#define NQ0 16
#define RGX 1
#define WARM 1
#define S64_HEAD 28
constexpr int OOB = 0x7ffffff0;
constexpr int AUX_SC1 = 16;

struct Params {
    const u16 *x, *mem0, *wq, *wk, *wv, *wo, *wg, *wu, *wd;
    u16* out;
    u8* ws;
};
__device__ __forceinline__ u32 pack2(float a, float b) { bf16x2 v = {(__bf16)a, (__bf16)b}; return __builtin_bit_cast(u32, v); }
__device__ __forceinline__ float bflo(u32 d) { return __builtin_bit_cast(float, d << 16); }
__device__ __forceinline__ float bfhi(u32 d) { return __builtin_bit_cast(float, d & 0xFFFF0000u); }
__device__ __forceinline__ float rbf(float a) { return (float)(__bf16)a; }
__device__ __forceinline__ void st128s(v4u v, rsrc_t rs, int voff) { __builtin_amdgcn_raw_buffer_store_b128(v, rs, voff, 0, AUX_SC1); }
__device__ __forceinline__ void st32s(u32 v, rsrc_t rs, int voff) { __builtin_amdgcn_raw_buffer_store_b32(v, rs, voff, 0, AUX_SC1); }
__device__ __forceinline__ u32 ld32s(rsrc_t rs, int voff) { return __builtin_amdgcn_raw_buffer_load_b32(rs, voff, 0, AUX_SC1); }
__device__ __forceinline__ u32 xcc_id() { return (u32)__builtin_amdgcn_s_getreg(20 | (3 << 11)) & 0xfu; }
__device__ __forceinline__ int rfl(int x) { return __builtin_amdgcn_readfirstlane(x); }
__device__ __forceinline__ v4u mksrd(const void* p, u32 bytes) {
    const u64 a = (u64)p;
    return v4u{(u32)rfl((u32)a), (u32)rfl((u32)(a >> 32) & 0xFFFFu), (u32)rfl(bytes), 0x00020000u};
}
#define CFENCE() asm volatile("" ::: "memory")
#define WAIT_V0() asm volatile("s_waitcnt vmcnt(0)" ::: "memory")
#define BAR() do { asm volatile("s_waitcnt lgkmcnt(0)" ::: "memory"); __builtin_amdgcn_s_barrier(); asm volatile("" ::: "memory"); } while (0)
#define RLXA __ATOMIC_RELAXED, __HIP_MEMORY_SCOPE_AGENT
__device__ __forceinline__ int amap(int off, int par) { return WS_HB + ((off >> 13) << 14) + (off & 8191) + (par << 13); }

constexpr int WS_WC = 112 * MB;
__device__ __forceinline__ const u16* wmat(const Params& p, int m) { return m == 0 ? p.wq : (m == 1 ? p.wk : (m == 2 ? p.wv : p.wo)); }
__device__ __forceinline__ int slowpos(const void* b) { return (3 - (int)((u64)b >> 7)) & 7; }
__device__ __forceinline__ int slowpos2(const void* b) { return (63 - (int)((u64)b >> 7)) & 63; }
__device__ __forceinline__ const u16* colsrc(const Params& p, int m, int j) {
    const u16* b = wmat(p, m);
    if (WARM && m < 3 && j == slowpos2(b)) return (const u16*)(p.ws + WS_WC) + (5 + m) * 64;
    return (WARM && (j & 7) == slowpos(b)) ? (const u16*)(p.ws + WS_WC) + (j >> 3) * 512 + (m == 3 ? 4 : m) * 64 : b + j * 64;
}
#define ASM_P1_QK \
"s_nop 7\n" \
"s_nop 7\n" \
"s_lshr_b32 s52, s48, 1\n" \
"s_lshl_b32 s52, s52, 13\n" \
"s_and_b32 s53, s48, 1\n" \
"s_lshl_b32 s53, s53, 10\n" \
"s_add_u32 s52, s52, s53\n" \
"s_mov_b32 s54, 0\n" \
"s_add_u32 m0, s52, 0\n" \
"s_nop 0\n" \
"buffer_load_dwordx4 v1, s[40:43], s54 offen nt lds\n" \
"s_add_u32 s54, s54, s44\n" \
"s_add_u32 m0, s52, 2048\n" \
"s_nop 0\n" \
"buffer_load_dwordx4 v1, s[40:43], s54 offen nt lds\n" \
"s_add_u32 s54, s54, s44\n" \
"s_add_u32 m0, s52, 4096\n" \
"s_nop 0\n" \
"buffer_load_dwordx4 v1, s[40:43], s54 offen nt lds\n" \
"s_add_u32 s54, s54, s44\n" \
"s_add_u32 m0, s52, 6144\n" \
"s_nop 0\n" \
"buffer_load_dwordx4 v1, s[40:43], s54 offen nt lds\n" \
"s_add_u32 s54, s54, s44\n" \
"s_add_u32 m0, s52, 16384\n" \
"s_nop 0\n" \
"buffer_load_dwordx4 v1, s[40:43], s54 offen nt lds\n" \
"s_add_u32 s54, s54, s44\n" \
"s_add_u32 m0, s52, 18432\n" \
"s_nop 0\n" \
"buffer_load_dwordx4 v1, s[40:43], s54 offen nt lds\n" \
"s_add_u32 s54, s54, s44\n" \
"s_add_u32 m0, s52, 20480\n" \
"s_nop 0\n" \
"buffer_load_dwordx4 v1, s[40:43], s54 offen nt lds\n" \
"s_add_u32 s54, s54, s44\n" \
"s_add_u32 m0, s52, 22528\n" \
"s_nop 0\n" \
"buffer_load_dwordx4 v1, s[40:43], s54 offen nt lds\n" \
"s_add_u32 s54, s54, s44\n" \
"s_add_u32 m0, s52, 32768\n" \
"s_nop 0\n" \
"buffer_load_dwordx4 v1, s[40:43], s54 offen nt lds\n" \
"s_add_u32 s54, s54, s44\n" \
"s_add_u32 m0, s52, 34816\n" \
"s_nop 0\n" \
"buffer_load_dwordx4 v1, s[40:43], s54 offen nt lds\n" \
"s_add_u32 s54, s54, s44\n" \
"s_add_u32 m0, s52, 36864\n" \
"s_nop 0\n" \
"buffer_load_dwordx4 v1, s[40:43], s54 offen nt lds\n" \
"s_add_u32 s54, s54, s44\n" \
"s_add_u32 m0, s52, 38912\n" \
"s_nop 0\n" \
"buffer_load_dwordx4 v1, s[40:43], s54 offen nt lds\n" \
"s_add_u32 s54, s54, s44\n" \
"s_add_u32 m0, s52, 49152\n" \
"s_nop 0\n" \
"buffer_load_dwordx4 v1, s[40:43], s54 offen nt lds\n" \
"s_add_u32 s54, s54, s44\n" \
"s_add_u32 m0, s52, 51200\n" \
"s_nop 0\n" \
"buffer_load_dwordx4 v1, s[40:43], s54 offen nt lds\n" \
"s_add_u32 s54, s54, s44\n" \
"s_add_u32 m0, s52, 53248\n" \
"s_nop 0\n" \
"buffer_load_dwordx4 v1, s[40:43], s54 offen nt lds\n" \
"s_add_u32 s54, s54, s44\n" \
"s_add_u32 m0, s52, 55296\n" \
"s_nop 0\n" \
"buffer_load_dwordx4 v1, s[40:43], s54 offen nt lds\n" \
"s_add_u32 s54, s54, s44\n" \
"s_add_u32 m0, s52, 65536\n" \
"s_nop 0\n" \
"buffer_load_dwordx4 v1, s[40:43], s54 offen nt lds\n" \
"s_add_u32 s54, s54, s44\n" \
"s_add_u32 m0, s52, 67584\n" \
"s_nop 0\n" \
"buffer_load_dwordx4 v1, s[40:43], s54 offen nt lds\n" \
"s_add_u32 s54, s54, s44\n" \
"s_add_u32 m0, s52, 69632\n" \
"s_nop 0\n" \
"buffer_load_dwordx4 v1, s[40:43], s54 offen nt lds\n" \
"s_add_u32 s54, s54, s44\n" \
"s_add_u32 m0, s52, 71680\n" \
"s_nop 0\n" \
"buffer_load_dwordx4 v1, s[40:43], s54 offen nt lds\n" \
"s_add_u32 s54, s54, s44\n" \
"s_add_u32 m0, s52, 81920\n" \
"s_nop 0\n" \
"buffer_load_dwordx4 v1, s[40:43], s54 offen nt lds\n" \
"s_add_u32 s54, s54, s44\n" \
"s_add_u32 m0, s52, 83968\n" \
"s_nop 0\n" \
"buffer_load_dwordx4 v1, s[40:43], s54 offen nt lds\n" \
"s_add_u32 s54, s54, s44\n" \
"s_add_u32 m0, s52, 86016\n" \
"s_nop 0\n" \
"buffer_load_dwordx4 v1, s[40:43], s54 offen nt lds\n" \
"s_add_u32 s54, s54, s44\n" \
"s_add_u32 m0, s52, 88064\n" \
"s_nop 0\n" \
"buffer_load_dwordx4 v1, s[40:43], s54 offen nt lds\n" \
"s_add_u32 s54, s54, s44\n" \
"s_add_u32 m0, s52, 98304\n" \
"s_nop 0\n" \
"buffer_load_dwordx4 v1, s[40:43], s54 offen nt lds\n" \
"s_add_u32 s54, s54, s44\n" \
"s_add_u32 m0, s52, 100352\n" \
"s_nop 0\n" \
"buffer_load_dwordx4 v1, s[40:43], s54 offen nt lds\n" \
"s_add_u32 s54, s54, s44\n" \
"s_add_u32 m0, s52, 102400\n" \
"s_nop 0\n" \
"buffer_load_dwordx4 v1, s[40:43], s54 offen nt lds\n" \
"s_add_u32 s54, s54, s44\n" \
"s_add_u32 m0, s52, 104448\n" \
"s_nop 0\n" \
"buffer_load_dwordx4 v1, s[40:43], s54 offen nt lds\n" \
"s_add_u32 s54, s54, s44\n" \
"s_add_u32 m0, s52, 114688\n" \
"s_nop 0\n" \
"buffer_load_dwordx4 v1, s[40:43], s54 offen nt lds\n" \
"s_add_u32 s54, s54, s44\n" \
"s_add_u32 m0, s52, 116736\n" \
"s_nop 0\n" \
"buffer_load_dwordx4 v1, s[40:43], s54 offen nt lds\n" \
"s_add_u32 s54, s54, s44\n" \
"s_add_u32 m0, s52, 118784\n" \
"s_nop 0\n" \
"buffer_load_dwordx4 v1, s[40:43], s54 offen nt lds\n" \
"s_add_u32 s54, s54, s44\n" \
"s_add_u32 m0, s52, 120832\n" \
"s_nop 0\n" \
"buffer_load_dwordx4 v1, s[40:43], s54 offen nt lds\n" \
"s_add_u32 s54, s54, s44\n" \
"v_add_u32 v8, 0x10000, v2\n" \
"v_add_u32 v9, 0x10000, v3\n" \
"s_waitcnt vmcnt(28)\n" \
"s_barrier\n" \
"ds_read_b64_tr_b16 a[0:1], v2 offset:0\n" \
"ds_read_b64_tr_b16 a[2:3], v2 offset:512\n" \
"ds_read_b64_tr_b16 a[128:129], v3 offset:0\n" \
"ds_read_b64_tr_b16 a[130:131], v3 offset:512\n" \
"ds_read_b64_tr_b16 a[4:5], v2 offset:4096\n" \
"ds_read_b64_tr_b16 a[6:7], v2 offset:4608\n" \
"ds_read_b64_tr_b16 a[132:133], v3 offset:4096\n" \
"ds_read_b64_tr_b16 a[134:135], v3 offset:4608\n" \
"s_waitcnt lgkmcnt(0)\n" \
"s_barrier\n" \
"s_add_u32 m0, s52, 0\n" \
"s_nop 0\n" \
"buffer_load_dwordx4 v1, s[40:43], s54 offen nt lds\n" \
"s_add_u32 s54, s54, s44\n" \
"s_add_u32 m0, s52, 2048\n" \
"s_nop 0\n" \
"buffer_load_dwordx4 v1, s[40:43], s54 offen nt lds\n" \
"s_add_u32 s54, s54, s44\n" \
"s_add_u32 m0, s52, 4096\n" \
"s_nop 0\n" \
"buffer_load_dwordx4 v1, s[40:43], s54 offen nt lds\n" \
"s_add_u32 s54, s54, s44\n" \
"s_add_u32 m0, s52, 6144\n" \
"s_nop 0\n" \
"buffer_load_dwordx4 v1, s[40:43], s54 offen nt lds\n" \
"s_add_u32 s54, s54, s44\n" \
"s_waitcnt vmcnt(28)\n" \
"s_barrier\n" \
"ds_read_b64_tr_b16 a[8:9], v2 offset:16384\n" \
"ds_read_b64_tr_b16 a[10:11], v2 offset:16896\n" \
"ds_read_b64_tr_b16 a[136:137], v3 offset:16384\n" \
"ds_read_b64_tr_b16 a[138:139], v3 offset:16896\n" \
"ds_read_b64_tr_b16 a[12:13], v2 offset:20480\n" \
"ds_read_b64_tr_b16 a[14:15], v2 offset:20992\n" \
"ds_read_b64_tr_b16 a[140:141], v3 offset:20480\n" \
"ds_read_b64_tr_b16 a[142:143], v3 offset:20992\n" \
"s_waitcnt lgkmcnt(0)\n" \
"s_barrier\n" \
"s_add_u32 m0, s52, 16384\n" \
"s_nop 0\n" \
"buffer_load_dwordx4 v1, s[40:43], s54 offen nt lds\n" \
"s_add_u32 s54, s54, s44\n" \
"s_add_u32 m0, s52, 18432\n" \
"s_nop 0\n" \
"buffer_load_dwordx4 v1, s[40:43], s54 offen nt lds\n" \
"s_add_u32 s54, s54, s44\n" \
"s_add_u32 m0, s52, 20480\n" \
"s_nop 0\n" \
"buffer_load_dwordx4 v1, s[40:43], s54 offen nt lds\n" \
"s_add_u32 s54, s54, s44\n" \
"s_add_u32 m0, s52, 22528\n" \
"s_nop 0\n" \
"buffer_load_dwordx4 v1, s[40:43], s54 offen nt lds\n" \
"s_add_u32 s54, s54, s44\n" \
"s_waitcnt vmcnt(28)\n" \
"s_barrier\n" \
"ds_read_b64_tr_b16 a[16:17], v2 offset:32768\n" \
"ds_read_b64_tr_b16 a[18:19], v2 offset:33280\n" \
"ds_read_b64_tr_b16 a[144:145], v3 offset:32768\n" \
"ds_read_b64_tr_b16 a[146:147], v3 offset:33280\n" \
"ds_read_b64_tr_b16 a[20:21], v2 offset:36864\n" \
"ds_read_b64_tr_b16 a[22:23], v2 offset:37376\n" \
"ds_read_b64_tr_b16 a[148:149], v3 offset:36864\n" \
"ds_read_b64_tr_b16 a[150:151], v3 offset:37376\n" \
"s_waitcnt lgkmcnt(0)\n" \
"s_barrier\n" \
"s_add_u32 m0, s52, 32768\n" \
"s_nop 0\n" \
"buffer_load_dwordx4 v1, s[40:43], s54 offen nt lds\n" \
"s_add_u32 s54, s54, s44\n" \
"s_add_u32 m0, s52, 34816\n" \
"s_nop 0\n" \
"buffer_load_dwordx4 v1, s[40:43], s54 offen nt lds\n" \
"s_add_u32 s54, s54, s44\n" \
"s_add_u32 m0, s52, 36864\n" \
"s_nop 0\n" \
"buffer_load_dwordx4 v1, s[40:43], s54 offen nt lds\n" \
"s_add_u32 s54, s54, s44\n" \
"s_add_u32 m0, s52, 38912\n" \
"s_nop 0\n" \
"buffer_load_dwordx4 v1, s[40:43], s54 offen nt lds\n" \
"s_add_u32 s54, s54, s44\n" \
"s_waitcnt vmcnt(28)\n" \
"s_barrier\n" \
"ds_read_b64_tr_b16 a[24:25], v2 offset:49152\n" \
"ds_read_b64_tr_b16 a[26:27], v2 offset:49664\n" \
"ds_read_b64_tr_b16 a[152:153], v3 offset:49152\n" \
"ds_read_b64_tr_b16 a[154:155], v3 offset:49664\n" \
"ds_read_b64_tr_b16 a[28:29], v2 offset:53248\n" \
"ds_read_b64_tr_b16 a[30:31], v2 offset:53760\n" \
"ds_read_b64_tr_b16 a[156:157], v3 offset:53248\n" \
"ds_read_b64_tr_b16 a[158:159], v3 offset:53760\n" \
"s_waitcnt lgkmcnt(0)\n" \
"s_barrier\n" \
"s_add_u32 m0, s52, 49152\n" \
"s_nop 0\n" \
"buffer_load_dwordx4 v1, s[40:43], s54 offen nt lds\n" \
"s_add_u32 s54, s54, s44\n" \
"s_add_u32 m0, s52, 51200\n" \
"s_nop 0\n" \
"buffer_load_dwordx4 v1, s[40:43], s54 offen nt lds\n" \
"s_add_u32 s54, s54, s44\n" \
"s_add_u32 m0, s52, 53248\n" \
"s_nop 0\n" \
"buffer_load_dwordx4 v1, s[40:43], s54 offen nt lds\n" \
"s_add_u32 s54, s54, s44\n" \
"s_add_u32 m0, s52, 55296\n" \
"s_nop 0\n" \
"buffer_load_dwordx4 v1, s[40:43], s54 offen nt lds\n" \
"s_add_u32 s54, s54, s44\n" \
"s_waitcnt vmcnt(28)\n" \
"s_barrier\n" \
"ds_read_b64_tr_b16 a[32:33], v8 offset:0\n" \
"ds_read_b64_tr_b16 a[34:35], v8 offset:512\n" \
"ds_read_b64_tr_b16 a[160:161], v9 offset:0\n" \
"ds_read_b64_tr_b16 a[162:163], v9 offset:512\n" \
"ds_read_b64_tr_b16 a[36:37], v8 offset:4096\n" \
"ds_read_b64_tr_b16 a[38:39], v8 offset:4608\n" \
"ds_read_b64_tr_b16 a[164:165], v9 offset:4096\n" \
"ds_read_b64_tr_b16 a[166:167], v9 offset:4608\n" \
"s_waitcnt lgkmcnt(0)\n" \
"s_barrier\n" \
"s_add_u32 m0, s52, 65536\n" \
"s_nop 0\n" \
"buffer_load_dwordx4 v1, s[40:43], s54 offen nt lds\n" \
"s_add_u32 s54, s54, s44\n" \
"s_add_u32 m0, s52, 67584\n" \
"s_nop 0\n" \
"buffer_load_dwordx4 v1, s[40:43], s54 offen nt lds\n" \
"s_add_u32 s54, s54, s44\n" \
"s_add_u32 m0, s52, 69632\n" \
"s_nop 0\n" \
"buffer_load_dwordx4 v1, s[40:43], s54 offen nt lds\n" \
"s_add_u32 s54, s54, s44\n" \
"s_add_u32 m0, s52, 71680\n" \
"s_nop 0\n" \
"buffer_load_dwordx4 v1, s[40:43], s54 offen nt lds\n" \
"s_add_u32 s54, s54, s44\n" \
"s_waitcnt vmcnt(28)\n" \
"s_barrier\n" \
"ds_read_b64_tr_b16 a[40:41], v8 offset:16384\n" \
"ds_read_b64_tr_b16 a[42:43], v8 offset:16896\n" \
"ds_read_b64_tr_b16 a[168:169], v9 offset:16384\n" \
"ds_read_b64_tr_b16 a[170:171], v9 offset:16896\n" \
"ds_read_b64_tr_b16 a[44:45], v8 offset:20480\n" \
"ds_read_b64_tr_b16 a[46:47], v8 offset:20992\n" \
"ds_read_b64_tr_b16 a[172:173], v9 offset:20480\n" \
"ds_read_b64_tr_b16 a[174:175], v9 offset:20992\n" \
"s_waitcnt lgkmcnt(0)\n" \
"s_barrier\n" \
"s_add_u32 m0, s52, 81920\n" \
"s_nop 0\n" \
"buffer_load_dwordx4 v1, s[40:43], s54 offen nt lds\n" \
"s_add_u32 s54, s54, s44\n" \
"s_add_u32 m0, s52, 83968\n" \
"s_nop 0\n" \
"buffer_load_dwordx4 v1, s[40:43], s54 offen nt lds\n" \
"s_add_u32 s54, s54, s44\n" \
"s_add_u32 m0, s52, 86016\n" \
"s_nop 0\n" \
"buffer_load_dwordx4 v1, s[40:43], s54 offen nt lds\n" \
"s_add_u32 s54, s54, s44\n" \
"s_add_u32 m0, s52, 88064\n" \
"s_nop 0\n" \
"buffer_load_dwordx4 v1, s[40:43], s54 offen nt lds\n" \
"s_add_u32 s54, s54, s44\n" \
"s_waitcnt vmcnt(28)\n" \
"s_barrier\n" \
"ds_read_b64_tr_b16 a[48:49], v8 offset:32768\n" \
"ds_read_b64_tr_b16 a[50:51], v8 offset:33280\n" \
"ds_read_b64_tr_b16 a[176:177], v9 offset:32768\n" \
"ds_read_b64_tr_b16 a[178:179], v9 offset:33280\n" \
"ds_read_b64_tr_b16 a[52:53], v8 offset:36864\n" \
"ds_read_b64_tr_b16 a[54:55], v8 offset:37376\n" \
"ds_read_b64_tr_b16 a[180:181], v9 offset:36864\n" \
"ds_read_b64_tr_b16 a[182:183], v9 offset:37376\n" \
"s_waitcnt lgkmcnt(0)\n" \
"s_barrier\n" \
"s_add_u32 m0, s52, 98304\n" \
"s_nop 0\n" \
"buffer_load_dwordx4 v1, s[40:43], s54 offen nt lds\n" \
"s_add_u32 s54, s54, s44\n" \
"s_add_u32 m0, s52, 100352\n" \
"s_nop 0\n" \
"buffer_load_dwordx4 v1, s[40:43], s54 offen nt lds\n" \
"s_add_u32 s54, s54, s44\n" \
"s_add_u32 m0, s52, 102400\n" \
"s_nop 0\n" \
"buffer_load_dwordx4 v1, s[40:43], s54 offen nt lds\n" \
"s_add_u32 s54, s54, s44\n" \
"s_add_u32 m0, s52, 104448\n" \
"s_nop 0\n" \
"buffer_load_dwordx4 v1, s[40:43], s54 offen nt lds\n" \
"s_add_u32 s54, s54, s44\n" \
"s_waitcnt vmcnt(28)\n" \
"s_barrier\n" \
"ds_read_b64_tr_b16 a[56:57], v8 offset:49152\n" \
"ds_read_b64_tr_b16 a[58:59], v8 offset:49664\n" \
"ds_read_b64_tr_b16 a[184:185], v9 offset:49152\n" \
"ds_read_b64_tr_b16 a[186:187], v9 offset:49664\n" \
"ds_read_b64_tr_b16 a[60:61], v8 offset:53248\n" \
"ds_read_b64_tr_b16 a[62:63], v8 offset:53760\n" \
"ds_read_b64_tr_b16 a[188:189], v9 offset:53248\n" \
"ds_read_b64_tr_b16 a[190:191], v9 offset:53760\n" \
"s_waitcnt lgkmcnt(0)\n" \
"s_barrier\n" \
"s_add_u32 m0, s52, 114688\n" \
"s_nop 0\n" \
"buffer_load_dwordx4 v1, s[40:43], s54 offen nt lds\n" \
"s_add_u32 s54, s54, s44\n" \
"s_add_u32 m0, s52, 116736\n" \
"s_nop 0\n" \
"buffer_load_dwordx4 v1, s[40:43], s54 offen nt lds\n" \
"s_add_u32 s54, s54, s44\n" \
"s_add_u32 m0, s52, 118784\n" \
"s_nop 0\n" \
"buffer_load_dwordx4 v1, s[40:43], s54 offen nt lds\n" \
"s_add_u32 s54, s54, s44\n" \
"s_add_u32 m0, s52, 120832\n" \
"s_nop 0\n" \
"buffer_load_dwordx4 v1, s[40:43], s54 offen nt lds\n" \
"s_add_u32 s54, s54, s44\n" \
"s_waitcnt vmcnt(28)\n" \
"s_barrier\n" \
"ds_read_b64_tr_b16 a[64:65], v2 offset:0\n" \
"ds_read_b64_tr_b16 a[66:67], v2 offset:512\n" \
"ds_read_b64_tr_b16 a[192:193], v3 offset:0\n" \
"ds_read_b64_tr_b16 a[194:195], v3 offset:512\n" \
"ds_read_b64_tr_b16 a[68:69], v2 offset:4096\n" \
"ds_read_b64_tr_b16 a[70:71], v2 offset:4608\n" \
"ds_read_b64_tr_b16 a[196:197], v3 offset:4096\n" \
"ds_read_b64_tr_b16 a[198:199], v3 offset:4608\n" \
"s_waitcnt vmcnt(24)\n" \
"s_barrier\n" \
"ds_read_b64_tr_b16 a[72:73], v2 offset:16384\n" \
"ds_read_b64_tr_b16 a[74:75], v2 offset:16896\n" \
"ds_read_b64_tr_b16 a[200:201], v3 offset:16384\n" \
"ds_read_b64_tr_b16 a[202:203], v3 offset:16896\n" \
"ds_read_b64_tr_b16 a[76:77], v2 offset:20480\n" \
"ds_read_b64_tr_b16 a[78:79], v2 offset:20992\n" \
"ds_read_b64_tr_b16 a[204:205], v3 offset:20480\n" \
"ds_read_b64_tr_b16 a[206:207], v3 offset:20992\n" \
"s_waitcnt vmcnt(20)\n" \
"s_barrier\n" \
"ds_read_b64_tr_b16 a[80:81], v2 offset:32768\n" \
"ds_read_b64_tr_b16 a[82:83], v2 offset:33280\n" \
"ds_read_b64_tr_b16 a[208:209], v3 offset:32768\n" \
"ds_read_b64_tr_b16 a[210:211], v3 offset:33280\n" \
"ds_read_b64_tr_b16 a[84:85], v2 offset:36864\n" \
"ds_read_b64_tr_b16 a[86:87], v2 offset:37376\n" \
"ds_read_b64_tr_b16 a[212:213], v3 offset:36864\n" \
"ds_read_b64_tr_b16 a[214:215], v3 offset:37376\n" \
"s_waitcnt vmcnt(16)\n" \
"s_barrier\n" \
"ds_read_b64_tr_b16 a[88:89], v2 offset:49152\n" \
"ds_read_b64_tr_b16 a[90:91], v2 offset:49664\n" \
"ds_read_b64_tr_b16 a[216:217], v3 offset:49152\n" \
"ds_read_b64_tr_b16 a[218:219], v3 offset:49664\n" \
"ds_read_b64_tr_b16 a[92:93], v2 offset:53248\n" \
"ds_read_b64_tr_b16 a[94:95], v2 offset:53760\n" \
"ds_read_b64_tr_b16 a[220:221], v3 offset:53248\n" \
"ds_read_b64_tr_b16 a[222:223], v3 offset:53760\n" \
"s_waitcnt vmcnt(12)\n" \
"s_barrier\n" \
"ds_read_b64_tr_b16 a[96:97], v8 offset:0\n" \
"ds_read_b64_tr_b16 a[98:99], v8 offset:512\n" \
"ds_read_b64_tr_b16 a[224:225], v9 offset:0\n" \
"ds_read_b64_tr_b16 a[226:227], v9 offset:512\n" \
"ds_read_b64_tr_b16 a[100:101], v8 offset:4096\n" \
"ds_read_b64_tr_b16 a[102:103], v8 offset:4608\n" \
"ds_read_b64_tr_b16 a[228:229], v9 offset:4096\n" \
"ds_read_b64_tr_b16 a[230:231], v9 offset:4608\n" \
"s_waitcnt vmcnt(8)\n" \
"s_barrier\n" \
"ds_read_b64_tr_b16 a[104:105], v8 offset:16384\n" \
"ds_read_b64_tr_b16 a[106:107], v8 offset:16896\n" \
"ds_read_b64_tr_b16 a[232:233], v9 offset:16384\n" \
"ds_read_b64_tr_b16 a[234:235], v9 offset:16896\n" \
"ds_read_b64_tr_b16 a[108:109], v8 offset:20480\n" \
"ds_read_b64_tr_b16 a[110:111], v8 offset:20992\n" \
"ds_read_b64_tr_b16 a[236:237], v9 offset:20480\n" \
"ds_read_b64_tr_b16 a[238:239], v9 offset:20992\n" \
"s_waitcnt vmcnt(4)\n" \
"s_barrier\n" \
"ds_read_b64_tr_b16 a[112:113], v8 offset:32768\n" \
"ds_read_b64_tr_b16 a[114:115], v8 offset:33280\n" \
"ds_read_b64_tr_b16 a[240:241], v9 offset:32768\n" \
"ds_read_b64_tr_b16 a[242:243], v9 offset:33280\n" \
"ds_read_b64_tr_b16 a[116:117], v8 offset:36864\n" \
"ds_read_b64_tr_b16 a[118:119], v8 offset:37376\n" \
"ds_read_b64_tr_b16 a[244:245], v9 offset:36864\n" \
"ds_read_b64_tr_b16 a[246:247], v9 offset:37376\n" \
"s_waitcnt vmcnt(0)\n" \
"s_barrier\n" \
"ds_read_b64_tr_b16 a[120:121], v8 offset:49152\n" \
"ds_read_b64_tr_b16 a[122:123], v8 offset:49664\n" \
"ds_read_b64_tr_b16 a[248:249], v9 offset:49152\n" \
"ds_read_b64_tr_b16 a[250:251], v9 offset:49664\n" \
"ds_read_b64_tr_b16 a[124:125], v8 offset:53248\n" \
"ds_read_b64_tr_b16 a[126:127], v8 offset:53760\n" \
"ds_read_b64_tr_b16 a[252:253], v9 offset:53248\n" \
"ds_read_b64_tr_b16 a[254:255], v9 offset:53760\n" \
"s_waitcnt lgkmcnt(0)\n" \
"s_barrier\n" \
"v_mov_b32 v14, 4096\n" \
"s_mov_b32 s59, s46\n" \
"s_add_u32 s63, s59, 0\n" \
"s_min_u32 s63, s63, 63\n" \
"s_lshr_b32 s63, s63, 3\n" \
"s_lshl_b32 s63, s63, 7\n" \
"s_branch .Lp1qk_l0_%=\n" \
".Lp1qk_l0k_%=:\n" \
"s_add_u32 s63, s59, 1\n" \
"s_min_u32 s63, s63, 63\n" \
"s_lshr_b32 s63, s63, 3\n" \
"s_lshl_b32 s63, s63, 7\n" \
"s_branch .Lp1qk_l1_%=\n" \
".Lp1qk_l1k_%=:\n" \
"s_lshl_b32 s55, s48, 13\n" \
"s_lshl_b32 s56, s46, 16\n" \
"s_lshl_b32 s60, s47, 16\n" \
"s_sub_u32 s60, s60, 0x10000\n" \
"s_mul_i32 s57, s46, s49\n" \
"s_sub_u32 s57, s57, s49\n" \
"s_add_u32 s58, s57, s50\n" \
"v_mov_b32 v7, v5\n" \
"v_mov_b32 v5, 0x7ffffff0\n" \
"v_mov_b32 v10, v0\n" \
"v_mov_b32 v11, v6\n" \
"v_mov_b32 v6, 0x7ffffff0\n" \
"s_mov_b32 s59, s46\n" \
"s_min_u32 s61, s56, s60\n" \
"s_add_u32 m0, s55, 0\n" \
"s_nop 0\n" \
"buffer_load_dwordx4 v4, s[36:39], s61 offen offset:0 sc1 lds\n" \
"buffer_load_dwordx4 v4, s[36:39], s61 offen offset:1024 sc1 lds\n" \
"buffer_load_dwordx4 v4, s[36:39], s61 offen offset:2048 sc1 lds\n" \
"buffer_load_dwordx4 v4, s[36:39], s61 offen offset:3072 sc1 lds\n" \
"s_add_u32 m0, s55, 4096\n" \
"s_add_u32 s61, s61, 4096\n" \
"buffer_load_dwordx4 v4, s[36:39], s61 offen offset:0 sc1 lds\n" \
"buffer_load_dwordx4 v4, s[36:39], s61 offen offset:1024 sc1 lds\n" \
"buffer_load_dwordx4 v4, s[36:39], s61 offen offset:2048 sc1 lds\n" \
"buffer_load_dwordx4 v4, s[36:39], s61 offen offset:3072 sc1 lds\n" \
"s_min_u32 s61, s56, s60\n" \
"s_add_u32 s61, s61, 16384\n" \
"s_add_u32 s56, s56, 0x10000\n" \
"s_add_u32 m0, s55, 32768\n" \
"s_nop 0\n" \
"buffer_load_dwordx4 v4, s[36:39], s61 offen offset:0 sc1 lds\n" \
"buffer_load_dwordx4 v4, s[36:39], s61 offen offset:1024 sc1 lds\n" \
"buffer_load_dwordx4 v4, s[36:39], s61 offen offset:2048 sc1 lds\n" \
"buffer_load_dwordx4 v4, s[36:39], s61 offen offset:3072 sc1 lds\n" \
"s_add_u32 m0, s55, 36864\n" \
"s_add_u32 s61, s61, 4096\n" \
"buffer_load_dwordx4 v4, s[36:39], s61 offen offset:0 sc1 lds\n" \
"buffer_load_dwordx4 v4, s[36:39], s61 offen offset:1024 sc1 lds\n" \
"buffer_load_dwordx4 v4, s[36:39], s61 offen offset:2048 sc1 lds\n" \
"buffer_load_dwordx4 v4, s[36:39], s61 offen offset:3072 sc1 lds\n" \
"s_mov_b32 s70, 0\n" \
"s_mov_b32 s72, 0\n" \
"s_waitcnt vmcnt(0) lgkmcnt(0)\n" \
"s_barrier\n" \
"v_add_u32 v12, 0x10000, v10\n" \
"ds_read_b128 v[16:19], v10 offset:0\n" \
"ds_read_b128 v[20:23], v10 offset:16384\n" \
"ds_read_b128 v[24:27], v10 offset:1024\n" \
"ds_read_b128 v[28:31], v10 offset:17408\n" \
"ds_read_b128 v[32:35], v10 offset:2048\n" \
"ds_read_b128 v[36:39], v10 offset:18432\n" \
"s_waitcnt lgkmcnt(4)\n" \
"s_nop 7\n" \
"s_nop 7\n" \
".p2align 6\n" \
".Lp1qk_loop_%=:\n" \
"v_mfma_f32_16x16x32_bf16 v[48:51], a[0:3], v[16:19], 0\n" \
"s_min_u32 s61, s56, s60\n" \
"s_add_u32 m0, s55, 65536\n" \
"s_nop 0\n" \
"buffer_load_dwordx4 v4, s[36:39], s61 offen offset:0 sc1 lds\n" \
"v_mov_b32_e64 v13, s59\n" \
"v_mfma_f32_16x16x32_bf16 v[52:55], a[0:3], v[20:23], 0\n" \
"ds_read_b128 v[40:43], v10 offset:3072\n" \
"s_nop 0\n" \
"v_mfma_f32_16x16x32_bf16 v[56:59], a[128:131], v[16:19], 0\n" \
"ds_read_b128 v[44:47], v10 offset:19456\n" \
"s_nop 0\n" \
"v_mfma_f32_16x16x32_bf16 v[60:63], a[128:131], v[20:23], 0\n" \
"s_waitcnt lgkmcnt(4)\n" \
"v_mul_f32 v64, s51, v64\n" \
"v_mul_f32_e64 v65, s51, v65\n" \
"v_mfma_f32_16x16x32_bf16 v[48:51], a[4:7], v[24:27], v[48:51]\n" \
"buffer_load_dwordx4 v4, s[36:39], s61 offen offset:1024 sc1 lds\n" \
"v_mfma_f32_16x16x32_bf16 v[52:55], a[4:7], v[28:31], v[52:55]\n" \
"ds_read_b128 v[16:19], v10 offset:4096\n" \
"v_mul_f32_e64 v66, s51, v66\n" \
"v_mfma_f32_16x16x32_bf16 v[56:59], a[132:135], v[24:27], v[56:59]\n" \
"ds_read_b128 v[20:23], v10 offset:20480\n" \
"v_mul_f32_e64 v67, s51, v67\n" \
"v_mfma_f32_16x16x32_bf16 v[60:63], a[132:135], v[28:31], v[60:63]\n" \
"s_waitcnt lgkmcnt(4)\n" \
"v_mul_f32 v72, s51, v72\n" \
"v_mul_f32_e64 v73, s51, v73\n" \
"v_mfma_f32_16x16x32_bf16 v[48:51], a[8:11], v[32:35], v[48:51]\n" \
"s_add_u32 s63, s59, 2\n" \
"s_min_u32 s63, s63, 63\n" \
"v_mfma_f32_16x16x32_bf16 v[52:55], a[8:11], v[36:39], v[52:55]\n" \
"ds_read_b128 v[24:27], v10 offset:5120\n" \
"s_lshr_b32 s63, s63, 3\n" \
"s_lshl_b32 s63, s63, 7\n" \
"v_mul_f32_e64 v74, s51, v74\n" \
"v_mfma_f32_16x16x32_bf16 v[56:59], a[136:139], v[32:35], v[56:59]\n" \
"ds_read_b128 v[28:31], v10 offset:21504\n" \
"v_mul_f32_e64 v75, s51, v75\n" \
"v_mfma_f32_16x16x32_bf16 v[60:63], a[136:139], v[36:39], v[60:63]\n" \
"s_waitcnt lgkmcnt(4)\n" \
"v_cvt_pk_bf16_f32 v80, v64, v65\n" \
"v_cvt_pk_bf16_f32 v81, v66, v67\n" \
"v_mfma_f32_16x16x32_bf16 v[48:51], a[12:15], v[40:43], v[48:51]\n" \
"buffer_load_dwordx4 v4, s[36:39], s61 offen offset:2048 sc1 lds\n" \
"v_mfma_f32_16x16x32_bf16 v[52:55], a[12:15], v[44:47], v[52:55]\n" \
"ds_read_b128 v[32:35], v10 offset:6144\n" \
"v_cvt_pk_bf16_f32 v82, v72, v73\n" \
"v_mfma_f32_16x16x32_bf16 v[56:59], a[140:143], v[40:43], v[56:59]\n" \
"ds_read_b128 v[36:39], v10 offset:22528\n" \
"v_cvt_pk_bf16_f32 v83, v74, v75\n" \
"v_mfma_f32_16x16x32_bf16 v[60:63], a[140:143], v[44:47], v[60:63]\n" \
"s_waitcnt lgkmcnt(4)\n" \
"buffer_store_dwordx4 v[80:83], v5, s[36:39], s57 offen sc1\n" \
"v_mul_f32_e64 v68, s51, v68\n" \
"v_mfma_f32_16x16x32_bf16 v[48:51], a[16:19], v[16:19], v[48:51]\n" \
"buffer_load_dwordx4 v4, s[36:39], s61 offen offset:3072 sc1 lds\n" \
"v_mfma_f32_16x16x32_bf16 v[52:55], a[16:19], v[20:23], v[52:55]\n" \
"ds_read_b128 v[40:43], v10 offset:7168\n" \
"v_mul_f32_e64 v69, s51, v69\n" \
"v_mfma_f32_16x16x32_bf16 v[56:59], a[144:147], v[16:19], v[56:59]\n" \
"ds_read_b128 v[44:47], v10 offset:23552\n" \
"v_mul_f32_e64 v70, s51, v70\n" \
"v_mfma_f32_16x16x32_bf16 v[60:63], a[144:147], v[20:23], v[60:63]\n" \
"s_waitcnt lgkmcnt(4)\n" \
"v_mul_f32 v71, s51, v71\n" \
"v_mul_f32_e64 v76, s51, v76\n" \
"v_mfma_f32_16x16x32_bf16 v[48:51], a[20:23], v[24:27], v[48:51]\n" \
"buffer_load_dword v15, v14, s[36:39], s63 offen sc1\n" \
"v_mfma_f32_16x16x32_bf16 v[52:55], a[20:23], v[28:31], v[52:55]\n" \
"ds_read_b128 v[16:19], v10 offset:8192\n" \
"v_mul_f32_e64 v77, s51, v77\n" \
"v_mfma_f32_16x16x32_bf16 v[56:59], a[148:151], v[24:27], v[56:59]\n" \
"ds_read_b128 v[20:23], v10 offset:24576\n" \
"v_mul_f32_e64 v78, s51, v78\n" \
"v_mfma_f32_16x16x32_bf16 v[60:63], a[148:151], v[28:31], v[60:63]\n" \
"s_waitcnt lgkmcnt(4)\n" \
"v_mul_f32 v79, s51, v79\n" \
"v_cvt_pk_bf16_f32 v84, v68, v69\n" \
"v_mfma_f32_16x16x32_bf16 v[48:51], a[24:27], v[32:35], v[48:51]\n" \
"s_add_u32 m0, s55, 69632\n" \
"s_add_u32 s61, s61, 4096\n" \
"buffer_load_dwordx4 v4, s[36:39], s61 offen offset:0 sc1 lds\n" \
"v_mfma_f32_16x16x32_bf16 v[52:55], a[24:27], v[36:39], v[52:55]\n" \
"ds_read_b128 v[24:27], v10 offset:9216\n" \
"v_cvt_pk_bf16_f32 v85, v70, v71\n" \
"v_mfma_f32_16x16x32_bf16 v[56:59], a[152:155], v[32:35], v[56:59]\n" \
"ds_read_b128 v[28:31], v10 offset:25600\n" \
"v_cvt_pk_bf16_f32 v86, v76, v77\n" \
"v_mfma_f32_16x16x32_bf16 v[60:63], a[152:155], v[36:39], v[60:63]\n" \
"s_waitcnt lgkmcnt(4)\n" \
"v_cvt_pk_bf16_f32 v87, v78, v79\n" \
"buffer_store_dwordx4 v[84:87], v5, s[36:39], s58 offen sc1\n" \
"v_mfma_f32_16x16x32_bf16 v[48:51], a[28:31], v[40:43], v[48:51]\n" \
"buffer_load_dwordx4 v4, s[36:39], s61 offen offset:1024 sc1 lds\n" \
"v_mfma_f32_16x16x32_bf16 v[52:55], a[28:31], v[44:47], v[52:55]\n" \
"ds_read_b128 v[32:35], v10 offset:10240\n" \
"v_mfma_f32_16x16x32_bf16 v[56:59], a[156:159], v[40:43], v[56:59]\n" \
"ds_read_b128 v[36:39], v10 offset:26624\n" \
"v_mfma_f32_16x16x32_bf16 v[60:63], a[156:159], v[44:47], v[60:63]\n" \
"s_waitcnt lgkmcnt(4)\n" \
"v_mfma_f32_16x16x32_bf16 v[48:51], a[32:35], v[16:19], v[48:51]\n" \
"v_mfma_f32_16x16x32_bf16 v[52:55], a[32:35], v[20:23], v[52:55]\n" \
"ds_read_b128 v[40:43], v10 offset:11264\n" \
"v_mfma_f32_16x16x32_bf16 v[56:59], a[160:163], v[16:19], v[56:59]\n" \
"ds_read_b128 v[44:47], v10 offset:27648\n" \
"v_mfma_f32_16x16x32_bf16 v[60:63], a[160:163], v[20:23], v[60:63]\n" \
"s_waitcnt lgkmcnt(4)\n" \
"v_mfma_f32_16x16x32_bf16 v[48:51], a[36:39], v[24:27], v[48:51]\n" \
"buffer_load_dwordx4 v4, s[36:39], s61 offen offset:2048 sc1 lds\n" \
"v_mfma_f32_16x16x32_bf16 v[52:55], a[36:39], v[28:31], v[52:55]\n" \
"ds_read_b128 v[16:19], v10 offset:12288\n" \
"v_mfma_f32_16x16x32_bf16 v[56:59], a[164:167], v[24:27], v[56:59]\n" \
"ds_read_b128 v[20:23], v10 offset:28672\n" \
"v_mfma_f32_16x16x32_bf16 v[60:63], a[164:167], v[28:31], v[60:63]\n" \
"s_waitcnt lgkmcnt(4)\n" \
"v_mfma_f32_16x16x32_bf16 v[48:51], a[40:43], v[32:35], v[48:51]\n" \
"buffer_load_dwordx4 v4, s[36:39], s61 offen offset:3072 sc1 lds\n" \
"v_mfma_f32_16x16x32_bf16 v[52:55], a[40:43], v[36:39], v[52:55]\n" \
"ds_read_b128 v[24:27], v10 offset:13312\n" \
"v_mfma_f32_16x16x32_bf16 v[56:59], a[168:171], v[32:35], v[56:59]\n" \
"ds_read_b128 v[28:31], v10 offset:29696\n" \
"v_mfma_f32_16x16x32_bf16 v[60:63], a[168:171], v[36:39], v[60:63]\n" \
"s_waitcnt lgkmcnt(4)\n" \
"v_mfma_f32_16x16x32_bf16 v[48:51], a[44:47], v[40:43], v[48:51]\n" \
"v_mfma_f32_16x16x32_bf16 v[52:55], a[44:47], v[44:47], v[52:55]\n" \
"ds_read_b128 v[32:35], v10 offset:14336\n" \
"v_mfma_f32_16x16x32_bf16 v[56:59], a[172:175], v[40:43], v[56:59]\n" \
"ds_read_b128 v[36:39], v10 offset:30720\n" \
"v_mfma_f32_16x16x32_bf16 v[60:63], a[172:175], v[44:47], v[60:63]\n" \
"s_waitcnt lgkmcnt(4)\n" \
"v_mfma_f32_16x16x32_bf16 v[48:51], a[48:51], v[16:19], v[48:51]\n" \
"v_mfma_f32_16x16x32_bf16 v[52:55], a[48:51], v[20:23], v[52:55]\n" \
"ds_read_b128 v[40:43], v10 offset:15360\n" \
"v_mfma_f32_16x16x32_bf16 v[56:59], a[176:179], v[16:19], v[56:59]\n" \
"ds_read_b128 v[44:47], v10 offset:31744\n" \
"v_mfma_f32_16x16x32_bf16 v[60:63], a[176:179], v[20:23], v[60:63]\n" \
"s_waitcnt lgkmcnt(4)\n" \
"v_mfma_f32_16x16x32_bf16 v[48:51], a[52:55], v[24:27], v[48:51]\n" \
"s_waitcnt vmcnt(12)\n" \
"s_barrier\n" \
"v_mfma_f32_16x16x32_bf16 v[52:55], a[52:55], v[28:31], v[52:55]\n" \
"ds_read_b128 v[16:19], v10 offset:32768\n" \
"v_mfma_f32_16x16x32_bf16 v[56:59], a[180:183], v[24:27], v[56:59]\n" \
"ds_read_b128 v[20:23], v10 offset:49152\n" \
"v_mfma_f32_16x16x32_bf16 v[60:63], a[180:183], v[28:31], v[60:63]\n" \
"s_waitcnt lgkmcnt(4)\n" \
"v_mfma_f32_16x16x32_bf16 v[48:51], a[56:59], v[32:35], v[48:51]\n" \
"v_mfma_f32_16x16x32_bf16 v[52:55], a[56:59], v[36:39], v[52:55]\n" \
"ds_read_b128 v[24:27], v10 offset:33792\n" \
"v_mfma_f32_16x16x32_bf16 v[56:59], a[184:187], v[32:35], v[56:59]\n" \
"ds_read_b128 v[28:31], v10 offset:50176\n" \
"v_mfma_f32_16x16x32_bf16 v[60:63], a[184:187], v[36:39], v[60:63]\n" \
"s_waitcnt lgkmcnt(4)\n" \
"v_mfma_f32_16x16x32_bf16 v[48:51], a[60:63], v[40:43], v[48:51]\n" \
"v_mfma_f32_16x16x32_bf16 v[52:55], a[60:63], v[44:47], v[52:55]\n" \
"ds_read_b128 v[32:35], v10 offset:34816\n" \
"v_mfma_f32_16x16x32_bf16 v[56:59], a[188:191], v[40:43], v[56:59]\n" \
"ds_read_b128 v[36:39], v10 offset:51200\n" \
"v_mfma_f32_16x16x32_bf16 v[60:63], a[188:191], v[44:47], v[60:63]\n" \
"s_waitcnt lgkmcnt(4)\n" \
"v_mfma_f32_16x16x32_bf16 v[48:51], a[64:67], v[16:19], v[48:51]\n" \
"s_min_u32 s61, s56, s60\n" \
"s_add_u32 s61, s61, 16384\n" \
"s_add_u32 s56, s56, 0x10000\n" \
"s_add_u32 m0, s55, 98304\n" \
"s_nop 0\n" \
"buffer_load_dwordx4 v4, s[36:39], s61 offen offset:0 sc1 lds\n" \
"v_mfma_f32_16x16x32_bf16 v[52:55], a[64:67], v[20:23], v[52:55]\n" \
"ds_read_b128 v[40:43], v10 offset:35840\n" \
"v_mfma_f32_16x16x32_bf16 v[56:59], a[192:195], v[16:19], v[56:59]\n" \
"ds_read_b128 v[44:47], v10 offset:52224\n" \
"v_mfma_f32_16x16x32_bf16 v[60:63], a[192:195], v[20:23], v[60:63]\n" \
"s_waitcnt lgkmcnt(4)\n" \
"v_mfma_f32_16x16x32_bf16 v[48:51], a[68:71], v[24:27], v[48:51]\n" \
"buffer_load_dwordx4 v4, s[36:39], s61 offen offset:1024 sc1 lds\n" \
"v_mfma_f32_16x16x32_bf16 v[52:55], a[68:71], v[28:31], v[52:55]\n" \
"ds_read_b128 v[16:19], v10 offset:36864\n" \
"v_mfma_f32_16x16x32_bf16 v[56:59], a[196:199], v[24:27], v[56:59]\n" \
"ds_read_b128 v[20:23], v10 offset:53248\n" \
"v_mfma_f32_16x16x32_bf16 v[60:63], a[196:199], v[28:31], v[60:63]\n" \
"s_waitcnt lgkmcnt(4)\n" \
"v_mfma_f32_16x16x32_bf16 v[48:51], a[72:75], v[32:35], v[48:51]\n" \
"v_mfma_f32_16x16x32_bf16 v[52:55], a[72:75], v[36:39], v[52:55]\n" \
"ds_read_b128 v[24:27], v10 offset:37888\n" \
"v_mfma_f32_16x16x32_bf16 v[56:59], a[200:203], v[32:35], v[56:59]\n" \
"ds_read_b128 v[28:31], v10 offset:54272\n" \
"v_mfma_f32_16x16x32_bf16 v[60:63], a[200:203], v[36:39], v[60:63]\n" \
"s_waitcnt lgkmcnt(4)\n" \
"v_mfma_f32_16x16x32_bf16 v[48:51], a[76:79], v[40:43], v[48:51]\n" \
"buffer_load_dwordx4 v4, s[36:39], s61 offen offset:2048 sc1 lds\n" \
"v_mfma_f32_16x16x32_bf16 v[52:55], a[76:79], v[44:47], v[52:55]\n" \
"ds_read_b128 v[32:35], v10 offset:38912\n" \
"v_mfma_f32_16x16x32_bf16 v[56:59], a[204:207], v[40:43], v[56:59]\n" \
"ds_read_b128 v[36:39], v10 offset:55296\n" \
"v_mfma_f32_16x16x32_bf16 v[60:63], a[204:207], v[44:47], v[60:63]\n" \
"s_waitcnt lgkmcnt(4)\n" \
"v_mfma_f32_16x16x32_bf16 v[48:51], a[80:83], v[16:19], v[48:51]\n" \
"buffer_load_dwordx4 v4, s[36:39], s61 offen offset:3072 sc1 lds\n" \
"v_mfma_f32_16x16x32_bf16 v[52:55], a[80:83], v[20:23], v[52:55]\n" \
"ds_read_b128 v[40:43], v10 offset:39936\n" \
"v_mfma_f32_16x16x32_bf16 v[56:59], a[208:211], v[16:19], v[56:59]\n" \
"ds_read_b128 v[44:47], v10 offset:56320\n" \
"v_mfma_f32_16x16x32_bf16 v[60:63], a[208:211], v[20:23], v[60:63]\n" \
"s_waitcnt lgkmcnt(4)\n" \
"v_mfma_f32_16x16x32_bf16 v[48:51], a[84:87], v[24:27], v[48:51]\n" \
"v_mfma_f32_16x16x32_bf16 v[52:55], a[84:87], v[28:31], v[52:55]\n" \
"ds_read_b128 v[16:19], v10 offset:40960\n" \
"v_mfma_f32_16x16x32_bf16 v[56:59], a[212:215], v[24:27], v[56:59]\n" \
"ds_read_b128 v[20:23], v10 offset:57344\n" \
"v_mfma_f32_16x16x32_bf16 v[60:63], a[212:215], v[28:31], v[60:63]\n" \
"s_waitcnt lgkmcnt(4)\n" \
"v_mfma_f32_16x16x32_bf16 v[48:51], a[88:91], v[32:35], v[48:51]\n" \
"s_add_u32 m0, s55, 102400\n" \
"s_add_u32 s61, s61, 4096\n" \
"buffer_load_dwordx4 v4, s[36:39], s61 offen offset:0 sc1 lds\n" \
"v_mfma_f32_16x16x32_bf16 v[52:55], a[88:91], v[36:39], v[52:55]\n" \
"ds_read_b128 v[24:27], v10 offset:41984\n" \
"v_mfma_f32_16x16x32_bf16 v[56:59], a[216:219], v[32:35], v[56:59]\n" \
"ds_read_b128 v[28:31], v10 offset:58368\n" \
"v_mfma_f32_16x16x32_bf16 v[60:63], a[216:219], v[36:39], v[60:63]\n" \
"s_waitcnt lgkmcnt(4)\n" \
"v_mfma_f32_16x16x32_bf16 v[48:51], a[92:95], v[40:43], v[48:51]\n" \
"buffer_load_dwordx4 v4, s[36:39], s61 offen offset:1024 sc1 lds\n" \
"v_mfma_f32_16x16x32_bf16 v[52:55], a[92:95], v[44:47], v[52:55]\n" \
"ds_read_b128 v[32:35], v10 offset:43008\n" \
"v_mfma_f32_16x16x32_bf16 v[56:59], a[220:223], v[40:43], v[56:59]\n" \
"ds_read_b128 v[36:39], v10 offset:59392\n" \
"v_mfma_f32_16x16x32_bf16 v[60:63], a[220:223], v[44:47], v[60:63]\n" \
"s_waitcnt lgkmcnt(4)\n" \
"v_mfma_f32_16x16x32_bf16 v[48:51], a[96:99], v[16:19], v[48:51]\n" \
"v_mfma_f32_16x16x32_bf16 v[52:55], a[96:99], v[20:23], v[52:55]\n" \
"ds_read_b128 v[40:43], v10 offset:44032\n" \
"v_mfma_f32_16x16x32_bf16 v[56:59], a[224:227], v[16:19], v[56:59]\n" \
"ds_read_b128 v[44:47], v10 offset:60416\n" \
"v_mfma_f32_16x16x32_bf16 v[60:63], a[224:227], v[20:23], v[60:63]\n" \
"s_waitcnt lgkmcnt(4)\n" \
"v_mfma_f32_16x16x32_bf16 v[48:51], a[100:103], v[24:27], v[48:51]\n" \
"buffer_load_dwordx4 v4, s[36:39], s61 offen offset:2048 sc1 lds\n" \
"v_mfma_f32_16x16x32_bf16 v[52:55], a[100:103], v[28:31], v[52:55]\n" \
"ds_read_b128 v[16:19], v10 offset:45056\n" \
"v_mfma_f32_16x16x32_bf16 v[56:59], a[228:231], v[24:27], v[56:59]\n" \
"ds_read_b128 v[20:23], v10 offset:61440\n" \
"v_mfma_f32_16x16x32_bf16 v[60:63], a[228:231], v[28:31], v[60:63]\n" \
"s_waitcnt lgkmcnt(4)\n" \
"v_mfma_f32_16x16x32_bf16 v[48:51], a[104:107], v[32:35], v[48:51]\n" \
"buffer_load_dwordx4 v4, s[36:39], s61 offen offset:3072 sc1 lds\n" \
"v_mfma_f32_16x16x32_bf16 v[52:55], a[104:107], v[36:39], v[52:55]\n" \
"ds_read_b128 v[24:27], v10 offset:46080\n" \
"v_mfma_f32_16x16x32_bf16 v[56:59], a[232:235], v[32:35], v[56:59]\n" \
"ds_read_b128 v[28:31], v10 offset:62464\n" \
"v_mfma_f32_16x16x32_bf16 v[60:63], a[232:235], v[36:39], v[60:63]\n" \
"s_waitcnt lgkmcnt(4)\n" \
"v_mfma_f32_16x16x32_bf16 v[48:51], a[108:111], v[40:43], v[48:51]\n" \
"v_mfma_f32_16x16x32_bf16 v[52:55], a[108:111], v[44:47], v[52:55]\n" \
"ds_read_b128 v[32:35], v10 offset:47104\n" \
"v_mfma_f32_16x16x32_bf16 v[56:59], a[236:239], v[40:43], v[56:59]\n" \
"ds_read_b128 v[36:39], v10 offset:63488\n" \
"v_mfma_f32_16x16x32_bf16 v[60:63], a[236:239], v[44:47], v[60:63]\n" \
"s_waitcnt lgkmcnt(4)\n" \
"v_mfma_f32_16x16x32_bf16 v[48:51], a[112:115], v[16:19], v[48:51]\n" \
"v_mfma_f32_16x16x32_bf16 v[52:55], a[112:115], v[20:23], v[52:55]\n" \
"ds_read_b128 v[40:43], v10 offset:48128\n" \
"v_mfma_f32_16x16x32_bf16 v[56:59], a[240:243], v[16:19], v[56:59]\n" \
"ds_read_b128 v[44:47], v10 offset:64512\n" \
"v_mfma_f32_16x16x32_bf16 v[60:63], a[240:243], v[20:23], v[60:63]\n" \
"s_waitcnt lgkmcnt(4)\n" \
"v_mfma_f32_16x16x32_bf16 v[48:51], a[116:119], v[24:27], v[48:51]\n" \
"s_waitcnt vmcnt(8)\n" \
"s_barrier\n" \
"v_mfma_f32_16x16x32_bf16 v[52:55], a[116:119], v[28:31], v[52:55]\n" \
"ds_read_b128 v[16:19], v12 offset:0\n" \
"v_mfma_f32_16x16x32_bf16 v[56:59], a[244:247], v[24:27], v[56:59]\n" \
"ds_read_b128 v[20:23], v12 offset:16384\n" \
"v_mfma_f32_16x16x32_bf16 v[60:63], a[244:247], v[28:31], v[60:63]\n" \
"s_waitcnt lgkmcnt(4)\n" \
"buffer_store_dword v13, v6, s[36:39], 0 offen sc1\n" \
"v_mfma_f32_16x16x32_bf16 v[48:51], a[120:123], v[32:35], v[48:51]\n" \
"v_cmp_gt_u32_e64 vcc, 16, v15\n" \
"v_mfma_f32_16x16x32_bf16 v[52:55], a[120:123], v[36:39], v[52:55]\n" \
"ds_read_b128 v[24:27], v12 offset:1024\n" \
"v_mfma_f32_16x16x32_bf16 v[56:59], a[248:251], v[32:35], v[56:59]\n" \
"ds_read_b128 v[28:31], v12 offset:17408\n" \
"v_mfma_f32_16x16x32_bf16 v[60:63], a[248:251], v[36:39], v[60:63]\n" \
"s_waitcnt lgkmcnt(4)\n" \
"v_mfma_f32_16x16x32_bf16 v[48:51], a[124:127], v[40:43], v[48:51]\n" \
"s_cbranch_vccnz .Lp1qk_m0_%=\n" \
".Lp1qk_m0k_%=:\n" \
"v_mfma_f32_16x16x32_bf16 v[52:55], a[124:127], v[44:47], v[52:55]\n" \
"ds_read_b128 v[32:35], v12 offset:2048\n" \
"v_mfma_f32_16x16x32_bf16 v[56:59], a[252:255], v[40:43], v[56:59]\n" \
"ds_read_b128 v[36:39], v12 offset:18432\n" \
"v_mfma_f32_16x16x32_bf16 v[60:63], a[252:255], v[44:47], v[60:63]\n" \
"s_waitcnt lgkmcnt(4)\n" \
"s_add_u32 s57, s57, s49\n" \
"s_add_u32 s58, s58, s49\n" \
"s_add_u32 s59, s59, 1\n" \
"v_mov_b32_e64 v5, v7\n" \
"v_mov_b32_e64 v6, v11\n" \
"s_cmp_ge_u32 s59, s47\n" \
"s_nop 0\n" \
"s_cbranch_scc1 .Lp1qk_out0_%=\n" \
"v_mfma_f32_16x16x32_bf16 v[64:67], a[0:3], v[16:19], 0\n" \
"s_min_u32 s61, s56, s60\n" \
"s_add_u32 m0, s55, 0\n" \
"s_nop 0\n" \
"buffer_load_dwordx4 v4, s[36:39], s61 offen offset:0 sc1 lds\n" \
"v_mov_b32_e64 v13, s59\n" \
"v_mfma_f32_16x16x32_bf16 v[68:71], a[0:3], v[20:23], 0\n" \
"ds_read_b128 v[40:43], v12 offset:3072\n" \
"s_nop 0\n" \
"v_mfma_f32_16x16x32_bf16 v[72:75], a[128:131], v[16:19], 0\n" \
"ds_read_b128 v[44:47], v12 offset:19456\n" \
"s_nop 0\n" \
"v_mfma_f32_16x16x32_bf16 v[76:79], a[128:131], v[20:23], 0\n" \
"s_waitcnt lgkmcnt(4)\n" \
"v_mul_f32 v48, s51, v48\n" \
"v_mul_f32_e64 v49, s51, v49\n" \
"v_mfma_f32_16x16x32_bf16 v[64:67], a[4:7], v[24:27], v[64:67]\n" \
"buffer_load_dwordx4 v4, s[36:39], s61 offen offset:1024 sc1 lds\n" \
"v_mfma_f32_16x16x32_bf16 v[68:71], a[4:7], v[28:31], v[68:71]\n" \
"ds_read_b128 v[16:19], v12 offset:4096\n" \
"v_mul_f32_e64 v50, s51, v50\n" \
"v_mfma_f32_16x16x32_bf16 v[72:75], a[132:135], v[24:27], v[72:75]\n" \
"ds_read_b128 v[20:23], v12 offset:20480\n" \
"v_mul_f32_e64 v51, s51, v51\n" \
"v_mfma_f32_16x16x32_bf16 v[76:79], a[132:135], v[28:31], v[76:79]\n" \
"s_waitcnt lgkmcnt(4)\n" \
"v_mul_f32 v56, s51, v56\n" \
"v_mul_f32_e64 v57, s51, v57\n" \
"v_mfma_f32_16x16x32_bf16 v[64:67], a[8:11], v[32:35], v[64:67]\n" \
"s_add_u32 s63, s59, 2\n" \
"s_min_u32 s63, s63, 63\n" \
"v_mfma_f32_16x16x32_bf16 v[68:71], a[8:11], v[36:39], v[68:71]\n" \
"ds_read_b128 v[24:27], v12 offset:5120\n" \
"s_lshr_b32 s63, s63, 3\n" \
"s_lshl_b32 s63, s63, 7\n" \
"v_mul_f32_e64 v58, s51, v58\n" \
"v_mfma_f32_16x16x32_bf16 v[72:75], a[136:139], v[32:35], v[72:75]\n" \
"ds_read_b128 v[28:31], v12 offset:21504\n" \
"v_mul_f32_e64 v59, s51, v59\n" \
"v_mfma_f32_16x16x32_bf16 v[76:79], a[136:139], v[36:39], v[76:79]\n" \
"s_waitcnt lgkmcnt(4)\n" \
"v_cvt_pk_bf16_f32 v80, v48, v49\n" \
"v_cvt_pk_bf16_f32 v81, v50, v51\n" \
"v_mfma_f32_16x16x32_bf16 v[64:67], a[12:15], v[40:43], v[64:67]\n" \
"buffer_load_dwordx4 v4, s[36:39], s61 offen offset:2048 sc1 lds\n" \
"v_mfma_f32_16x16x32_bf16 v[68:71], a[12:15], v[44:47], v[68:71]\n" \
"ds_read_b128 v[32:35], v12 offset:6144\n" \
"v_cvt_pk_bf16_f32 v82, v56, v57\n" \
"v_mfma_f32_16x16x32_bf16 v[72:75], a[140:143], v[40:43], v[72:75]\n" \
"ds_read_b128 v[36:39], v12 offset:22528\n" \
"v_cvt_pk_bf16_f32 v83, v58, v59\n" \
"v_mfma_f32_16x16x32_bf16 v[76:79], a[140:143], v[44:47], v[76:79]\n" \
"s_waitcnt lgkmcnt(4)\n" \
"buffer_store_dwordx4 v[80:83], v5, s[36:39], s57 offen sc1\n" \
"v_mul_f32_e64 v52, s51, v52\n" \
"v_mfma_f32_16x16x32_bf16 v[64:67], a[16:19], v[16:19], v[64:67]\n" \
"buffer_load_dwordx4 v4, s[36:39], s61 offen offset:3072 sc1 lds\n" \
"v_mfma_f32_16x16x32_bf16 v[68:71], a[16:19], v[20:23], v[68:71]\n" \
"ds_read_b128 v[40:43], v12 offset:7168\n" \
"v_mul_f32_e64 v53, s51, v53\n" \
"v_mfma_f32_16x16x32_bf16 v[72:75], a[144:147], v[16:19], v[72:75]\n" \
"ds_read_b128 v[44:47], v12 offset:23552\n" \
"v_mul_f32_e64 v54, s51, v54\n" \
"v_mfma_f32_16x16x32_bf16 v[76:79], a[144:147], v[20:23], v[76:79]\n" \
"s_waitcnt lgkmcnt(4)\n" \
"v_mul_f32 v55, s51, v55\n" \
"v_mul_f32_e64 v60, s51, v60\n" \
"v_mfma_f32_16x16x32_bf16 v[64:67], a[20:23], v[24:27], v[64:67]\n" \
"buffer_load_dword v15, v14, s[36:39], s63 offen sc1\n" \
"v_mfma_f32_16x16x32_bf16 v[68:71], a[20:23], v[28:31], v[68:71]\n" \
"ds_read_b128 v[16:19], v12 offset:8192\n" \
"v_mul_f32_e64 v61, s51, v61\n" \
"v_mfma_f32_16x16x32_bf16 v[72:75], a[148:151], v[24:27], v[72:75]\n" \
"ds_read_b128 v[20:23], v12 offset:24576\n" \
"v_mul_f32_e64 v62, s51, v62\n" \
"v_mfma_f32_16x16x32_bf16 v[76:79], a[148:151], v[28:31], v[76:79]\n" \
"s_waitcnt lgkmcnt(4)\n" \
"v_mul_f32 v63, s51, v63\n" \
"v_cvt_pk_bf16_f32 v84, v52, v53\n" \
"v_mfma_f32_16x16x32_bf16 v[64:67], a[24:27], v[32:35], v[64:67]\n" \
"s_add_u32 m0, s55, 4096\n" \
"s_add_u32 s61, s61, 4096\n" \
"buffer_load_dwordx4 v4, s[36:39], s61 offen offset:0 sc1 lds\n" \
"v_mfma_f32_16x16x32_bf16 v[68:71], a[24:27], v[36:39], v[68:71]\n" \
"ds_read_b128 v[24:27], v12 offset:9216\n" \
"v_cvt_pk_bf16_f32 v85, v54, v55\n" \
"v_mfma_f32_16x16x32_bf16 v[72:75], a[152:155], v[32:35], v[72:75]\n" \
"ds_read_b128 v[28:31], v12 offset:25600\n" \
"v_cvt_pk_bf16_f32 v86, v60, v61\n" \
"v_mfma_f32_16x16x32_bf16 v[76:79], a[152:155], v[36:39], v[76:79]\n" \
"s_waitcnt lgkmcnt(4)\n" \
"v_cvt_pk_bf16_f32 v87, v62, v63\n" \
"buffer_store_dwordx4 v[84:87], v5, s[36:39], s58 offen sc1\n" \
"v_mfma_f32_16x16x32_bf16 v[64:67], a[28:31], v[40:43], v[64:67]\n" \
"buffer_load_dwordx4 v4, s[36:39], s61 offen offset:1024 sc1 lds\n" \
"v_mfma_f32_16x16x32_bf16 v[68:71], a[28:31], v[44:47], v[68:71]\n" \
"ds_read_b128 v[32:35], v12 offset:10240\n" \
"v_mfma_f32_16x16x32_bf16 v[72:75], a[156:159], v[40:43], v[72:75]\n" \
"ds_read_b128 v[36:39], v12 offset:26624\n" \
"v_mfma_f32_16x16x32_bf16 v[76:79], a[156:159], v[44:47], v[76:79]\n" \
"s_waitcnt lgkmcnt(4)\n" \
"v_mfma_f32_16x16x32_bf16 v[64:67], a[32:35], v[16:19], v[64:67]\n" \
"v_mfma_f32_16x16x32_bf16 v[68:71], a[32:35], v[20:23], v[68:71]\n" \
"ds_read_b128 v[40:43], v12 offset:11264\n" \
"v_mfma_f32_16x16x32_bf16 v[72:75], a[160:163], v[16:19], v[72:75]\n" \
"ds_read_b128 v[44:47], v12 offset:27648\n" \
"v_mfma_f32_16x16x32_bf16 v[76:79], a[160:163], v[20:23], v[76:79]\n" \
"s_waitcnt lgkmcnt(4)\n" \
"v_mfma_f32_16x16x32_bf16 v[64:67], a[36:39], v[24:27], v[64:67]\n" \
"buffer_load_dwordx4 v4, s[36:39], s61 offen offset:2048 sc1 lds\n" \
"v_mfma_f32_16x16x32_bf16 v[68:71], a[36:39], v[28:31], v[68:71]\n" \
"ds_read_b128 v[16:19], v12 offset:12288\n" \
"v_mfma_f32_16x16x32_bf16 v[72:75], a[164:167], v[24:27], v[72:75]\n" \
"ds_read_b128 v[20:23], v12 offset:28672\n" \
"v_mfma_f32_16x16x32_bf16 v[76:79], a[164:167], v[28:31], v[76:79]\n" \
"s_waitcnt lgkmcnt(4)\n" \
"v_mfma_f32_16x16x32_bf16 v[64:67], a[40:43], v[32:35], v[64:67]\n" \
"buffer_load_dwordx4 v4, s[36:39], s61 offen offset:3072 sc1 lds\n" \
"v_mfma_f32_16x16x32_bf16 v[68:71], a[40:43], v[36:39], v[68:71]\n" \
"ds_read_b128 v[24:27], v12 offset:13312\n" \
"v_mfma_f32_16x16x32_bf16 v[72:75], a[168:171], v[32:35], v[72:75]\n" \
"ds_read_b128 v[28:31], v12 offset:29696\n" \
"v_mfma_f32_16x16x32_bf16 v[76:79], a[168:171], v[36:39], v[76:79]\n" \
"s_waitcnt lgkmcnt(4)\n" \
"v_mfma_f32_16x16x32_bf16 v[64:67], a[44:47], v[40:43], v[64:67]\n" \
"v_mfma_f32_16x16x32_bf16 v[68:71], a[44:47], v[44:47], v[68:71]\n" \
"ds_read_b128 v[32:35], v12 offset:14336\n" \
"v_mfma_f32_16x16x32_bf16 v[72:75], a[172:175], v[40:43], v[72:75]\n" \
"ds_read_b128 v[36:39], v12 offset:30720\n" \
"v_mfma_f32_16x16x32_bf16 v[76:79], a[172:175], v[44:47], v[76:79]\n" \
"s_waitcnt lgkmcnt(4)\n" \
"v_mfma_f32_16x16x32_bf16 v[64:67], a[48:51], v[16:19], v[64:67]\n" \
"v_mfma_f32_16x16x32_bf16 v[68:71], a[48:51], v[20:23], v[68:71]\n" \
"ds_read_b128 v[40:43], v12 offset:15360\n" \
"v_mfma_f32_16x16x32_bf16 v[72:75], a[176:179], v[16:19], v[72:75]\n" \
"ds_read_b128 v[44:47], v12 offset:31744\n" \
"v_mfma_f32_16x16x32_bf16 v[76:79], a[176:179], v[20:23], v[76:79]\n" \
"s_waitcnt lgkmcnt(4)\n" \
"v_mfma_f32_16x16x32_bf16 v[64:67], a[52:55], v[24:27], v[64:67]\n" \
"s_waitcnt vmcnt(12)\n" \
"s_barrier\n" \
"v_mfma_f32_16x16x32_bf16 v[68:71], a[52:55], v[28:31], v[68:71]\n" \
"ds_read_b128 v[16:19], v12 offset:32768\n" \
"v_mfma_f32_16x16x32_bf16 v[72:75], a[180:183], v[24:27], v[72:75]\n" \
"ds_read_b128 v[20:23], v12 offset:49152\n" \
"v_mfma_f32_16x16x32_bf16 v[76:79], a[180:183], v[28:31], v[76:79]\n" \
"s_waitcnt lgkmcnt(4)\n" \
"v_mfma_f32_16x16x32_bf16 v[64:67], a[56:59], v[32:35], v[64:67]\n" \
"v_mfma_f32_16x16x32_bf16 v[68:71], a[56:59], v[36:39], v[68:71]\n" \
"ds_read_b128 v[24:27], v12 offset:33792\n" \
"v_mfma_f32_16x16x32_bf16 v[72:75], a[184:187], v[32:35], v[72:75]\n" \
"ds_read_b128 v[28:31], v12 offset:50176\n" \
"v_mfma_f32_16x16x32_bf16 v[76:79], a[184:187], v[36:39], v[76:79]\n" \
"s_waitcnt lgkmcnt(4)\n" \
"v_mfma_f32_16x16x32_bf16 v[64:67], a[60:63], v[40:43], v[64:67]\n" \
"v_mfma_f32_16x16x32_bf16 v[68:71], a[60:63], v[44:47], v[68:71]\n" \
"ds_read_b128 v[32:35], v12 offset:34816\n" \
"v_mfma_f32_16x16x32_bf16 v[72:75], a[188:191], v[40:43], v[72:75]\n" \
"ds_read_b128 v[36:39], v12 offset:51200\n" \
"v_mfma_f32_16x16x32_bf16 v[76:79], a[188:191], v[44:47], v[76:79]\n" \
"s_waitcnt lgkmcnt(4)\n" \
"v_mfma_f32_16x16x32_bf16 v[64:67], a[64:67], v[16:19], v[64:67]\n" \
"s_min_u32 s61, s56, s60\n" \
"s_add_u32 s61, s61, 16384\n" \
"s_add_u32 s56, s56, 0x10000\n" \
"s_add_u32 m0, s55, 32768\n" \
"s_nop 0\n" \
"buffer_load_dwordx4 v4, s[36:39], s61 offen offset:0 sc1 lds\n" \
"v_mfma_f32_16x16x32_bf16 v[68:71], a[64:67], v[20:23], v[68:71]\n" \
"ds_read_b128 v[40:43], v12 offset:35840\n" \
"v_mfma_f32_16x16x32_bf16 v[72:75], a[192:195], v[16:19], v[72:75]\n" \
"ds_read_b128 v[44:47], v12 offset:52224\n" \
"v_mfma_f32_16x16x32_bf16 v[76:79], a[192:195], v[20:23], v[76:79]\n" \
"s_waitcnt lgkmcnt(4)\n" \
"v_mfma_f32_16x16x32_bf16 v[64:67], a[68:71], v[24:27], v[64:67]\n" \
"buffer_load_dwordx4 v4, s[36:39], s61 offen offset:1024 sc1 lds\n" \
"v_mfma_f32_16x16x32_bf16 v[68:71], a[68:71], v[28:31], v[68:71]\n" \
"ds_read_b128 v[16:19], v12 offset:36864\n" \
"v_mfma_f32_16x16x32_bf16 v[72:75], a[196:199], v[24:27], v[72:75]\n" \
"ds_read_b128 v[20:23], v12 offset:53248\n" \
"v_mfma_f32_16x16x32_bf16 v[76:79], a[196:199], v[28:31], v[76:79]\n" \
"s_waitcnt lgkmcnt(4)\n" \
"v_mfma_f32_16x16x32_bf16 v[64:67], a[72:75], v[32:35], v[64:67]\n" \
"v_mfma_f32_16x16x32_bf16 v[68:71], a[72:75], v[36:39], v[68:71]\n" \
"ds_read_b128 v[24:27], v12 offset:37888\n" \
"v_mfma_f32_16x16x32_bf16 v[72:75], a[200:203], v[32:35], v[72:75]\n" \
"ds_read_b128 v[28:31], v12 offset:54272\n" \
"v_mfma_f32_16x16x32_bf16 v[76:79], a[200:203], v[36:39], v[76:79]\n" \
"s_waitcnt lgkmcnt(4)\n" \
"v_mfma_f32_16x16x32_bf16 v[64:67], a[76:79], v[40:43], v[64:67]\n" \
"buffer_load_dwordx4 v4, s[36:39], s61 offen offset:2048 sc1 lds\n" \
"v_mfma_f32_16x16x32_bf16 v[68:71], a[76:79], v[44:47], v[68:71]\n" \
"ds_read_b128 v[32:35], v12 offset:38912\n" \
"v_mfma_f32_16x16x32_bf16 v[72:75], a[204:207], v[40:43], v[72:75]\n" \
"ds_read_b128 v[36:39], v12 offset:55296\n" \
"v_mfma_f32_16x16x32_bf16 v[76:79], a[204:207], v[44:47], v[76:79]\n" \
"s_waitcnt lgkmcnt(4)\n" \
"v_mfma_f32_16x16x32_bf16 v[64:67], a[80:83], v[16:19], v[64:67]\n" \
"buffer_load_dwordx4 v4, s[36:39], s61 offen offset:3072 sc1 lds\n" \
"v_mfma_f32_16x16x32_bf16 v[68:71], a[80:83], v[20:23], v[68:71]\n" \
"ds_read_b128 v[40:43], v12 offset:39936\n" \
"v_mfma_f32_16x16x32_bf16 v[72:75], a[208:211], v[16:19], v[72:75]\n" \
"ds_read_b128 v[44:47], v12 offset:56320\n" \
"v_mfma_f32_16x16x32_bf16 v[76:79], a[208:211], v[20:23], v[76:79]\n" \
"s_waitcnt lgkmcnt(4)\n" \
"v_mfma_f32_16x16x32_bf16 v[64:67], a[84:87], v[24:27], v[64:67]\n" \
"v_mfma_f32_16x16x32_bf16 v[68:71], a[84:87], v[28:31], v[68:71]\n" \
"ds_read_b128 v[16:19], v12 offset:40960\n" \
"v_mfma_f32_16x16x32_bf16 v[72:75], a[212:215], v[24:27], v[72:75]\n" \
"ds_read_b128 v[20:23], v12 offset:57344\n" \
"v_mfma_f32_16x16x32_bf16 v[76:79], a[212:215], v[28:31], v[76:79]\n" \
"s_waitcnt lgkmcnt(4)\n" \
"v_mfma_f32_16x16x32_bf16 v[64:67], a[88:91], v[32:35], v[64:67]\n" \
"s_add_u32 m0, s55, 36864\n" \
"s_add_u32 s61, s61, 4096\n" \
"buffer_load_dwordx4 v4, s[36:39], s61 offen offset:0 sc1 lds\n" \
"v_mfma_f32_16x16x32_bf16 v[68:71], a[88:91], v[36:39], v[68:71]\n" \
"ds_read_b128 v[24:27], v12 offset:41984\n" \
"v_mfma_f32_16x16x32_bf16 v[72:75], a[216:219], v[32:35], v[72:75]\n" \
"ds_read_b128 v[28:31], v12 offset:58368\n" \
"v_mfma_f32_16x16x32_bf16 v[76:79], a[216:219], v[36:39], v[76:79]\n" \
"s_waitcnt lgkmcnt(4)\n" \
"v_mfma_f32_16x16x32_bf16 v[64:67], a[92:95], v[40:43], v[64:67]\n" \
"buffer_load_dwordx4 v4, s[36:39], s61 offen offset:1024 sc1 lds\n" \
"v_mfma_f32_16x16x32_bf16 v[68:71], a[92:95], v[44:47], v[68:71]\n" \
"ds_read_b128 v[32:35], v12 offset:43008\n" \
"v_mfma_f32_16x16x32_bf16 v[72:75], a[220:223], v[40:43], v[72:75]\n" \
"ds_read_b128 v[36:39], v12 offset:59392\n" \
"v_mfma_f32_16x16x32_bf16 v[76:79], a[220:223], v[44:47], v[76:79]\n" \
"s_waitcnt lgkmcnt(4)\n" \
"v_mfma_f32_16x16x32_bf16 v[64:67], a[96:99], v[16:19], v[64:67]\n" \
"v_mfma_f32_16x16x32_bf16 v[68:71], a[96:99], v[20:23], v[68:71]\n" \
"ds_read_b128 v[40:43], v12 offset:44032\n" \
"v_mfma_f32_16x16x32_bf16 v[72:75], a[224:227], v[16:19], v[72:75]\n" \
"ds_read_b128 v[44:47], v12 offset:60416\n" \
"v_mfma_f32_16x16x32_bf16 v[76:79], a[224:227], v[20:23], v[76:79]\n" \
"s_waitcnt lgkmcnt(4)\n" \
"v_mfma_f32_16x16x32_bf16 v[64:67], a[100:103], v[24:27], v[64:67]\n" \
"buffer_load_dwordx4 v4, s[36:39], s61 offen offset:2048 sc1 lds\n" \
"v_mfma_f32_16x16x32_bf16 v[68:71], a[100:103], v[28:31], v[68:71]\n" \
"ds_read_b128 v[16:19], v12 offset:45056\n" \
"v_mfma_f32_16x16x32_bf16 v[72:75], a[228:231], v[24:27], v[72:75]\n" \
"ds_read_b128 v[20:23], v12 offset:61440\n" \
"v_mfma_f32_16x16x32_bf16 v[76:79], a[228:231], v[28:31], v[76:79]\n" \
"s_waitcnt lgkmcnt(4)\n" \
"v_mfma_f32_16x16x32_bf16 v[64:67], a[104:107], v[32:35], v[64:67]\n" \
"buffer_load_dwordx4 v4, s[36:39], s61 offen offset:3072 sc1 lds\n" \
"v_mfma_f32_16x16x32_bf16 v[68:71], a[104:107], v[36:39], v[68:71]\n" \
"ds_read_b128 v[24:27], v12 offset:46080\n" \
"v_mfma_f32_16x16x32_bf16 v[72:75], a[232:235], v[32:35], v[72:75]\n" \
"ds_read_b128 v[28:31], v12 offset:62464\n" \
"v_mfma_f32_16x16x32_bf16 v[76:79], a[232:235], v[36:39], v[76:79]\n" \
"s_waitcnt lgkmcnt(4)\n" \
"v_mfma_f32_16x16x32_bf16 v[64:67], a[108:111], v[40:43], v[64:67]\n" \
"v_mfma_f32_16x16x32_bf16 v[68:71], a[108:111], v[44:47], v[68:71]\n" \
"ds_read_b128 v[32:35], v12 offset:47104\n" \
"v_mfma_f32_16x16x32_bf16 v[72:75], a[236:239], v[40:43], v[72:75]\n" \
"ds_read_b128 v[36:39], v12 offset:63488\n" \
"v_mfma_f32_16x16x32_bf16 v[76:79], a[236:239], v[44:47], v[76:79]\n" \
"s_waitcnt lgkmcnt(4)\n" \
"v_mfma_f32_16x16x32_bf16 v[64:67], a[112:115], v[16:19], v[64:67]\n" \
"v_mfma_f32_16x16x32_bf16 v[68:71], a[112:115], v[20:23], v[68:71]\n" \
"ds_read_b128 v[40:43], v12 offset:48128\n" \
"v_mfma_f32_16x16x32_bf16 v[72:75], a[240:243], v[16:19], v[72:75]\n" \
"ds_read_b128 v[44:47], v12 offset:64512\n" \
"v_mfma_f32_16x16x32_bf16 v[76:79], a[240:243], v[20:23], v[76:79]\n" \
"s_waitcnt lgkmcnt(4)\n" \
"v_mfma_f32_16x16x32_bf16 v[64:67], a[116:119], v[24:27], v[64:67]\n" \
"s_waitcnt vmcnt(8)\n" \
"s_barrier\n" \
"v_mfma_f32_16x16x32_bf16 v[68:71], a[116:119], v[28:31], v[68:71]\n" \
"ds_read_b128 v[16:19], v10 offset:0\n" \
"v_mfma_f32_16x16x32_bf16 v[72:75], a[244:247], v[24:27], v[72:75]\n" \
"ds_read_b128 v[20:23], v10 offset:16384\n" \
"v_mfma_f32_16x16x32_bf16 v[76:79], a[244:247], v[28:31], v[76:79]\n" \
"s_waitcnt lgkmcnt(4)\n" \
"buffer_store_dword v13, v6, s[36:39], 0 offen sc1\n" \
"v_mfma_f32_16x16x32_bf16 v[64:67], a[120:123], v[32:35], v[64:67]\n" \
"v_cmp_gt_u32_e64 vcc, 16, v15\n" \
"v_mfma_f32_16x16x32_bf16 v[68:71], a[120:123], v[36:39], v[68:71]\n" \
"ds_read_b128 v[24:27], v10 offset:1024\n" \
"v_mfma_f32_16x16x32_bf16 v[72:75], a[248:251], v[32:35], v[72:75]\n" \
"ds_read_b128 v[28:31], v10 offset:17408\n" \
"v_mfma_f32_16x16x32_bf16 v[76:79], a[248:251], v[36:39], v[76:79]\n" \
"s_waitcnt lgkmcnt(4)\n" \
"v_mfma_f32_16x16x32_bf16 v[64:67], a[124:127], v[40:43], v[64:67]\n" \
"s_cbranch_vccnz .Lp1qk_m1_%=\n" \
".Lp1qk_m1k_%=:\n" \
"v_mfma_f32_16x16x32_bf16 v[68:71], a[124:127], v[44:47], v[68:71]\n" \
"ds_read_b128 v[32:35], v10 offset:2048\n" \
"v_mfma_f32_16x16x32_bf16 v[72:75], a[252:255], v[40:43], v[72:75]\n" \
"ds_read_b128 v[36:39], v10 offset:18432\n" \
"v_mfma_f32_16x16x32_bf16 v[76:79], a[252:255], v[44:47], v[76:79]\n" \
"s_waitcnt lgkmcnt(4)\n" \
"s_add_u32 s57, s57, s49\n" \
"s_add_u32 s58, s58, s49\n" \
"s_add_u32 s59, s59, 1\n" \
"v_mov_b32_e64 v5, v7\n" \
"v_mov_b32_e64 v6, v11\n" \
"s_cmp_ge_u32 s59, s47\n" \
"s_nop 0\n" \
"s_cbranch_scc0 .Lp1qk_loop_%=\n" \
".Lp1qk_out1_%=:\n" \
"s_nop 7\n" \
"s_nop 7\n" \
"v_mul_f32 v64, s51, v64\n" \
"v_mul_f32 v65, s51, v65\n" \
"v_mul_f32 v66, s51, v66\n" \
"v_mul_f32 v67, s51, v67\n" \
"v_mul_f32 v72, s51, v72\n" \
"v_mul_f32 v73, s51, v73\n" \
"v_mul_f32 v74, s51, v74\n" \
"v_mul_f32 v75, s51, v75\n" \
"v_cvt_pk_bf16_f32 v80, v64, v65\n" \
"v_cvt_pk_bf16_f32 v81, v66, v67\n" \
"v_cvt_pk_bf16_f32 v82, v72, v73\n" \
"v_cvt_pk_bf16_f32 v83, v74, v75\n" \
"buffer_store_dwordx4 v[80:83], v5, s[36:39], s57 offen sc1\n" \
"v_mul_f32 v68, s51, v68\n" \
"v_mul_f32 v69, s51, v69\n" \
"v_mul_f32 v70, s51, v70\n" \
"v_mul_f32 v71, s51, v71\n" \
"v_mul_f32 v76, s51, v76\n" \
"v_mul_f32 v77, s51, v77\n" \
"v_mul_f32 v78, s51, v78\n" \
"v_mul_f32 v79, s51, v79\n" \
"v_cvt_pk_bf16_f32 v84, v68, v69\n" \
"v_cvt_pk_bf16_f32 v85, v70, v71\n" \
"v_cvt_pk_bf16_f32 v86, v76, v77\n" \
"v_cvt_pk_bf16_f32 v87, v78, v79\n" \
"buffer_store_dwordx4 v[84:87], v5, s[36:39], s58 offen sc1\n" \
"s_branch .Lp1qk_end_%=\n" \
".Lp1qk_out0_%=:\n" \
"s_nop 7\n" \
"s_nop 7\n" \
"v_mul_f32 v48, s51, v48\n" \
"v_mul_f32 v49, s51, v49\n" \
"v_mul_f32 v50, s51, v50\n" \
"v_mul_f32 v51, s51, v51\n" \
"v_mul_f32 v56, s51, v56\n" \
"v_mul_f32 v57, s51, v57\n" \
"v_mul_f32 v58, s51, v58\n" \
"v_mul_f32 v59, s51, v59\n" \
"v_cvt_pk_bf16_f32 v80, v48, v49\n" \
"v_cvt_pk_bf16_f32 v81, v50, v51\n" \
"v_cvt_pk_bf16_f32 v82, v56, v57\n" \
"v_cvt_pk_bf16_f32 v83, v58, v59\n" \
"buffer_store_dwordx4 v[80:83], v5, s[36:39], s57 offen sc1\n" \
"v_mul_f32 v52, s51, v52\n" \
"v_mul_f32 v53, s51, v53\n" \
"v_mul_f32 v54, s51, v54\n" \
"v_mul_f32 v55, s51, v55\n" \
"v_mul_f32 v60, s51, v60\n" \
"v_mul_f32 v61, s51, v61\n" \
"v_mul_f32 v62, s51, v62\n" \
"v_mul_f32 v63, s51, v63\n" \
"v_cvt_pk_bf16_f32 v84, v52, v53\n" \
"v_cvt_pk_bf16_f32 v85, v54, v55\n" \
"v_cvt_pk_bf16_f32 v86, v60, v61\n" \
"v_cvt_pk_bf16_f32 v87, v62, v63\n" \
"buffer_store_dwordx4 v[84:87], v5, s[36:39], s58 offen sc1\n" \
".Lp1qk_end_%=:\n" \
"s_waitcnt vmcnt(0) lgkmcnt(0)\n" \
"s_barrier\n" \
"v_mov_b32 v13, s59\n" \
"buffer_store_dword v13, v6, s[36:39], 0 offen sc1\n" \
"s_waitcnt vmcnt(0) lgkmcnt(0)\n" \
"s_branch .Lp1qk_x_%=\n" \
".Lp1qk_l0_%=:\n" \
"s_nop 7\n" \
"s_nop 7\n" \
"s_waitcnt vmcnt(0) lgkmcnt(0)\n" \
"s_mov_b32 s62, 0\n" \
".Lp1qk_l0a_%=:\n" \
"buffer_load_dword v15, v14, s[36:39], s63 offen sc1\n" \
"s_waitcnt vmcnt(0)\n" \
"v_cmp_gt_u32 vcc, 16, v15\n" \
"s_nop 4\n" \
"s_cbranch_vccz .Lp1qk_l0k_%=\n" \
"s_sleep 4\n" \
"s_add_u32 s62, s62, 1\n" \
"s_cmp_lt_u32 s62, 0x10000\n" \
"s_cbranch_scc1 .Lp1qk_l0a_%=\n" \
"v_mov_b32 v15, 256\n" \
"buffer_store_dword v15, v15, s[36:39], 0 offen sc1\n" \
"s_waitcnt vmcnt(0) lgkmcnt(0)\n" \
"s_branch .Lp1qk_l0k_%=\n" \
".Lp1qk_l1_%=:\n" \
"s_nop 7\n" \
"s_nop 7\n" \
"s_waitcnt vmcnt(0) lgkmcnt(0)\n" \
"s_mov_b32 s62, 0\n" \
".Lp1qk_l1a_%=:\n" \
"buffer_load_dword v15, v14, s[36:39], s63 offen sc1\n" \
"s_waitcnt vmcnt(0)\n" \
"v_cmp_gt_u32 vcc, 16, v15\n" \
"s_nop 4\n" \
"s_cbranch_vccz .Lp1qk_l1k_%=\n" \
"s_sleep 4\n" \
"s_add_u32 s62, s62, 1\n" \
"s_cmp_lt_u32 s62, 0x10000\n" \
"s_cbranch_scc1 .Lp1qk_l1a_%=\n" \
"v_mov_b32 v15, 256\n" \
"buffer_store_dword v15, v15, s[36:39], 0 offen sc1\n" \
"s_waitcnt vmcnt(0) lgkmcnt(0)\n" \
"s_branch .Lp1qk_l1k_%=\n" \
".Lp1qk_m0_%=:\n" \
"s_nop 7\n" \
"s_nop 7\n" \
"s_waitcnt vmcnt(0) lgkmcnt(0)\n" \
"s_mov_b32 s62, 0\n" \
".Lp1qk_m0a_%=:\n" \
"buffer_load_dword v15, v14, s[36:39], s63 offen sc1\n" \
"s_waitcnt vmcnt(0)\n" \
"v_cmp_gt_u32 vcc, 16, v15\n" \
"s_nop 4\n" \
"s_cbranch_vccz .Lp1qk_m0k_%=\n" \
"s_sleep 4\n" \
"s_add_u32 s62, s62, 1\n" \
"s_cmp_lt_u32 s62, 0x10000\n" \
"s_cbranch_scc1 .Lp1qk_m0a_%=\n" \
"v_mov_b32 v15, 256\n" \
"buffer_store_dword v15, v15, s[36:39], 0 offen sc1\n" \
"s_waitcnt vmcnt(0) lgkmcnt(0)\n" \
"s_branch .Lp1qk_m0k_%=\n" \
".Lp1qk_m1_%=:\n" \
"s_nop 7\n" \
"s_nop 7\n" \
"s_waitcnt vmcnt(0) lgkmcnt(0)\n" \
"s_mov_b32 s62, 0\n" \
".Lp1qk_m1a_%=:\n" \
"buffer_load_dword v15, v14, s[36:39], s63 offen sc1\n" \
"s_waitcnt vmcnt(0)\n" \
"v_cmp_gt_u32 vcc, 16, v15\n" \
"s_nop 4\n" \
"s_cbranch_vccz .Lp1qk_m1k_%=\n" \
"s_sleep 4\n" \
"s_add_u32 s62, s62, 1\n" \
"s_cmp_lt_u32 s62, 0x10000\n" \
"s_cbranch_scc1 .Lp1qk_m1a_%=\n" \
"v_mov_b32 v15, 256\n" \
"buffer_store_dword v15, v15, s[36:39], 0 offen sc1\n" \
"s_waitcnt vmcnt(0) lgkmcnt(0)\n" \
"s_branch .Lp1qk_m1k_%=\n" \
".Lp1qk_x_%=:\n"
#define ASM_P1_V \
"s_nop 7\n" \
"s_nop 7\n" \
"s_lshr_b32 s52, s48, 1\n" \
"s_lshl_b32 s52, s52, 13\n" \
"s_and_b32 s53, s48, 1\n" \
"s_lshl_b32 s53, s53, 10\n" \
"s_add_u32 s52, s52, s53\n" \
"s_mov_b32 s54, 0\n" \
"s_add_u32 m0, s52, 0\n" \
"s_nop 0\n" \
"buffer_load_dwordx4 v1, s[40:43], s54 offen nt lds\n" \
"s_add_u32 s54, s54, s44\n" \
"s_add_u32 m0, s52, 2048\n" \
"s_nop 0\n" \
"buffer_load_dwordx4 v1, s[40:43], s54 offen nt lds\n" \
"s_add_u32 s54, s54, s44\n" \
"s_add_u32 m0, s52, 4096\n" \
"s_nop 0\n" \
"buffer_load_dwordx4 v1, s[40:43], s54 offen nt lds\n" \
"s_add_u32 s54, s54, s44\n" \
"s_add_u32 m0, s52, 6144\n" \
"s_nop 0\n" \
"buffer_load_dwordx4 v1, s[40:43], s54 offen nt lds\n" \
"s_add_u32 s54, s54, s44\n" \
"s_add_u32 m0, s52, 16384\n" \
"s_nop 0\n" \
"buffer_load_dwordx4 v1, s[40:43], s54 offen nt lds\n" \
"s_add_u32 s54, s54, s44\n" \
"s_add_u32 m0, s52, 18432\n" \
"s_nop 0\n" \
"buffer_load_dwordx4 v1, s[40:43], s54 offen nt lds\n" \
"s_add_u32 s54, s54, s44\n" \
"s_add_u32 m0, s52, 20480\n" \
"s_nop 0\n" \
"buffer_load_dwordx4 v1, s[40:43], s54 offen nt lds\n" \
"s_add_u32 s54, s54, s44\n" \
"s_add_u32 m0, s52, 22528\n" \
"s_nop 0\n" \
"buffer_load_dwordx4 v1, s[40:43], s54 offen nt lds\n" \
"s_add_u32 s54, s54, s44\n" \
"s_add_u32 m0, s52, 32768\n" \
"s_nop 0\n" \
"buffer_load_dwordx4 v1, s[40:43], s54 offen nt lds\n" \
"s_add_u32 s54, s54, s44\n" \
"s_add_u32 m0, s52, 34816\n" \
"s_nop 0\n" \
"buffer_load_dwordx4 v1, s[40:43], s54 offen nt lds\n" \
"s_add_u32 s54, s54, s44\n" \
"s_add_u32 m0, s52, 36864\n" \
"s_nop 0\n" \
"buffer_load_dwordx4 v1, s[40:43], s54 offen nt lds\n" \
"s_add_u32 s54, s54, s44\n" \
"s_add_u32 m0, s52, 38912\n" \
"s_nop 0\n" \
"buffer_load_dwordx4 v1, s[40:43], s54 offen nt lds\n" \
"s_add_u32 s54, s54, s44\n" \
"s_add_u32 m0, s52, 49152\n" \
"s_nop 0\n" \
"buffer_load_dwordx4 v1, s[40:43], s54 offen nt lds\n" \
"s_add_u32 s54, s54, s44\n" \
"s_add_u32 m0, s52, 51200\n" \
"s_nop 0\n" \
"buffer_load_dwordx4 v1, s[40:43], s54 offen nt lds\n" \
"s_add_u32 s54, s54, s44\n" \
"s_add_u32 m0, s52, 53248\n" \
"s_nop 0\n" \
"buffer_load_dwordx4 v1, s[40:43], s54 offen nt lds\n" \
"s_add_u32 s54, s54, s44\n" \
"s_add_u32 m0, s52, 55296\n" \
"s_nop 0\n" \
"buffer_load_dwordx4 v1, s[40:43], s54 offen nt lds\n" \
"s_add_u32 s54, s54, s44\n" \
"s_add_u32 m0, s52, 65536\n" \
"s_nop 0\n" \
"buffer_load_dwordx4 v1, s[40:43], s54 offen nt lds\n" \
"s_add_u32 s54, s54, s44\n" \
"s_add_u32 m0, s52, 67584\n" \
"s_nop 0\n" \
"buffer_load_dwordx4 v1, s[40:43], s54 offen nt lds\n" \
"s_add_u32 s54, s54, s44\n" \
"s_add_u32 m0, s52, 69632\n" \
"s_nop 0\n" \
"buffer_load_dwordx4 v1, s[40:43], s54 offen nt lds\n" \
"s_add_u32 s54, s54, s44\n" \
"s_add_u32 m0, s52, 71680\n" \
"s_nop 0\n" \
"buffer_load_dwordx4 v1, s[40:43], s54 offen nt lds\n" \
"s_add_u32 s54, s54, s44\n" \
"s_add_u32 m0, s52, 81920\n" \
"s_nop 0\n" \
"buffer_load_dwordx4 v1, s[40:43], s54 offen nt lds\n" \
"s_add_u32 s54, s54, s44\n" \
"s_add_u32 m0, s52, 83968\n" \
"s_nop 0\n" \
"buffer_load_dwordx4 v1, s[40:43], s54 offen nt lds\n" \
"s_add_u32 s54, s54, s44\n" \
"s_add_u32 m0, s52, 86016\n" \
"s_nop 0\n" \
"buffer_load_dwordx4 v1, s[40:43], s54 offen nt lds\n" \
"s_add_u32 s54, s54, s44\n" \
"s_add_u32 m0, s52, 88064\n" \
"s_nop 0\n" \
"buffer_load_dwordx4 v1, s[40:43], s54 offen nt lds\n" \
"s_add_u32 s54, s54, s44\n" \
"s_add_u32 m0, s52, 98304\n" \
"s_nop 0\n" \
"buffer_load_dwordx4 v1, s[40:43], s54 offen nt lds\n" \
"s_add_u32 s54, s54, s44\n" \
"s_add_u32 m0, s52, 100352\n" \
"s_nop 0\n" \
"buffer_load_dwordx4 v1, s[40:43], s54 offen nt lds\n" \
"s_add_u32 s54, s54, s44\n" \
"s_add_u32 m0, s52, 102400\n" \
"s_nop 0\n" \
"buffer_load_dwordx4 v1, s[40:43], s54 offen nt lds\n" \
"s_add_u32 s54, s54, s44\n" \
"s_add_u32 m0, s52, 104448\n" \
"s_nop 0\n" \
"buffer_load_dwordx4 v1, s[40:43], s54 offen nt lds\n" \
"s_add_u32 s54, s54, s44\n" \
"s_add_u32 m0, s52, 114688\n" \
"s_nop 0\n" \
"buffer_load_dwordx4 v1, s[40:43], s54 offen nt lds\n" \
"s_add_u32 s54, s54, s44\n" \
"s_add_u32 m0, s52, 116736\n" \
"s_nop 0\n" \
"buffer_load_dwordx4 v1, s[40:43], s54 offen nt lds\n" \
"s_add_u32 s54, s54, s44\n" \
"s_add_u32 m0, s52, 118784\n" \
"s_nop 0\n" \
"buffer_load_dwordx4 v1, s[40:43], s54 offen nt lds\n" \
"s_add_u32 s54, s54, s44\n" \
"s_add_u32 m0, s52, 120832\n" \
"s_nop 0\n" \
"buffer_load_dwordx4 v1, s[40:43], s54 offen nt lds\n" \
"s_add_u32 s54, s54, s44\n" \
"v_add_u32 v8, 0x10000, v2\n" \
"v_add_u32 v9, 0x10000, v3\n" \
"s_waitcnt vmcnt(28)\n" \
"s_barrier\n" \
"ds_read_b64_tr_b16 a[0:1], v2 offset:0\n" \
"ds_read_b64_tr_b16 a[2:3], v2 offset:512\n" \
"ds_read_b64_tr_b16 a[128:129], v3 offset:0\n" \
"ds_read_b64_tr_b16 a[130:131], v3 offset:512\n" \
"ds_read_b64_tr_b16 a[4:5], v2 offset:4096\n" \
"ds_read_b64_tr_b16 a[6:7], v2 offset:4608\n" \
"ds_read_b64_tr_b16 a[132:133], v3 offset:4096\n" \
"ds_read_b64_tr_b16 a[134:135], v3 offset:4608\n" \
"s_waitcnt lgkmcnt(0)\n" \
"s_barrier\n" \
"s_add_u32 m0, s52, 0\n" \
"s_nop 0\n" \
"buffer_load_dwordx4 v1, s[40:43], s54 offen nt lds\n" \
"s_add_u32 s54, s54, s44\n" \
"s_add_u32 m0, s52, 2048\n" \
"s_nop 0\n" \
"buffer_load_dwordx4 v1, s[40:43], s54 offen nt lds\n" \
"s_add_u32 s54, s54, s44\n" \
"s_add_u32 m0, s52, 4096\n" \
"s_nop 0\n" \
"buffer_load_dwordx4 v1, s[40:43], s54 offen nt lds\n" \
"s_add_u32 s54, s54, s44\n" \
"s_add_u32 m0, s52, 6144\n" \
"s_nop 0\n" \
"buffer_load_dwordx4 v1, s[40:43], s54 offen nt lds\n" \
"s_add_u32 s54, s54, s44\n" \
"s_waitcnt vmcnt(28)\n" \
"s_barrier\n" \
"ds_read_b64_tr_b16 a[8:9], v2 offset:16384\n" \
"ds_read_b64_tr_b16 a[10:11], v2 offset:16896\n" \
"ds_read_b64_tr_b16 a[136:137], v3 offset:16384\n" \
"ds_read_b64_tr_b16 a[138:139], v3 offset:16896\n" \
"ds_read_b64_tr_b16 a[12:13], v2 offset:20480\n" \
"ds_read_b64_tr_b16 a[14:15], v2 offset:20992\n" \
"ds_read_b64_tr_b16 a[140:141], v3 offset:20480\n" \
"ds_read_b64_tr_b16 a[142:143], v3 offset:20992\n" \
"s_waitcnt lgkmcnt(0)\n" \
"s_barrier\n" \
"s_add_u32 m0, s52, 16384\n" \
"s_nop 0\n" \
"buffer_load_dwordx4 v1, s[40:43], s54 offen nt lds\n" \
"s_add_u32 s54, s54, s44\n" \
"s_add_u32 m0, s52, 18432\n" \
"s_nop 0\n" \
"buffer_load_dwordx4 v1, s[40:43], s54 offen nt lds\n" \
"s_add_u32 s54, s54, s44\n" \
"s_add_u32 m0, s52, 20480\n" \
"s_nop 0\n" \
"buffer_load_dwordx4 v1, s[40:43], s54 offen nt lds\n" \
"s_add_u32 s54, s54, s44\n" \
"s_add_u32 m0, s52, 22528\n" \
"s_nop 0\n" \
"buffer_load_dwordx4 v1, s[40:43], s54 offen nt lds\n" \
"s_add_u32 s54, s54, s44\n" \
"s_waitcnt vmcnt(28)\n" \
"s_barrier\n" \
"ds_read_b64_tr_b16 a[16:17], v2 offset:32768\n" \
"ds_read_b64_tr_b16 a[18:19], v2 offset:33280\n" \
"ds_read_b64_tr_b16 a[144:145], v3 offset:32768\n" \
"ds_read_b64_tr_b16 a[146:147], v3 offset:33280\n" \
"ds_read_b64_tr_b16 a[20:21], v2 offset:36864\n" \
"ds_read_b64_tr_b16 a[22:23], v2 offset:37376\n" \
"ds_read_b64_tr_b16 a[148:149], v3 offset:36864\n" \
"ds_read_b64_tr_b16 a[150:151], v3 offset:37376\n" \
"s_waitcnt lgkmcnt(0)\n" \
"s_barrier\n" \
"s_add_u32 m0, s52, 32768\n" \
"s_nop 0\n" \
"buffer_load_dwordx4 v1, s[40:43], s54 offen nt lds\n" \
"s_add_u32 s54, s54, s44\n" \
"s_add_u32 m0, s52, 34816\n" \
"s_nop 0\n" \
"buffer_load_dwordx4 v1, s[40:43], s54 offen nt lds\n" \
"s_add_u32 s54, s54, s44\n" \
"s_add_u32 m0, s52, 36864\n" \
"s_nop 0\n" \
"buffer_load_dwordx4 v1, s[40:43], s54 offen nt lds\n" \
"s_add_u32 s54, s54, s44\n" \
"s_add_u32 m0, s52, 38912\n" \
"s_nop 0\n" \
"buffer_load_dwordx4 v1, s[40:43], s54 offen nt lds\n" \
"s_add_u32 s54, s54, s44\n" \
"s_waitcnt vmcnt(28)\n" \
"s_barrier\n" \
"ds_read_b64_tr_b16 a[24:25], v2 offset:49152\n" \
"ds_read_b64_tr_b16 a[26:27], v2 offset:49664\n" \
"ds_read_b64_tr_b16 a[152:153], v3 offset:49152\n" \
"ds_read_b64_tr_b16 a[154:155], v3 offset:49664\n" \
"ds_read_b64_tr_b16 a[28:29], v2 offset:53248\n" \
"ds_read_b64_tr_b16 a[30:31], v2 offset:53760\n" \
"ds_read_b64_tr_b16 a[156:157], v3 offset:53248\n" \
"ds_read_b64_tr_b16 a[158:159], v3 offset:53760\n" \
"s_waitcnt lgkmcnt(0)\n" \
"s_barrier\n" \
"s_add_u32 m0, s52, 49152\n" \
"s_nop 0\n" \
"buffer_load_dwordx4 v1, s[40:43], s54 offen nt lds\n" \
"s_add_u32 s54, s54, s44\n" \
"s_add_u32 m0, s52, 51200\n" \
"s_nop 0\n" \
"buffer_load_dwordx4 v1, s[40:43], s54 offen nt lds\n" \
"s_add_u32 s54, s54, s44\n" \
"s_add_u32 m0, s52, 53248\n" \
"s_nop 0\n" \
"buffer_load_dwordx4 v1, s[40:43], s54 offen nt lds\n" \
"s_add_u32 s54, s54, s44\n" \
"s_add_u32 m0, s52, 55296\n" \
"s_nop 0\n" \
"buffer_load_dwordx4 v1, s[40:43], s54 offen nt lds\n" \
"s_add_u32 s54, s54, s44\n" \
"s_waitcnt vmcnt(28)\n" \
"s_barrier\n" \
"ds_read_b64_tr_b16 a[32:33], v8 offset:0\n" \
"ds_read_b64_tr_b16 a[34:35], v8 offset:512\n" \
"ds_read_b64_tr_b16 a[160:161], v9 offset:0\n" \
"ds_read_b64_tr_b16 a[162:163], v9 offset:512\n" \
"ds_read_b64_tr_b16 a[36:37], v8 offset:4096\n" \
"ds_read_b64_tr_b16 a[38:39], v8 offset:4608\n" \
"ds_read_b64_tr_b16 a[164:165], v9 offset:4096\n" \
"ds_read_b64_tr_b16 a[166:167], v9 offset:4608\n" \
"s_waitcnt lgkmcnt(0)\n" \
"s_barrier\n" \
"s_add_u32 m0, s52, 65536\n" \
"s_nop 0\n" \
"buffer_load_dwordx4 v1, s[40:43], s54 offen nt lds\n" \
"s_add_u32 s54, s54, s44\n" \
"s_add_u32 m0, s52, 67584\n" \
"s_nop 0\n" \
"buffer_load_dwordx4 v1, s[40:43], s54 offen nt lds\n" \
"s_add_u32 s54, s54, s44\n" \
"s_add_u32 m0, s52, 69632\n" \
"s_nop 0\n" \
"buffer_load_dwordx4 v1, s[40:43], s54 offen nt lds\n" \
"s_add_u32 s54, s54, s44\n" \
"s_add_u32 m0, s52, 71680\n" \
"s_nop 0\n" \
"buffer_load_dwordx4 v1, s[40:43], s54 offen nt lds\n" \
"s_add_u32 s54, s54, s44\n" \
"s_waitcnt vmcnt(28)\n" \
"s_barrier\n" \
"ds_read_b64_tr_b16 a[40:41], v8 offset:16384\n" \
"ds_read_b64_tr_b16 a[42:43], v8 offset:16896\n" \
"ds_read_b64_tr_b16 a[168:169], v9 offset:16384\n" \
"ds_read_b64_tr_b16 a[170:171], v9 offset:16896\n" \
"ds_read_b64_tr_b16 a[44:45], v8 offset:20480\n" \
"ds_read_b64_tr_b16 a[46:47], v8 offset:20992\n" \
"ds_read_b64_tr_b16 a[172:173], v9 offset:20480\n" \
"ds_read_b64_tr_b16 a[174:175], v9 offset:20992\n" \
"s_waitcnt lgkmcnt(0)\n" \
"s_barrier\n" \
"s_add_u32 m0, s52, 81920\n" \
"s_nop 0\n" \
"buffer_load_dwordx4 v1, s[40:43], s54 offen nt lds\n" \
"s_add_u32 s54, s54, s44\n" \
"s_add_u32 m0, s52, 83968\n" \
"s_nop 0\n" \
"buffer_load_dwordx4 v1, s[40:43], s54 offen nt lds\n" \
"s_add_u32 s54, s54, s44\n" \
"s_add_u32 m0, s52, 86016\n" \
"s_nop 0\n" \
"buffer_load_dwordx4 v1, s[40:43], s54 offen nt lds\n" \
"s_add_u32 s54, s54, s44\n" \
"s_add_u32 m0, s52, 88064\n" \
"s_nop 0\n" \
"buffer_load_dwordx4 v1, s[40:43], s54 offen nt lds\n" \
"s_add_u32 s54, s54, s44\n" \
"s_waitcnt vmcnt(28)\n" \
"s_barrier\n" \
"ds_read_b64_tr_b16 a[48:49], v8 offset:32768\n" \
"ds_read_b64_tr_b16 a[50:51], v8 offset:33280\n" \
"ds_read_b64_tr_b16 a[176:177], v9 offset:32768\n" \
"ds_read_b64_tr_b16 a[178:179], v9 offset:33280\n" \
"ds_read_b64_tr_b16 a[52:53], v8 offset:36864\n" \
"ds_read_b64_tr_b16 a[54:55], v8 offset:37376\n" \
"ds_read_b64_tr_b16 a[180:181], v9 offset:36864\n" \
"ds_read_b64_tr_b16 a[182:183], v9 offset:37376\n" \
"s_waitcnt lgkmcnt(0)\n" \
"s_barrier\n" \
"s_add_u32 m0, s52, 98304\n" \
"s_nop 0\n" \
"buffer_load_dwordx4 v1, s[40:43], s54 offen nt lds\n" \
"s_add_u32 s54, s54, s44\n" \
"s_add_u32 m0, s52, 100352\n" \
"s_nop 0\n" \
"buffer_load_dwordx4 v1, s[40:43], s54 offen nt lds\n" \
"s_add_u32 s54, s54, s44\n" \
"s_add_u32 m0, s52, 102400\n" \
"s_nop 0\n" \
"buffer_load_dwordx4 v1, s[40:43], s54 offen nt lds\n" \
"s_add_u32 s54, s54, s44\n" \
"s_add_u32 m0, s52, 104448\n" \
"s_nop 0\n" \
"buffer_load_dwordx4 v1, s[40:43], s54 offen nt lds\n" \
"s_add_u32 s54, s54, s44\n" \
"s_waitcnt vmcnt(28)\n" \
"s_barrier\n" \
"ds_read_b64_tr_b16 a[56:57], v8 offset:49152\n" \
"ds_read_b64_tr_b16 a[58:59], v8 offset:49664\n" \
"ds_read_b64_tr_b16 a[184:185], v9 offset:49152\n" \
"ds_read_b64_tr_b16 a[186:187], v9 offset:49664\n" \
"ds_read_b64_tr_b16 a[60:61], v8 offset:53248\n" \
"ds_read_b64_tr_b16 a[62:63], v8 offset:53760\n" \
"ds_read_b64_tr_b16 a[188:189], v9 offset:53248\n" \
"ds_read_b64_tr_b16 a[190:191], v9 offset:53760\n" \
"s_waitcnt lgkmcnt(0)\n" \
"s_barrier\n" \
"s_add_u32 m0, s52, 114688\n" \
"s_nop 0\n" \
"buffer_load_dwordx4 v1, s[40:43], s54 offen nt lds\n" \
"s_add_u32 s54, s54, s44\n" \
"s_add_u32 m0, s52, 116736\n" \
"s_nop 0\n" \
"buffer_load_dwordx4 v1, s[40:43], s54 offen nt lds\n" \
"s_add_u32 s54, s54, s44\n" \
"s_add_u32 m0, s52, 118784\n" \
"s_nop 0\n" \
"buffer_load_dwordx4 v1, s[40:43], s54 offen nt lds\n" \
"s_add_u32 s54, s54, s44\n" \
"s_add_u32 m0, s52, 120832\n" \
"s_nop 0\n" \
"buffer_load_dwordx4 v1, s[40:43], s54 offen nt lds\n" \
"s_add_u32 s54, s54, s44\n" \
"s_waitcnt vmcnt(28)\n" \
"s_barrier\n" \
"ds_read_b64_tr_b16 a[64:65], v2 offset:0\n" \
"ds_read_b64_tr_b16 a[66:67], v2 offset:512\n" \
"ds_read_b64_tr_b16 a[192:193], v3 offset:0\n" \
"ds_read_b64_tr_b16 a[194:195], v3 offset:512\n" \
"ds_read_b64_tr_b16 a[68:69], v2 offset:4096\n" \
"ds_read_b64_tr_b16 a[70:71], v2 offset:4608\n" \
"ds_read_b64_tr_b16 a[196:197], v3 offset:4096\n" \
"ds_read_b64_tr_b16 a[198:199], v3 offset:4608\n" \
"s_waitcnt vmcnt(24)\n" \
"s_barrier\n" \
"ds_read_b64_tr_b16 a[72:73], v2 offset:16384\n" \
"ds_read_b64_tr_b16 a[74:75], v2 offset:16896\n" \
"ds_read_b64_tr_b16 a[200:201], v3 offset:16384\n" \
"ds_read_b64_tr_b16 a[202:203], v3 offset:16896\n" \
"ds_read_b64_tr_b16 a[76:77], v2 offset:20480\n" \
"ds_read_b64_tr_b16 a[78:79], v2 offset:20992\n" \
"ds_read_b64_tr_b16 a[204:205], v3 offset:20480\n" \
"ds_read_b64_tr_b16 a[206:207], v3 offset:20992\n" \
"s_waitcnt vmcnt(20)\n" \
"s_barrier\n" \
"ds_read_b64_tr_b16 a[80:81], v2 offset:32768\n" \
"ds_read_b64_tr_b16 a[82:83], v2 offset:33280\n" \
"ds_read_b64_tr_b16 a[208:209], v3 offset:32768\n" \
"ds_read_b64_tr_b16 a[210:211], v3 offset:33280\n" \
"ds_read_b64_tr_b16 a[84:85], v2 offset:36864\n" \
"ds_read_b64_tr_b16 a[86:87], v2 offset:37376\n" \
"ds_read_b64_tr_b16 a[212:213], v3 offset:36864\n" \
"ds_read_b64_tr_b16 a[214:215], v3 offset:37376\n" \
"s_waitcnt vmcnt(16)\n" \
"s_barrier\n" \
"ds_read_b64_tr_b16 a[88:89], v2 offset:49152\n" \
"ds_read_b64_tr_b16 a[90:91], v2 offset:49664\n" \
"ds_read_b64_tr_b16 a[216:217], v3 offset:49152\n" \
"ds_read_b64_tr_b16 a[218:219], v3 offset:49664\n" \
"ds_read_b64_tr_b16 a[92:93], v2 offset:53248\n" \
"ds_read_b64_tr_b16 a[94:95], v2 offset:53760\n" \
"ds_read_b64_tr_b16 a[220:221], v3 offset:53248\n" \
"ds_read_b64_tr_b16 a[222:223], v3 offset:53760\n" \
"s_waitcnt vmcnt(12)\n" \
"s_barrier\n" \
"ds_read_b64_tr_b16 a[96:97], v8 offset:0\n" \
"ds_read_b64_tr_b16 a[98:99], v8 offset:512\n" \
"ds_read_b64_tr_b16 a[224:225], v9 offset:0\n" \
"ds_read_b64_tr_b16 a[226:227], v9 offset:512\n" \
"ds_read_b64_tr_b16 a[100:101], v8 offset:4096\n" \
"ds_read_b64_tr_b16 a[102:103], v8 offset:4608\n" \
"ds_read_b64_tr_b16 a[228:229], v9 offset:4096\n" \
"ds_read_b64_tr_b16 a[230:231], v9 offset:4608\n" \
"s_waitcnt vmcnt(8)\n" \
"s_barrier\n" \
"ds_read_b64_tr_b16 a[104:105], v8 offset:16384\n" \
"ds_read_b64_tr_b16 a[106:107], v8 offset:16896\n" \
"ds_read_b64_tr_b16 a[232:233], v9 offset:16384\n" \
"ds_read_b64_tr_b16 a[234:235], v9 offset:16896\n" \
"ds_read_b64_tr_b16 a[108:109], v8 offset:20480\n" \
"ds_read_b64_tr_b16 a[110:111], v8 offset:20992\n" \
"ds_read_b64_tr_b16 a[236:237], v9 offset:20480\n" \
"ds_read_b64_tr_b16 a[238:239], v9 offset:20992\n" \
"s_waitcnt vmcnt(4)\n" \
"s_barrier\n" \
"ds_read_b64_tr_b16 a[112:113], v8 offset:32768\n" \
"ds_read_b64_tr_b16 a[114:115], v8 offset:33280\n" \
"ds_read_b64_tr_b16 a[240:241], v9 offset:32768\n" \
"ds_read_b64_tr_b16 a[242:243], v9 offset:33280\n" \
"ds_read_b64_tr_b16 a[116:117], v8 offset:36864\n" \
"ds_read_b64_tr_b16 a[118:119], v8 offset:37376\n" \
"ds_read_b64_tr_b16 a[244:245], v9 offset:36864\n" \
"ds_read_b64_tr_b16 a[246:247], v9 offset:37376\n" \
"s_waitcnt vmcnt(0)\n" \
"s_barrier\n" \
"ds_read_b64_tr_b16 a[120:121], v8 offset:49152\n" \
"ds_read_b64_tr_b16 a[122:123], v8 offset:49664\n" \
"ds_read_b64_tr_b16 a[248:249], v9 offset:49152\n" \
"ds_read_b64_tr_b16 a[250:251], v9 offset:49664\n" \
"ds_read_b64_tr_b16 a[124:125], v8 offset:53248\n" \
"ds_read_b64_tr_b16 a[126:127], v8 offset:53760\n" \
"ds_read_b64_tr_b16 a[252:253], v9 offset:53248\n" \
"ds_read_b64_tr_b16 a[254:255], v9 offset:53760\n" \
"s_waitcnt lgkmcnt(0)\n" \
"s_barrier\n" \
"v_mov_b32 v14, 4096\n" \
"s_mov_b32 s59, s46\n" \
"s_add_u32 s63, s59, 0\n" \
"s_min_u32 s63, s63, 63\n" \
"s_lshr_b32 s63, s63, 3\n" \
"s_lshl_b32 s63, s63, 7\n" \
"s_branch .Lp1v_l0_%=\n" \
".Lp1v_l0k_%=:\n" \
"s_add_u32 s63, s59, 1\n" \
"s_min_u32 s63, s63, 63\n" \
"s_lshr_b32 s63, s63, 3\n" \
"s_lshl_b32 s63, s63, 7\n" \
"s_branch .Lp1v_l1_%=\n" \
".Lp1v_l1k_%=:\n" \
"s_lshl_b32 s55, s48, 13\n" \
"s_lshl_b32 s56, s46, 16\n" \
"s_lshl_b32 s60, s47, 16\n" \
"s_sub_u32 s60, s60, 0x10000\n" \
"s_mul_i32 s57, s46, s49\n" \
"s_sub_u32 s57, s57, s49\n" \
"s_add_u32 s58, s57, s50\n" \
"v_mov_b32 v7, v5\n" \
"v_mov_b32 v5, 0x7ffffff0\n" \
"v_mov_b32 v10, v0\n" \
"v_mov_b32 v11, v6\n" \
"v_mov_b32 v6, 0x7ffffff0\n" \
"s_mov_b32 s59, s46\n" \
"s_min_u32 s61, s56, s60\n" \
"s_add_u32 m0, s55, 0\n" \
"s_nop 0\n" \
"buffer_load_dwordx4 v4, s[36:39], s61 offen offset:0 sc1 lds\n" \
"buffer_load_dwordx4 v4, s[36:39], s61 offen offset:1024 sc1 lds\n" \
"buffer_load_dwordx4 v4, s[36:39], s61 offen offset:2048 sc1 lds\n" \
"buffer_load_dwordx4 v4, s[36:39], s61 offen offset:3072 sc1 lds\n" \
"s_add_u32 m0, s55, 4096\n" \
"s_add_u32 s61, s61, 4096\n" \
"buffer_load_dwordx4 v4, s[36:39], s61 offen offset:0 sc1 lds\n" \
"buffer_load_dwordx4 v4, s[36:39], s61 offen offset:1024 sc1 lds\n" \
"buffer_load_dwordx4 v4, s[36:39], s61 offen offset:2048 sc1 lds\n" \
"buffer_load_dwordx4 v4, s[36:39], s61 offen offset:3072 sc1 lds\n" \
"s_min_u32 s61, s56, s60\n" \
"s_add_u32 s61, s61, 16384\n" \
"s_add_u32 s56, s56, 0x10000\n" \
"s_add_u32 m0, s55, 32768\n" \
"s_nop 0\n" \
"buffer_load_dwordx4 v4, s[36:39], s61 offen offset:0 sc1 lds\n" \
"buffer_load_dwordx4 v4, s[36:39], s61 offen offset:1024 sc1 lds\n" \
"buffer_load_dwordx4 v4, s[36:39], s61 offen offset:2048 sc1 lds\n" \
"buffer_load_dwordx4 v4, s[36:39], s61 offen offset:3072 sc1 lds\n" \
"s_add_u32 m0, s55, 36864\n" \
"s_add_u32 s61, s61, 4096\n" \
"buffer_load_dwordx4 v4, s[36:39], s61 offen offset:0 sc1 lds\n" \
"buffer_load_dwordx4 v4, s[36:39], s61 offen offset:1024 sc1 lds\n" \
"buffer_load_dwordx4 v4, s[36:39], s61 offen offset:2048 sc1 lds\n" \
"buffer_load_dwordx4 v4, s[36:39], s61 offen offset:3072 sc1 lds\n" \
"s_mov_b32 s70, 0\n" \
"s_mov_b32 s72, 0\n" \
"s_waitcnt vmcnt(0) lgkmcnt(0)\n" \
"s_barrier\n" \
"v_add_u32 v12, 0x10000, v10\n" \
"ds_read_b128 v[16:19], v10 offset:0\n" \
"ds_read_b128 v[20:23], v10 offset:16384\n" \
"ds_read_b128 v[24:27], v10 offset:1024\n" \
"ds_read_b128 v[28:31], v10 offset:17408\n" \
"ds_read_b128 v[32:35], v10 offset:2048\n" \
"ds_read_b128 v[36:39], v10 offset:18432\n" \
"s_waitcnt lgkmcnt(4)\n" \
"s_nop 7\n" \
"s_nop 7\n" \
".p2align 6\n" \
".Lp1v_loop_%=:\n" \
"v_mfma_f32_16x16x32_bf16 v[48:51], v[16:19], a[0:3], 0\n" \
"s_min_u32 s61, s56, s60\n" \
"s_add_u32 m0, s55, 65536\n" \
"s_nop 0\n" \
"buffer_load_dwordx4 v4, s[36:39], s61 offen offset:0 sc1 lds\n" \
"v_mov_b32_e64 v13, s59\n" \
"v_mfma_f32_16x16x32_bf16 v[52:55], v[20:23], a[0:3], 0\n" \
"ds_read_b128 v[40:43], v10 offset:3072\n" \
"s_nop 0\n" \
"v_mfma_f32_16x16x32_bf16 v[56:59], v[16:19], a[128:131], 0\n" \
"ds_read_b128 v[44:47], v10 offset:19456\n" \
"s_nop 0\n" \
"v_mfma_f32_16x16x32_bf16 v[60:63], v[20:23], a[128:131], 0\n" \
"s_waitcnt lgkmcnt(4)\n" \
"v_cvt_pk_bf16_f32 v80, v64, v65\n" \
"v_cvt_pk_bf16_f32 v81, v66, v67\n" \
"v_mfma_f32_16x16x32_bf16 v[48:51], v[24:27], a[4:7], v[48:51]\n" \
"buffer_load_dwordx4 v4, s[36:39], s61 offen offset:1024 sc1 lds\n" \
"v_mfma_f32_16x16x32_bf16 v[52:55], v[28:31], a[4:7], v[52:55]\n" \
"ds_read_b128 v[16:19], v10 offset:4096\n" \
"v_cvt_pk_bf16_f32 v82, v68, v69\n" \
"v_mfma_f32_16x16x32_bf16 v[56:59], v[24:27], a[132:135], v[56:59]\n" \
"ds_read_b128 v[20:23], v10 offset:20480\n" \
"v_cvt_pk_bf16_f32 v83, v70, v71\n" \
"v_mfma_f32_16x16x32_bf16 v[60:63], v[28:31], a[132:135], v[60:63]\n" \
"s_waitcnt lgkmcnt(4)\n" \
"buffer_store_dwordx4 v[80:83], v5, s[36:39], s57 offen sc1\n" \
"v_cvt_pk_bf16_f32 v84, v72, v73\n" \
"v_mfma_f32_16x16x32_bf16 v[48:51], v[32:35], a[8:11], v[48:51]\n" \
"s_add_u32 s63, s59, 2\n" \
"s_min_u32 s63, s63, 63\n" \
"v_mfma_f32_16x16x32_bf16 v[52:55], v[36:39], a[8:11], v[52:55]\n" \
"ds_read_b128 v[24:27], v10 offset:5120\n" \
"s_lshr_b32 s63, s63, 3\n" \
"s_lshl_b32 s63, s63, 7\n" \
"v_cvt_pk_bf16_f32 v85, v74, v75\n" \
"v_mfma_f32_16x16x32_bf16 v[56:59], v[32:35], a[136:139], v[56:59]\n" \
"ds_read_b128 v[28:31], v10 offset:21504\n" \
"v_cvt_pk_bf16_f32 v86, v76, v77\n" \
"v_mfma_f32_16x16x32_bf16 v[60:63], v[36:39], a[136:139], v[60:63]\n" \
"s_waitcnt lgkmcnt(4)\n" \
"v_cvt_pk_bf16_f32 v87, v78, v79\n" \
"buffer_store_dwordx4 v[84:87], v5, s[36:39], s58 offen sc1\n" \
"v_mfma_f32_16x16x32_bf16 v[48:51], v[40:43], a[12:15], v[48:51]\n" \
"buffer_load_dwordx4 v4, s[36:39], s61 offen offset:2048 sc1 lds\n" \
"v_mfma_f32_16x16x32_bf16 v[52:55], v[44:47], a[12:15], v[52:55]\n" \
"ds_read_b128 v[32:35], v10 offset:6144\n" \
"v_mfma_f32_16x16x32_bf16 v[56:59], v[40:43], a[140:143], v[56:59]\n" \
"ds_read_b128 v[36:39], v10 offset:22528\n" \
"v_mfma_f32_16x16x32_bf16 v[60:63], v[44:47], a[140:143], v[60:63]\n" \
"s_waitcnt lgkmcnt(4)\n" \
"v_mfma_f32_16x16x32_bf16 v[48:51], v[16:19], a[16:19], v[48:51]\n" \
"buffer_load_dwordx4 v4, s[36:39], s61 offen offset:3072 sc1 lds\n" \
"v_mfma_f32_16x16x32_bf16 v[52:55], v[20:23], a[16:19], v[52:55]\n" \
"ds_read_b128 v[40:43], v10 offset:7168\n" \
"v_mfma_f32_16x16x32_bf16 v[56:59], v[16:19], a[144:147], v[56:59]\n" \
"ds_read_b128 v[44:47], v10 offset:23552\n" \
"v_mfma_f32_16x16x32_bf16 v[60:63], v[20:23], a[144:147], v[60:63]\n" \
"s_waitcnt lgkmcnt(4)\n" \
"v_mfma_f32_16x16x32_bf16 v[48:51], v[24:27], a[20:23], v[48:51]\n" \
"buffer_load_dword v15, v14, s[36:39], s63 offen sc1\n" \
"v_mfma_f32_16x16x32_bf16 v[52:55], v[28:31], a[20:23], v[52:55]\n" \
"ds_read_b128 v[16:19], v10 offset:8192\n" \
"v_mfma_f32_16x16x32_bf16 v[56:59], v[24:27], a[148:151], v[56:59]\n" \
"ds_read_b128 v[20:23], v10 offset:24576\n" \
"v_mfma_f32_16x16x32_bf16 v[60:63], v[28:31], a[148:151], v[60:63]\n" \
"s_waitcnt lgkmcnt(4)\n" \
"v_mfma_f32_16x16x32_bf16 v[48:51], v[32:35], a[24:27], v[48:51]\n" \
"s_add_u32 m0, s55, 69632\n" \
"s_add_u32 s61, s61, 4096\n" \
"buffer_load_dwordx4 v4, s[36:39], s61 offen offset:0 sc1 lds\n" \
"v_mfma_f32_16x16x32_bf16 v[52:55], v[36:39], a[24:27], v[52:55]\n" \
"ds_read_b128 v[24:27], v10 offset:9216\n" \
"v_mfma_f32_16x16x32_bf16 v[56:59], v[32:35], a[152:155], v[56:59]\n" \
"ds_read_b128 v[28:31], v10 offset:25600\n" \
"v_mfma_f32_16x16x32_bf16 v[60:63], v[36:39], a[152:155], v[60:63]\n" \
"s_waitcnt lgkmcnt(4)\n" \
"v_mfma_f32_16x16x32_bf16 v[48:51], v[40:43], a[28:31], v[48:51]\n" \
"buffer_load_dwordx4 v4, s[36:39], s61 offen offset:1024 sc1 lds\n" \
"v_mfma_f32_16x16x32_bf16 v[52:55], v[44:47], a[28:31], v[52:55]\n" \
"ds_read_b128 v[32:35], v10 offset:10240\n" \
"v_mfma_f32_16x16x32_bf16 v[56:59], v[40:43], a[156:159], v[56:59]\n" \
"ds_read_b128 v[36:39], v10 offset:26624\n" \
"v_mfma_f32_16x16x32_bf16 v[60:63], v[44:47], a[156:159], v[60:63]\n" \
"s_waitcnt lgkmcnt(4)\n" \
"v_mfma_f32_16x16x32_bf16 v[48:51], v[16:19], a[32:35], v[48:51]\n" \
"v_mfma_f32_16x16x32_bf16 v[52:55], v[20:23], a[32:35], v[52:55]\n" \
"ds_read_b128 v[40:43], v10 offset:11264\n" \
"v_mfma_f32_16x16x32_bf16 v[56:59], v[16:19], a[160:163], v[56:59]\n" \
"ds_read_b128 v[44:47], v10 offset:27648\n" \
"v_mfma_f32_16x16x32_bf16 v[60:63], v[20:23], a[160:163], v[60:63]\n" \
"s_waitcnt lgkmcnt(4)\n" \
"v_mfma_f32_16x16x32_bf16 v[48:51], v[24:27], a[36:39], v[48:51]\n" \
"buffer_load_dwordx4 v4, s[36:39], s61 offen offset:2048 sc1 lds\n" \
"v_mfma_f32_16x16x32_bf16 v[52:55], v[28:31], a[36:39], v[52:55]\n" \
"ds_read_b128 v[16:19], v10 offset:12288\n" \
"v_mfma_f32_16x16x32_bf16 v[56:59], v[24:27], a[164:167], v[56:59]\n" \
"ds_read_b128 v[20:23], v10 offset:28672\n" \
"v_mfma_f32_16x16x32_bf16 v[60:63], v[28:31], a[164:167], v[60:63]\n" \
"s_waitcnt lgkmcnt(4)\n" \
"v_mfma_f32_16x16x32_bf16 v[48:51], v[32:35], a[40:43], v[48:51]\n" \
"buffer_load_dwordx4 v4, s[36:39], s61 offen offset:3072 sc1 lds\n" \
"v_mfma_f32_16x16x32_bf16 v[52:55], v[36:39], a[40:43], v[52:55]\n" \
"ds_read_b128 v[24:27], v10 offset:13312\n" \
"v_mfma_f32_16x16x32_bf16 v[56:59], v[32:35], a[168:171], v[56:59]\n" \
"ds_read_b128 v[28:31], v10 offset:29696\n" \
"v_mfma_f32_16x16x32_bf16 v[60:63], v[36:39], a[168:171], v[60:63]\n" \
"s_waitcnt lgkmcnt(4)\n" \
"v_mfma_f32_16x16x32_bf16 v[48:51], v[40:43], a[44:47], v[48:51]\n" \
"v_mfma_f32_16x16x32_bf16 v[52:55], v[44:47], a[44:47], v[52:55]\n" \
"ds_read_b128 v[32:35], v10 offset:14336\n" \
"v_mfma_f32_16x16x32_bf16 v[56:59], v[40:43], a[172:175], v[56:59]\n" \
"ds_read_b128 v[36:39], v10 offset:30720\n" \
"v_mfma_f32_16x16x32_bf16 v[60:63], v[44:47], a[172:175], v[60:63]\n" \
"s_waitcnt lgkmcnt(4)\n" \
"v_mfma_f32_16x16x32_bf16 v[48:51], v[16:19], a[48:51], v[48:51]\n" \
"v_mfma_f32_16x16x32_bf16 v[52:55], v[20:23], a[48:51], v[52:55]\n" \
"ds_read_b128 v[40:43], v10 offset:15360\n" \
"v_mfma_f32_16x16x32_bf16 v[56:59], v[16:19], a[176:179], v[56:59]\n" \
"ds_read_b128 v[44:47], v10 offset:31744\n" \
"v_mfma_f32_16x16x32_bf16 v[60:63], v[20:23], a[176:179], v[60:63]\n" \
"s_waitcnt lgkmcnt(4)\n" \
"v_mfma_f32_16x16x32_bf16 v[48:51], v[24:27], a[52:55], v[48:51]\n" \
"s_waitcnt vmcnt(12)\n" \
"s_barrier\n" \
"v_mfma_f32_16x16x32_bf16 v[52:55], v[28:31], a[52:55], v[52:55]\n" \
"ds_read_b128 v[16:19], v10 offset:32768\n" \
"v_mfma_f32_16x16x32_bf16 v[56:59], v[24:27], a[180:183], v[56:59]\n" \
"ds_read_b128 v[20:23], v10 offset:49152\n" \
"v_mfma_f32_16x16x32_bf16 v[60:63], v[28:31], a[180:183], v[60:63]\n" \
"s_waitcnt lgkmcnt(4)\n" \
"v_mfma_f32_16x16x32_bf16 v[48:51], v[32:35], a[56:59], v[48:51]\n" \
"v_mfma_f32_16x16x32_bf16 v[52:55], v[36:39], a[56:59], v[52:55]\n" \
"ds_read_b128 v[24:27], v10 offset:33792\n" \
"v_mfma_f32_16x16x32_bf16 v[56:59], v[32:35], a[184:187], v[56:59]\n" \
"ds_read_b128 v[28:31], v10 offset:50176\n" \
"v_mfma_f32_16x16x32_bf16 v[60:63], v[36:39], a[184:187], v[60:63]\n" \
"s_waitcnt lgkmcnt(4)\n" \
"v_mfma_f32_16x16x32_bf16 v[48:51], v[40:43], a[60:63], v[48:51]\n" \
"v_mfma_f32_16x16x32_bf16 v[52:55], v[44:47], a[60:63], v[52:55]\n" \
"ds_read_b128 v[32:35], v10 offset:34816\n" \
"v_mfma_f32_16x16x32_bf16 v[56:59], v[40:43], a[188:191], v[56:59]\n" \
"ds_read_b128 v[36:39], v10 offset:51200\n" \
"v_mfma_f32_16x16x32_bf16 v[60:63], v[44:47], a[188:191], v[60:63]\n" \
"s_waitcnt lgkmcnt(4)\n" \
"v_mfma_f32_16x16x32_bf16 v[48:51], v[16:19], a[64:67], v[48:51]\n" \
"s_min_u32 s61, s56, s60\n" \
"s_add_u32 s61, s61, 16384\n" \
"s_add_u32 s56, s56, 0x10000\n" \
"s_add_u32 m0, s55, 98304\n" \
"s_nop 0\n" \
"buffer_load_dwordx4 v4, s[36:39], s61 offen offset:0 sc1 lds\n" \
"v_mfma_f32_16x16x32_bf16 v[52:55], v[20:23], a[64:67], v[52:55]\n" \
"ds_read_b128 v[40:43], v10 offset:35840\n" \
"v_mfma_f32_16x16x32_bf16 v[56:59], v[16:19], a[192:195], v[56:59]\n" \
"ds_read_b128 v[44:47], v10 offset:52224\n" \
"v_mfma_f32_16x16x32_bf16 v[60:63], v[20:23], a[192:195], v[60:63]\n" \
"s_waitcnt lgkmcnt(4)\n" \
"v_mfma_f32_16x16x32_bf16 v[48:51], v[24:27], a[68:71], v[48:51]\n" \
"buffer_load_dwordx4 v4, s[36:39], s61 offen offset:1024 sc1 lds\n" \
"v_mfma_f32_16x16x32_bf16 v[52:55], v[28:31], a[68:71], v[52:55]\n" \
"ds_read_b128 v[16:19], v10 offset:36864\n" \
"v_mfma_f32_16x16x32_bf16 v[56:59], v[24:27], a[196:199], v[56:59]\n" \
"ds_read_b128 v[20:23], v10 offset:53248\n" \
"v_mfma_f32_16x16x32_bf16 v[60:63], v[28:31], a[196:199], v[60:63]\n" \
"s_waitcnt lgkmcnt(4)\n" \
"v_mfma_f32_16x16x32_bf16 v[48:51], v[32:35], a[72:75], v[48:51]\n" \
"v_mfma_f32_16x16x32_bf16 v[52:55], v[36:39], a[72:75], v[52:55]\n" \
"ds_read_b128 v[24:27], v10 offset:37888\n" \
"v_mfma_f32_16x16x32_bf16 v[56:59], v[32:35], a[200:203], v[56:59]\n" \
"ds_read_b128 v[28:31], v10 offset:54272\n" \
"v_mfma_f32_16x16x32_bf16 v[60:63], v[36:39], a[200:203], v[60:63]\n" \
"s_waitcnt lgkmcnt(4)\n" \
"v_mfma_f32_16x16x32_bf16 v[48:51], v[40:43], a[76:79], v[48:51]\n" \
"buffer_load_dwordx4 v4, s[36:39], s61 offen offset:2048 sc1 lds\n" \
"v_mfma_f32_16x16x32_bf16 v[52:55], v[44:47], a[76:79], v[52:55]\n" \
"ds_read_b128 v[32:35], v10 offset:38912\n" \
"v_mfma_f32_16x16x32_bf16 v[56:59], v[40:43], a[204:207], v[56:59]\n" \
"ds_read_b128 v[36:39], v10 offset:55296\n" \
"v_mfma_f32_16x16x32_bf16 v[60:63], v[44:47], a[204:207], v[60:63]\n" \
"s_waitcnt lgkmcnt(4)\n" \
"v_mfma_f32_16x16x32_bf16 v[48:51], v[16:19], a[80:83], v[48:51]\n" \
"buffer_load_dwordx4 v4, s[36:39], s61 offen offset:3072 sc1 lds\n" \
"v_mfma_f32_16x16x32_bf16 v[52:55], v[20:23], a[80:83], v[52:55]\n" \
"ds_read_b128 v[40:43], v10 offset:39936\n" \
"v_mfma_f32_16x16x32_bf16 v[56:59], v[16:19], a[208:211], v[56:59]\n" \
"ds_read_b128 v[44:47], v10 offset:56320\n" \
"v_mfma_f32_16x16x32_bf16 v[60:63], v[20:23], a[208:211], v[60:63]\n" \
"s_waitcnt lgkmcnt(4)\n" \
"v_mfma_f32_16x16x32_bf16 v[48:51], v[24:27], a[84:87], v[48:51]\n" \
"v_mfma_f32_16x16x32_bf16 v[52:55], v[28:31], a[84:87], v[52:55]\n" \
"ds_read_b128 v[16:19], v10 offset:40960\n" \
"v_mfma_f32_16x16x32_bf16 v[56:59], v[24:27], a[212:215], v[56:59]\n" \
"ds_read_b128 v[20:23], v10 offset:57344\n" \
"v_mfma_f32_16x16x32_bf16 v[60:63], v[28:31], a[212:215], v[60:63]\n" \
"s_waitcnt lgkmcnt(4)\n" \
"v_mfma_f32_16x16x32_bf16 v[48:51], v[32:35], a[88:91], v[48:51]\n" \
"s_add_u32 m0, s55, 102400\n" \
"s_add_u32 s61, s61, 4096\n" \
"buffer_load_dwordx4 v4, s[36:39], s61 offen offset:0 sc1 lds\n" \
"v_mfma_f32_16x16x32_bf16 v[52:55], v[36:39], a[88:91], v[52:55]\n" \
"ds_read_b128 v[24:27], v10 offset:41984\n" \
"v_mfma_f32_16x16x32_bf16 v[56:59], v[32:35], a[216:219], v[56:59]\n" \
"ds_read_b128 v[28:31], v10 offset:58368\n" \
"v_mfma_f32_16x16x32_bf16 v[60:63], v[36:39], a[216:219], v[60:63]\n" \
"s_waitcnt lgkmcnt(4)\n" \
"v_mfma_f32_16x16x32_bf16 v[48:51], v[40:43], a[92:95], v[48:51]\n" \
"buffer_load_dwordx4 v4, s[36:39], s61 offen offset:1024 sc1 lds\n" \
"v_mfma_f32_16x16x32_bf16 v[52:55], v[44:47], a[92:95], v[52:55]\n" \
"ds_read_b128 v[32:35], v10 offset:43008\n" \
"v_mfma_f32_16x16x32_bf16 v[56:59], v[40:43], a[220:223], v[56:59]\n" \
"ds_read_b128 v[36:39], v10 offset:59392\n" \
"v_mfma_f32_16x16x32_bf16 v[60:63], v[44:47], a[220:223], v[60:63]\n" \
"s_waitcnt lgkmcnt(4)\n" \
"v_mfma_f32_16x16x32_bf16 v[48:51], v[16:19], a[96:99], v[48:51]\n" \
"v_mfma_f32_16x16x32_bf16 v[52:55], v[20:23], a[96:99], v[52:55]\n" \
"ds_read_b128 v[40:43], v10 offset:44032\n" \
"v_mfma_f32_16x16x32_bf16 v[56:59], v[16:19], a[224:227], v[56:59]\n" \
"ds_read_b128 v[44:47], v10 offset:60416\n" \
"v_mfma_f32_16x16x32_bf16 v[60:63], v[20:23], a[224:227], v[60:63]\n" \
"s_waitcnt lgkmcnt(4)\n" \
"v_mfma_f32_16x16x32_bf16 v[48:51], v[24:27], a[100:103], v[48:51]\n" \
"buffer_load_dwordx4 v4, s[36:39], s61 offen offset:2048 sc1 lds\n" \
"v_mfma_f32_16x16x32_bf16 v[52:55], v[28:31], a[100:103], v[52:55]\n" \
"ds_read_b128 v[16:19], v10 offset:45056\n" \
"v_mfma_f32_16x16x32_bf16 v[56:59], v[24:27], a[228:231], v[56:59]\n" \
"ds_read_b128 v[20:23], v10 offset:61440\n" \
"v_mfma_f32_16x16x32_bf16 v[60:63], v[28:31], a[228:231], v[60:63]\n" \
"s_waitcnt lgkmcnt(4)\n" \
"v_mfma_f32_16x16x32_bf16 v[48:51], v[32:35], a[104:107], v[48:51]\n" \
"buffer_load_dwordx4 v4, s[36:39], s61 offen offset:3072 sc1 lds\n" \
"v_mfma_f32_16x16x32_bf16 v[52:55], v[36:39], a[104:107], v[52:55]\n" \
"ds_read_b128 v[24:27], v10 offset:46080\n" \
"v_mfma_f32_16x16x32_bf16 v[56:59], v[32:35], a[232:235], v[56:59]\n" \
"ds_read_b128 v[28:31], v10 offset:62464\n" \
"v_mfma_f32_16x16x32_bf16 v[60:63], v[36:39], a[232:235], v[60:63]\n" \
"s_waitcnt lgkmcnt(4)\n" \
"v_mfma_f32_16x16x32_bf16 v[48:51], v[40:43], a[108:111], v[48:51]\n" \
"v_mfma_f32_16x16x32_bf16 v[52:55], v[44:47], a[108:111], v[52:55]\n" \
"ds_read_b128 v[32:35], v10 offset:47104\n" \
"v_mfma_f32_16x16x32_bf16 v[56:59], v[40:43], a[236:239], v[56:59]\n" \
"ds_read_b128 v[36:39], v10 offset:63488\n" \
"v_mfma_f32_16x16x32_bf16 v[60:63], v[44:47], a[236:239], v[60:63]\n" \
"s_waitcnt lgkmcnt(4)\n" \
"v_mfma_f32_16x16x32_bf16 v[48:51], v[16:19], a[112:115], v[48:51]\n" \
"v_mfma_f32_16x16x32_bf16 v[52:55], v[20:23], a[112:115], v[52:55]\n" \
"ds_read_b128 v[40:43], v10 offset:48128\n" \
"v_mfma_f32_16x16x32_bf16 v[56:59], v[16:19], a[240:243], v[56:59]\n" \
"ds_read_b128 v[44:47], v10 offset:64512\n" \
"v_mfma_f32_16x16x32_bf16 v[60:63], v[20:23], a[240:243], v[60:63]\n" \
"s_waitcnt lgkmcnt(4)\n" \
"v_mfma_f32_16x16x32_bf16 v[48:51], v[24:27], a[116:119], v[48:51]\n" \
"s_waitcnt vmcnt(8)\n" \
"s_barrier\n" \
"v_mfma_f32_16x16x32_bf16 v[52:55], v[28:31], a[116:119], v[52:55]\n" \
"ds_read_b128 v[16:19], v12 offset:0\n" \
"v_mfma_f32_16x16x32_bf16 v[56:59], v[24:27], a[244:247], v[56:59]\n" \
"ds_read_b128 v[20:23], v12 offset:16384\n" \
"v_mfma_f32_16x16x32_bf16 v[60:63], v[28:31], a[244:247], v[60:63]\n" \
"s_waitcnt lgkmcnt(4)\n" \
"buffer_store_dword v13, v6, s[36:39], 0 offen sc1\n" \
"v_mfma_f32_16x16x32_bf16 v[48:51], v[32:35], a[120:123], v[48:51]\n" \
"v_cmp_gt_u32_e64 vcc, 16, v15\n" \
"v_mfma_f32_16x16x32_bf16 v[52:55], v[36:39], a[120:123], v[52:55]\n" \
"ds_read_b128 v[24:27], v12 offset:1024\n" \
"v_mfma_f32_16x16x32_bf16 v[56:59], v[32:35], a[248:251], v[56:59]\n" \
"ds_read_b128 v[28:31], v12 offset:17408\n" \
"v_mfma_f32_16x16x32_bf16 v[60:63], v[36:39], a[248:251], v[60:63]\n" \
"s_waitcnt lgkmcnt(4)\n" \
"v_mfma_f32_16x16x32_bf16 v[48:51], v[40:43], a[124:127], v[48:51]\n" \
"s_nop 0\n" \
"s_cbranch_vccnz .Lp1v_m0_%=\n" \
".Lp1v_m0k_%=:\n" \
"v_mfma_f32_16x16x32_bf16 v[52:55], v[44:47], a[124:127], v[52:55]\n" \
"ds_read_b128 v[32:35], v12 offset:2048\n" \
"v_mfma_f32_16x16x32_bf16 v[56:59], v[40:43], a[252:255], v[56:59]\n" \
"ds_read_b128 v[36:39], v12 offset:18432\n" \
"v_mfma_f32_16x16x32_bf16 v[60:63], v[44:47], a[252:255], v[60:63]\n" \
"s_waitcnt lgkmcnt(4)\n" \
"s_add_u32 s57, s57, s49\n" \
"s_add_u32 s58, s58, s49\n" \
"s_add_u32 s59, s59, 1\n" \
"v_mov_b32 v5, v7\n" \
"v_mov_b32 v6, v11\n" \
"s_cmp_ge_u32 s59, s47\n" \
"s_cbranch_scc1 .Lp1v_out0_%=\n" \
"v_mfma_f32_16x16x32_bf16 v[64:67], v[16:19], a[0:3], 0\n" \
"s_min_u32 s61, s56, s60\n" \
"s_add_u32 m0, s55, 0\n" \
"s_nop 0\n" \
"buffer_load_dwordx4 v4, s[36:39], s61 offen offset:0 sc1 lds\n" \
"v_mov_b32_e64 v13, s59\n" \
"v_mfma_f32_16x16x32_bf16 v[68:71], v[20:23], a[0:3], 0\n" \
"ds_read_b128 v[40:43], v12 offset:3072\n" \
"s_nop 0\n" \
"v_mfma_f32_16x16x32_bf16 v[72:75], v[16:19], a[128:131], 0\n" \
"ds_read_b128 v[44:47], v12 offset:19456\n" \
"s_nop 0\n" \
"v_mfma_f32_16x16x32_bf16 v[76:79], v[20:23], a[128:131], 0\n" \
"s_waitcnt lgkmcnt(4)\n" \
"v_cvt_pk_bf16_f32 v80, v48, v49\n" \
"v_cvt_pk_bf16_f32 v81, v50, v51\n" \
"v_mfma_f32_16x16x32_bf16 v[64:67], v[24:27], a[4:7], v[64:67]\n" \
"buffer_load_dwordx4 v4, s[36:39], s61 offen offset:1024 sc1 lds\n" \
"v_mfma_f32_16x16x32_bf16 v[68:71], v[28:31], a[4:7], v[68:71]\n" \
"ds_read_b128 v[16:19], v12 offset:4096\n" \
"v_cvt_pk_bf16_f32 v82, v52, v53\n" \
"v_mfma_f32_16x16x32_bf16 v[72:75], v[24:27], a[132:135], v[72:75]\n" \
"ds_read_b128 v[20:23], v12 offset:20480\n" \
"v_cvt_pk_bf16_f32 v83, v54, v55\n" \
"v_mfma_f32_16x16x32_bf16 v[76:79], v[28:31], a[132:135], v[76:79]\n" \
"s_waitcnt lgkmcnt(4)\n" \
"buffer_store_dwordx4 v[80:83], v5, s[36:39], s57 offen sc1\n" \
"v_cvt_pk_bf16_f32 v84, v56, v57\n" \
"v_mfma_f32_16x16x32_bf16 v[64:67], v[32:35], a[8:11], v[64:67]\n" \
"s_add_u32 s63, s59, 2\n" \
"s_min_u32 s63, s63, 63\n" \
"v_mfma_f32_16x16x32_bf16 v[68:71], v[36:39], a[8:11], v[68:71]\n" \
"ds_read_b128 v[24:27], v12 offset:5120\n" \
"s_lshr_b32 s63, s63, 3\n" \
"s_lshl_b32 s63, s63, 7\n" \
"v_cvt_pk_bf16_f32 v85, v58, v59\n" \
"v_mfma_f32_16x16x32_bf16 v[72:75], v[32:35], a[136:139], v[72:75]\n" \
"ds_read_b128 v[28:31], v12 offset:21504\n" \
"v_cvt_pk_bf16_f32 v86, v60, v61\n" \
"v_mfma_f32_16x16x32_bf16 v[76:79], v[36:39], a[136:139], v[76:79]\n" \
"s_waitcnt lgkmcnt(4)\n" \
"v_cvt_pk_bf16_f32 v87, v62, v63\n" \
"buffer_store_dwordx4 v[84:87], v5, s[36:39], s58 offen sc1\n" \
"v_mfma_f32_16x16x32_bf16 v[64:67], v[40:43], a[12:15], v[64:67]\n" \
"buffer_load_dwordx4 v4, s[36:39], s61 offen offset:2048 sc1 lds\n" \
"v_mfma_f32_16x16x32_bf16 v[68:71], v[44:47], a[12:15], v[68:71]\n" \
"ds_read_b128 v[32:35], v12 offset:6144\n" \
"v_mfma_f32_16x16x32_bf16 v[72:75], v[40:43], a[140:143], v[72:75]\n" \
"ds_read_b128 v[36:39], v12 offset:22528\n" \
"v_mfma_f32_16x16x32_bf16 v[76:79], v[44:47], a[140:143], v[76:79]\n" \
"s_waitcnt lgkmcnt(4)\n" \
"v_mfma_f32_16x16x32_bf16 v[64:67], v[16:19], a[16:19], v[64:67]\n" \
"buffer_load_dwordx4 v4, s[36:39], s61 offen offset:3072 sc1 lds\n" \
"v_mfma_f32_16x16x32_bf16 v[68:71], v[20:23], a[16:19], v[68:71]\n" \
"ds_read_b128 v[40:43], v12 offset:7168\n" \
"v_mfma_f32_16x16x32_bf16 v[72:75], v[16:19], a[144:147], v[72:75]\n" \
"ds_read_b128 v[44:47], v12 offset:23552\n" \
"v_mfma_f32_16x16x32_bf16 v[76:79], v[20:23], a[144:147], v[76:79]\n" \
"s_waitcnt lgkmcnt(4)\n" \
"v_mfma_f32_16x16x32_bf16 v[64:67], v[24:27], a[20:23], v[64:67]\n" \
"buffer_load_dword v15, v14, s[36:39], s63 offen sc1\n" \
"v_mfma_f32_16x16x32_bf16 v[68:71], v[28:31], a[20:23], v[68:71]\n" \
"ds_read_b128 v[16:19], v12 offset:8192\n" \
"v_mfma_f32_16x16x32_bf16 v[72:75], v[24:27], a[148:151], v[72:75]\n" \
"ds_read_b128 v[20:23], v12 offset:24576\n" \
"v_mfma_f32_16x16x32_bf16 v[76:79], v[28:31], a[148:151], v[76:79]\n" \
"s_waitcnt lgkmcnt(4)\n" \
"v_mfma_f32_16x16x32_bf16 v[64:67], v[32:35], a[24:27], v[64:67]\n" \
"s_add_u32 m0, s55, 4096\n" \
"s_add_u32 s61, s61, 4096\n" \
"buffer_load_dwordx4 v4, s[36:39], s61 offen offset:0 sc1 lds\n" \
"v_mfma_f32_16x16x32_bf16 v[68:71], v[36:39], a[24:27], v[68:71]\n" \
"ds_read_b128 v[24:27], v12 offset:9216\n" \
"v_mfma_f32_16x16x32_bf16 v[72:75], v[32:35], a[152:155], v[72:75]\n" \
"ds_read_b128 v[28:31], v12 offset:25600\n" \
"v_mfma_f32_16x16x32_bf16 v[76:79], v[36:39], a[152:155], v[76:79]\n" \
"s_waitcnt lgkmcnt(4)\n" \
"v_mfma_f32_16x16x32_bf16 v[64:67], v[40:43], a[28:31], v[64:67]\n" \
"buffer_load_dwordx4 v4, s[36:39], s61 offen offset:1024 sc1 lds\n" \
"v_mfma_f32_16x16x32_bf16 v[68:71], v[44:47], a[28:31], v[68:71]\n" \
"ds_read_b128 v[32:35], v12 offset:10240\n" \
"v_mfma_f32_16x16x32_bf16 v[72:75], v[40:43], a[156:159], v[72:75]\n" \
"ds_read_b128 v[36:39], v12 offset:26624\n" \
"v_mfma_f32_16x16x32_bf16 v[76:79], v[44:47], a[156:159], v[76:79]\n" \
"s_waitcnt lgkmcnt(4)\n" \
"v_mfma_f32_16x16x32_bf16 v[64:67], v[16:19], a[32:35], v[64:67]\n" \
"v_mfma_f32_16x16x32_bf16 v[68:71], v[20:23], a[32:35], v[68:71]\n" \
"ds_read_b128 v[40:43], v12 offset:11264\n" \
"v_mfma_f32_16x16x32_bf16 v[72:75], v[16:19], a[160:163], v[72:75]\n" \
"ds_read_b128 v[44:47], v12 offset:27648\n" \
"v_mfma_f32_16x16x32_bf16 v[76:79], v[20:23], a[160:163], v[76:79]\n" \
"s_waitcnt lgkmcnt(4)\n" \
"v_mfma_f32_16x16x32_bf16 v[64:67], v[24:27], a[36:39], v[64:67]\n" \
"buffer_load_dwordx4 v4, s[36:39], s61 offen offset:2048 sc1 lds\n" \
"v_mfma_f32_16x16x32_bf16 v[68:71], v[28:31], a[36:39], v[68:71]\n" \
"ds_read_b128 v[16:19], v12 offset:12288\n" \
"v_mfma_f32_16x16x32_bf16 v[72:75], v[24:27], a[164:167], v[72:75]\n" \
"ds_read_b128 v[20:23], v12 offset:28672\n" \
"v_mfma_f32_16x16x32_bf16 v[76:79], v[28:31], a[164:167], v[76:79]\n" \
"s_waitcnt lgkmcnt(4)\n" \
"v_mfma_f32_16x16x32_bf16 v[64:67], v[32:35], a[40:43], v[64:67]\n" \
"buffer_load_dwordx4 v4, s[36:39], s61 offen offset:3072 sc1 lds\n" \
"v_mfma_f32_16x16x32_bf16 v[68:71], v[36:39], a[40:43], v[68:71]\n" \
"ds_read_b128 v[24:27], v12 offset:13312\n" \
"v_mfma_f32_16x16x32_bf16 v[72:75], v[32:35], a[168:171], v[72:75]\n" \
"ds_read_b128 v[28:31], v12 offset:29696\n" \
"v_mfma_f32_16x16x32_bf16 v[76:79], v[36:39], a[168:171], v[76:79]\n" \
"s_waitcnt lgkmcnt(4)\n" \
"v_mfma_f32_16x16x32_bf16 v[64:67], v[40:43], a[44:47], v[64:67]\n" \
"v_mfma_f32_16x16x32_bf16 v[68:71], v[44:47], a[44:47], v[68:71]\n" \
"ds_read_b128 v[32:35], v12 offset:14336\n" \
"v_mfma_f32_16x16x32_bf16 v[72:75], v[40:43], a[172:175], v[72:75]\n" \
"ds_read_b128 v[36:39], v12 offset:30720\n" \
"v_mfma_f32_16x16x32_bf16 v[76:79], v[44:47], a[172:175], v[76:79]\n" \
"s_waitcnt lgkmcnt(4)\n" \
"v_mfma_f32_16x16x32_bf16 v[64:67], v[16:19], a[48:51], v[64:67]\n" \
"v_mfma_f32_16x16x32_bf16 v[68:71], v[20:23], a[48:51], v[68:71]\n" \
"ds_read_b128 v[40:43], v12 offset:15360\n" \
"v_mfma_f32_16x16x32_bf16 v[72:75], v[16:19], a[176:179], v[72:75]\n" \
"ds_read_b128 v[44:47], v12 offset:31744\n" \
"v_mfma_f32_16x16x32_bf16 v[76:79], v[20:23], a[176:179], v[76:79]\n" \
"s_waitcnt lgkmcnt(4)\n" \
"v_mfma_f32_16x16x32_bf16 v[64:67], v[24:27], a[52:55], v[64:67]\n" \
"s_waitcnt vmcnt(12)\n" \
"s_barrier\n" \
"v_mfma_f32_16x16x32_bf16 v[68:71], v[28:31], a[52:55], v[68:71]\n" \
"ds_read_b128 v[16:19], v12 offset:32768\n" \
"v_mfma_f32_16x16x32_bf16 v[72:75], v[24:27], a[180:183], v[72:75]\n" \
"ds_read_b128 v[20:23], v12 offset:49152\n" \
"v_mfma_f32_16x16x32_bf16 v[76:79], v[28:31], a[180:183], v[76:79]\n" \
"s_waitcnt lgkmcnt(4)\n" \
"v_mfma_f32_16x16x32_bf16 v[64:67], v[32:35], a[56:59], v[64:67]\n" \
"v_mfma_f32_16x16x32_bf16 v[68:71], v[36:39], a[56:59], v[68:71]\n" \
"ds_read_b128 v[24:27], v12 offset:33792\n" \
"v_mfma_f32_16x16x32_bf16 v[72:75], v[32:35], a[184:187], v[72:75]\n" \
"ds_read_b128 v[28:31], v12 offset:50176\n" \
"v_mfma_f32_16x16x32_bf16 v[76:79], v[36:39], a[184:187], v[76:79]\n" \
"s_waitcnt lgkmcnt(4)\n" \
"v_mfma_f32_16x16x32_bf16 v[64:67], v[40:43], a[60:63], v[64:67]\n" \
"v_mfma_f32_16x16x32_bf16 v[68:71], v[44:47], a[60:63], v[68:71]\n" \
"ds_read_b128 v[32:35], v12 offset:34816\n" \
"v_mfma_f32_16x16x32_bf16 v[72:75], v[40:43], a[188:191], v[72:75]\n" \
"ds_read_b128 v[36:39], v12 offset:51200\n" \
"v_mfma_f32_16x16x32_bf16 v[76:79], v[44:47], a[188:191], v[76:79]\n" \
"s_waitcnt lgkmcnt(4)\n" \
"v_mfma_f32_16x16x32_bf16 v[64:67], v[16:19], a[64:67], v[64:67]\n" \
"s_min_u32 s61, s56, s60\n" \
"s_add_u32 s61, s61, 16384\n" \
"s_add_u32 s56, s56, 0x10000\n" \
"s_add_u32 m0, s55, 32768\n" \
"s_nop 0\n" \
"buffer_load_dwordx4 v4, s[36:39], s61 offen offset:0 sc1 lds\n" \
"v_mfma_f32_16x16x32_bf16 v[68:71], v[20:23], a[64:67], v[68:71]\n" \
"ds_read_b128 v[40:43], v12 offset:35840\n" \
"v_mfma_f32_16x16x32_bf16 v[72:75], v[16:19], a[192:195], v[72:75]\n" \
"ds_read_b128 v[44:47], v12 offset:52224\n" \
"v_mfma_f32_16x16x32_bf16 v[76:79], v[20:23], a[192:195], v[76:79]\n" \
"s_waitcnt lgkmcnt(4)\n" \
"v_mfma_f32_16x16x32_bf16 v[64:67], v[24:27], a[68:71], v[64:67]\n" \
"buffer_load_dwordx4 v4, s[36:39], s61 offen offset:1024 sc1 lds\n" \
"v_mfma_f32_16x16x32_bf16 v[68:71], v[28:31], a[68:71], v[68:71]\n" \
"ds_read_b128 v[16:19], v12 offset:36864\n" \
"v_mfma_f32_16x16x32_bf16 v[72:75], v[24:27], a[196:199], v[72:75]\n" \
"ds_read_b128 v[20:23], v12 offset:53248\n" \
"v_mfma_f32_16x16x32_bf16 v[76:79], v[28:31], a[196:199], v[76:79]\n" \
"s_waitcnt lgkmcnt(4)\n" \
"v_mfma_f32_16x16x32_bf16 v[64:67], v[32:35], a[72:75], v[64:67]\n" \
"v_mfma_f32_16x16x32_bf16 v[68:71], v[36:39], a[72:75], v[68:71]\n" \
"ds_read_b128 v[24:27], v12 offset:37888\n" \
"v_mfma_f32_16x16x32_bf16 v[72:75], v[32:35], a[200:203], v[72:75]\n" \
"ds_read_b128 v[28:31], v12 offset:54272\n" \
"v_mfma_f32_16x16x32_bf16 v[76:79], v[36:39], a[200:203], v[76:79]\n" \
"s_waitcnt lgkmcnt(4)\n" \
"v_mfma_f32_16x16x32_bf16 v[64:67], v[40:43], a[76:79], v[64:67]\n" \
"buffer_load_dwordx4 v4, s[36:39], s61 offen offset:2048 sc1 lds\n" \
"v_mfma_f32_16x16x32_bf16 v[68:71], v[44:47], a[76:79], v[68:71]\n" \
"ds_read_b128 v[32:35], v12 offset:38912\n" \
"v_mfma_f32_16x16x32_bf16 v[72:75], v[40:43], a[204:207], v[72:75]\n" \
"ds_read_b128 v[36:39], v12 offset:55296\n" \
"v_mfma_f32_16x16x32_bf16 v[76:79], v[44:47], a[204:207], v[76:79]\n" \
"s_waitcnt lgkmcnt(4)\n" \
"v_mfma_f32_16x16x32_bf16 v[64:67], v[16:19], a[80:83], v[64:67]\n" \
"buffer_load_dwordx4 v4, s[36:39], s61 offen offset:3072 sc1 lds\n" \
"v_mfma_f32_16x16x32_bf16 v[68:71], v[20:23], a[80:83], v[68:71]\n" \
"ds_read_b128 v[40:43], v12 offset:39936\n" \
"v_mfma_f32_16x16x32_bf16 v[72:75], v[16:19], a[208:211], v[72:75]\n" \
"ds_read_b128 v[44:47], v12 offset:56320\n" \
"v_mfma_f32_16x16x32_bf16 v[76:79], v[20:23], a[208:211], v[76:79]\n" \
"s_waitcnt lgkmcnt(4)\n" \
"v_mfma_f32_16x16x32_bf16 v[64:67], v[24:27], a[84:87], v[64:67]\n" \
"v_mfma_f32_16x16x32_bf16 v[68:71], v[28:31], a[84:87], v[68:71]\n" \
"ds_read_b128 v[16:19], v12 offset:40960\n" \
"v_mfma_f32_16x16x32_bf16 v[72:75], v[24:27], a[212:215], v[72:75]\n" \
"ds_read_b128 v[20:23], v12 offset:57344\n" \
"v_mfma_f32_16x16x32_bf16 v[76:79], v[28:31], a[212:215], v[76:79]\n" \
"s_waitcnt lgkmcnt(4)\n" \
"v_mfma_f32_16x16x32_bf16 v[64:67], v[32:35], a[88:91], v[64:67]\n" \
"s_add_u32 m0, s55, 36864\n" \
"s_add_u32 s61, s61, 4096\n" \
"buffer_load_dwordx4 v4, s[36:39], s61 offen offset:0 sc1 lds\n" \
"v_mfma_f32_16x16x32_bf16 v[68:71], v[36:39], a[88:91], v[68:71]\n" \
"ds_read_b128 v[24:27], v12 offset:41984\n" \
"v_mfma_f32_16x16x32_bf16 v[72:75], v[32:35], a[216:219], v[72:75]\n" \
"ds_read_b128 v[28:31], v12 offset:58368\n" \
"v_mfma_f32_16x16x32_bf16 v[76:79], v[36:39], a[216:219], v[76:79]\n" \
"s_waitcnt lgkmcnt(4)\n" \
"v_mfma_f32_16x16x32_bf16 v[64:67], v[40:43], a[92:95], v[64:67]\n" \
"buffer_load_dwordx4 v4, s[36:39], s61 offen offset:1024 sc1 lds\n" \
"v_mfma_f32_16x16x32_bf16 v[68:71], v[44:47], a[92:95], v[68:71]\n" \
"ds_read_b128 v[32:35], v12 offset:43008\n" \
"v_mfma_f32_16x16x32_bf16 v[72:75], v[40:43], a[220:223], v[72:75]\n" \
"ds_read_b128 v[36:39], v12 offset:59392\n" \
"v_mfma_f32_16x16x32_bf16 v[76:79], v[44:47], a[220:223], v[76:79]\n" \
"s_waitcnt lgkmcnt(4)\n" \
"v_mfma_f32_16x16x32_bf16 v[64:67], v[16:19], a[96:99], v[64:67]\n" \
"v_mfma_f32_16x16x32_bf16 v[68:71], v[20:23], a[96:99], v[68:71]\n" \
"ds_read_b128 v[40:43], v12 offset:44032\n" \
"v_mfma_f32_16x16x32_bf16 v[72:75], v[16:19], a[224:227], v[72:75]\n" \
"ds_read_b128 v[44:47], v12 offset:60416\n" \
"v_mfma_f32_16x16x32_bf16 v[76:79], v[20:23], a[224:227], v[76:79]\n" \
"s_waitcnt lgkmcnt(4)\n" \
"v_mfma_f32_16x16x32_bf16 v[64:67], v[24:27], a[100:103], v[64:67]\n" \
"buffer_load_dwordx4 v4, s[36:39], s61 offen offset:2048 sc1 lds\n" \
"v_mfma_f32_16x16x32_bf16 v[68:71], v[28:31], a[100:103], v[68:71]\n" \
"ds_read_b128 v[16:19], v12 offset:45056\n" \
"v_mfma_f32_16x16x32_bf16 v[72:75], v[24:27], a[228:231], v[72:75]\n" \
"ds_read_b128 v[20:23], v12 offset:61440\n" \
"v_mfma_f32_16x16x32_bf16 v[76:79], v[28:31], a[228:231], v[76:79]\n" \
"s_waitcnt lgkmcnt(4)\n" \
"v_mfma_f32_16x16x32_bf16 v[64:67], v[32:35], a[104:107], v[64:67]\n" \
"buffer_load_dwordx4 v4, s[36:39], s61 offen offset:3072 sc1 lds\n" \
"v_mfma_f32_16x16x32_bf16 v[68:71], v[36:39], a[104:107], v[68:71]\n" \
"ds_read_b128 v[24:27], v12 offset:46080\n" \
"v_mfma_f32_16x16x32_bf16 v[72:75], v[32:35], a[232:235], v[72:75]\n" \
"ds_read_b128 v[28:31], v12 offset:62464\n" \
"v_mfma_f32_16x16x32_bf16 v[76:79], v[36:39], a[232:235], v[76:79]\n" \
"s_waitcnt lgkmcnt(4)\n" \
"v_mfma_f32_16x16x32_bf16 v[64:67], v[40:43], a[108:111], v[64:67]\n" \
"v_mfma_f32_16x16x32_bf16 v[68:71], v[44:47], a[108:111], v[68:71]\n" \
"ds_read_b128 v[32:35], v12 offset:47104\n" \
"v_mfma_f32_16x16x32_bf16 v[72:75], v[40:43], a[236:239], v[72:75]\n" \
"ds_read_b128 v[36:39], v12 offset:63488\n" \
"v_mfma_f32_16x16x32_bf16 v[76:79], v[44:47], a[236:239], v[76:79]\n" \
"s_waitcnt lgkmcnt(4)\n" \
"v_mfma_f32_16x16x32_bf16 v[64:67], v[16:19], a[112:115], v[64:67]\n" \
"v_mfma_f32_16x16x32_bf16 v[68:71], v[20:23], a[112:115], v[68:71]\n" \
"ds_read_b128 v[40:43], v12 offset:48128\n" \
"v_mfma_f32_16x16x32_bf16 v[72:75], v[16:19], a[240:243], v[72:75]\n" \
"ds_read_b128 v[44:47], v12 offset:64512\n" \
"v_mfma_f32_16x16x32_bf16 v[76:79], v[20:23], a[240:243], v[76:79]\n" \
"s_waitcnt lgkmcnt(4)\n" \
"v_mfma_f32_16x16x32_bf16 v[64:67], v[24:27], a[116:119], v[64:67]\n" \
"s_waitcnt vmcnt(8)\n" \
"s_barrier\n" \
"v_mfma_f32_16x16x32_bf16 v[68:71], v[28:31], a[116:119], v[68:71]\n" \
"ds_read_b128 v[16:19], v10 offset:0\n" \
"v_mfma_f32_16x16x32_bf16 v[72:75], v[24:27], a[244:247], v[72:75]\n" \
"ds_read_b128 v[20:23], v10 offset:16384\n" \
"v_mfma_f32_16x16x32_bf16 v[76:79], v[28:31], a[244:247], v[76:79]\n" \
"s_waitcnt lgkmcnt(4)\n" \
"buffer_store_dword v13, v6, s[36:39], 0 offen sc1\n" \
"v_mfma_f32_16x16x32_bf16 v[64:67], v[32:35], a[120:123], v[64:67]\n" \
"v_cmp_gt_u32_e64 vcc, 16, v15\n" \
"v_mfma_f32_16x16x32_bf16 v[68:71], v[36:39], a[120:123], v[68:71]\n" \
"ds_read_b128 v[24:27], v10 offset:1024\n" \
"v_mfma_f32_16x16x32_bf16 v[72:75], v[32:35], a[248:251], v[72:75]\n" \
"ds_read_b128 v[28:31], v10 offset:17408\n" \
"v_mfma_f32_16x16x32_bf16 v[76:79], v[36:39], a[248:251], v[76:79]\n" \
"s_waitcnt lgkmcnt(4)\n" \
"v_mfma_f32_16x16x32_bf16 v[64:67], v[40:43], a[124:127], v[64:67]\n" \
"s_cbranch_vccnz .Lp1v_m1_%=\n" \
".Lp1v_m1k_%=:\n" \
"v_mfma_f32_16x16x32_bf16 v[68:71], v[44:47], a[124:127], v[68:71]\n" \
"ds_read_b128 v[32:35], v10 offset:2048\n" \
"v_mfma_f32_16x16x32_bf16 v[72:75], v[40:43], a[252:255], v[72:75]\n" \
"ds_read_b128 v[36:39], v10 offset:18432\n" \
"v_mfma_f32_16x16x32_bf16 v[76:79], v[44:47], a[252:255], v[76:79]\n" \
"s_waitcnt lgkmcnt(4)\n" \
"s_add_u32 s57, s57, s49\n" \
"s_add_u32 s58, s58, s49\n" \
"s_add_u32 s59, s59, 1\n" \
"v_mov_b32 v5, v7\n" \
"v_mov_b32 v6, v11\n" \
"s_cmp_ge_u32 s59, s47\n" \
"s_cbranch_scc0 .Lp1v_loop_%=\n" \
".Lp1v_out1_%=:\n" \
"s_nop 7\n" \
"s_nop 7\n" \
"v_cvt_pk_bf16_f32 v80, v64, v65\n" \
"v_cvt_pk_bf16_f32 v81, v66, v67\n" \
"v_cvt_pk_bf16_f32 v82, v68, v69\n" \
"v_cvt_pk_bf16_f32 v83, v70, v71\n" \
"buffer_store_dwordx4 v[80:83], v5, s[36:39], s57 offen sc1\n" \
"v_cvt_pk_bf16_f32 v84, v72, v73\n" \
"v_cvt_pk_bf16_f32 v85, v74, v75\n" \
"v_cvt_pk_bf16_f32 v86, v76, v77\n" \
"v_cvt_pk_bf16_f32 v87, v78, v79\n" \
"buffer_store_dwordx4 v[84:87], v5, s[36:39], s58 offen sc1\n" \
"s_branch .Lp1v_end_%=\n" \
".Lp1v_out0_%=:\n" \
"s_nop 7\n" \
"s_nop 7\n" \
"v_cvt_pk_bf16_f32 v80, v48, v49\n" \
"v_cvt_pk_bf16_f32 v81, v50, v51\n" \
"v_cvt_pk_bf16_f32 v82, v52, v53\n" \
"v_cvt_pk_bf16_f32 v83, v54, v55\n" \
"buffer_store_dwordx4 v[80:83], v5, s[36:39], s57 offen sc1\n" \
"v_cvt_pk_bf16_f32 v84, v56, v57\n" \
"v_cvt_pk_bf16_f32 v85, v58, v59\n" \
"v_cvt_pk_bf16_f32 v86, v60, v61\n" \
"v_cvt_pk_bf16_f32 v87, v62, v63\n" \
"buffer_store_dwordx4 v[84:87], v5, s[36:39], s58 offen sc1\n" \
".Lp1v_end_%=:\n" \
"s_waitcnt vmcnt(0) lgkmcnt(0)\n" \
"s_barrier\n" \
"v_mov_b32 v13, s59\n" \
"buffer_store_dword v13, v6, s[36:39], 0 offen sc1\n" \
"s_waitcnt vmcnt(0) lgkmcnt(0)\n" \
"s_branch .Lp1v_x_%=\n" \
".Lp1v_l0_%=:\n" \
"s_nop 7\n" \
"s_nop 7\n" \
"s_waitcnt vmcnt(0) lgkmcnt(0)\n" \
"s_mov_b32 s62, 0\n" \
".Lp1v_l0a_%=:\n" \
"buffer_load_dword v15, v14, s[36:39], s63 offen sc1\n" \
"s_waitcnt vmcnt(0)\n" \
"v_cmp_gt_u32 vcc, 16, v15\n" \
"s_nop 4\n" \
"s_cbranch_vccz .Lp1v_l0k_%=\n" \
"s_sleep 4\n" \
"s_add_u32 s62, s62, 1\n" \
"s_cmp_lt_u32 s62, 0x10000\n" \
"s_cbranch_scc1 .Lp1v_l0a_%=\n" \
"v_mov_b32 v15, 256\n" \
"buffer_store_dword v15, v15, s[36:39], 0 offen sc1\n" \
"s_waitcnt vmcnt(0) lgkmcnt(0)\n" \
"s_branch .Lp1v_l0k_%=\n" \
".Lp1v_l1_%=:\n" \
"s_nop 7\n" \
"s_nop 7\n" \
"s_waitcnt vmcnt(0) lgkmcnt(0)\n" \
"s_mov_b32 s62, 0\n" \
".Lp1v_l1a_%=:\n" \
"buffer_load_dword v15, v14, s[36:39], s63 offen sc1\n" \
"s_waitcnt vmcnt(0)\n" \
"v_cmp_gt_u32 vcc, 16, v15\n" \
"s_nop 4\n" \
"s_cbranch_vccz .Lp1v_l1k_%=\n" \
"s_sleep 4\n" \
"s_add_u32 s62, s62, 1\n" \
"s_cmp_lt_u32 s62, 0x10000\n" \
"s_cbranch_scc1 .Lp1v_l1a_%=\n" \
"v_mov_b32 v15, 256\n" \
"buffer_store_dword v15, v15, s[36:39], 0 offen sc1\n" \
"s_waitcnt vmcnt(0) lgkmcnt(0)\n" \
"s_branch .Lp1v_l1k_%=\n" \
".Lp1v_m0_%=:\n" \
"s_nop 7\n" \
"s_nop 7\n" \
"s_waitcnt vmcnt(0) lgkmcnt(0)\n" \
"s_mov_b32 s62, 0\n" \
".Lp1v_m0a_%=:\n" \
"buffer_load_dword v15, v14, s[36:39], s63 offen sc1\n" \
"s_waitcnt vmcnt(0)\n" \
"v_cmp_gt_u32 vcc, 16, v15\n" \
"s_nop 4\n" \
"s_cbranch_vccz .Lp1v_m0k_%=\n" \
"s_sleep 4\n" \
"s_add_u32 s62, s62, 1\n" \
"s_cmp_lt_u32 s62, 0x10000\n" \
"s_cbranch_scc1 .Lp1v_m0a_%=\n" \
"v_mov_b32 v15, 256\n" \
"buffer_store_dword v15, v15, s[36:39], 0 offen sc1\n" \
"s_waitcnt vmcnt(0) lgkmcnt(0)\n" \
"s_branch .Lp1v_m0k_%=\n" \
".Lp1v_m1_%=:\n" \
"s_nop 7\n" \
"s_nop 7\n" \
"s_waitcnt vmcnt(0) lgkmcnt(0)\n" \
"s_mov_b32 s62, 0\n" \
".Lp1v_m1a_%=:\n" \
"buffer_load_dword v15, v14, s[36:39], s63 offen sc1\n" \
"s_waitcnt vmcnt(0)\n" \
"v_cmp_gt_u32 vcc, 16, v15\n" \
"s_nop 4\n" \
"s_cbranch_vccz .Lp1v_m1k_%=\n" \
"s_sleep 4\n" \
"s_add_u32 s62, s62, 1\n" \
"s_cmp_lt_u32 s62, 0x10000\n" \
"s_cbranch_scc1 .Lp1v_m1a_%=\n" \
"v_mov_b32 v15, 256\n" \
"buffer_store_dword v15, v15, s[36:39], 0 offen sc1\n" \
"s_waitcnt vmcnt(0) lgkmcnt(0)\n" \
"s_branch .Lp1v_m1k_%=\n" \
".Lp1v_x_%=:\n"
#define ASM_P1_G1 \
"s_nop 7\n" \
"s_nop 7\n" \
"s_lshr_b32 s52, s48, 1\n" \
"s_lshl_b32 s52, s52, 13\n" \
"s_and_b32 s53, s48, 1\n" \
"s_lshl_b32 s53, s53, 10\n" \
"s_add_u32 s52, s52, s53\n" \
"s_mov_b32 s54, 0\n" \
"s_add_u32 m0, s52, 0\n" \
"s_nop 0\n" \
"buffer_load_dwordx4 v1, s[40:43], s54 offen nt lds\n" \
"s_add_u32 s54, s54, s44\n" \
"s_add_u32 m0, s52, 2048\n" \
"s_nop 0\n" \
"buffer_load_dwordx4 v1, s[40:43], s54 offen nt lds\n" \
"s_add_u32 s54, s54, s44\n" \
"s_add_u32 m0, s52, 4096\n" \
"s_nop 0\n" \
"buffer_load_dwordx4 v1, s[40:43], s54 offen nt lds\n" \
"s_add_u32 s54, s54, s44\n" \
"s_add_u32 m0, s52, 6144\n" \
"s_nop 0\n" \
"buffer_load_dwordx4 v1, s[40:43], s54 offen nt lds\n" \
"s_add_u32 s54, s54, s44\n" \
"s_add_u32 m0, s52, 16384\n" \
"s_nop 0\n" \
"buffer_load_dwordx4 v1, s[40:43], s54 offen nt lds\n" \
"s_add_u32 s54, s54, s44\n" \
"s_add_u32 m0, s52, 18432\n" \
"s_nop 0\n" \
"buffer_load_dwordx4 v1, s[40:43], s54 offen nt lds\n" \
"s_add_u32 s54, s54, s44\n" \
"s_add_u32 m0, s52, 20480\n" \
"s_nop 0\n" \
"buffer_load_dwordx4 v1, s[40:43], s54 offen nt lds\n" \
"s_add_u32 s54, s54, s44\n" \
"s_add_u32 m0, s52, 22528\n" \
"s_nop 0\n" \
"buffer_load_dwordx4 v1, s[40:43], s54 offen nt lds\n" \
"s_add_u32 s54, s54, s44\n" \
"s_add_u32 m0, s52, 32768\n" \
"s_nop 0\n" \
"buffer_load_dwordx4 v1, s[40:43], s54 offen nt lds\n" \
"s_add_u32 s54, s54, s44\n" \
"s_add_u32 m0, s52, 34816\n" \
"s_nop 0\n" \
"buffer_load_dwordx4 v1, s[40:43], s54 offen nt lds\n" \
"s_add_u32 s54, s54, s44\n" \
"s_add_u32 m0, s52, 36864\n" \
"s_nop 0\n" \
"buffer_load_dwordx4 v1, s[40:43], s54 offen nt lds\n" \
"s_add_u32 s54, s54, s44\n" \
"s_add_u32 m0, s52, 38912\n" \
"s_nop 0\n" \
"buffer_load_dwordx4 v1, s[40:43], s54 offen nt lds\n" \
"s_add_u32 s54, s54, s44\n" \
"s_add_u32 m0, s52, 49152\n" \
"s_nop 0\n" \
"buffer_load_dwordx4 v1, s[40:43], s54 offen nt lds\n" \
"s_add_u32 s54, s54, s44\n" \
"s_add_u32 m0, s52, 51200\n" \
"s_nop 0\n" \
"buffer_load_dwordx4 v1, s[40:43], s54 offen nt lds\n" \
"s_add_u32 s54, s54, s44\n" \
"s_add_u32 m0, s52, 53248\n" \
"s_nop 0\n" \
"buffer_load_dwordx4 v1, s[40:43], s54 offen nt lds\n" \
"s_add_u32 s54, s54, s44\n" \
"s_add_u32 m0, s52, 55296\n" \
"s_nop 0\n" \
"buffer_load_dwordx4 v1, s[40:43], s54 offen nt lds\n" \
"s_add_u32 s54, s54, s44\n" \
"s_add_u32 m0, s52, 65536\n" \
"s_nop 0\n" \
"buffer_load_dwordx4 v1, s[40:43], s54 offen nt lds\n" \
"s_add_u32 s54, s54, s44\n" \
"s_add_u32 m0, s52, 67584\n" \
"s_nop 0\n" \
"buffer_load_dwordx4 v1, s[40:43], s54 offen nt lds\n" \
"s_add_u32 s54, s54, s44\n" \
"s_add_u32 m0, s52, 69632\n" \
"s_nop 0\n" \
"buffer_load_dwordx4 v1, s[40:43], s54 offen nt lds\n" \
"s_add_u32 s54, s54, s44\n" \
"s_add_u32 m0, s52, 71680\n" \
"s_nop 0\n" \
"buffer_load_dwordx4 v1, s[40:43], s54 offen nt lds\n" \
"s_add_u32 s54, s54, s44\n" \
"s_add_u32 m0, s52, 81920\n" \
"s_nop 0\n" \
"buffer_load_dwordx4 v1, s[40:43], s54 offen nt lds\n" \
"s_add_u32 s54, s54, s44\n" \
"s_add_u32 m0, s52, 83968\n" \
"s_nop 0\n" \
"buffer_load_dwordx4 v1, s[40:43], s54 offen nt lds\n" \
"s_add_u32 s54, s54, s44\n" \
"s_add_u32 m0, s52, 86016\n" \
"s_nop 0\n" \
"buffer_load_dwordx4 v1, s[40:43], s54 offen nt lds\n" \
"s_add_u32 s54, s54, s44\n" \
"s_add_u32 m0, s52, 88064\n" \
"s_nop 0\n" \
"buffer_load_dwordx4 v1, s[40:43], s54 offen nt lds\n" \
"s_add_u32 s54, s54, s44\n" \
"s_add_u32 m0, s52, 98304\n" \
"s_nop 0\n" \
"buffer_load_dwordx4 v1, s[40:43], s54 offen nt lds\n" \
"s_add_u32 s54, s54, s44\n" \
"s_add_u32 m0, s52, 100352\n" \
"s_nop 0\n" \
"buffer_load_dwordx4 v1, s[40:43], s54 offen nt lds\n" \
"s_add_u32 s54, s54, s44\n" \
"s_add_u32 m0, s52, 102400\n" \
"s_nop 0\n" \
"buffer_load_dwordx4 v1, s[40:43], s54 offen nt lds\n" \
"s_add_u32 s54, s54, s44\n" \
"s_add_u32 m0, s52, 104448\n" \
"s_nop 0\n" \
"buffer_load_dwordx4 v1, s[40:43], s54 offen nt lds\n" \
"s_add_u32 s54, s54, s44\n" \
"s_add_u32 m0, s52, 114688\n" \
"s_nop 0\n" \
"buffer_load_dwordx4 v1, s[40:43], s54 offen nt lds\n" \
"s_add_u32 s54, s54, s44\n" \
"s_add_u32 m0, s52, 116736\n" \
"s_nop 0\n" \
"buffer_load_dwordx4 v1, s[40:43], s54 offen nt lds\n" \
"s_add_u32 s54, s54, s44\n" \
"s_add_u32 m0, s52, 118784\n" \
"s_nop 0\n" \
"buffer_load_dwordx4 v1, s[40:43], s54 offen nt lds\n" \
"s_add_u32 s54, s54, s44\n" \
"s_add_u32 m0, s52, 120832\n" \
"s_nop 0\n" \
"buffer_load_dwordx4 v1, s[40:43], s54 offen nt lds\n" \
"s_add_u32 s54, s54, s44\n" \
"v_add_u32 v8, 0x10000, v2\n" \
"v_add_u32 v9, 0x10000, v3\n" \
"s_waitcnt vmcnt(28)\n" \
"s_barrier\n" \
"ds_read_b64_tr_b16 a[0:1], v2 offset:0\n" \
"ds_read_b64_tr_b16 a[2:3], v2 offset:512\n" \
"ds_read_b64_tr_b16 a[128:129], v3 offset:0\n" \
"ds_read_b64_tr_b16 a[130:131], v3 offset:512\n" \
"ds_read_b64_tr_b16 a[4:5], v2 offset:4096\n" \
"ds_read_b64_tr_b16 a[6:7], v2 offset:4608\n" \
"ds_read_b64_tr_b16 a[132:133], v3 offset:4096\n" \
"ds_read_b64_tr_b16 a[134:135], v3 offset:4608\n" \
"s_waitcnt lgkmcnt(0)\n" \
"s_barrier\n" \
"s_add_u32 m0, s52, 0\n" \
"s_nop 0\n" \
"buffer_load_dwordx4 v1, s[40:43], s54 offen nt lds\n" \
"s_add_u32 s54, s54, s44\n" \
"s_add_u32 m0, s52, 2048\n" \
"s_nop 0\n" \
"buffer_load_dwordx4 v1, s[40:43], s54 offen nt lds\n" \
"s_add_u32 s54, s54, s44\n" \
"s_add_u32 m0, s52, 4096\n" \
"s_nop 0\n" \
"buffer_load_dwordx4 v1, s[40:43], s54 offen nt lds\n" \
"s_add_u32 s54, s54, s44\n" \
"s_add_u32 m0, s52, 6144\n" \
"s_nop 0\n" \
"buffer_load_dwordx4 v1, s[40:43], s54 offen nt lds\n" \
"s_add_u32 s54, s54, s44\n" \
"s_waitcnt vmcnt(28)\n" \
"s_barrier\n" \
"ds_read_b64_tr_b16 a[8:9], v2 offset:16384\n" \
"ds_read_b64_tr_b16 a[10:11], v2 offset:16896\n" \
"ds_read_b64_tr_b16 a[136:137], v3 offset:16384\n" \
"ds_read_b64_tr_b16 a[138:139], v3 offset:16896\n" \
"ds_read_b64_tr_b16 a[12:13], v2 offset:20480\n" \
"ds_read_b64_tr_b16 a[14:15], v2 offset:20992\n" \
"ds_read_b64_tr_b16 a[140:141], v3 offset:20480\n" \
"ds_read_b64_tr_b16 a[142:143], v3 offset:20992\n" \
"s_waitcnt lgkmcnt(0)\n" \
"s_barrier\n" \
"s_add_u32 m0, s52, 16384\n" \
"s_nop 0\n" \
"buffer_load_dwordx4 v1, s[40:43], s54 offen nt lds\n" \
"s_add_u32 s54, s54, s44\n" \
"s_add_u32 m0, s52, 18432\n" \
"s_nop 0\n" \
"buffer_load_dwordx4 v1, s[40:43], s54 offen nt lds\n" \
"s_add_u32 s54, s54, s44\n" \
"s_add_u32 m0, s52, 20480\n" \
"s_nop 0\n" \
"buffer_load_dwordx4 v1, s[40:43], s54 offen nt lds\n" \
"s_add_u32 s54, s54, s44\n" \
"s_add_u32 m0, s52, 22528\n" \
"s_nop 0\n" \
"buffer_load_dwordx4 v1, s[40:43], s54 offen nt lds\n" \
"s_add_u32 s54, s54, s44\n" \
"s_waitcnt vmcnt(28)\n" \
"s_barrier\n" \
"ds_read_b64_tr_b16 a[16:17], v2 offset:32768\n" \
"ds_read_b64_tr_b16 a[18:19], v2 offset:33280\n" \
"ds_read_b64_tr_b16 a[144:145], v3 offset:32768\n" \
"ds_read_b64_tr_b16 a[146:147], v3 offset:33280\n" \
"ds_read_b64_tr_b16 a[20:21], v2 offset:36864\n" \
"ds_read_b64_tr_b16 a[22:23], v2 offset:37376\n" \
"ds_read_b64_tr_b16 a[148:149], v3 offset:36864\n" \
"ds_read_b64_tr_b16 a[150:151], v3 offset:37376\n" \
"s_waitcnt lgkmcnt(0)\n" \
"s_barrier\n" \
"s_add_u32 m0, s52, 32768\n" \
"s_nop 0\n" \
"buffer_load_dwordx4 v1, s[40:43], s54 offen nt lds\n" \
"s_add_u32 s54, s54, s44\n" \
"s_add_u32 m0, s52, 34816\n" \
"s_nop 0\n" \
"buffer_load_dwordx4 v1, s[40:43], s54 offen nt lds\n" \
"s_add_u32 s54, s54, s44\n" \
"s_add_u32 m0, s52, 36864\n" \
"s_nop 0\n" \
"buffer_load_dwordx4 v1, s[40:43], s54 offen nt lds\n" \
"s_add_u32 s54, s54, s44\n" \
"s_add_u32 m0, s52, 38912\n" \
"s_nop 0\n" \
"buffer_load_dwordx4 v1, s[40:43], s54 offen nt lds\n" \
"s_add_u32 s54, s54, s44\n" \
"s_waitcnt vmcnt(28)\n" \
"s_barrier\n" \
"ds_read_b64_tr_b16 a[24:25], v2 offset:49152\n" \
"ds_read_b64_tr_b16 a[26:27], v2 offset:49664\n" \
"ds_read_b64_tr_b16 a[152:153], v3 offset:49152\n" \
"ds_read_b64_tr_b16 a[154:155], v3 offset:49664\n" \
"ds_read_b64_tr_b16 a[28:29], v2 offset:53248\n" \
"ds_read_b64_tr_b16 a[30:31], v2 offset:53760\n" \
"ds_read_b64_tr_b16 a[156:157], v3 offset:53248\n" \
"ds_read_b64_tr_b16 a[158:159], v3 offset:53760\n" \
"s_waitcnt lgkmcnt(0)\n" \
"s_barrier\n" \
"s_add_u32 m0, s52, 49152\n" \
"s_nop 0\n" \
"buffer_load_dwordx4 v1, s[40:43], s54 offen nt lds\n" \
"s_add_u32 s54, s54, s44\n" \
"s_add_u32 m0, s52, 51200\n" \
"s_nop 0\n" \
"buffer_load_dwordx4 v1, s[40:43], s54 offen nt lds\n" \
"s_add_u32 s54, s54, s44\n" \
"s_add_u32 m0, s52, 53248\n" \
"s_nop 0\n" \
"buffer_load_dwordx4 v1, s[40:43], s54 offen nt lds\n" \
"s_add_u32 s54, s54, s44\n" \
"s_add_u32 m0, s52, 55296\n" \
"s_nop 0\n" \
"buffer_load_dwordx4 v1, s[40:43], s54 offen nt lds\n" \
"s_add_u32 s54, s54, s44\n" \
"s_waitcnt vmcnt(28)\n" \
"s_barrier\n" \
"ds_read_b64_tr_b16 a[32:33], v8 offset:0\n" \
"ds_read_b64_tr_b16 a[34:35], v8 offset:512\n" \
"ds_read_b64_tr_b16 a[160:161], v9 offset:0\n" \
"ds_read_b64_tr_b16 a[162:163], v9 offset:512\n" \
"ds_read_b64_tr_b16 a[36:37], v8 offset:4096\n" \
"ds_read_b64_tr_b16 a[38:39], v8 offset:4608\n" \
"ds_read_b64_tr_b16 a[164:165], v9 offset:4096\n" \
"ds_read_b64_tr_b16 a[166:167], v9 offset:4608\n" \
"s_waitcnt lgkmcnt(0)\n" \
"s_barrier\n" \
"s_add_u32 m0, s52, 65536\n" \
"s_nop 0\n" \
"buffer_load_dwordx4 v1, s[40:43], s54 offen nt lds\n" \
"s_add_u32 s54, s54, s44\n" \
"s_add_u32 m0, s52, 67584\n" \
"s_nop 0\n" \
"buffer_load_dwordx4 v1, s[40:43], s54 offen nt lds\n" \
"s_add_u32 s54, s54, s44\n" \
"s_add_u32 m0, s52, 69632\n" \
"s_nop 0\n" \
"buffer_load_dwordx4 v1, s[40:43], s54 offen nt lds\n" \
"s_add_u32 s54, s54, s44\n" \
"s_add_u32 m0, s52, 71680\n" \
"s_nop 0\n" \
"buffer_load_dwordx4 v1, s[40:43], s54 offen nt lds\n" \
"s_add_u32 s54, s54, s44\n" \
"s_waitcnt vmcnt(28)\n" \
"s_barrier\n" \
"ds_read_b64_tr_b16 a[40:41], v8 offset:16384\n" \
"ds_read_b64_tr_b16 a[42:43], v8 offset:16896\n" \
"ds_read_b64_tr_b16 a[168:169], v9 offset:16384\n" \
"ds_read_b64_tr_b16 a[170:171], v9 offset:16896\n" \
"ds_read_b64_tr_b16 a[44:45], v8 offset:20480\n" \
"ds_read_b64_tr_b16 a[46:47], v8 offset:20992\n" \
"ds_read_b64_tr_b16 a[172:173], v9 offset:20480\n" \
"ds_read_b64_tr_b16 a[174:175], v9 offset:20992\n" \
"s_waitcnt lgkmcnt(0)\n" \
"s_barrier\n" \
"s_add_u32 m0, s52, 81920\n" \
"s_nop 0\n" \
"buffer_load_dwordx4 v1, s[40:43], s54 offen nt lds\n" \
"s_add_u32 s54, s54, s44\n" \
"s_add_u32 m0, s52, 83968\n" \
"s_nop 0\n" \
"buffer_load_dwordx4 v1, s[40:43], s54 offen nt lds\n" \
"s_add_u32 s54, s54, s44\n" \
"s_add_u32 m0, s52, 86016\n" \
"s_nop 0\n" \
"buffer_load_dwordx4 v1, s[40:43], s54 offen nt lds\n" \
"s_add_u32 s54, s54, s44\n" \
"s_add_u32 m0, s52, 88064\n" \
"s_nop 0\n" \
"buffer_load_dwordx4 v1, s[40:43], s54 offen nt lds\n" \
"s_add_u32 s54, s54, s44\n" \
"s_waitcnt vmcnt(28)\n" \
"s_barrier\n" \
"ds_read_b64_tr_b16 a[48:49], v8 offset:32768\n" \
"ds_read_b64_tr_b16 a[50:51], v8 offset:33280\n" \
"ds_read_b64_tr_b16 a[176:177], v9 offset:32768\n" \
"ds_read_b64_tr_b16 a[178:179], v9 offset:33280\n" \
"ds_read_b64_tr_b16 a[52:53], v8 offset:36864\n" \
"ds_read_b64_tr_b16 a[54:55], v8 offset:37376\n" \
"ds_read_b64_tr_b16 a[180:181], v9 offset:36864\n" \
"ds_read_b64_tr_b16 a[182:183], v9 offset:37376\n" \
"s_waitcnt lgkmcnt(0)\n" \
"s_barrier\n" \
"s_add_u32 m0, s52, 98304\n" \
"s_nop 0\n" \
"buffer_load_dwordx4 v1, s[40:43], s54 offen nt lds\n" \
"s_add_u32 s54, s54, s44\n" \
"s_add_u32 m0, s52, 100352\n" \
"s_nop 0\n" \
"buffer_load_dwordx4 v1, s[40:43], s54 offen nt lds\n" \
"s_add_u32 s54, s54, s44\n" \
"s_add_u32 m0, s52, 102400\n" \
"s_nop 0\n" \
"buffer_load_dwordx4 v1, s[40:43], s54 offen nt lds\n" \
"s_add_u32 s54, s54, s44\n" \
"s_add_u32 m0, s52, 104448\n" \
"s_nop 0\n" \
"buffer_load_dwordx4 v1, s[40:43], s54 offen nt lds\n" \
"s_add_u32 s54, s54, s44\n" \
"s_waitcnt vmcnt(28)\n" \
"s_barrier\n" \
"ds_read_b64_tr_b16 a[56:57], v8 offset:49152\n" \
"ds_read_b64_tr_b16 a[58:59], v8 offset:49664\n" \
"ds_read_b64_tr_b16 a[184:185], v9 offset:49152\n" \
"ds_read_b64_tr_b16 a[186:187], v9 offset:49664\n" \
"ds_read_b64_tr_b16 a[60:61], v8 offset:53248\n" \
"ds_read_b64_tr_b16 a[62:63], v8 offset:53760\n" \
"ds_read_b64_tr_b16 a[188:189], v9 offset:53248\n" \
"ds_read_b64_tr_b16 a[190:191], v9 offset:53760\n" \
"s_waitcnt lgkmcnt(0)\n" \
"s_barrier\n" \
"s_add_u32 m0, s52, 114688\n" \
"s_nop 0\n" \
"buffer_load_dwordx4 v1, s[40:43], s54 offen nt lds\n" \
"s_add_u32 s54, s54, s44\n" \
"s_add_u32 m0, s52, 116736\n" \
"s_nop 0\n" \
"buffer_load_dwordx4 v1, s[40:43], s54 offen nt lds\n" \
"s_add_u32 s54, s54, s44\n" \
"s_add_u32 m0, s52, 118784\n" \
"s_nop 0\n" \
"buffer_load_dwordx4 v1, s[40:43], s54 offen nt lds\n" \
"s_add_u32 s54, s54, s44\n" \
"s_add_u32 m0, s52, 120832\n" \
"s_nop 0\n" \
"buffer_load_dwordx4 v1, s[40:43], s54 offen nt lds\n" \
"s_add_u32 s54, s54, s44\n" \
"s_waitcnt vmcnt(28)\n" \
"s_barrier\n" \
"ds_read_b64_tr_b16 a[64:65], v2 offset:0\n" \
"ds_read_b64_tr_b16 a[66:67], v2 offset:512\n" \
"ds_read_b64_tr_b16 a[192:193], v3 offset:0\n" \
"ds_read_b64_tr_b16 a[194:195], v3 offset:512\n" \
"ds_read_b64_tr_b16 a[68:69], v2 offset:4096\n" \
"ds_read_b64_tr_b16 a[70:71], v2 offset:4608\n" \
"ds_read_b64_tr_b16 a[196:197], v3 offset:4096\n" \
"ds_read_b64_tr_b16 a[198:199], v3 offset:4608\n" \
"s_waitcnt vmcnt(24)\n" \
"s_barrier\n" \
"ds_read_b64_tr_b16 a[72:73], v2 offset:16384\n" \
"ds_read_b64_tr_b16 a[74:75], v2 offset:16896\n" \
"ds_read_b64_tr_b16 a[200:201], v3 offset:16384\n" \
"ds_read_b64_tr_b16 a[202:203], v3 offset:16896\n" \
"ds_read_b64_tr_b16 a[76:77], v2 offset:20480\n" \
"ds_read_b64_tr_b16 a[78:79], v2 offset:20992\n" \
"ds_read_b64_tr_b16 a[204:205], v3 offset:20480\n" \
"ds_read_b64_tr_b16 a[206:207], v3 offset:20992\n" \
"s_waitcnt vmcnt(20)\n" \
"s_barrier\n" \
"ds_read_b64_tr_b16 a[80:81], v2 offset:32768\n" \
"ds_read_b64_tr_b16 a[82:83], v2 offset:33280\n" \
"ds_read_b64_tr_b16 a[208:209], v3 offset:32768\n" \
"ds_read_b64_tr_b16 a[210:211], v3 offset:33280\n" \
"ds_read_b64_tr_b16 a[84:85], v2 offset:36864\n" \
"ds_read_b64_tr_b16 a[86:87], v2 offset:37376\n" \
"ds_read_b64_tr_b16 a[212:213], v3 offset:36864\n" \
"ds_read_b64_tr_b16 a[214:215], v3 offset:37376\n" \
"s_waitcnt vmcnt(16)\n" \
"s_barrier\n" \
"ds_read_b64_tr_b16 a[88:89], v2 offset:49152\n" \
"ds_read_b64_tr_b16 a[90:91], v2 offset:49664\n" \
"ds_read_b64_tr_b16 a[216:217], v3 offset:49152\n" \
"ds_read_b64_tr_b16 a[218:219], v3 offset:49664\n" \
"ds_read_b64_tr_b16 a[92:93], v2 offset:53248\n" \
"ds_read_b64_tr_b16 a[94:95], v2 offset:53760\n" \
"ds_read_b64_tr_b16 a[220:221], v3 offset:53248\n" \
"ds_read_b64_tr_b16 a[222:223], v3 offset:53760\n" \
"s_waitcnt vmcnt(12)\n" \
"s_barrier\n" \
"ds_read_b64_tr_b16 a[96:97], v8 offset:0\n" \
"ds_read_b64_tr_b16 a[98:99], v8 offset:512\n" \
"ds_read_b64_tr_b16 a[224:225], v9 offset:0\n" \
"ds_read_b64_tr_b16 a[226:227], v9 offset:512\n" \
"ds_read_b64_tr_b16 a[100:101], v8 offset:4096\n" \
"ds_read_b64_tr_b16 a[102:103], v8 offset:4608\n" \
"ds_read_b64_tr_b16 a[228:229], v9 offset:4096\n" \
"ds_read_b64_tr_b16 a[230:231], v9 offset:4608\n" \
"s_waitcnt vmcnt(8)\n" \
"s_barrier\n" \
"ds_read_b64_tr_b16 a[104:105], v8 offset:16384\n" \
"ds_read_b64_tr_b16 a[106:107], v8 offset:16896\n" \
"ds_read_b64_tr_b16 a[232:233], v9 offset:16384\n" \
"ds_read_b64_tr_b16 a[234:235], v9 offset:16896\n" \
"ds_read_b64_tr_b16 a[108:109], v8 offset:20480\n" \
"ds_read_b64_tr_b16 a[110:111], v8 offset:20992\n" \
"ds_read_b64_tr_b16 a[236:237], v9 offset:20480\n" \
"ds_read_b64_tr_b16 a[238:239], v9 offset:20992\n" \
"s_waitcnt vmcnt(4)\n" \
"s_barrier\n" \
"ds_read_b64_tr_b16 a[112:113], v8 offset:32768\n" \
"ds_read_b64_tr_b16 a[114:115], v8 offset:33280\n" \
"ds_read_b64_tr_b16 a[240:241], v9 offset:32768\n" \
"ds_read_b64_tr_b16 a[242:243], v9 offset:33280\n" \
"ds_read_b64_tr_b16 a[116:117], v8 offset:36864\n" \
"ds_read_b64_tr_b16 a[118:119], v8 offset:37376\n" \
"ds_read_b64_tr_b16 a[244:245], v9 offset:36864\n" \
"ds_read_b64_tr_b16 a[246:247], v9 offset:37376\n" \
"s_waitcnt vmcnt(0)\n" \
"s_barrier\n" \
"ds_read_b64_tr_b16 a[120:121], v8 offset:49152\n" \
"ds_read_b64_tr_b16 a[122:123], v8 offset:49664\n" \
"ds_read_b64_tr_b16 a[248:249], v9 offset:49152\n" \
"ds_read_b64_tr_b16 a[250:251], v9 offset:49664\n" \
"ds_read_b64_tr_b16 a[124:125], v8 offset:53248\n" \
"ds_read_b64_tr_b16 a[126:127], v8 offset:53760\n" \
"ds_read_b64_tr_b16 a[252:253], v9 offset:53248\n" \
"ds_read_b64_tr_b16 a[254:255], v9 offset:53760\n" \
"s_waitcnt lgkmcnt(0)\n" \
"s_barrier\n" \
"v_mov_b32 v14, 4096\n" \
"s_mov_b32 s59, s46\n" \
"s_add_u32 s63, s59, 0\n" \
"s_min_u32 s63, s63, 63\n" \
"s_lshr_b32 s63, s63, 3\n" \
"s_lshl_b32 s63, s63, 7\n" \
"s_branch .Lp1g1_l0_%=\n" \
".Lp1g1_l0k_%=:\n" \
"s_add_u32 s63, s59, 1\n" \
"s_min_u32 s63, s63, 63\n" \
"s_lshr_b32 s63, s63, 3\n" \
"s_lshl_b32 s63, s63, 7\n" \
"s_branch .Lp1g1_l1_%=\n" \
".Lp1g1_l1k_%=:\n" \
"s_lshl_b32 s55, s48, 13\n" \
"s_lshl_b32 s56, s46, 16\n" \
"s_lshl_b32 s60, s47, 16\n" \
"s_sub_u32 s60, s60, 0x10000\n" \
"s_mul_i32 s57, s46, s49\n" \
"s_sub_u32 s57, s57, s49\n" \
"s_add_u32 s58, s57, s50\n" \
"v_mov_b32 v7, v5\n" \
"v_mov_b32 v5, 0x7ffffff0\n" \
"v_mov_b32 v10, v0\n" \
"v_mov_b32 v11, v6\n" \
"v_mov_b32 v6, 0x7ffffff0\n" \
"s_mov_b32 s59, s46\n" \
"s_min_u32 s61, s56, s60\n" \
"s_add_u32 m0, s55, 0\n" \
"s_nop 0\n" \
"buffer_load_dwordx4 v4, s[36:39], s61 offen offset:0 sc1 lds\n" \
"buffer_load_dwordx4 v4, s[36:39], s61 offen offset:1024 sc1 lds\n" \
"buffer_load_dwordx4 v4, s[36:39], s61 offen offset:2048 sc1 lds\n" \
"buffer_load_dwordx4 v4, s[36:39], s61 offen offset:3072 sc1 lds\n" \
"s_add_u32 m0, s55, 4096\n" \
"s_add_u32 s61, s61, 4096\n" \
"buffer_load_dwordx4 v4, s[36:39], s61 offen offset:0 sc1 lds\n" \
"buffer_load_dwordx4 v4, s[36:39], s61 offen offset:1024 sc1 lds\n" \
"buffer_load_dwordx4 v4, s[36:39], s61 offen offset:2048 sc1 lds\n" \
"buffer_load_dwordx4 v4, s[36:39], s61 offen offset:3072 sc1 lds\n" \
"s_min_u32 s61, s56, s60\n" \
"s_add_u32 s61, s61, 16384\n" \
"s_add_u32 s56, s56, 0x10000\n" \
"s_add_u32 m0, s55, 32768\n" \
"s_nop 0\n" \
"buffer_load_dwordx4 v4, s[36:39], s61 offen offset:0 sc1 lds\n" \
"buffer_load_dwordx4 v4, s[36:39], s61 offen offset:1024 sc1 lds\n" \
"buffer_load_dwordx4 v4, s[36:39], s61 offen offset:2048 sc1 lds\n" \
"buffer_load_dwordx4 v4, s[36:39], s61 offen offset:3072 sc1 lds\n" \
"s_add_u32 m0, s55, 36864\n" \
"s_add_u32 s61, s61, 4096\n" \
"buffer_load_dwordx4 v4, s[36:39], s61 offen offset:0 sc1 lds\n" \
"buffer_load_dwordx4 v4, s[36:39], s61 offen offset:1024 sc1 lds\n" \
"buffer_load_dwordx4 v4, s[36:39], s61 offen offset:2048 sc1 lds\n" \
"buffer_load_dwordx4 v4, s[36:39], s61 offen offset:3072 sc1 lds\n" \
"s_mov_b32 s70, 0\n" \
"s_mov_b32 s72, 0\n" \
"s_waitcnt vmcnt(0) lgkmcnt(0)\n" \
"s_barrier\n" \
"v_add_u32 v12, 0x10000, v10\n" \
"ds_read_b128 v[16:19], v10 offset:0\n" \
"ds_read_b128 v[20:23], v10 offset:16384\n" \
"ds_read_b128 v[24:27], v10 offset:1024\n" \
"ds_read_b128 v[28:31], v10 offset:17408\n" \
"ds_read_b128 v[32:35], v10 offset:2048\n" \
"ds_read_b128 v[36:39], v10 offset:18432\n" \
"s_waitcnt lgkmcnt(4)\n" \
"s_nop 7\n" \
"s_nop 7\n" \
".p2align 6\n" \
".Lp1g1_loop_%=:\n" \
"v_mfma_f32_16x16x32_bf16 v[48:51], a[0:3], v[16:19], 0\n" \
"s_min_u32 s61, s56, s60\n" \
"s_add_u32 m0, s55, 65536\n" \
"s_nop 0\n" \
"buffer_load_dwordx4 v4, s[36:39], s61 offen offset:0 sc1 lds\n" \
"v_add_u32_e64 v13, -1, s59\n" \
"v_mfma_f32_16x16x32_bf16 v[52:55], a[0:3], v[20:23], 0\n" \
"ds_read_b128 v[40:43], v10 offset:3072\n" \
"s_nop 0\n" \
"v_mfma_f32_16x16x32_bf16 v[56:59], a[128:131], v[16:19], 0\n" \
"ds_read_b128 v[44:47], v10 offset:19456\n" \
"s_nop 0\n" \
"v_mfma_f32_16x16x32_bf16 v[60:63], a[128:131], v[20:23], 0\n" \
"s_waitcnt lgkmcnt(4)\n" \
"v_mul_f32 v64, 0xbfb8aa3b, v64\n" \
"v_mul_f32 v65, 0xbfb8aa3b, v65\n" \
"v_mfma_f32_16x16x32_bf16 v[48:51], a[4:7], v[24:27], v[48:51]\n" \
"buffer_load_dwordx4 v4, s[36:39], s61 offen offset:1024 sc1 lds\n" \
"v_mfma_f32_16x16x32_bf16 v[52:55], a[4:7], v[28:31], v[52:55]\n" \
"ds_read_b128 v[16:19], v10 offset:4096\n" \
"v_mul_f32 v66, 0xbfb8aa3b, v66\n" \
"v_mfma_f32_16x16x32_bf16 v[56:59], a[132:135], v[24:27], v[56:59]\n" \
"ds_read_b128 v[20:23], v10 offset:20480\n" \
"v_mul_f32 v67, 0xbfb8aa3b, v67\n" \
"v_mfma_f32_16x16x32_bf16 v[60:63], a[132:135], v[28:31], v[60:63]\n" \
"s_waitcnt lgkmcnt(4)\n" \
"v_exp_f32_e64 v64, v64\n" \
"v_mfma_f32_16x16x32_bf16 v[48:51], a[8:11], v[32:35], v[48:51]\n" \
"s_add_u32 s63, s59, 2\n" \
"s_min_u32 s63, s63, 63\n" \
"v_mfma_f32_16x16x32_bf16 v[52:55], a[8:11], v[36:39], v[52:55]\n" \
"ds_read_b128 v[24:27], v10 offset:5120\n" \
"s_lshr_b32 s63, s63, 3\n" \
"s_lshl_b32 s63, s63, 7\n" \
"v_mfma_f32_16x16x32_bf16 v[56:59], a[136:139], v[32:35], v[56:59]\n" \
"ds_read_b128 v[28:31], v10 offset:21504\n" \
"v_mfma_f32_16x16x32_bf16 v[60:63], a[136:139], v[36:39], v[60:63]\n" \
"s_waitcnt lgkmcnt(4)\n" \
"v_exp_f32 v65, v65\n" \
"v_mfma_f32_16x16x32_bf16 v[48:51], a[12:15], v[40:43], v[48:51]\n" \
"buffer_load_dwordx4 v4, s[36:39], s61 offen offset:2048 sc1 lds\n" \
"v_mfma_f32_16x16x32_bf16 v[52:55], a[12:15], v[44:47], v[52:55]\n" \
"ds_read_b128 v[32:35], v10 offset:6144\n" \
"v_mfma_f32_16x16x32_bf16 v[56:59], a[140:143], v[40:43], v[56:59]\n" \
"ds_read_b128 v[36:39], v10 offset:22528\n" \
"v_mfma_f32_16x16x32_bf16 v[60:63], a[140:143], v[44:47], v[60:63]\n" \
"s_waitcnt lgkmcnt(4)\n" \
"v_exp_f32 v66, v66\n" \
"v_mfma_f32_16x16x32_bf16 v[48:51], a[16:19], v[16:19], v[48:51]\n" \
"buffer_load_dwordx4 v4, s[36:39], s61 offen offset:3072 sc1 lds\n" \
"v_mfma_f32_16x16x32_bf16 v[52:55], a[16:19], v[20:23], v[52:55]\n" \
"ds_read_b128 v[40:43], v10 offset:7168\n" \
"v_mfma_f32_16x16x32_bf16 v[56:59], a[144:147], v[16:19], v[56:59]\n" \
"ds_read_b128 v[44:47], v10 offset:23552\n" \
"v_mfma_f32_16x16x32_bf16 v[60:63], a[144:147], v[20:23], v[60:63]\n" \
"s_waitcnt lgkmcnt(4)\n" \
"v_exp_f32 v67, v67\n" \
"v_mfma_f32_16x16x32_bf16 v[48:51], a[20:23], v[24:27], v[48:51]\n" \
"buffer_load_dword v15, v14, s[36:39], s63 offen sc1\n" \
"v_mfma_f32_16x16x32_bf16 v[52:55], a[20:23], v[28:31], v[52:55]\n" \
"ds_read_b128 v[16:19], v10 offset:8192\n" \
"v_add_f32_e64 v64, 1.0, v64\n" \
"v_mfma_f32_16x16x32_bf16 v[56:59], a[148:151], v[24:27], v[56:59]\n" \
"ds_read_b128 v[20:23], v10 offset:24576\n" \
"v_add_f32_e64 v65, 1.0, v65\n" \
"v_mfma_f32_16x16x32_bf16 v[60:63], a[148:151], v[28:31], v[60:63]\n" \
"s_waitcnt lgkmcnt(4)\n" \
"v_add_f32 v66, 1.0, v66\n" \
"v_add_f32_e64 v67, 1.0, v67\n" \
"v_mfma_f32_16x16x32_bf16 v[48:51], a[24:27], v[32:35], v[48:51]\n" \
"s_add_u32 m0, s55, 69632\n" \
"s_add_u32 s61, s61, 4096\n" \
"buffer_load_dwordx4 v4, s[36:39], s61 offen offset:0 sc1 lds\n" \
"v_mfma_f32_16x16x32_bf16 v[52:55], a[24:27], v[36:39], v[52:55]\n" \
"ds_read_b128 v[24:27], v10 offset:9216\n" \
"v_mfma_f32_16x16x32_bf16 v[56:59], a[152:155], v[32:35], v[56:59]\n" \
"ds_read_b128 v[28:31], v10 offset:25600\n" \
"v_mfma_f32_16x16x32_bf16 v[60:63], a[152:155], v[36:39], v[60:63]\n" \
"s_waitcnt lgkmcnt(4)\n" \
"v_rcp_f32 v64, v64\n" \
"v_mfma_f32_16x16x32_bf16 v[48:51], a[28:31], v[40:43], v[48:51]\n" \
"buffer_load_dwordx4 v4, s[36:39], s61 offen offset:1024 sc1 lds\n" \
"v_mfma_f32_16x16x32_bf16 v[52:55], a[28:31], v[44:47], v[52:55]\n" \
"ds_read_b128 v[32:35], v10 offset:10240\n" \
"v_mfma_f32_16x16x32_bf16 v[56:59], a[156:159], v[40:43], v[56:59]\n" \
"ds_read_b128 v[36:39], v10 offset:26624\n" \
"v_mfma_f32_16x16x32_bf16 v[60:63], a[156:159], v[44:47], v[60:63]\n" \
"s_waitcnt lgkmcnt(4)\n" \
"v_rcp_f32 v65, v65\n" \
"v_mfma_f32_16x16x32_bf16 v[48:51], a[32:35], v[16:19], v[48:51]\n" \
"v_rcp_f32_e64 v66, v66\n" \
"v_mfma_f32_16x16x32_bf16 v[52:55], a[32:35], v[20:23], v[52:55]\n" \
"ds_read_b128 v[40:43], v10 offset:11264\n" \
"v_mfma_f32_16x16x32_bf16 v[56:59], a[160:163], v[16:19], v[56:59]\n" \
"ds_read_b128 v[44:47], v10 offset:27648\n" \
"v_mfma_f32_16x16x32_bf16 v[60:63], a[160:163], v[20:23], v[60:63]\n" \
"s_waitcnt lgkmcnt(4)\n" \
"v_rcp_f32 v67, v67\n" \
"v_mfma_f32_16x16x32_bf16 v[48:51], a[36:39], v[24:27], v[48:51]\n" \
"buffer_load_dwordx4 v4, s[36:39], s61 offen offset:2048 sc1 lds\n" \
"v_mfma_f32_16x16x32_bf16 v[52:55], a[36:39], v[28:31], v[52:55]\n" \
"ds_read_b128 v[16:19], v10 offset:12288\n" \
"v_mul_f32_e64 v64, v64, v72\n" \
"v_mfma_f32_16x16x32_bf16 v[56:59], a[164:167], v[24:27], v[56:59]\n" \
"ds_read_b128 v[20:23], v10 offset:28672\n" \
"v_mul_f32_e64 v65, v65, v73\n" \
"v_mfma_f32_16x16x32_bf16 v[60:63], a[164:167], v[28:31], v[60:63]\n" \
"s_waitcnt lgkmcnt(4)\n" \
"v_mul_f32 v66, v66, v74\n" \
"v_mul_f32_e64 v67, v67, v75\n" \
"v_mfma_f32_16x16x32_bf16 v[48:51], a[40:43], v[32:35], v[48:51]\n" \
"buffer_load_dwordx4 v4, s[36:39], s61 offen offset:3072 sc1 lds\n" \
"v_mfma_f32_16x16x32_bf16 v[52:55], a[40:43], v[36:39], v[52:55]\n" \
"ds_read_b128 v[24:27], v10 offset:13312\n" \
"v_cvt_pk_bf16_f32 v80, v64, v65\n" \
"v_mfma_f32_16x16x32_bf16 v[56:59], a[168:171], v[32:35], v[56:59]\n" \
"ds_read_b128 v[28:31], v10 offset:29696\n" \
"v_cvt_pk_bf16_f32 v81, v66, v67\n" \
"v_mfma_f32_16x16x32_bf16 v[60:63], a[168:171], v[36:39], v[60:63]\n" \
"s_waitcnt lgkmcnt(4)\n" \
"v_mul_f32 v68, 0xbfb8aa3b, v68\n" \
"v_mul_f32 v69, 0xbfb8aa3b, v69\n" \
"v_mfma_f32_16x16x32_bf16 v[48:51], a[44:47], v[40:43], v[48:51]\n" \
"v_mul_f32 v70, 0xbfb8aa3b, v70\n" \
"v_mul_f32 v71, 0xbfb8aa3b, v71\n" \
"v_mfma_f32_16x16x32_bf16 v[52:55], a[44:47], v[44:47], v[52:55]\n" \
"ds_read_b128 v[32:35], v10 offset:14336\n" \
"v_mfma_f32_16x16x32_bf16 v[56:59], a[172:175], v[40:43], v[56:59]\n" \
"ds_read_b128 v[36:39], v10 offset:30720\n" \
"v_mfma_f32_16x16x32_bf16 v[60:63], a[172:175], v[44:47], v[60:63]\n" \
"s_waitcnt lgkmcnt(4)\n" \
"v_exp_f32_e64 v68, v68\n" \
"v_mfma_f32_16x16x32_bf16 v[48:51], a[48:51], v[16:19], v[48:51]\n" \
"v_exp_f32_e64 v69, v69\n" \
"v_mfma_f32_16x16x32_bf16 v[52:55], a[48:51], v[20:23], v[52:55]\n" \
"ds_read_b128 v[40:43], v10 offset:15360\n" \
"v_mfma_f32_16x16x32_bf16 v[56:59], a[176:179], v[16:19], v[56:59]\n" \
"ds_read_b128 v[44:47], v10 offset:31744\n" \
"v_mfma_f32_16x16x32_bf16 v[60:63], a[176:179], v[20:23], v[60:63]\n" \
"s_waitcnt lgkmcnt(4)\n" \
"v_exp_f32 v70, v70\n" \
"v_mfma_f32_16x16x32_bf16 v[48:51], a[52:55], v[24:27], v[48:51]\n" \
"s_waitcnt vmcnt(9)\n" \
"s_barrier\n" \
"v_mfma_f32_16x16x32_bf16 v[52:55], a[52:55], v[28:31], v[52:55]\n" \
"ds_read_b128 v[16:19], v10 offset:32768\n" \
"v_mfma_f32_16x16x32_bf16 v[56:59], a[180:183], v[24:27], v[56:59]\n" \
"ds_read_b128 v[20:23], v10 offset:49152\n" \
"v_mfma_f32_16x16x32_bf16 v[60:63], a[180:183], v[28:31], v[60:63]\n" \
"s_waitcnt lgkmcnt(4)\n" \
"buffer_store_dword v13, v6, s[36:39], 0 offen sc1\n" \
"v_exp_f32 v71, v71\n" \
"v_mfma_f32_16x16x32_bf16 v[48:51], a[56:59], v[32:35], v[48:51]\n" \
"v_add_f32 v68, 1.0, v68\n" \
"v_add_f32 v69, 1.0, v69\n" \
"v_mfma_f32_16x16x32_bf16 v[52:55], a[56:59], v[36:39], v[52:55]\n" \
"ds_read_b128 v[24:27], v10 offset:33792\n" \
"v_add_f32_e64 v70, 1.0, v70\n" \
"v_mfma_f32_16x16x32_bf16 v[56:59], a[184:187], v[32:35], v[56:59]\n" \
"ds_read_b128 v[28:31], v10 offset:50176\n" \
"v_add_f32_e64 v71, 1.0, v71\n" \
"v_mfma_f32_16x16x32_bf16 v[60:63], a[184:187], v[36:39], v[60:63]\n" \
"s_waitcnt lgkmcnt(4)\n" \
"v_rcp_f32 v68, v68\n" \
"v_mfma_f32_16x16x32_bf16 v[48:51], a[60:63], v[40:43], v[48:51]\n" \
"v_rcp_f32_e64 v69, v69\n" \
"v_mfma_f32_16x16x32_bf16 v[52:55], a[60:63], v[44:47], v[52:55]\n" \
"ds_read_b128 v[32:35], v10 offset:34816\n" \
"v_mfma_f32_16x16x32_bf16 v[56:59], a[188:191], v[40:43], v[56:59]\n" \
"ds_read_b128 v[36:39], v10 offset:51200\n" \
"v_mfma_f32_16x16x32_bf16 v[60:63], a[188:191], v[44:47], v[60:63]\n" \
"s_waitcnt lgkmcnt(4)\n" \
"v_rcp_f32 v70, v70\n" \
"v_mfma_f32_16x16x32_bf16 v[48:51], a[64:67], v[16:19], v[48:51]\n" \
"s_min_u32 s61, s56, s60\n" \
"s_add_u32 s61, s61, 16384\n" \
"s_add_u32 s56, s56, 0x10000\n" \
"s_add_u32 m0, s55, 98304\n" \
"s_nop 0\n" \
"buffer_load_dwordx4 v4, s[36:39], s61 offen offset:0 sc1 lds\n" \
"v_mfma_f32_16x16x32_bf16 v[52:55], a[64:67], v[20:23], v[52:55]\n" \
"ds_read_b128 v[40:43], v10 offset:35840\n" \
"v_mfma_f32_16x16x32_bf16 v[56:59], a[192:195], v[16:19], v[56:59]\n" \
"ds_read_b128 v[44:47], v10 offset:52224\n" \
"v_mfma_f32_16x16x32_bf16 v[60:63], a[192:195], v[20:23], v[60:63]\n" \
"s_waitcnt lgkmcnt(4)\n" \
"v_rcp_f32 v71, v71\n" \
"v_mfma_f32_16x16x32_bf16 v[48:51], a[68:71], v[24:27], v[48:51]\n" \
"buffer_load_dwordx4 v4, s[36:39], s61 offen offset:1024 sc1 lds\n" \
"v_mfma_f32_16x16x32_bf16 v[52:55], a[68:71], v[28:31], v[52:55]\n" \
"ds_read_b128 v[16:19], v10 offset:36864\n" \
"v_mul_f32_e64 v68, v68, v76\n" \
"v_mfma_f32_16x16x32_bf16 v[56:59], a[196:199], v[24:27], v[56:59]\n" \
"ds_read_b128 v[20:23], v10 offset:53248\n" \
"v_mul_f32_e64 v69, v69, v77\n" \
"v_mfma_f32_16x16x32_bf16 v[60:63], a[196:199], v[28:31], v[60:63]\n" \
"s_waitcnt lgkmcnt(4)\n" \
"v_mul_f32 v70, v70, v78\n" \
"v_mul_f32_e64 v71, v71, v79\n" \
"v_mfma_f32_16x16x32_bf16 v[48:51], a[72:75], v[32:35], v[48:51]\n" \
"v_cvt_pk_bf16_f32 v82, v68, v69\n" \
"v_cvt_pk_bf16_f32 v83, v70, v71\n" \
"v_mfma_f32_16x16x32_bf16 v[52:55], a[72:75], v[36:39], v[52:55]\n" \
"ds_read_b128 v[24:27], v10 offset:37888\n" \
"s_nop 0\n" \
"v_mfma_f32_16x16x32_bf16 v[56:59], a[200:203], v[32:35], v[56:59]\n" \
"ds_read_b128 v[28:31], v10 offset:54272\n" \
"s_nop 0\n" \
"v_mfma_f32_16x16x32_bf16 v[60:63], a[200:203], v[36:39], v[60:63]\n" \
"s_waitcnt lgkmcnt(4)\n" \
"v_permlane16_swap_b32 v80, v82\n" \
"v_permlane16_swap_b32 v81, v83\n" \
"v_mfma_f32_16x16x32_bf16 v[48:51], a[76:79], v[40:43], v[48:51]\n" \
"buffer_load_dwordx4 v4, s[36:39], s61 offen offset:2048 sc1 lds\n" \
"v_mfma_f32_16x16x32_bf16 v[52:55], a[76:79], v[44:47], v[52:55]\n" \
"ds_read_b128 v[32:35], v10 offset:38912\n" \
"buffer_store_dwordx4 v[80:83], v5, s[36:39], s57 offen sc1\n" \
"v_mfma_f32_16x16x32_bf16 v[56:59], a[204:207], v[40:43], v[56:59]\n" \
"ds_read_b128 v[36:39], v10 offset:55296\n" \
"v_mfma_f32_16x16x32_bf16 v[60:63], a[204:207], v[44:47], v[60:63]\n" \
"s_waitcnt lgkmcnt(4)\n" \
"v_mfma_f32_16x16x32_bf16 v[48:51], a[80:83], v[16:19], v[48:51]\n" \
"buffer_load_dwordx4 v4, s[36:39], s61 offen offset:3072 sc1 lds\n" \
"v_mfma_f32_16x16x32_bf16 v[52:55], a[80:83], v[20:23], v[52:55]\n" \
"ds_read_b128 v[40:43], v10 offset:39936\n" \
"v_mfma_f32_16x16x32_bf16 v[56:59], a[208:211], v[16:19], v[56:59]\n" \
"ds_read_b128 v[44:47], v10 offset:56320\n" \
"v_mfma_f32_16x16x32_bf16 v[60:63], a[208:211], v[20:23], v[60:63]\n" \
"s_waitcnt lgkmcnt(4)\n" \
"v_mfma_f32_16x16x32_bf16 v[48:51], a[84:87], v[24:27], v[48:51]\n" \
"v_mfma_f32_16x16x32_bf16 v[52:55], a[84:87], v[28:31], v[52:55]\n" \
"ds_read_b128 v[16:19], v10 offset:40960\n" \
"v_mfma_f32_16x16x32_bf16 v[56:59], a[212:215], v[24:27], v[56:59]\n" \
"ds_read_b128 v[20:23], v10 offset:57344\n" \
"v_mfma_f32_16x16x32_bf16 v[60:63], a[212:215], v[28:31], v[60:63]\n" \
"s_waitcnt lgkmcnt(4)\n" \
"v_mfma_f32_16x16x32_bf16 v[48:51], a[88:91], v[32:35], v[48:51]\n" \
"s_add_u32 m0, s55, 102400\n" \
"s_add_u32 s61, s61, 4096\n" \
"buffer_load_dwordx4 v4, s[36:39], s61 offen offset:0 sc1 lds\n" \
"v_mfma_f32_16x16x32_bf16 v[52:55], a[88:91], v[36:39], v[52:55]\n" \
"ds_read_b128 v[24:27], v10 offset:41984\n" \
"v_mfma_f32_16x16x32_bf16 v[56:59], a[216:219], v[32:35], v[56:59]\n" \
"ds_read_b128 v[28:31], v10 offset:58368\n" \
"v_mfma_f32_16x16x32_bf16 v[60:63], a[216:219], v[36:39], v[60:63]\n" \
"s_waitcnt lgkmcnt(4)\n" \
"v_mfma_f32_16x16x32_bf16 v[48:51], a[92:95], v[40:43], v[48:51]\n" \
"buffer_load_dwordx4 v4, s[36:39], s61 offen offset:1024 sc1 lds\n" \
"v_mfma_f32_16x16x32_bf16 v[52:55], a[92:95], v[44:47], v[52:55]\n" \
"ds_read_b128 v[32:35], v10 offset:43008\n" \
"v_mfma_f32_16x16x32_bf16 v[56:59], a[220:223], v[40:43], v[56:59]\n" \
"ds_read_b128 v[36:39], v10 offset:59392\n" \
"v_mfma_f32_16x16x32_bf16 v[60:63], a[220:223], v[44:47], v[60:63]\n" \
"s_waitcnt lgkmcnt(4)\n" \
"v_mfma_f32_16x16x32_bf16 v[48:51], a[96:99], v[16:19], v[48:51]\n" \
"v_mfma_f32_16x16x32_bf16 v[52:55], a[96:99], v[20:23], v[52:55]\n" \
"ds_read_b128 v[40:43], v10 offset:44032\n" \
"v_mfma_f32_16x16x32_bf16 v[56:59], a[224:227], v[16:19], v[56:59]\n" \
"ds_read_b128 v[44:47], v10 offset:60416\n" \
"v_mfma_f32_16x16x32_bf16 v[60:63], a[224:227], v[20:23], v[60:63]\n" \
"s_waitcnt lgkmcnt(4)\n" \
"v_mfma_f32_16x16x32_bf16 v[48:51], a[100:103], v[24:27], v[48:51]\n" \
"buffer_load_dwordx4 v4, s[36:39], s61 offen offset:2048 sc1 lds\n" \
"v_mfma_f32_16x16x32_bf16 v[52:55], a[100:103], v[28:31], v[52:55]\n" \
"ds_read_b128 v[16:19], v10 offset:45056\n" \
"v_mfma_f32_16x16x32_bf16 v[56:59], a[228:231], v[24:27], v[56:59]\n" \
"ds_read_b128 v[20:23], v10 offset:61440\n" \
"v_mfma_f32_16x16x32_bf16 v[60:63], a[228:231], v[28:31], v[60:63]\n" \
"s_waitcnt lgkmcnt(4)\n" \
"v_mfma_f32_16x16x32_bf16 v[48:51], a[104:107], v[32:35], v[48:51]\n" \
"buffer_load_dwordx4 v4, s[36:39], s61 offen offset:3072 sc1 lds\n" \
"v_mfma_f32_16x16x32_bf16 v[52:55], a[104:107], v[36:39], v[52:55]\n" \
"ds_read_b128 v[24:27], v10 offset:46080\n" \
"v_mfma_f32_16x16x32_bf16 v[56:59], a[232:235], v[32:35], v[56:59]\n" \
"ds_read_b128 v[28:31], v10 offset:62464\n" \
"v_mfma_f32_16x16x32_bf16 v[60:63], a[232:235], v[36:39], v[60:63]\n" \
"s_waitcnt lgkmcnt(4)\n" \
"v_mfma_f32_16x16x32_bf16 v[48:51], a[108:111], v[40:43], v[48:51]\n" \
"v_mfma_f32_16x16x32_bf16 v[52:55], a[108:111], v[44:47], v[52:55]\n" \
"ds_read_b128 v[32:35], v10 offset:47104\n" \
"v_mfma_f32_16x16x32_bf16 v[56:59], a[236:239], v[40:43], v[56:59]\n" \
"ds_read_b128 v[36:39], v10 offset:63488\n" \
"v_mfma_f32_16x16x32_bf16 v[60:63], a[236:239], v[44:47], v[60:63]\n" \
"s_waitcnt lgkmcnt(4)\n" \
"v_mfma_f32_16x16x32_bf16 v[48:51], a[112:115], v[16:19], v[48:51]\n" \
"v_mfma_f32_16x16x32_bf16 v[52:55], a[112:115], v[20:23], v[52:55]\n" \
"ds_read_b128 v[40:43], v10 offset:48128\n" \
"v_mfma_f32_16x16x32_bf16 v[56:59], a[240:243], v[16:19], v[56:59]\n" \
"ds_read_b128 v[44:47], v10 offset:64512\n" \
"v_mfma_f32_16x16x32_bf16 v[60:63], a[240:243], v[20:23], v[60:63]\n" \
"s_waitcnt lgkmcnt(4)\n" \
"v_mfma_f32_16x16x32_bf16 v[48:51], a[116:119], v[24:27], v[48:51]\n" \
"s_waitcnt vmcnt(10)\n" \
"s_barrier\n" \
"v_mfma_f32_16x16x32_bf16 v[52:55], a[116:119], v[28:31], v[52:55]\n" \
"ds_read_b128 v[16:19], v12 offset:0\n" \
"v_mfma_f32_16x16x32_bf16 v[56:59], a[244:247], v[24:27], v[56:59]\n" \
"ds_read_b128 v[20:23], v12 offset:16384\n" \
"v_mfma_f32_16x16x32_bf16 v[60:63], a[244:247], v[28:31], v[60:63]\n" \
"s_waitcnt lgkmcnt(4)\n" \
"v_mfma_f32_16x16x32_bf16 v[48:51], a[120:123], v[32:35], v[48:51]\n" \
"v_cmp_gt_u32_e64 vcc, 16, v15\n" \
"v_mfma_f32_16x16x32_bf16 v[52:55], a[120:123], v[36:39], v[52:55]\n" \
"ds_read_b128 v[24:27], v12 offset:1024\n" \
"v_mfma_f32_16x16x32_bf16 v[56:59], a[248:251], v[32:35], v[56:59]\n" \
"ds_read_b128 v[28:31], v12 offset:17408\n" \
"v_mfma_f32_16x16x32_bf16 v[60:63], a[248:251], v[36:39], v[60:63]\n" \
"s_waitcnt lgkmcnt(4)\n" \
"v_mfma_f32_16x16x32_bf16 v[48:51], a[124:127], v[40:43], v[48:51]\n" \
"s_cbranch_vccnz .Lp1g1_m0_%=\n" \
".Lp1g1_m0k_%=:\n" \
"v_mfma_f32_16x16x32_bf16 v[52:55], a[124:127], v[44:47], v[52:55]\n" \
"ds_read_b128 v[32:35], v12 offset:2048\n" \
"v_mfma_f32_16x16x32_bf16 v[56:59], a[252:255], v[40:43], v[56:59]\n" \
"ds_read_b128 v[36:39], v12 offset:18432\n" \
"v_mfma_f32_16x16x32_bf16 v[60:63], a[252:255], v[44:47], v[60:63]\n" \
"s_waitcnt lgkmcnt(4)\n" \
"s_add_u32 s57, s57, s49\n" \
"s_add_u32 s58, s58, s49\n" \
"s_add_u32 s59, s59, 1\n" \
"v_mov_b32 v5, v7\n" \
"v_mov_b32 v6, v11\n" \
"s_cmp_ge_u32 s59, s47\n" \
"s_cbranch_scc1 .Lp1g1_out0_%=\n" \
"v_mfma_f32_16x16x32_bf16 v[64:67], a[0:3], v[16:19], 0\n" \
"s_min_u32 s61, s56, s60\n" \
"s_add_u32 m0, s55, 0\n" \
"s_nop 0\n" \
"buffer_load_dwordx4 v4, s[36:39], s61 offen offset:0 sc1 lds\n" \
"v_add_u32_e64 v13, -1, s59\n" \
"v_mfma_f32_16x16x32_bf16 v[68:71], a[0:3], v[20:23], 0\n" \
"ds_read_b128 v[40:43], v12 offset:3072\n" \
"s_nop 0\n" \
"v_mfma_f32_16x16x32_bf16 v[72:75], a[128:131], v[16:19], 0\n" \
"ds_read_b128 v[44:47], v12 offset:19456\n" \
"s_nop 0\n" \
"v_mfma_f32_16x16x32_bf16 v[76:79], a[128:131], v[20:23], 0\n" \
"s_waitcnt lgkmcnt(4)\n" \
"v_mul_f32 v48, 0xbfb8aa3b, v48\n" \
"v_mul_f32 v49, 0xbfb8aa3b, v49\n" \
"v_mfma_f32_16x16x32_bf16 v[64:67], a[4:7], v[24:27], v[64:67]\n" \
"buffer_load_dwordx4 v4, s[36:39], s61 offen offset:1024 sc1 lds\n" \
"v_mfma_f32_16x16x32_bf16 v[68:71], a[4:7], v[28:31], v[68:71]\n" \
"ds_read_b128 v[16:19], v12 offset:4096\n" \
"v_mul_f32 v50, 0xbfb8aa3b, v50\n" \
"v_mfma_f32_16x16x32_bf16 v[72:75], a[132:135], v[24:27], v[72:75]\n" \
"ds_read_b128 v[20:23], v12 offset:20480\n" \
"v_mul_f32 v51, 0xbfb8aa3b, v51\n" \
"v_mfma_f32_16x16x32_bf16 v[76:79], a[132:135], v[28:31], v[76:79]\n" \
"s_waitcnt lgkmcnt(4)\n" \
"v_exp_f32 v48, v48\n" \
"v_mfma_f32_16x16x32_bf16 v[64:67], a[8:11], v[32:35], v[64:67]\n" \
"s_add_u32 s63, s59, 2\n" \
"s_min_u32 s63, s63, 63\n" \
"v_mfma_f32_16x16x32_bf16 v[68:71], a[8:11], v[36:39], v[68:71]\n" \
"ds_read_b128 v[24:27], v12 offset:5120\n" \
"s_lshr_b32 s63, s63, 3\n" \
"s_lshl_b32 s63, s63, 7\n" \
"v_mfma_f32_16x16x32_bf16 v[72:75], a[136:139], v[32:35], v[72:75]\n" \
"ds_read_b128 v[28:31], v12 offset:21504\n" \
"v_mfma_f32_16x16x32_bf16 v[76:79], a[136:139], v[36:39], v[76:79]\n" \
"s_waitcnt lgkmcnt(4)\n" \
"v_exp_f32 v49, v49\n" \
"v_mfma_f32_16x16x32_bf16 v[64:67], a[12:15], v[40:43], v[64:67]\n" \
"buffer_load_dwordx4 v4, s[36:39], s61 offen offset:2048 sc1 lds\n" \
"v_mfma_f32_16x16x32_bf16 v[68:71], a[12:15], v[44:47], v[68:71]\n" \
"ds_read_b128 v[32:35], v12 offset:6144\n" \
"v_mfma_f32_16x16x32_bf16 v[72:75], a[140:143], v[40:43], v[72:75]\n" \
"ds_read_b128 v[36:39], v12 offset:22528\n" \
"v_mfma_f32_16x16x32_bf16 v[76:79], a[140:143], v[44:47], v[76:79]\n" \
"s_waitcnt lgkmcnt(4)\n" \
"v_exp_f32 v50, v50\n" \
"v_mfma_f32_16x16x32_bf16 v[64:67], a[16:19], v[16:19], v[64:67]\n" \
"buffer_load_dwordx4 v4, s[36:39], s61 offen offset:3072 sc1 lds\n" \
"v_mfma_f32_16x16x32_bf16 v[68:71], a[16:19], v[20:23], v[68:71]\n" \
"ds_read_b128 v[40:43], v12 offset:7168\n" \
"v_mfma_f32_16x16x32_bf16 v[72:75], a[144:147], v[16:19], v[72:75]\n" \
"ds_read_b128 v[44:47], v12 offset:23552\n" \
"v_mfma_f32_16x16x32_bf16 v[76:79], a[144:147], v[20:23], v[76:79]\n" \
"s_waitcnt lgkmcnt(4)\n" \
"v_exp_f32 v51, v51\n" \
"v_mfma_f32_16x16x32_bf16 v[64:67], a[20:23], v[24:27], v[64:67]\n" \
"buffer_load_dword v15, v14, s[36:39], s63 offen sc1\n" \
"v_mfma_f32_16x16x32_bf16 v[68:71], a[20:23], v[28:31], v[68:71]\n" \
"ds_read_b128 v[16:19], v12 offset:8192\n" \
"v_add_f32_e64 v48, 1.0, v48\n" \
"v_mfma_f32_16x16x32_bf16 v[72:75], a[148:151], v[24:27], v[72:75]\n" \
"ds_read_b128 v[20:23], v12 offset:24576\n" \
"v_add_f32_e64 v49, 1.0, v49\n" \
"v_mfma_f32_16x16x32_bf16 v[76:79], a[148:151], v[28:31], v[76:79]\n" \
"s_waitcnt lgkmcnt(4)\n" \
"v_add_f32 v50, 1.0, v50\n" \
"v_add_f32_e64 v51, 1.0, v51\n" \
"v_mfma_f32_16x16x32_bf16 v[64:67], a[24:27], v[32:35], v[64:67]\n" \
"s_add_u32 m0, s55, 4096\n" \
"s_add_u32 s61, s61, 4096\n" \
"buffer_load_dwordx4 v4, s[36:39], s61 offen offset:0 sc1 lds\n" \
"v_mfma_f32_16x16x32_bf16 v[68:71], a[24:27], v[36:39], v[68:71]\n" \
"ds_read_b128 v[24:27], v12 offset:9216\n" \
"v_mfma_f32_16x16x32_bf16 v[72:75], a[152:155], v[32:35], v[72:75]\n" \
"ds_read_b128 v[28:31], v12 offset:25600\n" \
"v_mfma_f32_16x16x32_bf16 v[76:79], a[152:155], v[36:39], v[76:79]\n" \
"s_waitcnt lgkmcnt(4)\n" \
"v_rcp_f32 v48, v48\n" \
"v_mfma_f32_16x16x32_bf16 v[64:67], a[28:31], v[40:43], v[64:67]\n" \
"buffer_load_dwordx4 v4, s[36:39], s61 offen offset:1024 sc1 lds\n" \
"v_mfma_f32_16x16x32_bf16 v[68:71], a[28:31], v[44:47], v[68:71]\n" \
"ds_read_b128 v[32:35], v12 offset:10240\n" \
"v_mfma_f32_16x16x32_bf16 v[72:75], a[156:159], v[40:43], v[72:75]\n" \
"ds_read_b128 v[36:39], v12 offset:26624\n" \
"v_mfma_f32_16x16x32_bf16 v[76:79], a[156:159], v[44:47], v[76:79]\n" \
"s_waitcnt lgkmcnt(4)\n" \
"v_rcp_f32 v49, v49\n" \
"v_mfma_f32_16x16x32_bf16 v[64:67], a[32:35], v[16:19], v[64:67]\n" \
"v_rcp_f32_e64 v50, v50\n" \
"v_mfma_f32_16x16x32_bf16 v[68:71], a[32:35], v[20:23], v[68:71]\n" \
"ds_read_b128 v[40:43], v12 offset:11264\n" \
"v_mfma_f32_16x16x32_bf16 v[72:75], a[160:163], v[16:19], v[72:75]\n" \
"ds_read_b128 v[44:47], v12 offset:27648\n" \
"v_mfma_f32_16x16x32_bf16 v[76:79], a[160:163], v[20:23], v[76:79]\n" \
"s_waitcnt lgkmcnt(4)\n" \
"v_rcp_f32 v51, v51\n" \
"v_mfma_f32_16x16x32_bf16 v[64:67], a[36:39], v[24:27], v[64:67]\n" \
"buffer_load_dwordx4 v4, s[36:39], s61 offen offset:2048 sc1 lds\n" \
"v_mfma_f32_16x16x32_bf16 v[68:71], a[36:39], v[28:31], v[68:71]\n" \
"ds_read_b128 v[16:19], v12 offset:12288\n" \
"v_mul_f32_e64 v48, v48, v56\n" \
"v_mfma_f32_16x16x32_bf16 v[72:75], a[164:167], v[24:27], v[72:75]\n" \
"ds_read_b128 v[20:23], v12 offset:28672\n" \
"v_mul_f32_e64 v49, v49, v57\n" \
"v_mfma_f32_16x16x32_bf16 v[76:79], a[164:167], v[28:31], v[76:79]\n" \
"s_waitcnt lgkmcnt(4)\n" \
"v_mul_f32 v50, v50, v58\n" \
"v_mul_f32_e64 v51, v51, v59\n" \
"v_mfma_f32_16x16x32_bf16 v[64:67], a[40:43], v[32:35], v[64:67]\n" \
"buffer_load_dwordx4 v4, s[36:39], s61 offen offset:3072 sc1 lds\n" \
"v_mfma_f32_16x16x32_bf16 v[68:71], a[40:43], v[36:39], v[68:71]\n" \
"ds_read_b128 v[24:27], v12 offset:13312\n" \
"v_cvt_pk_bf16_f32 v80, v48, v49\n" \
"v_mfma_f32_16x16x32_bf16 v[72:75], a[168:171], v[32:35], v[72:75]\n" \
"ds_read_b128 v[28:31], v12 offset:29696\n" \
"v_cvt_pk_bf16_f32 v81, v50, v51\n" \
"v_mfma_f32_16x16x32_bf16 v[76:79], a[168:171], v[36:39], v[76:79]\n" \
"s_waitcnt lgkmcnt(4)\n" \
"v_mul_f32 v52, 0xbfb8aa3b, v52\n" \
"v_mul_f32 v53, 0xbfb8aa3b, v53\n" \
"v_mfma_f32_16x16x32_bf16 v[64:67], a[44:47], v[40:43], v[64:67]\n" \
"v_mul_f32 v54, 0xbfb8aa3b, v54\n" \
"v_mul_f32 v55, 0xbfb8aa3b, v55\n" \
"v_mfma_f32_16x16x32_bf16 v[68:71], a[44:47], v[44:47], v[68:71]\n" \
"ds_read_b128 v[32:35], v12 offset:14336\n" \
"v_mfma_f32_16x16x32_bf16 v[72:75], a[172:175], v[40:43], v[72:75]\n" \
"ds_read_b128 v[36:39], v12 offset:30720\n" \
"v_mfma_f32_16x16x32_bf16 v[76:79], a[172:175], v[44:47], v[76:79]\n" \
"s_waitcnt lgkmcnt(4)\n" \
"v_exp_f32_e64 v52, v52\n" \
"v_mfma_f32_16x16x32_bf16 v[64:67], a[48:51], v[16:19], v[64:67]\n" \
"v_exp_f32_e64 v53, v53\n" \
"v_mfma_f32_16x16x32_bf16 v[68:71], a[48:51], v[20:23], v[68:71]\n" \
"ds_read_b128 v[40:43], v12 offset:15360\n" \
"v_mfma_f32_16x16x32_bf16 v[72:75], a[176:179], v[16:19], v[72:75]\n" \
"ds_read_b128 v[44:47], v12 offset:31744\n" \
"v_mfma_f32_16x16x32_bf16 v[76:79], a[176:179], v[20:23], v[76:79]\n" \
"s_waitcnt lgkmcnt(4)\n" \
"v_exp_f32 v54, v54\n" \
"v_mfma_f32_16x16x32_bf16 v[64:67], a[52:55], v[24:27], v[64:67]\n" \
"s_waitcnt vmcnt(9)\n" \
"s_barrier\n" \
"v_mfma_f32_16x16x32_bf16 v[68:71], a[52:55], v[28:31], v[68:71]\n" \
"ds_read_b128 v[16:19], v12 offset:32768\n" \
"v_mfma_f32_16x16x32_bf16 v[72:75], a[180:183], v[24:27], v[72:75]\n" \
"ds_read_b128 v[20:23], v12 offset:49152\n" \
"v_mfma_f32_16x16x32_bf16 v[76:79], a[180:183], v[28:31], v[76:79]\n" \
"s_waitcnt lgkmcnt(4)\n" \
"buffer_store_dword v13, v6, s[36:39], 0 offen sc1\n" \
"v_exp_f32 v55, v55\n" \
"v_mfma_f32_16x16x32_bf16 v[64:67], a[56:59], v[32:35], v[64:67]\n" \
"v_add_f32 v52, 1.0, v52\n" \
"v_add_f32 v53, 1.0, v53\n" \
"v_mfma_f32_16x16x32_bf16 v[68:71], a[56:59], v[36:39], v[68:71]\n" \
"ds_read_b128 v[24:27], v12 offset:33792\n" \
"v_add_f32_e64 v54, 1.0, v54\n" \
"v_mfma_f32_16x16x32_bf16 v[72:75], a[184:187], v[32:35], v[72:75]\n" \
"ds_read_b128 v[28:31], v12 offset:50176\n" \
"v_add_f32_e64 v55, 1.0, v55\n" \
"v_mfma_f32_16x16x32_bf16 v[76:79], a[184:187], v[36:39], v[76:79]\n" \
"s_waitcnt lgkmcnt(4)\n" \
"v_rcp_f32 v52, v52\n" \
"v_mfma_f32_16x16x32_bf16 v[64:67], a[60:63], v[40:43], v[64:67]\n" \
"v_rcp_f32_e64 v53, v53\n" \
"v_mfma_f32_16x16x32_bf16 v[68:71], a[60:63], v[44:47], v[68:71]\n" \
"ds_read_b128 v[32:35], v12 offset:34816\n" \
"v_mfma_f32_16x16x32_bf16 v[72:75], a[188:191], v[40:43], v[72:75]\n" \
"ds_read_b128 v[36:39], v12 offset:51200\n" \
"v_mfma_f32_16x16x32_bf16 v[76:79], a[188:191], v[44:47], v[76:79]\n" \
"s_waitcnt lgkmcnt(4)\n" \
"v_rcp_f32 v54, v54\n" \
"v_mfma_f32_16x16x32_bf16 v[64:67], a[64:67], v[16:19], v[64:67]\n" \
"s_min_u32 s61, s56, s60\n" \
"s_add_u32 s61, s61, 16384\n" \
"s_add_u32 s56, s56, 0x10000\n" \
"s_add_u32 m0, s55, 32768\n" \
"s_nop 0\n" \
"buffer_load_dwordx4 v4, s[36:39], s61 offen offset:0 sc1 lds\n" \
"v_mfma_f32_16x16x32_bf16 v[68:71], a[64:67], v[20:23], v[68:71]\n" \
"ds_read_b128 v[40:43], v12 offset:35840\n" \
"v_mfma_f32_16x16x32_bf16 v[72:75], a[192:195], v[16:19], v[72:75]\n" \
"ds_read_b128 v[44:47], v12 offset:52224\n" \
"v_mfma_f32_16x16x32_bf16 v[76:79], a[192:195], v[20:23], v[76:79]\n" \
"s_waitcnt lgkmcnt(4)\n" \
"v_rcp_f32 v55, v55\n" \
"v_mfma_f32_16x16x32_bf16 v[64:67], a[68:71], v[24:27], v[64:67]\n" \
"buffer_load_dwordx4 v4, s[36:39], s61 offen offset:1024 sc1 lds\n" \
"v_mfma_f32_16x16x32_bf16 v[68:71], a[68:71], v[28:31], v[68:71]\n" \
"ds_read_b128 v[16:19], v12 offset:36864\n" \
"v_mul_f32_e64 v52, v52, v60\n" \
"v_mfma_f32_16x16x32_bf16 v[72:75], a[196:199], v[24:27], v[72:75]\n" \
"ds_read_b128 v[20:23], v12 offset:53248\n" \
"v_mul_f32_e64 v53, v53, v61\n" \
"v_mfma_f32_16x16x32_bf16 v[76:79], a[196:199], v[28:31], v[76:79]\n" \
"s_waitcnt lgkmcnt(4)\n" \
"v_mul_f32 v54, v54, v62\n" \
"v_mul_f32_e64 v55, v55, v63\n" \
"v_mfma_f32_16x16x32_bf16 v[64:67], a[72:75], v[32:35], v[64:67]\n" \
"v_cvt_pk_bf16_f32 v82, v52, v53\n" \
"v_cvt_pk_bf16_f32 v83, v54, v55\n" \
"v_mfma_f32_16x16x32_bf16 v[68:71], a[72:75], v[36:39], v[68:71]\n" \
"ds_read_b128 v[24:27], v12 offset:37888\n" \
"s_nop 0\n" \
"v_mfma_f32_16x16x32_bf16 v[72:75], a[200:203], v[32:35], v[72:75]\n" \
"ds_read_b128 v[28:31], v12 offset:54272\n" \
"s_nop 0\n" \
"v_mfma_f32_16x16x32_bf16 v[76:79], a[200:203], v[36:39], v[76:79]\n" \
"s_waitcnt lgkmcnt(4)\n" \
"v_permlane16_swap_b32 v80, v82\n" \
"v_permlane16_swap_b32 v81, v83\n" \
"v_mfma_f32_16x16x32_bf16 v[64:67], a[76:79], v[40:43], v[64:67]\n" \
"buffer_load_dwordx4 v4, s[36:39], s61 offen offset:2048 sc1 lds\n" \
"v_mfma_f32_16x16x32_bf16 v[68:71], a[76:79], v[44:47], v[68:71]\n" \
"ds_read_b128 v[32:35], v12 offset:38912\n" \
"buffer_store_dwordx4 v[80:83], v5, s[36:39], s57 offen sc1\n" \
"v_mfma_f32_16x16x32_bf16 v[72:75], a[204:207], v[40:43], v[72:75]\n" \
"ds_read_b128 v[36:39], v12 offset:55296\n" \
"v_mfma_f32_16x16x32_bf16 v[76:79], a[204:207], v[44:47], v[76:79]\n" \
"s_waitcnt lgkmcnt(4)\n" \
"v_mfma_f32_16x16x32_bf16 v[64:67], a[80:83], v[16:19], v[64:67]\n" \
"buffer_load_dwordx4 v4, s[36:39], s61 offen offset:3072 sc1 lds\n" \
"v_mfma_f32_16x16x32_bf16 v[68:71], a[80:83], v[20:23], v[68:71]\n" \
"ds_read_b128 v[40:43], v12 offset:39936\n" \
"v_mfma_f32_16x16x32_bf16 v[72:75], a[208:211], v[16:19], v[72:75]\n" \
"ds_read_b128 v[44:47], v12 offset:56320\n" \
"v_mfma_f32_16x16x32_bf16 v[76:79], a[208:211], v[20:23], v[76:79]\n" \
"s_waitcnt lgkmcnt(4)\n" \
"v_mfma_f32_16x16x32_bf16 v[64:67], a[84:87], v[24:27], v[64:67]\n" \
"v_mfma_f32_16x16x32_bf16 v[68:71], a[84:87], v[28:31], v[68:71]\n" \
"ds_read_b128 v[16:19], v12 offset:40960\n" \
"v_mfma_f32_16x16x32_bf16 v[72:75], a[212:215], v[24:27], v[72:75]\n" \
"ds_read_b128 v[20:23], v12 offset:57344\n" \
"v_mfma_f32_16x16x32_bf16 v[76:79], a[212:215], v[28:31], v[76:79]\n" \
"s_waitcnt lgkmcnt(4)\n" \
"v_mfma_f32_16x16x32_bf16 v[64:67], a[88:91], v[32:35], v[64:67]\n" \
"s_add_u32 m0, s55, 36864\n" \
"s_add_u32 s61, s61, 4096\n" \
"buffer_load_dwordx4 v4, s[36:39], s61 offen offset:0 sc1 lds\n" \
"v_mfma_f32_16x16x32_bf16 v[68:71], a[88:91], v[36:39], v[68:71]\n" \
"ds_read_b128 v[24:27], v12 offset:41984\n" \
"v_mfma_f32_16x16x32_bf16 v[72:75], a[216:219], v[32:35], v[72:75]\n" \
"ds_read_b128 v[28:31], v12 offset:58368\n" \
"v_mfma_f32_16x16x32_bf16 v[76:79], a[216:219], v[36:39], v[76:79]\n" \
"s_waitcnt lgkmcnt(4)\n" \
"v_mfma_f32_16x16x32_bf16 v[64:67], a[92:95], v[40:43], v[64:67]\n" \
"buffer_load_dwordx4 v4, s[36:39], s61 offen offset:1024 sc1 lds\n" \
"v_mfma_f32_16x16x32_bf16 v[68:71], a[92:95], v[44:47], v[68:71]\n" \
"ds_read_b128 v[32:35], v12 offset:43008\n" \
"v_mfma_f32_16x16x32_bf16 v[72:75], a[220:223], v[40:43], v[72:75]\n" \
"ds_read_b128 v[36:39], v12 offset:59392\n" \
"v_mfma_f32_16x16x32_bf16 v[76:79], a[220:223], v[44:47], v[76:79]\n" \
"s_waitcnt lgkmcnt(4)\n" \
"v_mfma_f32_16x16x32_bf16 v[64:67], a[96:99], v[16:19], v[64:67]\n" \
"v_mfma_f32_16x16x32_bf16 v[68:71], a[96:99], v[20:23], v[68:71]\n" \
"ds_read_b128 v[40:43], v12 offset:44032\n" \
"v_mfma_f32_16x16x32_bf16 v[72:75], a[224:227], v[16:19], v[72:75]\n" \
"ds_read_b128 v[44:47], v12 offset:60416\n" \
"v_mfma_f32_16x16x32_bf16 v[76:79], a[224:227], v[20:23], v[76:79]\n" \
"s_waitcnt lgkmcnt(4)\n" \
"v_mfma_f32_16x16x32_bf16 v[64:67], a[100:103], v[24:27], v[64:67]\n" \
"buffer_load_dwordx4 v4, s[36:39], s61 offen offset:2048 sc1 lds\n" \
"v_mfma_f32_16x16x32_bf16 v[68:71], a[100:103], v[28:31], v[68:71]\n" \
"ds_read_b128 v[16:19], v12 offset:45056\n" \
"v_mfma_f32_16x16x32_bf16 v[72:75], a[228:231], v[24:27], v[72:75]\n" \
"ds_read_b128 v[20:23], v12 offset:61440\n" \
"v_mfma_f32_16x16x32_bf16 v[76:79], a[228:231], v[28:31], v[76:79]\n" \
"s_waitcnt lgkmcnt(4)\n" \
"v_mfma_f32_16x16x32_bf16 v[64:67], a[104:107], v[32:35], v[64:67]\n" \
"buffer_load_dwordx4 v4, s[36:39], s61 offen offset:3072 sc1 lds\n" \
"v_mfma_f32_16x16x32_bf16 v[68:71], a[104:107], v[36:39], v[68:71]\n" \
"ds_read_b128 v[24:27], v12 offset:46080\n" \
"v_mfma_f32_16x16x32_bf16 v[72:75], a[232:235], v[32:35], v[72:75]\n" \
"ds_read_b128 v[28:31], v12 offset:62464\n" \
"v_mfma_f32_16x16x32_bf16 v[76:79], a[232:235], v[36:39], v[76:79]\n" \
"s_waitcnt lgkmcnt(4)\n" \
"v_mfma_f32_16x16x32_bf16 v[64:67], a[108:111], v[40:43], v[64:67]\n" \
"v_mfma_f32_16x16x32_bf16 v[68:71], a[108:111], v[44:47], v[68:71]\n" \
"ds_read_b128 v[32:35], v12 offset:47104\n" \
"v_mfma_f32_16x16x32_bf16 v[72:75], a[236:239], v[40:43], v[72:75]\n" \
"ds_read_b128 v[36:39], v12 offset:63488\n" \
"v_mfma_f32_16x16x32_bf16 v[76:79], a[236:239], v[44:47], v[76:79]\n" \
"s_waitcnt lgkmcnt(4)\n" \
"v_mfma_f32_16x16x32_bf16 v[64:67], a[112:115], v[16:19], v[64:67]\n" \
"v_mfma_f32_16x16x32_bf16 v[68:71], a[112:115], v[20:23], v[68:71]\n" \
"ds_read_b128 v[40:43], v12 offset:48128\n" \
"v_mfma_f32_16x16x32_bf16 v[72:75], a[240:243], v[16:19], v[72:75]\n" \
"ds_read_b128 v[44:47], v12 offset:64512\n" \
"v_mfma_f32_16x16x32_bf16 v[76:79], a[240:243], v[20:23], v[76:79]\n" \
"s_waitcnt lgkmcnt(4)\n" \
"v_mfma_f32_16x16x32_bf16 v[64:67], a[116:119], v[24:27], v[64:67]\n" \
"s_waitcnt vmcnt(10)\n" \
"s_barrier\n" \
"v_mfma_f32_16x16x32_bf16 v[68:71], a[116:119], v[28:31], v[68:71]\n" \
"ds_read_b128 v[16:19], v10 offset:0\n" \
"v_mfma_f32_16x16x32_bf16 v[72:75], a[244:247], v[24:27], v[72:75]\n" \
"ds_read_b128 v[20:23], v10 offset:16384\n" \
"v_mfma_f32_16x16x32_bf16 v[76:79], a[244:247], v[28:31], v[76:79]\n" \
"s_waitcnt lgkmcnt(4)\n" \
"v_mfma_f32_16x16x32_bf16 v[64:67], a[120:123], v[32:35], v[64:67]\n" \
"v_cmp_gt_u32_e64 vcc, 16, v15\n" \
"v_mfma_f32_16x16x32_bf16 v[68:71], a[120:123], v[36:39], v[68:71]\n" \
"ds_read_b128 v[24:27], v10 offset:1024\n" \
"v_mfma_f32_16x16x32_bf16 v[72:75], a[248:251], v[32:35], v[72:75]\n" \
"ds_read_b128 v[28:31], v10 offset:17408\n" \
"v_mfma_f32_16x16x32_bf16 v[76:79], a[248:251], v[36:39], v[76:79]\n" \
"s_waitcnt lgkmcnt(4)\n" \
"v_mfma_f32_16x16x32_bf16 v[64:67], a[124:127], v[40:43], v[64:67]\n" \
"s_cbranch_vccnz .Lp1g1_m1_%=\n" \
".Lp1g1_m1k_%=:\n" \
"v_mfma_f32_16x16x32_bf16 v[68:71], a[124:127], v[44:47], v[68:71]\n" \
"ds_read_b128 v[32:35], v10 offset:2048\n" \
"v_mfma_f32_16x16x32_bf16 v[72:75], a[252:255], v[40:43], v[72:75]\n" \
"ds_read_b128 v[36:39], v10 offset:18432\n" \
"v_mfma_f32_16x16x32_bf16 v[76:79], a[252:255], v[44:47], v[76:79]\n" \
"s_waitcnt lgkmcnt(4)\n" \
"s_add_u32 s57, s57, s49\n" \
"s_add_u32 s58, s58, s49\n" \
"s_add_u32 s59, s59, 1\n" \
"v_mov_b32_e64 v5, v7\n" \
"v_mov_b32_e64 v6, v11\n" \
"s_cmp_ge_u32 s59, s47\n" \
"s_nop 0\n" \
"s_cbranch_scc0 .Lp1g1_loop_%=\n" \
".Lp1g1_out1_%=:\n" \
"s_nop 7\n" \
"s_nop 7\n" \
"v_mul_f32 v64, 0xbfb8aa3b, v64\n" \
"v_mul_f32 v65, 0xbfb8aa3b, v65\n" \
"v_mul_f32 v66, 0xbfb8aa3b, v66\n" \
"v_mul_f32 v67, 0xbfb8aa3b, v67\n" \
"v_exp_f32 v64, v64\n" \
"v_exp_f32 v65, v65\n" \
"v_exp_f32 v66, v66\n" \
"v_exp_f32 v67, v67\n" \
"v_add_f32 v64, 1.0, v64\n" \
"v_add_f32 v65, 1.0, v65\n" \
"v_add_f32 v66, 1.0, v66\n" \
"v_add_f32 v67, 1.0, v67\n" \
"v_rcp_f32 v64, v64\n" \
"v_rcp_f32 v65, v65\n" \
"v_rcp_f32 v66, v66\n" \
"v_rcp_f32 v67, v67\n" \
"v_mul_f32 v64, v64, v72\n" \
"v_mul_f32 v65, v65, v73\n" \
"v_mul_f32 v66, v66, v74\n" \
"v_mul_f32 v67, v67, v75\n" \
"v_cvt_pk_bf16_f32 v80, v64, v65\n" \
"v_cvt_pk_bf16_f32 v81, v66, v67\n" \
"v_mul_f32 v68, 0xbfb8aa3b, v68\n" \
"v_mul_f32 v69, 0xbfb8aa3b, v69\n" \
"v_mul_f32 v70, 0xbfb8aa3b, v70\n" \
"v_mul_f32 v71, 0xbfb8aa3b, v71\n" \
"v_exp_f32 v68, v68\n" \
"v_exp_f32 v69, v69\n" \
"v_exp_f32 v70, v70\n" \
"v_exp_f32 v71, v71\n" \
"v_add_f32 v68, 1.0, v68\n" \
"v_add_f32 v69, 1.0, v69\n" \
"v_add_f32 v70, 1.0, v70\n" \
"v_add_f32 v71, 1.0, v71\n" \
"v_rcp_f32 v68, v68\n" \
"v_rcp_f32 v69, v69\n" \
"v_rcp_f32 v70, v70\n" \
"v_rcp_f32 v71, v71\n" \
"v_mul_f32 v68, v68, v76\n" \
"v_mul_f32 v69, v69, v77\n" \
"v_mul_f32 v70, v70, v78\n" \
"v_mul_f32 v71, v71, v79\n" \
"v_cvt_pk_bf16_f32 v82, v68, v69\n" \
"v_cvt_pk_bf16_f32 v83, v70, v71\n" \
"s_nop 0\n" \
"s_nop 0\n" \
"v_permlane16_swap_b32 v80, v82\n" \
"v_permlane16_swap_b32 v81, v83\n" \
"buffer_store_dwordx4 v[80:83], v5, s[36:39], s57 offen sc1\n" \
"s_branch .Lp1g1_end_%=\n" \
".Lp1g1_out0_%=:\n" \
"s_nop 7\n" \
"s_nop 7\n" \
"v_mul_f32 v48, 0xbfb8aa3b, v48\n" \
"v_mul_f32 v49, 0xbfb8aa3b, v49\n" \
"v_mul_f32 v50, 0xbfb8aa3b, v50\n" \
"v_mul_f32 v51, 0xbfb8aa3b, v51\n" \
"v_exp_f32 v48, v48\n" \
"v_exp_f32 v49, v49\n" \
"v_exp_f32 v50, v50\n" \
"v_exp_f32 v51, v51\n" \
"v_add_f32 v48, 1.0, v48\n" \
"v_add_f32 v49, 1.0, v49\n" \
"v_add_f32 v50, 1.0, v50\n" \
"v_add_f32 v51, 1.0, v51\n" \
"v_rcp_f32 v48, v48\n" \
"v_rcp_f32 v49, v49\n" \
"v_rcp_f32 v50, v50\n" \
"v_rcp_f32 v51, v51\n" \
"v_mul_f32 v48, v48, v56\n" \
"v_mul_f32 v49, v49, v57\n" \
"v_mul_f32 v50, v50, v58\n" \
"v_mul_f32 v51, v51, v59\n" \
"v_cvt_pk_bf16_f32 v80, v48, v49\n" \
"v_cvt_pk_bf16_f32 v81, v50, v51\n" \
"v_mul_f32 v52, 0xbfb8aa3b, v52\n" \
"v_mul_f32 v53, 0xbfb8aa3b, v53\n" \
"v_mul_f32 v54, 0xbfb8aa3b, v54\n" \
"v_mul_f32 v55, 0xbfb8aa3b, v55\n" \
"v_exp_f32 v52, v52\n" \
"v_exp_f32 v53, v53\n" \
"v_exp_f32 v54, v54\n" \
"v_exp_f32 v55, v55\n" \
"v_add_f32 v52, 1.0, v52\n" \
"v_add_f32 v53, 1.0, v53\n" \
"v_add_f32 v54, 1.0, v54\n" \
"v_add_f32 v55, 1.0, v55\n" \
"v_rcp_f32 v52, v52\n" \
"v_rcp_f32 v53, v53\n" \
"v_rcp_f32 v54, v54\n" \
"v_rcp_f32 v55, v55\n" \
"v_mul_f32 v52, v52, v60\n" \
"v_mul_f32 v53, v53, v61\n" \
"v_mul_f32 v54, v54, v62\n" \
"v_mul_f32 v55, v55, v63\n" \
"v_cvt_pk_bf16_f32 v82, v52, v53\n" \
"v_cvt_pk_bf16_f32 v83, v54, v55\n" \
"s_nop 0\n" \
"s_nop 0\n" \
"v_permlane16_swap_b32 v80, v82\n" \
"v_permlane16_swap_b32 v81, v83\n" \
"buffer_store_dwordx4 v[80:83], v5, s[36:39], s57 offen sc1\n" \
".Lp1g1_end_%=:\n" \
"s_waitcnt vmcnt(0) lgkmcnt(0)\n" \
"s_barrier\n" \
"v_mov_b32 v13, s59\n" \
"buffer_store_dword v13, v6, s[36:39], 0 offen sc1\n" \
"s_waitcnt vmcnt(0) lgkmcnt(0)\n" \
"s_branch .Lp1g1_x_%=\n" \
".Lp1g1_l0_%=:\n" \
"s_nop 7\n" \
"s_nop 7\n" \
"s_waitcnt vmcnt(0) lgkmcnt(0)\n" \
"s_mov_b32 s62, 0\n" \
".Lp1g1_l0a_%=:\n" \
"buffer_load_dword v15, v14, s[36:39], s63 offen sc1\n" \
"s_waitcnt vmcnt(0)\n" \
"v_cmp_gt_u32 vcc, 16, v15\n" \
"s_nop 4\n" \
"s_cbranch_vccz .Lp1g1_l0k_%=\n" \
"s_sleep 4\n" \
"s_add_u32 s62, s62, 1\n" \
"s_cmp_lt_u32 s62, 0x10000\n" \
"s_cbranch_scc1 .Lp1g1_l0a_%=\n" \
"v_mov_b32 v15, 256\n" \
"buffer_store_dword v15, v15, s[36:39], 0 offen sc1\n" \
"s_waitcnt vmcnt(0) lgkmcnt(0)\n" \
"s_branch .Lp1g1_l0k_%=\n" \
".Lp1g1_l1_%=:\n" \
"s_nop 7\n" \
"s_nop 7\n" \
"s_waitcnt vmcnt(0) lgkmcnt(0)\n" \
"s_mov_b32 s62, 0\n" \
".Lp1g1_l1a_%=:\n" \
"buffer_load_dword v15, v14, s[36:39], s63 offen sc1\n" \
"s_waitcnt vmcnt(0)\n" \
"v_cmp_gt_u32 vcc, 16, v15\n" \
"s_nop 4\n" \
"s_cbranch_vccz .Lp1g1_l1k_%=\n" \
"s_sleep 4\n" \
"s_add_u32 s62, s62, 1\n" \
"s_cmp_lt_u32 s62, 0x10000\n" \
"s_cbranch_scc1 .Lp1g1_l1a_%=\n" \
"v_mov_b32 v15, 256\n" \
"buffer_store_dword v15, v15, s[36:39], 0 offen sc1\n" \
"s_waitcnt vmcnt(0) lgkmcnt(0)\n" \
"s_branch .Lp1g1_l1k_%=\n" \
".Lp1g1_m0_%=:\n" \
"s_nop 7\n" \
"s_nop 7\n" \
"s_waitcnt vmcnt(0) lgkmcnt(0)\n" \
"s_mov_b32 s62, 0\n" \
".Lp1g1_m0a_%=:\n" \
"buffer_load_dword v15, v14, s[36:39], s63 offen sc1\n" \
"s_waitcnt vmcnt(0)\n" \
"v_cmp_gt_u32 vcc, 16, v15\n" \
"s_nop 4\n" \
"s_cbranch_vccz .Lp1g1_m0k_%=\n" \
"s_sleep 4\n" \
"s_add_u32 s62, s62, 1\n" \
"s_cmp_lt_u32 s62, 0x10000\n" \
"s_cbranch_scc1 .Lp1g1_m0a_%=\n" \
"v_mov_b32 v15, 256\n" \
"buffer_store_dword v15, v15, s[36:39], 0 offen sc1\n" \
"s_waitcnt vmcnt(0) lgkmcnt(0)\n" \
"s_branch .Lp1g1_m0k_%=\n" \
".Lp1g1_m1_%=:\n" \
"s_nop 7\n" \
"s_nop 7\n" \
"s_waitcnt vmcnt(0) lgkmcnt(0)\n" \
"s_mov_b32 s62, 0\n" \
".Lp1g1_m1a_%=:\n" \
"buffer_load_dword v15, v14, s[36:39], s63 offen sc1\n" \
"s_waitcnt vmcnt(0)\n" \
"v_cmp_gt_u32 vcc, 16, v15\n" \
"s_nop 4\n" \
"s_cbranch_vccz .Lp1g1_m1k_%=\n" \
"s_sleep 4\n" \
"s_add_u32 s62, s62, 1\n" \
"s_cmp_lt_u32 s62, 0x10000\n" \
"s_cbranch_scc1 .Lp1g1_m1a_%=\n" \
"v_mov_b32 v15, 256\n" \
"buffer_store_dword v15, v15, s[36:39], 0 offen sc1\n" \
"s_waitcnt vmcnt(0) lgkmcnt(0)\n" \
"s_branch .Lp1g1_m1k_%=\n" \
".Lp1g1_x_%=:\n"
#define CLOB_P1 \
"a0", "a1", "a2", "a3", "a4", "a5", "a6", "a7", "a8", "a9", "a10", "a11", "a12", "a13", "a14", "a15", "a16", "a17", "a18", "a19", "a20", "a21", "a22", "a23", "a24", "a25", "a26", "a27", "a28", "a29", "a30", "a31", "a32", "a33", "a34", "a35", "a36", "a37", "a38", "a39", "a40", "a41", "a42", "a43", "a44", "a45", "a46", "a47", "a48", "a49", "a50", "a51", "a52", "a53", "a54", "a55", "a56", "a57", "a58", "a59", "a60", "a61", "a62", "a63", "a64", "a65", "a66", "a67", "a68", "a69", "a70", "a71", "a72", "a73", "a74", "a75", "a76", "a77", "a78", "a79", "a80", "a81", "a82", "a83", "a84", "a85", "a86", "a87", "a88", "a89", "a90", "a91", "a92", "a93", "a94", "a95", "a96", "a97", "a98", "a99", "a100", "a101", "a102", "a103", "a104", "a105", "a106", "a107", "a108", "a109", "a110", "a111", "a112", "a113", "a114", "a115", "a116", "a117", "a118", "a119", "a120", "a121", "a122", "a123", "a124", "a125", "a126", "a127", "a128", "a129", "a130", "a131", "a132", "a133", "a134", "a135", "a136", "a137", "a138", "a139", "a140", "a141", "a142", "a143", "a144", "a145", "a146", "a147", "a148", "a149", "a150", "a151", "a152", "a153", "a154", "a155", "a156", "a157", "a158", "a159", "a160", "a161", "a162", "a163", "a164", "a165", "a166", "a167", "a168", "a169", "a170", "a171", "a172", "a173", "a174", "a175", "a176", "a177", "a178", "a179", "a180", "a181", "a182", "a183", "a184", "a185", "a186", "a187", "a188", "a189", "a190", "a191", "a192", "a193", "a194", "a195", "a196", "a197", "a198", "a199", "a200", "a201", "a202", "a203", "a204", "a205", "a206", "a207", "a208", "a209", "a210", "a211", "a212", "a213", "a214", "a215", "a216", "a217", "a218", "a219", "a220", "a221", "a222", "a223", "a224", "a225", "a226", "a227", "a228", "a229", "a230", "a231", "a232", "a233", "a234", "a235", "a236", "a237", "a238", "a239", "a240", "a241", "a242", "a243", "a244", "a245", "a246", "a247", "a248", "a249", "a250", "a251", "a252", "a253", "a254", "a255", "s52", "s53", "s54", "s55", "s56", "s57", "s58", "s59", "s60", "s61", "s62", "s63", "v7", "v8", "v9", "v10", "v11", "v12", "v13", "v14", "v15", "v16", "v17", "v18", "v19", "v20", "v21", "v22", "v23", "v24", "v25", "v26", "v27", "v28", "v29", "v30", "v31", "v32", "v33", "v34", "v35", "v36", "v37", "v38", "v39", "v40", "v41", "v42", "v43", "v44", "v45", "v46", "v47", "v48", "v49", "v50", "v51", "v52", "v53", "v54", "v55", "v56", "v57", "v58", "v59", "v60", "v61", "v62", "v63", "v64", "v65", "v66", "v67", "v68", "v69", "v70", "v71", "v72", "v73", "v74", "v75", "v76", "v77", "v78", "v79", "v80", "v81", "v82", "v83", "v84", "v85", "v86", "v87", "vcc", "scc", "memory"
#define ASM_QK \
"s_nop 7\n" \
"s_nop 7\n" \
"v_add_u32 v36, 0x10000, v34\n" \
"v_add_u32 v38, 0x20000, v34\n" \
"v_add_u32 v37, 0x10000, v35\n" \
"v_add_u32 v39, 0x20000, v35\n" \
"s_lshl_b32 s62, s48, 12\n" \
"s_add_u32 m0, s62, 147456\n" \
"s_mov_b32 s54, 524288\n" \
"buffer_load_dwordx4 v32, s[40:43], s54 offen nt lds\n" \
"s_add_u32 m0, s62, 148480\n" \
"s_mov_b32 s54, 524288\n" \
"buffer_load_dwordx4 v33, s[40:43], s54 offen nt lds\n" \
"s_add_u32 m0, s62, 149504\n" \
"s_mov_b32 s54, 655360\n" \
"buffer_load_dwordx4 v32, s[40:43], s54 offen nt lds\n" \
"s_add_u32 m0, s62, 150528\n" \
"s_mov_b32 s54, 655360\n" \
"buffer_load_dwordx4 v33, s[40:43], s54 offen nt lds\n" \
"s_add_u32 m0, s62, 131072\n" \
"s_mov_b32 s54, 786432\n" \
"buffer_load_dwordx4 v32, s[40:43], s54 offen nt lds\n" \
"s_add_u32 m0, s62, 132096\n" \
"s_mov_b32 s54, 786432\n" \
"buffer_load_dwordx4 v33, s[40:43], s54 offen nt lds\n" \
"s_add_u32 m0, s62, 133120\n" \
"s_mov_b32 s54, 917504\n" \
"buffer_load_dwordx4 v32, s[40:43], s54 offen nt lds\n" \
"s_add_u32 m0, s62, 134144\n" \
"s_mov_b32 s54, 917504\n" \
"buffer_load_dwordx4 v33, s[40:43], s54 offen nt lds\n" \
"s_add_u32 m0, s62, 114688\n" \
"s_mov_b32 s54, 1572864\n" \
"buffer_load_dwordx4 v32, s[40:43], s54 offen nt lds\n" \
"s_add_u32 m0, s62, 115712\n" \
"s_mov_b32 s54, 1572864\n" \
"buffer_load_dwordx4 v33, s[40:43], s54 offen nt lds\n" \
"s_add_u32 m0, s62, 116736\n" \
"s_mov_b32 s54, 1703936\n" \
"buffer_load_dwordx4 v32, s[40:43], s54 offen nt lds\n" \
"s_add_u32 m0, s62, 117760\n" \
"s_mov_b32 s54, 1703936\n" \
"buffer_load_dwordx4 v33, s[40:43], s54 offen nt lds\n" \
"s_add_u32 m0, s62, 98304\n" \
"s_mov_b32 s54, 1835008\n" \
"buffer_load_dwordx4 v32, s[40:43], s54 offen nt lds\n" \
"s_add_u32 m0, s62, 99328\n" \
"s_mov_b32 s54, 1835008\n" \
"buffer_load_dwordx4 v33, s[40:43], s54 offen nt lds\n" \
"s_add_u32 m0, s62, 100352\n" \
"s_mov_b32 s54, 1966080\n" \
"buffer_load_dwordx4 v32, s[40:43], s54 offen nt lds\n" \
"s_add_u32 m0, s62, 101376\n" \
"s_mov_b32 s54, 1966080\n" \
"buffer_load_dwordx4 v33, s[40:43], s54 offen nt lds\n" \
"s_add_u32 m0, s62, 81920\n" \
"s_mov_b32 s54, 2621440\n" \
"buffer_load_dwordx4 v32, s[40:43], s54 offen nt lds\n" \
"s_add_u32 m0, s62, 82944\n" \
"s_mov_b32 s54, 2621440\n" \
"buffer_load_dwordx4 v33, s[40:43], s54 offen nt lds\n" \
"s_add_u32 m0, s62, 83968\n" \
"s_mov_b32 s54, 2752512\n" \
"buffer_load_dwordx4 v32, s[40:43], s54 offen nt lds\n" \
"s_add_u32 m0, s62, 84992\n" \
"s_mov_b32 s54, 2752512\n" \
"buffer_load_dwordx4 v33, s[40:43], s54 offen nt lds\n" \
"s_add_u32 m0, s62, 65536\n" \
"s_mov_b32 s54, 2883584\n" \
"buffer_load_dwordx4 v32, s[40:43], s54 offen nt lds\n" \
"s_add_u32 m0, s62, 66560\n" \
"s_mov_b32 s54, 2883584\n" \
"buffer_load_dwordx4 v33, s[40:43], s54 offen nt lds\n" \
"s_add_u32 m0, s62, 67584\n" \
"s_mov_b32 s54, 3014656\n" \
"buffer_load_dwordx4 v32, s[40:43], s54 offen nt lds\n" \
"s_add_u32 m0, s62, 68608\n" \
"s_mov_b32 s54, 3014656\n" \
"buffer_load_dwordx4 v33, s[40:43], s54 offen nt lds\n" \
"s_add_u32 m0, s62, 49152\n" \
"s_mov_b32 s54, 3670016\n" \
"buffer_load_dwordx4 v32, s[40:43], s54 offen nt lds\n" \
"s_add_u32 m0, s62, 50176\n" \
"s_mov_b32 s54, 3670016\n" \
"buffer_load_dwordx4 v33, s[40:43], s54 offen nt lds\n" \
"s_add_u32 m0, s62, 51200\n" \
"s_mov_b32 s54, 3801088\n" \
"buffer_load_dwordx4 v32, s[40:43], s54 offen nt lds\n" \
"s_add_u32 m0, s62, 52224\n" \
"s_mov_b32 s54, 3801088\n" \
"buffer_load_dwordx4 v33, s[40:43], s54 offen nt lds\n" \
"s_add_u32 m0, s62, 32768\n" \
"s_mov_b32 s54, 3932160\n" \
"buffer_load_dwordx4 v32, s[40:43], s54 offen nt lds\n" \
"s_add_u32 m0, s62, 33792\n" \
"s_mov_b32 s54, 3932160\n" \
"buffer_load_dwordx4 v33, s[40:43], s54 offen nt lds\n" \
"s_add_u32 m0, s62, 34816\n" \
"s_mov_b32 s54, 4063232\n" \
"buffer_load_dwordx4 v32, s[40:43], s54 offen nt lds\n" \
"s_add_u32 m0, s62, 35840\n" \
"s_mov_b32 s54, 4063232\n" \
"buffer_load_dwordx4 v33, s[40:43], s54 offen nt lds\n" \
"s_add_u32 m0, s62, 16384\n" \
"s_mov_b32 s54, 4718592\n" \
"buffer_load_dwordx4 v32, s[40:43], s54 offen nt lds\n" \
"s_add_u32 m0, s62, 17408\n" \
"s_mov_b32 s54, 4718592\n" \
"buffer_load_dwordx4 v33, s[40:43], s54 offen nt lds\n" \
"s_add_u32 m0, s62, 18432\n" \
"s_mov_b32 s54, 4849664\n" \
"buffer_load_dwordx4 v32, s[40:43], s54 offen nt lds\n" \
"s_add_u32 m0, s62, 19456\n" \
"s_mov_b32 s54, 4849664\n" \
"buffer_load_dwordx4 v33, s[40:43], s54 offen nt lds\n" \
"s_add_u32 m0, s62, 0\n" \
"s_mov_b32 s54, 4980736\n" \
"buffer_load_dwordx4 v32, s[40:43], s54 offen nt lds\n" \
"s_add_u32 m0, s62, 1024\n" \
"s_mov_b32 s54, 4980736\n" \
"buffer_load_dwordx4 v33, s[40:43], s54 offen nt lds\n" \
"s_add_u32 m0, s62, 2048\n" \
"s_mov_b32 s54, 5111808\n" \
"buffer_load_dwordx4 v32, s[40:43], s54 offen nt lds\n" \
"s_add_u32 m0, s62, 3072\n" \
"s_mov_b32 s54, 5111808\n" \
"buffer_load_dwordx4 v33, s[40:43], s54 offen nt lds\n" \
"s_waitcnt vmcnt(32)\n" \
"s_barrier\n" \
"ds_read_b64_tr_b16 a[16:17], v38 offset:16384\n" \
"ds_read_b64_tr_b16 a[18:19], v38 offset:16896\n" \
"ds_read_b64_tr_b16 a[24:25], v39 offset:16384\n" \
"ds_read_b64_tr_b16 a[26:27], v39 offset:16896\n" \
"ds_read_b64_tr_b16 v[112:113], v38 offset:24576\n" \
"ds_read_b64_tr_b16 v[114:115], v38 offset:25088\n" \
"ds_read_b64_tr_b16 v[120:121], v39 offset:24576\n" \
"ds_read_b64_tr_b16 v[122:123], v39 offset:25088\n" \
"ds_read_b64_tr_b16 a[20:21], v38 offset:0\n" \
"ds_read_b64_tr_b16 a[22:23], v38 offset:512\n" \
"ds_read_b64_tr_b16 a[28:29], v39 offset:0\n" \
"ds_read_b64_tr_b16 a[30:31], v39 offset:512\n" \
"ds_read_b64_tr_b16 v[116:117], v38 offset:8192\n" \
"ds_read_b64_tr_b16 v[118:119], v38 offset:8704\n" \
"ds_read_b64_tr_b16 v[124:125], v39 offset:8192\n" \
"ds_read_b64_tr_b16 v[126:127], v39 offset:8704\n" \
"s_waitcnt lgkmcnt(0)\n" \
"s_barrier\n" \
"s_add_u32 m0, s62, 147456\n" \
"s_mov_b32 s54, 5767168\n" \
"buffer_load_dwordx4 v32, s[40:43], s54 offen nt lds\n" \
"s_add_u32 m0, s62, 148480\n" \
"s_mov_b32 s54, 5767168\n" \
"buffer_load_dwordx4 v33, s[40:43], s54 offen nt lds\n" \
"s_add_u32 m0, s62, 149504\n" \
"s_mov_b32 s54, 5898240\n" \
"buffer_load_dwordx4 v32, s[40:43], s54 offen nt lds\n" \
"s_add_u32 m0, s62, 150528\n" \
"s_mov_b32 s54, 5898240\n" \
"buffer_load_dwordx4 v33, s[40:43], s54 offen nt lds\n" \
"s_add_u32 m0, s62, 131072\n" \
"s_mov_b32 s54, 6029312\n" \
"buffer_load_dwordx4 v32, s[40:43], s54 offen nt lds\n" \
"s_add_u32 m0, s62, 132096\n" \
"s_mov_b32 s54, 6029312\n" \
"buffer_load_dwordx4 v33, s[40:43], s54 offen nt lds\n" \
"s_add_u32 m0, s62, 133120\n" \
"s_mov_b32 s54, 6160384\n" \
"buffer_load_dwordx4 v32, s[40:43], s54 offen nt lds\n" \
"s_add_u32 m0, s62, 134144\n" \
"s_mov_b32 s54, 6160384\n" \
"buffer_load_dwordx4 v33, s[40:43], s54 offen nt lds\n" \
"s_waitcnt vmcnt(32)\n" \
"s_barrier\n" \
"ds_read_b64_tr_b16 a[48:49], v36 offset:49152\n" \
"ds_read_b64_tr_b16 a[50:51], v36 offset:49664\n" \
"ds_read_b64_tr_b16 a[56:57], v37 offset:49152\n" \
"ds_read_b64_tr_b16 a[58:59], v37 offset:49664\n" \
"ds_read_b64_tr_b16 v[128:129], v36 offset:57344\n" \
"ds_read_b64_tr_b16 v[130:131], v36 offset:57856\n" \
"ds_read_b64_tr_b16 v[136:137], v37 offset:57344\n" \
"ds_read_b64_tr_b16 v[138:139], v37 offset:57856\n" \
"ds_read_b64_tr_b16 a[52:53], v36 offset:32768\n" \
"ds_read_b64_tr_b16 a[54:55], v36 offset:33280\n" \
"ds_read_b64_tr_b16 a[60:61], v37 offset:32768\n" \
"ds_read_b64_tr_b16 a[62:63], v37 offset:33280\n" \
"ds_read_b64_tr_b16 v[132:133], v36 offset:40960\n" \
"ds_read_b64_tr_b16 v[134:135], v36 offset:41472\n" \
"ds_read_b64_tr_b16 v[140:141], v37 offset:40960\n" \
"ds_read_b64_tr_b16 v[142:143], v37 offset:41472\n" \
"s_waitcnt lgkmcnt(0)\n" \
"s_barrier\n" \
"s_add_u32 m0, s62, 114688\n" \
"s_mov_b32 s54, 6815744\n" \
"buffer_load_dwordx4 v32, s[40:43], s54 offen nt lds\n" \
"s_add_u32 m0, s62, 115712\n" \
"s_mov_b32 s54, 6815744\n" \
"buffer_load_dwordx4 v33, s[40:43], s54 offen nt lds\n" \
"s_add_u32 m0, s62, 116736\n" \
"s_mov_b32 s54, 6946816\n" \
"buffer_load_dwordx4 v32, s[40:43], s54 offen nt lds\n" \
"s_add_u32 m0, s62, 117760\n" \
"s_mov_b32 s54, 6946816\n" \
"buffer_load_dwordx4 v33, s[40:43], s54 offen nt lds\n" \
"s_add_u32 m0, s62, 98304\n" \
"s_mov_b32 s54, 7077888\n" \
"buffer_load_dwordx4 v32, s[40:43], s54 offen nt lds\n" \
"s_add_u32 m0, s62, 99328\n" \
"s_mov_b32 s54, 7077888\n" \
"buffer_load_dwordx4 v33, s[40:43], s54 offen nt lds\n" \
"s_add_u32 m0, s62, 100352\n" \
"s_mov_b32 s54, 7208960\n" \
"buffer_load_dwordx4 v32, s[40:43], s54 offen nt lds\n" \
"s_add_u32 m0, s62, 101376\n" \
"s_mov_b32 s54, 7208960\n" \
"buffer_load_dwordx4 v33, s[40:43], s54 offen nt lds\n" \
"s_waitcnt vmcnt(32)\n" \
"s_barrier\n" \
"ds_read_b64_tr_b16 a[80:81], v36 offset:16384\n" \
"ds_read_b64_tr_b16 a[82:83], v36 offset:16896\n" \
"ds_read_b64_tr_b16 a[88:89], v37 offset:16384\n" \
"ds_read_b64_tr_b16 a[90:91], v37 offset:16896\n" \
"ds_read_b64_tr_b16 v[144:145], v36 offset:24576\n" \
"ds_read_b64_tr_b16 v[146:147], v36 offset:25088\n" \
"ds_read_b64_tr_b16 v[152:153], v37 offset:24576\n" \
"ds_read_b64_tr_b16 v[154:155], v37 offset:25088\n" \
"ds_read_b64_tr_b16 a[84:85], v36 offset:0\n" \
"ds_read_b64_tr_b16 a[86:87], v36 offset:512\n" \
"ds_read_b64_tr_b16 a[92:93], v37 offset:0\n" \
"ds_read_b64_tr_b16 a[94:95], v37 offset:512\n" \
"ds_read_b64_tr_b16 v[148:149], v36 offset:8192\n" \
"ds_read_b64_tr_b16 v[150:151], v36 offset:8704\n" \
"ds_read_b64_tr_b16 v[156:157], v37 offset:8192\n" \
"ds_read_b64_tr_b16 v[158:159], v37 offset:8704\n" \
"s_waitcnt lgkmcnt(0)\n" \
"s_barrier\n" \
"s_add_u32 m0, s62, 81920\n" \
"s_mov_b32 s54, 7340032\n" \
"buffer_load_dwordx4 v32, s[40:43], s54 offen nt lds\n" \
"s_add_u32 m0, s62, 82944\n" \
"s_mov_b32 s54, 7340032\n" \
"buffer_load_dwordx4 v33, s[40:43], s54 offen nt lds\n" \
"s_add_u32 m0, s62, 83968\n" \
"s_mov_b32 s54, 7471104\n" \
"buffer_load_dwordx4 v32, s[40:43], s54 offen nt lds\n" \
"s_add_u32 m0, s62, 84992\n" \
"s_mov_b32 s54, 7471104\n" \
"buffer_load_dwordx4 v33, s[40:43], s54 offen nt lds\n" \
"s_add_u32 m0, s62, 65536\n" \
"s_mov_b32 s54, 7602176\n" \
"buffer_load_dwordx4 v32, s[40:43], s54 offen nt lds\n" \
"s_add_u32 m0, s62, 66560\n" \
"s_mov_b32 s54, 7602176\n" \
"buffer_load_dwordx4 v33, s[40:43], s54 offen nt lds\n" \
"s_add_u32 m0, s62, 67584\n" \
"s_mov_b32 s54, 7733248\n" \
"buffer_load_dwordx4 v32, s[40:43], s54 offen nt lds\n" \
"s_add_u32 m0, s62, 68608\n" \
"s_mov_b32 s54, 7733248\n" \
"buffer_load_dwordx4 v33, s[40:43], s54 offen nt lds\n" \
"s_waitcnt vmcnt(32)\n" \
"s_barrier\n" \
"ds_read_b64_tr_b16 a[112:113], v34 offset:49152\n" \
"ds_read_b64_tr_b16 a[114:115], v34 offset:49664\n" \
"ds_read_b64_tr_b16 a[120:121], v35 offset:49152\n" \
"ds_read_b64_tr_b16 a[122:123], v35 offset:49664\n" \
"ds_read_b64_tr_b16 v[160:161], v34 offset:57344\n" \
"ds_read_b64_tr_b16 v[162:163], v34 offset:57856\n" \
"ds_read_b64_tr_b16 v[168:169], v35 offset:57344\n" \
"ds_read_b64_tr_b16 v[170:171], v35 offset:57856\n" \
"ds_read_b64_tr_b16 a[116:117], v34 offset:32768\n" \
"ds_read_b64_tr_b16 a[118:119], v34 offset:33280\n" \
"ds_read_b64_tr_b16 a[124:125], v35 offset:32768\n" \
"ds_read_b64_tr_b16 a[126:127], v35 offset:33280\n" \
"ds_read_b64_tr_b16 v[164:165], v34 offset:40960\n" \
"ds_read_b64_tr_b16 v[166:167], v34 offset:41472\n" \
"ds_read_b64_tr_b16 v[172:173], v35 offset:40960\n" \
"ds_read_b64_tr_b16 v[174:175], v35 offset:41472\n" \
"s_waitcnt lgkmcnt(0)\n" \
"s_barrier\n" \
"s_add_u32 m0, s62, 49152\n" \
"s_mov_b32 s54, 7864320\n" \
"buffer_load_dwordx4 v32, s[40:43], s54 offen nt lds\n" \
"s_add_u32 m0, s62, 50176\n" \
"s_mov_b32 s54, 7864320\n" \
"buffer_load_dwordx4 v33, s[40:43], s54 offen nt lds\n" \
"s_add_u32 m0, s62, 51200\n" \
"s_mov_b32 s54, 7995392\n" \
"buffer_load_dwordx4 v32, s[40:43], s54 offen nt lds\n" \
"s_add_u32 m0, s62, 52224\n" \
"s_mov_b32 s54, 7995392\n" \
"buffer_load_dwordx4 v33, s[40:43], s54 offen nt lds\n" \
"s_add_u32 m0, s62, 32768\n" \
"s_mov_b32 s54, 8126464\n" \
"buffer_load_dwordx4 v32, s[40:43], s54 offen nt lds\n" \
"s_add_u32 m0, s62, 33792\n" \
"s_mov_b32 s54, 8126464\n" \
"buffer_load_dwordx4 v33, s[40:43], s54 offen nt lds\n" \
"s_add_u32 m0, s62, 34816\n" \
"s_mov_b32 s54, 8257536\n" \
"buffer_load_dwordx4 v32, s[40:43], s54 offen nt lds\n" \
"s_add_u32 m0, s62, 35840\n" \
"s_mov_b32 s54, 8257536\n" \
"buffer_load_dwordx4 v33, s[40:43], s54 offen nt lds\n" \
"s_waitcnt vmcnt(32)\n" \
"s_barrier\n" \
"ds_read_b64_tr_b16 a[144:145], v34 offset:16384\n" \
"ds_read_b64_tr_b16 a[146:147], v34 offset:16896\n" \
"ds_read_b64_tr_b16 a[152:153], v35 offset:16384\n" \
"ds_read_b64_tr_b16 a[154:155], v35 offset:16896\n" \
"ds_read_b64_tr_b16 v[176:177], v34 offset:24576\n" \
"ds_read_b64_tr_b16 v[178:179], v34 offset:25088\n" \
"ds_read_b64_tr_b16 v[184:185], v35 offset:24576\n" \
"ds_read_b64_tr_b16 v[186:187], v35 offset:25088\n" \
"ds_read_b64_tr_b16 a[148:149], v34 offset:0\n" \
"ds_read_b64_tr_b16 a[150:151], v34 offset:512\n" \
"ds_read_b64_tr_b16 a[156:157], v35 offset:0\n" \
"ds_read_b64_tr_b16 a[158:159], v35 offset:512\n" \
"ds_read_b64_tr_b16 v[180:181], v34 offset:8192\n" \
"ds_read_b64_tr_b16 v[182:183], v34 offset:8704\n" \
"ds_read_b64_tr_b16 v[188:189], v35 offset:8192\n" \
"ds_read_b64_tr_b16 v[190:191], v35 offset:8704\n" \
"s_waitcnt lgkmcnt(0)\n" \
"s_barrier\n" \
"s_add_u32 m0, s62, 0\n" \
"s_mov_b32 s54, 0\n" \
"buffer_load_dwordx4 v32, s[40:43], s54 offen nt lds\n" \
"s_add_u32 m0, s62, 1024\n" \
"s_mov_b32 s54, 0\n" \
"buffer_load_dwordx4 v33, s[40:43], s54 offen nt lds\n" \
"s_add_u32 m0, s62, 2048\n" \
"s_mov_b32 s54, 131072\n" \
"buffer_load_dwordx4 v32, s[40:43], s54 offen nt lds\n" \
"s_add_u32 m0, s62, 3072\n" \
"s_mov_b32 s54, 131072\n" \
"buffer_load_dwordx4 v33, s[40:43], s54 offen nt lds\n" \
"s_add_u32 m0, s62, 16384\n" \
"s_mov_b32 s54, 262144\n" \
"buffer_load_dwordx4 v32, s[40:43], s54 offen nt lds\n" \
"s_add_u32 m0, s62, 17408\n" \
"s_mov_b32 s54, 262144\n" \
"buffer_load_dwordx4 v33, s[40:43], s54 offen nt lds\n" \
"s_add_u32 m0, s62, 18432\n" \
"s_mov_b32 s54, 393216\n" \
"buffer_load_dwordx4 v32, s[40:43], s54 offen nt lds\n" \
"s_add_u32 m0, s62, 19456\n" \
"s_mov_b32 s54, 393216\n" \
"buffer_load_dwordx4 v33, s[40:43], s54 offen nt lds\n" \
"s_waitcnt vmcnt(32)\n" \
"s_barrier\n" \
"ds_read_b64_tr_b16 a[176:177], v38 offset:16384\n" \
"ds_read_b64_tr_b16 a[178:179], v38 offset:16896\n" \
"ds_read_b64_tr_b16 a[184:185], v39 offset:16384\n" \
"ds_read_b64_tr_b16 a[186:187], v39 offset:16896\n" \
"ds_read_b64_tr_b16 v[192:193], v38 offset:24576\n" \
"ds_read_b64_tr_b16 v[194:195], v38 offset:25088\n" \
"ds_read_b64_tr_b16 v[200:201], v39 offset:24576\n" \
"ds_read_b64_tr_b16 v[202:203], v39 offset:25088\n" \
"ds_read_b64_tr_b16 a[180:181], v38 offset:0\n" \
"ds_read_b64_tr_b16 a[182:183], v38 offset:512\n" \
"ds_read_b64_tr_b16 a[188:189], v39 offset:0\n" \
"ds_read_b64_tr_b16 a[190:191], v39 offset:512\n" \
"ds_read_b64_tr_b16 v[196:197], v38 offset:8192\n" \
"ds_read_b64_tr_b16 v[198:199], v38 offset:8704\n" \
"ds_read_b64_tr_b16 v[204:205], v39 offset:8192\n" \
"ds_read_b64_tr_b16 v[206:207], v39 offset:8704\n" \
"s_waitcnt lgkmcnt(0)\n" \
"s_barrier\n" \
"s_waitcnt vmcnt(24)\n" \
"s_barrier\n" \
"ds_read_b64_tr_b16 a[208:209], v36 offset:49152\n" \
"ds_read_b64_tr_b16 a[210:211], v36 offset:49664\n" \
"ds_read_b64_tr_b16 a[216:217], v37 offset:49152\n" \
"ds_read_b64_tr_b16 a[218:219], v37 offset:49664\n" \
"ds_read_b64_tr_b16 v[208:209], v36 offset:57344\n" \
"ds_read_b64_tr_b16 v[210:211], v36 offset:57856\n" \
"ds_read_b64_tr_b16 v[216:217], v37 offset:57344\n" \
"ds_read_b64_tr_b16 v[218:219], v37 offset:57856\n" \
"ds_read_b64_tr_b16 a[212:213], v36 offset:32768\n" \
"ds_read_b64_tr_b16 a[214:215], v36 offset:33280\n" \
"ds_read_b64_tr_b16 a[220:221], v37 offset:32768\n" \
"ds_read_b64_tr_b16 a[222:223], v37 offset:33280\n" \
"ds_read_b64_tr_b16 v[212:213], v36 offset:40960\n" \
"ds_read_b64_tr_b16 v[214:215], v36 offset:41472\n" \
"ds_read_b64_tr_b16 v[220:221], v37 offset:40960\n" \
"ds_read_b64_tr_b16 v[222:223], v37 offset:41472\n" \
"s_waitcnt lgkmcnt(0)\n" \
"s_barrier\n" \
"s_add_u32 m0, s62, 98304\n" \
"s_mov_b32 s54, 1048576\n" \
"buffer_load_dwordx4 v32, s[40:43], s54 offen nt lds\n" \
"s_add_u32 m0, s62, 99328\n" \
"s_mov_b32 s54, 1048576\n" \
"buffer_load_dwordx4 v33, s[40:43], s54 offen nt lds\n" \
"s_add_u32 m0, s62, 100352\n" \
"s_mov_b32 s54, 1179648\n" \
"buffer_load_dwordx4 v32, s[40:43], s54 offen nt lds\n" \
"s_add_u32 m0, s62, 101376\n" \
"s_mov_b32 s54, 1179648\n" \
"buffer_load_dwordx4 v33, s[40:43], s54 offen nt lds\n" \
"s_add_u32 m0, s62, 147456\n" \
"s_mov_b32 s54, 1310720\n" \
"buffer_load_dwordx4 v32, s[40:43], s54 offen nt lds\n" \
"s_add_u32 m0, s62, 148480\n" \
"s_mov_b32 s54, 1310720\n" \
"buffer_load_dwordx4 v33, s[40:43], s54 offen nt lds\n" \
"s_add_u32 m0, s62, 149504\n" \
"s_mov_b32 s54, 1441792\n" \
"buffer_load_dwordx4 v32, s[40:43], s54 offen nt lds\n" \
"s_add_u32 m0, s62, 150528\n" \
"s_mov_b32 s54, 1441792\n" \
"buffer_load_dwordx4 v33, s[40:43], s54 offen nt lds\n" \
"s_waitcnt vmcnt(24)\n" \
"s_barrier\n" \
"ds_read_b64_tr_b16 a[224:225], v36 offset:16384\n" \
"ds_read_b64_tr_b16 a[226:227], v36 offset:16896\n" \
"ds_read_b64_tr_b16 a[232:233], v37 offset:16384\n" \
"ds_read_b64_tr_b16 a[234:235], v37 offset:16896\n" \
"ds_read_b64_tr_b16 v[224:225], v36 offset:24576\n" \
"ds_read_b64_tr_b16 v[226:227], v36 offset:25088\n" \
"ds_read_b64_tr_b16 v[232:233], v37 offset:24576\n" \
"ds_read_b64_tr_b16 v[234:235], v37 offset:25088\n" \
"ds_read_b64_tr_b16 a[228:229], v36 offset:0\n" \
"ds_read_b64_tr_b16 a[230:231], v36 offset:512\n" \
"ds_read_b64_tr_b16 a[236:237], v37 offset:0\n" \
"ds_read_b64_tr_b16 a[238:239], v37 offset:512\n" \
"ds_read_b64_tr_b16 v[228:229], v36 offset:8192\n" \
"ds_read_b64_tr_b16 v[230:231], v36 offset:8704\n" \
"ds_read_b64_tr_b16 v[236:237], v37 offset:8192\n" \
"ds_read_b64_tr_b16 v[238:239], v37 offset:8704\n" \
"s_waitcnt lgkmcnt(0)\n" \
"s_barrier\n" \
"s_add_u32 m0, s62, 65536\n" \
"s_mov_b32 s54, 2097152\n" \
"buffer_load_dwordx4 v32, s[40:43], s54 offen nt lds\n" \
"s_add_u32 m0, s62, 66560\n" \
"s_mov_b32 s54, 2097152\n" \
"buffer_load_dwordx4 v33, s[40:43], s54 offen nt lds\n" \
"s_add_u32 m0, s62, 67584\n" \
"s_mov_b32 s54, 2228224\n" \
"buffer_load_dwordx4 v32, s[40:43], s54 offen nt lds\n" \
"s_add_u32 m0, s62, 68608\n" \
"s_mov_b32 s54, 2228224\n" \
"buffer_load_dwordx4 v33, s[40:43], s54 offen nt lds\n" \
"s_add_u32 m0, s62, 131072\n" \
"s_mov_b32 s54, 2359296\n" \
"buffer_load_dwordx4 v32, s[40:43], s54 offen nt lds\n" \
"s_add_u32 m0, s62, 132096\n" \
"s_mov_b32 s54, 2359296\n" \
"buffer_load_dwordx4 v33, s[40:43], s54 offen nt lds\n" \
"s_add_u32 m0, s62, 133120\n" \
"s_mov_b32 s54, 2490368\n" \
"buffer_load_dwordx4 v32, s[40:43], s54 offen nt lds\n" \
"s_add_u32 m0, s62, 134144\n" \
"s_mov_b32 s54, 2490368\n" \
"buffer_load_dwordx4 v33, s[40:43], s54 offen nt lds\n" \
"s_add_u32 m0, s62, 81920\n" \
"s_mov_b32 s54, 3145728\n" \
"buffer_load_dwordx4 v32, s[40:43], s54 offen nt lds\n" \
"s_add_u32 m0, s62, 82944\n" \
"s_mov_b32 s54, 3145728\n" \
"buffer_load_dwordx4 v33, s[40:43], s54 offen nt lds\n" \
"s_add_u32 m0, s62, 83968\n" \
"s_mov_b32 s54, 3276800\n" \
"buffer_load_dwordx4 v32, s[40:43], s54 offen nt lds\n" \
"s_add_u32 m0, s62, 84992\n" \
"s_mov_b32 s54, 3276800\n" \
"buffer_load_dwordx4 v33, s[40:43], s54 offen nt lds\n" \
"s_add_u32 m0, s62, 114688\n" \
"s_mov_b32 s54, 3407872\n" \
"buffer_load_dwordx4 v32, s[40:43], s54 offen nt lds\n" \
"s_add_u32 m0, s62, 115712\n" \
"s_mov_b32 s54, 3407872\n" \
"buffer_load_dwordx4 v33, s[40:43], s54 offen nt lds\n" \
"s_add_u32 m0, s62, 116736\n" \
"s_mov_b32 s54, 3538944\n" \
"buffer_load_dwordx4 v32, s[40:43], s54 offen nt lds\n" \
"s_add_u32 m0, s62, 117760\n" \
"s_mov_b32 s54, 3538944\n" \
"buffer_load_dwordx4 v33, s[40:43], s54 offen nt lds\n" \
"s_waitcnt vmcnt(32)\n" \
"s_barrier\n" \
"ds_read_b64_tr_b16 a[240:241], v34 offset:49152\n" \
"ds_read_b64_tr_b16 a[242:243], v34 offset:49664\n" \
"ds_read_b64_tr_b16 a[248:249], v35 offset:49152\n" \
"ds_read_b64_tr_b16 a[250:251], v35 offset:49664\n" \
"ds_read_b64_tr_b16 v[240:241], v34 offset:57344\n" \
"ds_read_b64_tr_b16 v[242:243], v34 offset:57856\n" \
"ds_read_b64_tr_b16 v[248:249], v35 offset:57344\n" \
"ds_read_b64_tr_b16 v[250:251], v35 offset:57856\n" \
"ds_read_b64_tr_b16 a[244:245], v34 offset:32768\n" \
"ds_read_b64_tr_b16 a[246:247], v34 offset:33280\n" \
"ds_read_b64_tr_b16 a[252:253], v35 offset:32768\n" \
"ds_read_b64_tr_b16 a[254:255], v35 offset:33280\n" \
"ds_read_b64_tr_b16 v[244:245], v34 offset:40960\n" \
"ds_read_b64_tr_b16 v[246:247], v34 offset:41472\n" \
"ds_read_b64_tr_b16 v[252:253], v35 offset:40960\n" \
"ds_read_b64_tr_b16 v[254:255], v35 offset:41472\n" \
"s_waitcnt lgkmcnt(0)\n" \
"s_barrier\n" \
"s_add_u32 m0, s62, 32768\n" \
"s_mov_b32 s54, 4194304\n" \
"buffer_load_dwordx4 v32, s[40:43], s54 offen nt lds\n" \
"s_add_u32 m0, s62, 33792\n" \
"s_mov_b32 s54, 4194304\n" \
"buffer_load_dwordx4 v33, s[40:43], s54 offen nt lds\n" \
"s_add_u32 m0, s62, 34816\n" \
"s_mov_b32 s54, 4325376\n" \
"buffer_load_dwordx4 v32, s[40:43], s54 offen nt lds\n" \
"s_add_u32 m0, s62, 35840\n" \
"s_mov_b32 s54, 4325376\n" \
"buffer_load_dwordx4 v33, s[40:43], s54 offen nt lds\n" \
"s_add_u32 m0, s62, 49152\n" \
"s_mov_b32 s54, 4456448\n" \
"buffer_load_dwordx4 v32, s[40:43], s54 offen nt lds\n" \
"s_add_u32 m0, s62, 50176\n" \
"s_mov_b32 s54, 4456448\n" \
"buffer_load_dwordx4 v33, s[40:43], s54 offen nt lds\n" \
"s_add_u32 m0, s62, 51200\n" \
"s_mov_b32 s54, 4587520\n" \
"buffer_load_dwordx4 v32, s[40:43], s54 offen nt lds\n" \
"s_add_u32 m0, s62, 52224\n" \
"s_mov_b32 s54, 4587520\n" \
"buffer_load_dwordx4 v33, s[40:43], s54 offen nt lds\n" \
"s_waitcnt vmcnt(32)\n" \
"s_barrier\n" \
"ds_read_b64_tr_b16 a[0:1], v34 offset:0\n" \
"ds_read_b64_tr_b16 a[2:3], v34 offset:512\n" \
"ds_read_b64_tr_b16 a[8:9], v35 offset:0\n" \
"ds_read_b64_tr_b16 a[10:11], v35 offset:512\n" \
"ds_read_b64_tr_b16 v[48:49], v34 offset:8192\n" \
"ds_read_b64_tr_b16 v[50:51], v34 offset:8704\n" \
"ds_read_b64_tr_b16 v[56:57], v35 offset:8192\n" \
"ds_read_b64_tr_b16 v[58:59], v35 offset:8704\n" \
"ds_read_b64_tr_b16 a[4:5], v34 offset:16384\n" \
"ds_read_b64_tr_b16 a[6:7], v34 offset:16896\n" \
"ds_read_b64_tr_b16 a[12:13], v35 offset:16384\n" \
"ds_read_b64_tr_b16 a[14:15], v35 offset:16896\n" \
"ds_read_b64_tr_b16 v[52:53], v34 offset:24576\n" \
"ds_read_b64_tr_b16 v[54:55], v34 offset:25088\n" \
"ds_read_b64_tr_b16 v[60:61], v35 offset:24576\n" \
"ds_read_b64_tr_b16 v[62:63], v35 offset:25088\n" \
"s_waitcnt lgkmcnt(0)\n" \
"s_barrier\n" \
"ds_write_b128 v1, v[48:51] offset:0\n" \
"ds_write_b128 v1, v[52:55] offset:1024\n" \
"ds_write_b128 v1, v[56:59] offset:2048\n" \
"ds_write_b128 v1, v[60:63] offset:3072\n" \
"s_add_u32 m0, s62, 16384\n" \
"s_mov_b32 s54, 5242880\n" \
"buffer_load_dwordx4 v32, s[40:43], s54 offen nt lds\n" \
"s_add_u32 m0, s62, 17408\n" \
"s_mov_b32 s54, 5242880\n" \
"buffer_load_dwordx4 v33, s[40:43], s54 offen nt lds\n" \
"s_add_u32 m0, s62, 18432\n" \
"s_mov_b32 s54, 5373952\n" \
"buffer_load_dwordx4 v32, s[40:43], s54 offen nt lds\n" \
"s_add_u32 m0, s62, 19456\n" \
"s_mov_b32 s54, 5373952\n" \
"buffer_load_dwordx4 v33, s[40:43], s54 offen nt lds\n" \
"s_waitcnt vmcnt(28)\n" \
"s_barrier\n" \
"ds_read_b64_tr_b16 a[32:33], v36 offset:32768\n" \
"ds_read_b64_tr_b16 a[34:35], v36 offset:33280\n" \
"ds_read_b64_tr_b16 a[40:41], v37 offset:32768\n" \
"ds_read_b64_tr_b16 a[42:43], v37 offset:33280\n" \
"ds_read_b64_tr_b16 v[48:49], v36 offset:40960\n" \
"ds_read_b64_tr_b16 v[50:51], v36 offset:41472\n" \
"ds_read_b64_tr_b16 v[56:57], v37 offset:40960\n" \
"ds_read_b64_tr_b16 v[58:59], v37 offset:41472\n" \
"ds_read_b64_tr_b16 a[36:37], v38 offset:16384\n" \
"ds_read_b64_tr_b16 a[38:39], v38 offset:16896\n" \
"ds_read_b64_tr_b16 a[44:45], v39 offset:16384\n" \
"ds_read_b64_tr_b16 a[46:47], v39 offset:16896\n" \
"ds_read_b64_tr_b16 v[52:53], v38 offset:24576\n" \
"ds_read_b64_tr_b16 v[54:55], v38 offset:25088\n" \
"ds_read_b64_tr_b16 v[60:61], v39 offset:24576\n" \
"ds_read_b64_tr_b16 v[62:63], v39 offset:25088\n" \
"s_waitcnt lgkmcnt(0)\n" \
"s_barrier\n" \
"ds_write_b128 v2, v[48:51] offset:32768\n" \
"ds_write_b128 v2, v[52:55] offset:33792\n" \
"ds_write_b128 v2, v[56:59] offset:34816\n" \
"ds_write_b128 v2, v[60:63] offset:35840\n" \
"s_add_u32 m0, s62, 147456\n" \
"s_mov_b32 s54, 5505024\n" \
"buffer_load_dwordx4 v32, s[40:43], s54 offen nt lds\n" \
"s_add_u32 m0, s62, 148480\n" \
"s_mov_b32 s54, 5505024\n" \
"buffer_load_dwordx4 v33, s[40:43], s54 offen nt lds\n" \
"s_add_u32 m0, s62, 149504\n" \
"s_mov_b32 s54, 5636096\n" \
"buffer_load_dwordx4 v32, s[40:43], s54 offen nt lds\n" \
"s_add_u32 m0, s62, 150528\n" \
"s_mov_b32 s54, 5636096\n" \
"buffer_load_dwordx4 v33, s[40:43], s54 offen nt lds\n" \
"s_waitcnt vmcnt(24)\n" \
"s_barrier\n" \
"ds_read_b64_tr_b16 a[64:65], v36 offset:0\n" \
"ds_read_b64_tr_b16 a[66:67], v36 offset:512\n" \
"ds_read_b64_tr_b16 a[72:73], v37 offset:0\n" \
"ds_read_b64_tr_b16 a[74:75], v37 offset:512\n" \
"ds_read_b64_tr_b16 v[48:49], v36 offset:8192\n" \
"ds_read_b64_tr_b16 v[50:51], v36 offset:8704\n" \
"ds_read_b64_tr_b16 v[56:57], v37 offset:8192\n" \
"ds_read_b64_tr_b16 v[58:59], v37 offset:8704\n" \
"ds_read_b64_tr_b16 a[68:69], v38 offset:0\n" \
"ds_read_b64_tr_b16 a[70:71], v38 offset:512\n" \
"ds_read_b64_tr_b16 a[76:77], v39 offset:0\n" \
"ds_read_b64_tr_b16 a[78:79], v39 offset:512\n" \
"ds_read_b64_tr_b16 v[52:53], v38 offset:8192\n" \
"ds_read_b64_tr_b16 v[54:55], v38 offset:8704\n" \
"ds_read_b64_tr_b16 v[60:61], v39 offset:8192\n" \
"ds_read_b64_tr_b16 v[62:63], v39 offset:8704\n" \
"s_waitcnt lgkmcnt(0)\n" \
"s_barrier\n" \
"ds_write_b128 v2, v[48:51] offset:0\n" \
"ds_write_b128 v2, v[52:55] offset:1024\n" \
"ds_write_b128 v2, v[56:59] offset:2048\n" \
"ds_write_b128 v2, v[60:63] offset:3072\n" \
"s_waitcnt vmcnt(16)\n" \
"s_barrier\n" \
"ds_read_b64_tr_b16 a[96:97], v36 offset:16384\n" \
"ds_read_b64_tr_b16 a[98:99], v36 offset:16896\n" \
"ds_read_b64_tr_b16 a[104:105], v37 offset:16384\n" \
"ds_read_b64_tr_b16 a[106:107], v37 offset:16896\n" \
"ds_read_b64_tr_b16 v[48:49], v36 offset:24576\n" \
"ds_read_b64_tr_b16 v[50:51], v36 offset:25088\n" \
"ds_read_b64_tr_b16 v[56:57], v37 offset:24576\n" \
"ds_read_b64_tr_b16 v[58:59], v37 offset:25088\n" \
"ds_read_b64_tr_b16 a[100:101], v36 offset:49152\n" \
"ds_read_b64_tr_b16 a[102:103], v36 offset:49664\n" \
"ds_read_b64_tr_b16 a[108:109], v37 offset:49152\n" \
"ds_read_b64_tr_b16 a[110:111], v37 offset:49664\n" \
"ds_read_b64_tr_b16 v[52:53], v36 offset:57344\n" \
"ds_read_b64_tr_b16 v[54:55], v36 offset:57856\n" \
"ds_read_b64_tr_b16 v[60:61], v37 offset:57344\n" \
"ds_read_b64_tr_b16 v[62:63], v37 offset:57856\n" \
"s_waitcnt lgkmcnt(0)\n" \
"s_barrier\n" \
"ds_write_b128 v2, v[48:51] offset:16384\n" \
"ds_write_b128 v2, v[52:55] offset:17408\n" \
"ds_write_b128 v2, v[56:59] offset:18432\n" \
"ds_write_b128 v2, v[60:63] offset:19456\n" \
"s_waitcnt vmcnt(8)\n" \
"s_barrier\n" \
"ds_read_b64_tr_b16 a[128:129], v34 offset:32768\n" \
"ds_read_b64_tr_b16 a[130:131], v34 offset:33280\n" \
"ds_read_b64_tr_b16 a[136:137], v35 offset:32768\n" \
"ds_read_b64_tr_b16 a[138:139], v35 offset:33280\n" \
"ds_read_b64_tr_b16 v[48:49], v34 offset:40960\n" \
"ds_read_b64_tr_b16 v[50:51], v34 offset:41472\n" \
"ds_read_b64_tr_b16 v[56:57], v35 offset:40960\n" \
"ds_read_b64_tr_b16 v[58:59], v35 offset:41472\n" \
"ds_read_b64_tr_b16 a[132:133], v34 offset:49152\n" \
"ds_read_b64_tr_b16 a[134:135], v34 offset:49664\n" \
"ds_read_b64_tr_b16 a[140:141], v35 offset:49152\n" \
"ds_read_b64_tr_b16 a[142:143], v35 offset:49664\n" \
"ds_read_b64_tr_b16 v[52:53], v34 offset:57344\n" \
"ds_read_b64_tr_b16 v[54:55], v34 offset:57856\n" \
"ds_read_b64_tr_b16 v[60:61], v35 offset:57344\n" \
"ds_read_b64_tr_b16 v[62:63], v35 offset:57856\n" \
"s_waitcnt lgkmcnt(0)\n" \
"s_barrier\n" \
"ds_write_b128 v1, v[48:51] offset:32768\n" \
"ds_write_b128 v1, v[52:55] offset:33792\n" \
"ds_write_b128 v1, v[56:59] offset:34816\n" \
"ds_write_b128 v1, v[60:63] offset:35840\n" \
"s_add_u32 m0, s62, 49152\n" \
"s_mov_b32 s54, 6291456\n" \
"buffer_load_dwordx4 v32, s[40:43], s54 offen nt lds\n" \
"s_add_u32 m0, s62, 50176\n" \
"s_mov_b32 s54, 6291456\n" \
"buffer_load_dwordx4 v33, s[40:43], s54 offen nt lds\n" \
"s_add_u32 m0, s62, 51200\n" \
"s_mov_b32 s54, 6422528\n" \
"buffer_load_dwordx4 v32, s[40:43], s54 offen nt lds\n" \
"s_add_u32 m0, s62, 52224\n" \
"s_mov_b32 s54, 6422528\n" \
"buffer_load_dwordx4 v33, s[40:43], s54 offen nt lds\n" \
"s_add_u32 m0, s62, 131072\n" \
"s_mov_b32 s54, 6553600\n" \
"buffer_load_dwordx4 v32, s[40:43], s54 offen nt lds\n" \
"s_add_u32 m0, s62, 132096\n" \
"s_mov_b32 s54, 6553600\n" \
"buffer_load_dwordx4 v33, s[40:43], s54 offen nt lds\n" \
"s_add_u32 m0, s62, 133120\n" \
"s_mov_b32 s54, 6684672\n" \
"buffer_load_dwordx4 v32, s[40:43], s54 offen nt lds\n" \
"s_add_u32 m0, s62, 134144\n" \
"s_mov_b32 s54, 6684672\n" \
"buffer_load_dwordx4 v33, s[40:43], s54 offen nt lds\n" \
"s_waitcnt vmcnt(8)\n" \
"s_barrier\n" \
"ds_read_b64_tr_b16 a[160:161], v34 offset:16384\n" \
"ds_read_b64_tr_b16 a[162:163], v34 offset:16896\n" \
"ds_read_b64_tr_b16 a[168:169], v35 offset:16384\n" \
"ds_read_b64_tr_b16 a[170:171], v35 offset:16896\n" \
"ds_read_b64_tr_b16 v[48:49], v34 offset:24576\n" \
"ds_read_b64_tr_b16 v[50:51], v34 offset:25088\n" \
"ds_read_b64_tr_b16 v[56:57], v35 offset:24576\n" \
"ds_read_b64_tr_b16 v[58:59], v35 offset:25088\n" \
"ds_read_b64_tr_b16 a[164:165], v38 offset:16384\n" \
"ds_read_b64_tr_b16 a[166:167], v38 offset:16896\n" \
"ds_read_b64_tr_b16 a[172:173], v39 offset:16384\n" \
"ds_read_b64_tr_b16 a[174:175], v39 offset:16896\n" \
"ds_read_b64_tr_b16 v[52:53], v38 offset:24576\n" \
"ds_read_b64_tr_b16 v[54:55], v38 offset:25088\n" \
"ds_read_b64_tr_b16 v[60:61], v39 offset:24576\n" \
"ds_read_b64_tr_b16 v[62:63], v39 offset:25088\n" \
"s_waitcnt lgkmcnt(0)\n" \
"s_barrier\n" \
"ds_write_b128 v1, v[48:51] offset:16384\n" \
"ds_write_b128 v1, v[52:55] offset:17408\n" \
"ds_write_b128 v1, v[56:59] offset:18432\n" \
"ds_write_b128 v1, v[60:63] offset:19456\n" \
"s_waitcnt vmcnt(0)\n" \
"s_barrier\n" \
"ds_read_b64_tr_b16 a[192:193], v34 offset:49152\n" \
"ds_read_b64_tr_b16 a[194:195], v34 offset:49664\n" \
"ds_read_b64_tr_b16 a[200:201], v35 offset:49152\n" \
"ds_read_b64_tr_b16 a[202:203], v35 offset:49664\n" \
"ds_read_b64_tr_b16 v[48:49], v34 offset:57344\n" \
"ds_read_b64_tr_b16 v[50:51], v34 offset:57856\n" \
"ds_read_b64_tr_b16 v[56:57], v35 offset:57344\n" \
"ds_read_b64_tr_b16 v[58:59], v35 offset:57856\n" \
"ds_read_b64_tr_b16 a[196:197], v38 offset:0\n" \
"ds_read_b64_tr_b16 a[198:199], v38 offset:512\n" \
"ds_read_b64_tr_b16 a[204:205], v39 offset:0\n" \
"ds_read_b64_tr_b16 a[206:207], v39 offset:512\n" \
"ds_read_b64_tr_b16 v[52:53], v38 offset:8192\n" \
"ds_read_b64_tr_b16 v[54:55], v38 offset:8704\n" \
"ds_read_b64_tr_b16 v[60:61], v39 offset:8192\n" \
"ds_read_b64_tr_b16 v[62:63], v39 offset:8704\n" \
"s_waitcnt lgkmcnt(0)\n" \
"s_barrier\n" \
"ds_write_b128 v1, v[48:51] offset:49152\n" \
"ds_write_b128 v1, v[52:55] offset:50176\n" \
"ds_write_b128 v1, v[56:59] offset:51200\n" \
"ds_write_b128 v1, v[60:63] offset:52224\n" \
"s_waitcnt vmcnt(0) lgkmcnt(0)\n" \
"s_barrier\n" \
"s_lshl_b32 s62, s48, 11\n" \
"s_add_u32 s63, s63, s62\n" \
"s_mov_b32 s49, 0\n" \
"s_mov_b32 s52, 0\n" \
"s_mov_b32 s81, 0\n" \
"s_mov_b32 s88, 0\n" \
"s_mov_b32 s95, 0x7ffffff0\n" \
"s_mov_b32 s98, 0x3e0293ee\n" \
"s_mov_b32 s99, 0x3e0293ee\n" \
"v_mov_b32 v16, v13\n" \
"v_mov_b32 v20, 0x3f803f80\n" \
"v_mov_b32 v17, v13\n" \
"v_mov_b32 v21, 0x3f803f80\n" \
"v_mov_b32 v18, v13\n" \
"v_mov_b32 v22, 0x3f803f80\n" \
"v_mov_b32 v19, v13\n" \
"v_mov_b32 v23, 0x3f803f80\n" \
"v_mov_b32 v24, 0\n" \
"v_mov_b32 v25, 0\n" \
"v_mov_b32 v26, 0\n" \
"v_mov_b32 v27, 0\n" \
"v_mov_b32 v28, 0\n" \
"v_mov_b32 v29, 0\n" \
"v_cmp_le_i32_e64 s[64:65], 0, v14\n" \
"v_cmp_le_i32_e64 s[66:67], 1, v14\n" \
"v_cmp_le_i32_e64 s[68:69], 2, v14\n" \
"v_cmp_le_i32_e64 s[70:71], 3, v14\n" \
"v_cmp_le_i32_e64 s[72:73], 16, v14\n" \
"v_cmp_le_i32_e64 s[74:75], 17, v14\n" \
"v_cmp_le_i32_e64 s[76:77], 18, v14\n" \
"v_cmp_le_i32_e64 s[78:79], 19, v14\n" \
"s_mov_b32 s82, 0\n" \
"s_mov_b32 s83, -1\n" \
"s_mov_b32 s22, 0xfffffc00\n" \
"s_mov_b32 s23, -1\n" \
"s_mul_i32 s32, s46, 0x1010101\n" \
"s_mov_b32 s33, s32\n" \
"s_mov_b32 s34, s46\n" \
"s_mov_b32 s35, s17\n" \
"s_mov_b32 s54, 0\n" \
"buffer_store_dwordx4 v[24:27], v7, s[36:39], s54 offen sc1\n" \
"s_mov_b32 s54, 327680\n" \
"buffer_store_dwordx4 v[24:27], v7, s[36:39], s54 offen sc1\n" \
"s_add_u32 s54, s54, s94\n" \
"buffer_store_dword v24, v10, s[36:39], s54 offen sc1\n" \
"s_mov_b32 s54, 655360\n" \
"buffer_store_dwordx4 v[24:27], v7, s[36:39], s54 offen sc1\n" \
"s_add_u32 s54, s54, s94\n" \
"buffer_store_dword v24, v10, s[36:39], s54 offen sc1\n" \
"s_mov_b32 s54, 983040\n" \
"buffer_store_dwordx4 v[24:27], v7, s[36:39], s54 offen sc1\n" \
"s_add_u32 s54, s54, s94\n" \
"buffer_store_dword v24, v10, s[36:39], s54 offen sc1\n" \
"buffer_load_dword v13, v15, s[36:39], s80 offen sc1\n" \
"s_waitcnt vmcnt(0)\n" \
"v_cmp_ge_u32_e64 s[96:97], s81, v13\n" \
"s_nop 4\n" \
"s_and_b32 s96, s96, 7\n" \
"s_cbranch_scc1 .Lq_f0_cold_%=\n" \
".Lq_f0_ok_%=:\n" \
"s_add_u32 m0, s62, 131072\n" \
"s_add_u32 s54, s63, 10371072\n" \
"buffer_load_dwordx4 v3, s[36:39], s54 offen offset:0 sc1 lds\n" \
"buffer_load_dwordx4 v3, s[36:39], s54 offen offset:1024 sc1 lds\n" \
"s_add_u32 m0, s62, 139264\n" \
"s_add_u32 s54, s63, 10379264\n" \
"buffer_load_dwordx4 v3, s[36:39], s54 offen offset:0 sc1 lds\n" \
"buffer_load_dwordx4 v3, s[36:39], s54 offen offset:1024 sc1 lds\n" \
"s_add_u32 m0, s62, 147456\n" \
"s_add_u32 s54, s63, 10387456\n" \
"buffer_load_dwordx4 v3, s[36:39], s54 offen offset:0 sc1 lds\n" \
"buffer_load_dwordx4 v3, s[36:39], s54 offen offset:1024 sc1 lds\n" \
"s_memrealtime s[56:57]\n" \
"s_waitcnt lgkmcnt(0)\n" \
"s_sub_u32 s50, s56, 20\n" \
"s_waitcnt vmcnt(0) lgkmcnt(0)\n" \
"s_nop 7\n" \
"s_nop 7\n" \
".p2align 6\n" \
".Lq_loop_%=:\n" \
"s_add_u32 s54, s49, 0\n" \
"s_and_b32 s54, s54, 3\n" \
"s_mul_i32 s58, s54, 327680\n" \
"s_add_u32 s54, s49, 1\n" \
"s_and_b32 s54, s54, 3\n" \
"s_mul_i32 s59, s54, 327680\n" \
"s_add_u32 s54, s49, 2\n" \
"s_and_b32 s54, s54, 3\n" \
"s_mul_i32 s60, s54, 327680\n" \
"s_add_u32 s54, s49, 3\n" \
"s_and_b32 s54, s54, 3\n" \
"s_mul_i32 s61, s54, 327680\n" \
"ds_read_b128 v[32:35], v1 offset:0\n" \
"ds_read_b128 v[36:39], v1 offset:1024\n" \
"ds_read_b128 v[40:43], v1 offset:2048\n" \
"ds_read_b128 v[44:47], v1 offset:3072\n" \
"ds_read_b128 v[48:51], v2 offset:32768\n" \
"ds_read_b128 v[52:55], v2 offset:33792\n" \
"ds_read_b128 v[56:59], v2 offset:34816\n" \
"ds_read_b128 v[60:63], v2 offset:35840\n" \
"v_mov_b64 v[80:81], 0\n" \
"v_mov_b64 v[82:83], 0\n" \
"v_mov_b64 v[84:85], 0\n" \
"v_mov_b64 v[86:87], 0\n" \
"v_mov_b64 v[88:89], 0\n" \
"v_mov_b64 v[90:91], 0\n" \
"v_mov_b64 v[92:93], 0\n" \
"v_mov_b64 v[94:95], 0\n" \
"v_mov_b32 v13, -1\n" \
"v_add_u32 v14, s62, v3\n" \
"v_xor_b32 v31, 0x400, v11\n" \
"s_max_u32 s53, s49, 1\n" \
"s_sub_u32 s53, s53, 1\n" \
"v_mov_b32 v30, s53\n" \
"s_waitcnt lgkmcnt(0)\n" \
"s_lshl_b32 s55, s48, 10\n" \
"s_add_u32 s55, s55, 114688\n" \
"s_add_u32 s89, s58, 4096\n" \
"s_add_u32 s90, s58, 16384\n" \
"s_add_u32 s91, s58, 20480\n" \
"s_add_u32 s92, s58, s94\n" \
"s_add_u32 s93, s60, s94\n" \
"s_add_u32 s81, s49, 1\n" \
"s_min_u32 s81, s81, 63\n" \
"s_mov_b32 m0, s55\n" \
"s_getpc_b64 s[18:19]\n" \
".Lq_wp_%=:\n" \
"s_add_u32 s18, s18, .Lq_ws_%=-.Lq_wp_%=\n" \
"s_addc_u32 s19, s19, 0\n" \
"s_sub_u32 s21, s50, 5\n" \
"s_nop 0\n" \
"s_nop 0\n" \
"s_nop 0\n" \
".Lq_w_%=:\n" \
"s_memrealtime s[56:57]\n" \
"s_waitcnt lgkmcnt(0)\n" \
"s_sub_u32 s54, s56, s21\n" \
"s_cmp_gt_u32 s54, s22\n" \
"s_cbranch_scc1 .Lq_w_%=\n" \
"s_min_u32 s21, s54, 3\n" \
"s_lshl2_add_u32 s18, s21, s18\n" \
"s_setpc_b64 s[18:19]\n" \
"s_nop 0\n" \
".Lq_ws_%=:\n" \
"s_nop 4\n" \
"s_nop 5\n" \
"s_nop 4\n" \
".Lq_go_%=:\n" \
"buffer_load_dwordx4 v4, s[36:39], s58 offen sc1 lds\n" \
"s_nop 7\n" \
"s_nop 7\n" \
"s_nop 7\n" \
"s_add_u32 m0, s55, 4096\n" \
"s_nop 0\n" \
"buffer_load_dwordx4 v4, s[36:39], s89 offen sc1 lds\n" \
"s_nop 7\n" \
"s_nop 7\n" \
"s_add_u32 m0, s55, 8192\n" \
"s_nop 0\n" \
"buffer_load_dwordx4 v4, s[36:39], s90 offen sc1 lds\n" \
"s_nop 7\n" \
"s_add_u32 m0, s55, 12288\n" \
"s_nop 0\n" \
"buffer_load_dwordx4 v4, s[36:39], s91 offen sc1 lds\n" \
"s_cmp_lt_u32 s54, 8\n" \
"s_cselect_b32 s51, s50, s56\n" \
"s_cselect_b32 s23, s23, s49\n" \
"s_cselect_b32 s53, 0, s14\n" \
"s_add_u32 s53, s53, s51\n" \
"s_add_u32 s53, s53, s34\n" \
"s_max_u32 s53, s53, 1\n" \
"v_mov_b32 v104, s53\n" \
"v_mov_b32_e64 v105, s35\n" \
"buffer_store_dword v30, v10, s[36:39], s87 offen sc1\n" \
"buffer_load_dword v30, v15, s[36:39], s80 offen sc1\n" \
"s_waitcnt vmcnt(5)\n" \
"s_barrier\n" \
".Lq_in_%=:\n" \
"ds_read_b128 v[64:67], v3 offset:0\n" \
"ds_read_b128 v[68:71], v3 offset:1024\n" \
"ds_read_b128 v[72:75], v3 offset:2048\n" \
"ds_read_b128 v[76:79], v3 offset:3072\n" \
"s_waitcnt lgkmcnt(3)\n" \
"v_smfmac_f32_16x16x64_bf16 v[80:83], v[64:67], a[0:7], v12\n" \
"v_min3_u32 v13, v13, v64, v66\n" \
"v_smfmac_f32_16x16x64_bf16 v[84:87], v[64:67], a[8:15], v12\n" \
"s_waitcnt lgkmcnt(2)\n" \
"v_min3_u32 v13, v13, v68, v70\n" \
"v_smfmac_f32_16x16x64_bf16 v[88:91], v[64:67], v[32:39], v12\n" \
"s_waitcnt lgkmcnt(1)\n" \
"v_min3_u32 v13, v13, v72, v74\n" \
"v_smfmac_f32_16x16x64_bf16 v[92:95], v[64:67], v[40:47], v12\n" \
"s_waitcnt lgkmcnt(0)\n" \
"v_min3_u32 v13, v13, v76, v78\n" \
"v_smfmac_f32_16x16x64_bf16 v[80:83], v[68:71], a[16:23], v12\n" \
"v_cmp_eq_u32_e64 vcc, 0, v13\n" \
"v_smfmac_f32_16x16x64_bf16 v[84:87], v[68:71], a[24:31], v12\n" \
"ds_read_b128 v[32:35], v2 offset:0\n" \
"v_smfmac_f32_16x16x64_bf16 v[88:91], v[68:71], v[112:119], v12\n" \
"ds_read_b128 v[36:39], v2 offset:1024\n" \
"v_smfmac_f32_16x16x64_bf16 v[92:95], v[68:71], v[120:127], v12\n" \
"s_cbranch_vccnz .Lq_retry_%=\n" \
"s_waitcnt vmcnt(4)\n" \
"s_barrier\n" \
"ds_read_b128 v[64:67], v3 offset:4096\n" \
"v_smfmac_f32_16x16x64_bf16 v[80:83], v[72:75], a[32:39], v12\n" \
"ds_read_b128 v[68:71], v3 offset:5120\n" \
"v_smfmac_f32_16x16x64_bf16 v[84:87], v[72:75], a[40:47], v12\n" \
"ds_read_b128 v[40:43], v2 offset:2048\n" \
"v_smfmac_f32_16x16x64_bf16 v[88:91], v[72:75], v[48:55], v12\n" \
"ds_read_b128 v[44:47], v2 offset:3072\n" \
"v_smfmac_f32_16x16x64_bf16 v[92:95], v[72:75], v[56:63], v12\n" \
"ds_read_b128 v[72:75], v3 offset:6144\n" \
"v_smfmac_f32_16x16x64_bf16 v[80:83], v[76:79], a[48:55], v12\n" \
"buffer_store_dwordx2 v[104:105], v10, s[36:39], s92 offen sc1\n" \
"v_smfmac_f32_16x16x64_bf16 v[84:87], v[76:79], a[56:63], v12\n" \
"ds_read_b128 v[48:51], v2 offset:16384\n" \
"v_smfmac_f32_16x16x64_bf16 v[88:91], v[76:79], v[128:135], v12\n" \
"ds_read_b128 v[52:55], v2 offset:17408\n" \
"v_smfmac_f32_16x16x64_bf16 v[92:95], v[76:79], v[136:143], v12\n" \
"ds_read_b128 v[76:79], v3 offset:7168\n" \
"s_waitcnt lgkmcnt(7)\n" \
"v_smfmac_f32_16x16x64_bf16 v[80:83], v[64:67], a[64:71], v12\n" \
"v_min3_u32 v13, v13, v64, v66\n" \
"v_smfmac_f32_16x16x64_bf16 v[84:87], v[64:67], a[72:79], v12\n" \
"ds_read_b128 v[56:59], v2 offset:18432\n" \
"v_smfmac_f32_16x16x64_bf16 v[88:91], v[64:67], v[32:39], v12\n" \
"s_waitcnt vmcnt(3)\n" \
"s_barrier\n" \
"ds_read_b128 v[60:63], v2 offset:19456\n" \
"s_waitcnt lgkmcnt(6)\n" \
"v_smfmac_f32_16x16x64_bf16 v[92:95], v[64:67], v[40:47], v12\n" \
"ds_read_b128 v[64:67], v3 offset:8192\n" \
"v_smfmac_f32_16x16x64_bf16 v[80:83], v[68:71], a[80:87], v12\n" \
"v_min3_u32 v13, v13, v68, v70\n" \
"v_smfmac_f32_16x16x64_bf16 v[84:87], v[68:71], a[88:95], v12\n" \
"ds_read_b128 v[32:35], v1 offset:32768\n" \
"v_smfmac_f32_16x16x64_bf16 v[88:91], v[68:71], v[144:151], v12\n" \
"ds_read_b128 v[36:39], v1 offset:33792\n" \
"v_smfmac_f32_16x16x64_bf16 v[92:95], v[68:71], v[152:159], v12\n" \
"ds_read_b128 v[68:71], v3 offset:9216\n" \
"s_waitcnt lgkmcnt(9)\n" \
"v_smfmac_f32_16x16x64_bf16 v[80:83], v[72:75], a[96:103], v12\n" \
"buffer_store_dwordx4 v[96:99], v11, s[36:39], s95 offen sc1\n" \
"v_min3_u32 v13, v13, v72, v74\n" \
"v_smfmac_f32_16x16x64_bf16 v[84:87], v[72:75], a[104:111], v12\n" \
"ds_read_b128 v[40:43], v1 offset:34816\n" \
"s_waitcnt lgkmcnt(8)\n" \
"v_smfmac_f32_16x16x64_bf16 v[88:91], v[72:75], v[48:55], v12\n" \
"ds_read_b128 v[44:47], v1 offset:35840\n" \
"s_waitcnt lgkmcnt(6)\n" \
"v_smfmac_f32_16x16x64_bf16 v[92:95], v[72:75], v[56:63], v12\n" \
"ds_read_b128 v[72:75], v3 offset:10240\n" \
"v_smfmac_f32_16x16x64_bf16 v[80:83], v[76:79], a[112:119], v12\n" \
"v_min3_u32 v13, v13, v76, v78\n" \
"v_smfmac_f32_16x16x64_bf16 v[84:87], v[76:79], a[120:127], v12\n" \
"ds_read_b128 v[48:51], v1 offset:16384\n" \
"v_smfmac_f32_16x16x64_bf16 v[88:91], v[76:79], v[160:167], v12\n" \
"ds_read_b128 v[52:55], v1 offset:17408\n" \
"v_smfmac_f32_16x16x64_bf16 v[92:95], v[76:79], v[168:175], v12\n" \
"ds_read_b128 v[76:79], v3 offset:11264\n" \
"s_waitcnt lgkmcnt(9)\n" \
"v_smfmac_f32_16x16x64_bf16 v[80:83], v[64:67], a[128:135], v12\n" \
"v_min3_u32 v13, v13, v64, v66\n" \
"v_smfmac_f32_16x16x64_bf16 v[84:87], v[64:67], a[136:143], v12\n" \
"ds_read_b128 v[56:59], v1 offset:18432\n" \
"s_waitcnt lgkmcnt(8)\n" \
"v_smfmac_f32_16x16x64_bf16 v[88:91], v[64:67], v[32:39], v12\n" \
"ds_read_b128 v[60:63], v1 offset:19456\n" \
"s_waitcnt lgkmcnt(6)\n" \
"v_smfmac_f32_16x16x64_bf16 v[92:95], v[64:67], v[40:47], v12\n" \
"ds_read_b128 v[64:67], v3 offset:12288\n" \
"v_smfmac_f32_16x16x64_bf16 v[80:83], v[68:71], a[144:151], v12\n" \
"v_min3_u32 v13, v13, v68, v70\n" \
"v_smfmac_f32_16x16x64_bf16 v[84:87], v[68:71], a[152:159], v12\n" \
"ds_read_b128 v[32:35], v1 offset:49152\n" \
"v_smfmac_f32_16x16x64_bf16 v[88:91], v[68:71], v[176:183], v12\n" \
"ds_read_b128 v[36:39], v1 offset:50176\n" \
"v_smfmac_f32_16x16x64_bf16 v[92:95], v[68:71], v[184:191], v12\n" \
"ds_read_b128 v[68:71], v3 offset:13312\n" \
"s_waitcnt lgkmcnt(9)\n" \
"v_smfmac_f32_16x16x64_bf16 v[80:83], v[72:75], a[160:167], v12\n" \
"buffer_store_dwordx4 v[100:103], v31, s[36:39], s95 offen sc1\n" \
"v_min3_u32 v13, v13, v72, v74\n" \
"v_smfmac_f32_16x16x64_bf16 v[84:87], v[72:75], a[168:175], v12\n" \
"ds_read_b128 v[40:43], v1 offset:51200\n" \
"s_waitcnt lgkmcnt(8)\n" \
"v_smfmac_f32_16x16x64_bf16 v[88:91], v[72:75], v[48:55], v12\n" \
"ds_read_b128 v[44:47], v1 offset:52224\n" \
"s_waitcnt lgkmcnt(6)\n" \
"v_smfmac_f32_16x16x64_bf16 v[92:95], v[72:75], v[56:63], v12\n" \
"ds_read_b128 v[72:75], v3 offset:14336\n" \
"v_smfmac_f32_16x16x64_bf16 v[80:83], v[76:79], a[176:183], v12\n" \
"s_mov_b32 s95, 0x7ffffff0\n" \
"v_min3_u32 v13, v13, v76, v78\n" \
"v_smfmac_f32_16x16x64_bf16 v[84:87], v[76:79], a[184:191], v12\n" \
"ds_read_b128 v[48:51], v3 offset:20480\n" \
"v_smfmac_f32_16x16x64_bf16 v[88:91], v[76:79], v[192:199], v12\n" \
"ds_read_b128 v[52:55], v3 offset:21504\n" \
"v_smfmac_f32_16x16x64_bf16 v[92:95], v[76:79], v[200:207], v12\n" \
"ds_read_b128 v[76:79], v3 offset:15360\n" \
"s_waitcnt lgkmcnt(9)\n" \
"v_smfmac_f32_16x16x64_bf16 v[80:83], v[64:67], a[192:199], v12\n" \
"v_min3_u32 v13, v13, v64, v66\n" \
"v_smfmac_f32_16x16x64_bf16 v[84:87], v[64:67], a[200:207], v12\n" \
"ds_read_b128 v[56:59], v3 offset:22528\n" \
"s_waitcnt lgkmcnt(8)\n" \
"v_smfmac_f32_16x16x64_bf16 v[88:91], v[64:67], v[32:39], v12\n" \
"ds_read_b128 v[60:63], v3 offset:23552\n" \
"s_waitcnt lgkmcnt(6)\n" \
"v_smfmac_f32_16x16x64_bf16 v[92:95], v[64:67], v[40:47], v12\n" \
"v_smfmac_f32_16x16x64_bf16 v[80:83], v[68:71], a[208:215], v12\n" \
"ds_read_b128 v[96:99], v14 offset:32768\n" \
"v_min3_u32 v13, v13, v68, v70\n" \
"v_smfmac_f32_16x16x64_bf16 v[84:87], v[68:71], a[216:223], v12\n" \
"ds_read_b128 v[32:35], v3 offset:16384\n" \
"v_smfmac_f32_16x16x64_bf16 v[88:91], v[68:71], v[208:215], v12\n" \
"ds_read_b128 v[36:39], v3 offset:17408\n" \
"v_smfmac_f32_16x16x64_bf16 v[92:95], v[68:71], v[216:223], v12\n" \
"s_waitcnt lgkmcnt(8)\n" \
"v_smfmac_f32_16x16x64_bf16 v[80:83], v[72:75], a[224:231], v12\n" \
"ds_read_b128 v[100:103], v14 offset:33792\n" \
"v_min3_u32 v13, v13, v72, v74\n" \
"v_smfmac_f32_16x16x64_bf16 v[84:87], v[72:75], a[232:239], v12\n" \
"ds_read_b128 v[40:43], v3 offset:18432\n" \
"s_waitcnt lgkmcnt(7)\n" \
"v_smfmac_f32_16x16x64_bf16 v[80:83], v[76:79], a[240:247], v12\n" \
"ds_read_b128 v[44:47], v3 offset:19456\n" \
"v_min3_u32 v13, v13, v76, v78\n" \
"v_smfmac_f32_16x16x64_bf16 v[84:87], v[76:79], a[248:255], v12\n" \
"v_cmp_eq_u32 vcc, 0, v13\n" \
"v_smfmac_f32_16x16x64_bf16 v[88:91], v[72:75], v[224:231], v12\n" \
"v_smfmac_f32_16x16x64_bf16 v[92:95], v[72:75], v[232:239], v12\n" \
"v_smfmac_f32_16x16x64_bf16 v[88:91], v[76:79], v[240:247], v12\n" \
"s_cbranch_vccnz .Lq_retry_%=\n" \
"v_smfmac_f32_16x16x64_bf16 v[92:95], v[76:79], v[248:255], v12\n" \
"v_pk_add_f32 v[80:81], v[80:81], v[82:83]\n" \
"v_pk_add_f32 v[84:85], v[84:85], v[86:87]\n" \
"v_pk_mul_f32 v[80:81], v[80:81], s[98:99]\n" \
"v_pk_mul_f32 v[84:85], v[84:85], s[98:99]\n" \
"v_cvt_pk_bf16_f32 v80, v80, v81\n" \
"v_cvt_pk_bf16_f32 v84, v84, v85\n" \
"ds_write2_b32 v5, v80, v84 offset1:128\n" \
"v_pk_add_f32 v[88:89], v[88:89], v[90:91]\n" \
"v_pk_add_f32 v[92:93], v[92:93], v[94:95]\n" \
"v_cvt_pk_bf16_f32 v88, v88, v89\n" \
"v_cvt_pk_bf16_f32 v92, v92, v93\n" \
"ds_write2_b32 v0, v88, v92 offset1:128\n" \
"buffer_load_dwordx2 v[104:105], v8, s[36:39], s58 offen offset:0 sc1\n" \
"buffer_load_dwordx2 v[106:107], v8, s[36:39], s58 offen offset:256 sc1\n" \
"s_cmp_eq_u32 s49, 63\n" \
"s_waitcnt lgkmcnt(0)\n" \
"s_barrier\n" \
"ds_read_b64_tr_b16 v[64:65], v6 offset:0\n" \
"ds_read_b64_tr_b16 v[66:67], v6 offset:512\n" \
"ds_read_b64_tr_b16 v[68:69], v6 offset:1024\n" \
"ds_read_b64_tr_b16 v[70:71], v6 offset:1536\n" \
"ds_read_b64_tr_b16 v[72:73], v6 offset:2048\n" \
"ds_read_b64_tr_b16 v[74:75], v6 offset:2560\n" \
"ds_read_b64_tr_b16 v[76:77], v6 offset:3072\n" \
"ds_read_b64_tr_b16 v[78:79], v6 offset:3584\n" \
"s_cbranch_scc1 .Lq_last_%=\n" \
"s_waitcnt lgkmcnt(6)\n" \
"v_mfma_f32_16x16x32_bf16 v[88:91], v[64:67], v[64:67], v[16:19]\n" \
"v_mfma_f32_16x16x32_bf16 v[80:83], v[32:35], v[64:67], 0\n" \
"s_waitcnt lgkmcnt(4)\n" \
"v_mfma_f32_16x16x32_bf16 v[88:91], v[68:71], v[68:71], v[88:91]\n" \
"v_mfma_f32_16x16x32_bf16 v[80:83], v[36:39], v[68:71], v[80:83]\n" \
"s_waitcnt lgkmcnt(2)\n" \
"v_mfma_f32_16x16x32_bf16 v[88:91], v[72:75], v[72:75], v[88:91]\n" \
"v_mfma_f32_16x16x32_bf16 v[80:83], v[40:43], v[72:75], v[80:83]\n" \
"s_waitcnt lgkmcnt(0)\n" \
"v_mfma_f32_16x16x32_bf16 v[88:91], v[76:79], v[76:79], v[88:91]\n" \
"v_mfma_f32_16x16x32_bf16 v[80:83], v[44:47], v[76:79], v[80:83]\n" \
"v_mfma_f32_16x16x32_bf16 v[84:87], v[48:51], v[64:67], 0\n" \
"v_mfma_f32_16x16x32_bf16 v[84:87], v[52:55], v[68:71], v[84:87]\n" \
"v_max3_f32 v13, v88, v89, v90\n" \
"v_mfma_f32_16x16x32_bf16 v[84:87], v[56:59], v[72:75], v[84:87]\n" \
"v_max3_f32 v14, v91, v80, v81\n" \
"v_mfma_f32_16x16x32_bf16 v[84:87], v[60:63], v[76:79], v[84:87]\n" \
"v_max3_f32 v13, v13, v82, v83\n" \
"s_nop 6\n" \
"v_max3_f32 v14, v14, v84, v85\n" \
"v_max3_f32 v13, v13, v86, v87\n" \
"v_max_f32 v13, v13, v14\n" \
"v_max_f32 v14, v13, v14\n" \
"s_nop 1\n" \
"v_permlane16_swap_b32 v13, v14\n" \
"v_max_f32 v13, v13, v14\n" \
"v_max_f32 v14, v13, v14\n" \
"s_nop 1\n" \
"v_permlane32_swap_b32 v13, v14\n" \
"v_max_f32_e64 v14, v13, v14\n" \
"v_pk_add_f32 v[80:81], v[80:81], v[14:15] op_sel_hi:[1,0] neg_lo:[0,1] neg_hi:[0,1]\n" \
"v_pk_add_f32 v[82:83], v[82:83], v[14:15] op_sel_hi:[1,0] neg_lo:[0,1] neg_hi:[0,1]\n" \
"v_pk_add_f32 v[84:85], v[84:85], v[14:15] op_sel_hi:[1,0] neg_lo:[0,1] neg_hi:[0,1]\n" \
"v_pk_add_f32 v[86:87], v[86:87], v[14:15] op_sel_hi:[1,0] neg_lo:[0,1] neg_hi:[0,1]\n" \
"v_pk_add_f32 v[88:89], v[88:89], v[14:15] op_sel_hi:[1,0] neg_lo:[0,1] neg_hi:[0,1]\n" \
"v_pk_add_f32 v[90:91], v[90:91], v[14:15] op_sel_hi:[1,0] neg_lo:[0,1] neg_hi:[0,1]\n" \
"v_exp_f32 v80, v80\n" \
"v_exp_f32 v81, v81\n" \
"v_exp_f32 v82, v82\n" \
"v_exp_f32 v83, v83\n" \
"v_exp_f32 v84, v84\n" \
"v_exp_f32 v85, v85\n" \
"v_exp_f32 v86, v86\n" \
"v_exp_f32 v87, v87\n" \
"v_exp_f32_e64 v88, v88\n" \
"v_cvt_pk_bf16_f32 v80, v80, v81\n" \
"v_cvt_pk_bf16_f32 v81, v82, v83\n" \
"v_cvt_pk_bf16_f32 v82, v84, v85\n" \
"v_cvt_pk_bf16_f32 v83, v86, v87\n" \
"v_exp_f32 v89, v89\n" \
"s_nop 0\n" \
"v_mfma_f32_16x16x32_bf16 v[84:87], v[96:99], v[80:83], 0\n" \
"v_exp_f32_e64 v90, v90\n" \
"v_mfma_f32_16x16x32_bf16 v[92:95], v[100:103], v[80:83], 0\n" \
"v_exp_f32_e64 v91, v91\n" \
"v_mfma_f32_16x16x32_bf16 v[108:111], v[20:23], v[80:83], 0\n" \
"v_cvt_pk_bf16_f32 v26, v88, v89\n" \
"v_cvt_pk_bf16_f32 v27, v90, v91\n" \
"s_nop 1\n" \
"v_mfma_f32_16x16x32_bf16 v[108:111], v[20:23], v[26:29], v[108:111]\n" \
"s_waitcnt vmcnt(0)\n" \
"v_mfma_f32_16x16x32_bf16 v[84:87], v[104:107], v[26:29], v[84:87]\n" \
"v_mfma_f32_16x16x32_bf16 v[92:95], v[104:107], v[24:27], v[92:95]\n" \
"v_min_u32 v13, v104, v106\n" \
"v_cmp_eq_u32_e64 vcc, 0, v13\n" \
"v_rcp_f32_e64 v14, v108\n" \
"s_and_b64 vcc, vcc, s[82:83]\n" \
"s_cbranch_vccnz .Lq_vr_%=\n" \
".Lq_vr2_%=:\n" \
"v_pk_mul_f32 v[84:85], v[84:85], v[14:15] op_sel_hi:[1,0]\n" \
"v_pk_mul_f32 v[86:87], v[86:87], v[14:15] op_sel_hi:[1,0]\n" \
"v_cvt_pk_bf16_f32 v80, v84, v85\n" \
"v_cvt_pk_bf16_f32 v81, v86, v87\n" \
"v_max_u32 v80, 1, v80\n" \
"v_pk_mul_f32 v[92:93], v[92:93], v[14:15] op_sel_hi:[1,0]\n" \
"v_pk_mul_f32 v[94:95], v[94:95], v[14:15] op_sel_hi:[1,0]\n" \
"v_cvt_pk_bf16_f32 v82, v92, v93\n" \
"v_cvt_pk_bf16_f32 v83, v94, v95\n" \
"v_max_u32_e64 v82, 1, v82\n" \
"buffer_store_dwordx4 v[80:83], v7, s[36:39], s58 offen sc1\n" \
".Lq_tok_%=:\n" \
"s_memrealtime s[24:25]\n" \
"s_and_b32 s54, s48, 1\n" \
"s_lshl_b32 s54, s54, 12\n" \
"v_add_u32_e64 v14, s54, v3\n" \
"ds_read_b128 v[96:99], v14 offset:24576\n" \
"ds_read_b128 v[100:103], v14 offset:25600\n" \
"s_memrealtime s[26:27]\n" \
"ds_read_b128 v[92:95], v14 offset:26624\n" \
"v_cmp_ge_u32_e64 s[96:97], s81, v30\n" \
"v_readlane_b32 s35, v30, 3\n" \
"v_readlane_b32 s53, v30, 4\n" \
"s_memrealtime s[28:29]\n" \
"v_xor_b32 v30, 512, v8\n" \
"s_max_u32 s35, s35, s53\n" \
"s_cmp_eq_u32 s35, 0\n" \
"s_cselect_b32 s35, s17, s35\n" \
"s_memrealtime s[30:31]\n" \
"buffer_load_dwordx2 v[108:109], v30, s[36:39], s58 offen offset:0 sc1\n" \
"buffer_load_dwordx2 v[110:111], v30, s[36:39], s58 offen offset:256 sc1\n" \
"v_mov_b64 v[26:27], 0\n" \
"buffer_store_dwordx4 v[24:27], v7, s[36:39], s60 offen sc1\n" \
"buffer_store_dword v24, v10, s[36:39], s93 offen sc1\n" \
"s_waitcnt lgkmcnt(0)\n" \
"v_mfma_f32_16x16x32_bf16 v[88:91], v[64:67], v[96:99], v[16:19]\n" \
"v_mfma_f32_16x16x32_bf16 v[80:83], v[32:35], v[96:99], 0\n" \
"v_mfma_f32_16x16x32_bf16 v[84:87], v[48:51], v[96:99], 0\n" \
"ds_read_b128 v[96:99], v14 offset:27648\n" \
"v_mfma_f32_16x16x32_bf16 v[88:91], v[68:71], v[100:103], v[88:91]\n" \
"v_mfma_f32_16x16x32_bf16 v[80:83], v[36:39], v[100:103], v[80:83]\n" \
"v_mfma_f32_16x16x32_bf16 v[84:87], v[52:55], v[100:103], v[84:87]\n" \
"v_mfma_f32_16x16x32_bf16 v[88:91], v[72:75], v[92:95], v[88:91]\n" \
"v_mfma_f32_16x16x32_bf16 v[80:83], v[40:43], v[92:95], v[80:83]\n" \
"v_mfma_f32_16x16x32_bf16 v[84:87], v[56:59], v[92:95], v[84:87]\n" \
"s_waitcnt lgkmcnt(0)\n" \
"v_mfma_f32_16x16x32_bf16 v[88:91], v[76:79], v[96:99], v[88:91]\n" \
"v_mfma_f32_16x16x32_bf16 v[80:83], v[44:47], v[96:99], v[80:83]\n" \
"v_mfma_f32_16x16x32_bf16 v[84:87], v[60:63], v[96:99], v[84:87]\n" \
"buffer_load_dwordx2 v[64:65], v9, s[36:39], s58 offen sc1\n" \
"s_add_u32 s25, s24, 3\n" \
"s_cmp_eq_u32 s26, s24\n" \
"s_subb_u32 s25, s25, 0\n" \
"s_cmp_eq_u32 s28, s24\n" \
"s_subb_u32 s25, s25, 0\n" \
"s_cmp_eq_u32 s30, s24\n" \
"s_subb_u32 s25, s25, 0\n" \
"s_sub_u32 s24, s25, s51\n" \
"s_min_u32 s24, s24, 255\n" \
"s_cmp_eq_u32 s23, s49\n" \
"s_cselect_b32 s24, s46, s24\n" \
"s_lshl_b64 s[32:33], s[32:33], 8\n" \
"s_or_b32 s32, s32, s24\n" \
"s_bfe_u32 s24, s32, 524312\n" \
"s_bfe_u32 s25, s33, 524312\n" \
"s_min_u32 s34, s24, s25\n" \
"v_add_u32 v14, s62, v3\n" \
"v_xor_b32 v30, 0x800, v14\n" \
"ds_read_b128 v[32:35], v14 offset:32768\n" \
"ds_read_b128 v[36:39], v14 offset:33792\n" \
"ds_read_b128 v[40:43], v30 offset:32768\n" \
"ds_read_b128 v[44:47], v30 offset:33792\n" \
"v_mov_b32 v13, 0xceabdc6e\n" \
"v_cndmask_b32_e64 v80, v13, v80, s[64:65]\n" \
"v_cndmask_b32_e64 v81, v13, v81, s[66:67]\n" \
"v_cndmask_b32_e64 v82, v13, v82, s[68:69]\n" \
"v_cndmask_b32_e64 v83, v13, v83, s[70:71]\n" \
"v_cndmask_b32_e64 v84, v13, v84, s[72:73]\n" \
"v_cndmask_b32_e64 v85, v13, v85, s[74:75]\n" \
"v_cndmask_b32_e64 v86, v13, v86, s[76:77]\n" \
"v_cndmask_b32_e64 v87, v13, v87, s[78:79]\n" \
"s_and_b32 s96, s96, 7\n" \
"s_cbranch_scc1 .Lq_f_cold_%=\n" \
".Lq_f_ok_%=:\n" \
"s_cmp_lt_u32 s49, 63\n" \
"s_cselect_b32 s54, 24576, 0\n" \
"s_add_u32 s63, s63, s54\n" \
"s_waitcnt lgkmcnt(0)\n" \
"s_barrier\n" \
"s_add_u32 m0, s62, 131072\n" \
"s_add_u32 s54, s63, 10371072\n" \
"buffer_load_dwordx4 v3, s[36:39], s54 offen offset:0 sc1 lds\n" \
"buffer_load_dwordx4 v3, s[36:39], s54 offen offset:1024 sc1 lds\n" \
"s_add_u32 m0, s62, 139264\n" \
"s_add_u32 s54, s63, 10379264\n" \
"buffer_load_dwordx4 v3, s[36:39], s54 offen offset:0 sc1 lds\n" \
"buffer_load_dwordx4 v3, s[36:39], s54 offen offset:1024 sc1 lds\n" \
"s_add_u32 m0, s62, 147456\n" \
"s_add_u32 s54, s63, 10387456\n" \
"buffer_load_dwordx4 v3, s[36:39], s54 offen offset:0 sc1 lds\n" \
"buffer_load_dwordx4 v3, s[36:39], s54 offen offset:1024 sc1 lds\n" \
"v_max3_f32 v13, v88, v89, v90\n" \
"v_max3_f32 v14, v91, v80, v81\n" \
"v_max3_f32 v13, v13, v82, v83\n" \
"v_max3_f32 v14, v14, v84, v85\n" \
"v_max3_f32 v13, v13, v86, v87\n" \
"v_max_f32 v13, v13, v14\n" \
"v_max_f32 v14, v13, v14\n" \
"s_nop 1\n" \
"v_permlane16_swap_b32 v13, v14\n" \
"v_max_f32 v13, v13, v14\n" \
"v_max_f32 v14, v13, v14\n" \
"s_nop 1\n" \
"v_permlane32_swap_b32 v13, v14\n" \
"v_max_f32_e64 v14, v13, v14\n" \
"v_pk_add_f32 v[80:81], v[80:81], v[14:15] op_sel_hi:[1,0] neg_lo:[0,1] neg_hi:[0,1]\n" \
"v_pk_add_f32 v[82:83], v[82:83], v[14:15] op_sel_hi:[1,0] neg_lo:[0,1] neg_hi:[0,1]\n" \
"v_pk_add_f32 v[84:85], v[84:85], v[14:15] op_sel_hi:[1,0] neg_lo:[0,1] neg_hi:[0,1]\n" \
"v_pk_add_f32 v[86:87], v[86:87], v[14:15] op_sel_hi:[1,0] neg_lo:[0,1] neg_hi:[0,1]\n" \
"v_pk_add_f32 v[88:89], v[88:89], v[14:15] op_sel_hi:[1,0] neg_lo:[0,1] neg_hi:[0,1]\n" \
"v_pk_add_f32 v[90:91], v[90:91], v[14:15] op_sel_hi:[1,0] neg_lo:[0,1] neg_hi:[0,1]\n" \
"v_exp_f32 v80, v80\n" \
"v_exp_f32 v81, v81\n" \
"v_exp_f32 v82, v82\n" \
"v_exp_f32 v83, v83\n" \
"v_exp_f32 v84, v84\n" \
"v_exp_f32 v85, v85\n" \
"v_exp_f32 v86, v86\n" \
"v_exp_f32 v87, v87\n" \
"v_exp_f32_e64 v88, v88\n" \
"v_cvt_pk_bf16_f32 v80, v80, v81\n" \
"v_cvt_pk_bf16_f32 v81, v82, v83\n" \
"v_cvt_pk_bf16_f32 v82, v84, v85\n" \
"v_cvt_pk_bf16_f32 v83, v86, v87\n" \
"v_exp_f32 v89, v89\n" \
"s_nop 0\n" \
"v_mfma_f32_16x16x32_bf16 v[48:51], v[32:35], v[80:83], 0\n" \
"v_exp_f32_e64 v90, v90\n" \
"v_mfma_f32_16x16x32_bf16 v[52:55], v[36:39], v[80:83], 0\n" \
"v_exp_f32_e64 v91, v91\n" \
"v_mfma_f32_16x16x32_bf16 v[56:59], v[40:43], v[80:83], 0\n" \
"v_cvt_pk_bf16_f32 v26, v88, v89\n" \
"v_cvt_pk_bf16_f32 v27, v90, v91\n" \
"v_mfma_f32_16x16x32_bf16 v[60:63], v[44:47], v[80:83], 0\n" \
"v_mfma_f32_16x16x32_bf16 v[92:95], v[20:23], v[80:83], 0\n" \
"s_waitcnt vmcnt(10)\n" \
"v_min3_u32 v13, v104, v106, v108\n" \
"s_waitcnt vmcnt(9)\n" \
"v_min3_u32 v13, v13, v110, v110\n" \
"v_cmp_eq_u32 vcc, 0, v13\n" \
"s_nop 0\n" \
"s_and_b64 vcc, vcc, s[82:83]\n" \
"s_cbranch_vccnz .Lq_v2_cold_%=\n" \
".Lq_v2_ok_%=:\n" \
"v_mfma_f32_16x16x32_bf16 v[92:95], v[20:23], v[26:29], v[92:95]\n" \
"v_mfma_f32_16x16x32_bf16 v[48:51], v[104:107], v[26:29], v[48:51]\n" \
"v_mfma_f32_16x16x32_bf16 v[52:55], v[104:107], v[24:27], v[52:55]\n" \
"v_mfma_f32_16x16x32_bf16 v[56:59], v[108:111], v[26:29], v[56:59]\n" \
"v_mfma_f32_16x16x32_bf16 v[60:63], v[108:111], v[24:27], v[60:63]\n" \
"s_lshl_b32 s95, s49, 13\n" \
"v_rcp_f32 v14, v92\n" \
"s_nop 0\n" \
"v_pk_mul_f32 v[48:49], v[48:49], v[14:15] op_sel_hi:[1,0]\n" \
"v_pk_mul_f32 v[50:51], v[50:51], v[14:15] op_sel_hi:[1,0]\n" \
"v_pk_mul_f32 v[52:53], v[52:53], v[14:15] op_sel_hi:[1,0]\n" \
"v_pk_mul_f32 v[54:55], v[54:55], v[14:15] op_sel_hi:[1,0]\n" \
"v_cvt_pk_bf16_f32 v96, v48, v49\n" \
"v_cvt_pk_bf16_f32 v97, v50, v51\n" \
"v_cvt_pk_bf16_f32 v98, v52, v53\n" \
"v_cvt_pk_bf16_f32 v99, v54, v55\n" \
"v_pk_mul_f32 v[56:57], v[56:57], v[14:15] op_sel_hi:[1,0]\n" \
"v_pk_mul_f32 v[58:59], v[58:59], v[14:15] op_sel_hi:[1,0]\n" \
"v_pk_mul_f32 v[60:61], v[60:61], v[14:15] op_sel_hi:[1,0]\n" \
"v_pk_mul_f32 v[62:63], v[62:63], v[14:15] op_sel_hi:[1,0]\n" \
"v_cvt_pk_bf16_f32 v100, v56, v57\n" \
"v_cvt_pk_bf16_f32 v101, v58, v59\n" \
"v_cvt_pk_bf16_f32 v102, v60, v61\n" \
"v_cvt_pk_bf16_f32 v103, v62, v63\n" \
"s_cmp_eq_u32 s49, 63\n" \
"s_cbranch_scc1 .Lq_end_%=\n" \
"s_not_b64 s[26:27], s[82:83]\n" \
"s_waitcnt vmcnt(6)\n" \
"v_cmp_eq_u32_e64 vcc, 0, v64\n" \
"s_nop 0\n" \
"s_and_b64 vcc, vcc, s[26:27]\n" \
"s_nop 0\n" \
"s_cbranch_vccnz .Lq_h_cold_%=\n" \
".Lq_h_ok_%=:\n" \
"v_subrev_u32 v64, s51, v64\n" \
"v_max_i32_dpp v65, v65, v65 row_ror:8 row_mask:0xf bank_mask:0xf\n" \
"s_nop 0\n" \
"v_max_i32_dpp v64, v64, v64 row_ror:8 row_mask:0xf bank_mask:0xf\n" \
"v_max_i32_dpp v65, v65, v65 row_ror:4 row_mask:0xf bank_mask:0xf\n" \
"s_nop 0\n" \
"v_max_i32_dpp v64, v64, v64 row_ror:4 row_mask:0xf bank_mask:0xf\n" \
"v_max_i32_dpp v65, v65, v65 row_ror:2 row_mask:0xf bank_mask:0xf\n" \
"s_nop 0\n" \
"v_max_i32_dpp v64, v64, v64 row_ror:2 row_mask:0xf bank_mask:0xf\n" \
"v_max_i32_dpp v65, v65, v65 row_ror:1 row_mask:0xf bank_mask:0xf\n" \
"s_nop 0\n" \
"v_max_i32_dpp v64, v64, v64 row_ror:1 row_mask:0xf bank_mask:0xf\n" \
"v_readlane_b32 s53, v65, 0\n" \
"v_readlane_b32 s50, v65, 16\n" \
"v_readlane_b32 s54, v64, 0\n" \
"v_readlane_b32 s55, v64, 16\n" \
"s_max_i32 s50, s50, s53\n" \
"s_max_i32 s55, s55, s54\n" \
"s_add_u32 s55, s55, s51\n" \
"s_add_u32 s55, s55, s44\n" \
"s_add_u32 s50, s50, s55\n" \
"s_add_u32 s50, s50, s45\n" \
"s_add_u32 s49, s49, 1\n" \
"s_branch .Lq_loop_%=\n" \
".Lq_f0_cold_%=:\n" \
"s_nop 7\n" \
"s_nop 7\n" \
"s_waitcnt vmcnt(0) lgkmcnt(0)\n" \
"s_mov_b32 s53, 0\n" \
".Lq_f0_again_%=:\n" \
"s_cmp_lg_u32 s52, 0\n" \
"s_cbranch_scc1 .Lq_f0_ok_%=\n" \
"s_sleep 4\n" \
"buffer_load_dword v13, v15, s[36:39], s80 offen sc1\n" \
"s_waitcnt vmcnt(0)\n" \
"v_cmp_ge_u32_e64 s[96:97], s81, v13\n" \
"s_nop 4\n" \
"s_and_b32 s96, s96, 7\n" \
"s_cbranch_scc0 .Lq_f0_ok_%=\n" \
"s_add_u32 s53, s53, 1\n" \
"s_cmp_lt_u32 s53, 32768\n" \
"s_cbranch_scc1 .Lq_f0_again_%=\n" \
"s_mov_b32 s52, 1\n" \
"v_mov_b32 v13, 256\n" \
"v_mov_b32 v14, 1\n" \
"buffer_store_dword v14, v13, s[36:39], 0 offen sc1\n" \
"s_waitcnt vmcnt(0) lgkmcnt(0)\n" \
"s_branch .Lq_f0_ok_%=\n" \
".Lq_vr_%=:\n" \
"s_nop 7\n" \
"s_nop 7\n" \
"s_waitcnt vmcnt(0) lgkmcnt(0)\n" \
"buffer_load_dwordx2 v[104:105], v8, s[36:39], s58 offen offset:0 sc1\n" \
"buffer_load_dwordx2 v[106:107], v8, s[36:39], s58 offen offset:256 sc1\n" \
"s_waitcnt vmcnt(0)\n" \
"v_min3_u32 v13, v104, v106, v106\n" \
"v_cmp_eq_u32 vcc, 0, v13\n" \
"s_nop 0\n" \
"s_and_b64 vcc, vcc, s[82:83]\n" \
"s_cbranch_vccnz .Lq_v_cold_%=\n" \
".Lq_v_ok_%=:\n" \
"s_mov_b32 s23, s49\n" \
"v_mfma_f32_16x16x32_bf16 v[84:87], v[96:99], v[80:83], 0\n" \
"v_mfma_f32_16x16x32_bf16 v[92:95], v[100:103], v[80:83], 0\n" \
"v_mfma_f32_16x16x32_bf16 v[84:87], v[104:107], v[26:29], v[84:87]\n" \
"v_mfma_f32_16x16x32_bf16 v[92:95], v[104:107], v[24:27], v[92:95]\n" \
"s_nop 7\n" \
"s_nop 7\n" \
"s_branch .Lq_vr2_%=\n" \
".Lq_v_cold_%=:\n" \
"s_nop 7\n" \
"s_nop 7\n" \
"s_waitcnt vmcnt(0) lgkmcnt(0)\n" \
"s_mov_b32 s53, 0\n" \
".Lq_v_again_%=:\n" \
"s_cmp_lg_u32 s52, 0\n" \
"s_cbranch_scc1 .Lq_v_ok_%=\n" \
"s_sleep 1\n" \
"buffer_load_dwordx2 v[104:105], v8, s[36:39], s58 offen offset:0 sc1\n" \
"buffer_load_dwordx2 v[106:107], v8, s[36:39], s58 offen offset:256 sc1\n" \
"s_waitcnt vmcnt(0)\n" \
"v_min3_u32 v13, v104, v106, v106\n" \
"v_cmp_eq_u32 vcc, 0, v13\n" \
"s_nop 0\n" \
"s_and_b64 vcc, vcc, s[82:83]\n" \
"s_nop 4\n" \
"s_cbranch_vccz .Lq_v_ok_%=\n" \
"s_add_u32 s53, s53, 1\n" \
"s_cmp_lt_u32 s53, 32768\n" \
"s_cbranch_scc1 .Lq_v_again_%=\n" \
"s_mov_b32 s52, 1\n" \
"v_mov_b32 v13, 256\n" \
"v_mov_b32 v14, 1\n" \
"buffer_store_dword v14, v13, s[36:39], 0 offen sc1\n" \
"s_waitcnt vmcnt(0) lgkmcnt(0)\n" \
"s_branch .Lq_v_ok_%=\n" \
".Lq_f_cold_%=:\n" \
"s_nop 7\n" \
"s_nop 7\n" \
"s_waitcnt vmcnt(0) lgkmcnt(0)\n" \
"s_mov_b32 s53, 0\n" \
".Lq_f_again_%=:\n" \
"s_cmp_lg_u32 s52, 0\n" \
"s_cbranch_scc1 .Lq_f_ok_%=\n" \
"s_sleep 4\n" \
"buffer_load_dword v13, v15, s[36:39], s80 offen sc1\n" \
"s_waitcnt vmcnt(0)\n" \
"v_cmp_ge_u32_e64 s[96:97], s81, v13\n" \
"s_nop 4\n" \
"s_and_b32 s96, s96, 7\n" \
"s_cbranch_scc0 .Lq_f_ok_%=\n" \
"s_add_u32 s53, s53, 1\n" \
"s_cmp_lt_u32 s53, 32768\n" \
"s_cbranch_scc1 .Lq_f_again_%=\n" \
"s_mov_b32 s52, 1\n" \
"v_mov_b32 v13, 256\n" \
"v_mov_b32 v14, 1\n" \
"buffer_store_dword v14, v13, s[36:39], 0 offen sc1\n" \
"s_waitcnt vmcnt(0) lgkmcnt(0)\n" \
"s_branch .Lq_f_ok_%=\n" \
".Lq_v2_cold_%=:\n" \
"s_nop 7\n" \
"s_nop 7\n" \
"s_waitcnt vmcnt(0) lgkmcnt(0)\n" \
"s_mov_b32 s53, 0\n" \
".Lq_v2_again_%=:\n" \
"s_cmp_lg_u32 s52, 0\n" \
"s_cbranch_scc1 .Lq_v2_ok_%=\n" \
"s_sleep 1\n" \
"v_xor_b32 v31, 512, v8\n" \
"buffer_load_dwordx2 v[104:105], v8, s[36:39], s58 offen offset:0 sc1\n" \
"buffer_load_dwordx2 v[106:107], v8, s[36:39], s58 offen offset:256 sc1\n" \
"buffer_load_dwordx2 v[108:109], v31, s[36:39], s58 offen offset:0 sc1\n" \
"buffer_load_dwordx2 v[110:111], v31, s[36:39], s58 offen offset:256 sc1\n" \
"s_waitcnt vmcnt(1)\n" \
"v_min3_u32 v13, v104, v106, v108\n" \
"s_waitcnt vmcnt(0)\n" \
"v_min3_u32 v13, v13, v110, v110\n" \
"v_cmp_eq_u32 vcc, 0, v13\n" \
"s_nop 0\n" \
"s_and_b64 vcc, vcc, s[82:83]\n" \
"s_nop 4\n" \
"s_cbranch_vccz .Lq_v2_ok_%=\n" \
"s_add_u32 s53, s53, 1\n" \
"s_cmp_lt_u32 s53, 32768\n" \
"s_cbranch_scc1 .Lq_v2_again_%=\n" \
"s_mov_b32 s52, 1\n" \
"v_mov_b32 v13, 256\n" \
"v_mov_b32 v14, 1\n" \
"buffer_store_dword v14, v13, s[36:39], 0 offen sc1\n" \
"s_waitcnt vmcnt(0) lgkmcnt(0)\n" \
"s_branch .Lq_v2_ok_%=\n" \
".Lq_h_cold_%=:\n" \
"s_nop 7\n" \
"s_nop 7\n" \
"s_waitcnt vmcnt(0) lgkmcnt(0)\n" \
"s_mov_b32 s53, 0\n" \
".Lq_h_again_%=:\n" \
"s_cmp_lg_u32 s52, 0\n" \
"s_cbranch_scc1 .Lq_h_ok_%=\n" \
"s_sleep 1\n" \
"buffer_load_dwordx2 v[64:65], v9, s[36:39], s58 offen sc1\n" \
"s_waitcnt vmcnt(0)\n" \
"v_cmp_eq_u32 vcc, 0, v64\n" \
"s_nop 0\n" \
"s_and_b64 vcc, vcc, s[26:27]\n" \
"s_nop 4\n" \
"s_cbranch_vccz .Lq_h_ok_%=\n" \
"s_add_u32 s53, s53, 1\n" \
"s_cmp_lt_u32 s53, 32768\n" \
"s_cbranch_scc1 .Lq_h_again_%=\n" \
"s_mov_b32 s52, 1\n" \
"v_mov_b32 v13, 256\n" \
"v_mov_b32 v14, 1\n" \
"buffer_store_dword v14, v13, s[36:39], 0 offen sc1\n" \
"s_waitcnt vmcnt(0) lgkmcnt(0)\n" \
"s_branch .Lq_h_ok_%=\n" \
".Lq_retry_%=:\n" \
"s_nop 7\n" \
"s_nop 7\n" \
"s_waitcnt vmcnt(0) lgkmcnt(0)\n" \
"ds_read_b128 v[32:35], v1 offset:0\n" \
"ds_read_b128 v[36:39], v1 offset:1024\n" \
"ds_read_b128 v[40:43], v1 offset:2048\n" \
"ds_read_b128 v[44:47], v1 offset:3072\n" \
"ds_read_b128 v[48:51], v2 offset:32768\n" \
"ds_read_b128 v[52:55], v2 offset:33792\n" \
"ds_read_b128 v[56:59], v2 offset:34816\n" \
"ds_read_b128 v[60:63], v2 offset:35840\n" \
"v_mov_b64 v[80:81], 0\n" \
"v_mov_b64 v[82:83], 0\n" \
"v_mov_b64 v[84:85], 0\n" \
"v_mov_b64 v[86:87], 0\n" \
"v_mov_b64 v[88:89], 0\n" \
"v_mov_b64 v[90:91], 0\n" \
"v_mov_b64 v[92:93], 0\n" \
"v_mov_b64 v[94:95], 0\n" \
"v_mov_b32 v13, -1\n" \
"v_add_u32 v14, s62, v3\n" \
"v_xor_b32 v31, 0x400, v11\n" \
"s_max_u32 s53, s49, 1\n" \
"s_sub_u32 s53, s53, 1\n" \
"v_mov_b32 v30, s53\n" \
"s_waitcnt lgkmcnt(0)\n" \
"s_memrealtime s[56:57]\n" \
"s_mov_b32 s54, 100\n" \
"s_mov_b32 m0, s55\n" \
"s_waitcnt vmcnt(0) lgkmcnt(0)\n" \
"s_barrier\n" \
"s_add_u32 s88, s88, 1\n" \
"s_cmp_lt_u32 s88, 32768\n" \
"s_cbranch_scc1 .Lq_r1_%=\n" \
"s_mov_b32 s52, 1\n" \
"v_mov_b32 v13, 256\n" \
"v_mov_b32 v14, 1\n" \
"buffer_store_dword v14, v13, s[36:39], 0 offen sc1\n" \
"s_waitcnt vmcnt(0) lgkmcnt(0)\n" \
"s_endpgm\n" \
".Lq_r1_%=:\n" \
"s_cmp_lg_u32 s49, 0\n" \
"s_cbranch_scc1 .Lq_r2_%=\n" \
"s_sleep 8\n" \
".Lq_r2_%=:\n" \
"s_branch .Lq_go_%=\n" \
".Lq_last_%=:\n" \
"s_waitcnt vmcnt(0) lgkmcnt(0)\n" \
"s_nop 7\n" \
"s_nop 7\n" \
"s_branch .Lq_tok_%=\n" \
".Lq_end_%=:\n" \
"s_nop 7\n" \
"s_nop 7\n" \
"v_xor_b32 v31, 0x400, v11\n" \
"s_add_u32 s49, s49, 1\n" \
"buffer_store_dwordx4 v[96:99], v11, s[36:39], s95 offen sc1\n" \
"buffer_store_dwordx4 v[100:103], v31, s[36:39], s95 offen sc1\n" \
"s_waitcnt vmcnt(0) lgkmcnt(0)\n" \
"s_barrier\n" \
"v_mov_b32 v31, 64\n" \
"buffer_store_dword v31, v10, s[36:39], s87 offen sc1\n" \
"s_waitcnt vmcnt(0) lgkmcnt(0)\n" \
"s_endpgm\n"
#define ASM_VO \
"s_nop 7\n" \
"s_nop 7\n" \
"buffer_load_dword v20, v13, s[68:71], 0 offen offset:0\n" \
"buffer_load_dword v21, v13, s[68:71], 0 offen offset:8\n" \
"buffer_load_dword v22, v13, s[68:71], 0 offen offset:16\n" \
"buffer_load_dword v23, v13, s[68:71], 0 offen offset:24\n" \
"v_mov_b32 v16, 0\n" \
"v_mov_b32 v17, 0\n" \
"v_mov_b32 v18, 0\n" \
"v_mov_b32 v19, 0\n" \
"s_mov_b32 s54, 0\n" \
"buffer_store_dwordx2 v[16:17], v7, s[36:39], s54 offen sc1\n" \
"s_mov_b32 s54, 327680\n" \
"buffer_store_dwordx2 v[16:17], v7, s[36:39], s54 offen sc1\n" \
"buffer_store_dwordx4 v[16:19], v8, s[36:39], s54 offen sc1\n" \
"s_mov_b32 s54, 655360\n" \
"buffer_store_dwordx2 v[16:17], v7, s[36:39], s54 offen sc1\n" \
"buffer_store_dwordx4 v[16:19], v8, s[36:39], s54 offen sc1\n" \
"s_mov_b32 s54, 983040\n" \
"buffer_store_dwordx2 v[16:17], v7, s[36:39], s54 offen sc1\n" \
"buffer_store_dwordx4 v[16:19], v8, s[36:39], s54 offen sc1\n" \
"s_mul_i32 s62, s48, 40960\n" \
"v_add_u32 v32, 40960, v3\n" \
"v_add_u32 v33, 81920, v3\n" \
"v_add_u32 v34, 122880, v3\n" \
"s_add_u32 m0, s62, 0\n" \
"s_mov_b32 s54, 0\n" \
"buffer_load_dwordx4 v1, s[40:43], s54 offen nt lds\n" \
"s_add_u32 m0, s62, 1024\n" \
"s_mov_b32 s54, 65536\n" \
"buffer_load_dwordx4 v1, s[40:43], s54 offen nt lds\n" \
"s_add_u32 m0, s62, 2048\n" \
"s_mov_b32 s54, 131072\n" \
"buffer_load_dwordx4 v1, s[40:43], s54 offen nt lds\n" \
"s_add_u32 m0, s62, 3072\n" \
"s_mov_b32 s54, 196608\n" \
"buffer_load_dwordx4 v1, s[40:43], s54 offen nt lds\n" \
"s_add_u32 m0, s62, 4096\n" \
"s_mov_b32 s54, 262144\n" \
"buffer_load_dwordx4 v1, s[40:43], s54 offen nt lds\n" \
"s_add_u32 m0, s62, 5120\n" \
"s_mov_b32 s54, 327680\n" \
"buffer_load_dwordx4 v1, s[40:43], s54 offen nt lds\n" \
"s_add_u32 m0, s62, 6144\n" \
"s_mov_b32 s54, 393216\n" \
"buffer_load_dwordx4 v1, s[40:43], s54 offen nt lds\n" \
"s_add_u32 m0, s62, 7168\n" \
"s_mov_b32 s54, 458752\n" \
"buffer_load_dwordx4 v1, s[40:43], s54 offen nt lds\n" \
"s_add_u32 m0, s62, 8192\n" \
"s_mov_b32 s54, 524288\n" \
"buffer_load_dwordx4 v1, s[40:43], s54 offen nt lds\n" \
"s_add_u32 m0, s62, 9216\n" \
"s_mov_b32 s54, 589824\n" \
"buffer_load_dwordx4 v1, s[40:43], s54 offen nt lds\n" \
"s_add_u32 m0, s62, 10240\n" \
"s_mov_b32 s54, 655360\n" \
"buffer_load_dwordx4 v1, s[40:43], s54 offen nt lds\n" \
"s_add_u32 m0, s62, 11264\n" \
"s_mov_b32 s54, 720896\n" \
"buffer_load_dwordx4 v1, s[40:43], s54 offen nt lds\n" \
"s_add_u32 m0, s62, 12288\n" \
"s_mov_b32 s54, 786432\n" \
"buffer_load_dwordx4 v1, s[40:43], s54 offen nt lds\n" \
"s_add_u32 m0, s62, 13312\n" \
"s_mov_b32 s54, 851968\n" \
"buffer_load_dwordx4 v1, s[40:43], s54 offen nt lds\n" \
"s_add_u32 m0, s62, 14336\n" \
"s_mov_b32 s54, 917504\n" \
"buffer_load_dwordx4 v1, s[40:43], s54 offen nt lds\n" \
"s_add_u32 m0, s62, 15360\n" \
"s_mov_b32 s54, 983040\n" \
"buffer_load_dwordx4 v1, s[40:43], s54 offen nt lds\n" \
"s_add_u32 m0, s62, 16384\n" \
"s_mov_b32 s54, 1048576\n" \
"buffer_load_dwordx4 v1, s[40:43], s54 offen nt lds\n" \
"s_add_u32 m0, s62, 17408\n" \
"s_mov_b32 s54, 1114112\n" \
"buffer_load_dwordx4 v1, s[40:43], s54 offen nt lds\n" \
"s_add_u32 m0, s62, 18432\n" \
"s_mov_b32 s54, 1179648\n" \
"buffer_load_dwordx4 v1, s[40:43], s54 offen nt lds\n" \
"s_add_u32 m0, s62, 19456\n" \
"s_mov_b32 s54, 1245184\n" \
"buffer_load_dwordx4 v1, s[40:43], s54 offen nt lds\n" \
"s_add_u32 m0, s62, 20480\n" \
"s_mov_b32 s54, 1310720\n" \
"buffer_load_dwordx4 v1, s[40:43], s54 offen nt lds\n" \
"s_add_u32 m0, s62, 21504\n" \
"s_mov_b32 s54, 1376256\n" \
"buffer_load_dwordx4 v1, s[40:43], s54 offen nt lds\n" \
"s_add_u32 m0, s62, 22528\n" \
"s_mov_b32 s54, 1441792\n" \
"buffer_load_dwordx4 v1, s[40:43], s54 offen nt lds\n" \
"s_add_u32 m0, s62, 23552\n" \
"s_mov_b32 s54, 1507328\n" \
"buffer_load_dwordx4 v1, s[40:43], s54 offen nt lds\n" \
"s_add_u32 m0, s62, 24576\n" \
"s_mov_b32 s54, 1572864\n" \
"buffer_load_dwordx4 v1, s[40:43], s54 offen nt lds\n" \
"s_add_u32 m0, s62, 25600\n" \
"s_mov_b32 s54, 1638400\n" \
"buffer_load_dwordx4 v1, s[40:43], s54 offen nt lds\n" \
"s_add_u32 m0, s62, 26624\n" \
"s_mov_b32 s54, 1703936\n" \
"buffer_load_dwordx4 v1, s[40:43], s54 offen nt lds\n" \
"s_add_u32 m0, s62, 27648\n" \
"s_mov_b32 s54, 1769472\n" \
"buffer_load_dwordx4 v1, s[40:43], s54 offen nt lds\n" \
"s_add_u32 m0, s62, 28672\n" \
"s_mov_b32 s54, 1835008\n" \
"buffer_load_dwordx4 v1, s[40:43], s54 offen nt lds\n" \
"s_add_u32 m0, s62, 29696\n" \
"s_mov_b32 s54, 1900544\n" \
"buffer_load_dwordx4 v1, s[40:43], s54 offen nt lds\n" \
"s_add_u32 m0, s62, 30720\n" \
"s_mov_b32 s54, 1966080\n" \
"buffer_load_dwordx4 v1, s[40:43], s54 offen nt lds\n" \
"s_add_u32 m0, s62, 31744\n" \
"s_mov_b32 s54, 2031616\n" \
"buffer_load_dwordx4 v1, s[40:43], s54 offen nt lds\n" \
"s_add_u32 m0, s62, 32768\n" \
"s_xor_b32 s54, s63, 0\n" \
"buffer_load_dwordx4 v2, s[64:67], s54 offen lds\n" \
"s_add_u32 m0, s62, 33792\n" \
"s_xor_b32 s54, s63, 32768\n" \
"buffer_load_dwordx4 v2, s[64:67], s54 offen lds\n" \
"s_add_u32 m0, s62, 34816\n" \
"s_xor_b32 s54, s63, 65536\n" \
"buffer_load_dwordx4 v2, s[64:67], s54 offen lds\n" \
"s_add_u32 m0, s62, 35840\n" \
"s_xor_b32 s54, s63, 98304\n" \
"buffer_load_dwordx4 v2, s[64:67], s54 offen lds\n" \
"s_add_u32 m0, s62, 36864\n" \
"s_xor_b32 s54, s63, 131072\n" \
"buffer_load_dwordx4 v2, s[64:67], s54 offen lds\n" \
"s_add_u32 m0, s62, 37888\n" \
"s_xor_b32 s54, s63, 163840\n" \
"buffer_load_dwordx4 v2, s[64:67], s54 offen lds\n" \
"s_add_u32 m0, s62, 38912\n" \
"s_xor_b32 s54, s63, 196608\n" \
"buffer_load_dwordx4 v2, s[64:67], s54 offen lds\n" \
"s_add_u32 m0, s62, 39936\n" \
"s_xor_b32 s54, s63, 229376\n" \
"buffer_load_dwordx4 v2, s[64:67], s54 offen lds\n" \
"s_waitcnt vmcnt(32)\n" \
"s_barrier\n" \
"ds_read_b64_tr_b16 v[128:129], v3 offset:0\n" \
"ds_read_b64_tr_b16 v[130:131], v3 offset:512\n" \
"ds_read_b64_tr_b16 v[132:133], v3 offset:4096\n" \
"ds_read_b64_tr_b16 v[134:135], v3 offset:4608\n" \
"ds_read_b64_tr_b16 v[160:161], v32 offset:0\n" \
"ds_read_b64_tr_b16 v[162:163], v32 offset:512\n" \
"ds_read_b64_tr_b16 v[164:165], v32 offset:4096\n" \
"ds_read_b64_tr_b16 v[166:167], v32 offset:4608\n" \
"ds_read_b64_tr_b16 v[192:193], v33 offset:0\n" \
"ds_read_b64_tr_b16 v[194:195], v33 offset:512\n" \
"ds_read_b64_tr_b16 v[196:197], v33 offset:4096\n" \
"ds_read_b64_tr_b16 v[198:199], v33 offset:4608\n" \
"ds_read_b64_tr_b16 v[224:225], v34 offset:0\n" \
"ds_read_b64_tr_b16 v[226:227], v34 offset:512\n" \
"ds_read_b64_tr_b16 v[228:229], v34 offset:4096\n" \
"ds_read_b64_tr_b16 v[230:231], v34 offset:4608\n" \
"s_waitcnt lgkmcnt(0)\n" \
"s_barrier\n" \
"s_add_u32 m0, s62, 0\n" \
"s_xor_b32 s54, s63, 262144\n" \
"buffer_load_dwordx4 v2, s[64:67], s54 offen lds\n" \
"s_add_u32 m0, s62, 1024\n" \
"s_xor_b32 s54, s63, 294912\n" \
"buffer_load_dwordx4 v2, s[64:67], s54 offen lds\n" \
"s_add_u32 m0, s62, 2048\n" \
"s_xor_b32 s54, s63, 327680\n" \
"buffer_load_dwordx4 v2, s[64:67], s54 offen lds\n" \
"s_add_u32 m0, s62, 3072\n" \
"s_xor_b32 s54, s63, 360448\n" \
"buffer_load_dwordx4 v2, s[64:67], s54 offen lds\n" \
"s_add_u32 m0, s62, 4096\n" \
"s_xor_b32 s54, s63, 393216\n" \
"buffer_load_dwordx4 v2, s[64:67], s54 offen lds\n" \
"s_add_u32 m0, s62, 5120\n" \
"s_xor_b32 s54, s63, 425984\n" \
"buffer_load_dwordx4 v2, s[64:67], s54 offen lds\n" \
"s_add_u32 m0, s62, 6144\n" \
"s_xor_b32 s54, s63, 458752\n" \
"buffer_load_dwordx4 v2, s[64:67], s54 offen lds\n" \
"s_add_u32 m0, s62, 7168\n" \
"s_xor_b32 s54, s63, 491520\n" \
"buffer_load_dwordx4 v2, s[64:67], s54 offen lds\n" \
"s_waitcnt vmcnt(32)\n" \
"s_barrier\n" \
"ds_read_b64_tr_b16 v[136:137], v3 offset:8192\n" \
"ds_read_b64_tr_b16 v[138:139], v3 offset:8704\n" \
"ds_read_b64_tr_b16 v[140:141], v3 offset:12288\n" \
"ds_read_b64_tr_b16 v[142:143], v3 offset:12800\n" \
"ds_read_b64_tr_b16 v[168:169], v32 offset:8192\n" \
"ds_read_b64_tr_b16 v[170:171], v32 offset:8704\n" \
"ds_read_b64_tr_b16 v[172:173], v32 offset:12288\n" \
"ds_read_b64_tr_b16 v[174:175], v32 offset:12800\n" \
"ds_read_b64_tr_b16 v[200:201], v33 offset:8192\n" \
"ds_read_b64_tr_b16 v[202:203], v33 offset:8704\n" \
"ds_read_b64_tr_b16 v[204:205], v33 offset:12288\n" \
"ds_read_b64_tr_b16 v[206:207], v33 offset:12800\n" \
"ds_read_b64_tr_b16 v[232:233], v34 offset:8192\n" \
"ds_read_b64_tr_b16 v[234:235], v34 offset:8704\n" \
"ds_read_b64_tr_b16 v[236:237], v34 offset:12288\n" \
"ds_read_b64_tr_b16 v[238:239], v34 offset:12800\n" \
"s_waitcnt lgkmcnt(0)\n" \
"s_barrier\n" \
"s_add_u32 m0, s62, 8192\n" \
"s_xor_b32 s54, s63, 524288\n" \
"buffer_load_dwordx4 v2, s[64:67], s54 offen lds\n" \
"s_add_u32 m0, s62, 9216\n" \
"s_xor_b32 s54, s63, 557056\n" \
"buffer_load_dwordx4 v2, s[64:67], s54 offen lds\n" \
"s_add_u32 m0, s62, 10240\n" \
"s_xor_b32 s54, s63, 589824\n" \
"buffer_load_dwordx4 v2, s[64:67], s54 offen lds\n" \
"s_add_u32 m0, s62, 11264\n" \
"s_xor_b32 s54, s63, 622592\n" \
"buffer_load_dwordx4 v2, s[64:67], s54 offen lds\n" \
"s_add_u32 m0, s62, 12288\n" \
"s_xor_b32 s54, s63, 655360\n" \
"buffer_load_dwordx4 v2, s[64:67], s54 offen lds\n" \
"s_add_u32 m0, s62, 13312\n" \
"s_xor_b32 s54, s63, 688128\n" \
"buffer_load_dwordx4 v2, s[64:67], s54 offen lds\n" \
"s_add_u32 m0, s62, 14336\n" \
"s_xor_b32 s54, s63, 720896\n" \
"buffer_load_dwordx4 v2, s[64:67], s54 offen lds\n" \
"s_add_u32 m0, s62, 15360\n" \
"s_xor_b32 s54, s63, 753664\n" \
"buffer_load_dwordx4 v2, s[64:67], s54 offen lds\n" \
"s_waitcnt vmcnt(32)\n" \
"s_barrier\n" \
"ds_read_b64_tr_b16 v[144:145], v3 offset:16384\n" \
"ds_read_b64_tr_b16 v[146:147], v3 offset:16896\n" \
"ds_read_b64_tr_b16 v[148:149], v3 offset:20480\n" \
"ds_read_b64_tr_b16 v[150:151], v3 offset:20992\n" \
"ds_read_b64_tr_b16 v[176:177], v32 offset:16384\n" \
"ds_read_b64_tr_b16 v[178:179], v32 offset:16896\n" \
"ds_read_b64_tr_b16 v[180:181], v32 offset:20480\n" \
"ds_read_b64_tr_b16 v[182:183], v32 offset:20992\n" \
"ds_read_b64_tr_b16 v[208:209], v33 offset:16384\n" \
"ds_read_b64_tr_b16 v[210:211], v33 offset:16896\n" \
"ds_read_b64_tr_b16 v[212:213], v33 offset:20480\n" \
"ds_read_b64_tr_b16 v[214:215], v33 offset:20992\n" \
"ds_read_b64_tr_b16 v[240:241], v34 offset:16384\n" \
"ds_read_b64_tr_b16 v[242:243], v34 offset:16896\n" \
"ds_read_b64_tr_b16 v[244:245], v34 offset:20480\n" \
"ds_read_b64_tr_b16 v[246:247], v34 offset:20992\n" \
"s_waitcnt lgkmcnt(0)\n" \
"s_barrier\n" \
"s_add_u32 m0, s62, 16384\n" \
"s_xor_b32 s54, s63, 786432\n" \
"buffer_load_dwordx4 v2, s[64:67], s54 offen lds\n" \
"s_add_u32 m0, s62, 17408\n" \
"s_xor_b32 s54, s63, 819200\n" \
"buffer_load_dwordx4 v2, s[64:67], s54 offen lds\n" \
"s_add_u32 m0, s62, 18432\n" \
"s_xor_b32 s54, s63, 851968\n" \
"buffer_load_dwordx4 v2, s[64:67], s54 offen lds\n" \
"s_add_u32 m0, s62, 19456\n" \
"s_xor_b32 s54, s63, 884736\n" \
"buffer_load_dwordx4 v2, s[64:67], s54 offen lds\n" \
"s_add_u32 m0, s62, 20480\n" \
"s_xor_b32 s54, s63, 917504\n" \
"buffer_load_dwordx4 v2, s[64:67], s54 offen lds\n" \
"s_add_u32 m0, s62, 21504\n" \
"s_xor_b32 s54, s63, 950272\n" \
"buffer_load_dwordx4 v2, s[64:67], s54 offen lds\n" \
"s_add_u32 m0, s62, 22528\n" \
"s_xor_b32 s54, s63, 983040\n" \
"buffer_load_dwordx4 v2, s[64:67], s54 offen lds\n" \
"s_add_u32 m0, s62, 23552\n" \
"s_xor_b32 s54, s63, 1015808\n" \
"buffer_load_dwordx4 v2, s[64:67], s54 offen lds\n" \
"s_waitcnt vmcnt(32)\n" \
"s_barrier\n" \
"ds_read_b64_tr_b16 v[152:153], v3 offset:24576\n" \
"ds_read_b64_tr_b16 v[154:155], v3 offset:25088\n" \
"ds_read_b64_tr_b16 v[156:157], v3 offset:28672\n" \
"ds_read_b64_tr_b16 v[158:159], v3 offset:29184\n" \
"ds_read_b64_tr_b16 v[184:185], v32 offset:24576\n" \
"ds_read_b64_tr_b16 v[186:187], v32 offset:25088\n" \
"ds_read_b64_tr_b16 v[188:189], v32 offset:28672\n" \
"ds_read_b64_tr_b16 v[190:191], v32 offset:29184\n" \
"ds_read_b64_tr_b16 v[216:217], v33 offset:24576\n" \
"ds_read_b64_tr_b16 v[218:219], v33 offset:25088\n" \
"ds_read_b64_tr_b16 v[220:221], v33 offset:28672\n" \
"ds_read_b64_tr_b16 v[222:223], v33 offset:29184\n" \
"ds_read_b64_tr_b16 v[248:249], v34 offset:24576\n" \
"ds_read_b64_tr_b16 v[250:251], v34 offset:25088\n" \
"ds_read_b64_tr_b16 v[252:253], v34 offset:28672\n" \
"ds_read_b64_tr_b16 v[254:255], v34 offset:29184\n" \
"s_waitcnt lgkmcnt(0)\n" \
"s_barrier\n" \
"s_add_u32 m0, s62, 24576\n" \
"s_xor_b32 s54, s63, 1048576\n" \
"buffer_load_dwordx4 v2, s[64:67], s54 offen lds\n" \
"s_add_u32 m0, s62, 25600\n" \
"s_xor_b32 s54, s63, 1081344\n" \
"buffer_load_dwordx4 v2, s[64:67], s54 offen lds\n" \
"s_add_u32 m0, s62, 26624\n" \
"s_xor_b32 s54, s63, 1114112\n" \
"buffer_load_dwordx4 v2, s[64:67], s54 offen lds\n" \
"s_add_u32 m0, s62, 27648\n" \
"s_xor_b32 s54, s63, 1146880\n" \
"buffer_load_dwordx4 v2, s[64:67], s54 offen lds\n" \
"s_add_u32 m0, s62, 28672\n" \
"s_xor_b32 s54, s63, 1179648\n" \
"buffer_load_dwordx4 v2, s[64:67], s54 offen lds\n" \
"s_add_u32 m0, s62, 29696\n" \
"s_xor_b32 s54, s63, 1212416\n" \
"buffer_load_dwordx4 v2, s[64:67], s54 offen lds\n" \
"s_add_u32 m0, s62, 30720\n" \
"s_xor_b32 s54, s63, 1245184\n" \
"buffer_load_dwordx4 v2, s[64:67], s54 offen lds\n" \
"s_add_u32 m0, s62, 31744\n" \
"s_xor_b32 s54, s63, 1277952\n" \
"buffer_load_dwordx4 v2, s[64:67], s54 offen lds\n" \
"s_waitcnt vmcnt(36)\n" \
"ds_read_b64_tr_b16 a[0:1], v4 offset:32768\n" \
"ds_read_b64_tr_b16 a[2:3], v4 offset:32896\n" \
"ds_read_b64_tr_b16 a[4:5], v4 offset:34816\n" \
"ds_read_b64_tr_b16 a[6:7], v4 offset:34944\n" \
"ds_read_b64_tr_b16 a[8:9], v4 offset:32800\n" \
"ds_read_b64_tr_b16 a[10:11], v4 offset:32928\n" \
"ds_read_b64_tr_b16 a[12:13], v4 offset:34848\n" \
"ds_read_b64_tr_b16 a[14:15], v4 offset:34976\n" \
"s_waitcnt lgkmcnt(0)\n" \
"s_add_u32 m0, s62, 32768\n" \
"s_xor_b32 s54, s63, 1310720\n" \
"buffer_load_dwordx4 v2, s[64:67], s54 offen lds\n" \
"s_add_u32 m0, s62, 33792\n" \
"s_xor_b32 s54, s63, 1343488\n" \
"buffer_load_dwordx4 v2, s[64:67], s54 offen lds\n" \
"s_add_u32 m0, s62, 34816\n" \
"s_xor_b32 s54, s63, 1376256\n" \
"buffer_load_dwordx4 v2, s[64:67], s54 offen lds\n" \
"s_add_u32 m0, s62, 35840\n" \
"s_xor_b32 s54, s63, 1409024\n" \
"buffer_load_dwordx4 v2, s[64:67], s54 offen lds\n" \
"s_waitcnt vmcnt(36)\n" \
"ds_read_b64_tr_b16 a[16:17], v4 offset:36864\n" \
"ds_read_b64_tr_b16 a[18:19], v4 offset:36992\n" \
"ds_read_b64_tr_b16 a[20:21], v4 offset:38912\n" \
"ds_read_b64_tr_b16 a[22:23], v4 offset:39040\n" \
"ds_read_b64_tr_b16 a[24:25], v4 offset:36896\n" \
"ds_read_b64_tr_b16 a[26:27], v4 offset:37024\n" \
"ds_read_b64_tr_b16 a[28:29], v4 offset:38944\n" \
"ds_read_b64_tr_b16 a[30:31], v4 offset:39072\n" \
"s_waitcnt lgkmcnt(0)\n" \
"s_add_u32 m0, s62, 36864\n" \
"s_xor_b32 s54, s63, 1441792\n" \
"buffer_load_dwordx4 v2, s[64:67], s54 offen lds\n" \
"s_add_u32 m0, s62, 37888\n" \
"s_xor_b32 s54, s63, 1474560\n" \
"buffer_load_dwordx4 v2, s[64:67], s54 offen lds\n" \
"s_add_u32 m0, s62, 38912\n" \
"s_xor_b32 s54, s63, 1507328\n" \
"buffer_load_dwordx4 v2, s[64:67], s54 offen lds\n" \
"s_add_u32 m0, s62, 39936\n" \
"s_xor_b32 s54, s63, 1540096\n" \
"buffer_load_dwordx4 v2, s[64:67], s54 offen lds\n" \
"s_waitcnt vmcnt(36)\n" \
"ds_read_b64_tr_b16 a[32:33], v4 offset:0\n" \
"ds_read_b64_tr_b16 a[34:35], v4 offset:128\n" \
"ds_read_b64_tr_b16 a[36:37], v4 offset:2048\n" \
"ds_read_b64_tr_b16 a[38:39], v4 offset:2176\n" \
"ds_read_b64_tr_b16 a[40:41], v4 offset:32\n" \
"ds_read_b64_tr_b16 a[42:43], v4 offset:160\n" \
"ds_read_b64_tr_b16 a[44:45], v4 offset:2080\n" \
"ds_read_b64_tr_b16 a[46:47], v4 offset:2208\n" \
"s_waitcnt lgkmcnt(0)\n" \
"s_add_u32 m0, s62, 0\n" \
"s_xor_b32 s54, s63, 1572864\n" \
"buffer_load_dwordx4 v2, s[64:67], s54 offen lds\n" \
"s_add_u32 m0, s62, 1024\n" \
"s_xor_b32 s54, s63, 1605632\n" \
"buffer_load_dwordx4 v2, s[64:67], s54 offen lds\n" \
"s_add_u32 m0, s62, 2048\n" \
"s_xor_b32 s54, s63, 1638400\n" \
"buffer_load_dwordx4 v2, s[64:67], s54 offen lds\n" \
"s_add_u32 m0, s62, 3072\n" \
"s_xor_b32 s54, s63, 1671168\n" \
"buffer_load_dwordx4 v2, s[64:67], s54 offen lds\n" \
"s_waitcnt vmcnt(36)\n" \
"ds_read_b64_tr_b16 a[48:49], v4 offset:4096\n" \
"ds_read_b64_tr_b16 a[50:51], v4 offset:4224\n" \
"ds_read_b64_tr_b16 a[52:53], v4 offset:6144\n" \
"ds_read_b64_tr_b16 a[54:55], v4 offset:6272\n" \
"ds_read_b64_tr_b16 a[56:57], v4 offset:4128\n" \
"ds_read_b64_tr_b16 a[58:59], v4 offset:4256\n" \
"ds_read_b64_tr_b16 a[60:61], v4 offset:6176\n" \
"ds_read_b64_tr_b16 a[62:63], v4 offset:6304\n" \
"s_waitcnt lgkmcnt(0)\n" \
"s_add_u32 m0, s62, 4096\n" \
"s_xor_b32 s54, s63, 1703936\n" \
"buffer_load_dwordx4 v2, s[64:67], s54 offen lds\n" \
"s_add_u32 m0, s62, 5120\n" \
"s_xor_b32 s54, s63, 1736704\n" \
"buffer_load_dwordx4 v2, s[64:67], s54 offen lds\n" \
"s_add_u32 m0, s62, 6144\n" \
"s_xor_b32 s54, s63, 1769472\n" \
"buffer_load_dwordx4 v2, s[64:67], s54 offen lds\n" \
"s_add_u32 m0, s62, 7168\n" \
"s_xor_b32 s54, s63, 1802240\n" \
"buffer_load_dwordx4 v2, s[64:67], s54 offen lds\n" \
"s_waitcnt vmcnt(36)\n" \
"ds_read_b64_tr_b16 a[64:65], v4 offset:8192\n" \
"ds_read_b64_tr_b16 a[66:67], v4 offset:8320\n" \
"ds_read_b64_tr_b16 a[68:69], v4 offset:10240\n" \
"ds_read_b64_tr_b16 a[70:71], v4 offset:10368\n" \
"ds_read_b64_tr_b16 a[72:73], v4 offset:8224\n" \
"ds_read_b64_tr_b16 a[74:75], v4 offset:8352\n" \
"ds_read_b64_tr_b16 a[76:77], v4 offset:10272\n" \
"ds_read_b64_tr_b16 a[78:79], v4 offset:10400\n" \
"s_waitcnt lgkmcnt(0)\n" \
"s_add_u32 m0, s62, 8192\n" \
"s_xor_b32 s54, s63, 1835008\n" \
"buffer_load_dwordx4 v2, s[64:67], s54 offen lds\n" \
"s_add_u32 m0, s62, 9216\n" \
"s_xor_b32 s54, s63, 1867776\n" \
"buffer_load_dwordx4 v2, s[64:67], s54 offen lds\n" \
"s_add_u32 m0, s62, 10240\n" \
"s_xor_b32 s54, s63, 1900544\n" \
"buffer_load_dwordx4 v2, s[64:67], s54 offen lds\n" \
"s_add_u32 m0, s62, 11264\n" \
"s_xor_b32 s54, s63, 1933312\n" \
"buffer_load_dwordx4 v2, s[64:67], s54 offen lds\n" \
"s_waitcnt vmcnt(36)\n" \
"ds_read_b64_tr_b16 a[80:81], v4 offset:12288\n" \
"ds_read_b64_tr_b16 a[82:83], v4 offset:12416\n" \
"ds_read_b64_tr_b16 a[84:85], v4 offset:14336\n" \
"ds_read_b64_tr_b16 a[86:87], v4 offset:14464\n" \
"ds_read_b64_tr_b16 a[88:89], v4 offset:12320\n" \
"ds_read_b64_tr_b16 a[90:91], v4 offset:12448\n" \
"ds_read_b64_tr_b16 a[92:93], v4 offset:14368\n" \
"ds_read_b64_tr_b16 a[94:95], v4 offset:14496\n" \
"s_waitcnt lgkmcnt(0)\n" \
"s_add_u32 m0, s62, 12288\n" \
"s_xor_b32 s54, s63, 1966080\n" \
"buffer_load_dwordx4 v2, s[64:67], s54 offen lds\n" \
"s_add_u32 m0, s62, 13312\n" \
"s_xor_b32 s54, s63, 1998848\n" \
"buffer_load_dwordx4 v2, s[64:67], s54 offen lds\n" \
"s_add_u32 m0, s62, 14336\n" \
"s_xor_b32 s54, s63, 2031616\n" \
"buffer_load_dwordx4 v2, s[64:67], s54 offen lds\n" \
"s_add_u32 m0, s62, 15360\n" \
"s_xor_b32 s54, s63, 2064384\n" \
"buffer_load_dwordx4 v2, s[64:67], s54 offen lds\n" \
"s_waitcnt vmcnt(36)\n" \
"ds_read_b64_tr_b16 a[96:97], v4 offset:16384\n" \
"ds_read_b64_tr_b16 a[98:99], v4 offset:16512\n" \
"ds_read_b64_tr_b16 a[100:101], v4 offset:18432\n" \
"ds_read_b64_tr_b16 a[102:103], v4 offset:18560\n" \
"ds_read_b64_tr_b16 a[104:105], v4 offset:16416\n" \
"ds_read_b64_tr_b16 a[106:107], v4 offset:16544\n" \
"ds_read_b64_tr_b16 a[108:109], v4 offset:18464\n" \
"ds_read_b64_tr_b16 a[110:111], v4 offset:18592\n" \
"s_waitcnt vmcnt(32)\n" \
"ds_read_b64_tr_b16 a[112:113], v4 offset:20480\n" \
"ds_read_b64_tr_b16 a[114:115], v4 offset:20608\n" \
"ds_read_b64_tr_b16 a[116:117], v4 offset:22528\n" \
"ds_read_b64_tr_b16 a[118:119], v4 offset:22656\n" \
"ds_read_b64_tr_b16 a[120:121], v4 offset:20512\n" \
"ds_read_b64_tr_b16 a[122:123], v4 offset:20640\n" \
"ds_read_b64_tr_b16 a[124:125], v4 offset:22560\n" \
"ds_read_b64_tr_b16 a[126:127], v4 offset:22688\n" \
"s_waitcnt vmcnt(28)\n" \
"ds_read_b64_tr_b16 a[128:129], v4 offset:24576\n" \
"ds_read_b64_tr_b16 a[130:131], v4 offset:24704\n" \
"ds_read_b64_tr_b16 a[132:133], v4 offset:26624\n" \
"ds_read_b64_tr_b16 a[134:135], v4 offset:26752\n" \
"ds_read_b64_tr_b16 a[136:137], v4 offset:24608\n" \
"ds_read_b64_tr_b16 a[138:139], v4 offset:24736\n" \
"ds_read_b64_tr_b16 a[140:141], v4 offset:26656\n" \
"ds_read_b64_tr_b16 a[142:143], v4 offset:26784\n" \
"s_waitcnt vmcnt(24)\n" \
"ds_read_b64_tr_b16 a[144:145], v4 offset:28672\n" \
"ds_read_b64_tr_b16 a[146:147], v4 offset:28800\n" \
"ds_read_b64_tr_b16 a[148:149], v4 offset:30720\n" \
"ds_read_b64_tr_b16 a[150:151], v4 offset:30848\n" \
"ds_read_b64_tr_b16 a[152:153], v4 offset:28704\n" \
"ds_read_b64_tr_b16 a[154:155], v4 offset:28832\n" \
"ds_read_b64_tr_b16 a[156:157], v4 offset:30752\n" \
"ds_read_b64_tr_b16 a[158:159], v4 offset:30880\n" \
"s_waitcnt vmcnt(20)\n" \
"ds_read_b64_tr_b16 a[160:161], v4 offset:32768\n" \
"ds_read_b64_tr_b16 a[162:163], v4 offset:32896\n" \
"ds_read_b64_tr_b16 a[164:165], v4 offset:34816\n" \
"ds_read_b64_tr_b16 a[166:167], v4 offset:34944\n" \
"ds_read_b64_tr_b16 a[168:169], v4 offset:32800\n" \
"ds_read_b64_tr_b16 a[170:171], v4 offset:32928\n" \
"ds_read_b64_tr_b16 a[172:173], v4 offset:34848\n" \
"ds_read_b64_tr_b16 a[174:175], v4 offset:34976\n" \
"s_waitcnt vmcnt(16)\n" \
"ds_read_b64_tr_b16 a[176:177], v4 offset:36864\n" \
"ds_read_b64_tr_b16 a[178:179], v4 offset:36992\n" \
"ds_read_b64_tr_b16 a[180:181], v4 offset:38912\n" \
"ds_read_b64_tr_b16 a[182:183], v4 offset:39040\n" \
"ds_read_b64_tr_b16 a[184:185], v4 offset:36896\n" \
"ds_read_b64_tr_b16 a[186:187], v4 offset:37024\n" \
"ds_read_b64_tr_b16 a[188:189], v4 offset:38944\n" \
"ds_read_b64_tr_b16 a[190:191], v4 offset:39072\n" \
"s_waitcnt vmcnt(12)\n" \
"ds_read_b64_tr_b16 a[192:193], v4 offset:0\n" \
"ds_read_b64_tr_b16 a[194:195], v4 offset:128\n" \
"ds_read_b64_tr_b16 a[196:197], v4 offset:2048\n" \
"ds_read_b64_tr_b16 a[198:199], v4 offset:2176\n" \
"ds_read_b64_tr_b16 a[200:201], v4 offset:32\n" \
"ds_read_b64_tr_b16 a[202:203], v4 offset:160\n" \
"ds_read_b64_tr_b16 a[204:205], v4 offset:2080\n" \
"ds_read_b64_tr_b16 a[206:207], v4 offset:2208\n" \
"s_waitcnt vmcnt(8)\n" \
"ds_read_b64_tr_b16 a[208:209], v4 offset:4096\n" \
"ds_read_b64_tr_b16 a[210:211], v4 offset:4224\n" \
"ds_read_b64_tr_b16 a[212:213], v4 offset:6144\n" \
"ds_read_b64_tr_b16 a[214:215], v4 offset:6272\n" \
"ds_read_b64_tr_b16 a[216:217], v4 offset:4128\n" \
"ds_read_b64_tr_b16 a[218:219], v4 offset:4256\n" \
"ds_read_b64_tr_b16 a[220:221], v4 offset:6176\n" \
"ds_read_b64_tr_b16 a[222:223], v4 offset:6304\n" \
"s_waitcnt vmcnt(4)\n" \
"ds_read_b64_tr_b16 a[224:225], v4 offset:8192\n" \
"ds_read_b64_tr_b16 a[226:227], v4 offset:8320\n" \
"ds_read_b64_tr_b16 a[228:229], v4 offset:10240\n" \
"ds_read_b64_tr_b16 a[230:231], v4 offset:10368\n" \
"ds_read_b64_tr_b16 a[232:233], v4 offset:8224\n" \
"ds_read_b64_tr_b16 a[234:235], v4 offset:8352\n" \
"ds_read_b64_tr_b16 a[236:237], v4 offset:10272\n" \
"ds_read_b64_tr_b16 a[238:239], v4 offset:10400\n" \
"s_waitcnt vmcnt(0)\n" \
"ds_read_b64_tr_b16 a[240:241], v4 offset:12288\n" \
"ds_read_b64_tr_b16 a[242:243], v4 offset:12416\n" \
"ds_read_b64_tr_b16 a[244:245], v4 offset:14336\n" \
"ds_read_b64_tr_b16 a[246:247], v4 offset:14464\n" \
"ds_read_b64_tr_b16 a[248:249], v4 offset:12320\n" \
"ds_read_b64_tr_b16 a[250:251], v4 offset:12448\n" \
"ds_read_b64_tr_b16 a[252:253], v4 offset:14368\n" \
"ds_read_b64_tr_b16 a[254:255], v4 offset:14496\n" \
"s_waitcnt vmcnt(0) lgkmcnt(0)\n" \
"s_barrier\n" \
"s_mov_b32 s49, 0\n" \
"s_mov_b32 s52, 0\n" \
"s_mov_b32 s88, 0\n" \
"s_mov_b32 s22, 0xfffffc00\n" \
"s_mov_b32 s23, -1\n" \
"s_mul_i32 s32, s17, 0x1010101\n" \
"s_mov_b32 s33, s32\n" \
"s_mov_b32 s34, s17\n" \
"v_max_u32 v20, 1, v20\n" \
"v_max_u32 v21, 1, v21\n" \
"v_max_u32 v22, 1, v22\n" \
"v_max_u32 v23, 1, v23\n" \
"buffer_store_dwordx4 v[20:23], v8, s[36:39], 0 offen sc1\n" \
"s_mov_b32 s72, 0\n" \
"s_mov_b32 s73, -1\n" \
"v_lshlrev_b32 v31, 1, v0\n" \
"v_add_u32 v31, 16384, v31\n" \
"s_lshl_b32 s79, s48, 10\n" \
"s_add_u32 s79, s79, 0\n" \
"v_xor_b32 v30, 0x1000, v6\n" \
"s_memrealtime s[56:57]\n" \
"s_waitcnt lgkmcnt(0)\n" \
"s_mov_b32 s50, s56\n" \
"s_waitcnt vmcnt(0) lgkmcnt(0)\n" \
"s_add_u32 s54, s49, 0\n" \
"s_and_b32 s54, s54, 3\n" \
"s_mul_i32 s58, s54, 327680\n" \
"s_add_u32 s54, s49, 1\n" \
"s_and_b32 s54, s54, 3\n" \
"s_mul_i32 s59, s54, 327680\n" \
"s_add_u32 s54, s49, 2\n" \
"s_and_b32 s54, s54, 3\n" \
"s_mul_i32 s60, s54, 327680\n" \
"s_add_u32 s54, s49, 3\n" \
"s_and_b32 s54, s54, 3\n" \
"s_mul_i32 s61, s54, 327680\n" \
"s_add_u32 s89, s58, 4096\n" \
"s_add_u32 s90, s58, 16384\n" \
"s_add_u32 s91, s58, 20480\n" \
"s_mov_b32 m0, s79\n" \
"s_mov_b32 s51, s50\n" \
"v_mov_b64 v[80:81], 0\n" \
"v_mov_b64 v[82:83], 0\n" \
"v_mov_b32 v13, -1\n" \
"s_mov_b32 s23, -1\n" \
"s_cmp_eq_u32 s48, 0\n" \
"s_cselect_b32 s80, -1, 0\n" \
"s_mov_b32 s81, 0\n" \
"s_lshl_b32 s54, s48, 3\n" \
"s_add_u32 s54, s54, 20480\n" \
"v_mov_b32 v29, s54\n" \
"s_nop 7\n" \
"s_nop 7\n" \
".p2align 6\n" \
".Lv_loop_%=:\n" \
"s_getpc_b64 s[18:19]\n" \
".Lv_wp_%=:\n" \
"s_add_u32 s18, s18, .Lv_ws_%=-.Lv_wp_%=\n" \
"s_addc_u32 s19, s19, 0\n" \
"s_sub_u32 s21, s50, 5\n" \
"s_nop 0\n" \
".Lv_w_%=:\n" \
"s_memrealtime s[56:57]\n" \
"s_waitcnt lgkmcnt(0)\n" \
"s_sub_u32 s54, s56, s21\n" \
"s_cmp_gt_u32 s54, s22\n" \
"s_cbranch_scc1 .Lv_w_%=\n" \
"s_min_u32 s21, s54, 3\n" \
"s_lshl2_add_u32 s18, s21, s18\n" \
"s_setpc_b64 s[18:19]\n" \
"s_nop 0\n" \
".Lv_ws_%=:\n" \
"s_nop 4\n" \
"s_nop 5\n" \
"s_nop 4\n" \
".Lv_go_%=:\n" \
"buffer_load_dwordx4 v5, s[36:39], s58 offen sc1 lds\n" \
"s_nop 7\n" \
"s_add_u32 m0, s79, 4096\n" \
"s_nop 0\n" \
"buffer_load_dwordx4 v5, s[36:39], s89 offen sc1 lds\n" \
"s_nop 7\n" \
"s_add_u32 m0, s79, 8192\n" \
"s_nop 0\n" \
"buffer_load_dwordx4 v5, s[36:39], s90 offen sc1 lds\n" \
"s_nop 7\n" \
"s_add_u32 m0, s79, 12288\n" \
"s_nop 0\n" \
"buffer_load_dwordx4 v5, s[36:39], s91 offen sc1 lds\n" \
".Lv_x_%=:\n" \
"s_waitcnt vmcnt(2)\n" \
"s_barrier\n" \
"ds_read_b128 v[32:35], v0 offset:0\n" \
"ds_read_b128 v[36:39], v0 offset:1024\n" \
"ds_read_b128 v[40:43], v0 offset:2048\n" \
"ds_read_b128 v[44:47], v0 offset:3072\n" \
"ds_read_b128 v[48:51], v0 offset:4096\n" \
"s_waitcnt lgkmcnt(4)\n" \
"v_smfmac_f32_16x16x64_bf16 v[80:83], v[32:35], v[128:135], v12\n" \
"ds_read_b128 v[52:55], v0 offset:5120\n" \
"s_waitcnt lgkmcnt(4)\n" \
"v_min3_u32 v13, v13, v32, v34\n" \
"v_smfmac_f32_16x16x64_bf16 v[80:83], v[36:39], v[136:143], v12\n" \
"ds_read_b128 v[56:59], v0 offset:6144\n" \
"s_waitcnt lgkmcnt(4)\n" \
"v_min3_u32 v13, v13, v36, v38\n" \
"v_smfmac_f32_16x16x64_bf16 v[80:83], v[40:43], v[144:151], v12\n" \
"ds_read_b128 v[60:63], v0 offset:7168\n" \
"s_waitcnt lgkmcnt(4)\n" \
"v_min3_u32 v13, v13, v40, v42\n" \
"v_smfmac_f32_16x16x64_bf16 v[80:83], v[44:47], v[152:159], v12\n" \
"s_waitcnt vmcnt(0)\n" \
"s_barrier\n" \
"ds_read_b128 v[32:35], v0 offset:8192\n" \
"s_waitcnt lgkmcnt(4)\n" \
"v_min3_u32 v13, v13, v44, v46\n" \
"v_smfmac_f32_16x16x64_bf16 v[80:83], v[48:51], v[160:167], v12\n" \
"ds_read_b128 v[36:39], v0 offset:9216\n" \
"s_waitcnt lgkmcnt(4)\n" \
"v_min3_u32 v13, v13, v48, v50\n" \
"v_cmp_eq_u32 vcc, 0, v13\n" \
"v_smfmac_f32_16x16x64_bf16 v[80:83], v[52:55], v[168:175], v12\n" \
"ds_read_b128 v[40:43], v0 offset:10240\n" \
"s_waitcnt lgkmcnt(4)\n" \
"v_min3_u32 v13, v13, v52, v54\n" \
"v_smfmac_f32_16x16x64_bf16 v[80:83], v[56:59], v[176:183], v12\n" \
"ds_read_b128 v[44:47], v0 offset:11264\n" \
"s_waitcnt lgkmcnt(4)\n" \
"v_min3_u32 v13, v13, v56, v58\n" \
"v_smfmac_f32_16x16x64_bf16 v[80:83], v[60:63], v[184:191], v12\n" \
"ds_read_b128 v[48:51], v0 offset:12288\n" \
"s_waitcnt lgkmcnt(4)\n" \
"s_nop 0\n" \
"s_nop 0\n" \
"s_nop 0\n" \
"s_cbranch_vccnz .Lv_mretry_%=\n" \
"v_min3_u32 v13, v13, v60, v62\n" \
"v_smfmac_f32_16x16x64_bf16 v[80:83], v[32:35], v[192:199], v12\n" \
"ds_read_b128 v[52:55], v0 offset:13312\n" \
"s_waitcnt lgkmcnt(4)\n" \
"v_min3_u32 v13, v13, v32, v34\n" \
"v_smfmac_f32_16x16x64_bf16 v[80:83], v[36:39], v[200:207], v12\n" \
"ds_read_b128 v[56:59], v0 offset:14336\n" \
"s_waitcnt lgkmcnt(4)\n" \
"v_min3_u32 v13, v13, v36, v38\n" \
"v_smfmac_f32_16x16x64_bf16 v[80:83], v[40:43], v[208:215], v12\n" \
"ds_read_b128 v[60:63], v0 offset:15360\n" \
"s_waitcnt lgkmcnt(4)\n" \
"v_min3_u32 v13, v13, v40, v42\n" \
"v_smfmac_f32_16x16x64_bf16 v[80:83], v[44:47], v[216:223], v12\n" \
"s_waitcnt lgkmcnt(3)\n" \
"v_min3_u32 v13, v13, v44, v46\n" \
"v_smfmac_f32_16x16x64_bf16 v[80:83], v[48:51], v[224:231], v12\n" \
"s_waitcnt lgkmcnt(2)\n" \
"v_min3_u32 v13, v13, v48, v50\n" \
"v_smfmac_f32_16x16x64_bf16 v[80:83], v[52:55], v[232:239], v12\n" \
"s_waitcnt lgkmcnt(1)\n" \
"v_min3_u32 v13, v13, v52, v54\n" \
"v_smfmac_f32_16x16x64_bf16 v[80:83], v[56:59], v[240:247], v12\n" \
"s_waitcnt lgkmcnt(0)\n" \
"v_min3_u32 v13, v13, v56, v58\n" \
"v_smfmac_f32_16x16x64_bf16 v[80:83], v[60:63], v[248:255], v12\n" \
"v_min3_u32 v13, v13, v60, v62\n" \
"v_cmp_eq_u32 vcc, 0, v13\n" \
"s_nop 5\n" \
"v_pk_add_f32 v[80:81], v[80:81], v[82:83]\n" \
"s_cbranch_vccnz .Lv_mretry_%=\n" \
"v_cvt_pk_bf16_f32 v84, v80, v81\n" \
"v_cvt_pk_bf16_f32 v85, v80, v81\n" \
"s_nop 1\n" \
"v_permlane16_swap_b32 v84, v85\n" \
"v_max_u32 v84, 1, v84\n" \
"buffer_store_dwordx2 v[84:85], v7, s[36:39], s58 offen sc1\n" \
"buffer_load_dwordx2 v[64:65], v9, s[36:39], s58 offen sc1\n" \
"s_sub_u32 s53, s49, 1\n" \
"s_add_u32 s25, s24, 3\n" \
"s_cmp_eq_u32 s26, s24\n" \
"s_subb_u32 s25, s25, 0\n" \
"s_cmp_eq_u32 s28, s24\n" \
"s_subb_u32 s25, s25, 0\n" \
"s_cmp_eq_u32 s30, s24\n" \
"s_subb_u32 s25, s25, 0\n" \
"s_sub_u32 s24, s25, s55\n" \
"s_min_u32 s24, s24, 255\n" \
"s_cmp_eq_u32 s23, s53\n" \
"s_cselect_b32 s24, s17, s24\n" \
"s_lshl_b64 s[32:33], s[32:33], 8\n" \
"s_or_b32 s32, s32, s24\n" \
"s_bfe_u32 s24, s32, 524296\n" \
"s_bfe_u32 s25, s33, 524296\n" \
"s_min_u32 s34, s24, s25\n" \
"s_cmp_eq_u32 s49, 63\n" \
"s_nop 0\n" \
"s_nop 0\n" \
"s_nop 0\n" \
"s_cbranch_scc1 .Lv_end_%=\n" \
"v_mov_b64 v[80:81], 0\n" \
"v_mov_b64 v[82:83], 0\n" \
"v_mov_b64 v[84:85], 0\n" \
"v_mov_b64 v[86:87], 0\n" \
"v_mov_b64 v[88:89], 0\n" \
"v_mov_b64 v[90:91], 0\n" \
"v_mov_b64 v[92:93], 0\n" \
"v_mov_b64 v[94:95], 0\n" \
"s_waitcnt vmcnt(0)\n" \
"v_cmp_eq_u32_e64 vcc, 0, v64\n" \
"s_nop 0\n" \
"s_and_b64 vcc, vcc, s[80:81]\n" \
"s_nop 0\n" \
"s_nop 0\n" \
"s_cbranch_vccnz .Lv_h_cold_%=\n" \
".Lv_h_ok_%=:\n" \
"v_subrev_u32 v64, s51, v64\n" \
"v_max_i32_dpp v65, v65, v65 row_ror:8 row_mask:0xf bank_mask:0xf\n" \
"s_nop 0\n" \
"v_max_i32_dpp v64, v64, v64 row_ror:8 row_mask:0xf bank_mask:0xf\n" \
"v_max_i32_dpp v65, v65, v65 row_ror:4 row_mask:0xf bank_mask:0xf\n" \
"s_nop 0\n" \
"v_max_i32_dpp v64, v64, v64 row_ror:4 row_mask:0xf bank_mask:0xf\n" \
"v_max_i32_dpp v65, v65, v65 row_ror:2 row_mask:0xf bank_mask:0xf\n" \
"s_nop 0\n" \
"v_max_i32_dpp v64, v64, v64 row_ror:2 row_mask:0xf bank_mask:0xf\n" \
"v_max_i32_dpp v65, v65, v65 row_ror:1 row_mask:0xf bank_mask:0xf\n" \
"s_nop 0\n" \
"v_max_i32_dpp v64, v64, v64 row_ror:1 row_mask:0xf bank_mask:0xf\n" \
"v_readlane_b32 s53, v65, 0\n" \
"v_readlane_b32 s50, v65, 16\n" \
"v_readlane_b32 s54, v64, 0\n" \
"v_readlane_b32 s55, v64, 16\n" \
"s_max_i32 s50, s50, s53\n" \
"s_max_i32 s55, s55, s54\n" \
"s_add_u32 s55, s55, s51\n" \
"s_add_u32 s55, s55, s44\n" \
"s_add_u32 s50, s50, s55\n" \
"s_add_u32 s50, s50, s45\n" \
"v_mov_b32 v64, s55\n" \
"v_mov_b32 v65, s50\n" \
"ds_write_b64 v29, v[64:65]\n" \
"buffer_store_dwordx2 v[16:17], v7, s[36:39], s60 offen sc1\n" \
"buffer_store_dwordx4 v[16:19], v8, s[36:39], s61 offen sc1\n" \
"v_mov_b32_e64 v13, s34\n" \
"buffer_store_dword v13, v28, s[36:39], 0 offen sc1\n" \
"s_waitcnt lgkmcnt(0)\n" \
"s_barrier\n" \
"ds_read_b64 v[64:65], v16 offset:20480\n" \
"s_waitcnt lgkmcnt(0)\n" \
"v_readfirstlane_b32 s55, v64\n" \
"v_readfirstlane_b32 s50, v65\n" \
"v_mov_b32 v13, -1\n" \
"s_getpc_b64 s[18:19]\n" \
".Lv_wop_%=:\n" \
"s_add_u32 s18, s18, .Lv_wos_%=-.Lv_wop_%=\n" \
"s_addc_u32 s19, s19, 0\n" \
"s_sub_u32 s21, s55, 5\n" \
"s_nop 0\n" \
".Lv_wo_%=:\n" \
"s_memrealtime s[56:57]\n" \
"s_waitcnt lgkmcnt(0)\n" \
"s_sub_u32 s54, s56, s21\n" \
"s_cmp_gt_u32 s54, s22\n" \
"s_cbranch_scc1 .Lv_wo_%=\n" \
"s_min_u32 s21, s54, 3\n" \
"s_lshl2_add_u32 s18, s21, s18\n" \
"s_setpc_b64 s[18:19]\n" \
"s_nop 0\n" \
".Lv_wos_%=:\n" \
"s_nop 4\n" \
"s_nop 5\n" \
"s_nop 4\n" \
"buffer_load_dwordx4 v[32:35], v6, s[36:39], s58 offen offset:0 sc1\n" \
"s_cmp_lt_u32 s54, 8\n" \
"s_cselect_b32 s23, s23, s49\n" \
"s_nop 8\n" \
"buffer_load_dwordx4 v[36:39], v6, s[36:39], s58 offen offset:1024 sc1\n" \
"s_nop 10\n" \
"buffer_load_dwordx4 v[40:43], v6, s[36:39], s58 offen offset:2048 sc1\n" \
"s_nop 10\n" \
"buffer_load_dwordx4 v[44:47], v6, s[36:39], s58 offen offset:3072 sc1\n" \
"s_nop 10\n" \
"buffer_load_dwordx4 v[48:51], v30, s[36:39], s58 offen offset:0 sc1\n" \
"s_nop 10\n" \
"buffer_load_dwordx4 v[52:55], v30, s[36:39], s58 offen offset:1024 sc1\n" \
"s_nop 10\n" \
"buffer_load_dwordx4 v[56:59], v30, s[36:39], s58 offen offset:2048 sc1\n" \
"s_nop 10\n" \
"buffer_load_dwordx4 v[60:63], v30, s[36:39], s58 offen offset:3072 sc1\n" \
".Lv_y_%=:\n" \
"s_waitcnt vmcnt(7)\n" \
"v_smfmac_f32_16x16x64_bf16 v[80:83], v[32:35], a[0:7], v12\n" \
"v_min3_u32 v13, v13, v32, v34\n" \
"v_smfmac_f32_16x16x64_bf16 v[84:87], v[32:35], a[8:15], v12\n" \
"v_smfmac_f32_16x16x64_bf16 v[88:91], v[32:35], a[16:23], v12\n" \
"v_smfmac_f32_16x16x64_bf16 v[92:95], v[32:35], a[24:31], v12\n" \
"s_waitcnt vmcnt(6)\n" \
"v_smfmac_f32_16x16x64_bf16 v[80:83], v[36:39], a[32:39], v12\n" \
"v_min3_u32 v13, v13, v36, v38\n" \
"v_smfmac_f32_16x16x64_bf16 v[84:87], v[36:39], a[40:47], v12\n" \
"v_cmp_eq_u32_e64 vcc, 0, v13\n" \
"v_smfmac_f32_16x16x64_bf16 v[88:91], v[36:39], a[48:55], v12\n" \
"v_smfmac_f32_16x16x64_bf16 v[92:95], v[36:39], a[56:63], v12\n" \
"s_cbranch_vccnz .Lv_retry_%=\n" \
"s_waitcnt vmcnt(5)\n" \
"v_smfmac_f32_16x16x64_bf16 v[80:83], v[40:43], a[64:71], v12\n" \
"v_min3_u32 v13, v13, v40, v42\n" \
"v_smfmac_f32_16x16x64_bf16 v[84:87], v[40:43], a[72:79], v12\n" \
"v_smfmac_f32_16x16x64_bf16 v[88:91], v[40:43], a[80:87], v12\n" \
"v_smfmac_f32_16x16x64_bf16 v[92:95], v[40:43], a[88:95], v12\n" \
"s_waitcnt vmcnt(4)\n" \
"v_smfmac_f32_16x16x64_bf16 v[80:83], v[44:47], a[96:103], v12\n" \
"v_min3_u32 v13, v13, v44, v46\n" \
"v_smfmac_f32_16x16x64_bf16 v[84:87], v[44:47], a[104:111], v12\n" \
"v_smfmac_f32_16x16x64_bf16 v[88:91], v[44:47], a[112:119], v12\n" \
"v_smfmac_f32_16x16x64_bf16 v[92:95], v[44:47], a[120:127], v12\n" \
"s_waitcnt vmcnt(3)\n" \
"v_smfmac_f32_16x16x64_bf16 v[80:83], v[48:51], a[128:135], v12\n" \
"v_min3_u32 v13, v13, v48, v50\n" \
"v_smfmac_f32_16x16x64_bf16 v[84:87], v[48:51], a[136:143], v12\n" \
"v_smfmac_f32_16x16x64_bf16 v[88:91], v[48:51], a[144:151], v12\n" \
"v_smfmac_f32_16x16x64_bf16 v[92:95], v[48:51], a[152:159], v12\n" \
"s_waitcnt vmcnt(2)\n" \
"v_smfmac_f32_16x16x64_bf16 v[80:83], v[52:55], a[160:167], v12\n" \
"v_min3_u32 v13, v13, v52, v54\n" \
"v_smfmac_f32_16x16x64_bf16 v[84:87], v[52:55], a[168:175], v12\n" \
"v_smfmac_f32_16x16x64_bf16 v[88:91], v[52:55], a[176:183], v12\n" \
"v_smfmac_f32_16x16x64_bf16 v[92:95], v[52:55], a[184:191], v12\n" \
"s_waitcnt vmcnt(1)\n" \
"v_smfmac_f32_16x16x64_bf16 v[80:83], v[56:59], a[192:199], v12\n" \
"v_min3_u32 v13, v13, v56, v58\n" \
"v_smfmac_f32_16x16x64_bf16 v[84:87], v[56:59], a[200:207], v12\n" \
"v_smfmac_f32_16x16x64_bf16 v[88:91], v[56:59], a[208:215], v12\n" \
"v_smfmac_f32_16x16x64_bf16 v[92:95], v[56:59], a[216:223], v12\n" \
"s_waitcnt vmcnt(0)\n" \
"v_smfmac_f32_16x16x64_bf16 v[80:83], v[60:63], a[224:231], v12\n" \
"v_min3_u32 v13, v13, v60, v62\n" \
"v_smfmac_f32_16x16x64_bf16 v[84:87], v[60:63], a[232:239], v12\n" \
"v_cmp_eq_u32 vcc, 0, v13\n" \
"v_smfmac_f32_16x16x64_bf16 v[88:91], v[60:63], a[240:247], v12\n" \
"v_smfmac_f32_16x16x64_bf16 v[92:95], v[60:63], a[248:255], v12\n" \
"s_cbranch_vccnz .Lv_retry_%=\n" \
".Lv_y2_%=:\n" \
"v_pk_add_f32 v[80:81], v[80:81], v[82:83]\n" \
"v_pk_add_f32 v[84:85], v[84:85], v[86:87]\n" \
"s_nop 0\n" \
"v_pk_add_f32 v[88:89], v[88:89], v[90:91]\n" \
"s_nop 2\n" \
"v_pk_add_f32 v[92:93], v[92:93], v[94:95]\n" \
"v_cndmask_b32_e64 v80, v80, v88, s[72:73]\n" \
"v_cndmask_b32_e64 v81, v81, v89, s[72:73]\n" \
"v_cndmask_b32_e64 v84, v84, v92, s[72:73]\n" \
"v_cndmask_b32_e64 v85, v85, v93, s[72:73]\n" \
"s_nop 0\n" \
"v_permlane32_swap_b32 v80, v84\n" \
"v_permlane32_swap_b32 v81, v85\n" \
"v_pk_add_f32 v[80:81], v[80:81], v[84:85]\n" \
"ds_write2_b32 v15, v80, v81 offset1:32\n" \
"s_cmp_lg_u32 s48, 0\n" \
"s_waitcnt lgkmcnt(0)\n" \
"s_barrier\n" \
"s_nop 0\n" \
"s_nop 0\n" \
"s_nop 0\n" \
"s_cbranch_scc1 .Lv_skip_%=\n" \
"ds_read_b128 v[64:67], v31\n" \
"ds_read_b128 v[68:71], v31 offset:16\n" \
"s_waitcnt lgkmcnt(1)\n" \
"v_pk_add_f32 v[64:65], v[64:65], v[66:67]\n" \
"s_waitcnt lgkmcnt(0)\n" \
"v_pk_add_f32 v[68:69], v[68:69], v[70:71]\n" \
"v_pk_add_f32 v[64:65], v[64:65], v[68:69]\n" \
"v_cvt_pk_bf16_f32 v72, v64, v65\n" \
"v_max_u32 v72, 1, v72\n" \
"s_nop 1\n" \
"v_mov_b32_dpp v73, v72 quad_perm:[1,1,1,1] row_mask:0xf bank_mask:0xf\n" \
"v_mov_b32_dpp v74, v72 quad_perm:[2,2,2,2] row_mask:0xf bank_mask:0xf\n" \
"v_mov_b32_dpp v75, v72 quad_perm:[3,3,3,3] row_mask:0xf bank_mask:0xf\n" \
".Lv_skip_%=:\n" \
"buffer_store_dwordx4 v[72:75], v8, s[36:39], s59 offen sc1\n" \
"s_memrealtime s[24:25]\n" \
"s_mov_b32 s54, s58\n" \
"s_mov_b32 s58, s59\n" \
"s_mov_b32 s59, s60\n" \
"s_mov_b32 s60, s61\n" \
"s_mov_b32 s61, s54\n" \
"s_memrealtime s[26:27]\n" \
"s_add_u32 s89, s58, 4096\n" \
"s_add_u32 s90, s58, 16384\n" \
"s_add_u32 s91, s58, 20480\n" \
"s_mov_b32 m0, s79\n" \
"s_mov_b32 s51, s50\n" \
"s_memrealtime s[28:29]\n" \
"v_mov_b64 v[80:81], 0\n" \
"v_mov_b64 v[82:83], 0\n" \
"v_mov_b32 v13, -1\n" \
"s_add_u32 s49, s49, 1\n" \
"s_memrealtime s[30:31]\n" \
"s_branch .Lv_loop_%=\n" \
".Lv_h_cold_%=:\n" \
"s_nop 7\n" \
"s_nop 7\n" \
"s_waitcnt vmcnt(0) lgkmcnt(0)\n" \
"s_mov_b32 s53, 0\n" \
".Lv_h_again_%=:\n" \
"s_cmp_lg_u32 s52, 0\n" \
"s_cbranch_scc1 .Lv_h_ok_%=\n" \
"s_sleep 1\n" \
"buffer_load_dwordx2 v[64:65], v9, s[36:39], s58 offen sc1\n" \
"s_waitcnt vmcnt(0)\n" \
"v_cmp_eq_u32 vcc, 0, v64\n" \
"s_nop 0\n" \
"s_and_b64 vcc, vcc, s[80:81]\n" \
"s_nop 4\n" \
"s_cbranch_vccz .Lv_h_ok_%=\n" \
"s_add_u32 s53, s53, 1\n" \
"s_cmp_lt_u32 s53, 32768\n" \
"s_cbranch_scc1 .Lv_h_again_%=\n" \
"s_mov_b32 s52, 1\n" \
"v_mov_b32 v13, 256\n" \
"v_mov_b32 v14, 1\n" \
"buffer_store_dword v14, v13, s[36:39], 0 offen sc1\n" \
"s_waitcnt vmcnt(0) lgkmcnt(0)\n" \
"s_branch .Lv_h_ok_%=\n" \
".Lv_retry_%=:\n" \
"s_nop 7\n" \
"s_nop 7\n" \
"s_waitcnt vmcnt(0) lgkmcnt(0)\n" \
"s_mov_b32 s23, s49\n" \
"s_cmp_lg_u32 s52, 0\n" \
"s_cbranch_scc1 .Lv_y2_%=\n" \
"buffer_load_dwordx4 v[32:35], v6, s[36:39], s58 offen offset:0 sc1\n" \
"buffer_load_dwordx4 v[36:39], v6, s[36:39], s58 offen offset:1024 sc1\n" \
"buffer_load_dwordx4 v[40:43], v6, s[36:39], s58 offen offset:2048 sc1\n" \
"buffer_load_dwordx4 v[44:47], v6, s[36:39], s58 offen offset:3072 sc1\n" \
"buffer_load_dwordx4 v[48:51], v30, s[36:39], s58 offen offset:0 sc1\n" \
"buffer_load_dwordx4 v[52:55], v30, s[36:39], s58 offen offset:1024 sc1\n" \
"buffer_load_dwordx4 v[56:59], v30, s[36:39], s58 offen offset:2048 sc1\n" \
"buffer_load_dwordx4 v[60:63], v30, s[36:39], s58 offen offset:3072 sc1\n" \
"s_waitcnt vmcnt(6)\n" \
"v_min3_u32 v13, v32, v34, v36\n" \
"s_waitcnt vmcnt(5)\n" \
"v_min3_u32 v13, v13, v38, v40\n" \
"s_waitcnt vmcnt(4)\n" \
"v_min3_u32 v13, v13, v42, v44\n" \
"s_waitcnt vmcnt(3)\n" \
"v_min3_u32 v13, v13, v46, v48\n" \
"s_waitcnt vmcnt(2)\n" \
"v_min3_u32 v13, v13, v50, v52\n" \
"s_waitcnt vmcnt(1)\n" \
"v_min3_u32 v13, v13, v54, v56\n" \
"s_waitcnt vmcnt(0)\n" \
"v_min3_u32 v13, v13, v58, v60\n" \
"v_min3_u32 v13, v13, v62, v62\n" \
"v_cmp_eq_u32 vcc, 0, v13\n" \
"s_nop 0\n" \
"s_cbranch_vccnz .Lv_o_cold_%=\n" \
".Lv_o_ok_%=:\n" \
"v_mov_b64 v[80:81], 0\n" \
"v_mov_b64 v[82:83], 0\n" \
"v_mov_b64 v[84:85], 0\n" \
"v_mov_b64 v[86:87], 0\n" \
"v_mov_b64 v[88:89], 0\n" \
"v_mov_b64 v[90:91], 0\n" \
"v_mov_b64 v[92:93], 0\n" \
"v_mov_b64 v[94:95], 0\n" \
"v_mov_b32 v13, -1\n" \
"s_nop 7\n" \
"s_nop 7\n" \
"s_branch .Lv_y_%=\n" \
".Lv_mretry_%=:\n" \
"s_nop 7\n" \
"s_nop 7\n" \
"v_mov_b64 v[80:81], 0\n" \
"v_mov_b64 v[82:83], 0\n" \
"v_mov_b32 v13, -1\n" \
"s_mov_b32 m0, s79\n" \
"s_waitcnt vmcnt(0) lgkmcnt(0)\n" \
"s_barrier\n" \
"s_add_u32 s88, s88, 1\n" \
"s_cmp_lt_u32 s88, 32768\n" \
"s_cbranch_scc1 .Lv_r1_%=\n" \
"s_mov_b32 s52, 1\n" \
"v_mov_b32 v13, 256\n" \
"v_mov_b32 v14, 1\n" \
"buffer_store_dword v14, v13, s[36:39], 0 offen sc1\n" \
"s_waitcnt vmcnt(0) lgkmcnt(0)\n" \
"s_endpgm\n" \
".Lv_r1_%=:\n" \
"s_cmp_lg_u32 s49, 0\n" \
"s_cbranch_scc1 .Lv_r2_%=\n" \
"s_sleep 8\n" \
".Lv_r2_%=:\n" \
"s_branch .Lv_go_%=\n" \
".Lv_o_cold_%=:\n" \
"s_nop 7\n" \
"s_nop 7\n" \
"s_waitcnt vmcnt(0) lgkmcnt(0)\n" \
"s_mov_b32 s53, 0\n" \
".Lv_o_again_%=:\n" \
"s_cmp_lg_u32 s52, 0\n" \
"s_cbranch_scc1 .Lv_o_ok_%=\n" \
"s_sleep 1\n" \
"buffer_load_dwordx4 v[32:35], v6, s[36:39], s58 offen offset:0 sc1\n" \
"buffer_load_dwordx4 v[36:39], v6, s[36:39], s58 offen offset:1024 sc1\n" \
"buffer_load_dwordx4 v[40:43], v6, s[36:39], s58 offen offset:2048 sc1\n" \
"buffer_load_dwordx4 v[44:47], v6, s[36:39], s58 offen offset:3072 sc1\n" \
"buffer_load_dwordx4 v[48:51], v30, s[36:39], s58 offen offset:0 sc1\n" \
"buffer_load_dwordx4 v[52:55], v30, s[36:39], s58 offen offset:1024 sc1\n" \
"buffer_load_dwordx4 v[56:59], v30, s[36:39], s58 offen offset:2048 sc1\n" \
"buffer_load_dwordx4 v[60:63], v30, s[36:39], s58 offen offset:3072 sc1\n" \
"s_waitcnt vmcnt(6)\n" \
"v_min3_u32 v13, v32, v34, v36\n" \
"s_waitcnt vmcnt(5)\n" \
"v_min3_u32 v13, v13, v38, v40\n" \
"s_waitcnt vmcnt(4)\n" \
"v_min3_u32 v13, v13, v42, v44\n" \
"s_waitcnt vmcnt(3)\n" \
"v_min3_u32 v13, v13, v46, v48\n" \
"s_waitcnt vmcnt(2)\n" \
"v_min3_u32 v13, v13, v50, v52\n" \
"s_waitcnt vmcnt(1)\n" \
"v_min3_u32 v13, v13, v54, v56\n" \
"s_waitcnt vmcnt(0)\n" \
"v_min3_u32 v13, v13, v58, v60\n" \
"v_min3_u32 v13, v13, v62, v62\n" \
"v_cmp_eq_u32 vcc, 0, v13\n" \
"s_nop 4\n" \
"s_cbranch_vccz .Lv_o_ok_%=\n" \
"s_add_u32 s53, s53, 1\n" \
"s_cmp_lt_u32 s53, 32768\n" \
"s_cbranch_scc1 .Lv_o_again_%=\n" \
"s_mov_b32 s52, 1\n" \
"v_mov_b32 v13, 256\n" \
"v_mov_b32 v14, 1\n" \
"buffer_store_dword v14, v13, s[36:39], 0 offen sc1\n" \
"s_waitcnt vmcnt(0) lgkmcnt(0)\n" \
"s_branch .Lv_o_ok_%=\n" \
".Lv_end_%=:\n" \
"s_nop 7\n" \
"s_nop 7\n" \
"s_waitcnt vmcnt(0) lgkmcnt(0)\n" \
"s_waitcnt vmcnt(0) lgkmcnt(0)\n" \
"s_endpgm\n"
#define ASM_S64_WO \
"s_nop 7\n" \
"s_nop 7\n" \
"s_lshl_b32 s52, s48, 15\n" \
"s_lshl_b32 s53, s48, 13\n" \
"s_add_u32 s53, s53, 98304\n" \
"s_sub_u32 s54, s53, s52\n" \
"v_add_u32 v9, s54, v1\n" \
"s_mov_b32 s54, s85\n" \
"s_mul_i32 s86, s54, 262144\n" \
"s_add_u32 s54, s54, 1\n" \
"s_cmp_eq_u32 s54, 8\n" \
"s_cselect_b32 s54, 0, s54\n" \
"s_mul_i32 s87, s54, 262144\n" \
"s_add_u32 s54, s54, 1\n" \
"s_cmp_eq_u32 s54, 8\n" \
"s_cselect_b32 s54, 0, s54\n" \
"s_mul_i32 s88, s54, 262144\n" \
"s_add_u32 s54, s54, 1\n" \
"s_cmp_eq_u32 s54, 8\n" \
"s_cselect_b32 s54, 0, s54\n" \
"s_mul_i32 s89, s54, 262144\n" \
"s_add_u32 s54, s54, 1\n" \
"s_cmp_eq_u32 s54, 8\n" \
"s_cselect_b32 s54, 0, s54\n" \
"s_mul_i32 s90, s54, 262144\n" \
"s_add_u32 s54, s54, 1\n" \
"s_cmp_eq_u32 s54, 8\n" \
"s_cselect_b32 s54, 0, s54\n" \
"s_mul_i32 s91, s54, 262144\n" \
"s_add_u32 s54, s54, 1\n" \
"s_cmp_eq_u32 s54, 8\n" \
"s_cselect_b32 s54, 0, s54\n" \
"s_mul_i32 s92, s54, 262144\n" \
"s_add_u32 s54, s54, 1\n" \
"s_cmp_eq_u32 s54, 8\n" \
"s_cselect_b32 s54, 0, s54\n" \
"s_mul_i32 s93, s54, 262144\n" \
"s_add_u32 s54, s54, 1\n" \
"s_cmp_eq_u32 s54, 8\n" \
"s_cselect_b32 s54, 0, s54\n" \
"s_add_u32 m0, s52, 0\n" \
"s_add_u32 s54, s86, 0\n" \
"buffer_load_dwordx4 v0, s[40:43], s54 offen nt lds\n" \
"s_add_u32 m0, s52, 1024\n" \
"s_add_u32 s54, s86, 16384\n" \
"buffer_load_dwordx4 v0, s[40:43], s54 offen nt lds\n" \
"s_add_u32 m0, s52, 2048\n" \
"s_add_u32 s54, s86, 32768\n" \
"buffer_load_dwordx4 v0, s[40:43], s54 offen nt lds\n" \
"s_add_u32 m0, s52, 3072\n" \
"s_add_u32 s54, s86, 49152\n" \
"buffer_load_dwordx4 v0, s[40:43], s54 offen nt lds\n" \
"s_mov_b32 s54, s99\n" \
".Lswo_z0_%=:\n" \
"s_sleep 1\n" \
"s_sub_u32 s54, s54, 1\n" \
"s_cbranch_scc0 .Lswo_z0_%=\n" \
"s_add_u32 m0, s52, 4096\n" \
"s_add_u32 s54, s86, 65536\n" \
"buffer_load_dwordx4 v0, s[40:43], s54 offen nt lds\n" \
"s_add_u32 m0, s52, 5120\n" \
"s_add_u32 s54, s86, 81920\n" \
"buffer_load_dwordx4 v0, s[40:43], s54 offen nt lds\n" \
"s_add_u32 m0, s52, 6144\n" \
"s_add_u32 s54, s86, 98304\n" \
"buffer_load_dwordx4 v0, s[40:43], s54 offen nt lds\n" \
"s_add_u32 m0, s52, 7168\n" \
"s_add_u32 s54, s86, 114688\n" \
"buffer_load_dwordx4 v0, s[40:43], s54 offen nt lds\n" \
"s_mov_b32 s54, s99\n" \
".Lswo_z1_%=:\n" \
"s_sleep 1\n" \
"s_sub_u32 s54, s54, 1\n" \
"s_cbranch_scc0 .Lswo_z1_%=\n" \
"s_add_u32 m0, s52, 8192\n" \
"s_add_u32 s54, s86, 131072\n" \
"buffer_load_dwordx4 v0, s[40:43], s54 offen nt lds\n" \
"s_add_u32 m0, s52, 9216\n" \
"s_add_u32 s54, s86, 147456\n" \
"buffer_load_dwordx4 v0, s[40:43], s54 offen nt lds\n" \
"s_add_u32 m0, s52, 10240\n" \
"s_add_u32 s54, s86, 163840\n" \
"buffer_load_dwordx4 v0, s[40:43], s54 offen nt lds\n" \
"s_add_u32 m0, s52, 11264\n" \
"s_add_u32 s54, s86, 180224\n" \
"buffer_load_dwordx4 v0, s[40:43], s54 offen nt lds\n" \
"s_mov_b32 s54, s99\n" \
".Lswo_z2_%=:\n" \
"s_sleep 1\n" \
"s_sub_u32 s54, s54, 1\n" \
"s_cbranch_scc0 .Lswo_z2_%=\n" \
"s_add_u32 m0, s52, 12288\n" \
"s_add_u32 s54, s86, 196608\n" \
"buffer_load_dwordx4 v0, s[40:43], s54 offen nt lds\n" \
"s_add_u32 m0, s52, 13312\n" \
"s_add_u32 s54, s86, 212992\n" \
"buffer_load_dwordx4 v0, s[40:43], s54 offen nt lds\n" \
"s_add_u32 m0, s52, 14336\n" \
"s_add_u32 s54, s86, 229376\n" \
"buffer_load_dwordx4 v0, s[40:43], s54 offen nt lds\n" \
"s_add_u32 m0, s52, 15360\n" \
"s_add_u32 s54, s86, 245760\n" \
"buffer_load_dwordx4 v0, s[40:43], s54 offen nt lds\n" \
"s_mov_b32 s54, s99\n" \
".Lswo_z3_%=:\n" \
"s_sleep 1\n" \
"s_sub_u32 s54, s54, 1\n" \
"s_cbranch_scc0 .Lswo_z3_%=\n" \
"s_add_u32 m0, s52, 16384\n" \
"s_add_u32 s54, s87, 0\n" \
"buffer_load_dwordx4 v0, s[40:43], s54 offen nt lds\n" \
"s_add_u32 m0, s52, 17408\n" \
"s_add_u32 s54, s87, 16384\n" \
"buffer_load_dwordx4 v0, s[40:43], s54 offen nt lds\n" \
"s_add_u32 m0, s52, 18432\n" \
"s_add_u32 s54, s87, 32768\n" \
"buffer_load_dwordx4 v0, s[40:43], s54 offen nt lds\n" \
"s_add_u32 m0, s52, 19456\n" \
"s_add_u32 s54, s87, 49152\n" \
"buffer_load_dwordx4 v0, s[40:43], s54 offen nt lds\n" \
"s_mov_b32 s54, s99\n" \
".Lswo_z4_%=:\n" \
"s_sleep 1\n" \
"s_sub_u32 s54, s54, 1\n" \
"s_cbranch_scc0 .Lswo_z4_%=\n" \
"s_add_u32 m0, s52, 20480\n" \
"s_add_u32 s54, s87, 65536\n" \
"buffer_load_dwordx4 v0, s[40:43], s54 offen nt lds\n" \
"s_add_u32 m0, s52, 21504\n" \
"s_add_u32 s54, s87, 81920\n" \
"buffer_load_dwordx4 v0, s[40:43], s54 offen nt lds\n" \
"s_add_u32 m0, s52, 22528\n" \
"s_add_u32 s54, s87, 98304\n" \
"buffer_load_dwordx4 v0, s[40:43], s54 offen nt lds\n" \
"s_add_u32 m0, s52, 23552\n" \
"s_add_u32 s54, s87, 114688\n" \
"buffer_load_dwordx4 v0, s[40:43], s54 offen nt lds\n" \
"s_mov_b32 s54, s99\n" \
".Lswo_z5_%=:\n" \
"s_sleep 1\n" \
"s_sub_u32 s54, s54, 1\n" \
"s_cbranch_scc0 .Lswo_z5_%=\n" \
"s_add_u32 m0, s52, 24576\n" \
"s_add_u32 s54, s87, 131072\n" \
"buffer_load_dwordx4 v0, s[40:43], s54 offen nt lds\n" \
"s_add_u32 m0, s52, 25600\n" \
"s_add_u32 s54, s87, 147456\n" \
"buffer_load_dwordx4 v0, s[40:43], s54 offen nt lds\n" \
"s_add_u32 m0, s52, 26624\n" \
"s_add_u32 s54, s87, 163840\n" \
"buffer_load_dwordx4 v0, s[40:43], s54 offen nt lds\n" \
"s_add_u32 m0, s52, 27648\n" \
"s_add_u32 s54, s87, 180224\n" \
"buffer_load_dwordx4 v0, s[40:43], s54 offen nt lds\n" \
"s_mov_b32 s54, s99\n" \
".Lswo_z6_%=:\n" \
"s_sleep 1\n" \
"s_sub_u32 s54, s54, 1\n" \
"s_cbranch_scc0 .Lswo_z6_%=\n" \
"s_add_u32 m0, s52, 28672\n" \
"s_add_u32 s54, s87, 196608\n" \
"buffer_load_dwordx4 v0, s[40:43], s54 offen nt lds\n" \
"s_add_u32 m0, s52, 29696\n" \
"s_add_u32 s54, s87, 212992\n" \
"buffer_load_dwordx4 v0, s[40:43], s54 offen nt lds\n" \
"s_add_u32 m0, s52, 30720\n" \
"s_add_u32 s54, s87, 229376\n" \
"buffer_load_dwordx4 v0, s[40:43], s54 offen nt lds\n" \
"s_add_u32 m0, s52, 31744\n" \
"s_add_u32 s54, s87, 245760\n" \
"buffer_load_dwordx4 v0, s[40:43], s54 offen nt lds\n" \
"s_mov_b32 s54, s99\n" \
".Lswo_z7_%=:\n" \
"s_sleep 1\n" \
"s_sub_u32 s54, s54, 1\n" \
"s_cbranch_scc0 .Lswo_z7_%=\n" \
"s_add_u32 m0, s53, 32768\n" \
"s_add_u32 s54, s88, 0\n" \
"buffer_load_dwordx4 v0, s[40:43], s54 offen nt lds\n" \
"s_add_u32 m0, s53, 33792\n" \
"s_add_u32 s54, s88, 16384\n" \
"buffer_load_dwordx4 v0, s[40:43], s54 offen nt lds\n" \
"s_add_u32 m0, s53, 34816\n" \
"s_add_u32 s54, s88, 32768\n" \
"buffer_load_dwordx4 v0, s[40:43], s54 offen nt lds\n" \
"s_add_u32 m0, s53, 35840\n" \
"s_add_u32 s54, s88, 49152\n" \
"buffer_load_dwordx4 v0, s[40:43], s54 offen nt lds\n" \
"s_mov_b32 s54, s99\n" \
".Lswo_z8_%=:\n" \
"s_sleep 1\n" \
"s_sub_u32 s54, s54, 1\n" \
"s_cbranch_scc0 .Lswo_z8_%=\n" \
"s_add_u32 m0, s53, 36864\n" \
"s_add_u32 s54, s88, 65536\n" \
"buffer_load_dwordx4 v0, s[40:43], s54 offen nt lds\n" \
"s_add_u32 m0, s53, 37888\n" \
"s_add_u32 s54, s88, 81920\n" \
"buffer_load_dwordx4 v0, s[40:43], s54 offen nt lds\n" \
"s_add_u32 m0, s53, 38912\n" \
"s_add_u32 s54, s88, 98304\n" \
"buffer_load_dwordx4 v0, s[40:43], s54 offen nt lds\n" \
"s_add_u32 m0, s53, 39936\n" \
"s_add_u32 s54, s88, 114688\n" \
"buffer_load_dwordx4 v0, s[40:43], s54 offen nt lds\n" \
"s_mov_b32 s54, s99\n" \
".Lswo_z9_%=:\n" \
"s_sleep 1\n" \
"s_sub_u32 s54, s54, 1\n" \
"s_cbranch_scc0 .Lswo_z9_%=\n" \
"s_waitcnt vmcnt(36)\n" \
"ds_read_b64_tr_b16 a[0:1], v1 offset:0\n" \
"ds_read_b64_tr_b16 a[2:3], v1 offset:2048\n" \
"ds_read_b64_tr_b16 a[128:129], v1 offset:32\n" \
"ds_read_b64_tr_b16 a[130:131], v1 offset:2080\n" \
"ds_read_b64_tr_b16 v[128:129], v1 offset:64\n" \
"ds_read_b64_tr_b16 v[130:131], v1 offset:2112\n" \
"ds_read_b64_tr_b16 v[16:17], v1 offset:96\n" \
"ds_read_b64_tr_b16 v[18:19], v1 offset:2144\n" \
"s_waitcnt lgkmcnt(0)\n" \
"s_add_u32 m0, s52, 0\n" \
"s_add_u32 s54, s88, 131072\n" \
"buffer_load_dwordx4 v0, s[40:43], s54 offen nt lds\n" \
"s_add_u32 m0, s52, 1024\n" \
"s_add_u32 s54, s88, 147456\n" \
"buffer_load_dwordx4 v0, s[40:43], s54 offen nt lds\n" \
"s_add_u32 m0, s52, 2048\n" \
"s_add_u32 s54, s88, 163840\n" \
"buffer_load_dwordx4 v0, s[40:43], s54 offen nt lds\n" \
"s_add_u32 m0, s52, 3072\n" \
"s_add_u32 s54, s88, 180224\n" \
"buffer_load_dwordx4 v0, s[40:43], s54 offen nt lds\n" \
"s_mov_b32 s54, s99\n" \
".Lswo_z10_%=:\n" \
"s_sleep 1\n" \
"s_sub_u32 s54, s54, 1\n" \
"s_cbranch_scc0 .Lswo_z10_%=\n" \
"s_waitcnt vmcnt(36)\n" \
"ds_read_b64_tr_b16 a[4:5], v1 offset:4096\n" \
"ds_read_b64_tr_b16 a[6:7], v1 offset:6144\n" \
"ds_read_b64_tr_b16 a[132:133], v1 offset:4128\n" \
"ds_read_b64_tr_b16 a[134:135], v1 offset:6176\n" \
"ds_read_b64_tr_b16 v[132:133], v1 offset:4160\n" \
"ds_read_b64_tr_b16 v[134:135], v1 offset:6208\n" \
"ds_read_b64_tr_b16 v[20:21], v1 offset:4192\n" \
"ds_read_b64_tr_b16 v[22:23], v1 offset:6240\n" \
"s_waitcnt lgkmcnt(0)\n" \
"s_add_u32 m0, s52, 4096\n" \
"s_add_u32 s54, s88, 196608\n" \
"buffer_load_dwordx4 v0, s[40:43], s54 offen nt lds\n" \
"s_add_u32 m0, s52, 5120\n" \
"s_add_u32 s54, s88, 212992\n" \
"buffer_load_dwordx4 v0, s[40:43], s54 offen nt lds\n" \
"s_add_u32 m0, s52, 6144\n" \
"s_add_u32 s54, s88, 229376\n" \
"buffer_load_dwordx4 v0, s[40:43], s54 offen nt lds\n" \
"s_add_u32 m0, s52, 7168\n" \
"s_add_u32 s54, s88, 245760\n" \
"buffer_load_dwordx4 v0, s[40:43], s54 offen nt lds\n" \
"s_mov_b32 s54, s99\n" \
".Lswo_z11_%=:\n" \
"s_sleep 1\n" \
"s_sub_u32 s54, s54, 1\n" \
"s_cbranch_scc0 .Lswo_z11_%=\n" \
"s_waitcnt vmcnt(36)\n" \
"ds_read_b64_tr_b16 a[8:9], v1 offset:8192\n" \
"ds_read_b64_tr_b16 a[10:11], v1 offset:10240\n" \
"ds_read_b64_tr_b16 a[136:137], v1 offset:8224\n" \
"ds_read_b64_tr_b16 a[138:139], v1 offset:10272\n" \
"ds_read_b64_tr_b16 v[136:137], v1 offset:8256\n" \
"ds_read_b64_tr_b16 v[138:139], v1 offset:10304\n" \
"ds_read_b64_tr_b16 v[24:25], v1 offset:8288\n" \
"ds_read_b64_tr_b16 v[26:27], v1 offset:10336\n" \
"s_waitcnt lgkmcnt(0)\n" \
"s_add_u32 m0, s52, 8192\n" \
"s_add_u32 s54, s89, 0\n" \
"buffer_load_dwordx4 v0, s[40:43], s54 offen nt lds\n" \
"s_add_u32 m0, s52, 9216\n" \
"s_add_u32 s54, s89, 16384\n" \
"buffer_load_dwordx4 v0, s[40:43], s54 offen nt lds\n" \
"s_add_u32 m0, s52, 10240\n" \
"s_add_u32 s54, s89, 32768\n" \
"buffer_load_dwordx4 v0, s[40:43], s54 offen nt lds\n" \
"s_add_u32 m0, s52, 11264\n" \
"s_add_u32 s54, s89, 49152\n" \
"buffer_load_dwordx4 v0, s[40:43], s54 offen nt lds\n" \
"s_mov_b32 s54, s99\n" \
".Lswo_z12_%=:\n" \
"s_sleep 1\n" \
"s_sub_u32 s54, s54, 1\n" \
"s_cbranch_scc0 .Lswo_z12_%=\n" \
"s_waitcnt vmcnt(36)\n" \
"ds_read_b64_tr_b16 a[12:13], v1 offset:12288\n" \
"ds_read_b64_tr_b16 a[14:15], v1 offset:14336\n" \
"ds_read_b64_tr_b16 a[140:141], v1 offset:12320\n" \
"ds_read_b64_tr_b16 a[142:143], v1 offset:14368\n" \
"ds_read_b64_tr_b16 v[140:141], v1 offset:12352\n" \
"ds_read_b64_tr_b16 v[142:143], v1 offset:14400\n" \
"ds_read_b64_tr_b16 v[28:29], v1 offset:12384\n" \
"ds_read_b64_tr_b16 v[30:31], v1 offset:14432\n" \
"s_waitcnt lgkmcnt(0)\n" \
"s_add_u32 m0, s52, 12288\n" \
"s_add_u32 s54, s89, 65536\n" \
"buffer_load_dwordx4 v0, s[40:43], s54 offen nt lds\n" \
"s_add_u32 m0, s52, 13312\n" \
"s_add_u32 s54, s89, 81920\n" \
"buffer_load_dwordx4 v0, s[40:43], s54 offen nt lds\n" \
"s_add_u32 m0, s52, 14336\n" \
"s_add_u32 s54, s89, 98304\n" \
"buffer_load_dwordx4 v0, s[40:43], s54 offen nt lds\n" \
"s_add_u32 m0, s52, 15360\n" \
"s_add_u32 s54, s89, 114688\n" \
"buffer_load_dwordx4 v0, s[40:43], s54 offen nt lds\n" \
"s_mov_b32 s54, s99\n" \
".Lswo_z13_%=:\n" \
"s_sleep 1\n" \
"s_sub_u32 s54, s54, 1\n" \
"s_cbranch_scc0 .Lswo_z13_%=\n" \
"s_waitcnt vmcnt(36)\n" \
"ds_read_b64_tr_b16 a[16:17], v1 offset:16384\n" \
"ds_read_b64_tr_b16 a[18:19], v1 offset:18432\n" \
"ds_read_b64_tr_b16 a[144:145], v1 offset:16416\n" \
"ds_read_b64_tr_b16 a[146:147], v1 offset:18464\n" \
"ds_read_b64_tr_b16 v[144:145], v1 offset:16448\n" \
"ds_read_b64_tr_b16 v[146:147], v1 offset:18496\n" \
"ds_read_b64_tr_b16 v[32:33], v1 offset:16480\n" \
"ds_read_b64_tr_b16 v[34:35], v1 offset:18528\n" \
"s_waitcnt lgkmcnt(0)\n" \
"s_add_u32 m0, s52, 16384\n" \
"s_add_u32 s54, s89, 131072\n" \
"buffer_load_dwordx4 v0, s[40:43], s54 offen nt lds\n" \
"s_add_u32 m0, s52, 17408\n" \
"s_add_u32 s54, s89, 147456\n" \
"buffer_load_dwordx4 v0, s[40:43], s54 offen nt lds\n" \
"s_add_u32 m0, s52, 18432\n" \
"s_add_u32 s54, s89, 163840\n" \
"buffer_load_dwordx4 v0, s[40:43], s54 offen nt lds\n" \
"s_add_u32 m0, s52, 19456\n" \
"s_add_u32 s54, s89, 180224\n" \
"buffer_load_dwordx4 v0, s[40:43], s54 offen nt lds\n" \
"s_mov_b32 s54, s99\n" \
".Lswo_z14_%=:\n" \
"s_sleep 1\n" \
"s_sub_u32 s54, s54, 1\n" \
"s_cbranch_scc0 .Lswo_z14_%=\n" \
"s_waitcnt vmcnt(36)\n" \
"ds_read_b64_tr_b16 a[20:21], v1 offset:20480\n" \
"ds_read_b64_tr_b16 a[22:23], v1 offset:22528\n" \
"ds_read_b64_tr_b16 a[148:149], v1 offset:20512\n" \
"ds_read_b64_tr_b16 a[150:151], v1 offset:22560\n" \
"ds_read_b64_tr_b16 v[148:149], v1 offset:20544\n" \
"ds_read_b64_tr_b16 v[150:151], v1 offset:22592\n" \
"ds_read_b64_tr_b16 v[36:37], v1 offset:20576\n" \
"ds_read_b64_tr_b16 v[38:39], v1 offset:22624\n" \
"s_waitcnt lgkmcnt(0)\n" \
"s_add_u32 m0, s52, 20480\n" \
"s_add_u32 s54, s89, 196608\n" \
"buffer_load_dwordx4 v0, s[40:43], s54 offen nt lds\n" \
"s_add_u32 m0, s52, 21504\n" \
"s_add_u32 s54, s89, 212992\n" \
"buffer_load_dwordx4 v0, s[40:43], s54 offen nt lds\n" \
"s_add_u32 m0, s52, 22528\n" \
"s_add_u32 s54, s89, 229376\n" \
"buffer_load_dwordx4 v0, s[40:43], s54 offen nt lds\n" \
"s_add_u32 m0, s52, 23552\n" \
"s_add_u32 s54, s89, 245760\n" \
"buffer_load_dwordx4 v0, s[40:43], s54 offen nt lds\n" \
"s_mov_b32 s54, s99\n" \
".Lswo_z15_%=:\n" \
"s_sleep 1\n" \
"s_sub_u32 s54, s54, 1\n" \
"s_cbranch_scc0 .Lswo_z15_%=\n" \
"s_waitcnt vmcnt(36)\n" \
"ds_read_b64_tr_b16 a[24:25], v1 offset:24576\n" \
"ds_read_b64_tr_b16 a[26:27], v1 offset:26624\n" \
"ds_read_b64_tr_b16 a[152:153], v1 offset:24608\n" \
"ds_read_b64_tr_b16 a[154:155], v1 offset:26656\n" \
"ds_read_b64_tr_b16 v[152:153], v1 offset:24640\n" \
"ds_read_b64_tr_b16 v[154:155], v1 offset:26688\n" \
"ds_read_b64_tr_b16 v[40:41], v1 offset:24672\n" \
"ds_read_b64_tr_b16 v[42:43], v1 offset:26720\n" \
"s_waitcnt lgkmcnt(0)\n" \
"s_add_u32 m0, s52, 24576\n" \
"s_add_u32 s54, s90, 0\n" \
"buffer_load_dwordx4 v0, s[40:43], s54 offen nt lds\n" \
"s_add_u32 m0, s52, 25600\n" \
"s_add_u32 s54, s90, 16384\n" \
"buffer_load_dwordx4 v0, s[40:43], s54 offen nt lds\n" \
"s_add_u32 m0, s52, 26624\n" \
"s_add_u32 s54, s90, 32768\n" \
"buffer_load_dwordx4 v0, s[40:43], s54 offen nt lds\n" \
"s_add_u32 m0, s52, 27648\n" \
"s_add_u32 s54, s90, 49152\n" \
"buffer_load_dwordx4 v0, s[40:43], s54 offen nt lds\n" \
"s_mov_b32 s54, s99\n" \
".Lswo_z16_%=:\n" \
"s_sleep 1\n" \
"s_sub_u32 s54, s54, 1\n" \
"s_cbranch_scc0 .Lswo_z16_%=\n" \
"s_waitcnt vmcnt(36)\n" \
"ds_read_b64_tr_b16 a[28:29], v1 offset:28672\n" \
"ds_read_b64_tr_b16 a[30:31], v1 offset:30720\n" \
"ds_read_b64_tr_b16 a[156:157], v1 offset:28704\n" \
"ds_read_b64_tr_b16 a[158:159], v1 offset:30752\n" \
"ds_read_b64_tr_b16 v[156:157], v1 offset:28736\n" \
"ds_read_b64_tr_b16 v[158:159], v1 offset:30784\n" \
"ds_read_b64_tr_b16 v[44:45], v1 offset:28768\n" \
"ds_read_b64_tr_b16 v[46:47], v1 offset:30816\n" \
"s_waitcnt lgkmcnt(0)\n" \
"s_add_u32 m0, s52, 28672\n" \
"s_add_u32 s54, s90, 65536\n" \
"buffer_load_dwordx4 v0, s[40:43], s54 offen nt lds\n" \
"s_add_u32 m0, s52, 29696\n" \
"s_add_u32 s54, s90, 81920\n" \
"buffer_load_dwordx4 v0, s[40:43], s54 offen nt lds\n" \
"s_add_u32 m0, s52, 30720\n" \
"s_add_u32 s54, s90, 98304\n" \
"buffer_load_dwordx4 v0, s[40:43], s54 offen nt lds\n" \
"s_add_u32 m0, s52, 31744\n" \
"s_add_u32 s54, s90, 114688\n" \
"buffer_load_dwordx4 v0, s[40:43], s54 offen nt lds\n" \
"s_mov_b32 s54, s99\n" \
".Lswo_z17_%=:\n" \
"s_sleep 1\n" \
"s_sub_u32 s54, s54, 1\n" \
"s_cbranch_scc0 .Lswo_z17_%=\n" \
"s_waitcnt vmcnt(36)\n" \
"ds_read_b64_tr_b16 a[32:33], v9 offset:32768\n" \
"ds_read_b64_tr_b16 a[34:35], v9 offset:34816\n" \
"ds_read_b64_tr_b16 a[160:161], v9 offset:32800\n" \
"ds_read_b64_tr_b16 a[162:163], v9 offset:34848\n" \
"ds_read_b64_tr_b16 v[160:161], v9 offset:32832\n" \
"ds_read_b64_tr_b16 v[162:163], v9 offset:34880\n" \
"ds_read_b64_tr_b16 v[48:49], v9 offset:32864\n" \
"ds_read_b64_tr_b16 v[50:51], v9 offset:34912\n" \
"s_waitcnt lgkmcnt(0)\n" \
"s_add_u32 m0, s53, 32768\n" \
"s_add_u32 s54, s90, 131072\n" \
"buffer_load_dwordx4 v0, s[40:43], s54 offen nt lds\n" \
"s_add_u32 m0, s53, 33792\n" \
"s_add_u32 s54, s90, 147456\n" \
"buffer_load_dwordx4 v0, s[40:43], s54 offen nt lds\n" \
"s_add_u32 m0, s53, 34816\n" \
"s_add_u32 s54, s90, 163840\n" \
"buffer_load_dwordx4 v0, s[40:43], s54 offen nt lds\n" \
"s_add_u32 m0, s53, 35840\n" \
"s_add_u32 s54, s90, 180224\n" \
"buffer_load_dwordx4 v0, s[40:43], s54 offen nt lds\n" \
"s_mov_b32 s54, s99\n" \
".Lswo_z18_%=:\n" \
"s_sleep 1\n" \
"s_sub_u32 s54, s54, 1\n" \
"s_cbranch_scc0 .Lswo_z18_%=\n" \
"s_waitcnt vmcnt(36)\n" \
"ds_read_b64_tr_b16 a[36:37], v9 offset:36864\n" \
"ds_read_b64_tr_b16 a[38:39], v9 offset:38912\n" \
"ds_read_b64_tr_b16 a[164:165], v9 offset:36896\n" \
"ds_read_b64_tr_b16 a[166:167], v9 offset:38944\n" \
"ds_read_b64_tr_b16 v[164:165], v9 offset:36928\n" \
"ds_read_b64_tr_b16 v[166:167], v9 offset:38976\n" \
"ds_read_b64_tr_b16 v[52:53], v9 offset:36960\n" \
"ds_read_b64_tr_b16 v[54:55], v9 offset:39008\n" \
"s_waitcnt lgkmcnt(0)\n" \
"s_add_u32 m0, s53, 36864\n" \
"s_add_u32 s54, s90, 196608\n" \
"buffer_load_dwordx4 v0, s[40:43], s54 offen nt lds\n" \
"s_add_u32 m0, s53, 37888\n" \
"s_add_u32 s54, s90, 212992\n" \
"buffer_load_dwordx4 v0, s[40:43], s54 offen nt lds\n" \
"s_add_u32 m0, s53, 38912\n" \
"s_add_u32 s54, s90, 229376\n" \
"buffer_load_dwordx4 v0, s[40:43], s54 offen nt lds\n" \
"s_add_u32 m0, s53, 39936\n" \
"s_add_u32 s54, s90, 245760\n" \
"buffer_load_dwordx4 v0, s[40:43], s54 offen nt lds\n" \
"s_mov_b32 s54, s99\n" \
".Lswo_z19_%=:\n" \
"s_sleep 1\n" \
"s_sub_u32 s54, s54, 1\n" \
"s_cbranch_scc0 .Lswo_z19_%=\n" \
"s_waitcnt vmcnt(36)\n" \
"ds_read_b64_tr_b16 a[40:41], v1 offset:0\n" \
"ds_read_b64_tr_b16 a[42:43], v1 offset:2048\n" \
"ds_read_b64_tr_b16 a[168:169], v1 offset:32\n" \
"ds_read_b64_tr_b16 a[170:171], v1 offset:2080\n" \
"ds_read_b64_tr_b16 v[168:169], v1 offset:64\n" \
"ds_read_b64_tr_b16 v[170:171], v1 offset:2112\n" \
"ds_read_b64_tr_b16 v[56:57], v1 offset:96\n" \
"ds_read_b64_tr_b16 v[58:59], v1 offset:2144\n" \
"s_waitcnt lgkmcnt(0)\n" \
"s_add_u32 m0, s52, 0\n" \
"s_add_u32 s54, s91, 0\n" \
"buffer_load_dwordx4 v0, s[40:43], s54 offen nt lds\n" \
"s_add_u32 m0, s52, 1024\n" \
"s_add_u32 s54, s91, 16384\n" \
"buffer_load_dwordx4 v0, s[40:43], s54 offen nt lds\n" \
"s_add_u32 m0, s52, 2048\n" \
"s_add_u32 s54, s91, 32768\n" \
"buffer_load_dwordx4 v0, s[40:43], s54 offen nt lds\n" \
"s_add_u32 m0, s52, 3072\n" \
"s_add_u32 s54, s91, 49152\n" \
"buffer_load_dwordx4 v0, s[40:43], s54 offen nt lds\n" \
"s_mov_b32 s54, s99\n" \
".Lswo_z20_%=:\n" \
"s_sleep 1\n" \
"s_sub_u32 s54, s54, 1\n" \
"s_cbranch_scc0 .Lswo_z20_%=\n" \
"s_waitcnt vmcnt(36)\n" \
"ds_read_b64_tr_b16 a[44:45], v1 offset:4096\n" \
"ds_read_b64_tr_b16 a[46:47], v1 offset:6144\n" \
"ds_read_b64_tr_b16 a[172:173], v1 offset:4128\n" \
"ds_read_b64_tr_b16 a[174:175], v1 offset:6176\n" \
"ds_read_b64_tr_b16 v[172:173], v1 offset:4160\n" \
"ds_read_b64_tr_b16 v[174:175], v1 offset:6208\n" \
"ds_read_b64_tr_b16 v[60:61], v1 offset:4192\n" \
"ds_read_b64_tr_b16 v[62:63], v1 offset:6240\n" \
"s_waitcnt lgkmcnt(0)\n" \
"s_add_u32 m0, s52, 4096\n" \
"s_add_u32 s54, s91, 65536\n" \
"buffer_load_dwordx4 v0, s[40:43], s54 offen nt lds\n" \
"s_add_u32 m0, s52, 5120\n" \
"s_add_u32 s54, s91, 81920\n" \
"buffer_load_dwordx4 v0, s[40:43], s54 offen nt lds\n" \
"s_add_u32 m0, s52, 6144\n" \
"s_add_u32 s54, s91, 98304\n" \
"buffer_load_dwordx4 v0, s[40:43], s54 offen nt lds\n" \
"s_add_u32 m0, s52, 7168\n" \
"s_add_u32 s54, s91, 114688\n" \
"buffer_load_dwordx4 v0, s[40:43], s54 offen nt lds\n" \
"s_mov_b32 s54, s99\n" \
".Lswo_z21_%=:\n" \
"s_sleep 1\n" \
"s_sub_u32 s54, s54, 1\n" \
"s_cbranch_scc0 .Lswo_z21_%=\n" \
"s_waitcnt vmcnt(36)\n" \
"ds_read_b64_tr_b16 a[48:49], v1 offset:8192\n" \
"ds_read_b64_tr_b16 a[50:51], v1 offset:10240\n" \
"ds_read_b64_tr_b16 a[176:177], v1 offset:8224\n" \
"ds_read_b64_tr_b16 a[178:179], v1 offset:10272\n" \
"ds_read_b64_tr_b16 v[176:177], v1 offset:8256\n" \
"ds_read_b64_tr_b16 v[178:179], v1 offset:10304\n" \
"ds_read_b64_tr_b16 v[64:65], v1 offset:8288\n" \
"ds_read_b64_tr_b16 v[66:67], v1 offset:10336\n" \
"s_waitcnt lgkmcnt(0)\n" \
"s_add_u32 m0, s52, 8192\n" \
"s_add_u32 s54, s91, 131072\n" \
"buffer_load_dwordx4 v0, s[40:43], s54 offen nt lds\n" \
"s_add_u32 m0, s52, 9216\n" \
"s_add_u32 s54, s91, 147456\n" \
"buffer_load_dwordx4 v0, s[40:43], s54 offen nt lds\n" \
"s_add_u32 m0, s52, 10240\n" \
"s_add_u32 s54, s91, 163840\n" \
"buffer_load_dwordx4 v0, s[40:43], s54 offen nt lds\n" \
"s_add_u32 m0, s52, 11264\n" \
"s_add_u32 s54, s91, 180224\n" \
"buffer_load_dwordx4 v0, s[40:43], s54 offen nt lds\n" \
"s_mov_b32 s54, s99\n" \
".Lswo_z22_%=:\n" \
"s_sleep 1\n" \
"s_sub_u32 s54, s54, 1\n" \
"s_cbranch_scc0 .Lswo_z22_%=\n" \
"s_waitcnt vmcnt(36)\n" \
"ds_read_b64_tr_b16 a[52:53], v1 offset:12288\n" \
"ds_read_b64_tr_b16 a[54:55], v1 offset:14336\n" \
"ds_read_b64_tr_b16 a[180:181], v1 offset:12320\n" \
"ds_read_b64_tr_b16 a[182:183], v1 offset:14368\n" \
"ds_read_b64_tr_b16 v[180:181], v1 offset:12352\n" \
"ds_read_b64_tr_b16 v[182:183], v1 offset:14400\n" \
"ds_read_b64_tr_b16 v[68:69], v1 offset:12384\n" \
"ds_read_b64_tr_b16 v[70:71], v1 offset:14432\n" \
"s_waitcnt lgkmcnt(0)\n" \
"s_add_u32 m0, s52, 12288\n" \
"s_add_u32 s54, s91, 196608\n" \
"buffer_load_dwordx4 v0, s[40:43], s54 offen nt lds\n" \
"s_add_u32 m0, s52, 13312\n" \
"s_add_u32 s54, s91, 212992\n" \
"buffer_load_dwordx4 v0, s[40:43], s54 offen nt lds\n" \
"s_add_u32 m0, s52, 14336\n" \
"s_add_u32 s54, s91, 229376\n" \
"buffer_load_dwordx4 v0, s[40:43], s54 offen nt lds\n" \
"s_add_u32 m0, s52, 15360\n" \
"s_add_u32 s54, s91, 245760\n" \
"buffer_load_dwordx4 v0, s[40:43], s54 offen nt lds\n" \
"s_mov_b32 s54, s99\n" \
".Lswo_z23_%=:\n" \
"s_sleep 1\n" \
"s_sub_u32 s54, s54, 1\n" \
"s_cbranch_scc0 .Lswo_z23_%=\n" \
"s_waitcnt vmcnt(36)\n" \
"ds_read_b64_tr_b16 a[56:57], v1 offset:16384\n" \
"ds_read_b64_tr_b16 a[58:59], v1 offset:18432\n" \
"ds_read_b64_tr_b16 a[184:185], v1 offset:16416\n" \
"ds_read_b64_tr_b16 a[186:187], v1 offset:18464\n" \
"ds_read_b64_tr_b16 v[184:185], v1 offset:16448\n" \
"ds_read_b64_tr_b16 v[186:187], v1 offset:18496\n" \
"ds_read_b64_tr_b16 v[72:73], v1 offset:16480\n" \
"ds_read_b64_tr_b16 v[74:75], v1 offset:18528\n" \
"s_waitcnt lgkmcnt(0)\n" \
"s_add_u32 m0, s52, 16384\n" \
"s_add_u32 s54, s92, 0\n" \
"buffer_load_dwordx4 v0, s[40:43], s54 offen nt lds\n" \
"s_add_u32 m0, s52, 17408\n" \
"s_add_u32 s54, s92, 16384\n" \
"buffer_load_dwordx4 v0, s[40:43], s54 offen nt lds\n" \
"s_add_u32 m0, s52, 18432\n" \
"s_add_u32 s54, s92, 32768\n" \
"buffer_load_dwordx4 v0, s[40:43], s54 offen nt lds\n" \
"s_add_u32 m0, s52, 19456\n" \
"s_add_u32 s54, s92, 49152\n" \
"buffer_load_dwordx4 v0, s[40:43], s54 offen nt lds\n" \
"s_mov_b32 s54, s99\n" \
".Lswo_z24_%=:\n" \
"s_sleep 1\n" \
"s_sub_u32 s54, s54, 1\n" \
"s_cbranch_scc0 .Lswo_z24_%=\n" \
"s_waitcnt vmcnt(36)\n" \
"ds_read_b64_tr_b16 a[60:61], v1 offset:20480\n" \
"ds_read_b64_tr_b16 a[62:63], v1 offset:22528\n" \
"ds_read_b64_tr_b16 a[188:189], v1 offset:20512\n" \
"ds_read_b64_tr_b16 a[190:191], v1 offset:22560\n" \
"ds_read_b64_tr_b16 v[188:189], v1 offset:20544\n" \
"ds_read_b64_tr_b16 v[190:191], v1 offset:22592\n" \
"ds_read_b64_tr_b16 v[76:77], v1 offset:20576\n" \
"ds_read_b64_tr_b16 v[78:79], v1 offset:22624\n" \
"s_waitcnt lgkmcnt(0)\n" \
"s_add_u32 m0, s52, 20480\n" \
"s_add_u32 s54, s92, 65536\n" \
"buffer_load_dwordx4 v0, s[40:43], s54 offen nt lds\n" \
"s_add_u32 m0, s52, 21504\n" \
"s_add_u32 s54, s92, 81920\n" \
"buffer_load_dwordx4 v0, s[40:43], s54 offen nt lds\n" \
"s_add_u32 m0, s52, 22528\n" \
"s_add_u32 s54, s92, 98304\n" \
"buffer_load_dwordx4 v0, s[40:43], s54 offen nt lds\n" \
"s_add_u32 m0, s52, 23552\n" \
"s_add_u32 s54, s92, 114688\n" \
"buffer_load_dwordx4 v0, s[40:43], s54 offen nt lds\n" \
"s_mov_b32 s54, s99\n" \
".Lswo_z25_%=:\n" \
"s_sleep 1\n" \
"s_sub_u32 s54, s54, 1\n" \
"s_cbranch_scc0 .Lswo_z25_%=\n" \
"s_waitcnt vmcnt(36)\n" \
"ds_read_b64_tr_b16 a[64:65], v1 offset:24576\n" \
"ds_read_b64_tr_b16 a[66:67], v1 offset:26624\n" \
"ds_read_b64_tr_b16 a[192:193], v1 offset:24608\n" \
"ds_read_b64_tr_b16 a[194:195], v1 offset:26656\n" \
"ds_read_b64_tr_b16 v[192:193], v1 offset:24640\n" \
"ds_read_b64_tr_b16 v[194:195], v1 offset:26688\n" \
"ds_read_b64_tr_b16 v[80:81], v1 offset:24672\n" \
"ds_read_b64_tr_b16 v[82:83], v1 offset:26720\n" \
"s_waitcnt lgkmcnt(0)\n" \
"s_add_u32 m0, s52, 24576\n" \
"s_add_u32 s54, s92, 131072\n" \
"buffer_load_dwordx4 v0, s[40:43], s54 offen nt lds\n" \
"s_add_u32 m0, s52, 25600\n" \
"s_add_u32 s54, s92, 147456\n" \
"buffer_load_dwordx4 v0, s[40:43], s54 offen nt lds\n" \
"s_add_u32 m0, s52, 26624\n" \
"s_add_u32 s54, s92, 163840\n" \
"buffer_load_dwordx4 v0, s[40:43], s54 offen nt lds\n" \
"s_add_u32 m0, s52, 27648\n" \
"s_add_u32 s54, s92, 180224\n" \
"buffer_load_dwordx4 v0, s[40:43], s54 offen nt lds\n" \
"s_mov_b32 s54, s99\n" \
".Lswo_z26_%=:\n" \
"s_sleep 1\n" \
"s_sub_u32 s54, s54, 1\n" \
"s_cbranch_scc0 .Lswo_z26_%=\n" \
"s_waitcnt vmcnt(36)\n" \
"ds_read_b64_tr_b16 a[68:69], v1 offset:28672\n" \
"ds_read_b64_tr_b16 a[70:71], v1 offset:30720\n" \
"ds_read_b64_tr_b16 a[196:197], v1 offset:28704\n" \
"ds_read_b64_tr_b16 a[198:199], v1 offset:30752\n" \
"ds_read_b64_tr_b16 v[196:197], v1 offset:28736\n" \
"ds_read_b64_tr_b16 v[198:199], v1 offset:30784\n" \
"ds_read_b64_tr_b16 v[84:85], v1 offset:28768\n" \
"ds_read_b64_tr_b16 v[86:87], v1 offset:30816\n" \
"s_waitcnt lgkmcnt(0)\n" \
"s_add_u32 m0, s52, 28672\n" \
"s_add_u32 s54, s92, 196608\n" \
"buffer_load_dwordx4 v0, s[40:43], s54 offen nt lds\n" \
"s_add_u32 m0, s52, 29696\n" \
"s_add_u32 s54, s92, 212992\n" \
"buffer_load_dwordx4 v0, s[40:43], s54 offen nt lds\n" \
"s_add_u32 m0, s52, 30720\n" \
"s_add_u32 s54, s92, 229376\n" \
"buffer_load_dwordx4 v0, s[40:43], s54 offen nt lds\n" \
"s_add_u32 m0, s52, 31744\n" \
"s_add_u32 s54, s92, 245760\n" \
"buffer_load_dwordx4 v0, s[40:43], s54 offen nt lds\n" \
"s_mov_b32 s54, s99\n" \
".Lswo_z27_%=:\n" \
"s_sleep 1\n" \
"s_sub_u32 s54, s54, 1\n" \
"s_cbranch_scc0 .Lswo_z27_%=\n" \
"s_waitcnt vmcnt(36)\n" \
"ds_read_b64_tr_b16 a[72:73], v9 offset:32768\n" \
"ds_read_b64_tr_b16 a[74:75], v9 offset:34816\n" \
"ds_read_b64_tr_b16 a[200:201], v9 offset:32800\n" \
"ds_read_b64_tr_b16 a[202:203], v9 offset:34848\n" \
"ds_read_b64_tr_b16 v[200:201], v9 offset:32832\n" \
"ds_read_b64_tr_b16 v[202:203], v9 offset:34880\n" \
"ds_read_b64_tr_b16 v[88:89], v9 offset:32864\n" \
"ds_read_b64_tr_b16 v[90:91], v9 offset:34912\n" \
"s_waitcnt lgkmcnt(0)\n" \
"s_add_u32 m0, s53, 32768\n" \
"s_add_u32 s54, s93, 0\n" \
"buffer_load_dwordx4 v0, s[40:43], s54 offen nt lds\n" \
"s_add_u32 m0, s53, 33792\n" \
"s_add_u32 s54, s93, 16384\n" \
"buffer_load_dwordx4 v0, s[40:43], s54 offen nt lds\n" \
"s_add_u32 m0, s53, 34816\n" \
"s_add_u32 s54, s93, 32768\n" \
"buffer_load_dwordx4 v0, s[40:43], s54 offen nt lds\n" \
"s_add_u32 m0, s53, 35840\n" \
"s_add_u32 s54, s93, 49152\n" \
"buffer_load_dwordx4 v0, s[40:43], s54 offen nt lds\n" \
"s_mov_b32 s54, s99\n" \
".Lswo_z28_%=:\n" \
"s_sleep 1\n" \
"s_sub_u32 s54, s54, 1\n" \
"s_cbranch_scc0 .Lswo_z28_%=\n" \
"s_waitcnt vmcnt(36)\n" \
"ds_read_b64_tr_b16 a[76:77], v9 offset:36864\n" \
"ds_read_b64_tr_b16 a[78:79], v9 offset:38912\n" \
"ds_read_b64_tr_b16 a[204:205], v9 offset:36896\n" \
"ds_read_b64_tr_b16 a[206:207], v9 offset:38944\n" \
"ds_read_b64_tr_b16 v[204:205], v9 offset:36928\n" \
"ds_read_b64_tr_b16 v[206:207], v9 offset:38976\n" \
"ds_read_b64_tr_b16 v[92:93], v9 offset:36960\n" \
"ds_read_b64_tr_b16 v[94:95], v9 offset:39008\n" \
"s_waitcnt lgkmcnt(0)\n" \
"s_add_u32 m0, s53, 36864\n" \
"s_add_u32 s54, s93, 65536\n" \
"buffer_load_dwordx4 v0, s[40:43], s54 offen nt lds\n" \
"s_add_u32 m0, s53, 37888\n" \
"s_add_u32 s54, s93, 81920\n" \
"buffer_load_dwordx4 v0, s[40:43], s54 offen nt lds\n" \
"s_add_u32 m0, s53, 38912\n" \
"s_add_u32 s54, s93, 98304\n" \
"buffer_load_dwordx4 v0, s[40:43], s54 offen nt lds\n" \
"s_add_u32 m0, s53, 39936\n" \
"s_add_u32 s54, s93, 114688\n" \
"buffer_load_dwordx4 v0, s[40:43], s54 offen nt lds\n" \
"s_mov_b32 s54, s99\n" \
".Lswo_z29_%=:\n" \
"s_sleep 1\n" \
"s_sub_u32 s54, s54, 1\n" \
"s_cbranch_scc0 .Lswo_z29_%=\n" \
"s_waitcnt vmcnt(36)\n" \
"ds_read_b64_tr_b16 a[80:81], v1 offset:0\n" \
"ds_read_b64_tr_b16 a[82:83], v1 offset:2048\n" \
"ds_read_b64_tr_b16 a[208:209], v1 offset:32\n" \
"ds_read_b64_tr_b16 a[210:211], v1 offset:2080\n" \
"ds_read_b64_tr_b16 v[208:209], v1 offset:64\n" \
"ds_read_b64_tr_b16 v[210:211], v1 offset:2112\n" \
"ds_read_b64_tr_b16 v[96:97], v1 offset:96\n" \
"ds_read_b64_tr_b16 v[98:99], v1 offset:2144\n" \
"s_waitcnt lgkmcnt(0)\n" \
"s_add_u32 m0, s52, 0\n" \
"s_add_u32 s54, s93, 131072\n" \
"buffer_load_dwordx4 v0, s[40:43], s54 offen nt lds\n" \
"s_add_u32 m0, s52, 1024\n" \
"s_add_u32 s54, s93, 147456\n" \
"buffer_load_dwordx4 v0, s[40:43], s54 offen nt lds\n" \
"s_add_u32 m0, s52, 2048\n" \
"s_add_u32 s54, s93, 163840\n" \
"buffer_load_dwordx4 v0, s[40:43], s54 offen nt lds\n" \
"s_add_u32 m0, s52, 3072\n" \
"s_add_u32 s54, s93, 180224\n" \
"buffer_load_dwordx4 v0, s[40:43], s54 offen nt lds\n" \
"s_mov_b32 s54, s99\n" \
".Lswo_z30_%=:\n" \
"s_sleep 1\n" \
"s_sub_u32 s54, s54, 1\n" \
"s_cbranch_scc0 .Lswo_z30_%=\n" \
"s_waitcnt vmcnt(36)\n" \
"ds_read_b64_tr_b16 a[84:85], v1 offset:4096\n" \
"ds_read_b64_tr_b16 a[86:87], v1 offset:6144\n" \
"ds_read_b64_tr_b16 a[212:213], v1 offset:4128\n" \
"ds_read_b64_tr_b16 a[214:215], v1 offset:6176\n" \
"ds_read_b64_tr_b16 v[212:213], v1 offset:4160\n" \
"ds_read_b64_tr_b16 v[214:215], v1 offset:6208\n" \
"ds_read_b64_tr_b16 v[100:101], v1 offset:4192\n" \
"ds_read_b64_tr_b16 v[102:103], v1 offset:6240\n" \
"s_waitcnt lgkmcnt(0)\n" \
"s_add_u32 m0, s52, 4096\n" \
"s_add_u32 s54, s93, 196608\n" \
"buffer_load_dwordx4 v0, s[40:43], s54 offen nt lds\n" \
"s_add_u32 m0, s52, 5120\n" \
"s_add_u32 s54, s93, 212992\n" \
"buffer_load_dwordx4 v0, s[40:43], s54 offen nt lds\n" \
"s_add_u32 m0, s52, 6144\n" \
"s_add_u32 s54, s93, 229376\n" \
"buffer_load_dwordx4 v0, s[40:43], s54 offen nt lds\n" \
"s_add_u32 m0, s52, 7168\n" \
"s_add_u32 s54, s93, 245760\n" \
"buffer_load_dwordx4 v0, s[40:43], s54 offen nt lds\n" \
"s_mov_b32 s54, s99\n" \
".Lswo_z31_%=:\n" \
"s_sleep 1\n" \
"s_sub_u32 s54, s54, 1\n" \
"s_cbranch_scc0 .Lswo_z31_%=\n" \
"s_waitcnt vmcnt(36)\n" \
"ds_read_b64_tr_b16 a[88:89], v1 offset:8192\n" \
"ds_read_b64_tr_b16 a[90:91], v1 offset:10240\n" \
"ds_read_b64_tr_b16 a[216:217], v1 offset:8224\n" \
"ds_read_b64_tr_b16 a[218:219], v1 offset:10272\n" \
"ds_read_b64_tr_b16 v[216:217], v1 offset:8256\n" \
"ds_read_b64_tr_b16 v[218:219], v1 offset:10304\n" \
"ds_read_b64_tr_b16 v[104:105], v1 offset:8288\n" \
"ds_read_b64_tr_b16 v[106:107], v1 offset:10336\n" \
"s_waitcnt lgkmcnt(0)\n" \
"ds_write_b128 v2, v[16:19] offset:8192\n" \
"ds_write_b128 v2, v[20:23] offset:9216\n" \
"ds_write_b128 v2, v[24:27] offset:10240\n" \
"ds_write_b128 v2, v[28:31] offset:11264\n" \
"s_waitcnt vmcnt(32)\n" \
"ds_read_b64_tr_b16 a[92:93], v1 offset:12288\n" \
"ds_read_b64_tr_b16 a[94:95], v1 offset:14336\n" \
"ds_read_b64_tr_b16 a[220:221], v1 offset:12320\n" \
"ds_read_b64_tr_b16 a[222:223], v1 offset:14368\n" \
"ds_read_b64_tr_b16 v[220:221], v1 offset:12352\n" \
"ds_read_b64_tr_b16 v[222:223], v1 offset:14400\n" \
"ds_read_b64_tr_b16 v[108:109], v1 offset:12384\n" \
"ds_read_b64_tr_b16 v[110:111], v1 offset:14432\n" \
"s_waitcnt lgkmcnt(0)\n" \
"ds_write_b128 v2, v[32:35] offset:12288\n" \
"ds_write_b128 v2, v[36:39] offset:13312\n" \
"ds_write_b128 v2, v[40:43] offset:14336\n" \
"ds_write_b128 v2, v[44:47] offset:15360\n" \
"s_waitcnt vmcnt(28)\n" \
"ds_read_b64_tr_b16 a[96:97], v1 offset:16384\n" \
"ds_read_b64_tr_b16 a[98:99], v1 offset:18432\n" \
"ds_read_b64_tr_b16 a[224:225], v1 offset:16416\n" \
"ds_read_b64_tr_b16 a[226:227], v1 offset:18464\n" \
"ds_read_b64_tr_b16 v[224:225], v1 offset:16448\n" \
"ds_read_b64_tr_b16 v[226:227], v1 offset:18496\n" \
"ds_read_b64_tr_b16 v[16:17], v1 offset:16480\n" \
"ds_read_b64_tr_b16 v[18:19], v1 offset:18528\n" \
"s_waitcnt lgkmcnt(0)\n" \
"ds_write_b128 v2, v[48:51] offset:16384\n" \
"ds_write_b128 v2, v[52:55] offset:17408\n" \
"ds_write_b128 v2, v[56:59] offset:18432\n" \
"ds_write_b128 v2, v[60:63] offset:19456\n" \
"s_waitcnt vmcnt(24)\n" \
"ds_read_b64_tr_b16 a[100:101], v1 offset:20480\n" \
"ds_read_b64_tr_b16 a[102:103], v1 offset:22528\n" \
"ds_read_b64_tr_b16 a[228:229], v1 offset:20512\n" \
"ds_read_b64_tr_b16 a[230:231], v1 offset:22560\n" \
"ds_read_b64_tr_b16 v[228:229], v1 offset:20544\n" \
"ds_read_b64_tr_b16 v[230:231], v1 offset:22592\n" \
"ds_read_b64_tr_b16 v[20:21], v1 offset:20576\n" \
"ds_read_b64_tr_b16 v[22:23], v1 offset:22624\n" \
"s_waitcnt lgkmcnt(0)\n" \
"ds_write_b128 v2, v[64:67] offset:20480\n" \
"ds_write_b128 v2, v[68:71] offset:21504\n" \
"ds_write_b128 v2, v[72:75] offset:22528\n" \
"ds_write_b128 v2, v[76:79] offset:23552\n" \
"s_waitcnt vmcnt(20)\n" \
"ds_read_b64_tr_b16 a[104:105], v1 offset:24576\n" \
"ds_read_b64_tr_b16 a[106:107], v1 offset:26624\n" \
"ds_read_b64_tr_b16 a[232:233], v1 offset:24608\n" \
"ds_read_b64_tr_b16 a[234:235], v1 offset:26656\n" \
"ds_read_b64_tr_b16 v[232:233], v1 offset:24640\n" \
"ds_read_b64_tr_b16 v[234:235], v1 offset:26688\n" \
"ds_read_b64_tr_b16 v[24:25], v1 offset:24672\n" \
"ds_read_b64_tr_b16 v[26:27], v1 offset:26720\n" \
"s_waitcnt lgkmcnt(0)\n" \
"ds_write_b128 v2, v[80:83] offset:24576\n" \
"ds_write_b128 v2, v[84:87] offset:25600\n" \
"ds_write_b128 v2, v[88:91] offset:26624\n" \
"ds_write_b128 v2, v[92:95] offset:27648\n" \
"s_waitcnt vmcnt(16)\n" \
"ds_read_b64_tr_b16 a[108:109], v1 offset:28672\n" \
"ds_read_b64_tr_b16 a[110:111], v1 offset:30720\n" \
"ds_read_b64_tr_b16 a[236:237], v1 offset:28704\n" \
"ds_read_b64_tr_b16 a[238:239], v1 offset:30752\n" \
"ds_read_b64_tr_b16 v[236:237], v1 offset:28736\n" \
"ds_read_b64_tr_b16 v[238:239], v1 offset:30784\n" \
"ds_read_b64_tr_b16 v[28:29], v1 offset:28768\n" \
"ds_read_b64_tr_b16 v[30:31], v1 offset:30816\n" \
"s_waitcnt lgkmcnt(0)\n" \
"ds_write_b128 v2, v[96:99] offset:28672\n" \
"ds_write_b128 v2, v[100:103] offset:29696\n" \
"ds_write_b128 v2, v[104:107] offset:30720\n" \
"ds_write_b128 v2, v[108:111] offset:31744\n" \
"s_waitcnt vmcnt(12)\n" \
"ds_read_b64_tr_b16 a[112:113], v9 offset:32768\n" \
"ds_read_b64_tr_b16 a[114:115], v9 offset:34816\n" \
"ds_read_b64_tr_b16 a[240:241], v9 offset:32800\n" \
"ds_read_b64_tr_b16 a[242:243], v9 offset:34848\n" \
"ds_read_b64_tr_b16 v[240:241], v9 offset:32832\n" \
"ds_read_b64_tr_b16 v[242:243], v9 offset:34880\n" \
"ds_read_b64_tr_b16 v[32:33], v9 offset:32864\n" \
"ds_read_b64_tr_b16 v[34:35], v9 offset:34912\n" \
"s_waitcnt lgkmcnt(0)\n" \
"s_waitcnt vmcnt(8)\n" \
"ds_read_b64_tr_b16 a[116:117], v9 offset:36864\n" \
"ds_read_b64_tr_b16 a[118:119], v9 offset:38912\n" \
"ds_read_b64_tr_b16 a[244:245], v9 offset:36896\n" \
"ds_read_b64_tr_b16 a[246:247], v9 offset:38944\n" \
"ds_read_b64_tr_b16 v[244:245], v9 offset:36928\n" \
"ds_read_b64_tr_b16 v[246:247], v9 offset:38976\n" \
"ds_read_b64_tr_b16 v[36:37], v9 offset:36960\n" \
"ds_read_b64_tr_b16 v[38:39], v9 offset:39008\n" \
"s_waitcnt lgkmcnt(0)\n" \
"s_waitcnt vmcnt(4)\n" \
"ds_read_b64_tr_b16 a[120:121], v1 offset:0\n" \
"ds_read_b64_tr_b16 a[122:123], v1 offset:2048\n" \
"ds_read_b64_tr_b16 a[248:249], v1 offset:32\n" \
"ds_read_b64_tr_b16 a[250:251], v1 offset:2080\n" \
"ds_read_b64_tr_b16 v[248:249], v1 offset:64\n" \
"ds_read_b64_tr_b16 v[250:251], v1 offset:2112\n" \
"ds_read_b64_tr_b16 v[40:41], v1 offset:96\n" \
"ds_read_b64_tr_b16 v[42:43], v1 offset:2144\n" \
"s_waitcnt lgkmcnt(0)\n" \
"ds_write_b128 v2, v[16:19] offset:0\n" \
"ds_write_b128 v2, v[20:23] offset:1024\n" \
"ds_write_b128 v2, v[24:27] offset:2048\n" \
"ds_write_b128 v2, v[28:31] offset:3072\n" \
"s_waitcnt vmcnt(0)\n" \
"ds_read_b64_tr_b16 a[124:125], v1 offset:4096\n" \
"ds_read_b64_tr_b16 a[126:127], v1 offset:6144\n" \
"ds_read_b64_tr_b16 a[252:253], v1 offset:4128\n" \
"ds_read_b64_tr_b16 a[254:255], v1 offset:6176\n" \
"ds_read_b64_tr_b16 v[252:253], v1 offset:4160\n" \
"ds_read_b64_tr_b16 v[254:255], v1 offset:6208\n" \
"ds_read_b64_tr_b16 v[44:45], v1 offset:4192\n" \
"ds_read_b64_tr_b16 v[46:47], v1 offset:6240\n" \
"s_waitcnt lgkmcnt(0)\n" \
"ds_write_b128 v2, v[32:35] offset:4096\n" \
"ds_write_b128 v2, v[36:39] offset:5120\n" \
"ds_write_b128 v2, v[40:43] offset:6144\n" \
"ds_write_b128 v2, v[44:47] offset:7168\n" \
"s_waitcnt vmcnt(0) lgkmcnt(0)\n" \
"s_barrier\n" \
"s_mov_b32 s54, s85\n" \
"s_mul_i32 s86, s54, 532480\n" \
"s_add_u32 s54, s54, 1\n" \
"s_cmp_eq_u32 s54, 8\n" \
"s_cselect_b32 s54, 0, s54\n" \
"s_mul_i32 s87, s54, 532480\n" \
"s_add_u32 s54, s54, 1\n" \
"s_cmp_eq_u32 s54, 8\n" \
"s_cselect_b32 s54, 0, s54\n" \
"s_mul_i32 s88, s54, 532480\n" \
"s_add_u32 s54, s54, 1\n" \
"s_cmp_eq_u32 s54, 8\n" \
"s_cselect_b32 s54, 0, s54\n" \
"s_mul_i32 s89, s54, 532480\n" \
"s_add_u32 s54, s54, 1\n" \
"s_cmp_eq_u32 s54, 8\n" \
"s_cselect_b32 s54, 0, s54\n" \
"s_mul_i32 s90, s54, 532480\n" \
"s_add_u32 s54, s54, 1\n" \
"s_cmp_eq_u32 s54, 8\n" \
"s_cselect_b32 s54, 0, s54\n" \
"s_mul_i32 s91, s54, 532480\n" \
"s_add_u32 s54, s54, 1\n" \
"s_cmp_eq_u32 s54, 8\n" \
"s_cselect_b32 s54, 0, s54\n" \
"s_mul_i32 s92, s54, 532480\n" \
"s_add_u32 s54, s54, 1\n" \
"s_cmp_eq_u32 s54, 8\n" \
"s_cselect_b32 s54, 0, s54\n" \
"s_mul_i32 s93, s54, 532480\n" \
"s_add_u32 s54, s54, 1\n" \
"s_cmp_eq_u32 s54, 8\n" \
"s_cselect_b32 s54, 0, s54\n" \
"s_mov_b32 s55, 0\n" \
"s_mov_b32 s60, 0\n" \
".Lswo_prime_%=:\n" \
"s_lshr_b32 s59, s44, 1\n" \
"s_add_u32 s59, s59, 1\n" \
"s_mul_i32 s56, s44, 4096\n" \
"buffer_load_dword v11, v4, s[36:39], 0 offen sc1\n" \
"s_waitcnt vmcnt(0)\n" \
"v_cmp_gt_u32_e64 s[62:63], s59, v11\n" \
"s_nop 4\n" \
"s_cmp_lg_u64 s[62:63], 0\n" \
"s_cbranch_scc1 .Lswo_w_%=\n" \
".Lswo_wk_%=:\n" \
"s_mov_b32 s78, 0x70000000\n" \
"s_mov_b32 s79, 0x70000000\n" \
"s_mov_b32 s81, 1\n" \
"s_mov_b32 s84, 0\n" \
"v_mov_b32 v10, 0\n" \
"s_waitcnt vmcnt(0) lgkmcnt(0)\n" \
"s_add_u32 s57, s56, s86\n" \
"buffer_load_dwordx4 v[16:19], v3, s[36:39], s57 offen offset:0 sc1\n" \
"buffer_load_dwordx4 v[20:23], v3, s[36:39], s57 offen offset:1024 sc1\n" \
"buffer_load_dwordx4 v[24:27], v3, s[36:39], s57 offen offset:2048 sc1\n" \
"buffer_load_dwordx4 v[28:31], v3, s[36:39], s57 offen offset:3072 sc1\n" \
"s_add_u32 s57, s56, s87\n" \
"buffer_load_dwordx4 v[32:35], v3, s[36:39], s57 offen offset:0 sc1\n" \
"buffer_load_dwordx4 v[36:39], v3, s[36:39], s57 offen offset:1024 sc1\n" \
"buffer_load_dwordx4 v[40:43], v3, s[36:39], s57 offen offset:2048 sc1\n" \
"buffer_load_dwordx4 v[44:47], v3, s[36:39], s57 offen offset:3072 sc1\n" \
"s_add_u32 s57, s56, s88\n" \
"buffer_load_dwordx4 v[48:51], v3, s[36:39], s57 offen offset:0 sc1\n" \
"buffer_load_dwordx4 v[52:55], v3, s[36:39], s57 offen offset:1024 sc1\n" \
"buffer_load_dwordx4 v[56:59], v3, s[36:39], s57 offen offset:2048 sc1\n" \
"buffer_load_dwordx4 v[60:63], v3, s[36:39], s57 offen offset:3072 sc1\n" \
"s_add_u32 s57, s56, s89\n" \
"buffer_load_dwordx4 v[64:67], v3, s[36:39], s57 offen offset:0 sc1\n" \
"buffer_load_dwordx4 v[68:71], v3, s[36:39], s57 offen offset:1024 sc1\n" \
"buffer_load_dwordx4 v[72:75], v3, s[36:39], s57 offen offset:2048 sc1\n" \
"buffer_load_dwordx4 v[76:79], v3, s[36:39], s57 offen offset:3072 sc1\n" \
"ds_read_b128 v[80:83], v2 offset:8192\n" \
"ds_read_b128 v[84:87], v2 offset:9216\n" \
"ds_read_b128 v[88:91], v2 offset:10240\n" \
"s_waitcnt lgkmcnt(0)\n" \
"v_mov_b32 v13, 0x7ffffff0\n" \
"buffer_store_dword v13, v13, s[36:39], 0 offen\n" \
"buffer_store_dword v13, v13, s[36:39], 0 offen\n" \
"buffer_store_dword v13, v13, s[36:39], 0 offen\n" \
"s_waitcnt vmcnt(18)\n" \
"s_nop 7\n" \
"s_nop 7\n" \
".p2align 6\n" \
".Lswo_loop_%=:\n" \
"v_mfma_f32_16x16x32_bf16 v[96:99], a[0:3], v[16:19], 0\n" \
"ds_write_b128 v7, v[104:107] offset:2048\n" \
"v_mfma_f32_16x16x32_bf16 v[100:103], a[128:131], v[16:19], 0\n" \
"ds_write_b128 v7, v[108:111] offset:3072\n" \
"ds_read_b128 v[92:95], v2 offset:11264\n" \
"v_mfma_f32_16x16x32_bf16 v[104:107], v[128:131], v[16:19], 0\n" \
"s_waitcnt vmcnt(17) lgkmcnt(7)\n" \
"v_mfma_f32_16x16x32_bf16 v[108:111], v[80:83], v[16:19], 0\n" \
"s_add_u32 s57, s56, s90\n" \
"buffer_load_dwordx4 v[16:19], v3, s[36:39], s57 offen offset:0 sc1\n" \
"v_mfma_f32_16x16x32_bf16 v[96:99], a[4:7], v[20:23], v[96:99]\n" \
"v_readfirstlane_b32 s96, v9\n" \
"v_mfma_f32_16x16x32_bf16 v[100:103], a[132:135], v[20:23], v[100:103]\n" \
"ds_read_b128 v[80:83], v2 offset:12288\n" \
"v_mfma_f32_16x16x32_bf16 v[104:107], v[132:135], v[20:23], v[104:107]\n" \
"s_waitcnt vmcnt(17) lgkmcnt(7)\n" \
"v_mfma_f32_16x16x32_bf16 v[108:111], v[84:87], v[20:23], v[108:111]\n" \
"buffer_load_dwordx4 v[20:23], v3, s[36:39], s57 offen offset:1024 sc1\n" \
"v_mfma_f32_16x16x32_bf16 v[96:99], a[8:11], v[24:27], v[96:99]\n" \
"s_waitcnt lgkmcnt(2)\n" \
"s_cmp_lg_u32 s81, 0\n" \
"s_cbranch_scc1 .Lswo_nb_%=\n" \
"s_barrier\n" \
".Lswo_nb_%=:\n" \
"v_mfma_f32_16x16x32_bf16 v[100:103], a[136:139], v[24:27], v[100:103]\n" \
"ds_read_b128 v[84:87], v2 offset:13312\n" \
"v_xor_b32_e64 v7, s84, v7\n" \
"v_mfma_f32_16x16x32_bf16 v[104:107], v[136:139], v[24:27], v[104:107]\n" \
"s_waitcnt vmcnt(17)\n" \
"v_mfma_f32_16x16x32_bf16 v[108:111], v[88:91], v[24:27], v[108:111]\n" \
"buffer_load_dwordx4 v[24:27], v3, s[36:39], s57 offen offset:2048 sc1\n" \
"v_mfma_f32_16x16x32_bf16 v[96:99], a[12:15], v[28:31], v[96:99]\n" \
"s_add_u32 s76, s78, 0\n" \
"s_add_u32 s80, s78, 4194304\n" \
"s_and_b32 s96, s96, 2\n" \
"v_mfma_f32_16x16x32_bf16 v[100:103], a[140:143], v[28:31], v[100:103]\n" \
"ds_read_b128 v[88:91], v2 offset:14336\n" \
"s_cselect_b32 s76, 0x7ffffff0, s76\n" \
"s_cselect_b32 s77, s78, 0x7ffffff0\n" \
"v_mfma_f32_16x16x32_bf16 v[104:107], v[140:143], v[28:31], v[104:107]\n" \
"s_waitcnt vmcnt(17) lgkmcnt(3)\n" \
"s_cselect_b32 s80, s80, 0x7ffffff0\n" \
"s_cselect_b32 s94, 0x7ffffff0, s79\n" \
"v_mfma_f32_16x16x32_bf16 v[108:111], v[92:95], v[28:31], v[108:111]\n" \
"buffer_load_dwordx4 v[28:31], v3, s[36:39], s57 offen offset:3072 sc1\n" \
"v_mfma_f32_16x16x32_bf16 v[96:99], a[16:19], v[32:35], v[96:99]\n" \
"s_cmp_lt_i32 s44, s50\n" \
"s_cselect_b32 s82, s45, s47\n" \
"v_mfma_f32_16x16x32_bf16 v[100:103], a[144:147], v[32:35], v[100:103]\n" \
"ds_read_b128 v[92:95], v2 offset:15360\n" \
"s_add_u32 s82, s82, s44\n" \
"v_mfma_f32_16x16x32_bf16 v[104:107], v[144:147], v[32:35], v[104:107]\n" \
"s_waitcnt vmcnt(17) lgkmcnt(3)\n" \
"s_lshr_b32 s59, s82, 1\n" \
"s_add_u32 s59, s59, 1\n" \
"v_mfma_f32_16x16x32_bf16 v[108:111], v[80:83], v[32:35], v[108:111]\n" \
"s_add_u32 s57, s56, s91\n" \
"buffer_load_dwordx4 v[32:35], v3, s[36:39], s57 offen offset:0 sc1\n" \
"v_mfma_f32_16x16x32_bf16 v[96:99], a[20:23], v[36:39], v[96:99]\n" \
"s_waitcnt vmcnt(7)\n" \
"v_readfirstlane_b32 s54, v10\n" \
"v_mfma_f32_16x16x32_bf16 v[100:103], a[148:151], v[36:39], v[100:103]\n" \
"ds_read_b128 v[80:83], v2 offset:16384\n" \
"v_mfma_f32_16x16x32_bf16 v[104:107], v[148:151], v[36:39], v[104:107]\n" \
"s_waitcnt lgkmcnt(3)\n" \
"s_and_b32 s54, s54, 2\n" \
"s_cbranch_scc1 .Lswo_p_%=\n" \
".Lswo_pk_%=:\n" \
"v_mfma_f32_16x16x32_bf16 v[108:111], v[84:87], v[36:39], v[108:111]\n" \
"buffer_load_dwordx4 v[36:39], v3, s[36:39], s57 offen offset:1024 sc1\n" \
"v_mfma_f32_16x16x32_bf16 v[96:99], a[24:27], v[40:43], v[96:99]\n" \
"buffer_load_dwordx2 v[14:15], v6, s[36:39], s80 offen sc1\n" \
"ds_read_b128 v[112:115], v8 offset:0\n" \
"v_mfma_f32_16x16x32_bf16 v[100:103], a[152:155], v[40:43], v[100:103]\n" \
"ds_read_b128 v[84:87], v2 offset:17408\n" \
"v_mfma_f32_16x16x32_bf16 v[104:107], v[152:155], v[40:43], v[104:107]\n" \
"s_waitcnt lgkmcnt(4)\n" \
"buffer_load_dwordx2 v[12:13], v5, s[64:67], s77 offen\n" \
"ds_read_b128 v[116:119], v8 offset:4096\n" \
"v_mfma_f32_16x16x32_bf16 v[108:111], v[88:91], v[40:43], v[108:111]\n" \
"buffer_load_dwordx4 v[40:43], v3, s[36:39], s57 offen offset:2048 sc1\n" \
"v_mfma_f32_16x16x32_bf16 v[96:99], a[28:31], v[44:47], v[96:99]\n" \
"ds_read_b128 v[120:123], v8 offset:8192\n" \
"v_mfma_f32_16x16x32_bf16 v[100:103], a[156:159], v[44:47], v[100:103]\n" \
"ds_read_b128 v[88:91], v2 offset:18432\n" \
"v_mfma_f32_16x16x32_bf16 v[104:107], v[156:159], v[44:47], v[104:107]\n" \
"s_waitcnt lgkmcnt(6)\n" \
"ds_read_b128 v[124:127], v8 offset:12288\n" \
"v_mfma_f32_16x16x32_bf16 v[108:111], v[92:95], v[44:47], v[108:111]\n" \
"buffer_load_dwordx4 v[44:47], v3, s[36:39], s57 offen offset:3072 sc1\n" \
"v_mfma_f32_16x16x32_bf16 v[96:99], a[32:35], v[48:51], v[96:99]\n" \
"v_mfma_f32_16x16x32_bf16 v[100:103], a[160:163], v[48:51], v[100:103]\n" \
"ds_read_b128 v[92:95], v2 offset:19456\n" \
"v_mfma_f32_16x16x32_bf16 v[104:107], v[160:163], v[48:51], v[104:107]\n" \
"s_waitcnt lgkmcnt(7)\n" \
"v_mfma_f32_16x16x32_bf16 v[108:111], v[80:83], v[48:51], v[108:111]\n" \
"s_add_u32 s57, s56, s92\n" \
"buffer_load_dwordx4 v[48:51], v3, s[36:39], s57 offen offset:0 sc1\n" \
"v_mfma_f32_16x16x32_bf16 v[96:99], a[36:39], v[52:55], v[96:99]\n" \
"s_waitcnt lgkmcnt(4)\n" \
"v_add_f32 v112, v112, v116\n" \
"v_add_f32_e64 v113, v113, v117\n" \
"v_mfma_f32_16x16x32_bf16 v[100:103], a[164:167], v[52:55], v[100:103]\n" \
"ds_read_b128 v[80:83], v2 offset:20480\n" \
"v_add_f32_e64 v114, v114, v118\n" \
"v_mfma_f32_16x16x32_bf16 v[104:107], v[164:167], v[52:55], v[104:107]\n" \
"v_add_f32_e64 v115, v115, v119\n" \
"v_mfma_f32_16x16x32_bf16 v[108:111], v[84:87], v[52:55], v[108:111]\n" \
"buffer_load_dwordx4 v[52:55], v3, s[36:39], s57 offen offset:1024 sc1\n" \
"s_waitcnt lgkmcnt(2)\n" \
"v_add_f32 v120, v120, v124\n" \
"v_mfma_f32_16x16x32_bf16 v[96:99], a[40:43], v[56:59], v[96:99]\n" \
"v_add_f32 v121, v121, v125\n" \
"v_add_f32 v122, v122, v126\n" \
"v_mfma_f32_16x16x32_bf16 v[100:103], a[168:171], v[56:59], v[100:103]\n" \
"ds_read_b128 v[84:87], v2 offset:21504\n" \
"v_add_f32_e64 v123, v123, v127\n" \
"v_mfma_f32_16x16x32_bf16 v[104:107], v[168:171], v[56:59], v[104:107]\n" \
"v_add_f32_e64 v112, v112, v120\n" \
"v_mfma_f32_16x16x32_bf16 v[108:111], v[88:91], v[56:59], v[108:111]\n" \
"buffer_load_dwordx4 v[56:59], v3, s[36:39], s57 offen offset:2048 sc1\n" \
"v_add_f32_e64 v113, v113, v121\n" \
"v_mfma_f32_16x16x32_bf16 v[96:99], a[44:47], v[60:63], v[96:99]\n" \
"v_add_f32 v114, v114, v122\n" \
"v_add_f32 v115, v115, v123\n" \
"v_mfma_f32_16x16x32_bf16 v[100:103], a[172:175], v[60:63], v[100:103]\n" \
"ds_read_b128 v[88:91], v2 offset:22528\n" \
"v_xor_b32_e64 v8, s84, v8\n" \
"v_mfma_f32_16x16x32_bf16 v[104:107], v[172:175], v[60:63], v[104:107]\n" \
"s_waitcnt lgkmcnt(3)\n" \
"v_cvt_pk_bf16_f32 v0, v112, v113\n" \
"v_mfma_f32_16x16x32_bf16 v[108:111], v[92:95], v[60:63], v[108:111]\n" \
"buffer_load_dwordx4 v[60:63], v3, s[36:39], s57 offen offset:3072 sc1\n" \
"v_cvt_pk_bf16_f32 v1, v114, v115\n" \
"v_mfma_f32_16x16x32_bf16 v[96:99], a[48:51], v[64:67], v[96:99]\n" \
"s_lshl_b32 s97, s44, 8\n" \
"s_add_u32 s97, s97, s49\n" \
"v_mfma_f32_16x16x32_bf16 v[100:103], a[176:179], v[64:67], v[100:103]\n" \
"ds_read_b128 v[92:95], v2 offset:23552\n" \
"buffer_store_dwordx2 v[0:1], v6, s[36:39], s76 offen sc1\n" \
"v_lshlrev_b32 v112, 16, v0\n" \
"v_mfma_f32_16x16x32_bf16 v[104:107], v[176:179], v[64:67], v[104:107]\n" \
"s_waitcnt lgkmcnt(3)\n" \
"v_and_b32 v113, 0xffff0000, v0\n" \
"v_mfma_f32_16x16x32_bf16 v[108:111], v[80:83], v[64:67], v[108:111]\n" \
"s_add_u32 s57, s56, s93\n" \
"buffer_load_dwordx4 v[64:67], v3, s[36:39], s57 offen offset:0 sc1\n" \
"v_lshlrev_b32_e64 v114, 16, v1\n" \
"v_mfma_f32_16x16x32_bf16 v[96:99], a[52:55], v[68:71], v[96:99]\n" \
"v_cmp_gt_u32_e64 s[62:63], s59, v11\n" \
"v_and_b32 v115, 0xffff0000, v1\n" \
"v_mfma_f32_16x16x32_bf16 v[100:103], a[180:183], v[68:71], v[100:103]\n" \
"ds_read_b128 v[80:83], v2 offset:24576\n" \
"v_mfma_f32_16x16x32_bf16 v[104:107], v[180:183], v[68:71], v[104:107]\n" \
"s_waitcnt vmcnt(17) lgkmcnt(3)\n" \
"v_mfma_f32_16x16x32_bf16 v[108:111], v[84:87], v[68:71], v[108:111]\n" \
"buffer_load_dwordx4 v[68:71], v3, s[36:39], s57 offen offset:1024 sc1\n" \
"v_mfma_f32_16x16x32_bf16 v[96:99], a[56:59], v[72:75], v[96:99]\n" \
"s_add_u32 s54, s55, 1\n" \
"s_cmp_lt_u32 s54, s46\n" \
"v_mfma_f32_16x16x32_bf16 v[100:103], a[184:187], v[72:75], v[100:103]\n" \
"ds_read_b128 v[84:87], v2 offset:25600\n" \
"s_cselect_b64 s[62:63], s[62:63], -1\n" \
"v_mfma_f32_16x16x32_bf16 v[104:107], v[184:187], v[72:75], v[104:107]\n" \
"s_waitcnt vmcnt(17) lgkmcnt(3)\n" \
"s_mul_i32 s83, s82, 4096\n" \
"v_mfma_f32_16x16x32_bf16 v[108:111], v[88:91], v[72:75], v[108:111]\n" \
"buffer_load_dwordx4 v[72:75], v3, s[36:39], s57 offen offset:2048 sc1\n" \
"v_mfma_f32_16x16x32_bf16 v[96:99], a[60:63], v[76:79], v[96:99]\n" \
"s_cmp_lg_u64 s[62:63], 0\n" \
"s_cselect_b32 s81, 1, 0\n" \
"v_mfma_f32_16x16x32_bf16 v[100:103], a[188:191], v[76:79], v[100:103]\n" \
"ds_read_b128 v[88:91], v2 offset:26624\n" \
"s_cselect_b32 s83, 0x70000000, s83\n" \
"v_mfma_f32_16x16x32_bf16 v[104:107], v[188:191], v[76:79], v[104:107]\n" \
"s_waitcnt vmcnt(17) lgkmcnt(3)\n" \
"v_mfma_f32_16x16x32_bf16 v[108:111], v[92:95], v[76:79], v[108:111]\n" \
"buffer_load_dwordx4 v[76:79], v3, s[36:39], s57 offen offset:3072 sc1\n" \
"v_mfma_f32_16x16x32_bf16 v[96:99], a[64:67], v[16:19], v[96:99]\n" \
"v_mfma_f32_16x16x32_bf16 v[100:103], a[192:195], v[16:19], v[100:103]\n" \
"ds_read_b128 v[92:95], v2 offset:27648\n" \
"v_mfma_f32_16x16x32_bf16 v[104:107], v[192:195], v[16:19], v[104:107]\n" \
"s_waitcnt vmcnt(17) lgkmcnt(3)\n" \
"s_mov_b32 s56, s83\n" \
"v_mfma_f32_16x16x32_bf16 v[108:111], v[80:83], v[16:19], v[108:111]\n" \
"s_add_u32 s57, s56, s86\n" \
"buffer_load_dwordx4 v[16:19], v3, s[36:39], s57 offen offset:0 sc1\n" \
"v_mfma_f32_16x16x32_bf16 v[96:99], a[68:71], v[20:23], v[96:99]\n" \
"buffer_load_dword v9, off, s[36:39], s97 sc1\n" \
"v_mfma_f32_16x16x32_bf16 v[100:103], a[196:199], v[20:23], v[100:103]\n" \
"ds_read_b128 v[80:83], v2 offset:28672\n" \
"v_mfma_f32_16x16x32_bf16 v[104:107], v[196:199], v[20:23], v[104:107]\n" \
"s_waitcnt vmcnt(18) lgkmcnt(3)\n" \
"v_mfma_f32_16x16x32_bf16 v[108:111], v[84:87], v[20:23], v[108:111]\n" \
"buffer_load_dwordx4 v[20:23], v3, s[36:39], s57 offen offset:1024 sc1\n" \
"v_mfma_f32_16x16x32_bf16 v[96:99], a[72:75], v[24:27], v[96:99]\n" \
"v_mfma_f32_16x16x32_bf16 v[100:103], a[200:203], v[24:27], v[100:103]\n" \
"ds_read_b128 v[84:87], v2 offset:29696\n" \
"v_mfma_f32_16x16x32_bf16 v[104:107], v[200:203], v[24:27], v[104:107]\n" \
"s_waitcnt vmcnt(18) lgkmcnt(3)\n" \
"v_mfma_f32_16x16x32_bf16 v[108:111], v[88:91], v[24:27], v[108:111]\n" \
"buffer_load_dwordx4 v[24:27], v3, s[36:39], s57 offen offset:2048 sc1\n" \
"v_mfma_f32_16x16x32_bf16 v[96:99], a[76:79], v[28:31], v[96:99]\n" \
"v_mfma_f32_16x16x32_bf16 v[100:103], a[204:207], v[28:31], v[100:103]\n" \
"ds_read_b128 v[88:91], v2 offset:30720\n" \
"v_mfma_f32_16x16x32_bf16 v[104:107], v[204:207], v[28:31], v[104:107]\n" \
"s_waitcnt vmcnt(18) lgkmcnt(3)\n" \
"v_mfma_f32_16x16x32_bf16 v[108:111], v[92:95], v[28:31], v[108:111]\n" \
"buffer_load_dwordx4 v[28:31], v3, s[36:39], s57 offen offset:3072 sc1\n" \
"v_mfma_f32_16x16x32_bf16 v[96:99], a[80:83], v[32:35], v[96:99]\n" \
"s_waitcnt vmcnt(17)\n" \
"v_lshlrev_b32 v120, 16, v14\n" \
"v_and_b32 v121, 0xffff0000, v14\n" \
"v_mfma_f32_16x16x32_bf16 v[100:103], a[208:211], v[32:35], v[100:103]\n" \
"ds_read_b128 v[92:95], v2 offset:31744\n" \
"s_waitcnt vmcnt(16)\n" \
"v_lshlrev_b32 v124, 16, v12\n" \
"v_mfma_f32_16x16x32_bf16 v[104:107], v[208:211], v[32:35], v[104:107]\n" \
"s_waitcnt lgkmcnt(3)\n" \
"v_and_b32 v125, 0xffff0000, v12\n" \
"v_mfma_f32_16x16x32_bf16 v[108:111], v[80:83], v[32:35], v[108:111]\n" \
"s_add_u32 s57, s56, s87\n" \
"buffer_load_dwordx4 v[32:35], v3, s[36:39], s57 offen offset:0 sc1\n" \
"v_lshlrev_b32_e64 v122, 16, v15\n" \
"v_mfma_f32_16x16x32_bf16 v[96:99], a[84:87], v[36:39], v[96:99]\n" \
"v_and_b32 v123, 0xffff0000, v15\n" \
"v_lshlrev_b32_e64 v126, 16, v13\n" \
"v_mfma_f32_16x16x32_bf16 v[100:103], a[212:215], v[36:39], v[100:103]\n" \
"ds_read_b128 v[80:83], v2 offset:0\n" \
"v_and_b32 v127, 0xffff0000, v13\n" \
"v_mfma_f32_16x16x32_bf16 v[104:107], v[212:215], v[36:39], v[104:107]\n" \
"s_waitcnt vmcnt(16) lgkmcnt(3)\n" \
"v_fmac_f32 v124, v112, v120\n" \
"v_mfma_f32_16x16x32_bf16 v[108:111], v[84:87], v[36:39], v[108:111]\n" \
"buffer_load_dwordx4 v[36:39], v3, s[36:39], s57 offen offset:1024 sc1\n" \
"v_fmac_f32_e64 v125, v113, v121\n" \
"v_mfma_f32_16x16x32_bf16 v[96:99], a[88:91], v[40:43], v[96:99]\n" \
"v_fmac_f32 v126, v114, v122\n" \
"v_fmac_f32 v127, v115, v123\n" \
"v_mfma_f32_16x16x32_bf16 v[100:103], a[216:219], v[40:43], v[100:103]\n" \
"ds_read_b128 v[84:87], v2 offset:1024\n" \
"v_cvt_pk_bf16_f32 v118, v124, v125\n" \
"v_mfma_f32_16x16x32_bf16 v[104:107], v[216:219], v[40:43], v[104:107]\n" \
"s_waitcnt vmcnt(16) lgkmcnt(3)\n" \
"v_cvt_pk_bf16_f32 v119, v126, v127\n" \
"v_mfma_f32_16x16x32_bf16 v[108:111], v[88:91], v[40:43], v[108:111]\n" \
"buffer_load_dwordx4 v[40:43], v3, s[36:39], s57 offen offset:2048 sc1\n" \
"v_mfma_f32_16x16x32_bf16 v[96:99], a[92:95], v[44:47], v[96:99]\n" \
"buffer_store_dwordx2 v[118:119], v5, s[68:71], s77 offen\n" \
"v_mov_b32 v10, 1\n" \
"v_mfma_f32_16x16x32_bf16 v[100:103], a[220:223], v[44:47], v[100:103]\n" \
"ds_read_b128 v[88:91], v2 offset:2048\n" \
"v_mfma_f32_16x16x32_bf16 v[104:107], v[220:223], v[44:47], v[104:107]\n" \
"s_waitcnt vmcnt(17) lgkmcnt(3)\n" \
"v_mfma_f32_16x16x32_bf16 v[108:111], v[92:95], v[44:47], v[108:111]\n" \
"buffer_load_dwordx4 v[44:47], v3, s[36:39], s57 offen offset:3072 sc1\n" \
"v_mfma_f32_16x16x32_bf16 v[96:99], a[96:99], v[48:51], v[96:99]\n" \
"v_mfma_f32_16x16x32_bf16 v[100:103], a[224:227], v[48:51], v[100:103]\n" \
"ds_read_b128 v[92:95], v2 offset:3072\n" \
"v_mfma_f32_16x16x32_bf16 v[104:107], v[224:227], v[48:51], v[104:107]\n" \
"s_waitcnt vmcnt(17) lgkmcnt(3)\n" \
"v_mfma_f32_16x16x32_bf16 v[108:111], v[80:83], v[48:51], v[108:111]\n" \
"s_add_u32 s57, s56, s88\n" \
"buffer_load_dwordx4 v[48:51], v3, s[36:39], s57 offen offset:0 sc1\n" \
"v_mfma_f32_16x16x32_bf16 v[96:99], a[100:103], v[52:55], v[96:99]\n" \
"v_mfma_f32_16x16x32_bf16 v[100:103], a[228:231], v[52:55], v[100:103]\n" \
"ds_read_b128 v[80:83], v2 offset:4096\n" \
"v_mfma_f32_16x16x32_bf16 v[104:107], v[228:231], v[52:55], v[104:107]\n" \
"s_waitcnt vmcnt(17) lgkmcnt(3)\n" \
"v_mfma_f32_16x16x32_bf16 v[108:111], v[84:87], v[52:55], v[108:111]\n" \
"buffer_load_dwordx4 v[52:55], v3, s[36:39], s57 offen offset:1024 sc1\n" \
"v_mfma_f32_16x16x32_bf16 v[96:99], a[104:107], v[56:59], v[96:99]\n" \
"v_mfma_f32_16x16x32_bf16 v[100:103], a[232:235], v[56:59], v[100:103]\n" \
"ds_read_b128 v[84:87], v2 offset:5120\n" \
"v_mfma_f32_16x16x32_bf16 v[104:107], v[232:235], v[56:59], v[104:107]\n" \
"s_waitcnt vmcnt(17) lgkmcnt(3)\n" \
"v_mfma_f32_16x16x32_bf16 v[108:111], v[88:91], v[56:59], v[108:111]\n" \
"buffer_load_dwordx4 v[56:59], v3, s[36:39], s57 offen offset:2048 sc1\n" \
"v_mfma_f32_16x16x32_bf16 v[96:99], a[108:111], v[60:63], v[96:99]\n" \
"v_mfma_f32_16x16x32_bf16 v[100:103], a[236:239], v[60:63], v[100:103]\n" \
"ds_read_b128 v[88:91], v2 offset:6144\n" \
"v_mfma_f32_16x16x32_bf16 v[104:107], v[236:239], v[60:63], v[104:107]\n" \
"s_waitcnt vmcnt(16) lgkmcnt(3)\n" \
"v_mfma_f32_16x16x32_bf16 v[108:111], v[92:95], v[60:63], v[108:111]\n" \
"buffer_load_dwordx4 v[60:63], v3, s[36:39], s57 offen offset:3072 sc1\n" \
"v_mfma_f32_16x16x32_bf16 v[96:99], a[112:115], v[64:67], v[96:99]\n" \
"v_mfma_f32_16x16x32_bf16 v[100:103], a[240:243], v[64:67], v[100:103]\n" \
"ds_read_b128 v[92:95], v2 offset:7168\n" \
"v_mfma_f32_16x16x32_bf16 v[104:107], v[240:243], v[64:67], v[104:107]\n" \
"s_waitcnt vmcnt(16) lgkmcnt(3)\n" \
"s_mov_b32 s84, 0x4000\n" \
"v_mfma_f32_16x16x32_bf16 v[108:111], v[80:83], v[64:67], v[108:111]\n" \
"s_add_u32 s57, s56, s89\n" \
"buffer_load_dwordx4 v[64:67], v3, s[36:39], s57 offen offset:0 sc1\n" \
"v_mfma_f32_16x16x32_bf16 v[96:99], a[116:119], v[68:71], v[96:99]\n" \
"buffer_load_dword v11, v4, s[36:39], 0 offen sc1\n" \
"v_mfma_f32_16x16x32_bf16 v[100:103], a[244:247], v[68:71], v[100:103]\n" \
"ds_read_b128 v[80:83], v2 offset:8192\n" \
"v_mfma_f32_16x16x32_bf16 v[104:107], v[244:247], v[68:71], v[104:107]\n" \
"s_waitcnt vmcnt(17) lgkmcnt(3)\n" \
"v_mfma_f32_16x16x32_bf16 v[108:111], v[84:87], v[68:71], v[108:111]\n" \
"buffer_load_dwordx4 v[68:71], v3, s[36:39], s57 offen offset:1024 sc1\n" \
"v_mfma_f32_16x16x32_bf16 v[96:99], a[120:123], v[72:75], v[96:99]\n" \
"v_mfma_f32_16x16x32_bf16 v[100:103], a[248:251], v[72:75], v[100:103]\n" \
"ds_read_b128 v[84:87], v2 offset:9216\n" \
"v_mfma_f32_16x16x32_bf16 v[104:107], v[248:251], v[72:75], v[104:107]\n" \
"s_waitcnt vmcnt(17) lgkmcnt(3)\n" \
"s_mov_b64 exec, 1\n" \
"buffer_atomic_or v10, off, s[36:39], s94 sc0\n" \
"s_mov_b64 exec, -1\n" \
"s_mov_b32 s95, s78\n" \
"v_mfma_f32_16x16x32_bf16 v[108:111], v[88:91], v[72:75], v[108:111]\n" \
"buffer_load_dwordx4 v[72:75], v3, s[36:39], s57 offen offset:2048 sc1\n" \
"v_mfma_f32_16x16x32_bf16 v[96:99], a[124:127], v[76:79], v[96:99]\n" \
"s_lshl_b32 s78, s44, 15\n" \
"s_mov_b32 s79, s97\n" \
"v_mfma_f32_16x16x32_bf16 v[100:103], a[252:255], v[76:79], v[100:103]\n" \
"ds_read_b128 v[88:91], v2 offset:10240\n" \
"s_mov_b32 s44, s82\n" \
"s_add_u32 s55, s55, 1\n" \
"v_mfma_f32_16x16x32_bf16 v[104:107], v[252:255], v[76:79], v[104:107]\n" \
"s_waitcnt vmcnt(18) lgkmcnt(3)\n" \
"ds_write_b128 v7, v[96:99] offset:0\n" \
"v_mfma_f32_16x16x32_bf16 v[108:111], v[92:95], v[76:79], v[108:111]\n" \
"buffer_load_dwordx4 v[76:79], v3, s[36:39], s57 offen offset:3072 sc1\n" \
"ds_write_b128 v7, v[100:103] offset:1024\n" \
"s_cmp_eq_u32 s81, 0\n" \
"s_cbranch_scc1 .Lswo_loop_%=\n" \
"s_nop 7\n" \
"s_nop 7\n" \
"s_waitcnt vmcnt(0) lgkmcnt(0)\n" \
"ds_write_b128 v7, v[104:107] offset:2048\n" \
"ds_write_b128 v7, v[108:111] offset:3072\n" \
"s_waitcnt lgkmcnt(0)\n" \
"s_barrier\n" \
"buffer_load_dword v9, off, s[36:39], s97 sc1\n" \
"v_readfirstlane_b32 s54, v10\n" \
"s_nop 0\n" \
"s_and_b32 s54, s54, 2\n" \
"s_cbranch_scc1 .Lswo_q_%=\n" \
".Lswo_qk_%=:\n" \
"s_waitcnt vmcnt(0)\n" \
"v_readfirstlane_b32 s96, v9\n" \
"s_add_u32 s76, s78, 0\n" \
"s_add_u32 s80, s78, 4194304\n" \
"s_and_b32 s96, s96, 2\n" \
"s_cselect_b32 s76, 0x7ffffff0, s76\n" \
"s_cselect_b32 s77, s78, 0x7ffffff0\n" \
"s_cselect_b32 s80, s80, 0x7ffffff0\n" \
"s_cselect_b32 s94, 0x7ffffff0, s79\n" \
"buffer_load_dwordx2 v[14:15], v6, s[36:39], s80 offen sc1\n" \
"buffer_load_dwordx2 v[12:13], v5, s[64:67], s77 offen\n" \
"ds_read_b128 v[112:115], v8 offset:0\n" \
"ds_read_b128 v[116:119], v8 offset:4096\n" \
"ds_read_b128 v[120:123], v8 offset:8192\n" \
"ds_read_b128 v[124:127], v8 offset:12288\n" \
"s_waitcnt lgkmcnt(2)\n" \
"v_add_f32 v112, v112, v116\n" \
"v_add_f32 v113, v113, v117\n" \
"v_add_f32 v114, v114, v118\n" \
"v_add_f32 v115, v115, v119\n" \
"s_waitcnt lgkmcnt(0)\n" \
"v_add_f32 v120, v120, v124\n" \
"v_add_f32 v121, v121, v125\n" \
"v_add_f32 v122, v122, v126\n" \
"v_add_f32 v123, v123, v127\n" \
"v_add_f32 v112, v112, v120\n" \
"v_add_f32 v113, v113, v121\n" \
"v_add_f32 v114, v114, v122\n" \
"v_add_f32 v115, v115, v123\n" \
"v_xor_b32 v8, s84, v8\n" \
"v_cvt_pk_bf16_f32 v0, v112, v113\n" \
"v_cvt_pk_bf16_f32 v1, v114, v115\n" \
"buffer_store_dwordx2 v[0:1], v6, s[36:39], s76 offen sc1\n" \
"v_lshlrev_b32 v112, 16, v0\n" \
"v_and_b32 v113, 0xffff0000, v0\n" \
"v_lshlrev_b32 v114, 16, v1\n" \
"v_and_b32 v115, 0xffff0000, v1\n" \
"s_waitcnt vmcnt(2)\n" \
"v_lshlrev_b32 v120, 16, v14\n" \
"v_and_b32 v121, 0xffff0000, v14\n" \
"s_waitcnt vmcnt(1)\n" \
"v_lshlrev_b32 v124, 16, v12\n" \
"v_and_b32 v125, 0xffff0000, v12\n" \
"v_lshlrev_b32 v122, 16, v15\n" \
"v_and_b32 v123, 0xffff0000, v15\n" \
"v_lshlrev_b32 v126, 16, v13\n" \
"v_and_b32 v127, 0xffff0000, v13\n" \
"v_fmac_f32 v124, v112, v120\n" \
"v_fmac_f32 v125, v113, v121\n" \
"v_fmac_f32 v126, v114, v122\n" \
"v_fmac_f32 v127, v115, v123\n" \
"v_cvt_pk_bf16_f32 v118, v124, v125\n" \
"v_cvt_pk_bf16_f32 v119, v126, v127\n" \
"buffer_store_dwordx2 v[118:119], v5, s[68:71], s77 offen\n" \
"v_mov_b32 v10, 1\n" \
"s_waitcnt vmcnt(1)\n" \
"s_mov_b64 exec, 1\n" \
"buffer_atomic_or v10, off, s[36:39], s94 sc0\n" \
"s_mov_b64 exec, -1\n" \
"s_mov_b32 s95, s78\n" \
"s_waitcnt vmcnt(0)\n" \
"v_readfirstlane_b32 s54, v10\n" \
"s_nop 0\n" \
"s_and_b32 s54, s54, 2\n" \
"s_cbranch_scc1 .Lswo_r_%=\n" \
".Lswo_rk_%=:\n" \
"v_xor_b32 v7, 0x4000, v7\n" \
"s_waitcnt vmcnt(0) lgkmcnt(0)\n" \
"s_cmp_lt_u32 s55, s46\n" \
"s_cbranch_scc1 .Lswo_prime_%=\n" \
"s_branch .Lswo_end_%=\n" \
".Lswo_w_%=:\n" \
"s_nop 7\n" \
"s_nop 7\n" \
"s_waitcnt vmcnt(0) lgkmcnt(0)\n" \
"s_mov_b32 s61, 0\n" \
".Lswo_wa_%=:\n" \
"s_cmp_lg_u32 s60, 0\n" \
"s_cbranch_scc1 .Lswo_wk_%=\n" \
"buffer_load_dword v11, v4, s[36:39], 0 offen sc1\n" \
"s_waitcnt vmcnt(0)\n" \
"v_cmp_gt_u32_e64 s[62:63], s59, v11\n" \
"s_nop 4\n" \
"s_cmp_eq_u64 s[62:63], 0\n" \
"s_cbranch_scc1 .Lswo_wk_%=\n" \
"s_sleep 2\n" \
"s_add_u32 s61, s61, 1\n" \
"s_cmp_lt_u32 s61, 65536\n" \
"s_cbranch_scc1 .Lswo_wa_%=\n" \
"s_mov_b32 s60, 1\n" \
"v_mov_b32 v11, 256\n" \
"v_mov_b32 v12, 1\n" \
"buffer_store_dword v12, v11, s[36:39], 0 offen sc1\n" \
"s_waitcnt vmcnt(0) lgkmcnt(0)\n" \
"s_branch .Lswo_wk_%=\n" \
".Lswo_p_%=:\n" \
"s_nop 7\n" \
"s_nop 7\n" \
"s_waitcnt vmcnt(0) lgkmcnt(0)\n" \
"s_add_u32 s54, s95, 4194304\n" \
"buffer_load_dwordx2 v[14:15], v6, s[36:39], s54 offen sc1\n" \
"buffer_load_dwordx2 v[12:13], v5, s[64:67], s95 offen\n" \
"v_lshlrev_b32 v112, 16, v0\n" \
"v_and_b32 v113, 0xffff0000, v0\n" \
"v_lshlrev_b32 v114, 16, v1\n" \
"v_and_b32 v115, 0xffff0000, v1\n" \
"s_waitcnt vmcnt(1)\n" \
"v_lshlrev_b32 v120, 16, v14\n" \
"v_and_b32 v121, 0xffff0000, v14\n" \
"s_waitcnt vmcnt(0)\n" \
"v_lshlrev_b32 v124, 16, v12\n" \
"v_and_b32 v125, 0xffff0000, v12\n" \
"v_lshlrev_b32 v122, 16, v15\n" \
"v_and_b32 v123, 0xffff0000, v15\n" \
"v_lshlrev_b32 v126, 16, v13\n" \
"v_and_b32 v127, 0xffff0000, v13\n" \
"v_fmac_f32 v124, v112, v120\n" \
"v_fmac_f32 v125, v113, v121\n" \
"v_fmac_f32 v126, v114, v122\n" \
"v_fmac_f32 v127, v115, v123\n" \
"v_cvt_pk_bf16_f32 v118, v124, v125\n" \
"v_cvt_pk_bf16_f32 v119, v126, v127\n" \
"buffer_store_dwordx2 v[118:119], v5, s[68:71], s95 offen\n" \
"s_waitcnt vmcnt(0) lgkmcnt(0)\n" \
"s_branch .Lswo_pk_%=\n" \
".Lswo_q_%=:\n" \
"s_nop 7\n" \
"s_nop 7\n" \
"s_waitcnt vmcnt(0) lgkmcnt(0)\n" \
"s_add_u32 s54, s95, 4194304\n" \
"buffer_load_dwordx2 v[14:15], v6, s[36:39], s54 offen sc1\n" \
"buffer_load_dwordx2 v[12:13], v5, s[64:67], s95 offen\n" \
"v_lshlrev_b32 v112, 16, v0\n" \
"v_and_b32 v113, 0xffff0000, v0\n" \
"v_lshlrev_b32 v114, 16, v1\n" \
"v_and_b32 v115, 0xffff0000, v1\n" \
"s_waitcnt vmcnt(1)\n" \
"v_lshlrev_b32 v120, 16, v14\n" \
"v_and_b32 v121, 0xffff0000, v14\n" \
"s_waitcnt vmcnt(0)\n" \
"v_lshlrev_b32 v124, 16, v12\n" \
"v_and_b32 v125, 0xffff0000, v12\n" \
"v_lshlrev_b32 v122, 16, v15\n" \
"v_and_b32 v123, 0xffff0000, v15\n" \
"v_lshlrev_b32 v126, 16, v13\n" \
"v_and_b32 v127, 0xffff0000, v13\n" \
"v_fmac_f32 v124, v112, v120\n" \
"v_fmac_f32 v125, v113, v121\n" \
"v_fmac_f32 v126, v114, v122\n" \
"v_fmac_f32 v127, v115, v123\n" \
"v_cvt_pk_bf16_f32 v118, v124, v125\n" \
"v_cvt_pk_bf16_f32 v119, v126, v127\n" \
"buffer_store_dwordx2 v[118:119], v5, s[68:71], s95 offen\n" \
"s_waitcnt vmcnt(0) lgkmcnt(0)\n" \
"s_branch .Lswo_qk_%=\n" \
".Lswo_r_%=:\n" \
"s_nop 7\n" \
"s_nop 7\n" \
"s_waitcnt vmcnt(0) lgkmcnt(0)\n" \
"s_add_u32 s54, s95, 4194304\n" \
"buffer_load_dwordx2 v[14:15], v6, s[36:39], s54 offen sc1\n" \
"buffer_load_dwordx2 v[12:13], v5, s[64:67], s95 offen\n" \
"v_lshlrev_b32 v112, 16, v0\n" \
"v_and_b32 v113, 0xffff0000, v0\n" \
"v_lshlrev_b32 v114, 16, v1\n" \
"v_and_b32 v115, 0xffff0000, v1\n" \
"s_waitcnt vmcnt(1)\n" \
"v_lshlrev_b32 v120, 16, v14\n" \
"v_and_b32 v121, 0xffff0000, v14\n" \
"s_waitcnt vmcnt(0)\n" \
"v_lshlrev_b32 v124, 16, v12\n" \
"v_and_b32 v125, 0xffff0000, v12\n" \
"v_lshlrev_b32 v122, 16, v15\n" \
"v_and_b32 v123, 0xffff0000, v15\n" \
"v_lshlrev_b32 v126, 16, v13\n" \
"v_and_b32 v127, 0xffff0000, v13\n" \
"v_fmac_f32 v124, v112, v120\n" \
"v_fmac_f32 v125, v113, v121\n" \
"v_fmac_f32 v126, v114, v122\n" \
"v_fmac_f32 v127, v115, v123\n" \
"v_cvt_pk_bf16_f32 v118, v124, v125\n" \
"v_cvt_pk_bf16_f32 v119, v126, v127\n" \
"buffer_store_dwordx2 v[118:119], v5, s[68:71], s95 offen\n" \
"s_waitcnt vmcnt(0) lgkmcnt(0)\n" \
"s_branch .Lswo_rk_%=\n" \
".Lswo_end_%=:\n" \
"s_endpgm\n"
#define ASM_S64_G2 \
"s_nop 7\n" \
"s_nop 7\n" \
"s_lshl_b32 s52, s48, 15\n" \
"s_lshl_b32 s53, s48, 13\n" \
"s_add_u32 s53, s53, 98304\n" \
"s_sub_u32 s54, s53, s52\n" \
"v_add_u32 v9, s54, v1\n" \
"s_mov_b32 s54, s85\n" \
"s_mul_i32 s86, s54, 262144\n" \
"s_add_u32 s54, s54, 1\n" \
"s_cmp_eq_u32 s54, 7\n" \
"s_cselect_b32 s54, 0, s54\n" \
"s_mul_i32 s87, s54, 262144\n" \
"s_add_u32 s54, s54, 1\n" \
"s_cmp_eq_u32 s54, 7\n" \
"s_cselect_b32 s54, 0, s54\n" \
"s_mul_i32 s88, s54, 262144\n" \
"s_add_u32 s54, s54, 1\n" \
"s_cmp_eq_u32 s54, 7\n" \
"s_cselect_b32 s54, 0, s54\n" \
"s_mul_i32 s89, s54, 262144\n" \
"s_add_u32 s54, s54, 1\n" \
"s_cmp_eq_u32 s54, 7\n" \
"s_cselect_b32 s54, 0, s54\n" \
"s_mul_i32 s90, s54, 262144\n" \
"s_add_u32 s54, s54, 1\n" \
"s_cmp_eq_u32 s54, 7\n" \
"s_cselect_b32 s54, 0, s54\n" \
"s_mul_i32 s91, s54, 262144\n" \
"s_add_u32 s54, s54, 1\n" \
"s_cmp_eq_u32 s54, 7\n" \
"s_cselect_b32 s54, 0, s54\n" \
"s_mul_i32 s92, s54, 262144\n" \
"s_add_u32 s54, s54, 1\n" \
"s_cmp_eq_u32 s54, 7\n" \
"s_cselect_b32 s54, 0, s54\n" \
"s_add_u32 m0, s52, 0\n" \
"s_add_u32 s54, s86, 0\n" \
"buffer_load_dwordx4 v0, s[40:43], s54 offen nt lds\n" \
"s_add_u32 m0, s52, 1024\n" \
"s_add_u32 s54, s86, 16384\n" \
"buffer_load_dwordx4 v0, s[40:43], s54 offen nt lds\n" \
"s_add_u32 m0, s52, 2048\n" \
"s_add_u32 s54, s86, 32768\n" \
"buffer_load_dwordx4 v0, s[40:43], s54 offen nt lds\n" \
"s_add_u32 m0, s52, 3072\n" \
"s_add_u32 s54, s86, 49152\n" \
"buffer_load_dwordx4 v0, s[40:43], s54 offen nt lds\n" \
"s_mov_b32 s54, s99\n" \
".Lsg2_z0_%=:\n" \
"s_sleep 1\n" \
"s_sub_u32 s54, s54, 1\n" \
"s_cbranch_scc0 .Lsg2_z0_%=\n" \
"s_add_u32 m0, s52, 4096\n" \
"s_add_u32 s54, s86, 65536\n" \
"buffer_load_dwordx4 v0, s[40:43], s54 offen nt lds\n" \
"s_add_u32 m0, s52, 5120\n" \
"s_add_u32 s54, s86, 81920\n" \
"buffer_load_dwordx4 v0, s[40:43], s54 offen nt lds\n" \
"s_add_u32 m0, s52, 6144\n" \
"s_add_u32 s54, s86, 98304\n" \
"buffer_load_dwordx4 v0, s[40:43], s54 offen nt lds\n" \
"s_add_u32 m0, s52, 7168\n" \
"s_add_u32 s54, s86, 114688\n" \
"buffer_load_dwordx4 v0, s[40:43], s54 offen nt lds\n" \
"s_mov_b32 s54, s99\n" \
".Lsg2_z1_%=:\n" \
"s_sleep 1\n" \
"s_sub_u32 s54, s54, 1\n" \
"s_cbranch_scc0 .Lsg2_z1_%=\n" \
"s_add_u32 m0, s52, 8192\n" \
"s_add_u32 s54, s86, 131072\n" \
"buffer_load_dwordx4 v0, s[40:43], s54 offen nt lds\n" \
"s_add_u32 m0, s52, 9216\n" \
"s_add_u32 s54, s86, 147456\n" \
"buffer_load_dwordx4 v0, s[40:43], s54 offen nt lds\n" \
"s_add_u32 m0, s52, 10240\n" \
"s_add_u32 s54, s86, 163840\n" \
"buffer_load_dwordx4 v0, s[40:43], s54 offen nt lds\n" \
"s_add_u32 m0, s52, 11264\n" \
"s_add_u32 s54, s86, 180224\n" \
"buffer_load_dwordx4 v0, s[40:43], s54 offen nt lds\n" \
"s_mov_b32 s54, s99\n" \
".Lsg2_z2_%=:\n" \
"s_sleep 1\n" \
"s_sub_u32 s54, s54, 1\n" \
"s_cbranch_scc0 .Lsg2_z2_%=\n" \
"s_add_u32 m0, s52, 12288\n" \
"s_add_u32 s54, s86, 196608\n" \
"buffer_load_dwordx4 v0, s[40:43], s54 offen nt lds\n" \
"s_add_u32 m0, s52, 13312\n" \
"s_add_u32 s54, s86, 212992\n" \
"buffer_load_dwordx4 v0, s[40:43], s54 offen nt lds\n" \
"s_add_u32 m0, s52, 14336\n" \
"s_add_u32 s54, s86, 229376\n" \
"buffer_load_dwordx4 v0, s[40:43], s54 offen nt lds\n" \
"s_add_u32 m0, s52, 15360\n" \
"s_add_u32 s54, s86, 245760\n" \
"buffer_load_dwordx4 v0, s[40:43], s54 offen nt lds\n" \
"s_mov_b32 s54, s99\n" \
".Lsg2_z3_%=:\n" \
"s_sleep 1\n" \
"s_sub_u32 s54, s54, 1\n" \
"s_cbranch_scc0 .Lsg2_z3_%=\n" \
"s_add_u32 m0, s52, 16384\n" \
"s_add_u32 s54, s87, 0\n" \
"buffer_load_dwordx4 v0, s[40:43], s54 offen nt lds\n" \
"s_add_u32 m0, s52, 17408\n" \
"s_add_u32 s54, s87, 16384\n" \
"buffer_load_dwordx4 v0, s[40:43], s54 offen nt lds\n" \
"s_add_u32 m0, s52, 18432\n" \
"s_add_u32 s54, s87, 32768\n" \
"buffer_load_dwordx4 v0, s[40:43], s54 offen nt lds\n" \
"s_add_u32 m0, s52, 19456\n" \
"s_add_u32 s54, s87, 49152\n" \
"buffer_load_dwordx4 v0, s[40:43], s54 offen nt lds\n" \
"s_mov_b32 s54, s99\n" \
".Lsg2_z4_%=:\n" \
"s_sleep 1\n" \
"s_sub_u32 s54, s54, 1\n" \
"s_cbranch_scc0 .Lsg2_z4_%=\n" \
"s_add_u32 m0, s52, 20480\n" \
"s_add_u32 s54, s87, 65536\n" \
"buffer_load_dwordx4 v0, s[40:43], s54 offen nt lds\n" \
"s_add_u32 m0, s52, 21504\n" \
"s_add_u32 s54, s87, 81920\n" \
"buffer_load_dwordx4 v0, s[40:43], s54 offen nt lds\n" \
"s_add_u32 m0, s52, 22528\n" \
"s_add_u32 s54, s87, 98304\n" \
"buffer_load_dwordx4 v0, s[40:43], s54 offen nt lds\n" \
"s_add_u32 m0, s52, 23552\n" \
"s_add_u32 s54, s87, 114688\n" \
"buffer_load_dwordx4 v0, s[40:43], s54 offen nt lds\n" \
"s_mov_b32 s54, s99\n" \
".Lsg2_z5_%=:\n" \
"s_sleep 1\n" \
"s_sub_u32 s54, s54, 1\n" \
"s_cbranch_scc0 .Lsg2_z5_%=\n" \
"s_add_u32 m0, s52, 24576\n" \
"s_add_u32 s54, s87, 131072\n" \
"buffer_load_dwordx4 v0, s[40:43], s54 offen nt lds\n" \
"s_add_u32 m0, s52, 25600\n" \
"s_add_u32 s54, s87, 147456\n" \
"buffer_load_dwordx4 v0, s[40:43], s54 offen nt lds\n" \
"s_add_u32 m0, s52, 26624\n" \
"s_add_u32 s54, s87, 163840\n" \
"buffer_load_dwordx4 v0, s[40:43], s54 offen nt lds\n" \
"s_add_u32 m0, s52, 27648\n" \
"s_add_u32 s54, s87, 180224\n" \
"buffer_load_dwordx4 v0, s[40:43], s54 offen nt lds\n" \
"s_mov_b32 s54, s99\n" \
".Lsg2_z6_%=:\n" \
"s_sleep 1\n" \
"s_sub_u32 s54, s54, 1\n" \
"s_cbranch_scc0 .Lsg2_z6_%=\n" \
"s_add_u32 m0, s52, 28672\n" \
"s_add_u32 s54, s87, 196608\n" \
"buffer_load_dwordx4 v0, s[40:43], s54 offen nt lds\n" \
"s_add_u32 m0, s52, 29696\n" \
"s_add_u32 s54, s87, 212992\n" \
"buffer_load_dwordx4 v0, s[40:43], s54 offen nt lds\n" \
"s_add_u32 m0, s52, 30720\n" \
"s_add_u32 s54, s87, 229376\n" \
"buffer_load_dwordx4 v0, s[40:43], s54 offen nt lds\n" \
"s_add_u32 m0, s52, 31744\n" \
"s_add_u32 s54, s87, 245760\n" \
"buffer_load_dwordx4 v0, s[40:43], s54 offen nt lds\n" \
"s_mov_b32 s54, s99\n" \
".Lsg2_z7_%=:\n" \
"s_sleep 1\n" \
"s_sub_u32 s54, s54, 1\n" \
"s_cbranch_scc0 .Lsg2_z7_%=\n" \
"s_add_u32 m0, s53, 32768\n" \
"s_add_u32 s54, s88, 0\n" \
"buffer_load_dwordx4 v0, s[40:43], s54 offen nt lds\n" \
"s_add_u32 m0, s53, 33792\n" \
"s_add_u32 s54, s88, 16384\n" \
"buffer_load_dwordx4 v0, s[40:43], s54 offen nt lds\n" \
"s_add_u32 m0, s53, 34816\n" \
"s_add_u32 s54, s88, 32768\n" \
"buffer_load_dwordx4 v0, s[40:43], s54 offen nt lds\n" \
"s_add_u32 m0, s53, 35840\n" \
"s_add_u32 s54, s88, 49152\n" \
"buffer_load_dwordx4 v0, s[40:43], s54 offen nt lds\n" \
"s_mov_b32 s54, s99\n" \
".Lsg2_z8_%=:\n" \
"s_sleep 1\n" \
"s_sub_u32 s54, s54, 1\n" \
"s_cbranch_scc0 .Lsg2_z8_%=\n" \
"s_add_u32 m0, s53, 36864\n" \
"s_add_u32 s54, s88, 65536\n" \
"buffer_load_dwordx4 v0, s[40:43], s54 offen nt lds\n" \
"s_add_u32 m0, s53, 37888\n" \
"s_add_u32 s54, s88, 81920\n" \
"buffer_load_dwordx4 v0, s[40:43], s54 offen nt lds\n" \
"s_add_u32 m0, s53, 38912\n" \
"s_add_u32 s54, s88, 98304\n" \
"buffer_load_dwordx4 v0, s[40:43], s54 offen nt lds\n" \
"s_add_u32 m0, s53, 39936\n" \
"s_add_u32 s54, s88, 114688\n" \
"buffer_load_dwordx4 v0, s[40:43], s54 offen nt lds\n" \
"s_mov_b32 s54, s99\n" \
".Lsg2_z9_%=:\n" \
"s_sleep 1\n" \
"s_sub_u32 s54, s54, 1\n" \
"s_cbranch_scc0 .Lsg2_z9_%=\n" \
"s_waitcnt vmcnt(36)\n" \
"ds_read_b64_tr_b16 a[0:1], v1 offset:0\n" \
"ds_read_b64_tr_b16 a[2:3], v1 offset:512\n" \
"ds_read_b64_tr_b16 a[128:129], v1 offset:32\n" \
"ds_read_b64_tr_b16 a[130:131], v1 offset:544\n" \
"ds_read_b64_tr_b16 v[128:129], v1 offset:64\n" \
"ds_read_b64_tr_b16 v[130:131], v1 offset:576\n" \
"ds_read_b64_tr_b16 v[16:17], v1 offset:96\n" \
"ds_read_b64_tr_b16 v[18:19], v1 offset:608\n" \
"s_waitcnt lgkmcnt(0)\n" \
"s_add_u32 m0, s52, 0\n" \
"s_add_u32 s54, s88, 131072\n" \
"buffer_load_dwordx4 v0, s[40:43], s54 offen nt lds\n" \
"s_add_u32 m0, s52, 1024\n" \
"s_add_u32 s54, s88, 147456\n" \
"buffer_load_dwordx4 v0, s[40:43], s54 offen nt lds\n" \
"s_add_u32 m0, s52, 2048\n" \
"s_add_u32 s54, s88, 163840\n" \
"buffer_load_dwordx4 v0, s[40:43], s54 offen nt lds\n" \
"s_add_u32 m0, s52, 3072\n" \
"s_add_u32 s54, s88, 180224\n" \
"buffer_load_dwordx4 v0, s[40:43], s54 offen nt lds\n" \
"s_mov_b32 s54, s99\n" \
".Lsg2_z10_%=:\n" \
"s_sleep 1\n" \
"s_sub_u32 s54, s54, 1\n" \
"s_cbranch_scc0 .Lsg2_z10_%=\n" \
"s_waitcnt vmcnt(36)\n" \
"ds_read_b64_tr_b16 a[4:5], v1 offset:4096\n" \
"ds_read_b64_tr_b16 a[6:7], v1 offset:4608\n" \
"ds_read_b64_tr_b16 a[132:133], v1 offset:4128\n" \
"ds_read_b64_tr_b16 a[134:135], v1 offset:4640\n" \
"ds_read_b64_tr_b16 v[132:133], v1 offset:4160\n" \
"ds_read_b64_tr_b16 v[134:135], v1 offset:4672\n" \
"ds_read_b64_tr_b16 v[20:21], v1 offset:4192\n" \
"ds_read_b64_tr_b16 v[22:23], v1 offset:4704\n" \
"s_waitcnt lgkmcnt(0)\n" \
"s_add_u32 m0, s52, 4096\n" \
"s_add_u32 s54, s88, 196608\n" \
"buffer_load_dwordx4 v0, s[40:43], s54 offen nt lds\n" \
"s_add_u32 m0, s52, 5120\n" \
"s_add_u32 s54, s88, 212992\n" \
"buffer_load_dwordx4 v0, s[40:43], s54 offen nt lds\n" \
"s_add_u32 m0, s52, 6144\n" \
"s_add_u32 s54, s88, 229376\n" \
"buffer_load_dwordx4 v0, s[40:43], s54 offen nt lds\n" \
"s_add_u32 m0, s52, 7168\n" \
"s_add_u32 s54, s88, 245760\n" \
"buffer_load_dwordx4 v0, s[40:43], s54 offen nt lds\n" \
"s_mov_b32 s54, s99\n" \
".Lsg2_z11_%=:\n" \
"s_sleep 1\n" \
"s_sub_u32 s54, s54, 1\n" \
"s_cbranch_scc0 .Lsg2_z11_%=\n" \
"s_waitcnt vmcnt(36)\n" \
"ds_read_b64_tr_b16 a[8:9], v1 offset:8192\n" \
"ds_read_b64_tr_b16 a[10:11], v1 offset:8704\n" \
"ds_read_b64_tr_b16 a[136:137], v1 offset:8224\n" \
"ds_read_b64_tr_b16 a[138:139], v1 offset:8736\n" \
"ds_read_b64_tr_b16 v[136:137], v1 offset:8256\n" \
"ds_read_b64_tr_b16 v[138:139], v1 offset:8768\n" \
"ds_read_b64_tr_b16 v[24:25], v1 offset:8288\n" \
"ds_read_b64_tr_b16 v[26:27], v1 offset:8800\n" \
"s_waitcnt lgkmcnt(0)\n" \
"s_add_u32 m0, s52, 8192\n" \
"s_add_u32 s54, s89, 0\n" \
"buffer_load_dwordx4 v0, s[40:43], s54 offen nt lds\n" \
"s_add_u32 m0, s52, 9216\n" \
"s_add_u32 s54, s89, 16384\n" \
"buffer_load_dwordx4 v0, s[40:43], s54 offen nt lds\n" \
"s_add_u32 m0, s52, 10240\n" \
"s_add_u32 s54, s89, 32768\n" \
"buffer_load_dwordx4 v0, s[40:43], s54 offen nt lds\n" \
"s_add_u32 m0, s52, 11264\n" \
"s_add_u32 s54, s89, 49152\n" \
"buffer_load_dwordx4 v0, s[40:43], s54 offen nt lds\n" \
"s_mov_b32 s54, s99\n" \
".Lsg2_z12_%=:\n" \
"s_sleep 1\n" \
"s_sub_u32 s54, s54, 1\n" \
"s_cbranch_scc0 .Lsg2_z12_%=\n" \
"s_waitcnt vmcnt(36)\n" \
"ds_read_b64_tr_b16 a[12:13], v1 offset:12288\n" \
"ds_read_b64_tr_b16 a[14:15], v1 offset:12800\n" \
"ds_read_b64_tr_b16 a[140:141], v1 offset:12320\n" \
"ds_read_b64_tr_b16 a[142:143], v1 offset:12832\n" \
"ds_read_b64_tr_b16 v[140:141], v1 offset:12352\n" \
"ds_read_b64_tr_b16 v[142:143], v1 offset:12864\n" \
"ds_read_b64_tr_b16 v[28:29], v1 offset:12384\n" \
"ds_read_b64_tr_b16 v[30:31], v1 offset:12896\n" \
"s_waitcnt lgkmcnt(0)\n" \
"s_add_u32 m0, s52, 12288\n" \
"s_add_u32 s54, s89, 65536\n" \
"buffer_load_dwordx4 v0, s[40:43], s54 offen nt lds\n" \
"s_add_u32 m0, s52, 13312\n" \
"s_add_u32 s54, s89, 81920\n" \
"buffer_load_dwordx4 v0, s[40:43], s54 offen nt lds\n" \
"s_add_u32 m0, s52, 14336\n" \
"s_add_u32 s54, s89, 98304\n" \
"buffer_load_dwordx4 v0, s[40:43], s54 offen nt lds\n" \
"s_add_u32 m0, s52, 15360\n" \
"s_add_u32 s54, s89, 114688\n" \
"buffer_load_dwordx4 v0, s[40:43], s54 offen nt lds\n" \
"s_mov_b32 s54, s99\n" \
".Lsg2_z13_%=:\n" \
"s_sleep 1\n" \
"s_sub_u32 s54, s54, 1\n" \
"s_cbranch_scc0 .Lsg2_z13_%=\n" \
"s_waitcnt vmcnt(36)\n" \
"ds_read_b64_tr_b16 a[16:17], v1 offset:16384\n" \
"ds_read_b64_tr_b16 a[18:19], v1 offset:16896\n" \
"ds_read_b64_tr_b16 a[144:145], v1 offset:16416\n" \
"ds_read_b64_tr_b16 a[146:147], v1 offset:16928\n" \
"ds_read_b64_tr_b16 v[144:145], v1 offset:16448\n" \
"ds_read_b64_tr_b16 v[146:147], v1 offset:16960\n" \
"ds_read_b64_tr_b16 v[32:33], v1 offset:16480\n" \
"ds_read_b64_tr_b16 v[34:35], v1 offset:16992\n" \
"s_waitcnt lgkmcnt(0)\n" \
"s_add_u32 m0, s52, 16384\n" \
"s_add_u32 s54, s89, 131072\n" \
"buffer_load_dwordx4 v0, s[40:43], s54 offen nt lds\n" \
"s_add_u32 m0, s52, 17408\n" \
"s_add_u32 s54, s89, 147456\n" \
"buffer_load_dwordx4 v0, s[40:43], s54 offen nt lds\n" \
"s_add_u32 m0, s52, 18432\n" \
"s_add_u32 s54, s89, 163840\n" \
"buffer_load_dwordx4 v0, s[40:43], s54 offen nt lds\n" \
"s_add_u32 m0, s52, 19456\n" \
"s_add_u32 s54, s89, 180224\n" \
"buffer_load_dwordx4 v0, s[40:43], s54 offen nt lds\n" \
"s_mov_b32 s54, s99\n" \
".Lsg2_z14_%=:\n" \
"s_sleep 1\n" \
"s_sub_u32 s54, s54, 1\n" \
"s_cbranch_scc0 .Lsg2_z14_%=\n" \
"s_waitcnt vmcnt(36)\n" \
"ds_read_b64_tr_b16 a[20:21], v1 offset:20480\n" \
"ds_read_b64_tr_b16 a[22:23], v1 offset:20992\n" \
"ds_read_b64_tr_b16 a[148:149], v1 offset:20512\n" \
"ds_read_b64_tr_b16 a[150:151], v1 offset:21024\n" \
"ds_read_b64_tr_b16 v[148:149], v1 offset:20544\n" \
"ds_read_b64_tr_b16 v[150:151], v1 offset:21056\n" \
"ds_read_b64_tr_b16 v[36:37], v1 offset:20576\n" \
"ds_read_b64_tr_b16 v[38:39], v1 offset:21088\n" \
"s_waitcnt lgkmcnt(0)\n" \
"s_add_u32 m0, s52, 20480\n" \
"s_add_u32 s54, s89, 196608\n" \
"buffer_load_dwordx4 v0, s[40:43], s54 offen nt lds\n" \
"s_add_u32 m0, s52, 21504\n" \
"s_add_u32 s54, s89, 212992\n" \
"buffer_load_dwordx4 v0, s[40:43], s54 offen nt lds\n" \
"s_add_u32 m0, s52, 22528\n" \
"s_add_u32 s54, s89, 229376\n" \
"buffer_load_dwordx4 v0, s[40:43], s54 offen nt lds\n" \
"s_add_u32 m0, s52, 23552\n" \
"s_add_u32 s54, s89, 245760\n" \
"buffer_load_dwordx4 v0, s[40:43], s54 offen nt lds\n" \
"s_mov_b32 s54, s99\n" \
".Lsg2_z15_%=:\n" \
"s_sleep 1\n" \
"s_sub_u32 s54, s54, 1\n" \
"s_cbranch_scc0 .Lsg2_z15_%=\n" \
"s_waitcnt vmcnt(36)\n" \
"ds_read_b64_tr_b16 a[24:25], v1 offset:24576\n" \
"ds_read_b64_tr_b16 a[26:27], v1 offset:25088\n" \
"ds_read_b64_tr_b16 a[152:153], v1 offset:24608\n" \
"ds_read_b64_tr_b16 a[154:155], v1 offset:25120\n" \
"ds_read_b64_tr_b16 v[152:153], v1 offset:24640\n" \
"ds_read_b64_tr_b16 v[154:155], v1 offset:25152\n" \
"ds_read_b64_tr_b16 v[40:41], v1 offset:24672\n" \
"ds_read_b64_tr_b16 v[42:43], v1 offset:25184\n" \
"s_waitcnt lgkmcnt(0)\n" \
"s_add_u32 m0, s52, 24576\n" \
"s_add_u32 s54, s90, 0\n" \
"buffer_load_dwordx4 v0, s[40:43], s54 offen nt lds\n" \
"s_add_u32 m0, s52, 25600\n" \
"s_add_u32 s54, s90, 16384\n" \
"buffer_load_dwordx4 v0, s[40:43], s54 offen nt lds\n" \
"s_add_u32 m0, s52, 26624\n" \
"s_add_u32 s54, s90, 32768\n" \
"buffer_load_dwordx4 v0, s[40:43], s54 offen nt lds\n" \
"s_add_u32 m0, s52, 27648\n" \
"s_add_u32 s54, s90, 49152\n" \
"buffer_load_dwordx4 v0, s[40:43], s54 offen nt lds\n" \
"s_mov_b32 s54, s99\n" \
".Lsg2_z16_%=:\n" \
"s_sleep 1\n" \
"s_sub_u32 s54, s54, 1\n" \
"s_cbranch_scc0 .Lsg2_z16_%=\n" \
"s_waitcnt vmcnt(36)\n" \
"ds_read_b64_tr_b16 a[28:29], v1 offset:28672\n" \
"ds_read_b64_tr_b16 a[30:31], v1 offset:29184\n" \
"ds_read_b64_tr_b16 a[156:157], v1 offset:28704\n" \
"ds_read_b64_tr_b16 a[158:159], v1 offset:29216\n" \
"ds_read_b64_tr_b16 v[156:157], v1 offset:28736\n" \
"ds_read_b64_tr_b16 v[158:159], v1 offset:29248\n" \
"ds_read_b64_tr_b16 v[44:45], v1 offset:28768\n" \
"ds_read_b64_tr_b16 v[46:47], v1 offset:29280\n" \
"s_waitcnt lgkmcnt(0)\n" \
"s_add_u32 m0, s52, 28672\n" \
"s_add_u32 s54, s90, 65536\n" \
"buffer_load_dwordx4 v0, s[40:43], s54 offen nt lds\n" \
"s_add_u32 m0, s52, 29696\n" \
"s_add_u32 s54, s90, 81920\n" \
"buffer_load_dwordx4 v0, s[40:43], s54 offen nt lds\n" \
"s_add_u32 m0, s52, 30720\n" \
"s_add_u32 s54, s90, 98304\n" \
"buffer_load_dwordx4 v0, s[40:43], s54 offen nt lds\n" \
"s_add_u32 m0, s52, 31744\n" \
"s_add_u32 s54, s90, 114688\n" \
"buffer_load_dwordx4 v0, s[40:43], s54 offen nt lds\n" \
"s_mov_b32 s54, s99\n" \
".Lsg2_z17_%=:\n" \
"s_sleep 1\n" \
"s_sub_u32 s54, s54, 1\n" \
"s_cbranch_scc0 .Lsg2_z17_%=\n" \
"s_waitcnt vmcnt(36)\n" \
"ds_read_b64_tr_b16 a[32:33], v9 offset:32768\n" \
"ds_read_b64_tr_b16 a[34:35], v9 offset:33280\n" \
"ds_read_b64_tr_b16 a[160:161], v9 offset:32800\n" \
"ds_read_b64_tr_b16 a[162:163], v9 offset:33312\n" \
"ds_read_b64_tr_b16 v[160:161], v9 offset:32832\n" \
"ds_read_b64_tr_b16 v[162:163], v9 offset:33344\n" \
"ds_read_b64_tr_b16 v[48:49], v9 offset:32864\n" \
"ds_read_b64_tr_b16 v[50:51], v9 offset:33376\n" \
"s_waitcnt lgkmcnt(0)\n" \
"s_add_u32 m0, s53, 32768\n" \
"s_add_u32 s54, s90, 131072\n" \
"buffer_load_dwordx4 v0, s[40:43], s54 offen nt lds\n" \
"s_add_u32 m0, s53, 33792\n" \
"s_add_u32 s54, s90, 147456\n" \
"buffer_load_dwordx4 v0, s[40:43], s54 offen nt lds\n" \
"s_add_u32 m0, s53, 34816\n" \
"s_add_u32 s54, s90, 163840\n" \
"buffer_load_dwordx4 v0, s[40:43], s54 offen nt lds\n" \
"s_add_u32 m0, s53, 35840\n" \
"s_add_u32 s54, s90, 180224\n" \
"buffer_load_dwordx4 v0, s[40:43], s54 offen nt lds\n" \
"s_mov_b32 s54, s99\n" \
".Lsg2_z18_%=:\n" \
"s_sleep 1\n" \
"s_sub_u32 s54, s54, 1\n" \
"s_cbranch_scc0 .Lsg2_z18_%=\n" \
"s_waitcnt vmcnt(36)\n" \
"ds_read_b64_tr_b16 a[36:37], v9 offset:36864\n" \
"ds_read_b64_tr_b16 a[38:39], v9 offset:37376\n" \
"ds_read_b64_tr_b16 a[164:165], v9 offset:36896\n" \
"ds_read_b64_tr_b16 a[166:167], v9 offset:37408\n" \
"ds_read_b64_tr_b16 v[164:165], v9 offset:36928\n" \
"ds_read_b64_tr_b16 v[166:167], v9 offset:37440\n" \
"ds_read_b64_tr_b16 v[52:53], v9 offset:36960\n" \
"ds_read_b64_tr_b16 v[54:55], v9 offset:37472\n" \
"s_waitcnt lgkmcnt(0)\n" \
"s_add_u32 m0, s53, 36864\n" \
"s_add_u32 s54, s90, 196608\n" \
"buffer_load_dwordx4 v0, s[40:43], s54 offen nt lds\n" \
"s_add_u32 m0, s53, 37888\n" \
"s_add_u32 s54, s90, 212992\n" \
"buffer_load_dwordx4 v0, s[40:43], s54 offen nt lds\n" \
"s_add_u32 m0, s53, 38912\n" \
"s_add_u32 s54, s90, 229376\n" \
"buffer_load_dwordx4 v0, s[40:43], s54 offen nt lds\n" \
"s_add_u32 m0, s53, 39936\n" \
"s_add_u32 s54, s90, 245760\n" \
"buffer_load_dwordx4 v0, s[40:43], s54 offen nt lds\n" \
"s_mov_b32 s54, s99\n" \
".Lsg2_z19_%=:\n" \
"s_sleep 1\n" \
"s_sub_u32 s54, s54, 1\n" \
"s_cbranch_scc0 .Lsg2_z19_%=\n" \
"s_waitcnt vmcnt(36)\n" \
"ds_read_b64_tr_b16 a[40:41], v1 offset:0\n" \
"ds_read_b64_tr_b16 a[42:43], v1 offset:512\n" \
"ds_read_b64_tr_b16 a[168:169], v1 offset:32\n" \
"ds_read_b64_tr_b16 a[170:171], v1 offset:544\n" \
"ds_read_b64_tr_b16 v[168:169], v1 offset:64\n" \
"ds_read_b64_tr_b16 v[170:171], v1 offset:576\n" \
"ds_read_b64_tr_b16 v[56:57], v1 offset:96\n" \
"ds_read_b64_tr_b16 v[58:59], v1 offset:608\n" \
"s_waitcnt lgkmcnt(0)\n" \
"s_add_u32 m0, s52, 0\n" \
"s_add_u32 s54, s91, 0\n" \
"buffer_load_dwordx4 v0, s[40:43], s54 offen nt lds\n" \
"s_add_u32 m0, s52, 1024\n" \
"s_add_u32 s54, s91, 16384\n" \
"buffer_load_dwordx4 v0, s[40:43], s54 offen nt lds\n" \
"s_add_u32 m0, s52, 2048\n" \
"s_add_u32 s54, s91, 32768\n" \
"buffer_load_dwordx4 v0, s[40:43], s54 offen nt lds\n" \
"s_add_u32 m0, s52, 3072\n" \
"s_add_u32 s54, s91, 49152\n" \
"buffer_load_dwordx4 v0, s[40:43], s54 offen nt lds\n" \
"s_mov_b32 s54, s99\n" \
".Lsg2_z20_%=:\n" \
"s_sleep 1\n" \
"s_sub_u32 s54, s54, 1\n" \
"s_cbranch_scc0 .Lsg2_z20_%=\n" \
"s_waitcnt vmcnt(36)\n" \
"ds_read_b64_tr_b16 a[44:45], v1 offset:4096\n" \
"ds_read_b64_tr_b16 a[46:47], v1 offset:4608\n" \
"ds_read_b64_tr_b16 a[172:173], v1 offset:4128\n" \
"ds_read_b64_tr_b16 a[174:175], v1 offset:4640\n" \
"ds_read_b64_tr_b16 v[172:173], v1 offset:4160\n" \
"ds_read_b64_tr_b16 v[174:175], v1 offset:4672\n" \
"ds_read_b64_tr_b16 v[60:61], v1 offset:4192\n" \
"ds_read_b64_tr_b16 v[62:63], v1 offset:4704\n" \
"s_waitcnt lgkmcnt(0)\n" \
"s_add_u32 m0, s52, 4096\n" \
"s_add_u32 s54, s91, 65536\n" \
"buffer_load_dwordx4 v0, s[40:43], s54 offen nt lds\n" \
"s_add_u32 m0, s52, 5120\n" \
"s_add_u32 s54, s91, 81920\n" \
"buffer_load_dwordx4 v0, s[40:43], s54 offen nt lds\n" \
"s_add_u32 m0, s52, 6144\n" \
"s_add_u32 s54, s91, 98304\n" \
"buffer_load_dwordx4 v0, s[40:43], s54 offen nt lds\n" \
"s_add_u32 m0, s52, 7168\n" \
"s_add_u32 s54, s91, 114688\n" \
"buffer_load_dwordx4 v0, s[40:43], s54 offen nt lds\n" \
"s_mov_b32 s54, s99\n" \
".Lsg2_z21_%=:\n" \
"s_sleep 1\n" \
"s_sub_u32 s54, s54, 1\n" \
"s_cbranch_scc0 .Lsg2_z21_%=\n" \
"s_waitcnt vmcnt(36)\n" \
"ds_read_b64_tr_b16 a[48:49], v1 offset:8192\n" \
"ds_read_b64_tr_b16 a[50:51], v1 offset:8704\n" \
"ds_read_b64_tr_b16 a[176:177], v1 offset:8224\n" \
"ds_read_b64_tr_b16 a[178:179], v1 offset:8736\n" \
"ds_read_b64_tr_b16 v[176:177], v1 offset:8256\n" \
"ds_read_b64_tr_b16 v[178:179], v1 offset:8768\n" \
"ds_read_b64_tr_b16 v[64:65], v1 offset:8288\n" \
"ds_read_b64_tr_b16 v[66:67], v1 offset:8800\n" \
"s_waitcnt lgkmcnt(0)\n" \
"s_add_u32 m0, s52, 8192\n" \
"s_add_u32 s54, s91, 131072\n" \
"buffer_load_dwordx4 v0, s[40:43], s54 offen nt lds\n" \
"s_add_u32 m0, s52, 9216\n" \
"s_add_u32 s54, s91, 147456\n" \
"buffer_load_dwordx4 v0, s[40:43], s54 offen nt lds\n" \
"s_add_u32 m0, s52, 10240\n" \
"s_add_u32 s54, s91, 163840\n" \
"buffer_load_dwordx4 v0, s[40:43], s54 offen nt lds\n" \
"s_add_u32 m0, s52, 11264\n" \
"s_add_u32 s54, s91, 180224\n" \
"buffer_load_dwordx4 v0, s[40:43], s54 offen nt lds\n" \
"s_mov_b32 s54, s99\n" \
".Lsg2_z22_%=:\n" \
"s_sleep 1\n" \
"s_sub_u32 s54, s54, 1\n" \
"s_cbranch_scc0 .Lsg2_z22_%=\n" \
"s_waitcnt vmcnt(36)\n" \
"ds_read_b64_tr_b16 a[52:53], v1 offset:12288\n" \
"ds_read_b64_tr_b16 a[54:55], v1 offset:12800\n" \
"ds_read_b64_tr_b16 a[180:181], v1 offset:12320\n" \
"ds_read_b64_tr_b16 a[182:183], v1 offset:12832\n" \
"ds_read_b64_tr_b16 v[180:181], v1 offset:12352\n" \
"ds_read_b64_tr_b16 v[182:183], v1 offset:12864\n" \
"ds_read_b64_tr_b16 v[68:69], v1 offset:12384\n" \
"ds_read_b64_tr_b16 v[70:71], v1 offset:12896\n" \
"s_waitcnt lgkmcnt(0)\n" \
"s_add_u32 m0, s52, 12288\n" \
"s_add_u32 s54, s91, 196608\n" \
"buffer_load_dwordx4 v0, s[40:43], s54 offen nt lds\n" \
"s_add_u32 m0, s52, 13312\n" \
"s_add_u32 s54, s91, 212992\n" \
"buffer_load_dwordx4 v0, s[40:43], s54 offen nt lds\n" \
"s_add_u32 m0, s52, 14336\n" \
"s_add_u32 s54, s91, 229376\n" \
"buffer_load_dwordx4 v0, s[40:43], s54 offen nt lds\n" \
"s_add_u32 m0, s52, 15360\n" \
"s_add_u32 s54, s91, 245760\n" \
"buffer_load_dwordx4 v0, s[40:43], s54 offen nt lds\n" \
"s_mov_b32 s54, s99\n" \
".Lsg2_z23_%=:\n" \
"s_sleep 1\n" \
"s_sub_u32 s54, s54, 1\n" \
"s_cbranch_scc0 .Lsg2_z23_%=\n" \
"s_waitcnt vmcnt(36)\n" \
"ds_read_b64_tr_b16 a[56:57], v1 offset:16384\n" \
"ds_read_b64_tr_b16 a[58:59], v1 offset:16896\n" \
"ds_read_b64_tr_b16 a[184:185], v1 offset:16416\n" \
"ds_read_b64_tr_b16 a[186:187], v1 offset:16928\n" \
"ds_read_b64_tr_b16 v[184:185], v1 offset:16448\n" \
"ds_read_b64_tr_b16 v[186:187], v1 offset:16960\n" \
"ds_read_b64_tr_b16 v[72:73], v1 offset:16480\n" \
"ds_read_b64_tr_b16 v[74:75], v1 offset:16992\n" \
"s_waitcnt lgkmcnt(0)\n" \
"s_add_u32 m0, s52, 16384\n" \
"s_add_u32 s54, s92, 0\n" \
"buffer_load_dwordx4 v0, s[40:43], s54 offen nt lds\n" \
"s_add_u32 m0, s52, 17408\n" \
"s_add_u32 s54, s92, 16384\n" \
"buffer_load_dwordx4 v0, s[40:43], s54 offen nt lds\n" \
"s_add_u32 m0, s52, 18432\n" \
"s_add_u32 s54, s92, 32768\n" \
"buffer_load_dwordx4 v0, s[40:43], s54 offen nt lds\n" \
"s_add_u32 m0, s52, 19456\n" \
"s_add_u32 s54, s92, 49152\n" \
"buffer_load_dwordx4 v0, s[40:43], s54 offen nt lds\n" \
"s_mov_b32 s54, s99\n" \
".Lsg2_z24_%=:\n" \
"s_sleep 1\n" \
"s_sub_u32 s54, s54, 1\n" \
"s_cbranch_scc0 .Lsg2_z24_%=\n" \
"s_waitcnt vmcnt(36)\n" \
"ds_read_b64_tr_b16 a[60:61], v1 offset:20480\n" \
"ds_read_b64_tr_b16 a[62:63], v1 offset:20992\n" \
"ds_read_b64_tr_b16 a[188:189], v1 offset:20512\n" \
"ds_read_b64_tr_b16 a[190:191], v1 offset:21024\n" \
"ds_read_b64_tr_b16 v[188:189], v1 offset:20544\n" \
"ds_read_b64_tr_b16 v[190:191], v1 offset:21056\n" \
"ds_read_b64_tr_b16 v[76:77], v1 offset:20576\n" \
"ds_read_b64_tr_b16 v[78:79], v1 offset:21088\n" \
"s_waitcnt lgkmcnt(0)\n" \
"s_add_u32 m0, s52, 20480\n" \
"s_add_u32 s54, s92, 65536\n" \
"buffer_load_dwordx4 v0, s[40:43], s54 offen nt lds\n" \
"s_add_u32 m0, s52, 21504\n" \
"s_add_u32 s54, s92, 81920\n" \
"buffer_load_dwordx4 v0, s[40:43], s54 offen nt lds\n" \
"s_add_u32 m0, s52, 22528\n" \
"s_add_u32 s54, s92, 98304\n" \
"buffer_load_dwordx4 v0, s[40:43], s54 offen nt lds\n" \
"s_add_u32 m0, s52, 23552\n" \
"s_add_u32 s54, s92, 114688\n" \
"buffer_load_dwordx4 v0, s[40:43], s54 offen nt lds\n" \
"s_mov_b32 s54, s99\n" \
".Lsg2_z25_%=:\n" \
"s_sleep 1\n" \
"s_sub_u32 s54, s54, 1\n" \
"s_cbranch_scc0 .Lsg2_z25_%=\n" \
"s_waitcnt vmcnt(36)\n" \
"ds_read_b64_tr_b16 a[64:65], v1 offset:24576\n" \
"ds_read_b64_tr_b16 a[66:67], v1 offset:25088\n" \
"ds_read_b64_tr_b16 a[192:193], v1 offset:24608\n" \
"ds_read_b64_tr_b16 a[194:195], v1 offset:25120\n" \
"ds_read_b64_tr_b16 v[192:193], v1 offset:24640\n" \
"ds_read_b64_tr_b16 v[194:195], v1 offset:25152\n" \
"ds_read_b64_tr_b16 v[80:81], v1 offset:24672\n" \
"ds_read_b64_tr_b16 v[82:83], v1 offset:25184\n" \
"s_waitcnt lgkmcnt(0)\n" \
"s_add_u32 m0, s52, 24576\n" \
"s_add_u32 s54, s92, 131072\n" \
"buffer_load_dwordx4 v0, s[40:43], s54 offen nt lds\n" \
"s_add_u32 m0, s52, 25600\n" \
"s_add_u32 s54, s92, 147456\n" \
"buffer_load_dwordx4 v0, s[40:43], s54 offen nt lds\n" \
"s_add_u32 m0, s52, 26624\n" \
"s_add_u32 s54, s92, 163840\n" \
"buffer_load_dwordx4 v0, s[40:43], s54 offen nt lds\n" \
"s_add_u32 m0, s52, 27648\n" \
"s_add_u32 s54, s92, 180224\n" \
"buffer_load_dwordx4 v0, s[40:43], s54 offen nt lds\n" \
"s_mov_b32 s54, s99\n" \
".Lsg2_z26_%=:\n" \
"s_sleep 1\n" \
"s_sub_u32 s54, s54, 1\n" \
"s_cbranch_scc0 .Lsg2_z26_%=\n" \
"s_waitcnt vmcnt(36)\n" \
"ds_read_b64_tr_b16 a[68:69], v1 offset:28672\n" \
"ds_read_b64_tr_b16 a[70:71], v1 offset:29184\n" \
"ds_read_b64_tr_b16 a[196:197], v1 offset:28704\n" \
"ds_read_b64_tr_b16 a[198:199], v1 offset:29216\n" \
"ds_read_b64_tr_b16 v[196:197], v1 offset:28736\n" \
"ds_read_b64_tr_b16 v[198:199], v1 offset:29248\n" \
"ds_read_b64_tr_b16 v[84:85], v1 offset:28768\n" \
"ds_read_b64_tr_b16 v[86:87], v1 offset:29280\n" \
"s_waitcnt lgkmcnt(0)\n" \
"s_add_u32 m0, s52, 28672\n" \
"s_add_u32 s54, s92, 196608\n" \
"buffer_load_dwordx4 v0, s[40:43], s54 offen nt lds\n" \
"s_add_u32 m0, s52, 29696\n" \
"s_add_u32 s54, s92, 212992\n" \
"buffer_load_dwordx4 v0, s[40:43], s54 offen nt lds\n" \
"s_add_u32 m0, s52, 30720\n" \
"s_add_u32 s54, s92, 229376\n" \
"buffer_load_dwordx4 v0, s[40:43], s54 offen nt lds\n" \
"s_add_u32 m0, s52, 31744\n" \
"s_add_u32 s54, s92, 245760\n" \
"buffer_load_dwordx4 v0, s[40:43], s54 offen nt lds\n" \
"s_mov_b32 s54, s99\n" \
".Lsg2_z27_%=:\n" \
"s_sleep 1\n" \
"s_sub_u32 s54, s54, 1\n" \
"s_cbranch_scc0 .Lsg2_z27_%=\n" \
"s_waitcnt vmcnt(36)\n" \
"ds_read_b64_tr_b16 a[72:73], v9 offset:32768\n" \
"ds_read_b64_tr_b16 a[74:75], v9 offset:33280\n" \
"ds_read_b64_tr_b16 a[200:201], v9 offset:32800\n" \
"ds_read_b64_tr_b16 a[202:203], v9 offset:33312\n" \
"ds_read_b64_tr_b16 v[200:201], v9 offset:32832\n" \
"ds_read_b64_tr_b16 v[202:203], v9 offset:33344\n" \
"ds_read_b64_tr_b16 v[88:89], v9 offset:32864\n" \
"ds_read_b64_tr_b16 v[90:91], v9 offset:33376\n" \
"s_waitcnt lgkmcnt(0)\n" \
"s_waitcnt vmcnt(32)\n" \
"ds_read_b64_tr_b16 a[76:77], v9 offset:36864\n" \
"ds_read_b64_tr_b16 a[78:79], v9 offset:37376\n" \
"ds_read_b64_tr_b16 a[204:205], v9 offset:36896\n" \
"ds_read_b64_tr_b16 a[206:207], v9 offset:37408\n" \
"ds_read_b64_tr_b16 v[204:205], v9 offset:36928\n" \
"ds_read_b64_tr_b16 v[206:207], v9 offset:37440\n" \
"ds_read_b64_tr_b16 v[92:93], v9 offset:36960\n" \
"ds_read_b64_tr_b16 v[94:95], v9 offset:37472\n" \
"s_waitcnt lgkmcnt(0)\n" \
"s_waitcnt vmcnt(28)\n" \
"ds_read_b64_tr_b16 a[80:81], v1 offset:0\n" \
"ds_read_b64_tr_b16 a[82:83], v1 offset:512\n" \
"ds_read_b64_tr_b16 a[208:209], v1 offset:32\n" \
"ds_read_b64_tr_b16 a[210:211], v1 offset:544\n" \
"ds_read_b64_tr_b16 v[208:209], v1 offset:64\n" \
"ds_read_b64_tr_b16 v[210:211], v1 offset:576\n" \
"ds_read_b64_tr_b16 v[96:97], v1 offset:96\n" \
"ds_read_b64_tr_b16 v[98:99], v1 offset:608\n" \
"s_waitcnt lgkmcnt(0)\n" \
"ds_write_b128 v2, v[16:19] offset:0\n" \
"ds_write_b128 v2, v[20:23] offset:1024\n" \
"ds_write_b128 v2, v[24:27] offset:2048\n" \
"ds_write_b128 v2, v[28:31] offset:3072\n" \
"s_waitcnt vmcnt(24)\n" \
"ds_read_b64_tr_b16 a[84:85], v1 offset:4096\n" \
"ds_read_b64_tr_b16 a[86:87], v1 offset:4608\n" \
"ds_read_b64_tr_b16 a[212:213], v1 offset:4128\n" \
"ds_read_b64_tr_b16 a[214:215], v1 offset:4640\n" \
"ds_read_b64_tr_b16 v[212:213], v1 offset:4160\n" \
"ds_read_b64_tr_b16 v[214:215], v1 offset:4672\n" \
"ds_read_b64_tr_b16 v[100:101], v1 offset:4192\n" \
"ds_read_b64_tr_b16 v[102:103], v1 offset:4704\n" \
"s_waitcnt lgkmcnt(0)\n" \
"ds_write_b128 v2, v[32:35] offset:4096\n" \
"ds_write_b128 v2, v[36:39] offset:5120\n" \
"ds_write_b128 v2, v[40:43] offset:6144\n" \
"ds_write_b128 v2, v[44:47] offset:7168\n" \
"s_waitcnt vmcnt(20)\n" \
"ds_read_b64_tr_b16 a[88:89], v1 offset:8192\n" \
"ds_read_b64_tr_b16 a[90:91], v1 offset:8704\n" \
"ds_read_b64_tr_b16 a[216:217], v1 offset:8224\n" \
"ds_read_b64_tr_b16 a[218:219], v1 offset:8736\n" \
"ds_read_b64_tr_b16 v[216:217], v1 offset:8256\n" \
"ds_read_b64_tr_b16 v[218:219], v1 offset:8768\n" \
"ds_read_b64_tr_b16 v[104:105], v1 offset:8288\n" \
"ds_read_b64_tr_b16 v[106:107], v1 offset:8800\n" \
"s_waitcnt lgkmcnt(0)\n" \
"ds_write_b128 v2, v[48:51] offset:8192\n" \
"ds_write_b128 v2, v[52:55] offset:9216\n" \
"ds_write_b128 v2, v[56:59] offset:10240\n" \
"ds_write_b128 v2, v[60:63] offset:11264\n" \
"s_waitcnt vmcnt(16)\n" \
"ds_read_b64_tr_b16 a[92:93], v1 offset:12288\n" \
"ds_read_b64_tr_b16 a[94:95], v1 offset:12800\n" \
"ds_read_b64_tr_b16 a[220:221], v1 offset:12320\n" \
"ds_read_b64_tr_b16 a[222:223], v1 offset:12832\n" \
"ds_read_b64_tr_b16 v[220:221], v1 offset:12352\n" \
"ds_read_b64_tr_b16 v[222:223], v1 offset:12864\n" \
"ds_read_b64_tr_b16 v[108:109], v1 offset:12384\n" \
"ds_read_b64_tr_b16 v[110:111], v1 offset:12896\n" \
"s_waitcnt lgkmcnt(0)\n" \
"ds_write_b128 v2, v[64:67] offset:12288\n" \
"ds_write_b128 v2, v[68:71] offset:13312\n" \
"ds_write_b128 v2, v[72:75] offset:14336\n" \
"ds_write_b128 v2, v[76:79] offset:15360\n" \
"s_waitcnt vmcnt(12)\n" \
"ds_read_b64_tr_b16 a[96:97], v1 offset:16384\n" \
"ds_read_b64_tr_b16 a[98:99], v1 offset:16896\n" \
"ds_read_b64_tr_b16 a[224:225], v1 offset:16416\n" \
"ds_read_b64_tr_b16 a[226:227], v1 offset:16928\n" \
"ds_read_b64_tr_b16 v[224:225], v1 offset:16448\n" \
"ds_read_b64_tr_b16 v[226:227], v1 offset:16960\n" \
"ds_read_b64_tr_b16 v[16:17], v1 offset:16480\n" \
"ds_read_b64_tr_b16 v[18:19], v1 offset:16992\n" \
"s_waitcnt lgkmcnt(0)\n" \
"ds_write_b128 v2, v[80:83] offset:16384\n" \
"ds_write_b128 v2, v[84:87] offset:17408\n" \
"ds_write_b128 v2, v[88:91] offset:18432\n" \
"ds_write_b128 v2, v[92:95] offset:19456\n" \
"s_waitcnt vmcnt(8)\n" \
"ds_read_b64_tr_b16 a[100:101], v1 offset:20480\n" \
"ds_read_b64_tr_b16 a[102:103], v1 offset:20992\n" \
"ds_read_b64_tr_b16 a[228:229], v1 offset:20512\n" \
"ds_read_b64_tr_b16 a[230:231], v1 offset:21024\n" \
"ds_read_b64_tr_b16 v[228:229], v1 offset:20544\n" \
"ds_read_b64_tr_b16 v[230:231], v1 offset:21056\n" \
"ds_read_b64_tr_b16 v[20:21], v1 offset:20576\n" \
"ds_read_b64_tr_b16 v[22:23], v1 offset:21088\n" \
"s_waitcnt lgkmcnt(0)\n" \
"ds_write_b128 v2, v[96:99] offset:20480\n" \
"ds_write_b128 v2, v[100:103] offset:21504\n" \
"ds_write_b128 v2, v[104:107] offset:22528\n" \
"ds_write_b128 v2, v[108:111] offset:23552\n" \
"s_waitcnt vmcnt(4)\n" \
"ds_read_b64_tr_b16 a[104:105], v1 offset:24576\n" \
"ds_read_b64_tr_b16 a[106:107], v1 offset:25088\n" \
"ds_read_b64_tr_b16 a[232:233], v1 offset:24608\n" \
"ds_read_b64_tr_b16 a[234:235], v1 offset:25120\n" \
"ds_read_b64_tr_b16 v[232:233], v1 offset:24640\n" \
"ds_read_b64_tr_b16 v[234:235], v1 offset:25152\n" \
"ds_read_b64_tr_b16 v[24:25], v1 offset:24672\n" \
"ds_read_b64_tr_b16 v[26:27], v1 offset:25184\n" \
"s_waitcnt lgkmcnt(0)\n" \
"ds_write_b128 v2, v[16:19] offset:24576\n" \
"ds_write_b128 v2, v[20:23] offset:25600\n" \
"ds_write_b128 v2, v[24:27] offset:26624\n" \
"s_waitcnt vmcnt(0)\n" \
"ds_read_b64_tr_b16 a[108:109], v1 offset:28672\n" \
"ds_read_b64_tr_b16 a[110:111], v1 offset:29184\n" \
"ds_read_b64_tr_b16 a[236:237], v1 offset:28704\n" \
"ds_read_b64_tr_b16 a[238:239], v1 offset:29216\n" \
"ds_read_b64_tr_b16 v[236:237], v1 offset:28736\n" \
"ds_read_b64_tr_b16 v[238:239], v1 offset:29248\n" \
"ds_read_b64_tr_b16 v[28:29], v1 offset:28768\n" \
"ds_read_b64_tr_b16 v[30:31], v1 offset:29280\n" \
"s_waitcnt lgkmcnt(0)\n" \
"ds_write_b128 v2, v[28:31] offset:28672\n" \
"s_waitcnt vmcnt(0) lgkmcnt(0)\n" \
"s_barrier\n" \
"s_mov_b32 s54, s85\n" \
"s_mul_i32 s86, s54, 4096\n" \
"s_add_u32 s54, s54, 1\n" \
"s_cmp_eq_u32 s54, 7\n" \
"s_cselect_b32 s54, 0, s54\n" \
"s_mul_i32 s87, s54, 4096\n" \
"s_add_u32 s54, s54, 1\n" \
"s_cmp_eq_u32 s54, 7\n" \
"s_cselect_b32 s54, 0, s54\n" \
"s_mul_i32 s88, s54, 4096\n" \
"s_add_u32 s54, s54, 1\n" \
"s_cmp_eq_u32 s54, 7\n" \
"s_cselect_b32 s54, 0, s54\n" \
"s_mul_i32 s89, s54, 4096\n" \
"s_add_u32 s54, s54, 1\n" \
"s_cmp_eq_u32 s54, 7\n" \
"s_cselect_b32 s54, 0, s54\n" \
"s_mul_i32 s90, s54, 4096\n" \
"s_add_u32 s54, s54, 1\n" \
"s_cmp_eq_u32 s54, 7\n" \
"s_cselect_b32 s54, 0, s54\n" \
"s_mul_i32 s91, s54, 4096\n" \
"s_add_u32 s54, s54, 1\n" \
"s_cmp_eq_u32 s54, 7\n" \
"s_cselect_b32 s54, 0, s54\n" \
"s_mul_i32 s92, s54, 4096\n" \
"s_add_u32 s54, s54, 1\n" \
"s_cmp_eq_u32 s54, 7\n" \
"s_cselect_b32 s54, 0, s54\n" \
"s_mov_b32 s55, 0\n" \
"s_mov_b32 s60, 0\n" \
".Lsg2_prime_%=:\n" \
"s_lshr_b32 s59, s44, 1\n" \
"s_add_u32 s59, s59, 1\n" \
"s_mul_i32 s56, s44, 114688\n" \
"buffer_load_dword v11, v4, s[36:39], 0 offen sc1\n" \
"s_waitcnt vmcnt(0)\n" \
"v_cmp_gt_u32_e64 s[62:63], s59, v11\n" \
"s_nop 4\n" \
"s_cmp_lg_u64 s[62:63], 0\n" \
"s_cbranch_scc1 .Lsg2_w_%=\n" \
".Lsg2_wk_%=:\n" \
"s_mov_b32 s78, 0x70000000\n" \
"s_mov_b32 s79, 0x70000000\n" \
"s_mov_b32 s81, 1\n" \
"s_mov_b32 s84, 0\n" \
"v_mov_b32 v10, 0\n" \
"s_waitcnt vmcnt(0) lgkmcnt(0)\n" \
"s_add_u32 s57, s56, s86\n" \
"buffer_load_dwordx4 v[16:19], v3, s[36:39], s57 offen offset:0 sc1\n" \
"buffer_load_dwordx4 v[20:23], v3, s[36:39], s57 offen offset:1024 sc1\n" \
"buffer_load_dwordx4 v[24:27], v3, s[36:39], s57 offen offset:2048 sc1\n" \
"buffer_load_dwordx4 v[28:31], v3, s[36:39], s57 offen offset:3072 sc1\n" \
"s_add_u32 s57, s56, s87\n" \
"buffer_load_dwordx4 v[32:35], v3, s[36:39], s57 offen offset:0 sc1\n" \
"buffer_load_dwordx4 v[36:39], v3, s[36:39], s57 offen offset:1024 sc1\n" \
"buffer_load_dwordx4 v[40:43], v3, s[36:39], s57 offen offset:2048 sc1\n" \
"buffer_load_dwordx4 v[44:47], v3, s[36:39], s57 offen offset:3072 sc1\n" \
"s_add_u32 s57, s56, s88\n" \
"buffer_load_dwordx4 v[48:51], v3, s[36:39], s57 offen offset:0 sc1\n" \
"buffer_load_dwordx4 v[52:55], v3, s[36:39], s57 offen offset:1024 sc1\n" \
"buffer_load_dwordx4 v[56:59], v3, s[36:39], s57 offen offset:2048 sc1\n" \
"buffer_load_dwordx4 v[60:63], v3, s[36:39], s57 offen offset:3072 sc1\n" \
"s_add_u32 s57, s56, s89\n" \
"buffer_load_dwordx4 v[64:67], v3, s[36:39], s57 offen offset:0 sc1\n" \
"buffer_load_dwordx4 v[68:71], v3, s[36:39], s57 offen offset:1024 sc1\n" \
"ds_read_b128 v[80:83], v2 offset:0\n" \
"ds_read_b128 v[84:87], v2 offset:1024\n" \
"ds_read_b128 v[88:91], v2 offset:2048\n" \
"s_waitcnt lgkmcnt(0)\n" \
"v_mov_b32 v13, 0x7ffffff0\n" \
"buffer_store_dword v13, v13, s[36:39], 0 offen\n" \
"buffer_store_dword v13, v13, s[36:39], 0 offen\n" \
"buffer_store_dword v13, v13, s[36:39], 0 offen\n" \
"s_waitcnt vmcnt(14)\n" \
"s_nop 7\n" \
"s_nop 7\n" \
".p2align 6\n" \
".Lsg2_loop_%=:\n" \
"v_mfma_f32_16x16x32_bf16 v[96:99], a[0:3], v[16:19], 0\n" \
"ds_write_b128 v7, v[104:107] offset:2048\n" \
"v_mfma_f32_16x16x32_bf16 v[100:103], a[128:131], v[16:19], 0\n" \
"ds_write_b128 v7, v[108:111] offset:3072\n" \
"ds_read_b128 v[92:95], v2 offset:3072\n" \
"v_mfma_f32_16x16x32_bf16 v[104:107], v[128:131], v[16:19], 0\n" \
"s_waitcnt lgkmcnt(7)\n" \
"v_mfma_f32_16x16x32_bf16 v[108:111], v[80:83], v[16:19], 0\n" \
"buffer_load_dwordx4 v[16:19], v3, s[36:39], s57 offen offset:2048 sc1\n" \
"v_mfma_f32_16x16x32_bf16 v[96:99], a[4:7], v[20:23], v[96:99]\n" \
"v_readfirstlane_b32 s96, v9\n" \
"v_mfma_f32_16x16x32_bf16 v[100:103], a[132:135], v[20:23], v[100:103]\n" \
"ds_read_b128 v[80:83], v2 offset:4096\n" \
"v_mfma_f32_16x16x32_bf16 v[104:107], v[132:135], v[20:23], v[104:107]\n" \
"s_waitcnt lgkmcnt(7)\n" \
"v_mfma_f32_16x16x32_bf16 v[108:111], v[84:87], v[20:23], v[108:111]\n" \
"buffer_load_dwordx4 v[20:23], v3, s[36:39], s57 offen offset:3072 sc1\n" \
"v_mfma_f32_16x16x32_bf16 v[96:99], a[8:11], v[24:27], v[96:99]\n" \
"s_waitcnt lgkmcnt(2)\n" \
"s_cmp_lg_u32 s81, 0\n" \
"s_cbranch_scc1 .Lsg2_nb_%=\n" \
"s_barrier\n" \
".Lsg2_nb_%=:\n" \
"v_mfma_f32_16x16x32_bf16 v[100:103], a[136:139], v[24:27], v[100:103]\n" \
"ds_read_b128 v[84:87], v2 offset:5120\n" \
"v_xor_b32 v7, s84, v7\n" \
"v_mfma_f32_16x16x32_bf16 v[104:107], v[136:139], v[24:27], v[104:107]\n" \
"s_waitcnt vmcnt(15)\n" \
"v_mfma_f32_16x16x32_bf16 v[108:111], v[88:91], v[24:27], v[108:111]\n" \
"s_add_u32 s57, s56, s90\n" \
"buffer_load_dwordx4 v[24:27], v3, s[36:39], s57 offen offset:0 sc1\n" \
"v_mfma_f32_16x16x32_bf16 v[96:99], a[12:15], v[28:31], v[96:99]\n" \
"s_add_u32 s76, s78, 4194304\n" \
"s_add_u32 s80, s78, 0\n" \
"s_and_b32 s96, s96, 1\n" \
"v_mfma_f32_16x16x32_bf16 v[100:103], a[140:143], v[28:31], v[100:103]\n" \
"ds_read_b128 v[88:91], v2 offset:6144\n" \
"s_cselect_b32 s76, 0x7ffffff0, s76\n" \
"s_cselect_b32 s77, s78, 0x7ffffff0\n" \
"v_mfma_f32_16x16x32_bf16 v[104:107], v[140:143], v[28:31], v[104:107]\n" \
"s_waitcnt vmcnt(15) lgkmcnt(3)\n" \
"s_cselect_b32 s80, s80, 0x7ffffff0\n" \
"s_cselect_b32 s94, 0x7ffffff0, s79\n" \
"v_mfma_f32_16x16x32_bf16 v[108:111], v[92:95], v[28:31], v[108:111]\n" \
"buffer_load_dwordx4 v[28:31], v3, s[36:39], s57 offen offset:1024 sc1\n" \
"v_mfma_f32_16x16x32_bf16 v[96:99], a[16:19], v[32:35], v[96:99]\n" \
"s_cmp_lt_i32 s44, s50\n" \
"s_cselect_b32 s82, s45, s47\n" \
"v_mfma_f32_16x16x32_bf16 v[100:103], a[144:147], v[32:35], v[100:103]\n" \
"ds_read_b128 v[92:95], v2 offset:7168\n" \
"s_add_u32 s82, s82, s44\n" \
"v_mfma_f32_16x16x32_bf16 v[104:107], v[144:147], v[32:35], v[104:107]\n" \
"s_waitcnt vmcnt(15) lgkmcnt(3)\n" \
"s_lshr_b32 s59, s82, 1\n" \
"s_add_u32 s59, s59, 1\n" \
"v_mfma_f32_16x16x32_bf16 v[108:111], v[80:83], v[32:35], v[108:111]\n" \
"buffer_load_dwordx4 v[32:35], v3, s[36:39], s57 offen offset:2048 sc1\n" \
"v_mfma_f32_16x16x32_bf16 v[96:99], a[20:23], v[36:39], v[96:99]\n" \
"s_waitcnt vmcnt(7)\n" \
"v_readfirstlane_b32 s54, v10\n" \
"v_mfma_f32_16x16x32_bf16 v[100:103], a[148:151], v[36:39], v[100:103]\n" \
"ds_read_b128 v[80:83], v2 offset:8192\n" \
"v_mfma_f32_16x16x32_bf16 v[104:107], v[148:151], v[36:39], v[104:107]\n" \
"s_waitcnt lgkmcnt(3)\n" \
"s_and_b32 s54, s54, 1\n" \
"s_cbranch_scc1 .Lsg2_p_%=\n" \
".Lsg2_pk_%=:\n" \
"v_mfma_f32_16x16x32_bf16 v[108:111], v[84:87], v[36:39], v[108:111]\n" \
"buffer_load_dwordx4 v[36:39], v3, s[36:39], s57 offen offset:3072 sc1\n" \
"v_mfma_f32_16x16x32_bf16 v[96:99], a[24:27], v[40:43], v[96:99]\n" \
"buffer_load_dwordx2 v[14:15], v6, s[36:39], s80 offen sc1\n" \
"ds_read_b128 v[112:115], v8 offset:0\n" \
"v_mfma_f32_16x16x32_bf16 v[100:103], a[152:155], v[40:43], v[100:103]\n" \
"ds_read_b128 v[84:87], v2 offset:9216\n" \
"v_mfma_f32_16x16x32_bf16 v[104:107], v[152:155], v[40:43], v[104:107]\n" \
"s_waitcnt lgkmcnt(4)\n" \
"buffer_load_dwordx2 v[12:13], v5, s[64:67], s77 offen\n" \
"ds_read_b128 v[116:119], v8 offset:4096\n" \
"v_mfma_f32_16x16x32_bf16 v[108:111], v[88:91], v[40:43], v[108:111]\n" \
"s_add_u32 s57, s56, s91\n" \
"buffer_load_dwordx4 v[40:43], v3, s[36:39], s57 offen offset:0 sc1\n" \
"v_mfma_f32_16x16x32_bf16 v[96:99], a[28:31], v[44:47], v[96:99]\n" \
"ds_read_b128 v[120:123], v8 offset:8192\n" \
"v_mfma_f32_16x16x32_bf16 v[100:103], a[156:159], v[44:47], v[100:103]\n" \
"ds_read_b128 v[88:91], v2 offset:10240\n" \
"v_mfma_f32_16x16x32_bf16 v[104:107], v[156:159], v[44:47], v[104:107]\n" \
"s_waitcnt lgkmcnt(6)\n" \
"ds_read_b128 v[124:127], v8 offset:12288\n" \
"v_mfma_f32_16x16x32_bf16 v[108:111], v[92:95], v[44:47], v[108:111]\n" \
"buffer_load_dwordx4 v[44:47], v3, s[36:39], s57 offen offset:1024 sc1\n" \
"v_mfma_f32_16x16x32_bf16 v[96:99], a[32:35], v[48:51], v[96:99]\n" \
"v_mfma_f32_16x16x32_bf16 v[100:103], a[160:163], v[48:51], v[100:103]\n" \
"ds_read_b128 v[92:95], v2 offset:11264\n" \
"v_mfma_f32_16x16x32_bf16 v[104:107], v[160:163], v[48:51], v[104:107]\n" \
"s_waitcnt lgkmcnt(7)\n" \
"v_mfma_f32_16x16x32_bf16 v[108:111], v[80:83], v[48:51], v[108:111]\n" \
"buffer_load_dwordx4 v[48:51], v3, s[36:39], s57 offen offset:2048 sc1\n" \
"v_mfma_f32_16x16x32_bf16 v[96:99], a[36:39], v[52:55], v[96:99]\n" \
"s_waitcnt lgkmcnt(4)\n" \
"v_add_f32 v112, v112, v116\n" \
"v_add_f32_e64 v113, v113, v117\n" \
"v_mfma_f32_16x16x32_bf16 v[100:103], a[164:167], v[52:55], v[100:103]\n" \
"ds_read_b128 v[80:83], v2 offset:12288\n" \
"v_add_f32_e64 v114, v114, v118\n" \
"v_mfma_f32_16x16x32_bf16 v[104:107], v[164:167], v[52:55], v[104:107]\n" \
"v_add_f32_e64 v115, v115, v119\n" \
"v_mfma_f32_16x16x32_bf16 v[108:111], v[84:87], v[52:55], v[108:111]\n" \
"buffer_load_dwordx4 v[52:55], v3, s[36:39], s57 offen offset:3072 sc1\n" \
"s_waitcnt lgkmcnt(2)\n" \
"v_add_f32 v120, v120, v124\n" \
"v_mfma_f32_16x16x32_bf16 v[96:99], a[40:43], v[56:59], v[96:99]\n" \
"s_lshl_b32 s97, s44, 8\n" \
"s_add_u32 s97, s97, s49\n" \
"v_add_f32 v121, v121, v125\n" \
"v_add_f32 v122, v122, v126\n" \
"v_mfma_f32_16x16x32_bf16 v[100:103], a[168:171], v[56:59], v[100:103]\n" \
"ds_read_b128 v[84:87], v2 offset:13312\n" \
"v_add_f32_e64 v123, v123, v127\n" \
"v_mfma_f32_16x16x32_bf16 v[104:107], v[168:171], v[56:59], v[104:107]\n" \
"v_add_f32_e64 v112, v112, v120\n" \
"v_mfma_f32_16x16x32_bf16 v[108:111], v[88:91], v[56:59], v[108:111]\n" \
"s_add_u32 s57, s56, s92\n" \
"buffer_load_dwordx4 v[56:59], v3, s[36:39], s57 offen offset:0 sc1\n" \
"v_add_f32 v113, v113, v121\n" \
"v_mfma_f32_16x16x32_bf16 v[96:99], a[44:47], v[60:63], v[96:99]\n" \
"v_cmp_gt_u32_e64 s[62:63], s59, v11\n" \
"v_add_f32 v114, v114, v122\n" \
"v_add_f32 v115, v115, v123\n" \
"v_mfma_f32_16x16x32_bf16 v[100:103], a[172:175], v[60:63], v[100:103]\n" \
"ds_read_b128 v[88:91], v2 offset:14336\n" \
"v_xor_b32_e64 v8, s84, v8\n" \
"v_mfma_f32_16x16x32_bf16 v[104:107], v[172:175], v[60:63], v[104:107]\n" \
"s_waitcnt vmcnt(14) lgkmcnt(3)\n" \
"v_mul_f32 v116, 0xbfb8aa3b, v112\n" \
"v_mfma_f32_16x16x32_bf16 v[108:111], v[92:95], v[60:63], v[108:111]\n" \
"buffer_load_dwordx4 v[60:63], v3, s[36:39], s57 offen offset:1024 sc1\n" \
"v_mul_f32 v117, 0xbfb8aa3b, v113\n" \
"v_mfma_f32_16x16x32_bf16 v[96:99], a[48:51], v[64:67], v[96:99]\n" \
"s_add_u32 s54, s55, 1\n" \
"s_cmp_lt_u32 s54, s46\n" \
"v_mul_f32 v118, 0xbfb8aa3b, v114\n" \
"v_mul_f32 v119, 0xbfb8aa3b, v115\n" \
"v_mfma_f32_16x16x32_bf16 v[100:103], a[176:179], v[64:67], v[100:103]\n" \
"ds_read_b128 v[92:95], v2 offset:15360\n" \
"s_cselect_b64 s[62:63], s[62:63], -1\n" \
"v_exp_f32_e64 v116, v116\n" \
"v_mfma_f32_16x16x32_bf16 v[104:107], v[176:179], v[64:67], v[104:107]\n" \
"s_waitcnt vmcnt(14) lgkmcnt(3)\n" \
"s_mul_i32 s83, s82, 114688\n" \
"v_exp_f32 v117, v117\n" \
"v_mfma_f32_16x16x32_bf16 v[108:111], v[80:83], v[64:67], v[108:111]\n" \
"buffer_load_dwordx4 v[64:67], v3, s[36:39], s57 offen offset:2048 sc1\n" \
"v_exp_f32_e64 v118, v118\n" \
"v_mfma_f32_16x16x32_bf16 v[96:99], a[52:55], v[68:71], v[96:99]\n" \
"s_cmp_lg_u64 s[62:63], 0\n" \
"s_cselect_b32 s81, 1, 0\n" \
"v_exp_f32_e64 v119, v119\n" \
"v_mfma_f32_16x16x32_bf16 v[100:103], a[180:183], v[68:71], v[100:103]\n" \
"ds_read_b128 v[80:83], v2 offset:16384\n" \
"s_cselect_b32 s83, 0x70000000, s83\n" \
"v_add_f32_e64 v116, 1.0, v116\n" \
"v_mfma_f32_16x16x32_bf16 v[104:107], v[180:183], v[68:71], v[104:107]\n" \
"s_waitcnt vmcnt(14) lgkmcnt(3)\n" \
"v_add_f32 v117, 1.0, v117\n" \
"v_mfma_f32_16x16x32_bf16 v[108:111], v[84:87], v[68:71], v[108:111]\n" \
"buffer_load_dwordx4 v[68:71], v3, s[36:39], s57 offen offset:3072 sc1\n" \
"v_add_f32_e64 v118, 1.0, v118\n" \
"v_mfma_f32_16x16x32_bf16 v[96:99], a[56:59], v[16:19], v[96:99]\n" \
"v_add_f32_e64 v119, 1.0, v119\n" \
"v_mfma_f32_16x16x32_bf16 v[100:103], a[184:187], v[16:19], v[100:103]\n" \
"ds_read_b128 v[84:87], v2 offset:17408\n" \
"v_rcp_f32_e64 v116, v116\n" \
"v_mfma_f32_16x16x32_bf16 v[104:107], v[184:187], v[16:19], v[104:107]\n" \
"s_waitcnt vmcnt(14) lgkmcnt(3)\n" \
"s_mov_b32 s56, s83\n" \
"v_rcp_f32_e64 v117, v117\n" \
"v_mfma_f32_16x16x32_bf16 v[108:111], v[88:91], v[16:19], v[108:111]\n" \
"s_add_u32 s57, s56, s86\n" \
"buffer_load_dwordx4 v[16:19], v3, s[36:39], s57 offen offset:0 sc1\n" \
"v_rcp_f32 v118, v118\n" \
"v_mfma_f32_16x16x32_bf16 v[96:99], a[60:63], v[20:23], v[96:99]\n" \
"buffer_load_dword v9, off, s[36:39], s97 sc1\n" \
"v_rcp_f32_e64 v119, v119\n" \
"v_mfma_f32_16x16x32_bf16 v[100:103], a[188:191], v[20:23], v[100:103]\n" \
"ds_read_b128 v[88:91], v2 offset:18432\n" \
"v_mul_f32_e64 v112, v112, v116\n" \
"v_mfma_f32_16x16x32_bf16 v[104:107], v[188:191], v[20:23], v[104:107]\n" \
"s_waitcnt vmcnt(15) lgkmcnt(3)\n" \
"v_mul_f32 v113, v113, v117\n" \
"v_mfma_f32_16x16x32_bf16 v[108:111], v[92:95], v[20:23], v[108:111]\n" \
"buffer_load_dwordx4 v[20:23], v3, s[36:39], s57 offen offset:1024 sc1\n" \
"v_mul_f32_e64 v114, v114, v118\n" \
"v_mfma_f32_16x16x32_bf16 v[96:99], a[64:67], v[24:27], v[96:99]\n" \
"v_mul_f32_e64 v115, v115, v119\n" \
"v_cvt_pk_bf16_f32 v0, v112, v113\n" \
"v_mfma_f32_16x16x32_bf16 v[100:103], a[192:195], v[24:27], v[100:103]\n" \
"ds_read_b128 v[92:95], v2 offset:19456\n" \
"v_cvt_pk_bf16_f32 v1, v114, v115\n" \
"v_mfma_f32_16x16x32_bf16 v[104:107], v[192:195], v[24:27], v[104:107]\n" \
"s_waitcnt vmcnt(15) lgkmcnt(3)\n" \
"v_mfma_f32_16x16x32_bf16 v[108:111], v[80:83], v[24:27], v[108:111]\n" \
"buffer_load_dwordx4 v[24:27], v3, s[36:39], s57 offen offset:2048 sc1\n" \
"buffer_store_dwordx2 v[0:1], v6, s[36:39], s76 offen sc1\n" \
"v_lshlrev_b32 v112, 16, v0\n" \
"v_mfma_f32_16x16x32_bf16 v[96:99], a[68:71], v[28:31], v[96:99]\n" \
"v_and_b32 v113, 0xffff0000, v0\n" \
"v_lshlrev_b32_e64 v114, 16, v1\n" \
"v_mfma_f32_16x16x32_bf16 v[100:103], a[196:199], v[28:31], v[100:103]\n" \
"ds_read_b128 v[80:83], v2 offset:20480\n" \
"v_and_b32 v115, 0xffff0000, v1\n" \
"v_mfma_f32_16x16x32_bf16 v[104:107], v[196:199], v[28:31], v[104:107]\n" \
"s_waitcnt vmcnt(16) lgkmcnt(3)\n" \
"v_mfma_f32_16x16x32_bf16 v[108:111], v[84:87], v[28:31], v[108:111]\n" \
"buffer_load_dwordx4 v[28:31], v3, s[36:39], s57 offen offset:3072 sc1\n" \
"v_mfma_f32_16x16x32_bf16 v[96:99], a[72:75], v[32:35], v[96:99]\n" \
"s_waitcnt vmcnt(15)\n" \
"v_lshlrev_b32_e64 v120, 16, v14\n" \
"v_and_b32 v121, 0xffff0000, v14\n" \
"v_mfma_f32_16x16x32_bf16 v[100:103], a[200:203], v[32:35], v[100:103]\n" \
"ds_read_b128 v[84:87], v2 offset:21504\n" \
"s_waitcnt vmcnt(14)\n" \
"v_lshlrev_b32 v124, 16, v12\n" \
"v_mfma_f32_16x16x32_bf16 v[104:107], v[200:203], v[32:35], v[104:107]\n" \
"s_waitcnt lgkmcnt(3)\n" \
"v_and_b32 v125, 0xffff0000, v12\n" \
"v_mfma_f32_16x16x32_bf16 v[108:111], v[88:91], v[32:35], v[108:111]\n" \
"s_add_u32 s57, s56, s87\n" \
"buffer_load_dwordx4 v[32:35], v3, s[36:39], s57 offen offset:0 sc1\n" \
"v_lshlrev_b32_e64 v122, 16, v15\n" \
"v_mfma_f32_16x16x32_bf16 v[96:99], a[76:79], v[36:39], v[96:99]\n" \
"v_and_b32 v123, 0xffff0000, v15\n" \
"v_lshlrev_b32_e64 v126, 16, v13\n" \
"v_mfma_f32_16x16x32_bf16 v[100:103], a[204:207], v[36:39], v[100:103]\n" \
"ds_read_b128 v[88:91], v2 offset:22528\n" \
"v_and_b32 v127, 0xffff0000, v13\n" \
"v_mfma_f32_16x16x32_bf16 v[104:107], v[204:207], v[36:39], v[104:107]\n" \
"s_waitcnt vmcnt(14) lgkmcnt(3)\n" \
"v_fmac_f32 v124, v112, v120\n" \
"v_mfma_f32_16x16x32_bf16 v[108:111], v[92:95], v[36:39], v[108:111]\n" \
"buffer_load_dwordx4 v[36:39], v3, s[36:39], s57 offen offset:1024 sc1\n" \
"v_fmac_f32_e64 v125, v113, v121\n" \
"v_mfma_f32_16x16x32_bf16 v[96:99], a[80:83], v[40:43], v[96:99]\n" \
"v_fmac_f32 v126, v114, v122\n" \
"v_fmac_f32 v127, v115, v123\n" \
"v_mfma_f32_16x16x32_bf16 v[100:103], a[208:211], v[40:43], v[100:103]\n" \
"ds_read_b128 v[92:95], v2 offset:23552\n" \
"v_cvt_pk_bf16_f32 v118, v124, v125\n" \
"v_mfma_f32_16x16x32_bf16 v[104:107], v[208:211], v[40:43], v[104:107]\n" \
"s_waitcnt vmcnt(14) lgkmcnt(3)\n" \
"v_cvt_pk_bf16_f32 v119, v126, v127\n" \
"v_mfma_f32_16x16x32_bf16 v[108:111], v[80:83], v[40:43], v[108:111]\n" \
"buffer_load_dwordx4 v[40:43], v3, s[36:39], s57 offen offset:2048 sc1\n" \
"v_mfma_f32_16x16x32_bf16 v[96:99], a[84:87], v[44:47], v[96:99]\n" \
"buffer_store_dwordx2 v[118:119], v5, s[68:71], s77 offen\n" \
"v_mov_b32 v10, 2\n" \
"v_mfma_f32_16x16x32_bf16 v[100:103], a[212:215], v[44:47], v[100:103]\n" \
"ds_read_b128 v[80:83], v2 offset:24576\n" \
"v_mfma_f32_16x16x32_bf16 v[104:107], v[212:215], v[44:47], v[104:107]\n" \
"s_waitcnt vmcnt(15) lgkmcnt(3)\n" \
"v_mfma_f32_16x16x32_bf16 v[108:111], v[84:87], v[44:47], v[108:111]\n" \
"buffer_load_dwordx4 v[44:47], v3, s[36:39], s57 offen offset:3072 sc1\n" \
"v_mfma_f32_16x16x32_bf16 v[96:99], a[88:91], v[48:51], v[96:99]\n" \
"v_mfma_f32_16x16x32_bf16 v[100:103], a[216:219], v[48:51], v[100:103]\n" \
"ds_read_b128 v[84:87], v2 offset:25600\n" \
"v_mfma_f32_16x16x32_bf16 v[104:107], v[216:219], v[48:51], v[104:107]\n" \
"s_waitcnt vmcnt(15) lgkmcnt(3)\n" \
"v_mfma_f32_16x16x32_bf16 v[108:111], v[88:91], v[48:51], v[108:111]\n" \
"s_add_u32 s57, s56, s88\n" \
"buffer_load_dwordx4 v[48:51], v3, s[36:39], s57 offen offset:0 sc1\n" \
"v_mfma_f32_16x16x32_bf16 v[96:99], a[92:95], v[52:55], v[96:99]\n" \
"v_mfma_f32_16x16x32_bf16 v[100:103], a[220:223], v[52:55], v[100:103]\n" \
"ds_read_b128 v[88:91], v2 offset:26624\n" \
"v_mfma_f32_16x16x32_bf16 v[104:107], v[220:223], v[52:55], v[104:107]\n" \
"s_waitcnt vmcnt(15) lgkmcnt(3)\n" \
"v_mfma_f32_16x16x32_bf16 v[108:111], v[92:95], v[52:55], v[108:111]\n" \
"buffer_load_dwordx4 v[52:55], v3, s[36:39], s57 offen offset:1024 sc1\n" \
"v_mfma_f32_16x16x32_bf16 v[96:99], a[96:99], v[56:59], v[96:99]\n" \
"v_mfma_f32_16x16x32_bf16 v[100:103], a[224:227], v[56:59], v[100:103]\n" \
"ds_read_b128 v[92:95], v2 offset:28672\n" \
"v_mfma_f32_16x16x32_bf16 v[104:107], v[224:227], v[56:59], v[104:107]\n" \
"s_waitcnt vmcnt(15) lgkmcnt(3)\n" \
"s_mov_b32 s84, 0x4000\n" \
"v_mfma_f32_16x16x32_bf16 v[108:111], v[80:83], v[56:59], v[108:111]\n" \
"buffer_load_dwordx4 v[56:59], v3, s[36:39], s57 offen offset:2048 sc1\n" \
"v_mfma_f32_16x16x32_bf16 v[96:99], a[100:103], v[60:63], v[96:99]\n" \
"buffer_load_dword v11, v4, s[36:39], 0 offen sc1\n" \
"v_mfma_f32_16x16x32_bf16 v[100:103], a[228:231], v[60:63], v[100:103]\n" \
"ds_read_b128 v[80:83], v2 offset:0\n" \
"v_mfma_f32_16x16x32_bf16 v[104:107], v[228:231], v[60:63], v[104:107]\n" \
"s_waitcnt vmcnt(16) lgkmcnt(3)\n" \
"v_mfma_f32_16x16x32_bf16 v[108:111], v[84:87], v[60:63], v[108:111]\n" \
"buffer_load_dwordx4 v[60:63], v3, s[36:39], s57 offen offset:3072 sc1\n" \
"v_mfma_f32_16x16x32_bf16 v[96:99], a[104:107], v[64:67], v[96:99]\n" \
"v_mfma_f32_16x16x32_bf16 v[100:103], a[232:235], v[64:67], v[100:103]\n" \
"ds_read_b128 v[84:87], v2 offset:1024\n" \
"v_mfma_f32_16x16x32_bf16 v[104:107], v[232:235], v[64:67], v[104:107]\n" \
"s_waitcnt vmcnt(16) lgkmcnt(3)\n" \
"s_waitcnt vmcnt(11)\n" \
"s_mov_b64 exec, 1\n" \
"buffer_atomic_or v10, off, s[36:39], s94 sc0\n" \
"s_mov_b64 exec, -1\n" \
"s_mov_b32 s95, s78\n" \
"v_mfma_f32_16x16x32_bf16 v[108:111], v[88:91], v[64:67], v[108:111]\n" \
"s_add_u32 s57, s56, s89\n" \
"buffer_load_dwordx4 v[64:67], v3, s[36:39], s57 offen offset:0 sc1\n" \
"v_mfma_f32_16x16x32_bf16 v[96:99], a[108:111], v[68:71], v[96:99]\n" \
"s_lshl_b32 s78, s44, 15\n" \
"s_mov_b32 s79, s97\n" \
"v_mfma_f32_16x16x32_bf16 v[100:103], a[236:239], v[68:71], v[100:103]\n" \
"ds_read_b128 v[88:91], v2 offset:2048\n" \
"s_mov_b32 s44, s82\n" \
"s_add_u32 s55, s55, 1\n" \
"v_mfma_f32_16x16x32_bf16 v[104:107], v[236:239], v[68:71], v[104:107]\n" \
"s_waitcnt lgkmcnt(3)\n" \
"ds_write_b128 v7, v[96:99] offset:0\n" \
"v_mfma_f32_16x16x32_bf16 v[108:111], v[92:95], v[68:71], v[108:111]\n" \
"buffer_load_dwordx4 v[68:71], v3, s[36:39], s57 offen offset:1024 sc1\n" \
"ds_write_b128 v7, v[100:103] offset:1024\n" \
"s_cmp_eq_u32 s81, 0\n" \
"s_cbranch_scc1 .Lsg2_loop_%=\n" \
"s_nop 7\n" \
"s_nop 7\n" \
"s_waitcnt vmcnt(0) lgkmcnt(0)\n" \
"ds_write_b128 v7, v[104:107] offset:2048\n" \
"ds_write_b128 v7, v[108:111] offset:3072\n" \
"s_waitcnt lgkmcnt(0)\n" \
"s_barrier\n" \
"buffer_load_dword v9, off, s[36:39], s97 sc1\n" \
"v_readfirstlane_b32 s54, v10\n" \
"s_nop 0\n" \
"s_and_b32 s54, s54, 1\n" \
"s_cbranch_scc1 .Lsg2_q_%=\n" \
".Lsg2_qk_%=:\n" \
"s_waitcnt vmcnt(0)\n" \
"v_readfirstlane_b32 s96, v9\n" \
"s_add_u32 s76, s78, 4194304\n" \
"s_add_u32 s80, s78, 0\n" \
"s_and_b32 s96, s96, 1\n" \
"s_cselect_b32 s76, 0x7ffffff0, s76\n" \
"s_cselect_b32 s77, s78, 0x7ffffff0\n" \
"s_cselect_b32 s80, s80, 0x7ffffff0\n" \
"s_cselect_b32 s94, 0x7ffffff0, s79\n" \
"buffer_load_dwordx2 v[14:15], v6, s[36:39], s80 offen sc1\n" \
"buffer_load_dwordx2 v[12:13], v5, s[64:67], s77 offen\n" \
"ds_read_b128 v[112:115], v8 offset:0\n" \
"ds_read_b128 v[116:119], v8 offset:4096\n" \
"ds_read_b128 v[120:123], v8 offset:8192\n" \
"ds_read_b128 v[124:127], v8 offset:12288\n" \
"s_waitcnt lgkmcnt(2)\n" \
"v_add_f32 v112, v112, v116\n" \
"v_add_f32 v113, v113, v117\n" \
"v_add_f32 v114, v114, v118\n" \
"v_add_f32 v115, v115, v119\n" \
"s_waitcnt lgkmcnt(0)\n" \
"v_add_f32 v120, v120, v124\n" \
"v_add_f32 v121, v121, v125\n" \
"v_add_f32 v122, v122, v126\n" \
"v_add_f32 v123, v123, v127\n" \
"v_add_f32 v112, v112, v120\n" \
"v_add_f32 v113, v113, v121\n" \
"v_add_f32 v114, v114, v122\n" \
"v_add_f32 v115, v115, v123\n" \
"v_xor_b32 v8, s84, v8\n" \
"v_mul_f32 v116, 0xbfb8aa3b, v112\n" \
"v_mul_f32 v117, 0xbfb8aa3b, v113\n" \
"v_mul_f32 v118, 0xbfb8aa3b, v114\n" \
"v_mul_f32 v119, 0xbfb8aa3b, v115\n" \
"v_exp_f32 v116, v116\n" \
"v_exp_f32 v117, v117\n" \
"v_exp_f32 v118, v118\n" \
"v_exp_f32 v119, v119\n" \
"v_add_f32 v116, 1.0, v116\n" \
"v_add_f32 v117, 1.0, v117\n" \
"v_add_f32 v118, 1.0, v118\n" \
"v_add_f32 v119, 1.0, v119\n" \
"v_rcp_f32 v116, v116\n" \
"v_rcp_f32 v117, v117\n" \
"v_rcp_f32 v118, v118\n" \
"v_rcp_f32 v119, v119\n" \
"v_mul_f32 v112, v112, v116\n" \
"v_mul_f32 v113, v113, v117\n" \
"v_mul_f32 v114, v114, v118\n" \
"v_mul_f32 v115, v115, v119\n" \
"v_cvt_pk_bf16_f32 v0, v112, v113\n" \
"v_cvt_pk_bf16_f32 v1, v114, v115\n" \
"buffer_store_dwordx2 v[0:1], v6, s[36:39], s76 offen sc1\n" \
"v_lshlrev_b32 v112, 16, v0\n" \
"v_and_b32 v113, 0xffff0000, v0\n" \
"v_lshlrev_b32 v114, 16, v1\n" \
"v_and_b32 v115, 0xffff0000, v1\n" \
"s_waitcnt vmcnt(2)\n" \
"v_lshlrev_b32 v120, 16, v14\n" \
"v_and_b32 v121, 0xffff0000, v14\n" \
"s_waitcnt vmcnt(1)\n" \
"v_lshlrev_b32 v124, 16, v12\n" \
"v_and_b32 v125, 0xffff0000, v12\n" \
"v_lshlrev_b32 v122, 16, v15\n" \
"v_and_b32 v123, 0xffff0000, v15\n" \
"v_lshlrev_b32 v126, 16, v13\n" \
"v_and_b32 v127, 0xffff0000, v13\n" \
"v_fmac_f32 v124, v112, v120\n" \
"v_fmac_f32 v125, v113, v121\n" \
"v_fmac_f32 v126, v114, v122\n" \
"v_fmac_f32 v127, v115, v123\n" \
"v_cvt_pk_bf16_f32 v118, v124, v125\n" \
"v_cvt_pk_bf16_f32 v119, v126, v127\n" \
"buffer_store_dwordx2 v[118:119], v5, s[68:71], s77 offen\n" \
"v_mov_b32 v10, 2\n" \
"s_waitcnt vmcnt(1)\n" \
"s_mov_b64 exec, 1\n" \
"buffer_atomic_or v10, off, s[36:39], s94 sc0\n" \
"s_mov_b64 exec, -1\n" \
"s_mov_b32 s95, s78\n" \
"s_waitcnt vmcnt(0)\n" \
"v_readfirstlane_b32 s54, v10\n" \
"s_nop 0\n" \
"s_and_b32 s54, s54, 1\n" \
"s_cbranch_scc1 .Lsg2_r_%=\n" \
".Lsg2_rk_%=:\n" \
"v_xor_b32 v7, 0x4000, v7\n" \
"s_waitcnt vmcnt(0) lgkmcnt(0)\n" \
"s_cmp_lt_u32 s55, s46\n" \
"s_cbranch_scc1 .Lsg2_prime_%=\n" \
"s_branch .Lsg2_end_%=\n" \
".Lsg2_w_%=:\n" \
"s_nop 7\n" \
"s_nop 7\n" \
"s_waitcnt vmcnt(0) lgkmcnt(0)\n" \
"s_mov_b32 s61, 0\n" \
".Lsg2_wa_%=:\n" \
"s_cmp_lg_u32 s60, 0\n" \
"s_cbranch_scc1 .Lsg2_wk_%=\n" \
"buffer_load_dword v11, v4, s[36:39], 0 offen sc1\n" \
"s_waitcnt vmcnt(0)\n" \
"v_cmp_gt_u32_e64 s[62:63], s59, v11\n" \
"s_nop 4\n" \
"s_cmp_eq_u64 s[62:63], 0\n" \
"s_cbranch_scc1 .Lsg2_wk_%=\n" \
"s_sleep 2\n" \
"s_add_u32 s61, s61, 1\n" \
"s_cmp_lt_u32 s61, 65536\n" \
"s_cbranch_scc1 .Lsg2_wa_%=\n" \
"s_mov_b32 s60, 1\n" \
"v_mov_b32 v11, 256\n" \
"v_mov_b32 v12, 1\n" \
"buffer_store_dword v12, v11, s[36:39], 0 offen sc1\n" \
"s_waitcnt vmcnt(0) lgkmcnt(0)\n" \
"s_branch .Lsg2_wk_%=\n" \
".Lsg2_p_%=:\n" \
"s_nop 7\n" \
"s_nop 7\n" \
"s_waitcnt vmcnt(0) lgkmcnt(0)\n" \
"s_add_u32 s54, s95, 0\n" \
"buffer_load_dwordx2 v[14:15], v6, s[36:39], s54 offen sc1\n" \
"buffer_load_dwordx2 v[12:13], v5, s[64:67], s95 offen\n" \
"v_lshlrev_b32 v112, 16, v0\n" \
"v_and_b32 v113, 0xffff0000, v0\n" \
"v_lshlrev_b32 v114, 16, v1\n" \
"v_and_b32 v115, 0xffff0000, v1\n" \
"s_waitcnt vmcnt(1)\n" \
"v_lshlrev_b32 v120, 16, v14\n" \
"v_and_b32 v121, 0xffff0000, v14\n" \
"s_waitcnt vmcnt(0)\n" \
"v_lshlrev_b32 v124, 16, v12\n" \
"v_and_b32 v125, 0xffff0000, v12\n" \
"v_lshlrev_b32 v122, 16, v15\n" \
"v_and_b32 v123, 0xffff0000, v15\n" \
"v_lshlrev_b32 v126, 16, v13\n" \
"v_and_b32 v127, 0xffff0000, v13\n" \
"v_fmac_f32 v124, v112, v120\n" \
"v_fmac_f32 v125, v113, v121\n" \
"v_fmac_f32 v126, v114, v122\n" \
"v_fmac_f32 v127, v115, v123\n" \
"v_cvt_pk_bf16_f32 v118, v124, v125\n" \
"v_cvt_pk_bf16_f32 v119, v126, v127\n" \
"buffer_store_dwordx2 v[118:119], v5, s[68:71], s95 offen\n" \
"s_waitcnt vmcnt(0) lgkmcnt(0)\n" \
"s_branch .Lsg2_pk_%=\n" \
".Lsg2_q_%=:\n" \
"s_nop 7\n" \
"s_nop 7\n" \
"s_waitcnt vmcnt(0) lgkmcnt(0)\n" \
"s_add_u32 s54, s95, 0\n" \
"buffer_load_dwordx2 v[14:15], v6, s[36:39], s54 offen sc1\n" \
"buffer_load_dwordx2 v[12:13], v5, s[64:67], s95 offen\n" \
"v_lshlrev_b32 v112, 16, v0\n" \
"v_and_b32 v113, 0xffff0000, v0\n" \
"v_lshlrev_b32 v114, 16, v1\n" \
"v_and_b32 v115, 0xffff0000, v1\n" \
"s_waitcnt vmcnt(1)\n" \
"v_lshlrev_b32 v120, 16, v14\n" \
"v_and_b32 v121, 0xffff0000, v14\n" \
"s_waitcnt vmcnt(0)\n" \
"v_lshlrev_b32 v124, 16, v12\n" \
"v_and_b32 v125, 0xffff0000, v12\n" \
"v_lshlrev_b32 v122, 16, v15\n" \
"v_and_b32 v123, 0xffff0000, v15\n" \
"v_lshlrev_b32 v126, 16, v13\n" \
"v_and_b32 v127, 0xffff0000, v13\n" \
"v_fmac_f32 v124, v112, v120\n" \
"v_fmac_f32 v125, v113, v121\n" \
"v_fmac_f32 v126, v114, v122\n" \
"v_fmac_f32 v127, v115, v123\n" \
"v_cvt_pk_bf16_f32 v118, v124, v125\n" \
"v_cvt_pk_bf16_f32 v119, v126, v127\n" \
"buffer_store_dwordx2 v[118:119], v5, s[68:71], s95 offen\n" \
"s_waitcnt vmcnt(0) lgkmcnt(0)\n" \
"s_branch .Lsg2_qk_%=\n" \
".Lsg2_r_%=:\n" \
"s_nop 7\n" \
"s_nop 7\n" \
"s_waitcnt vmcnt(0) lgkmcnt(0)\n" \
"s_add_u32 s54, s95, 0\n" \
"buffer_load_dwordx2 v[14:15], v6, s[36:39], s54 offen sc1\n" \
"buffer_load_dwordx2 v[12:13], v5, s[64:67], s95 offen\n" \
"v_lshlrev_b32 v112, 16, v0\n" \
"v_and_b32 v113, 0xffff0000, v0\n" \
"v_lshlrev_b32 v114, 16, v1\n" \
"v_and_b32 v115, 0xffff0000, v1\n" \
"s_waitcnt vmcnt(1)\n" \
"v_lshlrev_b32 v120, 16, v14\n" \
"v_and_b32 v121, 0xffff0000, v14\n" \
"s_waitcnt vmcnt(0)\n" \
"v_lshlrev_b32 v124, 16, v12\n" \
"v_and_b32 v125, 0xffff0000, v12\n" \
"v_lshlrev_b32 v122, 16, v15\n" \
"v_and_b32 v123, 0xffff0000, v15\n" \
"v_lshlrev_b32 v126, 16, v13\n" \
"v_and_b32 v127, 0xffff0000, v13\n" \
"v_fmac_f32 v124, v112, v120\n" \
"v_fmac_f32 v125, v113, v121\n" \
"v_fmac_f32 v126, v114, v122\n" \
"v_fmac_f32 v127, v115, v123\n" \
"v_cvt_pk_bf16_f32 v118, v124, v125\n" \
"v_cvt_pk_bf16_f32 v119, v126, v127\n" \
"buffer_store_dwordx2 v[118:119], v5, s[68:71], s95 offen\n" \
"s_waitcnt vmcnt(0) lgkmcnt(0)\n" \
"s_branch .Lsg2_rk_%=\n" \
".Lsg2_end_%=:\n" \
"s_endpgm\n"

typedef u32 v32u __attribute__((ext_vector_type(32)));
typedef float f32x2 __attribute__((ext_vector_type(2)));
__device__ __forceinline__ float xsum(float s) {
    auto a = __builtin_amdgcn_permlane16_swap(__builtin_bit_cast(u32, s), __builtin_bit_cast(u32, s), false, false);
    s = __builtin_bit_cast(float, (u32)a[0]) + __builtin_bit_cast(float, (u32)a[1]);
    auto b = __builtin_amdgcn_permlane32_swap(__builtin_bit_cast(u32, s), __builtin_bit_cast(u32, s), false, false);
    return __builtin_bit_cast(float, (u32)b[0]) + __builtin_bit_cast(float, (u32)b[1]);
}
__device__ __forceinline__ void ln_tile(const Params& p, const v4u (&xv)[8], int rt, int w, int lane) {
    const int n = lane & 15, g = lane >> 4;
    const rsrc_t rs = __builtin_amdgcn_make_buffer_rsrc(p.ws, 0, WS_END, 0x00020000);
    LAS float* red = (LAS float*)(uintptr_t)(136 * 1024);
    f32x2 xf[32], s2 = {0.f, 0.f};
#pragma unroll
    for (int i = 0; i < 32; ++i) { xf[i] = f32x2{bflo(xv[i >> 2][i & 3]), bfhi(xv[i >> 2][i & 3])}; s2 += xf[i]; }
    const float s = xsum(s2.x + s2.y);
    if (g == 0) red[n * 4 + w] = s;
    BAR();
    f32x4 t = *(LAS f32x4*)(red + n * 4);
    const float mean = ((t.x + t.y) + (t.z + t.w)) * (1.0f / DM);
    f32x2 q2 = {0.f, 0.f};
#pragma unroll
    for (int i = 0; i < 32; ++i) { const f32x2 a = xf[i] - mean; q2 += a * a; }
    const float q = xsum(q2.x + q2.y);
    if (g == 0) red[64 + n * 4 + w] = q;
    BAR();
    t = *(LAS f32x4*)(red + 64 + n * 4);
    const float var = ((t.x + t.y) + (t.z + t.w)) * (1.0f / DM);
    const float mb = rbf(mean);
    const float rd = 1.0f / rbf(sqrtf(rbf(rbf(var) + 1e-5f)));
#pragma unroll
    for (int i = 0; i < 8; ++i) {
        v4u o;
#pragma unroll
        for (int e = 0; e < 4; ++e) { const f32x2 a = xf[4 * i + e] - mb; const u32 d = pack2(a.x, a.y); const f32x2 y = f32x2{bflo(d), bfhi(d)} * rd; o[e] = pack2(y.x, y.y); }
        st128s(o, rs, WS_XF + rt * 32768 + (w * 8 + i) * 1024 + lane * 16);
    }
    WAIT_V0();
    __builtin_amdgcn_s_barrier(); CFENCE();
    if (w == 0 && lane == 0) __hip_atomic_fetch_add((GAS u32*)(p.ws + WS_CT) + CT_LN + (rt >> 4) * 32, 1u, RLXA);
}
__device__ __forceinline__ void ln_role(const Params& p, int c, int w) {
    const int lane = threadIdx.x & 63;
    const u16* xr = p.x + (size_t)(c * 16 + (lane & 15)) * DM + w * 256 + (lane >> 4) * 8;
    v4u xa[8], xb[8];
#pragma unroll
    for (int i = 0; i < 8; ++i) { xa[i] = *(const v4u*)(xr + i * 32); xb[i] = *(const v4u*)(xr + 128 * DM + i * 32); }
#pragma unroll 1
    for (int k = 0; k < 16; ++k) {
        v4u xc[8];
#pragma unroll
        for (int i = 0; i < 8; ++i) xc[i] = *(const v4u*)(xr + (size_t)min(k + 2, 15) * 128 * DM + i * 32);
        ln_tile(p, xa, k * 8 + c, w, lane);
#pragma unroll
        for (int i = 0; i < 8; ++i) { xa[i] = xb[i]; xb[i] = xc[i]; }
    }
}

__device__ __forceinline__ void p1_role(const Params& p, int b, int s0, int s1, int id, int w) {
    const int lane = __builtin_amdgcn_mbcnt_hi(~0u, __builtin_amdgcn_mbcnt_lo(~0u, 0u)), n = lane & 15, g = lane >> 4;
    const bool g1 = b >= 96;
    const int h = b & 31, kind = b >> 5;
    const int stride = g1 ? FF * 2 : 8192;
    const u16* src = g1 ? ((w >> 1) ? p.wu : p.wg) + (b - 96) * 64 : colsrc(p, kind == 0 ? 1 : (kind == 1 ? 0 : 2), 2 * h + (w >> 1));
    const v4u rs = mksrd(p.ws, WS_END), rw = mksrd(src, 1023 * stride + 128);
    const int v0 = lane * 16;
    const int v1 = (8 * (w & 1) + (lane >> 3)) * stride + ((lane & 7) ^ ((((lane >> 4) & 1) | ((w & 1) << 1)) << 1)) * 16;
    const int swz = ((n >> 3) | ((g & 1) << 1)), rowo = (8 * g + (n >> 2)) * 128 + (n & 3) * 8;
    const int v2 = (g1 ? 0 : (w >> 1) * 8192) + rowo + (((g1 ? w : 2 * (w & 1)) ^ swz) * 32);
    const int v3 = (g1 ? 8192 : (w >> 1) * 8192) + rowo + (((g1 ? w : 2 * (w & 1) + 1) ^ swz) * 32);
    const int v4 = WS_XF + (w >> 1) * 32768 + (w & 1) * 8192 + lane * 16;
    int v5, ostr, od;
    if (g1) { v5 = WS_HF + (g & 1) * (112 * 1024) + ((b - 96) * 2 + (w >> 1)) * 1024 + (2 * (w & 1) + (g >> 1)) * 256 + n * 16; ostr = 2 * 112 * 1024; od = 0; }
    else if (kind == 2) { v5 = WS_TK + h * TK_STR + 16384 + w * 2048 + lane * 16; ostr = 24576; od = 1024; }
    else { v5 = WS_TK + h * TK_STR + (kind == 0 ? 0 : 8192) + w * 1024 + lane * 16; ostr = 24576; od = 4096; }
    const int v6 = (w == 0 && lane == 0) ? WS_CT + (g1 ? CT_G1 + (b - 96) * 32 : CT_P1 + h * 32 + kind * 4) * 4 : OOB;
#define P1_IN "{v0}"(v0), "{v1}"(v1), "{v2}"(v2), "{v3}"(v3), "{v4}"(v4), "{s[36:39]}"(rs), "{s[40:43]}"(rw), "{s44}"(16 * stride), "{s46}"(s0), "{s47}"(s1), "{s48}"(w), "{s49}"(ostr), "{s50}"(od), "{s51}"(rfl(kind == 1 ? 0x3e0293ee : 0x3f800000))
    int a5 = v5, a6 = v6; u32 tc, tr;
    if (g1) asm volatile(ASM_P1_G1 : "+{v5}"(a5), "+{v6}"(a6), "={s70}"(tc), "={s72}"(tr) : P1_IN : CLOB_P1);
    else if (kind == 2) asm volatile(ASM_P1_V : "+{v5}"(a5), "+{v6}"(a6), "={s70}"(tc), "={s72}"(tr) : P1_IN : CLOB_P1);
    else asm volatile(ASM_P1_QK : "+{v5}"(a5), "+{v6}"(a6), "={s70}"(tc), "={s72}"(tr) : P1_IN : CLOB_P1);
}

#define TUNEV(i, c) (c)
#define C_MO 8
#define C_MM 8
#define C_X 136
#define C_Y 82
#define C_DS 40
#define C_PW 4
#define C_PW3 4
#define C_GD 0
#define C_KD 0
__device__ __forceinline__ int mlane(int n, int g) { return (g >> 1) * 512 + (n >> 3) * 256 + (g & 1) * 128 + (n & 7) * 16; }
__device__ __forceinline__ void qk_role(const Params& p, int h, int par) {
    const int lane = __builtin_amdgcn_mbcnt_hi(~0u, __builtin_amdgcn_mbcnt_lo(~0u, 0u)), w = rfl(threadIdx.x >> 6), n = lane & 15, g = lane >> 4;
    const v4u rs = mksrd(p.ws, WS_END), rw = mksrd(colsrc(p, w >> 1, 2 * h + (w & 1)), 1023 * 8192 + 128);
    const int l16 = lane * 16, v1 = l16 + w * 4096, v2 = v1 + 65536, v3 = l16 + 112 * 1024;
    const int v4 = amap(HB_M + w * 1024 + mlane(n, g), par);
    const int v5 = 152 * 1024 + (32 * w + n) * 32 + ((g >> 1) ^ (n >> 2)) * 8 + (g & 1) * 4, v0 = v5 ^ 16;
    const int v6 = 152 * 1024 + (4 * g + (n >> 2)) * 32 + ((n & 3) ^ g) * 8;
    const int v15 = min(l16, 64);
    const int v7 = n < 8 ? amap(HB_O + (n >> 2) * 32768 + h * 1024 + w * 256 + (g >> 1) * 128 + ((n >> 1) & 1) * 64 + (g & 1) * 32 + (n & 1) * 16, par) : OOB;
    const int v8 = g >= 2 ? amap(HB_V + h * 2048 + w * 512 + (g - 2) * 128 + n * 8, par) : OOB;
    const int v9 = lane < 32 ? amap(HB_H + lane * 128, par) : OOB;
    const int v10 = (w == 0 && lane == 0) ? 0 : OOB;
    const int v11 = WS_OF + h * OF_STR + (w & 1) * 4096 + w * 1024 + l16;
    const int v12 = (n & 2) ? 0xEEEE : 0x4444;
    const int v13 = g < 2 ? 0xFF800000 : 0;
    const int v14 = 16 * (w & 1) + n - 4 * g;
    const int r8 = lane >> 3;
    const int v32 = r8 * 8192 + ((lane & 7) ^ (((r8 >> 1) & 1) << 1)) * 16, v33 = (8 + r8) * 8192 + ((lane & 7) ^ ((((r8 >> 1) & 1) | 2) << 1)) * 16;
    const int swz = (n >> 3) | ((g & 1) << 1), rowo = (w >> 1) * 4096 + (8 * g + (n >> 2)) * 128 + (n & 3) * 8;
    const int v34 = rowo + ((2 * (w & 1)) ^ swz) * 32, v35 = rowo + ((2 * (w & 1) + 1) ^ swz) * 32;
    asm volatile(ASM_QK :: "{v0}"(v0), "{v1}"(v1), "{v2}"(v2), "{v3}"(v3), "{v4}"(v4), "{v5}"(v5), "{v6}"(v6), "{v7}"(v7), "{v8}"(v8), "{v9}"(v9), "{v10}"(v10), "{v11}"(v11), "{v12}"(v12),
        "{v13}"(v13), "{v14}"(v14), "{v15}"(v15), "{v[32:35]}"(v4u{(u32)v32, (u32)v33, (u32)v34, (u32)v35}), "{s[36:39]}"(rs), "{s[40:43]}"(rw), "{s[44:45]}"(v2u{(u32)TUNEV(0, C_MO), (u32)TUNEV(1, C_MM)}), "{s46}"(TUNEV(2, C_X)), "{s17}"(TUNEV(3, C_Y)), "{s14}"(TUNEV(4, C_DS)), "{s47}"(WS_DBG + 131072 + (h * 4 + w) * 2048), "{s48}"(w),
        "{s63}"(h * TK_STR), "{s80}"(WS_CT + (CT_P1 + h * 32) * 4), "{s87}"(WS_CT + (CT_AT + h * 32) * 4), "{s94}"(amap(HB_H + h * 128, par)) : "v255", "a255", "s99", "memory");
    __builtin_unreachable();
}
__device__ __forceinline__ void vo_role(const Params& p, int c, int par) {
    const int lane = __builtin_amdgcn_mbcnt_hi(~0u, __builtin_amdgcn_mbcnt_lo(~0u, 0u)), w = rfl(threadIdx.x >> 6), n = lane & 15, g = lane >> 4;
    const int rg = RGX ? c >> 5 : c & 1, cs = RGX ? c & 31 : c >> 1;
    const v4u rs = mksrd(p.ws, WS_END), rw = mksrd(colsrc(p, 2, c) + (size_t)(256 * w) * 4096, 255 * 8192 + 128), ro = mksrd(colsrc(p, 3, cs >> 1) + (size_t)(1024 * w) * 1024 + (cs & 1) * 32, 1023 * 2048 + 64), rm = mksrd(p.mem0, 16384);
    const int v0 = lane * 16, v1 = (lane >> 3) * 8192 + (lane & 7) * 16, v2 = (lane >> 2) * 2048 + (lane & 3) * 16;
    const int v3 = (8 * g + (n >> 2)) * 128 + w * 32 + (n & 3) * 8;
    const int v4 = w * 40960 + (16 * (g & 1) + 8 * (g >> 1) + 4 * (n >> 3) + ((n >> 2) & 1)) * 64 + (n & 3) * 8;
    const int v5 = amap(HB_M + w * 1024 + mlane(n, g), par);
    const int v6 = amap(HB_O + rg * 32768 + w * 8192 + (2 * (n >> 3) + (g >> 1)) * 256 + (g & 1) * 128 + (n & 7) * 16, par);
    const int v7 = (g & 1) == 0 ? amap(HB_V + c * 1024 + w * 256 + (g >> 1) * 128 + n * 8, par) : OOB;
    const int v8 = (w == 0 && (lane & 3) == 0) ? amap(HB_M + (cs * 2 + rg) * 256 + lane * 4, par) : OOB;
    const int v9 = (w == 0 && lane < 32) ? amap(HB_H + lane * 128, par) : OOB;
    const int v12 = (n & 2) ? 0xEEEE : 0x4444;
    const int v13 = (4 * rg + 2 * ((lane >> 4) & 1) + ((lane >> 2) & 1)) * 2048 + (32 * cs + 16 * (lane >> 5) + 2 * ((lane >> 3) & 1)) * 2;
    const int v15 = 16384 + (g * 16 + ((n >> 1) & 1) * 8 + (n >> 2)) * 32 + w * 8 + (n & 1) * 4;
    asm volatile(ASM_VO :: "{v0}"(v0), "{v1}"(v1), "{v2}"(v2), "{v3}"(v3), "{v4}"(v4), "{v5}"(v5), "{v6}"(v6), "{v7}"(v7), "{v8}"(v8), "{v9}"(v9), "{v12}"(v12),
        "{v13}"(v13), "{v15}"(v15), "{v28}"((w == 0 && lane == 0) ? WS_CT + (CT_P1 + (c >> 1) * 32 + 12 + (c & 1) * 4) * 4 : OOB), "{s[36:39]}"(rs), "{s[40:43]}"(rw), "{s[64:67]}"(ro), "{s[68:71]}"(rm), "{s[44:45]}"(v2u{(u32)TUNEV(0, C_MO), (u32)TUNEV(1, C_MM)}), "{s46}"(TUNEV(2, C_X)), "{s17}"(TUNEV(3, C_Y)),
        "{s63}"(0), "{s47}"(WS_DBG + 131072 + ((32 + c) * 4 + w) * 2048), "{s48}"(w) : "v255", "a255", "s99", "memory");
    __builtin_unreachable();
}
__device__ __forceinline__ void s64_role(const Params& p, int kind, int cb, int r, int id, int w) {
    const int lane = __builtin_amdgcn_mbcnt_hi(~0u, __builtin_amdgcn_mbcnt_lo(~0u, 0u)), n = lane & 15, g = lane >> 4;
    const int kr = kind ? 896 : 1024;
    const v4u rs = mksrd(p.ws, WS_END), rw = mksrd((kind ? p.wd + cb * 64 : colsrc(p, 3, cb)) + (size_t)(kr * w) * 1024, (kr - 1) * 2048 + 128), rx = mksrd(p.x, 2048 * 2048), ro = mksrd(p.out, 2048 * 2048);
    const int v2 = lane * 16 + w * 32768, v0 = (lane >> 3) * 2048 + (lane & 7) * 16;
    const int v1 = w * 32768 + ((kind ? 8 : 4) * g + (n >> 2)) * 128 + (n & 3) * 8;
    const int v3 = (kind ? WS_HF + w * 28 * 1024 : WS_OF + 8 * w * OF_STR) + lane * 16;
    const int v4 = WS_CT + (kind ? CT_G1 + (14 * w + min(n, 13)) * 32 : CT_AT + (8 * w + (n & 7)) * 32) * 4;
    const int v5 = n * 2048 + cb * 128 + w * 32 + g * 8, v6 = WS_GT + cb * 2048 + w * 512 + lane * 8;
    const int v7 = 131072 + w * 4096 + lane * 16, v8 = 131072 + w * 1024 + lane * 16;
    const int ha = TUNEV(6, S64_HEAD);
    const int nt = kind ? (r < 2 ? 22 : 21) : (128 - ha) / 4 + (r < 2 ? ha / 2 : 0), rt = kind ? 127 - r : (r < 2 ? r : ha + r);
#define S64_IO "{v7}"(v7), "{v8}"(v8), "{s44}"(rt), "{v0}"(v0), "{v1}"(v1), "{v2}"(v2), "{v3}"(v3), "{v4}"(v4), "{v5}"(v5), "{v6}"(v6), "{s[36:39]}"(rs), "{s[40:43]}"(rw), "{s[64:67]}"(rx), "{s[68:71]}"(ro), \
        "{s45}"(2), "{s47}"(kind ? -6 : 4), "{s50}"(kind ? -128 : ha), "{s46}"(nt), "{s48}"(w), "{s49}"(WS_CT + CT_S * 4 + cb * 16 + w * 4), "{s51}"(w ? OOB : WS_DBG + id * 128), "{s99}"(kind == 0 && r < 2 ? TUNEV(9, C_PW3) : TUNEV(5, C_PW)), "{s85}"(kind ? cb % 7 : cb & 7)
    if (kind) asm volatile(ASM_S64_G2 :: S64_IO : "v255", "a255", "memory");
    else asm volatile(ASM_S64_WO :: S64_IO : "v255", "a255", "memory");
    __builtin_unreachable();
}
#define MAIN_BODY \
if (id < 96) { \
        const int x = id >> 5, t = id & 31, nq = x == 2 ? 32 - 2 * NQ0 : NQ0; \
        if (t < nq) qk_role(p, NQ0 * x + t, par); \
        else vo_role(p, (32 - NQ0) * x + t - nq, par); \
    } \
    const int gx = (id >> 5) - 4, gr = id & 31; \
    if (gx >= 0 && gr >= 16 && gr < 30) for (int i = TUNEV(7, C_GD); i > 0; --i) __builtin_amdgcn_s_sleep(32); \
    if (gx < 0 || gr < 16) for (int i = TUNEV(8, C_KD); i > 0; --i) __builtin_amdgcn_s_sleep(32); \
    if (gx < 0 || gr < 30) p1_role(p, gx < 0 ? 64 + gr : (gr < 16 ? gx * 8 + (gr & 7) + (gr >> 3) * 32 : 96 + gx * 14 + gr - 16), 0, 64, id, wv_); \
    else ln_role(p, gx * 2 + gr - 30, wv_); \
    STAMP(2); \
    s64_role(p, gx < 1 ? 0 : 1, gr & 15, (gx < 1 ? 2 * gx + 2 : 2 * gx - 2) + (gr >> 4), id, wv_);
constexpr int LDS_BYTES = 160 * 1024;
__device__ __forceinline__ int take_role(const Params& p, LAS u32* L) {
    if (threadIdx.x == 0) {
        GAS u32* tk = (GAS u32*)(p.ws + WS_CT) + CT_TICK;
        const u32 x = xcc_id() & 7u;
        const u32 t = __hip_atomic_fetch_add(tk + 64 * x, 1u, RLXA);
        u32 id = 32 * x + t;
        if (t >= 32u) {
            u32 o = __hip_atomic_fetch_add(tk + 64 * 8, 1u, RLXA);
            for (u32 spins = 0; spins < (1u << 20); ++spins) {
                u32 tot = 0;
                for (int k = 0; k < 8; ++k) tot += __hip_atomic_load(tk + 64 * k, RLXA);
                if (tot >= 256u) break;
                __builtin_amdgcn_s_sleep(8);
            }
            id = 255u;
            for (int k = 0; k < 8; ++k) {
                const u32 ck = __hip_atomic_load(tk + 64 * k, RLXA);
                const u32 fr = ck < 32u ? 32u - ck : 0u;
                if (o < fr) { id = 32 * k + ck + o; break; }
                o -= fr;
            }
        }
        *L = id;
    }
    BAR();
    const int id = rfl(*L);
    BAR();
    return id;
}
#define STAMP(i) do {} while (0)
__global__ void __launch_bounds__(256, 1) k_main(Params p) {
    extern __shared__ __attribute__((aligned(16))) u8 smem[];
    const int par = rfl(*(const GAS u32*)(p.ws + WS_CT + CT_PAR * 4)) & 1;
    const int id = take_role(p, (LAS u32*)smem);
    const int wv_ = rfl(threadIdx.x >> 6);
    STAMP(0);
    MAIN_BODY
    STAMP(7);
}
__global__ void __launch_bounds__(256) k_zero(Params p) {
    __shared__ u32 vote[4];
    u8* ws = p.ws;
    v4u z = {0u, 0u, 0u, 0u};
    if (WARM && blockIdx.x != 0) {
        const int i = (blockIdx.x - 1) * 32 + (threadIdx.x >> 3), m2 = min(i & 7, 2);
        v4u t[5];
#pragma unroll
        for (int m = 0; m < 4; ++m) t[m] = __builtin_nontemporal_load((const v4u*)(wmat(p, m) + (size_t)i * 512 + slowpos(wmat(p, m)) * 64 + (threadIdx.x & 7) * 8));
        if ((i & 7) < 3) t[4] = __builtin_nontemporal_load((const v4u*)(wmat(p, m2) + (size_t)(i >> 3) * 4096 + slowpos2(wmat(p, m2)) * 64 + (threadIdx.x & 7) * 8));
#pragma unroll
        for (int m = 0; m < 4; ++m) *(v4u*)(ws + WS_WC + (size_t)i * 1024 + (m == 3 ? 4 : m) * 128 + (threadIdx.x & 7) * 16) = t[m];
        if ((i & 7) < 3) *(v4u*)(ws + WS_WC + (size_t)(i >> 3) * 8192 + (5 + m2) * 128 + (threadIdx.x & 7) * 16) = t[4];
    }
    if (blockIdx.x == 0) {
        const rsrc_t rs = __builtin_amdgcn_make_buffer_rsrc(ws, 0, 2 * MB, 0x00020000);
        const int lane = threadIdx.x & 63, w = threadIdx.x >> 6, base = 2 * MB - 32768 + (w & 1) * 16384 + (w >> 1) * 2048 + (lane & 31) * 4;
        st32s(0u, rs, base + 1024); st32s(0u, rs, base + 8192 + 1024);
        u32 a = 65535u, b = 65535u;
#pragma unroll 1
        for (int k = 0; k < 8; ++k) {
            u64 t0, t1, t2;
            asm volatile("s_waitcnt vmcnt(0)\n\ts_memtime %0\n\ts_waitcnt lgkmcnt(0)" : "=s"(t0) :: "memory");
            st32s(0u, rs, base);
            asm volatile("s_waitcnt vmcnt(0)\n\ts_memtime %0\n\ts_waitcnt lgkmcnt(0)" : "=s"(t1) :: "memory");
            st32s(0u, rs, base + 8192);
            asm volatile("s_waitcnt vmcnt(0)\n\ts_memtime %0\n\ts_waitcnt lgkmcnt(0)" : "=s"(t2) :: "memory");
            a = min(a, (u32)(t1 - t0)); b = min(b, (u32)(t2 - t1));
            if (2 * min(a, b) > max(a, b) && 8 * max(a, b) > 9 * min(a, b) && max(a, b) < 1000u) break;
        }
        vote[w] = (a << 16) | b;
        __syncthreads();
        if (threadIdx.x == 0) {
            for (int k = 0; k < 4; ++k) { a = min(a, vote[k] >> 16); b = min(b, vote[k] & 65535u); }
            z.x = (b < a ? 1u : 0u) ^ (xcc_id() >= 4 ? 1u : 0u);
        }
    }
    if (blockIdx.x < ZERO_BYTES / 4096) ((v4u*)ws)[blockIdx.x * 256 + threadIdx.x] = z;
}

extern "C" void kernel_launch(void* const* d_in, const int* in_sizes, int n_in, void* d_out, int out_size, void* d_ws, size_t ws_size, hipStream_t stream) {
    Params p;
    p.x = (const u16*)d_in[0]; p.mem0 = (const u16*)d_in[1]; p.wq = (const u16*)d_in[2]; p.wk = (const u16*)d_in[3]; p.wv = (const u16*)d_in[4]; p.wo = (const u16*)d_in[5];
    p.wg = (const u16*)d_in[8]; p.wu = (const u16*)d_in[9]; p.wd = (const u16*)d_in[10];
    p.out = (u16*)d_out;
    p.ws = (u8*)(((uintptr_t)d_ws + (2u * MB - 1)) & ~(uintptr_t)(2u * MB - 1));
    (void)hipFuncSetAttribute((const void*)k_main, hipFuncAttributeMaxDynamicSharedMemorySize, LDS_BYTES);
    hipLaunchKernelGGL(k_zero, dim3(WARM ? 257 : ZERO_BYTES / 4096), dim3(256), 0, stream, p);
    hipLaunchKernelGGL(k_main, dim3(256), dim3(256), LDS_BYTES, stream, p);
}
```

```cpp
#pragma clang diagnostic ignored "-Wint-to-pointer-cast"
#pragma clang diagnostic ignored "-Winline-asm"
#include <hip/hip_runtime.h>
#include <hip/hip_bf16.h>
#include <stdint.h>

#define LAS __attribute__((address_space(3)))
#define GAS __attribute__((address_space(1)))
typedef unsigned u32;
typedef unsigned char u8;
typedef unsigned short u16;
typedef unsigned long long u64;
typedef u32 v2u __attribute__((ext_vector_type(2)));
typedef u32 v4u __attribute__((ext_vector_type(4)));
typedef float f32x4 __attribute__((ext_vector_type(4)));
typedef __bf16 bf16x2 __attribute__((ext_vector_type(2)));
typedef __amdgpu_buffer_rsrc_t rsrc_t;

constexpr int NSEG = 64, DM = 1024, NH = 32, FF = 3584;
constexpr int MB = 1 << 20;
constexpr int WS_CT = 0;
constexpr int WS_M = 262144;
constexpr int WS_O = 524288, WS_V = 1048576;
constexpr int WS_DBG = 4 * MB;
constexpr int WS_XF = 6 * MB;
constexpr int WS_TK = 10 * MB;
constexpr int WS_OF = 66 * MB;
constexpr int WS_HF = 84 * MB;
constexpr int WS_GT = 100 * MB;
constexpr int WS_END = 108 * MB;
constexpr int TK_STR = 64 * 24576 + 8192, OF_STR = 524288 + 8192;
constexpr int CT_PAR = 0, CT_TMO = 64, CT_TICK = 256;
constexpr int CT_LN = 1024;
constexpr int CT_P1 = 2048;
constexpr int CT_G1 = 4096;
constexpr int CT_AT = 3072;
constexpr int CT_S = 8192;
#define PLACE 2
#define RGX 0
#define WARM 1
#define S64_HEAD 28
constexpr int OOB = 0x7ffffff0;
constexpr int AUX_SC1 = 16;

struct Params {
    const u16 *x, *mem0, *wq, *wk, *wv, *wo, *wg, *wu, *wd;
    u16* out;
    u8* ws;
};
__device__ __forceinline__ u32 pack2(float a, float b) { bf16x2 v = {(__bf16)a, (__bf16)b}; return __builtin_bit_cast(u32, v); }
__device__ __forceinline__ float bflo(u32 d) { return __builtin_bit_cast(float, d << 16); }
__device__ __forceinline__ float bfhi(u32 d) { return __builtin_bit_cast(float, d & 0xFFFF0000u); }
__device__ __forceinline__ float rbf(float a) { return (float)(__bf16)a; }
__device__ __forceinline__ void st128s(v4u v, rsrc_t rs, int voff) { __builtin_amdgcn_raw_buffer_store_b128(v, rs, voff, 0, AUX_SC1); }
__device__ __forceinline__ void st32s(u32 v, rsrc_t rs, int voff) { __builtin_amdgcn_raw_buffer_store_b32(v, rs, voff, 0, AUX_SC1); }
__device__ __forceinline__ u32 ld32s(rsrc_t rs, int voff) { return __builtin_amdgcn_raw_buffer_load_b32(rs, voff, 0, AUX_SC1); }
__device__ __forceinline__ u32 xcc_id() { return (u32)__builtin_amdgcn_s_getreg(20 | (3 << 11)) & 0xfu; }
__device__ __forceinline__ int rfl(int x) { return __builtin_amdgcn_readfirstlane(x); }
__device__ __forceinline__ v4u mksrd(const void* p, u32 bytes) {
    const u64 a = (u64)p;
    return v4u{(u32)rfl((u32)a), (u32)rfl((u32)(a >> 32) & 0xFFFFu), (u32)rfl(bytes), 0x00020000u};
}
#define CFENCE() asm volatile("" ::: "memory")
#define WAIT_V0() asm volatile("s_waitcnt vmcnt(0)" ::: "memory")
#define BAR() do { asm volatile("s_waitcnt lgkmcnt(0)" ::: "memory"); __builtin_amdgcn_s_barrier(); asm volatile("" ::: "memory"); } while (0)
#define RLXA __ATOMIC_RELAXED, __HIP_MEMORY_SCOPE_AGENT
__device__ __forceinline__ int amap(int off, int cls) { return ((off >> 13) << 14) + (off & 8191) + (cls & 8192); }

constexpr int WS_WC = 112 * MB;
__device__ __forceinline__ const u16* wmat(const Params& p, int m) { return m == 0 ? p.wq : (m == 1 ? p.wk : (m == 2 ? p.wv : p.wo)); }
__device__ __forceinline__ int slowpos(const void* b) { return (3 - (int)((u64)b >> 7)) & 7; }
__device__ __forceinline__ int slowpos2(const void* b) { return (63 - (int)((u64)b >> 7)) & 63; }
__device__ __forceinline__ const u16* colsrc(const Params& p, int m, int j) {
    const u16* b = wmat(p, m);
    if (WARM && m < 3 && j == slowpos2(b)) return (const u16*)(p.ws + WS_WC) + (5 + m) * 64;
    return (WARM && (j & 7) == slowpos(b)) ? (const u16*)(p.ws + WS_WC) + (j >> 3) * 512 + (m == 3 ? 4 : m) * 64 : b + j * 64;
}
#define ASM_P1_QK \
"s_nop 7\n" \
"s_nop 7\n" \
"s_lshr_b32 s52, s48, 1\n" \
"s_lshl_b32 s52, s52, 13\n" \
"s_and_b32 s53, s48, 1\n" \
"s_lshl_b32 s53, s53, 10\n" \
"s_add_u32 s52, s52, s53\n" \
"s_mov_b32 s54, 0\n" \
"s_add_u32 m0, s52, 0\n" \
"s_nop 0\n" \
"buffer_load_dwordx4 v1, s[40:43], s54 offen nt lds\n" \
"s_add_u32 s54, s54, s44\n" \
"s_add_u32 m0, s52, 2048\n" \
"s_nop 0\n" \
"buffer_load_dwordx4 v1, s[40:43], s54 offen nt lds\n" \
"s_add_u32 s54, s54, s44\n" \
"s_add_u32 m0, s52, 4096\n" \
"s_nop 0\n" \
"buffer_load_dwordx4 v1, s[40:43], s54 offen nt lds\n" \
"s_add_u32 s54, s54, s44\n" \
"s_add_u32 m0, s52, 6144\n" \
"s_nop 0\n" \
"buffer_load_dwordx4 v1, s[40:43], s54 offen nt lds\n" \
"s_add_u32 s54, s54, s44\n" \
"s_add_u32 m0, s52, 16384\n" \
"s_nop 0\n" \
"buffer_load_dwordx4 v1, s[40:43], s54 offen nt lds\n" \
"s_add_u32 s54, s54, s44\n" \
"s_add_u32 m0, s52, 18432\n" \
"s_nop 0\n" \
"buffer_load_dwordx4 v1, s[40:43], s54 offen nt lds\n" \
"s_add_u32 s54, s54, s44\n" \
"s_add_u32 m0, s52, 20480\n" \
"s_nop 0\n" \
"buffer_load_dwordx4 v1, s[40:43], s54 offen nt lds\n" \
"s_add_u32 s54, s54, s44\n" \
"s_add_u32 m0, s52, 22528\n" \
"s_nop 0\n" \
"buffer_load_dwordx4 v1, s[40:43], s54 offen nt lds\n" \
"s_add_u32 s54, s54, s44\n" \
"s_add_u32 m0, s52, 32768\n" \
"s_nop 0\n" \
"buffer_load_dwordx4 v1, s[40:43], s54 offen nt lds\n" \
"s_add_u32 s54, s54, s44\n" \
"s_add_u32 m0, s52, 34816\n" \
"s_nop 0\n" \
"buffer_load_dwordx4 v1, s[40:43], s54 offen nt lds\n" \
"s_add_u32 s54, s54, s44\n" \
"s_add_u32 m0, s52, 36864\n" \
"s_nop 0\n" \
"buffer_load_dwordx4 v1, s[40:43], s54 offen nt lds\n" \
"s_add_u32 s54, s54, s44\n" \
"s_add_u32 m0, s52, 38912\n" \
"s_nop 0\n" \
"buffer_load_dwordx4 v1, s[40:43], s54 offen nt lds\n" \
"s_add_u32 s54, s54, s44\n" \
"s_add_u32 m0, s52, 49152\n" \
"s_nop 0\n" \
"buffer_load_dwordx4 v1, s[40:43], s54 offen nt lds\n" \
"s_add_u32 s54, s54, s44\n" \
"s_add_u32 m0, s52, 51200\n" \
"s_nop 0\n" \
"buffer_load_dwordx4 v1, s[40:43], s54 offen nt lds\n" \
"s_add_u32 s54, s54, s44\n" \
"s_add_u32 m0, s52, 53248\n" \
"s_nop 0\n" \
"buffer_load_dwordx4 v1, s[40:43], s54 offen nt lds\n" \
"s_add_u32 s54, s54, s44\n" \
"s_add_u32 m0, s52, 55296\n" \
"s_nop 0\n" \
"buffer_load_dwordx4 v1, s[40:43], s54 offen nt lds\n" \
"s_add_u32 s54, s54, s44\n" \
"s_add_u32 m0, s52, 65536\n" \
"s_nop 0\n" \
"buffer_load_dwordx4 v1, s[40:43], s54 offen nt lds\n" \
"s_add_u32 s54, s54, s44\n" \
"s_add_u32 m0, s52, 67584\n" \
"s_nop 0\n" \
"buffer_load_dwordx4 v1, s[40:43], s54 offen nt lds\n" \
"s_add_u32 s54, s54, s44\n" \
"s_add_u32 m0, s52, 69632\n" \
"s_nop 0\n" \
"buffer_load_dwordx4 v1, s[40:43], s54 offen nt lds\n" \
"s_add_u32 s54, s54, s44\n" \
"s_add_u32 m0, s52, 71680\n" \
"s_nop 0\n" \
"buffer_load_dwordx4 v1, s[40:43], s54 offen nt lds\n" \
"s_add_u32 s54, s54, s44\n" \
"s_add_u32 m0, s52, 81920\n" \
"s_nop 0\n" \
"buffer_load_dwordx4 v1, s[40:43], s54 offen nt lds\n" \
"s_add_u32 s54, s54, s44\n" \
"s_add_u32 m0, s52, 83968\n" \
"s_nop 0\n" \
"buffer_load_dwordx4 v1, s[40:43], s54 offen nt lds\n" \
"s_add_u32 s54, s54, s44\n" \
"s_add_u32 m0, s52, 86016\n" \
"s_nop 0\n" \
"buffer_load_dwordx4 v1, s[40:43], s54 offen nt lds\n" \
"s_add_u32 s54, s54, s44\n" \
"s_add_u32 m0, s52, 88064\n" \
"s_nop 0\n" \
"buffer_load_dwordx4 v1, s[40:43], s54 offen nt lds\n" \
"s_add_u32 s54, s54, s44\n" \
"s_add_u32 m0, s52, 98304\n" \
"s_nop 0\n" \
"buffer_load_dwordx4 v1, s[40:43], s54 offen nt lds\n" \
"s_add_u32 s54, s54, s44\n" \
"s_add_u32 m0, s52, 100352\n" \
"s_nop 0\n" \
"buffer_load_dwordx4 v1, s[40:43], s54 offen nt lds\n" \
"s_add_u32 s54, s54, s44\n" \
"s_add_u32 m0, s52, 102400\n" \
"s_nop 0\n" \
"buffer_load_dwordx4 v1, s[40:43], s54 offen nt lds\n" \
"s_add_u32 s54, s54, s44\n" \
"s_add_u32 m0, s52, 104448\n" \
"s_nop 0\n" \
"buffer_load_dwordx4 v1, s[40:43], s54 offen nt lds\n" \
"s_add_u32 s54, s54, s44\n" \
"s_add_u32 m0, s52, 114688\n" \
"s_nop 0\n" \
"buffer_load_dwordx4 v1, s[40:43], s54 offen nt lds\n" \
"s_add_u32 s54, s54, s44\n" \
"s_add_u32 m0, s52, 116736\n" \
"s_nop 0\n" \
"buffer_load_dwordx4 v1, s[40:43], s54 offen nt lds\n" \
"s_add_u32 s54, s54, s44\n" \
"s_add_u32 m0, s52, 118784\n" \
"s_nop 0\n" \
"buffer_load_dwordx4 v1, s[40:43], s54 offen nt lds\n" \
"s_add_u32 s54, s54, s44\n" \
"s_add_u32 m0, s52, 120832\n" \
"s_nop 0\n" \
"buffer_load_dwordx4 v1, s[40:43], s54 offen nt lds\n" \
"s_add_u32 s54, s54, s44\n" \
"v_add_u32 v8, 0x10000, v2\n" \
"v_add_u32 v9, 0x10000, v3\n" \
"s_waitcnt vmcnt(28)\n" \
"s_barrier\n" \
"ds_read_b64_tr_b16 a[0:1], v2 offset:0\n" \
"ds_read_b64_tr_b16 a[2:3], v2 offset:512\n" \
"ds_read_b64_tr_b16 a[128:129], v3 offset:0\n" \
"ds_read_b64_tr_b16 a[130:131], v3 offset:512\n" \
"ds_read_b64_tr_b16 a[4:5], v2 offset:4096\n" \
"ds_read_b64_tr_b16 a[6:7], v2 offset:4608\n" \
"ds_read_b64_tr_b16 a[132:133], v3 offset:4096\n" \
"ds_read_b64_tr_b16 a[134:135], v3 offset:4608\n" \
"s_waitcnt lgkmcnt(0)\n" \
"s_barrier\n" \
"s_add_u32 m0, s52, 0\n" \
"s_nop 0\n" \
"buffer_load_dwordx4 v1, s[40:43], s54 offen nt lds\n" \
"s_add_u32 s54, s54, s44\n" \
"s_add_u32 m0, s52, 2048\n" \
"s_nop 0\n" \
"buffer_load_dwordx4 v1, s[40:43], s54 offen nt lds\n" \
"s_add_u32 s54, s54, s44\n" \
"s_add_u32 m0, s52, 4096\n" \
"s_nop 0\n" \
"buffer_load_dwordx4 v1, s[40:43], s54 offen nt lds\n" \
"s_add_u32 s54, s54, s44\n" \
"s_add_u32 m0, s52, 6144\n" \
"s_nop 0\n" \
"buffer_load_dwordx4 v1, s[40:43], s54 offen nt lds\n" \
"s_add_u32 s54, s54, s44\n" \
"s_waitcnt vmcnt(28)\n" \
"s_barrier\n" \
"ds_read_b64_tr_b16 a[8:9], v2 offset:16384\n" \
"ds_read_b64_tr_b16 a[10:11], v2 offset:16896\n" \
"ds_read_b64_tr_b16 a[136:137], v3 offset:16384\n" \
"ds_read_b64_tr_b16 a[138:139], v3 offset:16896\n" \
"ds_read_b64_tr_b16 a[12:13], v2 offset:20480\n" \
"ds_read_b64_tr_b16 a[14:15], v2 offset:20992\n" \
"ds_read_b64_tr_b16 a[140:141], v3 offset:20480\n" \
"ds_read_b64_tr_b16 a[142:143], v3 offset:20992\n" \
"s_waitcnt lgkmcnt(0)\n" \
"s_barrier\n" \
"s_add_u32 m0, s52, 16384\n" \
"s_nop 0\n" \
"buffer_load_dwordx4 v1, s[40:43], s54 offen nt lds\n" \
"s_add_u32 s54, s54, s44\n" \
"s_add_u32 m0, s52, 18432\n" \
"s_nop 0\n" \
"buffer_load_dwordx4 v1, s[40:43], s54 offen nt lds\n" \
"s_add_u32 s54, s54, s44\n" \
"s_add_u32 m0, s52, 20480\n" \
"s_nop 0\n" \
"buffer_load_dwordx4 v1, s[40:43], s54 offen nt lds\n" \
"s_add_u32 s54, s54, s44\n" \
"s_add_u32 m0, s52, 22528\n" \
"s_nop 0\n" \
"buffer_load_dwordx4 v1, s[40:43], s54 offen nt lds\n" \
"s_add_u32 s54, s54, s44\n" \
"s_waitcnt vmcnt(28)\n" \
"s_barrier\n" \
"ds_read_b64_tr_b16 a[16:17], v2 offset:32768\n" \
"ds_read_b64_tr_b16 a[18:19], v2 offset:33280\n" \
"ds_read_b64_tr_b16 a[144:145], v3 offset:32768\n" \
"ds_read_b64_tr_b16 a[146:147], v3 offset:33280\n" \
"ds_read_b64_tr_b16 a[20:21], v2 offset:36864\n" \
"ds_read_b64_tr_b16 a[22:23], v2 offset:37376\n" \
"ds_read_b64_tr_b16 a[148:149], v3 offset:36864\n" \
"ds_read_b64_tr_b16 a[150:151], v3 offset:37376\n" \
"s_waitcnt lgkmcnt(0)\n" \
"s_barrier\n" \
"s_add_u32 m0, s52, 32768\n" \
"s_nop 0\n" \
"buffer_load_dwordx4 v1, s[40:43], s54 offen nt lds\n" \
"s_add_u32 s54, s54, s44\n" \
"s_add_u32 m0, s52, 34816\n" \
"s_nop 0\n" \
"buffer_load_dwordx4 v1, s[40:43], s54 offen nt lds\n" \
"s_add_u32 s54, s54, s44\n" \
"s_add_u32 m0, s52, 36864\n" \
"s_nop 0\n" \
"buffer_load_dwordx4 v1, s[40:43], s54 offen nt lds\n" \
"s_add_u32 s54, s54, s44\n" \
"s_add_u32 m0, s52, 38912\n" \
"s_nop 0\n" \
"buffer_load_dwordx4 v1, s[40:43], s54 offen nt lds\n" \
"s_add_u32 s54, s54, s44\n" \
"s_waitcnt vmcnt(28)\n" \
"s_barrier\n" \
"ds_read_b64_tr_b16 a[24:25], v2 offset:49152\n" \
"ds_read_b64_tr_b16 a[26:27], v2 offset:49664\n" \
"ds_read_b64_tr_b16 a[152:153], v3 offset:49152\n" \
"ds_read_b64_tr_b16 a[154:155], v3 offset:49664\n" \
"ds_read_b64_tr_b16 a[28:29], v2 offset:53248\n" \
"ds_read_b64_tr_b16 a[30:31], v2 offset:53760\n" \
"ds_read_b64_tr_b16 a[156:157], v3 offset:53248\n" \
"ds_read_b64_tr_b16 a[158:159], v3 offset:53760\n" \
"s_waitcnt lgkmcnt(0)\n" \
"s_barrier\n" \
"s_add_u32 m0, s52, 49152\n" \
"s_nop 0\n" \
"buffer_load_dwordx4 v1, s[40:43], s54 offen nt lds\n" \
"s_add_u32 s54, s54, s44\n" \
"s_add_u32 m0, s52, 51200\n" \
"s_nop 0\n" \
"buffer_load_dwordx4 v1, s[40:43], s54 offen nt lds\n" \
"s_add_u32 s54, s54, s44\n" \
"s_add_u32 m0, s52, 53248\n" \
"s_nop 0\n" \
"buffer_load_dwordx4 v1, s[40:43], s54 offen nt lds\n" \
"s_add_u32 s54, s54, s44\n" \
"s_add_u32 m0, s52, 55296\n" \
"s_nop 0\n" \
"buffer_load_dwordx4 v1, s[40:43], s54 offen nt lds\n" \
"s_add_u32 s54, s54, s44\n" \
"s_waitcnt vmcnt(28)\n" \
"s_barrier\n" \
"ds_read_b64_tr_b16 a[32:33], v8 offset:0\n" \
"ds_read_b64_tr_b16 a[34:35], v8 offset:512\n" \
"ds_read_b64_tr_b16 a[160:161], v9 offset:0\n" \
"ds_read_b64_tr_b16 a[162:163], v9 offset:512\n" \
"ds_read_b64_tr_b16 a[36:37], v8 offset:4096\n" \
"ds_read_b64_tr_b16 a[38:39], v8 offset:4608\n" \
"ds_read_b64_tr_b16 a[164:165], v9 offset:4096\n" \
"ds_read_b64_tr_b16 a[166:167], v9 offset:4608\n" \
"s_waitcnt lgkmcnt(0)\n" \
"s_barrier\n" \
"s_add_u32 m0, s52, 65536\n" \
"s_nop 0\n" \
"buffer_load_dwordx4 v1, s[40:43], s54 offen nt lds\n" \
"s_add_u32 s54, s54, s44\n" \
"s_add_u32 m0, s52, 67584\n" \
"s_nop 0\n" \
"buffer_load_dwordx4 v1, s[40:43], s54 offen nt lds\n" \
"s_add_u32 s54, s54, s44\n" \
"s_add_u32 m0, s52, 69632\n" \
"s_nop 0\n" \
"buffer_load_dwordx4 v1, s[40:43], s54 offen nt lds\n" \
"s_add_u32 s54, s54, s44\n" \
"s_add_u32 m0, s52, 71680\n" \
"s_nop 0\n" \
"buffer_load_dwordx4 v1, s[40:43], s54 offen nt lds\n" \
"s_add_u32 s54, s54, s44\n" \
"s_waitcnt vmcnt(28)\n" \
"s_barrier\n" \
"ds_read_b64_tr_b16 a[40:41], v8 offset:16384\n" \
"ds_read_b64_tr_b16 a[42:43], v8 offset:16896\n" \
"ds_read_b64_tr_b16 a[168:169], v9 offset:16384\n" \
"ds_read_b64_tr_b16 a[170:171], v9 offset:16896\n" \
"ds_read_b64_tr_b16 a[44:45], v8 offset:20480\n" \
"ds_read_b64_tr_b16 a[46:47], v8 offset:20992\n" \
"ds_read_b64_tr_b16 a[172:173], v9 offset:20480\n" \
"ds_read_b64_tr_b16 a[174:175], v9 offset:20992\n" \
"s_waitcnt lgkmcnt(0)\n" \
"s_barrier\n" \
"s_add_u32 m0, s52, 81920\n" \
"s_nop 0\n" \
"buffer_load_dwordx4 v1, s[40:43], s54 offen nt lds\n" \
"s_add_u32 s54, s54, s44\n" \
"s_add_u32 m0, s52, 83968\n" \
"s_nop 0\n" \
"buffer_load_dwordx4 v1, s[40:43], s54 offen nt lds\n" \
"s_add_u32 s54, s54, s44\n" \
"s_add_u32 m0, s52, 86016\n" \
"s_nop 0\n" \
"buffer_load_dwordx4 v1, s[40:43], s54 offen nt lds\n" \
"s_add_u32 s54, s54, s44\n" \
"s_add_u32 m0, s52, 88064\n" \
"s_nop 0\n" \
"buffer_load_dwordx4 v1, s[40:43], s54 offen nt lds\n" \
"s_add_u32 s54, s54, s44\n" \
"s_waitcnt vmcnt(28)\n" \
"s_barrier\n" \
"ds_read_b64_tr_b16 a[48:49], v8 offset:32768\n" \
"ds_read_b64_tr_b16 a[50:51], v8 offset:33280\n" \
"ds_read_b64_tr_b16 a[176:177], v9 offset:32768\n" \
"ds_read_b64_tr_b16 a[178:179], v9 offset:33280\n" \
"ds_read_b64_tr_b16 a[52:53], v8 offset:36864\n" \
"ds_read_b64_tr_b16 a[54:55], v8 offset:37376\n" \
"ds_read_b64_tr_b16 a[180:181], v9 offset:36864\n" \
"ds_read_b64_tr_b16 a[182:183], v9 offset:37376\n" \
"s_waitcnt lgkmcnt(0)\n" \
"s_barrier\n" \
"s_add_u32 m0, s52, 98304\n" \
"s_nop 0\n" \
"buffer_load_dwordx4 v1, s[40:43], s54 offen nt lds\n" \
"s_add_u32 s54, s54, s44\n" \
"s_add_u32 m0, s52, 100352\n" \
"s_nop 0\n" \
"buffer_load_dwordx4 v1, s[40:43], s54 offen nt lds\n" \
"s_add_u32 s54, s54, s44\n" \
"s_add_u32 m0, s52, 102400\n" \
"s_nop 0\n" \
"buffer_load_dwordx4 v1, s[40:43], s54 offen nt lds\n" \
"s_add_u32 s54, s54, s44\n" \
"s_add_u32 m0, s52, 104448\n" \
"s_nop 0\n" \
"buffer_load_dwordx4 v1, s[40:43], s54 offen nt lds\n" \
"s_add_u32 s54, s54, s44\n" \
"s_waitcnt vmcnt(28)\n" \
"s_barrier\n" \
"ds_read_b64_tr_b16 a[56:57], v8 offset:49152\n" \
"ds_read_b64_tr_b16 a[58:59], v8 offset:49664\n" \
"ds_read_b64_tr_b16 a[184:185], v9 offset:49152\n" \
"ds_read_b64_tr_b16 a[186:187], v9 offset:49664\n" \
"ds_read_b64_tr_b16 a[60:61], v8 offset:53248\n" \
"ds_read_b64_tr_b16 a[62:63], v8 offset:53760\n" \
"ds_read_b64_tr_b16 a[188:189], v9 offset:53248\n" \
"ds_read_b64_tr_b16 a[190:191], v9 offset:53760\n" \
"s_waitcnt lgkmcnt(0)\n" \
"s_barrier\n" \
"s_add_u32 m0, s52, 114688\n" \
"s_nop 0\n" \
"buffer_load_dwordx4 v1, s[40:43], s54 offen nt lds\n" \
"s_add_u32 s54, s54, s44\n" \
"s_add_u32 m0, s52, 116736\n" \
"s_nop 0\n" \
"buffer_load_dwordx4 v1, s[40:43], s54 offen nt lds\n" \
"s_add_u32 s54, s54, s44\n" \
"s_add_u32 m0, s52, 118784\n" \
"s_nop 0\n" \
"buffer_load_dwordx4 v1, s[40:43], s54 offen nt lds\n" \
"s_add_u32 s54, s54, s44\n" \
"s_add_u32 m0, s52, 120832\n" \
"s_nop 0\n" \
"buffer_load_dwordx4 v1, s[40:43], s54 offen nt lds\n" \
"s_add_u32 s54, s54, s44\n" \
"s_waitcnt vmcnt(28)\n" \
"s_barrier\n" \
"ds_read_b64_tr_b16 a[64:65], v2 offset:0\n" \
"ds_read_b64_tr_b16 a[66:67], v2 offset:512\n" \
"ds_read_b64_tr_b16 a[192:193], v3 offset:0\n" \
"ds_read_b64_tr_b16 a[194:195], v3 offset:512\n" \
"ds_read_b64_tr_b16 a[68:69], v2 offset:4096\n" \
"ds_read_b64_tr_b16 a[70:71], v2 offset:4608\n" \
"ds_read_b64_tr_b16 a[196:197], v3 offset:4096\n" \
"ds_read_b64_tr_b16 a[198:199], v3 offset:4608\n" \
"s_waitcnt vmcnt(24)\n" \
"s_barrier\n" \
"ds_read_b64_tr_b16 a[72:73], v2 offset:16384\n" \
"ds_read_b64_tr_b16 a[74:75], v2 offset:16896\n" \
"ds_read_b64_tr_b16 a[200:201], v3 offset:16384\n" \
"ds_read_b64_tr_b16 a[202:203], v3 offset:16896\n" \
"ds_read_b64_tr_b16 a[76:77], v2 offset:20480\n" \
"ds_read_b64_tr_b16 a[78:79], v2 offset:20992\n" \
"ds_read_b64_tr_b16 a[204:205], v3 offset:20480\n" \
"ds_read_b64_tr_b16 a[206:207], v3 offset:20992\n" \
"s_waitcnt vmcnt(20)\n" \
"s_barrier\n" \
"ds_read_b64_tr_b16 a[80:81], v2 offset:32768\n" \
"ds_read_b64_tr_b16 a[82:83], v2 offset:33280\n" \
"ds_read_b64_tr_b16 a[208:209], v3 offset:32768\n" \
"ds_read_b64_tr_b16 a[210:211], v3 offset:33280\n" \
"ds_read_b64_tr_b16 a[84:85], v2 offset:36864\n" \
"ds_read_b64_tr_b16 a[86:87], v2 offset:37376\n" \
"ds_read_b64_tr_b16 a[212:213], v3 offset:36864\n" \
"ds_read_b64_tr_b16 a[214:215], v3 offset:37376\n" \
"s_waitcnt vmcnt(16)\n" \
"s_barrier\n" \
"ds_read_b64_tr_b16 a[88:89], v2 offset:49152\n" \
"ds_read_b64_tr_b16 a[90:91], v2 offset:49664\n" \
"ds_read_b64_tr_b16 a[216:217], v3 offset:49152\n" \
"ds_read_b64_tr_b16 a[218:219], v3 offset:49664\n" \
"ds_read_b64_tr_b16 a[92:93], v2 offset:53248\n" \
"ds_read_b64_tr_b16 a[94:95], v2 offset:53760\n" \
"ds_read_b64_tr_b16 a[220:221], v3 offset:53248\n" \
"ds_read_b64_tr_b16 a[222:223], v3 offset:53760\n" \
"s_waitcnt vmcnt(12)\n" \
"s_barrier\n" \
"ds_read_b64_tr_b16 a[96:97], v8 offset:0\n" \
"ds_read_b64_tr_b16 a[98:99], v8 offset:512\n" \
"ds_read_b64_tr_b16 a[224:225], v9 offset:0\n" \
"ds_read_b64_tr_b16 a[226:227], v9 offset:512\n" \
"ds_read_b64_tr_b16 a[100:101], v8 offset:4096\n" \
"ds_read_b64_tr_b16 a[102:103], v8 offset:4608\n" \
"ds_read_b64_tr_b16 a[228:229], v9 offset:4096\n" \
"ds_read_b64_tr_b16 a[230:231], v9 offset:4608\n" \
"s_waitcnt vmcnt(8)\n" \
"s_barrier\n" \
"ds_read_b64_tr_b16 a[104:105], v8 offset:16384\n" \
"ds_read_b64_tr_b16 a[106:107], v8 offset:16896\n" \
"ds_read_b64_tr_b16 a[232:233], v9 offset:16384\n" \
"ds_read_b64_tr_b16 a[234:235], v9 offset:16896\n" \
"ds_read_b64_tr_b16 a[108:109], v8 offset:20480\n" \
"ds_read_b64_tr_b16 a[110:111], v8 offset:20992\n" \
"ds_read_b64_tr_b16 a[236:237], v9 offset:20480\n" \
"ds_read_b64_tr_b16 a[238:239], v9 offset:20992\n" \
"s_waitcnt vmcnt(4)\n" \
"s_barrier\n" \
"ds_read_b64_tr_b16 a[112:113], v8 offset:32768\n" \
"ds_read_b64_tr_b16 a[114:115], v8 offset:33280\n" \
"ds_read_b64_tr_b16 a[240:241], v9 offset:32768\n" \
"ds_read_b64_tr_b16 a[242:243], v9 offset:33280\n" \
"ds_read_b64_tr_b16 a[116:117], v8 offset:36864\n" \
"ds_read_b64_tr_b16 a[118:119], v8 offset:37376\n" \
"ds_read_b64_tr_b16 a[244:245], v9 offset:36864\n" \
"ds_read_b64_tr_b16 a[246:247], v9 offset:37376\n" \
"s_waitcnt vmcnt(0)\n" \
"s_barrier\n" \
"ds_read_b64_tr_b16 a[120:121], v8 offset:49152\n" \
"ds_read_b64_tr_b16 a[122:123], v8 offset:49664\n" \
"ds_read_b64_tr_b16 a[248:249], v9 offset:49152\n" \
"ds_read_b64_tr_b16 a[250:251], v9 offset:49664\n" \
"ds_read_b64_tr_b16 a[124:125], v8 offset:53248\n" \
"ds_read_b64_tr_b16 a[126:127], v8 offset:53760\n" \
"ds_read_b64_tr_b16 a[252:253], v9 offset:53248\n" \
"ds_read_b64_tr_b16 a[254:255], v9 offset:53760\n" \
"s_waitcnt lgkmcnt(0)\n" \
"s_barrier\n" \
"v_mov_b32 v14, 4096\n" \
"s_mov_b32 s59, s46\n" \
"s_add_u32 s63, s59, 0\n" \
"s_min_u32 s63, s63, 63\n" \
"s_lshr_b32 s63, s63, 3\n" \
"s_lshl_b32 s63, s63, 7\n" \
"s_branch .Lp1qk_l0_%=\n" \
".Lp1qk_l0k_%=:\n" \
"s_add_u32 s63, s59, 1\n" \
"s_min_u32 s63, s63, 63\n" \
"s_lshr_b32 s63, s63, 3\n" \
"s_lshl_b32 s63, s63, 7\n" \
"s_branch .Lp1qk_l1_%=\n" \
".Lp1qk_l1k_%=:\n" \
"s_lshl_b32 s55, s48, 13\n" \
"s_lshl_b32 s56, s46, 16\n" \
"s_lshl_b32 s60, s47, 16\n" \
"s_sub_u32 s60, s60, 0x10000\n" \
"s_mul_i32 s57, s46, s49\n" \
"s_sub_u32 s57, s57, s49\n" \
"s_add_u32 s58, s57, s50\n" \
"v_mov_b32 v7, v5\n" \
"v_mov_b32 v5, 0x7ffffff0\n" \
"v_mov_b32 v10, v0\n" \
"v_mov_b32 v11, v6\n" \
"v_mov_b32 v6, 0x7ffffff0\n" \
"s_mov_b32 s59, s46\n" \
"s_min_u32 s61, s56, s60\n" \
"s_add_u32 m0, s55, 0\n" \
"s_nop 0\n" \
"buffer_load_dwordx4 v4, s[36:39], s61 offen offset:0 sc1 lds\n" \
"buffer_load_dwordx4 v4, s[36:39], s61 offen offset:1024 sc1 lds\n" \
"buffer_load_dwordx4 v4, s[36:39], s61 offen offset:2048 sc1 lds\n" \
"buffer_load_dwordx4 v4, s[36:39], s61 offen offset:3072 sc1 lds\n" \
"s_add_u32 m0, s55, 4096\n" \
"s_add_u32 s61, s61, 4096\n" \
"buffer_load_dwordx4 v4, s[36:39], s61 offen offset:0 sc1 lds\n" \
"buffer_load_dwordx4 v4, s[36:39], s61 offen offset:1024 sc1 lds\n" \
"buffer_load_dwordx4 v4, s[36:39], s61 offen offset:2048 sc1 lds\n" \
"buffer_load_dwordx4 v4, s[36:39], s61 offen offset:3072 sc1 lds\n" \
"s_min_u32 s61, s56, s60\n" \
"s_add_u32 s61, s61, 16384\n" \
"s_add_u32 s56, s56, 0x10000\n" \
"s_add_u32 m0, s55, 32768\n" \
"s_nop 0\n" \
"buffer_load_dwordx4 v4, s[36:39], s61 offen offset:0 sc1 lds\n" \
"buffer_load_dwordx4 v4, s[36:39], s61 offen offset:1024 sc1 lds\n" \
"buffer_load_dwordx4 v4, s[36:39], s61 offen offset:2048 sc1 lds\n" \
"buffer_load_dwordx4 v4, s[36:39], s61 offen offset:3072 sc1 lds\n" \
"s_add_u32 m0, s55, 36864\n" \
"s_add_u32 s61, s61, 4096\n" \
"buffer_load_dwordx4 v4, s[36:39], s61 offen offset:0 sc1 lds\n" \
"buffer_load_dwordx4 v4, s[36:39], s61 offen offset:1024 sc1 lds\n" \
"buffer_load_dwordx4 v4, s[36:39], s61 offen offset:2048 sc1 lds\n" \
"buffer_load_dwordx4 v4, s[36:39], s61 offen offset:3072 sc1 lds\n" \
"s_mov_b32 s70, 0\n" \
"s_mov_b32 s72, 0\n" \
"s_waitcnt vmcnt(0) lgkmcnt(0)\n" \
"s_barrier\n" \
"v_add_u32 v12, 0x10000, v10\n" \
"ds_read_b128 v[16:19], v10 offset:0\n" \
"ds_read_b128 v[20:23], v10 offset:16384\n" \
"ds_read_b128 v[24:27], v10 offset:1024\n" \
"ds_read_b128 v[28:31], v10 offset:17408\n" \
"ds_read_b128 v[32:35], v10 offset:2048\n" \
"ds_read_b128 v[36:39], v10 offset:18432\n" \
"s_waitcnt lgkmcnt(4)\n" \
"s_nop 7\n" \
"s_nop 7\n" \
".p2align 6\n" \
".Lp1qk_loop_%=:\n" \
"v_mfma_f32_16x16x32_bf16 v[48:51], a[0:3], v[16:19], 0\n" \
"s_min_u32 s61, s56, s60\n" \
"s_add_u32 m0, s55, 65536\n" \
"s_nop 0\n" \
"buffer_load_dwordx4 v4, s[36:39], s61 offen offset:0 sc1 lds\n" \
"v_mov_b32_e64 v13, s59\n" \
"v_mfma_f32_16x16x32_bf16 v[52:55], a[0:3], v[20:23], 0\n" \
"ds_read_b128 v[40:43], v10 offset:3072\n" \
"s_nop 0\n" \
"v_mfma_f32_16x16x32_bf16 v[56:59], a[128:131], v[16:19], 0\n" \
"ds_read_b128 v[44:47], v10 offset:19456\n" \
"s_nop 0\n" \
"v_mfma_f32_16x16x32_bf16 v[60:63], a[128:131], v[20:23], 0\n" \
"s_waitcnt lgkmcnt(4)\n" \
"v_mul_f32 v64, s51, v64\n" \
"v_mul_f32_e64 v65, s51, v65\n" \
"v_mfma_f32_16x16x32_bf16 v[48:51], a[4:7], v[24:27], v[48:51]\n" \
"buffer_load_dwordx4 v4, s[36:39], s61 offen offset:1024 sc1 lds\n" \
"v_mfma_f32_16x16x32_bf16 v[52:55], a[4:7], v[28:31], v[52:55]\n" \
"ds_read_b128 v[16:19], v10 offset:4096\n" \
"v_mul_f32_e64 v66, s51, v66\n" \
"v_mfma_f32_16x16x32_bf16 v[56:59], a[132:135], v[24:27], v[56:59]\n" \
"ds_read_b128 v[20:23], v10 offset:20480\n" \
"v_mul_f32_e64 v67, s51, v67\n" \
"v_mfma_f32_16x16x32_bf16 v[60:63], a[132:135], v[28:31], v[60:63]\n" \
"s_waitcnt lgkmcnt(4)\n" \
"v_mul_f32 v72, s51, v72\n" \
"v_mul_f32_e64 v73, s51, v73\n" \
"v_mfma_f32_16x16x32_bf16 v[48:51], a[8:11], v[32:35], v[48:51]\n" \
"s_add_u32 s63, s59, 2\n" \
"s_min_u32 s63, s63, 63\n" \
"v_mfma_f32_16x16x32_bf16 v[52:55], a[8:11], v[36:39], v[52:55]\n" \
"ds_read_b128 v[24:27], v10 offset:5120\n" \
"s_lshr_b32 s63, s63, 3\n" \
"s_lshl_b32 s63, s63, 7\n" \
"v_mul_f32_e64 v74, s51, v74\n" \
"v_mfma_f32_16x16x32_bf16 v[56:59], a[136:139], v[32:35], v[56:59]\n" \
"ds_read_b128 v[28:31], v10 offset:21504\n" \
"v_mul_f32_e64 v75, s51, v75\n" \
"v_mfma_f32_16x16x32_bf16 v[60:63], a[136:139], v[36:39], v[60:63]\n" \
"s_waitcnt lgkmcnt(4)\n" \
"v_cvt_pk_bf16_f32 v80, v64, v65\n" \
"v_cvt_pk_bf16_f32 v81, v66, v67\n" \
"v_mfma_f32_16x16x32_bf16 v[48:51], a[12:15], v[40:43], v[48:51]\n" \
"buffer_load_dwordx4 v4, s[36:39], s61 offen offset:2048 sc1 lds\n" \
"v_mfma_f32_16x16x32_bf16 v[52:55], a[12:15], v[44:47], v[52:55]\n" \
"ds_read_b128 v[32:35], v10 offset:6144\n" \
"v_cvt_pk_bf16_f32 v82, v72, v73\n" \
"v_mfma_f32_16x16x32_bf16 v[56:59], a[140:143], v[40:43], v[56:59]\n" \
"ds_read_b128 v[36:39], v10 offset:22528\n" \
"v_cvt_pk_bf16_f32 v83, v74, v75\n" \
"v_mfma_f32_16x16x32_bf16 v[60:63], a[140:143], v[44:47], v[60:63]\n" \
"s_waitcnt lgkmcnt(4)\n" \
"buffer_store_dwordx4 v[80:83], v5, s[36:39], s57 offen sc1\n" \
"v_mul_f32_e64 v68, s51, v68\n" \
"v_mfma_f32_16x16x32_bf16 v[48:51], a[16:19], v[16:19], v[48:51]\n" \
"buffer_load_dwordx4 v4, s[36:39], s61 offen offset:3072 sc1 lds\n" \
"v_mfma_f32_16x16x32_bf16 v[52:55], a[16:19], v[20:23], v[52:55]\n" \
"ds_read_b128 v[40:43], v10 offset:7168\n" \
"v_mul_f32_e64 v69, s51, v69\n" \
"v_mfma_f32_16x16x32_bf16 v[56:59], a[144:147], v[16:19], v[56:59]\n" \
"ds_read_b128 v[44:47], v10 offset:23552\n" \
"v_mul_f32_e64 v70, s51, v70\n" \
"v_mfma_f32_16x16x32_bf16 v[60:63], a[144:147], v[20:23], v[60:63]\n" \
"s_waitcnt lgkmcnt(4)\n" \
"v_mul_f32 v71, s51, v71\n" \
"v_mul_f32_e64 v76, s51, v76\n" \
"v_mfma_f32_16x16x32_bf16 v[48:51], a[20:23], v[24:27], v[48:51]\n" \
"buffer_load_dword v15, v14, s[36:39], s63 offen sc1\n" \
"v_mfma_f32_16x16x32_bf16 v[52:55], a[20:23], v[28:31], v[52:55]\n" \
"ds_read_b128 v[16:19], v10 offset:8192\n" \
"v_mul_f32_e64 v77, s51, v77\n" \
"v_mfma_f32_16x16x32_bf16 v[56:59], a[148:151], v[24:27], v[56:59]\n" \
"ds_read_b128 v[20:23], v10 offset:24576\n" \
"v_mul_f32_e64 v78, s51, v78\n" \
"v_mfma_f32_16x16x32_bf16 v[60:63], a[148:151], v[28:31], v[60:63]\n" \
"s_waitcnt lgkmcnt(4)\n" \
"v_mul_f32 v79, s51, v79\n" \
"v_cvt_pk_bf16_f32 v84, v68, v69\n" \
"v_mfma_f32_16x16x32_bf16 v[48:51], a[24:27], v[32:35], v[48:51]\n" \
"s_add_u32 m0, s55, 69632\n" \
"s_add_u32 s61, s61, 4096\n" \
"buffer_load_dwordx4 v4, s[36:39], s61 offen offset:0 sc1 lds\n" \
"v_mfma_f32_16x16x32_bf16 v[52:55], a[24:27], v[36:39], v[52:55]\n" \
"ds_read_b128 v[24:27], v10 offset:9216\n" \
"v_cvt_pk_bf16_f32 v85, v70, v71\n" \
"v_mfma_f32_16x16x32_bf16 v[56:59], a[152:155], v[32:35], v[56:59]\n" \
"ds_read_b128 v[28:31], v10 offset:25600\n" \
"v_cvt_pk_bf16_f32 v86, v76, v77\n" \
"v_mfma_f32_16x16x32_bf16 v[60:63], a[152:155], v[36:39], v[60:63]\n" \
"s_waitcnt lgkmcnt(4)\n" \
"v_cvt_pk_bf16_f32 v87, v78, v79\n" \
"buffer_store_dwordx4 v[84:87], v5, s[36:39], s58 offen sc1\n" \
"v_mfma_f32_16x16x32_bf16 v[48:51], a[28:31], v[40:43], v[48:51]\n" \
"buffer_load_dwordx4 v4, s[36:39], s61 offen offset:1024 sc1 lds\n" \
"v_mfma_f32_16x16x32_bf16 v[52:55], a[28:31], v[44:47], v[52:55]\n" \
"ds_read_b128 v[32:35], v10 offset:10240\n" \
"v_mfma_f32_16x16x32_bf16 v[56:59], a[156:159], v[40:43], v[56:59]\n" \
"ds_read_b128 v[36:39], v10 offset:26624\n" \
"v_mfma_f32_16x16x32_bf16 v[60:63], a[156:159], v[44:47], v[60:63]\n" \
"s_waitcnt lgkmcnt(4)\n" \
"v_mfma_f32_16x16x32_bf16 v[48:51], a[32:35], v[16:19], v[48:51]\n" \
"v_mfma_f32_16x16x32_bf16 v[52:55], a[32:35], v[20:23], v[52:55]\n" \
"ds_read_b128 v[40:43], v10 offset:11264\n" \
"v_mfma_f32_16x16x32_bf16 v[56:59], a[160:163], v[16:19], v[56:59]\n" \
"ds_read_b128 v[44:47], v10 offset:27648\n" \
"v_mfma_f32_16x16x32_bf16 v[60:63], a[160:163], v[20:23], v[60:63]\n" \
"s_waitcnt lgkmcnt(4)\n" \
"v_mfma_f32_16x16x32_bf16 v[48:51], a[36:39], v[24:27], v[48:51]\n" \
"buffer_load_dwordx4 v4, s[36:39], s61 offen offset:2048 sc1 lds\n" \
"v_mfma_f32_16x16x32_bf16 v[52:55], a[36:39], v[28:31], v[52:55]\n" \
"ds_read_b128 v[16:19], v10 offset:12288\n" \
"v_mfma_f32_16x16x32_bf16 v[56:59], a[164:167], v[24:27], v[56:59]\n" \
"ds_read_b128 v[20:23], v10 offset:28672\n" \
"v_mfma_f32_16x16x32_bf16 v[60:63], a[164:167], v[28:31], v[60:63]\n" \
"s_waitcnt lgkmcnt(4)\n" \
"v_mfma_f32_16x16x32_bf16 v[48:51], a[40:43], v[32:35], v[48:51]\n" \
"buffer_load_dwordx4 v4, s[36:39], s61 offen offset:3072 sc1 lds\n" \
"v_mfma_f32_16x16x32_bf16 v[52:55], a[40:43], v[36:39], v[52:55]\n" \
"ds_read_b128 v[24:27], v10 offset:13312\n" \
"v_mfma_f32_16x16x32_bf16 v[56:59], a[168:171], v[32:35], v[56:59]\n" \
"ds_read_b128 v[28:31], v10 offset:29696\n" \
"v_mfma_f32_16x16x32_bf16 v[60:63], a[168:171], v[36:39], v[60:63]\n" \
"s_waitcnt lgkmcnt(4)\n" \
"v_mfma_f32_16x16x32_bf16 v[48:51], a[44:47], v[40:43], v[48:51]\n" \
"v_mfma_f32_16x16x32_bf16 v[52:55], a[44:47], v[44:47], v[52:55]\n" \
"ds_read_b128 v[32:35], v10 offset:14336\n" \
"v_mfma_f32_16x16x32_bf16 v[56:59], a[172:175], v[40:43], v[56:59]\n" \
"ds_read_b128 v[36:39], v10 offset:30720\n" \
"v_mfma_f32_16x16x32_bf16 v[60:63], a[172:175], v[44:47], v[60:63]\n" \
"s_waitcnt lgkmcnt(4)\n" \
"v_mfma_f32_16x16x32_bf16 v[48:51], a[48:51], v[16:19], v[48:51]\n" \
"v_mfma_f32_16x16x32_bf16 v[52:55], a[48:51], v[20:23], v[52:55]\n" \
"ds_read_b128 v[40:43], v10 offset:15360\n" \
"v_mfma_f32_16x16x32_bf16 v[56:59], a[176:179], v[16:19], v[56:59]\n" \
"ds_read_b128 v[44:47], v10 offset:31744\n" \
"v_mfma_f32_16x16x32_bf16 v[60:63], a[176:179], v[20:23], v[60:63]\n" \
"s_waitcnt lgkmcnt(4)\n" \
"v_mfma_f32_16x16x32_bf16 v[48:51], a[52:55], v[24:27], v[48:51]\n" \
"s_waitcnt vmcnt(12)\n" \
"s_barrier\n" \
"v_mfma_f32_16x16x32_bf16 v[52:55], a[52:55], v[28:31], v[52:55]\n" \
"ds_read_b128 v[16:19], v10 offset:32768\n" \
"v_mfma_f32_16x16x32_bf16 v[56:59], a[180:183], v[24:27], v[56:59]\n" \
"ds_read_b128 v[20:23], v10 offset:49152\n" \
"v_mfma_f32_16x16x32_bf16 v[60:63], a[180:183], v[28:31], v[60:63]\n" \
"s_waitcnt lgkmcnt(4)\n" \
"v_mfma_f32_16x16x32_bf16 v[48:51], a[56:59], v[32:35], v[48:51]\n" \
"v_mfma_f32_16x16x32_bf16 v[52:55], a[56:59], v[36:39], v[52:55]\n" \
"ds_read_b128 v[24:27], v10 offset:33792\n" \
"v_mfma_f32_16x16x32_bf16 v[56:59], a[184:187], v[32:35], v[56:59]\n" \
"ds_read_b128 v[28:31], v10 offset:50176\n" \
"v_mfma_f32_16x16x32_bf16 v[60:63], a[184:187], v[36:39], v[60:63]\n" \
"s_waitcnt lgkmcnt(4)\n" \
"v_mfma_f32_16x16x32_bf16 v[48:51], a[60:63], v[40:43], v[48:51]\n" \
"v_mfma_f32_16x16x32_bf16 v[52:55], a[60:63], v[44:47], v[52:55]\n" \
"ds_read_b128 v[32:35], v10 offset:34816\n" \
"v_mfma_f32_16x16x32_bf16 v[56:59], a[188:191], v[40:43], v[56:59]\n" \
"ds_read_b128 v[36:39], v10 offset:51200\n" \
"v_mfma_f32_16x16x32_bf16 v[60:63], a[188:191], v[44:47], v[60:63]\n" \
"s_waitcnt lgkmcnt(4)\n" \
"v_mfma_f32_16x16x32_bf16 v[48:51], a[64:67], v[16:19], v[48:51]\n" \
"s_min_u32 s61, s56, s60\n" \
"s_add_u32 s61, s61, 16384\n" \
"s_add_u32 s56, s56, 0x10000\n" \
"s_add_u32 m0, s55, 98304\n" \
"s_nop 0\n" \
"buffer_load_dwordx4 v4, s[36:39], s61 offen offset:0 sc1 lds\n" \
"v_mfma_f32_16x16x32_bf16 v[52:55], a[64:67], v[20:23], v[52:55]\n" \
"ds_read_b128 v[40:43], v10 offset:35840\n" \
"v_mfma_f32_16x16x32_bf16 v[56:59], a[192:195], v[16:19], v[56:59]\n" \
"ds_read_b128 v[44:47], v10 offset:52224\n" \
"v_mfma_f32_16x16x32_bf16 v[60:63], a[192:195], v[20:23], v[60:63]\n" \
"s_waitcnt lgkmcnt(4)\n" \
"v_mfma_f32_16x16x32_bf16 v[48:51], a[68:71], v[24:27], v[48:51]\n" \
"buffer_load_dwordx4 v4, s[36:39], s61 offen offset:1024 sc1 lds\n" \
"v_mfma_f32_16x16x32_bf16 v[52:55], a[68:71], v[28:31], v[52:55]\n" \
"ds_read_b128 v[16:19], v10 offset:36864\n" \
"v_mfma_f32_16x16x32_bf16 v[56:59], a[196:199], v[24:27], v[56:59]\n" \
"ds_read_b128 v[20:23], v10 offset:53248\n" \
"v_mfma_f32_16x16x32_bf16 v[60:63], a[196:199], v[28:31], v[60:63]\n" \
"s_waitcnt lgkmcnt(4)\n" \
"v_mfma_f32_16x16x32_bf16 v[48:51], a[72:75], v[32:35], v[48:51]\n" \
"v_mfma_f32_16x16x32_bf16 v[52:55], a[72:75], v[36:39], v[52:55]\n" \
"ds_read_b128 v[24:27], v10 offset:37888\n" \
"v_mfma_f32_16x16x32_bf16 v[56:59], a[200:203], v[32:35], v[56:59]\n" \
"ds_read_b128 v[28:31], v10 offset:54272\n" \
"v_mfma_f32_16x16x32_bf16 v[60:63], a[200:203], v[36:39], v[60:63]\n" \
"s_waitcnt lgkmcnt(4)\n" \
"v_mfma_f32_16x16x32_bf16 v[48:51], a[76:79], v[40:43], v[48:51]\n" \
"buffer_load_dwordx4 v4, s[36:39], s61 offen offset:2048 sc1 lds\n" \
"v_mfma_f32_16x16x32_bf16 v[52:55], a[76:79], v[44:47], v[52:55]\n" \
"ds_read_b128 v[32:35], v10 offset:38912\n" \
"v_mfma_f32_16x16x32_bf16 v[56:59], a[204:207], v[40:43], v[56:59]\n" \
"ds_read_b128 v[36:39], v10 offset:55296\n" \
"v_mfma_f32_16x16x32_bf16 v[60:63], a[204:207], v[44:47], v[60:63]\n" \
"s_waitcnt lgkmcnt(4)\n" \
"v_mfma_f32_16x16x32_bf16 v[48:51], a[80:83], v[16:19], v[48:51]\n" \
"buffer_load_dwordx4 v4, s[36:39], s61 offen offset:3072 sc1 lds\n" \
"v_mfma_f32_16x16x32_bf16 v[52:55], a[80:83], v[20:23], v[52:55]\n" \
"ds_read_b128 v[40:43], v10 offset:39936\n" \
"v_mfma_f32_16x16x32_bf16 v[56:59], a[208:211], v[16:19], v[56:59]\n" \
"ds_read_b128 v[44:47], v10 offset:56320\n" \
"v_mfma_f32_16x16x32_bf16 v[60:63], a[208:211], v[20:23], v[60:63]\n" \
"s_waitcnt lgkmcnt(4)\n" \
"v_mfma_f32_16x16x32_bf16 v[48:51], a[84:87], v[24:27], v[48:51]\n" \
"v_mfma_f32_16x16x32_bf16 v[52:55], a[84:87], v[28:31], v[52:55]\n" \
"ds_read_b128 v[16:19], v10 offset:40960\n" \
"v_mfma_f32_16x16x32_bf16 v[56:59], a[212:215], v[24:27], v[56:59]\n" \
"ds_read_b128 v[20:23], v10 offset:57344\n" \
"v_mfma_f32_16x16x32_bf16 v[60:63], a[212:215], v[28:31], v[60:63]\n" \
"s_waitcnt lgkmcnt(4)\n" \
"v_mfma_f32_16x16x32_bf16 v[48:51], a[88:91], v[32:35], v[48:51]\n" \
"s_add_u32 m0, s55, 102400\n" \
"s_add_u32 s61, s61, 4096\n" \
"buffer_load_dwordx4 v4, s[36:39], s61 offen offset:0 sc1 lds\n" \
"v_mfma_f32_16x16x32_bf16 v[52:55], a[88:91], v[36:39], v[52:55]\n" \
"ds_read_b128 v[24:27], v10 offset:41984\n" \
"v_mfma_f32_16x16x32_bf16 v[56:59], a[216:219], v[32:35], v[56:59]\n" \
"ds_read_b128 v[28:31], v10 offset:58368\n" \
"v_mfma_f32_16x16x32_bf16 v[60:63], a[216:219], v[36:39], v[60:63]\n" \
"s_waitcnt lgkmcnt(4)\n" \
"v_mfma_f32_16x16x32_bf16 v[48:51], a[92:95], v[40:43], v[48:51]\n" \
"buffer_load_dwordx4 v4, s[36:39], s61 offen offset:1024 sc1 lds\n" \
"v_mfma_f32_16x16x32_bf16 v[52:55], a[92:95], v[44:47], v[52:55]\n" \
"ds_read_b128 v[32:35], v10 offset:43008\n" \
"v_mfma_f32_16x16x32_bf16 v[56:59], a[220:223], v[40:43], v[56:59]\n" \
"ds_read_b128 v[36:39], v10 offset:59392\n" \
"v_mfma_f32_16x16x32_bf16 v[60:63], a[220:223], v[44:47], v[60:63]\n" \
"s_waitcnt lgkmcnt(4)\n" \
"v_mfma_f32_16x16x32_bf16 v[48:51], a[96:99], v[16:19], v[48:51]\n" \
"v_mfma_f32_16x16x32_bf16 v[52:55], a[96:99], v[20:23], v[52:55]\n" \
"ds_read_b128 v[40:43], v10 offset:44032\n" \
"v_mfma_f32_16x16x32_bf16 v[56:59], a[224:227], v[16:19], v[56:59]\n" \
"ds_read_b128 v[44:47], v10 offset:60416\n" \
"v_mfma_f32_16x16x32_bf16 v[60:63], a[224:227], v[20:23], v[60:63]\n" \
"s_waitcnt lgkmcnt(4)\n" \
"v_mfma_f32_16x16x32_bf16 v[48:51], a[100:103], v[24:27], v[48:51]\n" \
"buffer_load_dwordx4 v4, s[36:39], s61 offen offset:2048 sc1 lds\n" \
"v_mfma_f32_16x16x32_bf16 v[52:55], a[100:103], v[28:31], v[52:55]\n" \
"ds_read_b128 v[16:19], v10 offset:45056\n" \
"v_mfma_f32_16x16x32_bf16 v[56:59], a[228:231], v[24:27], v[56:59]\n" \
"ds_read_b128 v[20:23], v10 offset:61440\n" \
"v_mfma_f32_16x16x32_bf16 v[60:63], a[228:231], v[28:31], v[60:63]\n" \
"s_waitcnt lgkmcnt(4)\n" \
"v_mfma_f32_16x16x32_bf16 v[48:51], a[104:107], v[32:35], v[48:51]\n" \
"buffer_load_dwordx4 v4, s[36:39], s61 offen offset:3072 sc1 lds\n" \
"v_mfma_f32_16x16x32_bf16 v[52:55], a[104:107], v[36:39], v[52:55]\n" \
"ds_read_b128 v[24:27], v10 offset:46080\n" \
"v_mfma_f32_16x16x32_bf16 v[56:59], a[232:235], v[32:35], v[56:59]\n" \
"ds_read_b128 v[28:31], v10 offset:62464\n" \
"v_mfma_f32_16x16x32_bf16 v[60:63], a[232:235], v[36:39], v[60:63]\n" \
"s_waitcnt lgkmcnt(4)\n" \
"v_mfma_f32_16x16x32_bf16 v[48:51], a[108:111], v[40:43], v[48:51]\n" \
"v_mfma_f32_16x16x32_bf16 v[52:55], a[108:111], v[44:47], v[52:55]\n" \
"ds_read_b128 v[32:35], v10 offset:47104\n" \
"v_mfma_f32_16x16x32_bf16 v[56:59], a[236:239], v[40:43], v[56:59]\n" \
"ds_read_b128 v[36:39], v10 offset:63488\n" \
"v_mfma_f32_16x16x32_bf16 v[60:63], a[236:239], v[44:47], v[60:63]\n" \
"s_waitcnt lgkmcnt(4)\n" \
"v_mfma_f32_16x16x32_bf16 v[48:51], a[112:115], v[16:19], v[48:51]\n" \
"v_mfma_f32_16x16x32_bf16 v[52:55], a[112:115], v[20:23], v[52:55]\n" \
"ds_read_b128 v[40:43], v10 offset:48128\n" \
"v_mfma_f32_16x16x32_bf16 v[56:59], a[240:243], v[16:19], v[56:59]\n" \
"ds_read_b128 v[44:47], v10 offset:64512\n" \
"v_mfma_f32_16x16x32_bf16 v[60:63], a[240:243], v[20:23], v[60:63]\n" \
"s_waitcnt lgkmcnt(4)\n" \
"v_mfma_f32_16x16x32_bf16 v[48:51], a[116:119], v[24:27], v[48:51]\n" \
"s_waitcnt vmcnt(8)\n" \
"s_barrier\n" \
"v_mfma_f32_16x16x32_bf16 v[52:55], a[116:119], v[28:31], v[52:55]\n" \
"ds_read_b128 v[16:19], v12 offset:0\n" \
"v_mfma_f32_16x16x32_bf16 v[56:59], a[244:247], v[24:27], v[56:59]\n" \
"ds_read_b128 v[20:23], v12 offset:16384\n" \
"v_mfma_f32_16x16x32_bf16 v[60:63], a[244:247], v[28:31], v[60:63]\n" \
"s_waitcnt lgkmcnt(4)\n" \
"buffer_store_dword v13, v6, s[36:39], 0 offen sc1\n" \
"v_mfma_f32_16x16x32_bf16 v[48:51], a[120:123], v[32:35], v[48:51]\n" \
"v_cmp_gt_u32_e64 vcc, 16, v15\n" \
"v_mfma_f32_16x16x32_bf16 v[52:55], a[120:123], v[36:39], v[52:55]\n" \
"ds_read_b128 v[24:27], v12 offset:1024\n" \
"v_mfma_f32_16x16x32_bf16 v[56:59], a[248:251], v[32:35], v[56:59]\n" \
"ds_read_b128 v[28:31], v12 offset:17408\n" \
"v_mfma_f32_16x16x32_bf16 v[60:63], a[248:251], v[36:39], v[60:63]\n" \
"s_waitcnt lgkmcnt(4)\n" \
"v_mfma_f32_16x16x32_bf16 v[48:51], a[124:127], v[40:43], v[48:51]\n" \
"s_cbranch_vccnz .Lp1qk_m0_%=\n" \
".Lp1qk_m0k_%=:\n" \
"v_mfma_f32_16x16x32_bf16 v[52:55], a[124:127], v[44:47], v[52:55]\n" \
"ds_read_b128 v[32:35], v12 offset:2048\n" \
"v_mfma_f32_16x16x32_bf16 v[56:59], a[252:255], v[40:43], v[56:59]\n" \
"ds_read_b128 v[36:39], v12 offset:18432\n" \
"v_mfma_f32_16x16x32_bf16 v[60:63], a[252:255], v[44:47], v[60:63]\n" \
"s_waitcnt lgkmcnt(4)\n" \
"s_add_u32 s57, s57, s49\n" \
"s_add_u32 s58, s58, s49\n" \
"s_add_u32 s59, s59, 1\n" \
"v_mov_b32_e64 v5, v7\n" \
"v_mov_b32_e64 v6, v11\n" \
"s_cmp_ge_u32 s59, s47\n" \
"s_nop 0\n" \
"s_cbranch_scc1 .Lp1qk_out0_%=\n" \
"v_mfma_f32_16x16x32_bf16 v[64:67], a[0:3], v[16:19], 0\n" \
"s_min_u32 s61, s56, s60\n" \
"s_add_u32 m0, s55, 0\n" \
"s_nop 0\n" \
"buffer_load_dwordx4 v4, s[36:39], s61 offen offset:0 sc1 lds\n" \
"v_mov_b32_e64 v13, s59\n" \
"v_mfma_f32_16x16x32_bf16 v[68:71], a[0:3], v[20:23], 0\n" \
"ds_read_b128 v[40:43], v12 offset:3072\n" \
"s_nop 0\n" \
"v_mfma_f32_16x16x32_bf16 v[72:75], a[128:131], v[16:19], 0\n" \
"ds_read_b128 v[44:47], v12 offset:19456\n" \
"s_nop 0\n" \
"v_mfma_f32_16x16x32_bf16 v[76:79], a[128:131], v[20:23], 0\n" \
"s_waitcnt lgkmcnt(4)\n" \
"v_mul_f32 v48, s51, v48\n" \
"v_mul_f32_e64 v49, s51, v49\n" \
"v_mfma_f32_16x16x32_bf16 v[64:67], a[4:7], v[24:27], v[64:67]\n" \
"buffer_load_dwordx4 v4, s[36:39], s61 offen offset:1024 sc1 lds\n" \
"v_mfma_f32_16x16x32_bf16 v[68:71], a[4:7], v[28:31], v[68:71]\n" \
"ds_read_b128 v[16:19], v12 offset:4096\n" \
"v_mul_f32_e64 v50, s51, v50\n" \
"v_mfma_f32_16x16x32_bf16 v[72:75], a[132:135], v[24:27], v[72:75]\n" \
"ds_read_b128 v[20:23], v12 offset:20480\n" \
"v_mul_f32_e64 v51, s51, v51\n" \
"v_mfma_f32_16x16x32_bf16 v[76:79], a[132:135], v[28:31], v[76:79]\n" \
"s_waitcnt lgkmcnt(4)\n" \
"v_mul_f32 v56, s51, v56\n" \
"v_mul_f32_e64 v57, s51, v57\n" \
"v_mfma_f32_16x16x32_bf16 v[64:67], a[8:11], v[32:35], v[64:67]\n" \
"s_add_u32 s63, s59, 2\n" \
"s_min_u32 s63, s63, 63\n" \
"v_mfma_f32_16x16x32_bf16 v[68:71], a[8:11], v[36:39], v[68:71]\n" \
"ds_read_b128 v[24:27], v12 offset:5120\n" \
"s_lshr_b32 s63, s63, 3\n" \
"s_lshl_b32 s63, s63, 7\n" \
"v_mul_f32_e64 v58, s51, v58\n" \
"v_mfma_f32_16x16x32_bf16 v[72:75], a[136:139], v[32:35], v[72:75]\n" \
"ds_read_b128 v[28:31], v12 offset:21504\n" \
"v_mul_f32_e64 v59, s51, v59\n" \
"v_mfma_f32_16x16x32_bf16 v[76:79], a[136:139], v[36:39], v[76:79]\n" \
"s_waitcnt lgkmcnt(4)\n" \
"v_cvt_pk_bf16_f32 v80, v48, v49\n" \
"v_cvt_pk_bf16_f32 v81, v50, v51\n" \
"v_mfma_f32_16x16x32_bf16 v[64:67], a[12:15], v[40:43], v[64:67]\n" \
"buffer_load_dwordx4 v4, s[36:39], s61 offen offset:2048 sc1 lds\n" \
"v_mfma_f32_16x16x32_bf16 v[68:71], a[12:15], v[44:47], v[68:71]\n" \
"ds_read_b128 v[32:35], v12 offset:6144\n" \
"v_cvt_pk_bf16_f32 v82, v56, v57\n" \
"v_mfma_f32_16x16x32_bf16 v[72:75], a[140:143], v[40:43], v[72:75]\n" \
"ds_read_b128 v[36:39], v12 offset:22528\n" \
"v_cvt_pk_bf16_f32 v83, v58, v59\n" \
"v_mfma_f32_16x16x32_bf16 v[76:79], a[140:143], v[44:47], v[76:79]\n" \
"s_waitcnt lgkmcnt(4)\n" \
"buffer_store_dwordx4 v[80:83], v5, s[36:39], s57 offen sc1\n" \
"v_mul_f32_e64 v52, s51, v52\n" \
"v_mfma_f32_16x16x32_bf16 v[64:67], a[16:19], v[16:19], v[64:67]\n" \
"buffer_load_dwordx4 v4, s[36:39], s61 offen offset:3072 sc1 lds\n" \
"v_mfma_f32_16x16x32_bf16 v[68:71], a[16:19], v[20:23], v[68:71]\n" \
"ds_read_b128 v[40:43], v12 offset:7168\n" \
"v_mul_f32_e64 v53, s51, v53\n" \
"v_mfma_f32_16x16x32_bf16 v[72:75], a[144:147], v[16:19], v[72:75]\n" \
"ds_read_b128 v[44:47], v12 offset:23552\n" \
"v_mul_f32_e64 v54, s51, v54\n" \
"v_mfma_f32_16x16x32_bf16 v[76:79], a[144:147], v[20:23], v[76:79]\n" \
"s_waitcnt lgkmcnt(4)\n" \
"v_mul_f32 v55, s51, v55\n" \
"v_mul_f32_e64 v60, s51, v60\n" \
"v_mfma_f32_16x16x32_bf16 v[64:67], a[20:23], v[24:27], v[64:67]\n" \
"buffer_load_dword v15, v14, s[36:39], s63 offen sc1\n" \
"v_mfma_f32_16x16x32_bf16 v[68:71], a[20:23], v[28:31], v[68:71]\n" \
"ds_read_b128 v[16:19], v12 offset:8192\n" \
"v_mul_f32_e64 v61, s51, v61\n" \
"v_mfma_f32_16x16x32_bf16 v[72:75], a[148:151], v[24:27], v[72:75]\n" \
"ds_read_b128 v[20:23], v12 offset:24576\n" \
"v_mul_f32_e64 v62, s51, v62\n" \
"v_mfma_f32_16x16x32_bf16 v[76:79], a[148:151], v[28:31], v[76:79]\n" \
"s_waitcnt lgkmcnt(4)\n" \
"v_mul_f32 v63, s51, v63\n" \
"v_cvt_pk_bf16_f32 v84, v52, v53\n" \
"v_mfma_f32_16x16x32_bf16 v[64:67], a[24:27], v[32:35], v[64:67]\n" \
"s_add_u32 m0, s55, 4096\n" \
"s_add_u32 s61, s61, 4096\n" \
"buffer_load_dwordx4 v4, s[36:39], s61 offen offset:0 sc1 lds\n" \
"v_mfma_f32_16x16x32_bf16 v[68:71], a[24:27], v[36:39], v[68:71]\n" \
"ds_read_b128 v[24:27], v12 offset:9216\n" \
"v_cvt_pk_bf16_f32 v85, v54, v55\n" \
"v_mfma_f32_16x16x32_bf16 v[72:75], a[152:155], v[32:35], v[72:75]\n" \
"ds_read_b128 v[28:31], v12 offset:25600\n" \
"v_cvt_pk_bf16_f32 v86, v60, v61\n" \
"v_mfma_f32_16x16x32_bf16 v[76:79], a[152:155], v[36:39], v[76:79]\n" \
"s_waitcnt lgkmcnt(4)\n" \
"v_cvt_pk_bf16_f32 v87, v62, v63\n" \
"buffer_store_dwordx4 v[84:87], v5, s[36:39], s58 offen sc1\n" \
"v_mfma_f32_16x16x32_bf16 v[64:67], a[28:31], v[40:43], v[64:67]\n" \
"buffer_load_dwordx4 v4, s[36:39], s61 offen offset:1024 sc1 lds\n" \
"v_mfma_f32_16x16x32_bf16 v[68:71], a[28:31], v[44:47], v[68:71]\n" \
"ds_read_b128 v[32:35], v12 offset:10240\n" \
"v_mfma_f32_16x16x32_bf16 v[72:75], a[156:159], v[40:43], v[72:75]\n" \
"ds_read_b128 v[36:39], v12 offset:26624\n" \
"v_mfma_f32_16x16x32_bf16 v[76:79], a[156:159], v[44:47], v[76:79]\n" \
"s_waitcnt lgkmcnt(4)\n" \
"v_mfma_f32_16x16x32_bf16 v[64:67], a[32:35], v[16:19], v[64:67]\n" \
"v_mfma_f32_16x16x32_bf16 v[68:71], a[32:35], v[20:23], v[68:71]\n" \
"ds_read_b128 v[40:43], v12 offset:11264\n" \
"v_mfma_f32_16x16x32_bf16 v[72:75], a[160:163], v[16:19], v[72:75]\n" \
"ds_read_b128 v[44:47], v12 offset:27648\n" \
"v_mfma_f32_16x16x32_bf16 v[76:79], a[160:163], v[20:23], v[76:79]\n" \
"s_waitcnt lgkmcnt(4)\n" \
"v_mfma_f32_16x16x32_bf16 v[64:67], a[36:39], v[24:27], v[64:67]\n" \
"buffer_load_dwordx4 v4, s[36:39], s61 offen offset:2048 sc1 lds\n" \
"v_mfma_f32_16x16x32_bf16 v[68:71], a[36:39], v[28:31], v[68:71]\n" \
"ds_read_b128 v[16:19], v12 offset:12288\n" \
"v_mfma_f32_16x16x32_bf16 v[72:75], a[164:167], v[24:27], v[72:75]\n" \
"ds_read_b128 v[20:23], v12 offset:28672\n" \
"v_mfma_f32_16x16x32_bf16 v[76:79], a[164:167], v[28:31], v[76:79]\n" \
"s_waitcnt lgkmcnt(4)\n" \
"v_mfma_f32_16x16x32_bf16 v[64:67], a[40:43], v[32:35], v[64:67]\n" \
"buffer_load_dwordx4 v4, s[36:39], s61 offen offset:3072 sc1 lds\n" \
"v_mfma_f32_16x16x32_bf16 v[68:71], a[40:43], v[36:39], v[68:71]\n" \
"ds_read_b128 v[24:27], v12 offset:13312\n" \
"v_mfma_f32_16x16x32_bf16 v[72:75], a[168:171], v[32:35], v[72:75]\n" \
"ds_read_b128 v[28:31], v12 offset:29696\n" \
"v_mfma_f32_16x16x32_bf16 v[76:79], a[168:171], v[36:39], v[76:79]\n" \
"s_waitcnt lgkmcnt(4)\n" \
"v_mfma_f32_16x16x32_bf16 v[64:67], a[44:47], v[40:43], v[64:67]\n" \
"v_mfma_f32_16x16x32_bf16 v[68:71], a[44:47], v[44:47], v[68:71]\n" \
"ds_read_b128 v[32:35], v12 offset:14336\n" \
"v_mfma_f32_16x16x32_bf16 v[72:75], a[172:175], v[40:43], v[72:75]\n" \
"ds_read_b128 v[36:39], v12 offset:30720\n" \
"v_mfma_f32_16x16x32_bf16 v[76:79], a[172:175], v[44:47], v[76:79]\n" \
"s_waitcnt lgkmcnt(4)\n" \
"v_mfma_f32_16x16x32_bf16 v[64:67], a[48:51], v[16:19], v[64:67]\n" \
"v_mfma_f32_16x16x32_bf16 v[68:71], a[48:51], v[20:23], v[68:71]\n" \
"ds_read_b128 v[40:43], v12 offset:15360\n" \
"v_mfma_f32_16x16x32_bf16 v[72:75], a[176:179], v[16:19], v[72:75]\n" \
"ds_read_b128 v[44:47], v12 offset:31744\n" \
"v_mfma_f32_16x16x32_bf16 v[76:79], a[176:179], v[20:23], v[76:79]\n" \
"s_waitcnt lgkmcnt(4)\n" \
"v_mfma_f32_16x16x32_bf16 v[64:67], a[52:55], v[24:27], v[64:67]\n" \
"s_waitcnt vmcnt(12)\n" \
"s_barrier\n" \
"v_mfma_f32_16x16x32_bf16 v[68:71], a[52:55], v[28:31], v[68:71]\n" \
"ds_read_b128 v[16:19], v12 offset:32768\n" \
"v_mfma_f32_16x16x32_bf16 v[72:75], a[180:183], v[24:27], v[72:75]\n" \
"ds_read_b128 v[20:23], v12 offset:49152\n" \
"v_mfma_f32_16x16x32_bf16 v[76:79], a[180:183], v[28:31], v[76:79]\n" \
"s_waitcnt lgkmcnt(4)\n" \
"v_mfma_f32_16x16x32_bf16 v[64:67], a[56:59], v[32:35], v[64:67]\n" \
"v_mfma_f32_16x16x32_bf16 v[68:71], a[56:59], v[36:39], v[68:71]\n" \
"ds_read_b128 v[24:27], v12 offset:33792\n" \
"v_mfma_f32_16x16x32_bf16 v[72:75], a[184:187], v[32:35], v[72:75]\n" \
"ds_read_b128 v[28:31], v12 offset:50176\n" \
"v_mfma_f32_16x16x32_bf16 v[76:79], a[184:187], v[36:39], v[76:79]\n" \
"s_waitcnt lgkmcnt(4)\n" \
"v_mfma_f32_16x16x32_bf16 v[64:67], a[60:63], v[40:43], v[64:67]\n" \
"v_mfma_f32_16x16x32_bf16 v[68:71], a[60:63], v[44:47], v[68:71]\n" \
"ds_read_b128 v[32:35], v12 offset:34816\n" \
"v_mfma_f32_16x16x32_bf16 v[72:75], a[188:191], v[40:43], v[72:75]\n" \
"ds_read_b128 v[36:39], v12 offset:51200\n" \
"v_mfma_f32_16x16x32_bf16 v[76:79], a[188:191], v[44:47], v[76:79]\n" \
"s_waitcnt lgkmcnt(4)\n" \
"v_mfma_f32_16x16x32_bf16 v[64:67], a[64:67], v[16:19], v[64:67]\n" \
"s_min_u32 s61, s56, s60\n" \
"s_add_u32 s61, s61, 16384\n" \
"s_add_u32 s56, s56, 0x10000\n" \
"s_add_u32 m0, s55, 32768\n" \
"s_nop 0\n" \
"buffer_load_dwordx4 v4, s[36:39], s61 offen offset:0 sc1 lds\n" \
"v_mfma_f32_16x16x32_bf16 v[68:71], a[64:67], v[20:23], v[68:71]\n" \
"ds_read_b128 v[40:43], v12 offset:35840\n" \
"v_mfma_f32_16x16x32_bf16 v[72:75], a[192:195], v[16:19], v[72:75]\n" \
"ds_read_b128 v[44:47], v12 offset:52224\n" \
"v_mfma_f32_16x16x32_bf16 v[76:79], a[192:195], v[20:23], v[76:79]\n" \
"s_waitcnt lgkmcnt(4)\n" \
"v_mfma_f32_16x16x32_bf16 v[64:67], a[68:71], v[24:27], v[64:67]\n" \
"buffer_load_dwordx4 v4, s[36:39], s61 offen offset:1024 sc1 lds\n" \
"v_mfma_f32_16x16x32_bf16 v[68:71], a[68:71], v[28:31], v[68:71]\n" \
"ds_read_b128 v[16:19], v12 offset:36864\n" \
"v_mfma_f32_16x16x32_bf16 v[72:75], a[196:199], v[24:27], v[72:75]\n" \
"ds_read_b128 v[20:23], v12 offset:53248\n" \
"v_mfma_f32_16x16x32_bf16 v[76:79], a[196:199], v[28:31], v[76:79]\n" \
"s_waitcnt lgkmcnt(4)\n" \
"v_mfma_f32_16x16x32_bf16 v[64:67], a[72:75], v[32:35], v[64:67]\n" \
"v_mfma_f32_16x16x32_bf16 v[68:71], a[72:75], v[36:39], v[68:71]\n" \
"ds_read_b128 v[24:27], v12 offset:37888\n" \
"v_mfma_f32_16x16x32_bf16 v[72:75], a[200:203], v[32:35], v[72:75]\n" \
"ds_read_b128 v[28:31], v12 offset:54272\n" \
"v_mfma_f32_16x16x32_bf16 v[76:79], a[200:203], v[36:39], v[76:79]\n" \
"s_waitcnt lgkmcnt(4)\n" \
"v_mfma_f32_16x16x32_bf16 v[64:67], a[76:79], v[40:43], v[64:67]\n" \
"buffer_load_dwordx4 v4, s[36:39], s61 offen offset:2048 sc1 lds\n" \
"v_mfma_f32_16x16x32_bf16 v[68:71], a[76:79], v[44:47], v[68:71]\n" \
"ds_read_b128 v[32:35], v12 offset:38912\n" \
"v_mfma_f32_16x16x32_bf16 v[72:75], a[204:207], v[40:43], v[72:75]\n" \
"ds_read_b128 v[36:39], v12 offset:55296\n" \
"v_mfma_f32_16x16x32_bf16 v[76:79], a[204:207], v[44:47], v[76:79]\n" \
"s_waitcnt lgkmcnt(4)\n" \
"v_mfma_f32_16x16x32_bf16 v[64:67], a[80:83], v[16:19], v[64:67]\n" \
"buffer_load_dwordx4 v4, s[36:39], s61 offen offset:3072 sc1 lds\n" \
"v_mfma_f32_16x16x32_bf16 v[68:71], a[80:83], v[20:23], v[68:71]\n" \
"ds_read_b128 v[40:43], v12 offset:39936\n" \
"v_mfma_f32_16x16x32_bf16 v[72:75], a[208:211], v[16:19], v[72:75]\n" \
"ds_read_b128 v[44:47], v12 offset:56320\n" \
"v_mfma_f32_16x16x32_bf16 v[76:79], a[208:211], v[20:23], v[76:79]\n" \
"s_waitcnt lgkmcnt(4)\n" \
"v_mfma_f32_16x16x32_bf16 v[64:67], a[84:87], v[24:27], v[64:67]\n" \
"v_mfma_f32_16x16x32_bf16 v[68:71], a[84:87], v[28:31], v[68:71]\n" \
"ds_read_b128 v[16:19], v12 offset:40960\n" \
"v_mfma_f32_16x16x32_bf16 v[72:75], a[212:215], v[24:27], v[72:75]\n" \
"ds_read_b128 v[20:23], v12 offset:57344\n" \
"v_mfma_f32_16x16x32_bf16 v[76:79], a[212:215], v[28:31], v[76:79]\n" \
"s_waitcnt lgkmcnt(4)\n" \
"v_mfma_f32_16x16x32_bf16 v[64:67], a[88:91], v[32:35], v[64:67]\n" \
"s_add_u32 m0, s55, 36864\n" \
"s_add_u32 s61, s61, 4096\n" \
"buffer_load_dwordx4 v4, s[36:39], s61 offen offset:0 sc1 lds\n" \
"v_mfma_f32_16x16x32_bf16 v[68:71], a[88:91], v[36:39], v[68:71]\n" \
"ds_read_b128 v[24:27], v12 offset:41984\n" \
"v_mfma_f32_16x16x32_bf16 v[72:75], a[216:219], v[32:35], v[72:75]\n" \
"ds_read_b128 v[28:31], v12 offset:58368\n" \
"v_mfma_f32_16x16x32_bf16 v[76:79], a[216:219], v[36:39], v[76:79]\n" \
"s_waitcnt lgkmcnt(4)\n" \
"v_mfma_f32_16x16x32_bf16 v[64:67], a[92:95], v[40:43], v[64:67]\n" \
"buffer_load_dwordx4 v4, s[36:39], s61 offen offset:1024 sc1 lds\n" \
"v_mfma_f32_16x16x32_bf16 v[68:71], a[92:95], v[44:47], v[68:71]\n" \
"ds_read_b128 v[32:35], v12 offset:43008\n" \
"v_mfma_f32_16x16x32_bf16 v[72:75], a[220:223], v[40:43], v[72:75]\n" \
"ds_read_b128 v[36:39], v12 offset:59392\n" \
"v_mfma_f32_16x16x32_bf16 v[76:79], a[220:223], v[44:47], v[76:79]\n" \
"s_waitcnt lgkmcnt(4)\n" \
"v_mfma_f32_16x16x32_bf16 v[64:67], a[96:99], v[16:19], v[64:67]\n" \
"v_mfma_f32_16x16x32_bf16 v[68:71], a[96:99], v[20:23], v[68:71]\n" \
"ds_read_b128 v[40:43], v12 offset:44032\n" \
"v_mfma_f32_16x16x32_bf16 v[72:75], a[224:227], v[16:19], v[72:75]\n" \
"ds_read_b128 v[44:47], v12 offset:60416\n" \
"v_mfma_f32_16x16x32_bf16 v[76:79], a[224:227], v[20:23], v[76:79]\n" \
"s_waitcnt lgkmcnt(4)\n" \
"v_mfma_f32_16x16x32_bf16 v[64:67], a[100:103], v[24:27], v[64:67]\n" \
"buffer_load_dwordx4 v4, s[36:39], s61 offen offset:2048 sc1 lds\n" \
"v_mfma_f32_16x16x32_bf16 v[68:71], a[100:103], v[28:31], v[68:71]\n" \
"ds_read_b128 v[16:19], v12 offset:45056\n" \
"v_mfma_f32_16x16x32_bf16 v[72:75], a[228:231], v[24:27], v[72:75]\n" \
"ds_read_b128 v[20:23], v12 offset:61440\n" \
"v_mfma_f32_16x16x32_bf16 v[76:79], a[228:231], v[28:31], v[76:79]\n" \
"s_waitcnt lgkmcnt(4)\n" \
"v_mfma_f32_16x16x32_bf16 v[64:67], a[104:107], v[32:35], v[64:67]\n" \
"buffer_load_dwordx4 v4, s[36:39], s61 offen offset:3072 sc1 lds\n" \
"v_mfma_f32_16x16x32_bf16 v[68:71], a[104:107], v[36:39], v[68:71]\n" \
"ds_read_b128 v[24:27], v12 offset:46080\n" \
"v_mfma_f32_16x16x32_bf16 v[72:75], a[232:235], v[32:35], v[72:75]\n" \
"ds_read_b128 v[28:31], v12 offset:62464\n" \
"v_mfma_f32_16x16x32_bf16 v[76:79], a[232:235], v[36:39], v[76:79]\n" \
"s_waitcnt lgkmcnt(4)\n" \
"v_mfma_f32_16x16x32_bf16 v[64:67], a[108:111], v[40:43], v[64:67]\n" \
"v_mfma_f32_16x16x32_bf16 v[68:71], a[108:111], v[44:47], v[68:71]\n" \
"ds_read_b128 v[32:35], v12 offset:47104\n" \
"v_mfma_f32_16x16x32_bf16 v[72:75], a[236:239], v[40:43], v[72:75]\n" \
"ds_read_b128 v[36:39], v12 offset:63488\n" \
"v_mfma_f32_16x16x32_bf16 v[76:79], a[236:239], v[44:47], v[76:79]\n" \
"s_waitcnt lgkmcnt(4)\n" \
"v_mfma_f32_16x16x32_bf16 v[64:67], a[112:115], v[16:19], v[64:67]\n" \
"v_mfma_f32_16x16x32_bf16 v[68:71], a[112:115], v[20:23], v[68:71]\n" \
"ds_read_b128 v[40:43], v12 offset:48128\n" \
"v_mfma_f32_16x16x32_bf16 v[72:75], a[240:243], v[16:19], v[72:75]\n" \
"ds_read_b128 v[44:47], v12 offset:64512\n" \
"v_mfma_f32_16x16x32_bf16 v[76:79], a[240:243], v[20:23], v[76:79]\n" \
"s_waitcnt lgkmcnt(4)\n" \
"v_mfma_f32_16x16x32_bf16 v[64:67], a[116:119], v[24:27], v[64:67]\n" \
"s_waitcnt vmcnt(8)\n" \
"s_barrier\n" \
"v_mfma_f32_16x16x32_bf16 v[68:71], a[116:119], v[28:31], v[68:71]\n" \
"ds_read_b128 v[16:19], v10 offset:0\n" \
"v_mfma_f32_16x16x32_bf16 v[72:75], a[244:247], v[24:27], v[72:75]\n" \
"ds_read_b128 v[20:23], v10 offset:16384\n" \
"v_mfma_f32_16x16x32_bf16 v[76:79], a[244:247], v[28:31], v[76:79]\n" \
"s_waitcnt lgkmcnt(4)\n" \
"buffer_store_dword v13, v6, s[36:39], 0 offen sc1\n" \
"v_mfma_f32_16x16x32_bf16 v[64:67], a[120:123], v[32:35], v[64:67]\n" \
"v_cmp_gt_u32_e64 vcc, 16, v15\n" \
"v_mfma_f32_16x16x32_bf16 v[68:71], a[120:123], v[36:39], v[68:71]\n" \
"ds_read_b128 v[24:27], v10 offset:1024\n" \
"v_mfma_f32_16x16x32_bf16 v[72:75], a[248:251], v[32:35], v[72:75]\n" \
"ds_read_b128 v[28:31], v10 offset:17408\n" \
"v_mfma_f32_16x16x32_bf16 v[76:79], a[248:251], v[36:39], v[76:79]\n" \
"s_waitcnt lgkmcnt(4)\n" \
"v_mfma_f32_16x16x32_bf16 v[64:67], a[124:127], v[40:43], v[64:67]\n" \
"s_cbranch_vccnz .Lp1qk_m1_%=\n" \
".Lp1qk_m1k_%=:\n" \
"v_mfma_f32_16x16x32_bf16 v[68:71], a[124:127], v[44:47], v[68:71]\n" \
"ds_read_b128 v[32:35], v10 offset:2048\n" \
"v_mfma_f32_16x16x32_bf16 v[72:75], a[252:255], v[40:43], v[72:75]\n" \
"ds_read_b128 v[36:39], v10 offset:18432\n" \
"v_mfma_f32_16x16x32_bf16 v[76:79], a[252:255], v[44:47], v[76:79]\n" \
"s_waitcnt lgkmcnt(4)\n" \
"s_add_u32 s57, s57, s49\n" \
"s_add_u32 s58, s58, s49\n" \
"s_add_u32 s59, s59, 1\n" \
"v_mov_b32_e64 v5, v7\n" \
"v_mov_b32_e64 v6, v11\n" \
"s_cmp_ge_u32 s59, s47\n" \
"s_nop 0\n" \
"s_cbranch_scc0 .Lp1qk_loop_%=\n" \
".Lp1qk_out1_%=:\n" \
"s_nop 7\n" \
"s_nop 7\n" \
"v_mul_f32 v64, s51, v64\n" \
"v_mul_f32 v65, s51, v65\n" \
"v_mul_f32 v66, s51, v66\n" \
"v_mul_f32 v67, s51, v67\n" \
"v_mul_f32 v72, s51, v72\n" \
"v_mul_f32 v73, s51, v73\n" \
"v_mul_f32 v74, s51, v74\n" \
"v_mul_f32 v75, s51, v75\n" \
"v_cvt_pk_bf16_f32 v80, v64, v65\n" \
"v_cvt_pk_bf16_f32 v81, v66, v67\n" \
"v_cvt_pk_bf16_f32 v82, v72, v73\n" \
"v_cvt_pk_bf16_f32 v83, v74, v75\n" \
"buffer_store_dwordx4 v[80:83], v5, s[36:39], s57 offen sc1\n" \
"v_mul_f32 v68, s51, v68\n" \
"v_mul_f32 v69, s51, v69\n" \
"v_mul_f32 v70, s51, v70\n" \
"v_mul_f32 v71, s51, v71\n" \
"v_mul_f32 v76, s51, v76\n" \
"v_mul_f32 v77, s51, v77\n" \
"v_mul_f32 v78, s51, v78\n" \
"v_mul_f32 v79, s51, v79\n" \
"v_cvt_pk_bf16_f32 v84, v68, v69\n" \
"v_cvt_pk_bf16_f32 v85, v70, v71\n" \
"v_cvt_pk_bf16_f32 v86, v76, v77\n" \
"v_cvt_pk_bf16_f32 v87, v78, v79\n" \
"buffer_store_dwordx4 v[84:87], v5, s[36:39], s58 offen sc1\n" \
"s_branch .Lp1qk_end_%=\n" \
".Lp1qk_out0_%=:\n" \
"s_nop 7\n" \
"s_nop 7\n" \
"v_mul_f32 v48, s51, v48\n" \
"v_mul_f32 v49, s51, v49\n" \
"v_mul_f32 v50, s51, v50\n" \
"v_mul_f32 v51, s51, v51\n" \
"v_mul_f32 v56, s51, v56\n" \
"v_mul_f32 v57, s51, v57\n" \
"v_mul_f32 v58, s51, v58\n" \
"v_mul_f32 v59, s51, v59\n" \
"v_cvt_pk_bf16_f32 v80, v48, v49\n" \
"v_cvt_pk_bf16_f32 v81, v50, v51\n" \
"v_cvt_pk_bf16_f32 v82, v56, v57\n" \
"v_cvt_pk_bf16_f32 v83, v58, v59\n" \
"buffer_store_dwordx4 v[80:83], v5, s[36:39], s57 offen sc1\n" \
"v_mul_f32 v52, s51, v52\n" \
"v_mul_f32 v53, s51, v53\n" \
"v_mul_f32 v54, s51, v54\n" \
"v_mul_f32 v55, s51, v55\n" \
"v_mul_f32 v60, s51, v60\n" \
"v_mul_f32 v61, s51, v61\n" \
"v_mul_f32 v62, s51, v62\n" \
"v_mul_f32 v63, s51, v63\n" \
"v_cvt_pk_bf16_f32 v84, v52, v53\n" \
"v_cvt_pk_bf16_f32 v85, v54, v55\n" \
"v_cvt_pk_bf16_f32 v86, v60, v61\n" \
"v_cvt_pk_bf16_f32 v87, v62, v63\n" \
"buffer_store_dwordx4 v[84:87], v5, s[36:39], s58 offen sc1\n" \
".Lp1qk_end_%=:\n" \
"s_waitcnt vmcnt(0) lgkmcnt(0)\n" \
"s_barrier\n" \
"v_mov_b32 v13, s59\n" \
"buffer_store_dword v13, v6, s[36:39], 0 offen sc1\n" \
"s_waitcnt vmcnt(0) lgkmcnt(0)\n" \
"s_branch .Lp1qk_x_%=\n" \
".Lp1qk_l0_%=:\n" \
"s_nop 7\n" \
"s_nop 7\n" \
"s_waitcnt vmcnt(0) lgkmcnt(0)\n" \
"s_mov_b32 s62, 0\n" \
".Lp1qk_l0a_%=:\n" \
"buffer_load_dword v15, v14, s[36:39], s63 offen sc1\n" \
"s_waitcnt vmcnt(0)\n" \
"v_cmp_gt_u32 vcc, 16, v15\n" \
"s_nop 4\n" \
"s_cbranch_vccz .Lp1qk_l0k_%=\n" \
"s_sleep 4\n" \
"s_add_u32 s62, s62, 1\n" \
"s_cmp_lt_u32 s62, 0x10000\n" \
"s_cbranch_scc1 .Lp1qk_l0a_%=\n" \
"v_mov_b32 v15, 256\n" \
"buffer_store_dword v15, v15, s[36:39], 0 offen sc1\n" \
"s_waitcnt vmcnt(0) lgkmcnt(0)\n" \
"s_branch .Lp1qk_l0k_%=\n" \
".Lp1qk_l1_%=:\n" \
"s_nop 7\n" \
"s_nop 7\n" \
"s_waitcnt vmcnt(0) lgkmcnt(0)\n" \
"s_mov_b32 s62, 0\n" \
".Lp1qk_l1a_%=:\n" \
"buffer_load_dword v15, v14, s[36:39], s63 offen sc1\n" \
"s_waitcnt vmcnt(0)\n" \
"v_cmp_gt_u32 vcc, 16, v15\n" \
"s_nop 4\n" \
"s_cbranch_vccz .Lp1qk_l1k_%=\n" \
"s_sleep 4\n" \
"s_add_u32 s62, s62, 1\n" \
"s_cmp_lt_u32 s62, 0x10000\n" \
"s_cbranch_scc1 .Lp1qk_l1a_%=\n" \
"v_mov_b32 v15, 256\n" \
"buffer_store_dword v15, v15, s[36:39], 0 offen sc1\n" \
"s_waitcnt vmcnt(0) lgkmcnt(0)\n" \
"s_branch .Lp1qk_l1k_%=\n" \
".Lp1qk_m0_%=:\n" \
"s_nop 7\n" \
"s_nop 7\n" \
"s_waitcnt vmcnt(0) lgkmcnt(0)\n" \
"s_mov_b32 s62, 0\n" \
".Lp1qk_m0a_%=:\n" \
"buffer_load_dword v15, v14, s[36:39], s63 offen sc1\n" \
"s_waitcnt vmcnt(0)\n" \
"v_cmp_gt_u32 vcc, 16, v15\n" \
"s_nop 4\n" \
"s_cbranch_vccz .Lp1qk_m0k_%=\n" \
"s_sleep 4\n" \
"s_add_u32 s62, s62, 1\n" \
"s_cmp_lt_u32 s62, 0x10000\n" \
"s_cbranch_scc1 .Lp1qk_m0a_%=\n" \
"v_mov_b32 v15, 256\n" \
"buffer_store_dword v15, v15, s[36:39], 0 offen sc1\n" \
"s_waitcnt vmcnt(0) lgkmcnt(0)\n" \
"s_branch .Lp1qk_m0k_%=\n" \
".Lp1qk_m1_%=:\n" \
"s_nop 7\n" \
"s_nop 7\n" \
"s_waitcnt vmcnt(0) lgkmcnt(0)\n" \
"s_mov_b32 s62, 0\n" \
".Lp1qk_m1a_%=:\n" \
"buffer_load_dword v15, v14, s[36:39], s63 offen sc1\n" \
"s_waitcnt vmcnt(0)\n" \
"v_cmp_gt_u32 vcc, 16, v15\n" \
"s_nop 4\n" \
"s_cbranch_vccz .Lp1qk_m1k_%=\n" \
"s_sleep 4\n" \
"s_add_u32 s62, s62, 1\n" \
"s_cmp_lt_u32 s62, 0x10000\n" \
"s_cbranch_scc1 .Lp1qk_m1a_%=\n" \
"v_mov_b32 v15, 256\n" \
"buffer_store_dword v15, v15, s[36:39], 0 offen sc1\n" \
"s_waitcnt vmcnt(0) lgkmcnt(0)\n" \
"s_branch .Lp1qk_m1k_%=\n" \
".Lp1qk_x_%=:\n"
#define ASM_P1_V \
"s_nop 7\n" \
"s_nop 7\n" \
"s_lshr_b32 s52, s48, 1\n" \
"s_lshl_b32 s52, s52, 13\n" \
"s_and_b32 s53, s48, 1\n" \
"s_lshl_b32 s53, s53, 10\n" \
"s_add_u32 s52, s52, s53\n" \
"s_mov_b32 s54, 0\n" \
"s_add_u32 m0, s52, 0\n" \
"s_nop 0\n" \
"buffer_load_dwordx4 v1, s[40:43], s54 offen nt lds\n" \
"s_add_u32 s54, s54, s44\n" \
"s_add_u32 m0, s52, 2048\n" \
"s_nop 0\n" \
"buffer_load_dwordx4 v1, s[40:43], s54 offen nt lds\n" \
"s_add_u32 s54, s54, s44\n" \
"s_add_u32 m0, s52, 4096\n" \
"s_nop 0\n" \
"buffer_load_dwordx4 v1, s[40:43], s54 offen nt lds\n" \
"s_add_u32 s54, s54, s44\n" \
"s_add_u32 m0, s52, 6144\n" \
"s_nop 0\n" \
"buffer_load_dwordx4 v1, s[40:43], s54 offen nt lds\n" \
"s_add_u32 s54, s54, s44\n" \
"s_add_u32 m0, s52, 16384\n" \
"s_nop 0\n" \
"buffer_load_dwordx4 v1, s[40:43], s54 offen nt lds\n" \
"s_add_u32 s54, s54, s44\n" \
"s_add_u32 m0, s52, 18432\n" \
"s_nop 0\n" \
"buffer_load_dwordx4 v1, s[40:43], s54 offen nt lds\n" \
"s_add_u32 s54, s54, s44\n" \
"s_add_u32 m0, s52, 20480\n" \
"s_nop 0\n" \
"buffer_load_dwordx4 v1, s[40:43], s54 offen nt lds\n" \
"s_add_u32 s54, s54, s44\n" \
"s_add_u32 m0, s52, 22528\n" \
"s_nop 0\n" \
"buffer_load_dwordx4 v1, s[40:43], s54 offen nt lds\n" \
"s_add_u32 s54, s54, s44\n" \
"s_add_u32 m0, s52, 32768\n" \
"s_nop 0\n" \
"buffer_load_dwordx4 v1, s[40:43], s54 offen nt lds\n" \
"s_add_u32 s54, s54, s44\n" \
"s_add_u32 m0, s52, 34816\n" \
"s_nop 0\n" \
"buffer_load_dwordx4 v1, s[40:43], s54 offen nt lds\n" \
"s_add_u32 s54, s54, s44\n" \
"s_add_u32 m0, s52, 36864\n" \
"s_nop 0\n" \
"buffer_load_dwordx4 v1, s[40:43], s54 offen nt lds\n" \
"s_add_u32 s54, s54, s44\n" \
"s_add_u32 m0, s52, 38912\n" \
"s_nop 0\n" \
"buffer_load_dwordx4 v1, s[40:43], s54 offen nt lds\n" \
"s_add_u32 s54, s54, s44\n" \
"s_add_u32 m0, s52, 49152\n" \
"s_nop 0\n" \
"buffer_load_dwordx4 v1, s[40:43], s54 offen nt lds\n" \
"s_add_u32 s54, s54, s44\n" \
"s_add_u32 m0, s52, 51200\n" \
"s_nop 0\n" \
"buffer_load_dwordx4 v1, s[40:43], s54 offen nt lds\n" \
"s_add_u32 s54, s54, s44\n" \
"s_add_u32 m0, s52, 53248\n" \
"s_nop 0\n" \
"buffer_load_dwordx4 v1, s[40:43], s54 offen nt lds\n" \
"s_add_u32 s54, s54, s44\n" \
"s_add_u32 m0, s52, 55296\n" \
"s_nop 0\n" \
"buffer_load_dwordx4 v1, s[40:43], s54 offen nt lds\n" \
"s_add_u32 s54, s54, s44\n" \
"s_add_u32 m0, s52, 65536\n" \
"s_nop 0\n" \
"buffer_load_dwordx4 v1, s[40:43], s54 offen nt lds\n" \
"s_add_u32 s54, s54, s44\n" \
"s_add_u32 m0, s52, 67584\n" \
"s_nop 0\n" \
"buffer_load_dwordx4 v1, s[40:43], s54 offen nt lds\n" \
"s_add_u32 s54, s54, s44\n" \
"s_add_u32 m0, s52, 69632\n" \
"s_nop 0\n" \
"buffer_load_dwordx4 v1, s[40:43], s54 offen nt lds\n" \
"s_add_u32 s54, s54, s44\n" \
"s_add_u32 m0, s52, 71680\n" \
"s_nop 0\n" \
"buffer_load_dwordx4 v1, s[40:43], s54 offen nt lds\n" \
"s_add_u32 s54, s54, s44\n" \
"s_add_u32 m0, s52, 81920\n" \
"s_nop 0\n" \
"buffer_load_dwordx4 v1, s[40:43], s54 offen nt lds\n" \
"s_add_u32 s54, s54, s44\n" \
"s_add_u32 m0, s52, 83968\n" \
"s_nop 0\n" \
"buffer_load_dwordx4 v1, s[40:43], s54 offen nt lds\n" \
"s_add_u32 s54, s54, s44\n" \
"s_add_u32 m0, s52, 86016\n" \
"s_nop 0\n" \
"buffer_load_dwordx4 v1, s[40:43], s54 offen nt lds\n" \
"s_add_u32 s54, s54, s44\n" \
"s_add_u32 m0, s52, 88064\n" \
"s_nop 0\n" \
"buffer_load_dwordx4 v1, s[40:43], s54 offen nt lds\n" \
"s_add_u32 s54, s54, s44\n" \
"s_add_u32 m0, s52, 98304\n" \
"s_nop 0\n" \
"buffer_load_dwordx4 v1, s[40:43], s54 offen nt lds\n" \
"s_add_u32 s54, s54, s44\n" \
"s_add_u32 m0, s52, 100352\n" \
"s_nop 0\n" \
"buffer_load_dwordx4 v1, s[40:43], s54 offen nt lds\n" \
"s_add_u32 s54, s54, s44\n" \
"s_add_u32 m0, s52, 102400\n" \
"s_nop 0\n" \
"buffer_load_dwordx4 v1, s[40:43], s54 offen nt lds\n" \
"s_add_u32 s54, s54, s44\n" \
"s_add_u32 m0, s52, 104448\n" \
"s_nop 0\n" \
"buffer_load_dwordx4 v1, s[40:43], s54 offen nt lds\n" \
"s_add_u32 s54, s54, s44\n" \
"s_add_u32 m0, s52, 114688\n" \
"s_nop 0\n" \
"buffer_load_dwordx4 v1, s[40:43], s54 offen nt lds\n" \
"s_add_u32 s54, s54, s44\n" \
"s_add_u32 m0, s52, 116736\n" \
"s_nop 0\n" \
"buffer_load_dwordx4 v1, s[40:43], s54 offen nt lds\n" \
"s_add_u32 s54, s54, s44\n" \
"s_add_u32 m0, s52, 118784\n" \
"s_nop 0\n" \
"buffer_load_dwordx4 v1, s[40:43], s54 offen nt lds\n" \
"s_add_u32 s54, s54, s44\n" \
"s_add_u32 m0, s52, 120832\n" \
"s_nop 0\n" \
"buffer_load_dwordx4 v1, s[40:43], s54 offen nt lds\n" \
"s_add_u32 s54, s54, s44\n" \
"v_add_u32 v8, 0x10000, v2\n" \
"v_add_u32 v9, 0x10000, v3\n" \
"s_waitcnt vmcnt(28)\n" \
"s_barrier\n" \
"ds_read_b64_tr_b16 a[0:1], v2 offset:0\n" \
"ds_read_b64_tr_b16 a[2:3], v2 offset:512\n" \
"ds_read_b64_tr_b16 a[128:129], v3 offset:0\n" \
"ds_read_b64_tr_b16 a[130:131], v3 offset:512\n" \
"ds_read_b64_tr_b16 a[4:5], v2 offset:4096\n" \
"ds_read_b64_tr_b16 a[6:7], v2 offset:4608\n" \
"ds_read_b64_tr_b16 a[132:133], v3 offset:4096\n" \
"ds_read_b64_tr_b16 a[134:135], v3 offset:4608\n" \
"s_waitcnt lgkmcnt(0)\n" \
"s_barrier\n" \
"s_add_u32 m0, s52, 0\n" \
"s_nop 0\n" \
"buffer_load_dwordx4 v1, s[40:43], s54 offen nt lds\n" \
"s_add_u32 s54, s54, s44\n" \
"s_add_u32 m0, s52, 2048\n" \
"s_nop 0\n" \
"buffer_load_dwordx4 v1, s[40:43], s54 offen nt lds\n" \
"s_add_u32 s54, s54, s44\n" \
"s_add_u32 m0, s52, 4096\n" \
"s_nop 0\n" \
"buffer_load_dwordx4 v1, s[40:43], s54 offen nt lds\n" \
"s_add_u32 s54, s54, s44\n" \
"s_add_u32 m0, s52, 6144\n" \
"s_nop 0\n" \
"buffer_load_dwordx4 v1, s[40:43], s54 offen nt lds\n" \
"s_add_u32 s54, s54, s44\n" \
"s_waitcnt vmcnt(28)\n" \
"s_barrier\n" \
"ds_read_b64_tr_b16 a[8:9], v2 offset:16384\n" \
"ds_read_b64_tr_b16 a[10:11], v2 offset:16896\n" \
"ds_read_b64_tr_b16 a[136:137], v3 offset:16384\n" \
"ds_read_b64_tr_b16 a[138:139], v3 offset:16896\n" \
"ds_read_b64_tr_b16 a[12:13], v2 offset:20480\n" \
"ds_read_b64_tr_b16 a[14:15], v2 offset:20992\n" \
"ds_read_b64_tr_b16 a[140:141], v3 offset:20480\n" \
"ds_read_b64_tr_b16 a[142:143], v3 offset:20992\n" \
"s_waitcnt lgkmcnt(0)\n" \
"s_barrier\n" \
"s_add_u32 m0, s52, 16384\n" \
"s_nop 0\n" \
"buffer_load_dwordx4 v1, s[40:43], s54 offen nt lds\n" \
"s_add_u32 s54, s54, s44\n" \
"s_add_u32 m0, s52, 18432\n" \
"s_nop 0\n" \
"buffer_load_dwordx4 v1, s[40:43], s54 offen nt lds\n" \
"s_add_u32 s54, s54, s44\n" \
"s_add_u32 m0, s52, 20480\n" \
"s_nop 0\n" \
"buffer_load_dwordx4 v1, s[40:43], s54 offen nt lds\n" \
"s_add_u32 s54, s54, s44\n" \
"s_add_u32 m0, s52, 22528\n" \
"s_nop 0\n" \
"buffer_load_dwordx4 v1, s[40:43], s54 offen nt lds\n" \
"s_add_u32 s54, s54, s44\n" \
"s_waitcnt vmcnt(28)\n" \
"s_barrier\n" \
"ds_read_b64_tr_b16 a[16:17], v2 offset:32768\n" \
"ds_read_b64_tr_b16 a[18:19], v2 offset:33280\n" \
"ds_read_b64_tr_b16 a[144:145], v3 offset:32768\n" \
"ds_read_b64_tr_b16 a[146:147], v3 offset:33280\n" \
"ds_read_b64_tr_b16 a[20:21], v2 offset:36864\n" \
"ds_read_b64_tr_b16 a[22:23], v2 offset:37376\n" \
"ds_read_b64_tr_b16 a[148:149], v3 offset:36864\n" \
"ds_read_b64_tr_b16 a[150:151], v3 offset:37376\n" \
"s_waitcnt lgkmcnt(0)\n" \
"s_barrier\n" \
"s_add_u32 m0, s52, 32768\n" \
"s_nop 0\n" \
"buffer_load_dwordx4 v1, s[40:43], s54 offen nt lds\n" \
"s_add_u32 s54, s54, s44\n" \
"s_add_u32 m0, s52, 34816\n" \
"s_nop 0\n" \
"buffer_load_dwordx4 v1, s[40:43], s54 offen nt lds\n" \
"s_add_u32 s54, s54, s44\n" \
"s_add_u32 m0, s52, 36864\n" \
"s_nop 0\n" \
"buffer_load_dwordx4 v1, s[40:43], s54 offen nt lds\n" \
"s_add_u32 s54, s54, s44\n" \
"s_add_u32 m0, s52, 38912\n" \
"s_nop 0\n" \
"buffer_load_dwordx4 v1, s[40:43], s54 offen nt lds\n" \
"s_add_u32 s54, s54, s44\n" \
"s_waitcnt vmcnt(28)\n" \
"s_barrier\n" \
"ds_read_b64_tr_b16 a[24:25], v2 offset:49152\n" \
"ds_read_b64_tr_b16 a[26:27], v2 offset:49664\n" \
"ds_read_b64_tr_b16 a[152:153], v3 offset:49152\n" \
"ds_read_b64_tr_b16 a[154:155], v3 offset:49664\n" \
"ds_read_b64_tr_b16 a[28:29], v2 offset:53248\n" \
"ds_read_b64_tr_b16 a[30:31], v2 offset:53760\n" \
"ds_read_b64_tr_b16 a[156:157], v3 offset:53248\n" \
"ds_read_b64_tr_b16 a[158:159], v3 offset:53760\n" \
"s_waitcnt lgkmcnt(0)\n" \
"s_barrier\n" \
"s_add_u32 m0, s52, 49152\n" \
"s_nop 0\n" \
"buffer_load_dwordx4 v1, s[40:43], s54 offen nt lds\n" \
"s_add_u32 s54, s54, s44\n" \
"s_add_u32 m0, s52, 51200\n" \
"s_nop 0\n" \
"buffer_load_dwordx4 v1, s[40:43], s54 offen nt lds\n" \
"s_add_u32 s54, s54, s44\n" \
"s_add_u32 m0, s52, 53248\n" \
"s_nop 0\n" \
"buffer_load_dwordx4 v1, s[40:43], s54 offen nt lds\n" \
"s_add_u32 s54, s54, s44\n" \
"s_add_u32 m0, s52, 55296\n" \
"s_nop 0\n" \
"buffer_load_dwordx4 v1, s[40:43], s54 offen nt lds\n" \
"s_add_u32 s54, s54, s44\n" \
"s_waitcnt vmcnt(28)\n" \
"s_barrier\n" \
"ds_read_b64_tr_b16 a[32:33], v8 offset:0\n" \
"ds_read_b64_tr_b16 a[34:35], v8 offset:512\n" \
"ds_read_b64_tr_b16 a[160:161], v9 offset:0\n" \
"ds_read_b64_tr_b16 a[162:163], v9 offset:512\n" \
"ds_read_b64_tr_b16 a[36:37], v8 offset:4096\n" \
"ds_read_b64_tr_b16 a[38:39], v8 offset:4608\n" \
"ds_read_b64_tr_b16 a[164:165], v9 offset:4096\n" \
"ds_read_b64_tr_b16 a[166:167], v9 offset:4608\n" \
"s_waitcnt lgkmcnt(0)\n" \
"s_barrier\n" \
"s_add_u32 m0, s52, 65536\n" \
"s_nop 0\n" \
"buffer_load_dwordx4 v1, s[40:43], s54 offen nt lds\n" \
"s_add_u32 s54, s54, s44\n" \
"s_add_u32 m0, s52, 67584\n" \
"s_nop 0\n" \
"buffer_load_dwordx4 v1, s[40:43], s54 offen nt lds\n" \
"s_add_u32 s54, s54, s44\n" \
"s_add_u32 m0, s52, 69632\n" \
"s_nop 0\n" \
"buffer_load_dwordx4 v1, s[40:43], s54 offen nt lds\n" \
"s_add_u32 s54, s54, s44\n" \
"s_add_u32 m0, s52, 71680\n" \
"s_nop 0\n" \
"buffer_load_dwordx4 v1, s[40:43], s54 offen nt lds\n" \
"s_add_u32 s54, s54, s44\n" \
"s_waitcnt vmcnt(28)\n" \
"s_barrier\n" \
"ds_read_b64_tr_b16 a[40:41], v8 offset:16384\n" \
"ds_read_b64_tr_b16 a[42:43], v8 offset:16896\n" \
"ds_read_b64_tr_b16 a[168:169], v9 offset:16384\n" \
"ds_read_b64_tr_b16 a[170:171], v9 offset:16896\n" \
"ds_read_b64_tr_b16 a[44:45], v8 offset:20480\n" \
"ds_read_b64_tr_b16 a[46:47], v8 offset:20992\n" \
"ds_read_b64_tr_b16 a[172:173], v9 offset:20480\n" \
"ds_read_b64_tr_b16 a[174:175], v9 offset:20992\n" \
"s_waitcnt lgkmcnt(0)\n" \
"s_barrier\n" \
"s_add_u32 m0, s52, 81920\n" \
"s_nop 0\n" \
"buffer_load_dwordx4 v1, s[40:43], s54 offen nt lds\n" \
"s_add_u32 s54, s54, s44\n" \
"s_add_u32 m0, s52, 83968\n" \
"s_nop 0\n" \
"buffer_load_dwordx4 v1, s[40:43], s54 offen nt lds\n" \
"s_add_u32 s54, s54, s44\n" \
"s_add_u32 m0, s52, 86016\n" \
"s_nop 0\n" \
"buffer_load_dwordx4 v1, s[40:43], s54 offen nt lds\n" \
"s_add_u32 s54, s54, s44\n" \
"s_add_u32 m0, s52, 88064\n" \
"s_nop 0\n" \
"buffer_load_dwordx4 v1, s[40:43], s54 offen nt lds\n" \
"s_add_u32 s54, s54, s44\n" \
"s_waitcnt vmcnt(28)\n" \
"s_barrier\n" \
"ds_read_b64_tr_b16 a[48:49], v8 offset:32768\n" \
"ds_read_b64_tr_b16 a[50:51], v8 offset:33280\n" \
"ds_read_b64_tr_b16 a[176:177], v9 offset:32768\n" \
"ds_read_b64_tr_b16 a[178:179], v9 offset:33280\n" \
"ds_read_b64_tr_b16 a[52:53], v8 offset:36864\n" \
"ds_read_b64_tr_b16 a[54:55], v8 offset:37376\n" \
"ds_read_b64_tr_b16 a[180:181], v9 offset:36864\n" \
"ds_read_b64_tr_b16 a[182:183], v9 offset:37376\n" \
"s_waitcnt lgkmcnt(0)\n" \
"s_barrier\n" \
"s_add_u32 m0, s52, 98304\n" \
"s_nop 0\n" \
"buffer_load_dwordx4 v1, s[40:43], s54 offen nt lds\n" \
"s_add_u32 s54, s54, s44\n" \
"s_add_u32 m0, s52, 100352\n" \
"s_nop 0\n" \
"buffer_load_dwordx4 v1, s[40:43], s54 offen nt lds\n" \
"s_add_u32 s54, s54, s44\n" \
"s_add_u32 m0, s52, 102400\n" \
"s_nop 0\n" \
"buffer_load_dwordx4 v1, s[40:43], s54 offen nt lds\n" \
"s_add_u32 s54, s54, s44\n" \
"s_add_u32 m0, s52, 104448\n" \
"s_nop 0\n" \
"buffer_load_dwordx4 v1, s[40:43], s54 offen nt lds\n" \
"s_add_u32 s54, s54, s44\n" \
"s_waitcnt vmcnt(28)\n" \
"s_barrier\n" \
"ds_read_b64_tr_b16 a[56:57], v8 offset:49152\n" \
"ds_read_b64_tr_b16 a[58:59], v8 offset:49664\n" \
"ds_read_b64_tr_b16 a[184:185], v9 offset:49152\n" \
"ds_read_b64_tr_b16 a[186:187], v9 offset:49664\n" \
"ds_read_b64_tr_b16 a[60:61], v8 offset:53248\n" \
"ds_read_b64_tr_b16 a[62:63], v8 offset:53760\n" \
"ds_read_b64_tr_b16 a[188:189], v9 offset:53248\n" \
"ds_read_b64_tr_b16 a[190:191], v9 offset:53760\n" \
"s_waitcnt lgkmcnt(0)\n" \
"s_barrier\n" \
"s_add_u32 m0, s52, 114688\n" \
"s_nop 0\n" \
"buffer_load_dwordx4 v1, s[40:43], s54 offen nt lds\n" \
"s_add_u32 s54, s54, s44\n" \
"s_add_u32 m0, s52, 116736\n" \
"s_nop 0\n" \
"buffer_load_dwordx4 v1, s[40:43], s54 offen nt lds\n" \
"s_add_u32 s54, s54, s44\n" \
"s_add_u32 m0, s52, 118784\n" \
"s_nop 0\n" \
"buffer_load_dwordx4 v1, s[40:43], s54 offen nt lds\n" \
"s_add_u32 s54, s54, s44\n" \
"s_add_u32 m0, s52, 120832\n" \
"s_nop 0\n" \
"buffer_load_dwordx4 v1, s[40:43], s54 offen nt lds\n" \
"s_add_u32 s54, s54, s44\n" \
"s_waitcnt vmcnt(28)\n" \
"s_barrier\n" \
"ds_read_b64_tr_b16 a[64:65], v2 offset:0\n" \
"ds_read_b64_tr_b16 a[66:67], v2 offset:512\n" \
"ds_read_b64_tr_b16 a[192:193], v3 offset:0\n" \
"ds_read_b64_tr_b16 a[194:195], v3 offset:512\n" \
"ds_read_b64_tr_b16 a[68:69], v2 offset:4096\n" \
"ds_read_b64_tr_b16 a[70:71], v2 offset:4608\n" \
"ds_read_b64_tr_b16 a[196:197], v3 offset:4096\n" \
"ds_read_b64_tr_b16 a[198:199], v3 offset:4608\n" \
"s_waitcnt vmcnt(24)\n" \
"s_barrier\n" \
"ds_read_b64_tr_b16 a[72:73], v2 offset:16384\n" \
"ds_read_b64_tr_b16 a[74:75], v2 offset:16896\n" \
"ds_read_b64_tr_b16 a[200:201], v3 offset:16384\n" \
"ds_read_b64_tr_b16 a[202:203], v3 offset:16896\n" \
"ds_read_b64_tr_b16 a[76:77], v2 offset:20480\n" \
"ds_read_b64_tr_b16 a[78:79], v2 offset:20992\n" \
"ds_read_b64_tr_b16 a[204:205], v3 offset:20480\n" \
"ds_read_b64_tr_b16 a[206:207], v3 offset:20992\n" \
"s_waitcnt vmcnt(20)\n" \
"s_barrier\n" \
"ds_read_b64_tr_b16 a[80:81], v2 offset:32768\n" \
"ds_read_b64_tr_b16 a[82:83], v2 offset:33280\n" \
"ds_read_b64_tr_b16 a[208:209], v3 offset:32768\n" \
"ds_read_b64_tr_b16 a[210:211], v3 offset:33280\n" \
"ds_read_b64_tr_b16 a[84:85], v2 offset:36864\n" \
"ds_read_b64_tr_b16 a[86:87], v2 offset:37376\n" \
"ds_read_b64_tr_b16 a[212:213], v3 offset:36864\n" \
"ds_read_b64_tr_b16 a[214:215], v3 offset:37376\n" \
"s_waitcnt vmcnt(16)\n" \
"s_barrier\n" \
"ds_read_b64_tr_b16 a[88:89], v2 offset:49152\n" \
"ds_read_b64_tr_b16 a[90:91], v2 offset:49664\n" \
"ds_read_b64_tr_b16 a[216:217], v3 offset:49152\n" \
"ds_read_b64_tr_b16 a[218:219], v3 offset:49664\n" \
"ds_read_b64_tr_b16 a[92:93], v2 offset:53248\n" \
"ds_read_b64_tr_b16 a[94:95], v2 offset:53760\n" \
"ds_read_b64_tr_b16 a[220:221], v3 offset:53248\n" \
"ds_read_b64_tr_b16 a[222:223], v3 offset:53760\n" \
"s_waitcnt vmcnt(12)\n" \
"s_barrier\n" \
"ds_read_b64_tr_b16 a[96:97], v8 offset:0\n" \
"ds_read_b64_tr_b16 a[98:99], v8 offset:512\n" \
"ds_read_b64_tr_b16 a[224:225], v9 offset:0\n" \
"ds_read_b64_tr_b16 a[226:227], v9 offset:512\n" \
"ds_read_b64_tr_b16 a[100:101], v8 offset:4096\n" \
"ds_read_b64_tr_b16 a[102:103], v8 offset:4608\n" \
"ds_read_b64_tr_b16 a[228:229], v9 offset:4096\n" \
"ds_read_b64_tr_b16 a[230:231], v9 offset:4608\n" \
"s_waitcnt vmcnt(8)\n" \
"s_barrier\n" \
"ds_read_b64_tr_b16 a[104:105], v8 offset:16384\n" \
"ds_read_b64_tr_b16 a[106:107], v8 offset:16896\n" \
"ds_read_b64_tr_b16 a[232:233], v9 offset:16384\n" \
"ds_read_b64_tr_b16 a[234:235], v9 offset:16896\n" \
"ds_read_b64_tr_b16 a[108:109], v8 offset:20480\n" \
"ds_read_b64_tr_b16 a[110:111], v8 offset:20992\n" \
"ds_read_b64_tr_b16 a[236:237], v9 offset:20480\n" \
"ds_read_b64_tr_b16 a[238:239], v9 offset:20992\n" \
"s_waitcnt vmcnt(4)\n" \
"s_barrier\n" \
"ds_read_b64_tr_b16 a[112:113], v8 offset:32768\n" \
"ds_read_b64_tr_b16 a[114:115], v8 offset:33280\n" \
"ds_read_b64_tr_b16 a[240:241], v9 offset:32768\n" \
"ds_read_b64_tr_b16 a[242:243], v9 offset:33280\n" \
"ds_read_b64_tr_b16 a[116:117], v8 offset:36864\n" \
"ds_read_b64_tr_b16 a[118:119], v8 offset:37376\n" \
"ds_read_b64_tr_b16 a[244:245], v9 offset:36864\n" \
"ds_read_b64_tr_b16 a[246:247], v9 offset:37376\n" \
"s_waitcnt vmcnt(0)\n" \
"s_barrier\n" \
"ds_read_b64_tr_b16 a[120:121], v8 offset:49152\n" \
"ds_read_b64_tr_b16 a[122:123], v8 offset:49664\n" \
"ds_read_b64_tr_b16 a[248:249], v9 offset:49152\n" \
"ds_read_b64_tr_b16 a[250:251], v9 offset:49664\n" \
"ds_read_b64_tr_b16 a[124:125], v8 offset:53248\n" \
"ds_read_b64_tr_b16 a[126:127], v8 offset:53760\n" \
"ds_read_b64_tr_b16 a[252:253], v9 offset:53248\n" \
"ds_read_b64_tr_b16 a[254:255], v9 offset:53760\n" \
"s_waitcnt lgkmcnt(0)\n" \
"s_barrier\n" \
"v_mov_b32 v14, 4096\n" \
"s_mov_b32 s59, s46\n" \
"s_add_u32 s63, s59, 0\n" \
"s_min_u32 s63, s63, 63\n" \
"s_lshr_b32 s63, s63, 3\n" \
"s_lshl_b32 s63, s63, 7\n" \
"s_branch .Lp1v_l0_%=\n" \
".Lp1v_l0k_%=:\n" \
"s_add_u32 s63, s59, 1\n" \
"s_min_u32 s63, s63, 63\n" \
"s_lshr_b32 s63, s63, 3\n" \
"s_lshl_b32 s63, s63, 7\n" \
"s_branch .Lp1v_l1_%=\n" \
".Lp1v_l1k_%=:\n" \
"s_lshl_b32 s55, s48, 13\n" \
"s_lshl_b32 s56, s46, 16\n" \
"s_lshl_b32 s60, s47, 16\n" \
"s_sub_u32 s60, s60, 0x10000\n" \
"s_mul_i32 s57, s46, s49\n" \
"s_sub_u32 s57, s57, s49\n" \
"s_add_u32 s58, s57, s50\n" \
"v_mov_b32 v7, v5\n" \
"v_mov_b32 v5, 0x7ffffff0\n" \
"v_mov_b32 v10, v0\n" \
"v_mov_b32 v11, v6\n" \
"v_mov_b32 v6, 0x7ffffff0\n" \
"s_mov_b32 s59, s46\n" \
"s_min_u32 s61, s56, s60\n" \
"s_add_u32 m0, s55, 0\n" \
"s_nop 0\n" \
"buffer_load_dwordx4 v4, s[36:39], s61 offen offset:0 sc1 lds\n" \
"buffer_load_dwordx4 v4, s[36:39], s61 offen offset:1024 sc1 lds\n" \
"buffer_load_dwordx4 v4, s[36:39], s61 offen offset:2048 sc1 lds\n" \
"buffer_load_dwordx4 v4, s[36:39], s61 offen offset:3072 sc1 lds\n" \
"s_add_u32 m0, s55, 4096\n" \
"s_add_u32 s61, s61, 4096\n" \
"buffer_load_dwordx4 v4, s[36:39], s61 offen offset:0 sc1 lds\n" \
"buffer_load_dwordx4 v4, s[36:39], s61 offen offset:1024 sc1 lds\n" \
"buffer_load_dwordx4 v4, s[36:39], s61 offen offset:2048 sc1 lds\n" \
"buffer_load_dwordx4 v4, s[36:39], s61 offen offset:3072 sc1 lds\n" \
"s_min_u32 s61, s56, s60\n" \
"s_add_u32 s61, s61, 16384\n" \
"s_add_u32 s56, s56, 0x10000\n" \
"s_add_u32 m0, s55, 32768\n" \
"s_nop 0\n" \
"buffer_load_dwordx4 v4, s[36:39], s61 offen offset:0 sc1 lds\n" \
"buffer_load_dwordx4 v4, s[36:39], s61 offen offset:1024 sc1 lds\n" \
"buffer_load_dwordx4 v4, s[36:39], s61 offen offset:2048 sc1 lds\n" \
"buffer_load_dwordx4 v4, s[36:39], s61 offen offset:3072 sc1 lds\n" \
"s_add_u32 m0, s55, 36864\n" \
"s_add_u32 s61, s61, 4096\n" \
"buffer_load_dwordx4 v4, s[36:39], s61 offen offset:0 sc1 lds\n" \
"buffer_load_dwordx4 v4, s[36:39], s61 offen offset:1024 sc1 lds\n" \
"buffer_load_dwordx4 v4, s[36:39], s61 offen offset:2048 sc1 lds\n" \
"buffer_load_dwordx4 v4, s[36:39], s61 offen offset:3072 sc1 lds\n" \
"s_mov_b32 s70, 0\n" \
"s_mov_b32 s72, 0\n" \
"s_waitcnt vmcnt(0) lgkmcnt(0)\n" \
"s_barrier\n" \
"v_add_u32 v12, 0x10000, v10\n" \
"ds_read_b128 v[16:19], v10 offset:0\n" \
"ds_read_b128 v[20:23], v10 offset:16384\n" \
"ds_read_b128 v[24:27], v10 offset:1024\n" \
"ds_read_b128 v[28:31], v10 offset:17408\n" \
"ds_read_b128 v[32:35], v10 offset:2048\n" \
"ds_read_b128 v[36:39], v10 offset:18432\n" \
"s_waitcnt lgkmcnt(4)\n" \
"s_nop 7\n" \
"s_nop 7\n" \
".p2align 6\n" \
".Lp1v_loop_%=:\n" \
"v_mfma_f32_16x16x32_bf16 v[48:51], v[16:19], a[0:3], 0\n" \
"s_min_u32 s61, s56, s60\n" \
"s_add_u32 m0, s55, 65536\n" \
"s_nop 0\n" \
"buffer_load_dwordx4 v4, s[36:39], s61 offen offset:0 sc1 lds\n" \
"v_mov_b32_e64 v13, s59\n" \
"v_mfma_f32_16x16x32_bf16 v[52:55], v[20:23], a[0:3], 0\n" \
"ds_read_b128 v[40:43], v10 offset:3072\n" \
"s_nop 0\n" \
"v_mfma_f32_16x16x32_bf16 v[56:59], v[16:19], a[128:131], 0\n" \
"ds_read_b128 v[44:47], v10 offset:19456\n" \
"s_nop 0\n" \
"v_mfma_f32_16x16x32_bf16 v[60:63], v[20:23], a[128:131], 0\n" \
"s_waitcnt lgkmcnt(4)\n" \
"v_cvt_pk_bf16_f32 v80, v64, v65\n" \
"v_cvt_pk_bf16_f32 v81, v66, v67\n" \
"v_mfma_f32_16x16x32_bf16 v[48:51], v[24:27], a[4:7], v[48:51]\n" \
"buffer_load_dwordx4 v4, s[36:39], s61 offen offset:1024 sc1 lds\n" \
"v_mfma_f32_16x16x32_bf16 v[52:55], v[28:31], a[4:7], v[52:55]\n" \
"ds_read_b128 v[16:19], v10 offset:4096\n" \
"v_cvt_pk_bf16_f32 v82, v68, v69\n" \
"v_mfma_f32_16x16x32_bf16 v[56:59], v[24:27], a[132:135], v[56:59]\n" \
"ds_read_b128 v[20:23], v10 offset:20480\n" \
"v_cvt_pk_bf16_f32 v83, v70, v71\n" \
"v_mfma_f32_16x16x32_bf16 v[60:63], v[28:31], a[132:135], v[60:63]\n" \
"s_waitcnt lgkmcnt(4)\n" \
"buffer_store_dwordx4 v[80:83], v5, s[36:39], s57 offen sc1\n" \
"v_cvt_pk_bf16_f32 v84, v72, v73\n" \
"v_mfma_f32_16x16x32_bf16 v[48:51], v[32:35], a[8:11], v[48:51]\n" \
"s_add_u32 s63, s59, 2\n" \
"s_min_u32 s63, s63, 63\n" \
"v_mfma_f32_16x16x32_bf16 v[52:55], v[36:39], a[8:11], v[52:55]\n" \
"ds_read_b128 v[24:27], v10 offset:5120\n" \
"s_lshr_b32 s63, s63, 3\n" \
"s_lshl_b32 s63, s63, 7\n" \
"v_cvt_pk_bf16_f32 v85, v74, v75\n" \
"v_mfma_f32_16x16x32_bf16 v[56:59], v[32:35], a[136:139], v[56:59]\n" \
"ds_read_b128 v[28:31], v10 offset:21504\n" \
"v_cvt_pk_bf16_f32 v86, v76, v77\n" \
"v_mfma_f32_16x16x32_bf16 v[60:63], v[36:39], a[136:139], v[60:63]\n" \
"s_waitcnt lgkmcnt(4)\n" \
"v_cvt_pk_bf16_f32 v87, v78, v79\n" \
"buffer_store_dwordx4 v[84:87], v5, s[36:39], s58 offen sc1\n" \
"v_mfma_f32_16x16x32_bf16 v[48:51], v[40:43], a[12:15], v[48:51]\n" \
"buffer_load_dwordx4 v4, s[36:39], s61 offen offset:2048 sc1 lds\n" \
"v_mfma_f32_16x16x32_bf16 v[52:55], v[44:47], a[12:15], v[52:55]\n" \
"ds_read_b128 v[32:35], v10 offset:6144\n" \
"v_mfma_f32_16x16x32_bf16 v[56:59], v[40:43], a[140:143], v[56:59]\n" \
"ds_read_b128 v[36:39], v10 offset:22528\n" \
"v_mfma_f32_16x16x32_bf16 v[60:63], v[44:47], a[140:143], v[60:63]\n" \
"s_waitcnt lgkmcnt(4)\n" \
"v_mfma_f32_16x16x32_bf16 v[48:51], v[16:19], a[16:19], v[48:51]\n" \
"buffer_load_dwordx4 v4, s[36:39], s61 offen offset:3072 sc1 lds\n" \
"v_mfma_f32_16x16x32_bf16 v[52:55], v[20:23], a[16:19], v[52:55]\n" \
"ds_read_b128 v[40:43], v10 offset:7168\n" \
"v_mfma_f32_16x16x32_bf16 v[56:59], v[16:19], a[144:147], v[56:59]\n" \
"ds_read_b128 v[44:47], v10 offset:23552\n" \
"v_mfma_f32_16x16x32_bf16 v[60:63], v[20:23], a[144:147], v[60:63]\n" \
"s_waitcnt lgkmcnt(4)\n" \
"v_mfma_f32_16x16x32_bf16 v[48:51], v[24:27], a[20:23], v[48:51]\n" \
"buffer_load_dword v15, v14, s[36:39], s63 offen sc1\n" \
"v_mfma_f32_16x16x32_bf16 v[52:55], v[28:31], a[20:23], v[52:55]\n" \
"ds_read_b128 v[16:19], v10 offset:8192\n" \
"v_mfma_f32_16x16x32_bf16 v[56:59], v[24:27], a[148:151], v[56:59]\n" \
"ds_read_b128 v[20:23], v10 offset:24576\n" \
"v_mfma_f32_16x16x32_bf16 v[60:63], v[28:31], a[148:151], v[60:63]\n" \
"s_waitcnt lgkmcnt(4)\n" \
"v_mfma_f32_16x16x32_bf16 v[48:51], v[32:35], a[24:27], v[48:51]\n" \
"s_add_u32 m0, s55, 69632\n" \
"s_add_u32 s61, s61, 4096\n" \
"buffer_load_dwordx4 v4, s[36:39], s61 offen offset:0 sc1 lds\n" \
"v_mfma_f32_16x16x32_bf16 v[52:55], v[36:39], a[24:27], v[52:55]\n" \
"ds_read_b128 v[24:27], v10 offset:9216\n" \
"v_mfma_f32_16x16x32_bf16 v[56:59], v[32:35], a[152:155], v[56:59]\n" \
"ds_read_b128 v[28:31], v10 offset:25600\n" \
"v_mfma_f32_16x16x32_bf16 v[60:63], v[36:39], a[152:155], v[60:63]\n" \
"s_waitcnt lgkmcnt(4)\n" \
"v_mfma_f32_16x16x32_bf16 v[48:51], v[40:43], a[28:31], v[48:51]\n" \
"buffer_load_dwordx4 v4, s[36:39], s61 offen offset:1024 sc1 lds\n" \
"v_mfma_f32_16x16x32_bf16 v[52:55], v[44:47], a[28:31], v[52:55]\n" \
"ds_read_b128 v[32:35], v10 offset:10240\n" \
"v_mfma_f32_16x16x32_bf16 v[56:59], v[40:43], a[156:159], v[56:59]\n" \
"ds_read_b128 v[36:39], v10 offset:26624\n" \
"v_mfma_f32_16x16x32_bf16 v[60:63], v[44:47], a[156:159], v[60:63]\n" \
"s_waitcnt lgkmcnt(4)\n" \
"v_mfma_f32_16x16x32_bf16 v[48:51], v[16:19], a[32:35], v[48:51]\n" \
"v_mfma_f32_16x16x32_bf16 v[52:55], v[20:23], a[32:35], v[52:55]\n" \
"ds_read_b128 v[40:43], v10 offset:11264\n" \
"v_mfma_f32_16x16x32_bf16 v[56:59], v[16:19], a[160:163], v[56:59]\n" \
"ds_read_b128 v[44:47], v10 offset:27648\n" \
"v_mfma_f32_16x16x32_bf16 v[60:63], v[20:23], a[160:163], v[60:63]\n" \
"s_waitcnt lgkmcnt(4)\n" \
"v_mfma_f32_16x16x32_bf16 v[48:51], v[24:27], a[36:39], v[48:51]\n" \
"buffer_load_dwordx4 v4, s[36:39], s61 offen offset:2048 sc1 lds\n" \
"v_mfma_f32_16x16x32_bf16 v[52:55], v[28:31], a[36:39], v[52:55]\n" \
"ds_read_b128 v[16:19], v10 offset:12288\n" \
"v_mfma_f32_16x16x32_bf16 v[56:59], v[24:27], a[164:167], v[56:59]\n" \
"ds_read_b128 v[20:23], v10 offset:28672\n" \
"v_mfma_f32_16x16x32_bf16 v[60:63], v[28:31], a[164:167], v[60:63]\n" \
"s_waitcnt lgkmcnt(4)\n" \
"v_mfma_f32_16x16x32_bf16 v[48:51], v[32:35], a[40:43], v[48:51]\n" \
"buffer_load_dwordx4 v4, s[36:39], s61 offen offset:3072 sc1 lds\n" \
"v_mfma_f32_16x16x32_bf16 v[52:55], v[36:39], a[40:43], v[52:55]\n" \
"ds_read_b128 v[24:27], v10 offset:13312\n" \
"v_mfma_f32_16x16x32_bf16 v[56:59], v[32:35], a[168:171], v[56:59]\n" \
"ds_read_b128 v[28:31], v10 offset:29696\n" \
"v_mfma_f32_16x16x32_bf16 v[60:63], v[36:39], a[168:171], v[60:63]\n" \
"s_waitcnt lgkmcnt(4)\n" \
"v_mfma_f32_16x16x32_bf16 v[48:51], v[40:43], a[44:47], v[48:51]\n" \
"v_mfma_f32_16x16x32_bf16 v[52:55], v[44:47], a[44:47], v[52:55]\n" \
"ds_read_b128 v[32:35], v10 offset:14336\n" \
"v_mfma_f32_16x16x32_bf16 v[56:59], v[40:43], a[172:175], v[56:59]\n" \
"ds_read_b128 v[36:39], v10 offset:30720\n" \
"v_mfma_f32_16x16x32_bf16 v[60:63], v[44:47], a[172:175], v[60:63]\n" \
"s_waitcnt lgkmcnt(4)\n" \
"v_mfma_f32_16x16x32_bf16 v[48:51], v[16:19], a[48:51], v[48:51]\n" \
"v_mfma_f32_16x16x32_bf16 v[52:55], v[20:23], a[48:51], v[52:55]\n" \
"ds_read_b128 v[40:43], v10 offset:15360\n" \
"v_mfma_f32_16x16x32_bf16 v[56:59], v[16:19], a[176:179], v[56:59]\n" \
"ds_read_b128 v[44:47], v10 offset:31744\n" \
"v_mfma_f32_16x16x32_bf16 v[60:63], v[20:23], a[176:179], v[60:63]\n" \
"s_waitcnt lgkmcnt(4)\n" \
"v_mfma_f32_16x16x32_bf16 v[48:51], v[24:27], a[52:55], v[48:51]\n" \
"s_waitcnt vmcnt(12)\n" \
"s_barrier\n" \
"v_mfma_f32_16x16x32_bf16 v[52:55], v[28:31], a[52:55], v[52:55]\n" \
"ds_read_b128 v[16:19], v10 offset:32768\n" \
"v_mfma_f32_16x16x32_bf16 v[56:59], v[24:27], a[180:183], v[56:59]\n" \
"ds_read_b128 v[20:23], v10 offset:49152\n" \
"v_mfma_f32_16x16x32_bf16 v[60:63], v[28:31], a[180:183], v[60:63]\n" \
"s_waitcnt lgkmcnt(4)\n" \
"v_mfma_f32_16x16x32_bf16 v[48:51], v[32:35], a[56:59], v[48:51]\n" \
"v_mfma_f32_16x16x32_bf16 v[52:55], v[36:39], a[56:59], v[52:55]\n" \
"ds_read_b128 v[24:27], v10 offset:33792\n" \
"v_mfma_f32_16x16x32_bf16 v[56:59], v[32:35], a[184:187], v[56:59]\n" \
"ds_read_b128 v[28:31], v10 offset:50176\n" \
"v_mfma_f32_16x16x32_bf16 v[60:63], v[36:39], a[184:187], v[60:63]\n" \
"s_waitcnt lgkmcnt(4)\n" \
"v_mfma_f32_16x16x32_bf16 v[48:51], v[40:43], a[60:63], v[48:51]\n" \
"v_mfma_f32_16x16x32_bf16 v[52:55], v[44:47], a[60:63], v[52:55]\n" \
"ds_read_b128 v[32:35], v10 offset:34816\n" \
"v_mfma_f32_16x16x32_bf16 v[56:59], v[40:43], a[188:191], v[56:59]\n" \
"ds_read_b128 v[36:39], v10 offset:51200\n" \
"v_mfma_f32_16x16x32_bf16 v[60:63], v[44:47], a[188:191], v[60:63]\n" \
"s_waitcnt lgkmcnt(4)\n" \
"v_mfma_f32_16x16x32_bf16 v[48:51], v[16:19], a[64:67], v[48:51]\n" \
"s_min_u32 s61, s56, s60\n" \
"s_add_u32 s61, s61, 16384\n" \
"s_add_u32 s56, s56, 0x10000\n" \
"s_add_u32 m0, s55, 98304\n" \
"s_nop 0\n" \
"buffer_load_dwordx4 v4, s[36:39], s61 offen offset:0 sc1 lds\n" \
"v_mfma_f32_16x16x32_bf16 v[52:55], v[20:23], a[64:67], v[52:55]\n" \
"ds_read_b128 v[40:43], v10 offset:35840\n" \
"v_mfma_f32_16x16x32_bf16 v[56:59], v[16:19], a[192:195], v[56:59]\n" \
"ds_read_b128 v[44:47], v10 offset:52224\n" \
"v_mfma_f32_16x16x32_bf16 v[60:63], v[20:23], a[192:195], v[60:63]\n" \
"s_waitcnt lgkmcnt(4)\n" \
"v_mfma_f32_16x16x32_bf16 v[48:51], v[24:27], a[68:71], v[48:51]\n" \
"buffer_load_dwordx4 v4, s[36:39], s61 offen offset:1024 sc1 lds\n" \
"v_mfma_f32_16x16x32_bf16 v[52:55], v[28:31], a[68:71], v[52:55]\n" \
"ds_read_b128 v[16:19], v10 offset:36864\n" \
"v_mfma_f32_16x16x32_bf16 v[56:59], v[24:27], a[196:199], v[56:59]\n" \
"ds_read_b128 v[20:23], v10 offset:53248\n" \
"v_mfma_f32_16x16x32_bf16 v[60:63], v[28:31], a[196:199], v[60:63]\n" \
"s_waitcnt lgkmcnt(4)\n" \
"v_mfma_f32_16x16x32_bf16 v[48:51], v[32:35], a[72:75], v[48:51]\n" \
"v_mfma_f32_16x16x32_bf16 v[52:55], v[36:39], a[72:75], v[52:55]\n" \
"ds_read_b128 v[24:27], v10 offset:37888\n" \
"v_mfma_f32_16x16x32_bf16 v[56:59], v[32:35], a[200:203], v[56:59]\n" \
"ds_read_b128 v[28:31], v10 offset:54272\n" \
"v_mfma_f32_16x16x32_bf16 v[60:63], v[36:39], a[200:203], v[60:63]\n" \
"s_waitcnt lgkmcnt(4)\n" \
"v_mfma_f32_16x16x32_bf16 v[48:51], v[40:43], a[76:79], v[48:51]\n" \
"buffer_load_dwordx4 v4, s[36:39], s61 offen offset:2048 sc1 lds\n" \
"v_mfma_f32_16x16x32_bf16 v[52:55], v[44:47], a[76:79], v[52:55]\n" \
"ds_read_b128 v[32:35], v10 offset:38912\n" \
"v_mfma_f32_16x16x32_bf16 v[56:59], v[40:43], a[204:207], v[56:59]\n" \
"ds_read_b128 v[36:39], v10 offset:55296\n" \
"v_mfma_f32_16x16x32_bf16 v[60:63], v[44:47], a[204:207], v[60:63]\n" \
"s_waitcnt lgkmcnt(4)\n" \
"v_mfma_f32_16x16x32_bf16 v[48:51], v[16:19], a[80:83], v[48:51]\n" \
"buffer_load_dwordx4 v4, s[36:39], s61 offen offset:3072 sc1 lds\n" \
"v_mfma_f32_16x16x32_bf16 v[52:55], v[20:23], a[80:83], v[52:55]\n" \
"ds_read_b128 v[40:43], v10 offset:39936\n" \
"v_mfma_f32_16x16x32_bf16 v[56:59], v[16:19], a[208:211], v[56:59]\n" \
"ds_read_b128 v[44:47], v10 offset:56320\n" \
"v_mfma_f32_16x16x32_bf16 v[60:63], v[20:23], a[208:211], v[60:63]\n" \
"s_waitcnt lgkmcnt(4)\n" \
"v_mfma_f32_16x16x32_bf16 v[48:51], v[24:27], a[84:87], v[48:51]\n" \
"v_mfma_f32_16x16x32_bf16 v[52:55], v[28:31], a[84:87], v[52:55]\n" \
"ds_read_b128 v[16:19], v10 offset:40960\n" \
"v_mfma_f32_16x16x32_bf16 v[56:59], v[24:27], a[212:215], v[56:59]\n" \
"ds_read_b128 v[20:23], v10 offset:57344\n" \
"v_mfma_f32_16x16x32_bf16 v[60:63], v[28:31], a[212:215], v[60:63]\n" \
"s_waitcnt lgkmcnt(4)\n" \
"v_mfma_f32_16x16x32_bf16 v[48:51], v[32:35], a[88:91], v[48:51]\n" \
"s_add_u32 m0, s55, 102400\n" \
"s_add_u32 s61, s61, 4096\n" \
"buffer_load_dwordx4 v4, s[36:39], s61 offen offset:0 sc1 lds\n" \
"v_mfma_f32_16x16x32_bf16 v[52:55], v[36:39], a[88:91], v[52:55]\n" \
"ds_read_b128 v[24:27], v10 offset:41984\n" \
"v_mfma_f32_16x16x32_bf16 v[56:59], v[32:35], a[216:219], v[56:59]\n" \
"ds_read_b128 v[28:31], v10 offset:58368\n" \
"v_mfma_f32_16x16x32_bf16 v[60:63], v[36:39], a[216:219], v[60:63]\n" \
"s_waitcnt lgkmcnt(4)\n" \
"v_mfma_f32_16x16x32_bf16 v[48:51], v[40:43], a[92:95], v[48:51]\n" \
"buffer_load_dwordx4 v4, s[36:39], s61 offen offset:1024 sc1 lds\n" \
"v_mfma_f32_16x16x32_bf16 v[52:55], v[44:47], a[92:95], v[52:55]\n" \
"ds_read_b128 v[32:35], v10 offset:43008\n" \
"v_mfma_f32_16x16x32_bf16 v[56:59], v[40:43], a[220:223], v[56:59]\n" \
"ds_read_b128 v[36:39], v10 offset:59392\n" \
"v_mfma_f32_16x16x32_bf16 v[60:63], v[44:47], a[220:223], v[60:63]\n" \
"s_waitcnt lgkmcnt(4)\n" \
"v_mfma_f32_16x16x32_bf16 v[48:51], v[16:19], a[96:99], v[48:51]\n" \
"v_mfma_f32_16x16x32_bf16 v[52:55], v[20:23], a[96:99], v[52:55]\n" \
"ds_read_b128 v[40:43], v10 offset:44032\n" \
"v_mfma_f32_16x16x32_bf16 v[56:59], v[16:19], a[224:227], v[56:59]\n" \
"ds_read_b128 v[44:47], v10 offset:60416\n" \
"v_mfma_f32_16x16x32_bf16 v[60:63], v[20:23], a[224:227], v[60:63]\n" \
"s_waitcnt lgkmcnt(4)\n" \
"v_mfma_f32_16x16x32_bf16 v[48:51], v[24:27], a[100:103], v[48:51]\n" \
"buffer_load_dwordx4 v4, s[36:39], s61 offen offset:2048 sc1 lds\n" \
"v_mfma_f32_16x16x32_bf16 v[52:55], v[28:31], a[100:103], v[52:55]\n" \
"ds_read_b128 v[16:19], v10 offset:45056\n" \
"v_mfma_f32_16x16x32_bf16 v[56:59], v[24:27], a[228:231], v[56:59]\n" \
"ds_read_b128 v[20:23], v10 offset:61440\n" \
"v_mfma_f32_16x16x32_bf16 v[60:63], v[28:31], a[228:231], v[60:63]\n" \
"s_waitcnt lgkmcnt(4)\n" \
"v_mfma_f32_16x16x32_bf16 v[48:51], v[32:35], a[104:107], v[48:51]\n" \
"buffer_load_dwordx4 v4, s[36:39], s61 offen offset:3072 sc1 lds\n" \
"v_mfma_f32_16x16x32_bf16 v[52:55], v[36:39], a[104:107], v[52:55]\n" \
"ds_read_b128 v[24:27], v10 offset:46080\n" \
"v_mfma_f32_16x16x32_bf16 v[56:59], v[32:35], a[232:235], v[56:59]\n" \
"ds_read_b128 v[28:31], v10 offset:62464\n" \
"v_mfma_f32_16x16x32_bf16 v[60:63], v[36:39], a[232:235], v[60:63]\n" \
"s_waitcnt lgkmcnt(4)\n" \
"v_mfma_f32_16x16x32_bf16 v[48:51], v[40:43], a[108:111], v[48:51]\n" \
"v_mfma_f32_16x16x32_bf16 v[52:55], v[44:47], a[108:111], v[52:55]\n" \
"ds_read_b128 v[32:35], v10 offset:47104\n" \
"v_mfma_f32_16x16x32_bf16 v[56:59], v[40:43], a[236:239], v[56:59]\n" \
"ds_read_b128 v[36:39], v10 offset:63488\n" \
"v_mfma_f32_16x16x32_bf16 v[60:63], v[44:47], a[236:239], v[60:63]\n" \
"s_waitcnt lgkmcnt(4)\n" \
"v_mfma_f32_16x16x32_bf16 v[48:51], v[16:19], a[112:115], v[48:51]\n" \
"v_mfma_f32_16x16x32_bf16 v[52:55], v[20:23], a[112:115], v[52:55]\n" \
"ds_read_b128 v[40:43], v10 offset:48128\n" \
"v_mfma_f32_16x16x32_bf16 v[56:59], v[16:19], a[240:243], v[56:59]\n" \
"ds_read_b128 v[44:47], v10 offset:64512\n" \
"v_mfma_f32_16x16x32_bf16 v[60:63], v[20:23], a[240:243], v[60:63]\n" \
"s_waitcnt lgkmcnt(4)\n" \
"v_mfma_f32_16x16x32_bf16 v[48:51], v[24:27], a[116:119], v[48:51]\n" \
"s_waitcnt vmcnt(8)\n" \
"s_barrier\n" \
"v_mfma_f32_16x16x32_bf16 v[52:55], v[28:31], a[116:119], v[52:55]\n" \
"ds_read_b128 v[16:19], v12 offset:0\n" \
"v_mfma_f32_16x16x32_bf16 v[56:59], v[24:27], a[244:247], v[56:59]\n" \
"ds_read_b128 v[20:23], v12 offset:16384\n" \
"v_mfma_f32_16x16x32_bf16 v[60:63], v[28:31], a[244:247], v[60:63]\n" \
"s_waitcnt lgkmcnt(4)\n" \
"buffer_store_dword v13, v6, s[36:39], 0 offen sc1\n" \
"v_mfma_f32_16x16x32_bf16 v[48:51], v[32:35], a[120:123], v[48:51]\n" \
"v_cmp_gt_u32_e64 vcc, 16, v15\n" \
"v_mfma_f32_16x16x32_bf16 v[52:55], v[36:39], a[120:123], v[52:55]\n" \
"ds_read_b128 v[24:27], v12 offset:1024\n" \
"v_mfma_f32_16x16x32_bf16 v[56:59], v[32:35], a[248:251], v[56:59]\n" \
"ds_read_b128 v[28:31], v12 offset:17408\n" \
"v_mfma_f32_16x16x32_bf16 v[60:63], v[36:39], a[248:251], v[60:63]\n" \
"s_waitcnt lgkmcnt(4)\n" \
"v_mfma_f32_16x16x32_bf16 v[48:51], v[40:43], a[124:127], v[48:51]\n" \
"s_nop 0\n" \
"s_cbranch_vccnz .Lp1v_m0_%=\n" \
".Lp1v_m0k_%=:\n" \
"v_mfma_f32_16x16x32_bf16 v[52:55], v[44:47], a[124:127], v[52:55]\n" \
"ds_read_b128 v[32:35], v12 offset:2048\n" \
"v_mfma_f32_16x16x32_bf16 v[56:59], v[40:43], a[252:255], v[56:59]\n" \
"ds_read_b128 v[36:39], v12 offset:18432\n" \
"v_mfma_f32_16x16x32_bf16 v[60:63], v[44:47], a[252:255], v[60:63]\n" \
"s_waitcnt lgkmcnt(4)\n" \
"s_add_u32 s57, s57, s49\n" \
"s_add_u32 s58, s58, s49\n" \
"s_add_u32 s59, s59, 1\n" \
"v_mov_b32 v5, v7\n" \
"v_mov_b32 v6, v11\n" \
"s_cmp_ge_u32 s59, s47\n" \
"s_cbranch_scc1 .Lp1v_out0_%=\n" \
"v_mfma_f32_16x16x32_bf16 v[64:67], v[16:19], a[0:3], 0\n" \
"s_min_u32 s61, s56, s60\n" \
"s_add_u32 m0, s55, 0\n" \
"s_nop 0\n" \
"buffer_load_dwordx4 v4, s[36:39], s61 offen offset:0 sc1 lds\n" \
"v_mov_b32_e64 v13, s59\n" \
"v_mfma_f32_16x16x32_bf16 v[68:71], v[20:23], a[0:3], 0\n" \
"ds_read_b128 v[40:43], v12 offset:3072\n" \
"s_nop 0\n" \
"v_mfma_f32_16x16x32_bf16 v[72:75], v[16:19], a[128:131], 0\n" \
"ds_read_b128 v[44:47], v12 offset:19456\n" \
"s_nop 0\n" \
"v_mfma_f32_16x16x32_bf16 v[76:79], v[20:23], a[128:131], 0\n" \
"s_waitcnt lgkmcnt(4)\n" \
"v_cvt_pk_bf16_f32 v80, v48, v49\n" \
"v_cvt_pk_bf16_f32 v81, v50, v51\n" \
"v_mfma_f32_16x16x32_bf16 v[64:67], v[24:27], a[4:7], v[64:67]\n" \
"buffer_load_dwordx4 v4, s[36:39], s61 offen offset:1024 sc1 lds\n" \
"v_mfma_f32_16x16x32_bf16 v[68:71], v[28:31], a[4:7], v[68:71]\n" \
"ds_read_b128 v[16:19], v12 offset:4096\n" \
"v_cvt_pk_bf16_f32 v82, v52, v53\n" \
"v_mfma_f32_16x16x32_bf16 v[72:75], v[24:27], a[132:135], v[72:75]\n" \
"ds_read_b128 v[20:23], v12 offset:20480\n" \
"v_cvt_pk_bf16_f32 v83, v54, v55\n" \
"v_mfma_f32_16x16x32_bf16 v[76:79], v[28:31], a[132:135], v[76:79]\n" \
"s_waitcnt lgkmcnt(4)\n" \
"buffer_store_dwordx4 v[80:83], v5, s[36:39], s57 offen sc1\n" \
"v_cvt_pk_bf16_f32 v84, v56, v57\n" \
"v_mfma_f32_16x16x32_bf16 v[64:67], v[32:35], a[8:11], v[64:67]\n" \
"s_add_u32 s63, s59, 2\n" \
"s_min_u32 s63, s63, 63\n" \
"v_mfma_f32_16x16x32_bf16 v[68:71], v[36:39], a[8:11], v[68:71]\n" \
"ds_read_b128 v[24:27], v12 offset:5120\n" \
"s_lshr_b32 s63, s63, 3\n" \
"s_lshl_b32 s63, s63, 7\n" \
"v_cvt_pk_bf16_f32 v85, v58, v59\n" \
"v_mfma_f32_16x16x32_bf16 v[72:75], v[32:35], a[136:139], v[72:75]\n" \
"ds_read_b128 v[28:31], v12 offset:21504\n" \
"v_cvt_pk_bf16_f32 v86, v60, v61\n" \
"v_mfma_f32_16x16x32_bf16 v[76:79], v[36:39], a[136:139], v[76:79]\n" \
"s_waitcnt lgkmcnt(4)\n" \
"v_cvt_pk_bf16_f32 v87, v62, v63\n" \
"buffer_store_dwordx4 v[84:87], v5, s[36:39], s58 offen sc1\n" \
"v_mfma_f32_16x16x32_bf16 v[64:67], v[40:43], a[12:15], v[64:67]\n" \
"buffer_load_dwordx4 v4, s[36:39], s61 offen offset:2048 sc1 lds\n" \
"v_mfma_f32_16x16x32_bf16 v[68:71], v[44:47], a[12:15], v[68:71]\n" \
"ds_read_b128 v[32:35], v12 offset:6144\n" \
"v_mfma_f32_16x16x32_bf16 v[72:75], v[40:43], a[140:143], v[72:75]\n" \
"ds_read_b128 v[36:39], v12 offset:22528\n" \
"v_mfma_f32_16x16x32_bf16 v[76:79], v[44:47], a[140:143], v[76:79]\n" \
"s_waitcnt lgkmcnt(4)\n" \
"v_mfma_f32_16x16x32_bf16 v[64:67], v[16:19], a[16:19], v[64:67]\n" \
"buffer_load_dwordx4 v4, s[36:39], s61 offen offset:3072 sc1 lds\n" \
"v_mfma_f32_16x16x32_bf16 v[68:71], v[20:23], a[16:19], v[68:71]\n" \
"ds_read_b128 v[40:43], v12 offset:7168\n" \
"v_mfma_f32_16x16x32_bf16 v[72:75], v[16:19], a[144:147], v[72:75]\n" \
"ds_read_b128 v[44:47], v12 offset:23552\n" \
"v_mfma_f32_16x16x32_bf16 v[76:79], v[20:23], a[144:147], v[76:79]\n" \
"s_waitcnt lgkmcnt(4)\n" \
"v_mfma_f32_16x16x32_bf16 v[64:67], v[24:27], a[20:23], v[64:67]\n" \
"buffer_load_dword v15, v14, s[36:39], s63 offen sc1\n" \
"v_mfma_f32_16x16x32_bf16 v[68:71], v[28:31], a[20:23], v[68:71]\n" \
"ds_read_b128 v[16:19], v12 offset:8192\n" \
"v_mfma_f32_16x16x32_bf16 v[72:75], v[24:27], a[148:151], v[72:75]\n" \
"ds_read_b128 v[20:23], v12 offset:24576\n" \
"v_mfma_f32_16x16x32_bf16 v[76:79], v[28:31], a[148:151], v[76:79]\n" \
"s_waitcnt lgkmcnt(4)\n" \
"v_mfma_f32_16x16x32_bf16 v[64:67], v[32:35], a[24:27], v[64:67]\n" \
"s_add_u32 m0, s55, 4096\n" \
"s_add_u32 s61, s61, 4096\n" \
"buffer_load_dwordx4 v4, s[36:39], s61 offen offset:0 sc1 lds\n" \
"v_mfma_f32_16x16x32_bf16 v[68:71], v[36:39], a[24:27], v[68:71]\n" \
"ds_read_b128 v[24:27], v12 offset:9216\n" \
"v_mfma_f32_16x16x32_bf16 v[72:75], v[32:35], a[152:155], v[72:75]\n" \
"ds_read_b128 v[28:31], v12 offset:25600\n" \
"v_mfma_f32_16x16x32_bf16 v[76:79], v[36:39], a[152:155], v[76:79]\n" \
"s_waitcnt lgkmcnt(4)\n" \
"v_mfma_f32_16x16x32_bf16 v[64:67], v[40:43], a[28:31], v[64:67]\n" \
"buffer_load_dwordx4 v4, s[36:39], s61 offen offset:1024 sc1 lds\n" \
"v_mfma_f32_16x16x32_bf16 v[68:71], v[44:47], a[28:31], v[68:71]\n" \
"ds_read_b128 v[32:35], v12 offset:10240\n" \
"v_mfma_f32_16x16x32_bf16 v[72:75], v[40:43], a[156:159], v[72:75]\n" \
"ds_read_b128 v[36:39], v12 offset:26624\n" \
"v_mfma_f32_16x16x32_bf16 v[76:79], v[44:47], a[156:159], v[76:79]\n" \
"s_waitcnt lgkmcnt(4)\n" \
"v_mfma_f32_16x16x32_bf16 v[64:67], v[16:19], a[32:35], v[64:67]\n" \
"v_mfma_f32_16x16x32_bf16 v[68:71], v[20:23], a[32:35], v[68:71]\n" \
"ds_read_b128 v[40:43], v12 offset:11264\n" \
"v_mfma_f32_16x16x32_bf16 v[72:75], v[16:19], a[160:163], v[72:75]\n" \
"ds_read_b128 v[44:47], v12 offset:27648\n" \
"v_mfma_f32_16x16x32_bf16 v[76:79], v[20:23], a[160:163], v[76:79]\n" \
"s_waitcnt lgkmcnt(4)\n" \
"v_mfma_f32_16x16x32_bf16 v[64:67], v[24:27], a[36:39], v[64:67]\n" \
"buffer_load_dwordx4 v4, s[36:39], s61 offen offset:2048 sc1 lds\n" \
"v_mfma_f32_16x16x32_bf16 v[68:71], v[28:31], a[36:39], v[68:71]\n" \
"ds_read_b128 v[16:19], v12 offset:12288\n" \
"v_mfma_f32_16x16x32_bf16 v[72:75], v[24:27], a[164:167], v[72:75]\n" \
"ds_read_b128 v[20:23], v12 offset:28672\n" \
"v_mfma_f32_16x16x32_bf16 v[76:79], v[28:31], a[164:167], v[76:79]\n" \
"s_waitcnt lgkmcnt(4)\n" \
"v_mfma_f32_16x16x32_bf16 v[64:67], v[32:35], a[40:43], v[64:67]\n" \
"buffer_load_dwordx4 v4, s[36:39], s61 offen offset:3072 sc1 lds\n" \
"v_mfma_f32_16x16x32_bf16 v[68:71], v[36:39], a[40:43], v[68:71]\n" \
"ds_read_b128 v[24:27], v12 offset:13312\n" \
"v_mfma_f32_16x16x32_bf16 v[72:75], v[32:35], a[168:171], v[72:75]\n" \
"ds_read_b128 v[28:31], v12 offset:29696\n" \
"v_mfma_f32_16x16x32_bf16 v[76:79], v[36:39], a[168:171], v[76:79]\n" \
"s_waitcnt lgkmcnt(4)\n" \
"v_mfma_f32_16x16x32_bf16 v[64:67], v[40:43], a[44:47], v[64:67]\n" \
"v_mfma_f32_16x16x32_bf16 v[68:71], v[44:47], a[44:47], v[68:71]\n" \
"ds_read_b128 v[32:35], v12 offset:14336\n" \
"v_mfma_f32_16x16x32_bf16 v[72:75], v[40:43], a[172:175], v[72:75]\n" \
"ds_read_b128 v[36:39], v12 offset:30720\n" \
"v_mfma_f32_16x16x32_bf16 v[76:79], v[44:47], a[172:175], v[76:79]\n" \
"s_waitcnt lgkmcnt(4)\n" \
"v_mfma_f32_16x16x32_bf16 v[64:67], v[16:19], a[48:51], v[64:67]\n" \
"v_mfma_f32_16x16x32_bf16 v[68:71], v[20:23], a[48:51], v[68:71]\n" \
"ds_read_b128 v[40:43], v12 offset:15360\n" \
"v_mfma_f32_16x16x32_bf16 v[72:75], v[16:19], a[176:179], v[72:75]\n" \
"ds_read_b128 v[44:47], v12 offset:31744\n" \
"v_mfma_f32_16x16x32_bf16 v[76:79], v[20:23], a[176:179], v[76:79]\n" \
"s_waitcnt lgkmcnt(4)\n" \
"v_mfma_f32_16x16x32_bf16 v[64:67], v[24:27], a[52:55], v[64:67]\n" \
"s_waitcnt vmcnt(12)\n" \
"s_barrier\n" \
"v_mfma_f32_16x16x32_bf16 v[68:71], v[28:31], a[52:55], v[68:71]\n" \
"ds_read_b128 v[16:19], v12 offset:32768\n" \
"v_mfma_f32_16x16x32_bf16 v[72:75], v[24:27], a[180:183], v[72:75]\n" \
"ds_read_b128 v[20:23], v12 offset:49152\n" \
"v_mfma_f32_16x16x32_bf16 v[76:79], v[28:31], a[180:183], v[76:79]\n" \
"s_waitcnt lgkmcnt(4)\n" \
"v_mfma_f32_16x16x32_bf16 v[64:67], v[32:35], a[56:59], v[64:67]\n" \
"v_mfma_f32_16x16x32_bf16 v[68:71], v[36:39], a[56:59], v[68:71]\n" \
"ds_read_b128 v[24:27], v12 offset:33792\n" \
"v_mfma_f32_16x16x32_bf16 v[72:75], v[32:35], a[184:187], v[72:75]\n" \
"ds_read_b128 v[28:31], v12 offset:50176\n" \
"v_mfma_f32_16x16x32_bf16 v[76:79], v[36:39], a[184:187], v[76:79]\n" \
"s_waitcnt lgkmcnt(4)\n" \
"v_mfma_f32_16x16x32_bf16 v[64:67], v[40:43], a[60:63], v[64:67]\n" \
"v_mfma_f32_16x16x32_bf16 v[68:71], v[44:47], a[60:63], v[68:71]\n" \
"ds_read_b128 v[32:35], v12 offset:34816\n" \
"v_mfma_f32_16x16x32_bf16 v[72:75], v[40:43], a[188:191], v[72:75]\n" \
"ds_read_b128 v[36:39], v12 offset:51200\n" \
"v_mfma_f32_16x16x32_bf16 v[76:79], v[44:47], a[188:191], v[76:79]\n" \
"s_waitcnt lgkmcnt(4)\n" \
"v_mfma_f32_16x16x32_bf16 v[64:67], v[16:19], a[64:67], v[64:67]\n" \
"s_min_u32 s61, s56, s60\n" \
"s_add_u32 s61, s61, 16384\n" \
"s_add_u32 s56, s56, 0x10000\n" \
"s_add_u32 m0, s55, 32768\n" \
"s_nop 0\n" \
"buffer_load_dwordx4 v4, s[36:39], s61 offen offset:0 sc1 lds\n" \
"v_mfma_f32_16x16x32_bf16 v[68:71], v[20:23], a[64:67], v[68:71]\n" \
"ds_read_b128 v[40:43], v12 offset:35840\n" \
"v_mfma_f32_16x16x32_bf16 v[72:75], v[16:19], a[192:195], v[72:75]\n" \
"ds_read_b128 v[44:47], v12 offset:52224\n" \
"v_mfma_f32_16x16x32_bf16 v[76:79], v[20:23], a[192:195], v[76:79]\n" \
"s_waitcnt lgkmcnt(4)\n" \
"v_mfma_f32_16x16x32_bf16 v[64:67], v[24:27], a[68:71], v[64:67]\n" \
"buffer_load_dwordx4 v4, s[36:39], s61 offen offset:1024 sc1 lds\n" \
"v_mfma_f32_16x16x32_bf16 v[68:71], v[28:31], a[68:71], v[68:71]\n" \
"ds_read_b128 v[16:19], v12 offset:36864\n" \
"v_mfma_f32_16x16x32_bf16 v[72:75], v[24:27], a[196:199], v[72:75]\n" \
"ds_read_b128 v[20:23], v12 offset:53248\n" \
"v_mfma_f32_16x16x32_bf16 v[76:79], v[28:31], a[196:199], v[76:79]\n" \
"s_waitcnt lgkmcnt(4)\n" \
"v_mfma_f32_16x16x32_bf16 v[64:67], v[32:35], a[72:75], v[64:67]\n" \
"v_mfma_f32_16x16x32_bf16 v[68:71], v[36:39], a[72:75], v[68:71]\n" \
"ds_read_b128 v[24:27], v12 offset:37888\n" \
"v_mfma_f32_16x16x32_bf16 v[72:75], v[32:35], a[200:203], v[72:75]\n" \
"ds_read_b128 v[28:31], v12 offset:54272\n" \
"v_mfma_f32_16x16x32_bf16 v[76:79], v[36:39], a[200:203], v[76:79]\n" \
"s_waitcnt lgkmcnt(4)\n" \
"v_mfma_f32_16x16x32_bf16 v[64:67], v[40:43], a[76:79], v[64:67]\n" \
"buffer_load_dwordx4 v4, s[36:39], s61 offen offset:2048 sc1 lds\n" \
"v_mfma_f32_16x16x32_bf16 v[68:71], v[44:47], a[76:79], v[68:71]\n" \
"ds_read_b128 v[32:35], v12 offset:38912\n" \
"v_mfma_f32_16x16x32_bf16 v[72:75], v[40:43], a[204:207], v[72:75]\n" \
"ds_read_b128 v[36:39], v12 offset:55296\n" \
"v_mfma_f32_16x16x32_bf16 v[76:79], v[44:47], a[204:207], v[76:79]\n" \
"s_waitcnt lgkmcnt(4)\n" \
"v_mfma_f32_16x16x32_bf16 v[64:67], v[16:19], a[80:83], v[64:67]\n" \
"buffer_load_dwordx4 v4, s[36:39], s61 offen offset:3072 sc1 lds\n" \
"v_mfma_f32_16x16x32_bf16 v[68:71], v[20:23], a[80:83], v[68:71]\n" \
"ds_read_b128 v[40:43], v12 offset:39936\n" \
"v_mfma_f32_16x16x32_bf16 v[72:75], v[16:19], a[208:211], v[72:75]\n" \
"ds_read_b128 v[44:47], v12 offset:56320\n" \
"v_mfma_f32_16x16x32_bf16 v[76:79], v[20:23], a[208:211], v[76:79]\n" \
"s_waitcnt lgkmcnt(4)\n" \
"v_mfma_f32_16x16x32_bf16 v[64:67], v[24:27], a[84:87], v[64:67]\n" \
"v_mfma_f32_16x16x32_bf16 v[68:71], v[28:31], a[84:87], v[68:71]\n" \
"ds_read_b128 v[16:19], v12 offset:40960\n" \
"v_mfma_f32_16x16x32_bf16 v[72:75], v[24:27], a[212:215], v[72:75]\n" \
"ds_read_b128 v[20:23], v12 offset:57344\n" \
"v_mfma_f32_16x16x32_bf16 v[76:79], v[28:31], a[212:215], v[76:79]\n" \
"s_waitcnt lgkmcnt(4)\n" \
"v_mfma_f32_16x16x32_bf16 v[64:67], v[32:35], a[88:91], v[64:67]\n" \
"s_add_u32 m0, s55, 36864\n" \
"s_add_u32 s61, s61, 4096\n" \
"buffer_load_dwordx4 v4, s[36:39], s61 offen offset:0 sc1 lds\n" \
"v_mfma_f32_16x16x32_bf16 v[68:71], v[36:39], a[88:91], v[68:71]\n" \
"ds_read_b128 v[24:27], v12 offset:41984\n" \
"v_mfma_f32_16x16x32_bf16 v[72:75], v[32:35], a[216:219], v[72:75]\n" \
"ds_read_b128 v[28:31], v12 offset:58368\n" \
"v_mfma_f32_16x16x32_bf16 v[76:79], v[36:39], a[216:219], v[76:79]\n" \
"s_waitcnt lgkmcnt(4)\n" \
"v_mfma_f32_16x16x32_bf16 v[64:67], v[40:43], a[92:95], v[64:67]\n" \
"buffer_load_dwordx4 v4, s[36:39], s61 offen offset:1024 sc1 lds\n" \
"v_mfma_f32_16x16x32_bf16 v[68:71], v[44:47], a[92:95], v[68:71]\n" \
"ds_read_b128 v[32:35], v12 offset:43008\n" \
"v_mfma_f32_16x16x32_bf16 v[72:75], v[40:43], a[220:223], v[72:75]\n" \
"ds_read_b128 v[36:39], v12 offset:59392\n" \
"v_mfma_f32_16x16x32_bf16 v[76:79], v[44:47], a[220:223], v[76:79]\n" \
"s_waitcnt lgkmcnt(4)\n" \
"v_mfma_f32_16x16x32_bf16 v[64:67], v[16:19], a[96:99], v[64:67]\n" \
"v_mfma_f32_16x16x32_bf16 v[68:71], v[20:23], a[96:99], v[68:71]\n" \
"ds_read_b128 v[40:43], v12 offset:44032\n" \
"v_mfma_f32_16x16x32_bf16 v[72:75], v[16:19], a[224:227], v[72:75]\n" \
"ds_read_b128 v[44:47], v12 offset:60416\n" \
"v_mfma_f32_16x16x32_bf16 v[76:79], v[20:23], a[224:227], v[76:79]\n" \
"s_waitcnt lgkmcnt(4)\n" \
"v_mfma_f32_16x16x32_bf16 v[64:67], v[24:27], a[100:103], v[64:67]\n" \
"buffer_load_dwordx4 v4, s[36:39], s61 offen offset:2048 sc1 lds\n" \
"v_mfma_f32_16x16x32_bf16 v[68:71], v[28:31], a[100:103], v[68:71]\n" \
"ds_read_b128 v[16:19], v12 offset:45056\n" \
"v_mfma_f32_16x16x32_bf16 v[72:75], v[24:27], a[228:231], v[72:75]\n" \
"ds_read_b128 v[20:23], v12 offset:61440\n" \
"v_mfma_f32_16x16x32_bf16 v[76:79], v[28:31], a[228:231], v[76:79]\n" \
"s_waitcnt lgkmcnt(4)\n" \
"v_mfma_f32_16x16x32_bf16 v[64:67], v[32:35], a[104:107], v[64:67]\n" \
"buffer_load_dwordx4 v4, s[36:39], s61 offen offset:3072 sc1 lds\n" \
"v_mfma_f32_16x16x32_bf16 v[68:71], v[36:39], a[104:107], v[68:71]\n" \
"ds_read_b128 v[24:27], v12 offset:46080\n" \
"v_mfma_f32_16x16x32_bf16 v[72:75], v[32:35], a[232:235], v[72:75]\n" \
"ds_read_b128 v[28:31], v12 offset:62464\n" \
"v_mfma_f32_16x16x32_bf16 v[76:79], v[36:39], a[232:235], v[76:79]\n" \
"s_waitcnt lgkmcnt(4)\n" \
"v_mfma_f32_16x16x32_bf16 v[64:67], v[40:43], a[108:111], v[64:67]\n" \
"v_mfma_f32_16x16x32_bf16 v[68:71], v[44:47], a[108:111], v[68:71]\n" \
"ds_read_b128 v[32:35], v12 offset:47104\n" \
"v_mfma_f32_16x16x32_bf16 v[72:75], v[40:43], a[236:239], v[72:75]\n" \
"ds_read_b128 v[36:39], v12 offset:63488\n" \
"v_mfma_f32_16x16x32_bf16 v[76:79], v[44:47], a[236:239], v[76:79]\n" \
"s_waitcnt lgkmcnt(4)\n" \
"v_mfma_f32_16x16x32_bf16 v[64:67], v[16:19], a[112:115], v[64:67]\n" \
"v_mfma_f32_16x16x32_bf16 v[68:71], v[20:23], a[112:115], v[68:71]\n" \
"ds_read_b128 v[40:43], v12 offset:48128\n" \
"v_mfma_f32_16x16x32_bf16 v[72:75], v[16:19], a[240:243], v[72:75]\n" \
"ds_read_b128 v[44:47], v12 offset:64512\n" \
"v_mfma_f32_16x16x32_bf16 v[76:79], v[20:23], a[240:243], v[76:79]\n" \
"s_waitcnt lgkmcnt(4)\n" \
"v_mfma_f32_16x16x32_bf16 v[64:67], v[24:27], a[116:119], v[64:67]\n" \
"s_waitcnt vmcnt(8)\n" \
"s_barrier\n" \
"v_mfma_f32_16x16x32_bf16 v[68:71], v[28:31], a[116:119], v[68:71]\n" \
"ds_read_b128 v[16:19], v10 offset:0\n" \
"v_mfma_f32_16x16x32_bf16 v[72:75], v[24:27], a[244:247], v[72:75]\n" \
"ds_read_b128 v[20:23], v10 offset:16384\n" \
"v_mfma_f32_16x16x32_bf16 v[76:79], v[28:31], a[244:247], v[76:79]\n" \
"s_waitcnt lgkmcnt(4)\n" \
"buffer_store_dword v13, v6, s[36:39], 0 offen sc1\n" \
"v_mfma_f32_16x16x32_bf16 v[64:67], v[32:35], a[120:123], v[64:67]\n" \
"v_cmp_gt_u32_e64 vcc, 16, v15\n" \
"v_mfma_f32_16x16x32_bf16 v[68:71], v[36:39], a[120:123], v[68:71]\n" \
"ds_read_b128 v[24:27], v10 offset:1024\n" \
"v_mfma_f32_16x16x32_bf16 v[72:75], v[32:35], a[248:251], v[72:75]\n" \
"ds_read_b128 v[28:31], v10 offset:17408\n" \
"v_mfma_f32_16x16x32_bf16 v[76:79], v[36:39], a[248:251], v[76:79]\n" \
"s_waitcnt lgkmcnt(4)\n" \
"v_mfma_f32_16x16x32_bf16 v[64:67], v[40:43], a[124:127], v[64:67]\n" \
"s_cbranch_vccnz .Lp1v_m1_%=\n" \
".Lp1v_m1k_%=:\n" \
"v_mfma_f32_16x16x32_bf16 v[68:71], v[44:47], a[124:127], v[68:71]\n" \
"ds_read_b128 v[32:35], v10 offset:2048\n" \
"v_mfma_f32_16x16x32_bf16 v[72:75], v[40:43], a[252:255], v[72:75]\n" \
"ds_read_b128 v[36:39], v10 offset:18432\n" \
"v_mfma_f32_16x16x32_bf16 v[76:79], v[44:47], a[252:255], v[76:79]\n" \
"s_waitcnt lgkmcnt(4)\n" \
"s_add_u32 s57, s57, s49\n" \
"s_add_u32 s58, s58, s49\n" \
"s_add_u32 s59, s59, 1\n" \
"v_mov_b32 v5, v7\n" \
"v_mov_b32 v6, v11\n" \
"s_cmp_ge_u32 s59, s47\n" \
"s_cbranch_scc0 .Lp1v_loop_%=\n" \
".Lp1v_out1_%=:\n" \
"s_nop 7\n" \
"s_nop 7\n" \
"v_cvt_pk_bf16_f32 v80, v64, v65\n" \
"v_cvt_pk_bf16_f32 v81, v66, v67\n" \
"v_cvt_pk_bf16_f32 v82, v68, v69\n" \
"v_cvt_pk_bf16_f32 v83, v70, v71\n" \
"buffer_store_dwordx4 v[80:83], v5, s[36:39], s57 offen sc1\n" \
"v_cvt_pk_bf16_f32 v84, v72, v73\n" \
"v_cvt_pk_bf16_f32 v85, v74, v75\n" \
"v_cvt_pk_bf16_f32 v86, v76, v77\n" \
"v_cvt_pk_bf16_f32 v87, v78, v79\n" \
"buffer_store_dwordx4 v[84:87], v5, s[36:39], s58 offen sc1\n" \
"s_branch .Lp1v_end_%=\n" \
".Lp1v_out0_%=:\n" \
"s_nop 7\n" \
"s_nop 7\n" \
"v_cvt_pk_bf16_f32 v80, v48, v49\n" \
"v_cvt_pk_bf16_f32 v81, v50, v51\n" \
"v_cvt_pk_bf16_f32 v82, v52, v53\n" \
"v_cvt_pk_bf16_f32 v83, v54, v55\n" \
"buffer_store_dwordx4 v[80:83], v5, s[36:39], s57 offen sc1\n" \
"v_cvt_pk_bf16_f32 v84, v56, v57\n" \
"v_cvt_pk_bf16_f32 v85, v58, v59\n" \
"v_cvt_pk_bf16_f32 v86, v60, v61\n" \
"v_cvt_pk_bf16_f32 v87, v62, v63\n" \
"buffer_store_dwordx4 v[84:87], v5, s[36:39], s58 offen sc1\n" \
".Lp1v_end_%=:\n" \
"s_waitcnt vmcnt(0) lgkmcnt(0)\n" \
"s_barrier\n" \
"v_mov_b32 v13, s59\n" \
"buffer_store_dword v13, v6, s[36:39], 0 offen sc1\n" \
"s_waitcnt vmcnt(0) lgkmcnt(0)\n" \
"s_branch .Lp1v_x_%=\n" \
".Lp1v_l0_%=:\n" \
"s_nop 7\n" \
"s_nop 7\n" \
"s_waitcnt vmcnt(0) lgkmcnt(0)\n" \
"s_mov_b32 s62, 0\n" \
".Lp1v_l0a_%=:\n" \
"buffer_load_dword v15, v14, s[36:39], s63 offen sc1\n" \
"s_waitcnt vmcnt(0)\n" \
"v_cmp_gt_u32 vcc, 16, v15\n" \
"s_nop 4\n" \
"s_cbranch_vccz .Lp1v_l0k_%=\n" \
"s_sleep 4\n" \
"s_add_u32 s62, s62, 1\n" \
"s_cmp_lt_u32 s62, 0x10000\n" \
"s_cbranch_scc1 .Lp1v_l0a_%=\n" \
"v_mov_b32 v15, 256\n" \
"buffer_store_dword v15, v15, s[36:39], 0 offen sc1\n" \
"s_waitcnt vmcnt(0) lgkmcnt(0)\n" \
"s_branch .Lp1v_l0k_%=\n" \
".Lp1v_l1_%=:\n" \
"s_nop 7\n" \
"s_nop 7\n" \
"s_waitcnt vmcnt(0) lgkmcnt(0)\n" \
"s_mov_b32 s62, 0\n" \
".Lp1v_l1a_%=:\n" \
"buffer_load_dword v15, v14, s[36:39], s63 offen sc1\n" \
"s_waitcnt vmcnt(0)\n" \
"v_cmp_gt_u32 vcc, 16, v15\n" \
"s_nop 4\n" \
"s_cbranch_vccz .Lp1v_l1k_%=\n" \
"s_sleep 4\n" \
"s_add_u32 s62, s62, 1\n" \
"s_cmp_lt_u32 s62, 0x10000\n" \
"s_cbranch_scc1 .Lp1v_l1a_%=\n" \
"v_mov_b32 v15, 256\n" \
"buffer_store_dword v15, v15, s[36:39], 0 offen sc1\n" \
"s_waitcnt vmcnt(0) lgkmcnt(0)\n" \
"s_branch .Lp1v_l1k_%=\n" \
".Lp1v_m0_%=:\n" \
"s_nop 7\n" \
"s_nop 7\n" \
"s_waitcnt vmcnt(0) lgkmcnt(0)\n" \
"s_mov_b32 s62, 0\n" \
".Lp1v_m0a_%=:\n" \
"buffer_load_dword v15, v14, s[36:39], s63 offen sc1\n" \
"s_waitcnt vmcnt(0)\n" \
"v_cmp_gt_u32 vcc, 16, v15\n" \
"s_nop 4\n" \
"s_cbranch_vccz .Lp1v_m0k_%=\n" \
"s_sleep 4\n" \
"s_add_u32 s62, s62, 1\n" \
"s_cmp_lt_u32 s62, 0x10000\n" \
"s_cbranch_scc1 .Lp1v_m0a_%=\n" \
"v_mov_b32 v15, 256\n" \
"buffer_store_dword v15, v15, s[36:39], 0 offen sc1\n" \
"s_waitcnt vmcnt(0) lgkmcnt(0)\n" \
"s_branch .Lp1v_m0k_%=\n" \
".Lp1v_m1_%=:\n" \
"s_nop 7\n" \
"s_nop 7\n" \
"s_waitcnt vmcnt(0) lgkmcnt(0)\n" \
"s_mov_b32 s62, 0\n" \
".Lp1v_m1a_%=:\n" \
"buffer_load_dword v15, v14, s[36:39], s63 offen sc1\n" \
"s_waitcnt vmcnt(0)\n" \
"v_cmp_gt_u32 vcc, 16, v15\n" \
"s_nop 4\n" \
"s_cbranch_vccz .Lp1v_m1k_%=\n" \
"s_sleep 4\n" \
"s_add_u32 s62, s62, 1\n" \
"s_cmp_lt_u32 s62, 0x10000\n" \
"s_cbranch_scc1 .Lp1v_m1a_%=\n" \
"v_mov_b32 v15, 256\n" \
"buffer_store_dword v15, v15, s[36:39], 0 offen sc1\n" \
"s_waitcnt vmcnt(0) lgkmcnt(0)\n" \
"s_branch .Lp1v_m1k_%=\n" \
".Lp1v_x_%=:\n"
#define ASM_P1_G1 \
"s_nop 7\n" \
"s_nop 7\n" \
"s_lshr_b32 s52, s48, 1\n" \
"s_lshl_b32 s52, s52, 13\n" \
"s_and_b32 s53, s48, 1\n" \
"s_lshl_b32 s53, s53, 10\n" \
"s_add_u32 s52, s52, s53\n" \
"s_mov_b32 s54, 0\n" \
"s_add_u32 m0, s52, 0\n" \
"s_nop 0\n" \
"buffer_load_dwordx4 v1, s[40:43], s54 offen nt lds\n" \
"s_add_u32 s54, s54, s44\n" \
"s_add_u32 m0, s52, 2048\n" \
"s_nop 0\n" \
"buffer_load_dwordx4 v1, s[40:43], s54 offen nt lds\n" \
"s_add_u32 s54, s54, s44\n" \
"s_add_u32 m0, s52, 4096\n" \
"s_nop 0\n" \
"buffer_load_dwordx4 v1, s[40:43], s54 offen nt lds\n" \
"s_add_u32 s54, s54, s44\n" \
"s_add_u32 m0, s52, 6144\n" \
"s_nop 0\n" \
"buffer_load_dwordx4 v1, s[40:43], s54 offen nt lds\n" \
"s_add_u32 s54, s54, s44\n" \
"s_add_u32 m0, s52, 16384\n" \
"s_nop 0\n" \
"buffer_load_dwordx4 v1, s[40:43], s54 offen nt lds\n" \
"s_add_u32 s54, s54, s44\n" \
"s_add_u32 m0, s52, 18432\n" \
"s_nop 0\n" \
"buffer_load_dwordx4 v1, s[40:43], s54 offen nt lds\n" \
"s_add_u32 s54, s54, s44\n" \
"s_add_u32 m0, s52, 20480\n" \
"s_nop 0\n" \
"buffer_load_dwordx4 v1, s[40:43], s54 offen nt lds\n" \
"s_add_u32 s54, s54, s44\n" \
"s_add_u32 m0, s52, 22528\n" \
"s_nop 0\n" \
"buffer_load_dwordx4 v1, s[40:43], s54 offen nt lds\n" \
"s_add_u32 s54, s54, s44\n" \
"s_add_u32 m0, s52, 32768\n" \
"s_nop 0\n" \
"buffer_load_dwordx4 v1, s[40:43], s54 offen nt lds\n" \
"s_add_u32 s54, s54, s44\n" \
"s_add_u32 m0, s52, 34816\n" \
"s_nop 0\n" \
"buffer_load_dwordx4 v1, s[40:43], s54 offen nt lds\n" \
"s_add_u32 s54, s54, s44\n" \
"s_add_u32 m0, s52, 36864\n" \
"s_nop 0\n" \
"buffer_load_dwordx4 v1, s[40:43], s54 offen nt lds\n" \
"s_add_u32 s54, s54, s44\n" \
"s_add_u32 m0, s52, 38912\n" \
"s_nop 0\n" \
"buffer_load_dwordx4 v1, s[40:43], s54 offen nt lds\n" \
"s_add_u32 s54, s54, s44\n" \
"s_add_u32 m0, s52, 49152\n" \
"s_nop 0\n" \
"buffer_load_dwordx4 v1, s[40:43], s54 offen nt lds\n" \
"s_add_u32 s54, s54, s44\n" \
"s_add_u32 m0, s52, 51200\n" \
"s_nop 0\n" \
"buffer_load_dwordx4 v1, s[40:43], s54 offen nt lds\n" \
"s_add_u32 s54, s54, s44\n" \
"s_add_u32 m0, s52, 53248\n" \
"s_nop 0\n" \
"buffer_load_dwordx4 v1, s[40:43], s54 offen nt lds\n" \
"s_add_u32 s54, s54, s44\n" \
"s_add_u32 m0, s52, 55296\n" \
"s_nop 0\n" \
"buffer_load_dwordx4 v1, s[40:43], s54 offen nt lds\n" \
"s_add_u32 s54, s54, s44\n" \
"s_add_u32 m0, s52, 65536\n" \
"s_nop 0\n" \
"buffer_load_dwordx4 v1, s[40:43], s54 offen nt lds\n" \
"s_add_u32 s54, s54, s44\n" \
"s_add_u32 m0, s52, 67584\n" \
"s_nop 0\n" \
"buffer_load_dwordx4 v1, s[40:43], s54 offen nt lds\n" \
"s_add_u32 s54, s54, s44\n" \
"s_add_u32 m0, s52, 69632\n" \
"s_nop 0\n" \
"buffer_load_dwordx4 v1, s[40:43], s54 offen nt lds\n" \
"s_add_u32 s54, s54, s44\n" \
"s_add_u32 m0, s52, 71680\n" \
"s_nop 0\n" \
"buffer_load_dwordx4 v1, s[40:43], s54 offen nt lds\n" \
"s_add_u32 s54, s54, s44\n" \
"s_add_u32 m0, s52, 81920\n" \
"s_nop 0\n" \
"buffer_load_dwordx4 v1, s[40:43], s54 offen nt lds\n" \
"s_add_u32 s54, s54, s44\n" \
"s_add_u32 m0, s52, 83968\n" \
"s_nop 0\n" \
"buffer_load_dwordx4 v1, s[40:43], s54 offen nt lds\n" \
"s_add_u32 s54, s54, s44\n" \
"s_add_u32 m0, s52, 86016\n" \
"s_nop 0\n" \
"buffer_load_dwordx4 v1, s[40:43], s54 offen nt lds\n" \
"s_add_u32 s54, s54, s44\n" \
"s_add_u32 m0, s52, 88064\n" \
"s_nop 0\n" \
"buffer_load_dwordx4 v1, s[40:43], s54 offen nt lds\n" \
"s_add_u32 s54, s54, s44\n" \
"s_add_u32 m0, s52, 98304\n" \
"s_nop 0\n" \
"buffer_load_dwordx4 v1, s[40:43], s54 offen nt lds\n" \
"s_add_u32 s54, s54, s44\n" \
"s_add_u32 m0, s52, 100352\n" \
"s_nop 0\n" \
"buffer_load_dwordx4 v1, s[40:43], s54 offen nt lds\n" \
"s_add_u32 s54, s54, s44\n" \
"s_add_u32 m0, s52, 102400\n" \
"s_nop 0\n" \
"buffer_load_dwordx4 v1, s[40:43], s54 offen nt lds\n" \
"s_add_u32 s54, s54, s44\n" \
"s_add_u32 m0, s52, 104448\n" \
"s_nop 0\n" \
"buffer_load_dwordx4 v1, s[40:43], s54 offen nt lds\n" \
"s_add_u32 s54, s54, s44\n" \
"s_add_u32 m0, s52, 114688\n" \
"s_nop 0\n" \
"buffer_load_dwordx4 v1, s[40:43], s54 offen nt lds\n" \
"s_add_u32 s54, s54, s44\n" \
"s_add_u32 m0, s52, 116736\n" \
"s_nop 0\n" \
"buffer_load_dwordx4 v1, s[40:43], s54 offen nt lds\n" \
"s_add_u32 s54, s54, s44\n" \
"s_add_u32 m0, s52, 118784\n" \
"s_nop 0\n" \
"buffer_load_dwordx4 v1, s[40:43], s54 offen nt lds\n" \
"s_add_u32 s54, s54, s44\n" \
"s_add_u32 m0, s52, 120832\n" \
"s_nop 0\n" \
"buffer_load_dwordx4 v1, s[40:43], s54 offen nt lds\n" \
"s_add_u32 s54, s54, s44\n" \
"v_add_u32 v8, 0x10000, v2\n" \
"v_add_u32 v9, 0x10000, v3\n" \
"s_waitcnt vmcnt(28)\n" \
"s_barrier\n" \
"ds_read_b64_tr_b16 a[0:1], v2 offset:0\n" \
"ds_read_b64_tr_b16 a[2:3], v2 offset:512\n" \
"ds_read_b64_tr_b16 a[128:129], v3 offset:0\n" \
"ds_read_b64_tr_b16 a[130:131], v3 offset:512\n" \
"ds_read_b64_tr_b16 a[4:5], v2 offset:4096\n" \
"ds_read_b64_tr_b16 a[6:7], v2 offset:4608\n" \
"ds_read_b64_tr_b16 a[132:133], v3 offset:4096\n" \
"ds_read_b64_tr_b16 a[134:135], v3 offset:4608\n" \
"s_waitcnt lgkmcnt(0)\n" \
"s_barrier\n" \
"s_add_u32 m0, s52, 0\n" \
"s_nop 0\n" \
"buffer_load_dwordx4 v1, s[40:43], s54 offen nt lds\n" \
"s_add_u32 s54, s54, s44\n" \
"s_add_u32 m0, s52, 2048\n" \
"s_nop 0\n" \
"buffer_load_dwordx4 v1, s[40:43], s54 offen nt lds\n" \
"s_add_u32 s54, s54, s44\n" \
"s_add_u32 m0, s52, 4096\n" \
"s_nop 0\n" \
"buffer_load_dwordx4 v1, s[40:43], s54 offen nt lds\n" \
"s_add_u32 s54, s54, s44\n" \
"s_add_u32 m0, s52, 6144\n" \
"s_nop 0\n" \
"buffer_load_dwordx4 v1, s[40:43], s54 offen nt lds\n" \
"s_add_u32 s54, s54, s44\n" \
"s_waitcnt vmcnt(28)\n" \
"s_barrier\n" \
"ds_read_b64_tr_b16 a[8:9], v2 offset:16384\n" \
"ds_read_b64_tr_b16 a[10:11], v2 offset:16896\n" \
"ds_read_b64_tr_b16 a[136:137], v3 offset:16384\n" \
"ds_read_b64_tr_b16 a[138:139], v3 offset:16896\n" \
"ds_read_b64_tr_b16 a[12:13], v2 offset:20480\n" \
"ds_read_b64_tr_b16 a[14:15], v2 offset:20992\n" \
"ds_read_b64_tr_b16 a[140:141], v3 offset:20480\n" \
"ds_read_b64_tr_b16 a[142:143], v3 offset:20992\n" \
"s_waitcnt lgkmcnt(0)\n" \
"s_barrier\n" \
"s_add_u32 m0, s52, 16384\n" \
"s_nop 0\n" \
"buffer_load_dwordx4 v1, s[40:43], s54 offen nt lds\n" \
"s_add_u32 s54, s54, s44\n" \
"s_add_u32 m0, s52, 18432\n" \
"s_nop 0\n" \
"buffer_load_dwordx4 v1, s[40:43], s54 offen nt lds\n" \
"s_add_u32 s54, s54, s44\n" \
"s_add_u32 m0, s52, 20480\n" \
"s_nop 0\n" \
"buffer_load_dwordx4 v1, s[40:43], s54 offen nt lds\n" \
"s_add_u32 s54, s54, s44\n" \
"s_add_u32 m0, s52, 22528\n" \
"s_nop 0\n" \
"buffer_load_dwordx4 v1, s[40:43], s54 offen nt lds\n" \
"s_add_u32 s54, s54, s44\n" \
"s_waitcnt vmcnt(28)\n" \
"s_barrier\n" \
"ds_read_b64_tr_b16 a[16:17], v2 offset:32768\n" \
"ds_read_b64_tr_b16 a[18:19], v2 offset:33280\n" \
"ds_read_b64_tr_b16 a[144:145], v3 offset:32768\n" \
"ds_read_b64_tr_b16 a[146:147], v3 offset:33280\n" \
"ds_read_b64_tr_b16 a[20:21], v2 offset:36864\n" \
"ds_read_b64_tr_b16 a[22:23], v2 offset:37376\n" \
"ds_read_b64_tr_b16 a[148:149], v3 offset:36864\n" \
"ds_read_b64_tr_b16 a[150:151], v3 offset:37376\n" \
"s_waitcnt lgkmcnt(0)\n" \
"s_barrier\n" \
"s_add_u32 m0, s52, 32768\n" \
"s_nop 0\n" \
"buffer_load_dwordx4 v1, s[40:43], s54 offen nt lds\n" \
"s_add_u32 s54, s54, s44\n" \
"s_add_u32 m0, s52, 34816\n" \
"s_nop 0\n" \
"buffer_load_dwordx4 v1, s[40:43], s54 offen nt lds\n" \
"s_add_u32 s54, s54, s44\n" \
"s_add_u32 m0, s52, 36864\n" \
"s_nop 0\n" \
"buffer_load_dwordx4 v1, s[40:43], s54 offen nt lds\n" \
"s_add_u32 s54, s54, s44\n" \
"s_add_u32 m0, s52, 38912\n" \
"s_nop 0\n" \
"buffer_load_dwordx4 v1, s[40:43], s54 offen nt lds\n" \
"s_add_u32 s54, s54, s44\n" \
"s_waitcnt vmcnt(28)\n" \
"s_barrier\n" \
"ds_read_b64_tr_b16 a[24:25], v2 offset:49152\n" \
"ds_read_b64_tr_b16 a[26:27], v2 offset:49664\n" \
"ds_read_b64_tr_b16 a[152:153], v3 offset:49152\n" \
"ds_read_b64_tr_b16 a[154:155], v3 offset:49664\n" \
"ds_read_b64_tr_b16 a[28:29], v2 offset:53248\n" \
"ds_read_b64_tr_b16 a[30:31], v2 offset:53760\n" \
"ds_read_b64_tr_b16 a[156:157], v3 offset:53248\n" \
"ds_read_b64_tr_b16 a[158:159], v3 offset:53760\n" \
"s_waitcnt lgkmcnt(0)\n" \
"s_barrier\n" \
"s_add_u32 m0, s52, 49152\n" \
"s_nop 0\n" \
"buffer_load_dwordx4 v1, s[40:43], s54 offen nt lds\n" \
"s_add_u32 s54, s54, s44\n" \
"s_add_u32 m0, s52, 51200\n" \
"s_nop 0\n" \
"buffer_load_dwordx4 v1, s[40:43], s54 offen nt lds\n" \
"s_add_u32 s54, s54, s44\n" \
"s_add_u32 m0, s52, 53248\n" \
"s_nop 0\n" \
"buffer_load_dwordx4 v1, s[40:43], s54 offen nt lds\n" \
"s_add_u32 s54, s54, s44\n" \
"s_add_u32 m0, s52, 55296\n" \
"s_nop 0\n" \
"buffer_load_dwordx4 v1, s[40:43], s54 offen nt lds\n" \
"s_add_u32 s54, s54, s44\n" \
"s_waitcnt vmcnt(28)\n" \
"s_barrier\n" \
"ds_read_b64_tr_b16 a[32:33], v8 offset:0\n" \
"ds_read_b64_tr_b16 a[34:35], v8 offset:512\n" \
"ds_read_b64_tr_b16 a[160:161], v9 offset:0\n" \
"ds_read_b64_tr_b16 a[162:163], v9 offset:512\n" \
"ds_read_b64_tr_b16 a[36:37], v8 offset:4096\n" \
"ds_read_b64_tr_b16 a[38:39], v8 offset:4608\n" \
"ds_read_b64_tr_b16 a[164:165], v9 offset:4096\n" \
"ds_read_b64_tr_b16 a[166:167], v9 offset:4608\n" \
"s_waitcnt lgkmcnt(0)\n" \
"s_barrier\n" \
"s_add_u32 m0, s52, 65536\n" \
"s_nop 0\n" \
"buffer_load_dwordx4 v1, s[40:43], s54 offen nt lds\n" \
"s_add_u32 s54, s54, s44\n" \
"s_add_u32 m0, s52, 67584\n" \
"s_nop 0\n" \
"buffer_load_dwordx4 v1, s[40:43], s54 offen nt lds\n" \
"s_add_u32 s54, s54, s44\n" \
"s_add_u32 m0, s52, 69632\n" \
"s_nop 0\n" \
"buffer_load_dwordx4 v1, s[40:43], s54 offen nt lds\n" \
"s_add_u32 s54, s54, s44\n" \
"s_add_u32 m0, s52, 71680\n" \
"s_nop 0\n" \
"buffer_load_dwordx4 v1, s[40:43], s54 offen nt lds\n" \
"s_add_u32 s54, s54, s44\n" \
"s_waitcnt vmcnt(28)\n" \
"s_barrier\n" \
"ds_read_b64_tr_b16 a[40:41], v8 offset:16384\n" \
"ds_read_b64_tr_b16 a[42:43], v8 offset:16896\n" \
"ds_read_b64_tr_b16 a[168:169], v9 offset:16384\n" \
"ds_read_b64_tr_b16 a[170:171], v9 offset:16896\n" \
"ds_read_b64_tr_b16 a[44:45], v8 offset:20480\n" \
"ds_read_b64_tr_b16 a[46:47], v8 offset:20992\n" \
"ds_read_b64_tr_b16 a[172:173], v9 offset:20480\n" \
"ds_read_b64_tr_b16 a[174:175], v9 offset:20992\n" \
"s_waitcnt lgkmcnt(0)\n" \
"s_barrier\n" \
"s_add_u32 m0, s52, 81920\n" \
"s_nop 0\n" \
"buffer_load_dwordx4 v1, s[40:43], s54 offen nt lds\n" \
"s_add_u32 s54, s54, s44\n" \
"s_add_u32 m0, s52, 83968\n" \
"s_nop 0\n" \
"buffer_load_dwordx4 v1, s[40:43], s54 offen nt lds\n" \
"s_add_u32 s54, s54, s44\n" \
"s_add_u32 m0, s52, 86016\n" \
"s_nop 0\n" \
"buffer_load_dwordx4 v1, s[40:43], s54 offen nt lds\n" \
"s_add_u32 s54, s54, s44\n" \
"s_add_u32 m0, s52, 88064\n" \
"s_nop 0\n" \
"buffer_load_dwordx4 v1, s[40:43], s54 offen nt lds\n" \
"s_add_u32 s54, s54, s44\n" \
"s_waitcnt vmcnt(28)\n" \
"s_barrier\n" \
"ds_read_b64_tr_b16 a[48:49], v8 offset:32768\n" \
"ds_read_b64_tr_b16 a[50:51], v8 offset:33280\n" \
"ds_read_b64_tr_b16 a[176:177], v9 offset:32768\n" \
"ds_read_b64_tr_b16 a[178:179], v9 offset:33280\n" \
"ds_read_b64_tr_b16 a[52:53], v8 offset:36864\n" \
"ds_read_b64_tr_b16 a[54:55], v8 offset:37376\n" \
"ds_read_b64_tr_b16 a[180:181], v9 offset:36864\n" \
"ds_read_b64_tr_b16 a[182:183], v9 offset:37376\n" \
"s_waitcnt lgkmcnt(0)\n" \
"s_barrier\n" \
"s_add_u32 m0, s52, 98304\n" \
"s_nop 0\n" \
"buffer_load_dwordx4 v1, s[40:43], s54 offen nt lds\n" \
"s_add_u32 s54, s54, s44\n" \
"s_add_u32 m0, s52, 100352\n" \
"s_nop 0\n" \
"buffer_load_dwordx4 v1, s[40:43], s54 offen nt lds\n" \
"s_add_u32 s54, s54, s44\n" \
"s_add_u32 m0, s52, 102400\n" \
"s_nop 0\n" \
"buffer_load_dwordx4 v1, s[40:43], s54 offen nt lds\n" \
"s_add_u32 s54, s54, s44\n" \
"s_add_u32 m0, s52, 104448\n" \
"s_nop 0\n" \
"buffer_load_dwordx4 v1, s[40:43], s54 offen nt lds\n" \
"s_add_u32 s54, s54, s44\n" \
"s_waitcnt vmcnt(28)\n" \
"s_barrier\n" \
"ds_read_b64_tr_b16 a[56:57], v8 offset:49152\n" \
"ds_read_b64_tr_b16 a[58:59], v8 offset:49664\n" \
"ds_read_b64_tr_b16 a[184:185], v9 offset:49152\n" \
"ds_read_b64_tr_b16 a[186:187], v9 offset:49664\n" \
"ds_read_b64_tr_b16 a[60:61], v8 offset:53248\n" \
"ds_read_b64_tr_b16 a[62:63], v8 offset:53760\n" \
"ds_read_b64_tr_b16 a[188:189], v9 offset:53248\n" \
"ds_read_b64_tr_b16 a[190:191], v9 offset:53760\n" \
"s_waitcnt lgkmcnt(0)\n" \
"s_barrier\n" \
"s_add_u32 m0, s52, 114688\n" \
"s_nop 0\n" \
"buffer_load_dwordx4 v1, s[40:43], s54 offen nt lds\n" \
"s_add_u32 s54, s54, s44\n" \
"s_add_u32 m0, s52, 116736\n" \
"s_nop 0\n" \
"buffer_load_dwordx4 v1, s[40:43], s54 offen nt lds\n" \
"s_add_u32 s54, s54, s44\n" \
"s_add_u32 m0, s52, 118784\n" \
"s_nop 0\n" \
"buffer_load_dwordx4 v1, s[40:43], s54 offen nt lds\n" \
"s_add_u32 s54, s54, s44\n" \
"s_add_u32 m0, s52, 120832\n" \
"s_nop 0\n" \
"buffer_load_dwordx4 v1, s[40:43], s54 offen nt lds\n" \
"s_add_u32 s54, s54, s44\n" \
"s_waitcnt vmcnt(28)\n" \
"s_barrier\n" \
"ds_read_b64_tr_b16 a[64:65], v2 offset:0\n" \
"ds_read_b64_tr_b16 a[66:67], v2 offset:512\n" \
"ds_read_b64_tr_b16 a[192:193], v3 offset:0\n" \
"ds_read_b64_tr_b16 a[194:195], v3 offset:512\n" \
"ds_read_b64_tr_b16 a[68:69], v2 offset:4096\n" \
"ds_read_b64_tr_b16 a[70:71], v2 offset:4608\n" \
"ds_read_b64_tr_b16 a[196:197], v3 offset:4096\n" \
"ds_read_b64_tr_b16 a[198:199], v3 offset:4608\n" \
"s_waitcnt vmcnt(24)\n" \
"s_barrier\n" \
"ds_read_b64_tr_b16 a[72:73], v2 offset:16384\n" \
"ds_read_b64_tr_b16 a[74:75], v2 offset:16896\n" \
"ds_read_b64_tr_b16 a[200:201], v3 offset:16384\n" \
"ds_read_b64_tr_b16 a[202:203], v3 offset:16896\n" \
"ds_read_b64_tr_b16 a[76:77], v2 offset:20480\n" \
"ds_read_b64_tr_b16 a[78:79], v2 offset:20992\n" \
"ds_read_b64_tr_b16 a[204:205], v3 offset:20480\n" \
"ds_read_b64_tr_b16 a[206:207], v3 offset:20992\n" \
"s_waitcnt vmcnt(20)\n" \
"s_barrier\n" \
"ds_read_b64_tr_b16 a[80:81], v2 offset:32768\n" \
"ds_read_b64_tr_b16 a[82:83], v2 offset:33280\n" \
"ds_read_b64_tr_b16 a[208:209], v3 offset:32768\n" \
"ds_read_b64_tr_b16 a[210:211], v3 offset:33280\n" \
"ds_read_b64_tr_b16 a[84:85], v2 offset:36864\n" \
"ds_read_b64_tr_b16 a[86:87], v2 offset:37376\n" \
"ds_read_b64_tr_b16 a[212:213], v3 offset:36864\n" \
"ds_read_b64_tr_b16 a[214:215], v3 offset:37376\n" \
"s_waitcnt vmcnt(16)\n" \
"s_barrier\n" \
"ds_read_b64_tr_b16 a[88:89], v2 offset:49152\n" \
"ds_read_b64_tr_b16 a[90:91], v2 offset:49664\n" \
"ds_read_b64_tr_b16 a[216:217], v3 offset:49152\n" \
"ds_read_b64_tr_b16 a[218:219], v3 offset:49664\n" \
"ds_read_b64_tr_b16 a[92:93], v2 offset:53248\n" \
"ds_read_b64_tr_b16 a[94:95], v2 offset:53760\n" \
"ds_read_b64_tr_b16 a[220:221], v3 offset:53248\n" \
"ds_read_b64_tr_b16 a[222:223], v3 offset:53760\n" \
"s_waitcnt vmcnt(12)\n" \
"s_barrier\n" \
"ds_read_b64_tr_b16 a[96:97], v8 offset:0\n" \
"ds_read_b64_tr_b16 a[98:99], v8 offset:512\n" \
"ds_read_b64_tr_b16 a[224:225], v9 offset:0\n" \
"ds_read_b64_tr_b16 a[226:227], v9 offset:512\n" \
"ds_read_b64_tr_b16 a[100:101], v8 offset:4096\n" \
"ds_read_b64_tr_b16 a[102:103], v8 offset:4608\n" \
"ds_read_b64_tr_b16 a[228:229], v9 offset:4096\n" \
"ds_read_b64_tr_b16 a[230:231], v9 offset:4608\n" \
"s_waitcnt vmcnt(8)\n" \
"s_barrier\n" \
"ds_read_b64_tr_b16 a[104:105], v8 offset:16384\n" \
"ds_read_b64_tr_b16 a[106:107], v8 offset:16896\n" \
"ds_read_b64_tr_b16 a[232:233], v9 offset:16384\n" \
"ds_read_b64_tr_b16 a[234:235], v9 offset:16896\n" \
"ds_read_b64_tr_b16 a[108:109], v8 offset:20480\n" \
"ds_read_b64_tr_b16 a[110:111], v8 offset:20992\n" \
"ds_read_b64_tr_b16 a[236:237], v9 offset:20480\n" \
"ds_read_b64_tr_b16 a[238:239], v9 offset:20992\n" \
"s_waitcnt vmcnt(4)\n" \
"s_barrier\n" \
"ds_read_b64_tr_b16 a[112:113], v8 offset:32768\n" \
"ds_read_b64_tr_b16 a[114:115], v8 offset:33280\n" \
"ds_read_b64_tr_b16 a[240:241], v9 offset:32768\n" \
"ds_read_b64_tr_b16 a[242:243], v9 offset:33280\n" \
"ds_read_b64_tr_b16 a[116:117], v8 offset:36864\n" \
"ds_read_b64_tr_b16 a[118:119], v8 offset:37376\n" \
"ds_read_b64_tr_b16 a[244:245], v9 offset:36864\n" \
"ds_read_b64_tr_b16 a[246:247], v9 offset:37376\n" \
"s_waitcnt vmcnt(0)\n" \
"s_barrier\n" \
"ds_read_b64_tr_b16 a[120:121], v8 offset:49152\n" \
"ds_read_b64_tr_b16 a[122:123], v8 offset:49664\n" \
"ds_read_b64_tr_b16 a[248:249], v9 offset:49152\n" \
"ds_read_b64_tr_b16 a[250:251], v9 offset:49664\n" \
"ds_read_b64_tr_b16 a[124:125], v8 offset:53248\n" \
"ds_read_b64_tr_b16 a[126:127], v8 offset:53760\n" \
"ds_read_b64_tr_b16 a[252:253], v9 offset:53248\n" \
"ds_read_b64_tr_b16 a[254:255], v9 offset:53760\n" \
"s_waitcnt lgkmcnt(0)\n" \
"s_barrier\n" \
"v_mov_b32 v14, 4096\n" \
"s_mov_b32 s59, s46\n" \
"s_add_u32 s63, s59, 0\n" \
"s_min_u32 s63, s63, 63\n" \
"s_lshr_b32 s63, s63, 3\n" \
"s_lshl_b32 s63, s63, 7\n" \
"s_branch .Lp1g1_l0_%=\n" \
".Lp1g1_l0k_%=:\n" \
"s_add_u32 s63, s59, 1\n" \
"s_min_u32 s63, s63, 63\n" \
"s_lshr_b32 s63, s63, 3\n" \
"s_lshl_b32 s63, s63, 7\n" \
"s_branch .Lp1g1_l1_%=\n" \
".Lp1g1_l1k_%=:\n" \
"s_lshl_b32 s55, s48, 13\n" \
"s_lshl_b32 s56, s46, 16\n" \
"s_lshl_b32 s60, s47, 16\n" \
"s_sub_u32 s60, s60, 0x10000\n" \
"s_mul_i32 s57, s46, s49\n" \
"s_sub_u32 s57, s57, s49\n" \
"s_add_u32 s58, s57, s50\n" \
"v_mov_b32 v7, v5\n" \
"v_mov_b32 v5, 0x7ffffff0\n" \
"v_mov_b32 v10, v0\n" \
"v_mov_b32 v11, v6\n" \
"v_mov_b32 v6, 0x7ffffff0\n" \
"s_mov_b32 s59, s46\n" \
"s_min_u32 s61, s56, s60\n" \
"s_add_u32 m0, s55, 0\n" \
"s_nop 0\n" \
"buffer_load_dwordx4 v4, s[36:39], s61 offen offset:0 sc1 lds\n" \
"buffer_load_dwordx4 v4, s[36:39], s61 offen offset:1024 sc1 lds\n" \
"buffer_load_dwordx4 v4, s[36:39], s61 offen offset:2048 sc1 lds\n" \
"buffer_load_dwordx4 v4, s[36:39], s61 offen offset:3072 sc1 lds\n" \
"s_add_u32 m0, s55, 4096\n" \
"s_add_u32 s61, s61, 4096\n" \
"buffer_load_dwordx4 v4, s[36:39], s61 offen offset:0 sc1 lds\n" \
"buffer_load_dwordx4 v4, s[36:39], s61 offen offset:1024 sc1 lds\n" \
"buffer_load_dwordx4 v4, s[36:39], s61 offen offset:2048 sc1 lds\n" \
"buffer_load_dwordx4 v4, s[36:39], s61 offen offset:3072 sc1 lds\n" \
"s_min_u32 s61, s56, s60\n" \
"s_add_u32 s61, s61, 16384\n" \
"s_add_u32 s56, s56, 0x10000\n" \
"s_add_u32 m0, s55, 32768\n" \
"s_nop 0\n" \
"buffer_load_dwordx4 v4, s[36:39], s61 offen offset:0 sc1 lds\n" \
"buffer_load_dwordx4 v4, s[36:39], s61 offen offset:1024 sc1 lds\n" \
"buffer_load_dwordx4 v4, s[36:39], s61 offen offset:2048 sc1 lds\n" \
"buffer_load_dwordx4 v4, s[36:39], s61 offen offset:3072 sc1 lds\n" \
"s_add_u32 m0, s55, 36864\n" \
"s_add_u32 s61, s61, 4096\n" \
"buffer_load_dwordx4 v4, s[36:39], s61 offen offset:0 sc1 lds\n" \
"buffer_load_dwordx4 v4, s[36:39], s61 offen offset:1024 sc1 lds\n" \
"buffer_load_dwordx4 v4, s[36:39], s61 offen offset:2048 sc1 lds\n" \
"buffer_load_dwordx4 v4, s[36:39], s61 offen offset:3072 sc1 lds\n" \
"s_mov_b32 s70, 0\n" \
"s_mov_b32 s72, 0\n" \
"s_waitcnt vmcnt(0) lgkmcnt(0)\n" \
"s_barrier\n" \
"v_add_u32 v12, 0x10000, v10\n" \
"ds_read_b128 v[16:19], v10 offset:0\n" \
"ds_read_b128 v[20:23], v10 offset:16384\n" \
"ds_read_b128 v[24:27], v10 offset:1024\n" \
"ds_read_b128 v[28:31], v10 offset:17408\n" \
"ds_read_b128 v[32:35], v10 offset:2048\n" \
"ds_read_b128 v[36:39], v10 offset:18432\n" \
"s_waitcnt lgkmcnt(4)\n" \
"s_nop 7\n" \
"s_nop 7\n" \
".p2align 6\n" \
".Lp1g1_loop_%=:\n" \
"v_mfma_f32_16x16x32_bf16 v[48:51], a[0:3], v[16:19], 0\n" \
"s_min_u32 s61, s56, s60\n" \
"s_add_u32 m0, s55, 65536\n" \
"s_nop 0\n" \
"buffer_load_dwordx4 v4, s[36:39], s61 offen offset:0 sc1 lds\n" \
"v_add_u32_e64 v13, -1, s59\n" \
"v_mfma_f32_16x16x32_bf16 v[52:55], a[0:3], v[20:23], 0\n" \
"ds_read_b128 v[40:43], v10 offset:3072\n" \
"s_nop 0\n" \
"v_mfma_f32_16x16x32_bf16 v[56:59], a[128:131], v[16:19], 0\n" \
"ds_read_b128 v[44:47], v10 offset:19456\n" \
"s_nop 0\n" \
"v_mfma_f32_16x16x32_bf16 v[60:63], a[128:131], v[20:23], 0\n" \
"s_waitcnt lgkmcnt(4)\n" \
"v_mul_f32 v64, 0xbfb8aa3b, v64\n" \
"v_mul_f32 v65, 0xbfb8aa3b, v65\n" \
"v_mfma_f32_16x16x32_bf16 v[48:51], a[4:7], v[24:27], v[48:51]\n" \
"buffer_load_dwordx4 v4, s[36:39], s61 offen offset:1024 sc1 lds\n" \
"v_mfma_f32_16x16x32_bf16 v[52:55], a[4:7], v[28:31], v[52:55]\n" \
"ds_read_b128 v[16:19], v10 offset:4096\n" \
"v_mul_f32 v66, 0xbfb8aa3b, v66\n" \
"v_mfma_f32_16x16x32_bf16 v[56:59], a[132:135], v[24:27], v[56:59]\n" \
"ds_read_b128 v[20:23], v10 offset:20480\n" \
"v_mul_f32 v67, 0xbfb8aa3b, v67\n" \
"v_mfma_f32_16x16x32_bf16 v[60:63], a[132:135], v[28:31], v[60:63]\n" \
"s_waitcnt lgkmcnt(4)\n" \
"v_exp_f32_e64 v64, v64\n" \
"v_mfma_f32_16x16x32_bf16 v[48:51], a[8:11], v[32:35], v[48:51]\n" \
"s_add_u32 s63, s59, 2\n" \
"s_min_u32 s63, s63, 63\n" \
"v_mfma_f32_16x16x32_bf16 v[52:55], a[8:11], v[36:39], v[52:55]\n" \
"ds_read_b128 v[24:27], v10 offset:5120\n" \
"s_lshr_b32 s63, s63, 3\n" \
"s_lshl_b32 s63, s63, 7\n" \
"v_mfma_f32_16x16x32_bf16 v[56:59], a[136:139], v[32:35], v[56:59]\n" \
"ds_read_b128 v[28:31], v10 offset:21504\n" \
"v_mfma_f32_16x16x32_bf16 v[60:63], a[136:139], v[36:39], v[60:63]\n" \
"s_waitcnt lgkmcnt(4)\n" \
"v_exp_f32 v65, v65\n" \
"v_mfma_f32_16x16x32_bf16 v[48:51], a[12:15], v[40:43], v[48:51]\n" \
"buffer_load_dwordx4 v4, s[36:39], s61 offen offset:2048 sc1 lds\n" \
"v_mfma_f32_16x16x32_bf16 v[52:55], a[12:15], v[44:47], v[52:55]\n" \
"ds_read_b128 v[32:35], v10 offset:6144\n" \
"v_mfma_f32_16x16x32_bf16 v[56:59], a[140:143], v[40:43], v[56:59]\n" \
"ds_read_b128 v[36:39], v10 offset:22528\n" \
"v_mfma_f32_16x16x32_bf16 v[60:63], a[140:143], v[44:47], v[60:63]\n" \
"s_waitcnt lgkmcnt(4)\n" \
"v_exp_f32 v66, v66\n" \
"v_mfma_f32_16x16x32_bf16 v[48:51], a[16:19], v[16:19], v[48:51]\n" \
"buffer_load_dwordx4 v4, s[36:39], s61 offen offset:3072 sc1 lds\n" \
"v_mfma_f32_16x16x32_bf16 v[52:55], a[16:19], v[20:23], v[52:55]\n" \
"ds_read_b128 v[40:43], v10 offset:7168\n" \
"v_mfma_f32_16x16x32_bf16 v[56:59], a[144:147], v[16:19], v[56:59]\n" \
"ds_read_b128 v[44:47], v10 offset:23552\n" \
"v_mfma_f32_16x16x32_bf16 v[60:63], a[144:147], v[20:23], v[60:63]\n" \
"s_waitcnt lgkmcnt(4)\n" \
"v_exp_f32 v67, v67\n" \
"v_mfma_f32_16x16x32_bf16 v[48:51], a[20:23], v[24:27], v[48:51]\n" \
"buffer_load_dword v15, v14, s[36:39], s63 offen sc1\n" \
"v_mfma_f32_16x16x32_bf16 v[52:55], a[20:23], v[28:31], v[52:55]\n" \
"ds_read_b128 v[16:19], v10 offset:8192\n" \
"v_add_f32_e64 v64, 1.0, v64\n" \
"v_mfma_f32_16x16x32_bf16 v[56:59], a[148:151], v[24:27], v[56:59]\n" \
"ds_read_b128 v[20:23], v10 offset:24576\n" \
"v_add_f32_e64 v65, 1.0, v65\n" \
"v_mfma_f32_16x16x32_bf16 v[60:63], a[148:151], v[28:31], v[60:63]\n" \
"s_waitcnt lgkmcnt(4)\n" \
"v_add_f32 v66, 1.0, v66\n" \
"v_add_f32_e64 v67, 1.0, v67\n" \
"v_mfma_f32_16x16x32_bf16 v[48:51], a[24:27], v[32:35], v[48:51]\n" \
"s_add_u32 m0, s55, 69632\n" \
"s_add_u32 s61, s61, 4096\n" \
"buffer_load_dwordx4 v4, s[36:39], s61 offen offset:0 sc1 lds\n" \
"v_mfma_f32_16x16x32_bf16 v[52:55], a[24:27], v[36:39], v[52:55]\n" \
"ds_read_b128 v[24:27], v10 offset:9216\n" \
"v_mfma_f32_16x16x32_bf16 v[56:59], a[152:155], v[32:35], v[56:59]\n" \
"ds_read_b128 v[28:31], v10 offset:25600\n" \
"v_mfma_f32_16x16x32_bf16 v[60:63], a[152:155], v[36:39], v[60:63]\n" \
"s_waitcnt lgkmcnt(4)\n" \
"v_rcp_f32 v64, v64\n" \
"v_mfma_f32_16x16x32_bf16 v[48:51], a[28:31], v[40:43], v[48:51]\n" \
"buffer_load_dwordx4 v4, s[36:39], s61 offen offset:1024 sc1 lds\n" \
"v_mfma_f32_16x16x32_bf16 v[52:55], a[28:31], v[44:47], v[52:55]\n" \
"ds_read_b128 v[32:35], v10 offset:10240\n" \
"v_mfma_f32_16x16x32_bf16 v[56:59], a[156:159], v[40:43], v[56:59]\n" \
"ds_read_b128 v[36:39], v10 offset:26624\n" \
"v_mfma_f32_16x16x32_bf16 v[60:63], a[156:159], v[44:47], v[60:63]\n" \
"s_waitcnt lgkmcnt(4)\n" \
"v_rcp_f32 v65, v65\n" \
"v_mfma_f32_16x16x32_bf16 v[48:51], a[32:35], v[16:19], v[48:51]\n" \
"v_rcp_f32_e64 v66, v66\n" \
"v_mfma_f32_16x16x32_bf16 v[52:55], a[32:35], v[20:23], v[52:55]\n" \
"ds_read_b128 v[40:43], v10 offset:11264\n" \
"v_mfma_f32_16x16x32_bf16 v[56:59], a[160:163], v[16:19], v[56:59]\n" \
"ds_read_b128 v[44:47], v10 offset:27648\n" \
"v_mfma_f32_16x16x32_bf16 v[60:63], a[160:163], v[20:23], v[60:63]\n" \
"s_waitcnt lgkmcnt(4)\n" \
"v_rcp_f32 v67, v67\n" \
"v_mfma_f32_16x16x32_bf16 v[48:51], a[36:39], v[24:27], v[48:51]\n" \
"buffer_load_dwordx4 v4, s[36:39], s61 offen offset:2048 sc1 lds\n" \
"v_mfma_f32_16x16x32_bf16 v[52:55], a[36:39], v[28:31], v[52:55]\n" \
"ds_read_b128 v[16:19], v10 offset:12288\n" \
"v_mul_f32_e64 v64, v64, v72\n" \
"v_mfma_f32_16x16x32_bf16 v[56:59], a[164:167], v[24:27], v[56:59]\n" \
"ds_read_b128 v[20:23], v10 offset:28672\n" \
"v_mul_f32_e64 v65, v65, v73\n" \
"v_mfma_f32_16x16x32_bf16 v[60:63], a[164:167], v[28:31], v[60:63]\n" \
"s_waitcnt lgkmcnt(4)\n" \
"v_mul_f32 v66, v66, v74\n" \
"v_mul_f32_e64 v67, v67, v75\n" \
"v_mfma_f32_16x16x32_bf16 v[48:51], a[40:43], v[32:35], v[48:51]\n" \
"buffer_load_dwordx4 v4, s[36:39], s61 offen offset:3072 sc1 lds\n" \
"v_mfma_f32_16x16x32_bf16 v[52:55], a[40:43], v[36:39], v[52:55]\n" \
"ds_read_b128 v[24:27], v10 offset:13312\n" \
"v_cvt_pk_bf16_f32 v80, v64, v65\n" \
"v_mfma_f32_16x16x32_bf16 v[56:59], a[168:171], v[32:35], v[56:59]\n" \
"ds_read_b128 v[28:31], v10 offset:29696\n" \
"v_cvt_pk_bf16_f32 v81, v66, v67\n" \
"v_mfma_f32_16x16x32_bf16 v[60:63], a[168:171], v[36:39], v[60:63]\n" \
"s_waitcnt lgkmcnt(4)\n" \
"v_mul_f32 v68, 0xbfb8aa3b, v68\n" \
"v_mul_f32 v69, 0xbfb8aa3b, v69\n" \
"v_mfma_f32_16x16x32_bf16 v[48:51], a[44:47], v[40:43], v[48:51]\n" \
"v_mul_f32 v70, 0xbfb8aa3b, v70\n" \
"v_mul_f32 v71, 0xbfb8aa3b, v71\n" \
"v_mfma_f32_16x16x32_bf16 v[52:55], a[44:47], v[44:47], v[52:55]\n" \
"ds_read_b128 v[32:35], v10 offset:14336\n" \
"v_mfma_f32_16x16x32_bf16 v[56:59], a[172:175], v[40:43], v[56:59]\n" \
"ds_read_b128 v[36:39], v10 offset:30720\n" \
"v_mfma_f32_16x16x32_bf16 v[60:63], a[172:175], v[44:47], v[60:63]\n" \
"s_waitcnt lgkmcnt(4)\n" \
"v_exp_f32_e64 v68, v68\n" \
"v_mfma_f32_16x16x32_bf16 v[48:51], a[48:51], v[16:19], v[48:51]\n" \
"v_exp_f32_e64 v69, v69\n" \
"v_mfma_f32_16x16x32_bf16 v[52:55], a[48:51], v[20:23], v[52:55]\n" \
"ds_read_b128 v[40:43], v10 offset:15360\n" \
"v_mfma_f32_16x16x32_bf16 v[56:59], a[176:179], v[16:19], v[56:59]\n" \
"ds_read_b128 v[44:47], v10 offset:31744\n" \
"v_mfma_f32_16x16x32_bf16 v[60:63], a[176:179], v[20:23], v[60:63]\n" \
"s_waitcnt lgkmcnt(4)\n" \
"v_exp_f32 v70, v70\n" \
"v_mfma_f32_16x16x32_bf16 v[48:51], a[52:55], v[24:27], v[48:51]\n" \
"s_waitcnt vmcnt(9)\n" \
"s_barrier\n" \
"v_mfma_f32_16x16x32_bf16 v[52:55], a[52:55], v[28:31], v[52:55]\n" \
"ds_read_b128 v[16:19], v10 offset:32768\n" \
"v_mfma_f32_16x16x32_bf16 v[56:59], a[180:183], v[24:27], v[56:59]\n" \
"ds_read_b128 v[20:23], v10 offset:49152\n" \
"v_mfma_f32_16x16x32_bf16 v[60:63], a[180:183], v[28:31], v[60:63]\n" \
"s_waitcnt lgkmcnt(4)\n" \
"buffer_store_dword v13, v6, s[36:39], 0 offen sc1\n" \
"v_exp_f32 v71, v71\n" \
"v_mfma_f32_16x16x32_bf16 v[48:51], a[56:59], v[32:35], v[48:51]\n" \
"v_add_f32 v68, 1.0, v68\n" \
"v_add_f32 v69, 1.0, v69\n" \
"v_mfma_f32_16x16x32_bf16 v[52:55], a[56:59], v[36:39], v[52:55]\n" \
"ds_read_b128 v[24:27], v10 offset:33792\n" \
"v_add_f32_e64 v70, 1.0, v70\n" \
"v_mfma_f32_16x16x32_bf16 v[56:59], a[184:187], v[32:35], v[56:59]\n" \
"ds_read_b128 v[28:31], v10 offset:50176\n" \
"v_add_f32_e64 v71, 1.0, v71\n" \
"v_mfma_f32_16x16x32_bf16 v[60:63], a[184:187], v[36:39], v[60:63]\n" \
"s_waitcnt lgkmcnt(4)\n" \
"v_rcp_f32 v68, v68\n" \
"v_mfma_f32_16x16x32_bf16 v[48:51], a[60:63], v[40:43], v[48:51]\n" \
"v_rcp_f32_e64 v69, v69\n" \
"v_mfma_f32_16x16x32_bf16 v[52:55], a[60:63], v[44:47], v[52:55]\n" \
"ds_read_b128 v[32:35], v10 offset:34816\n" \
"v_mfma_f32_16x16x32_bf16 v[56:59], a[188:191], v[40:43], v[56:59]\n" \
"ds_read_b128 v[36:39], v10 offset:51200\n" \
"v_mfma_f32_16x16x32_bf16 v[60:63], a[188:191], v[44:47], v[60:63]\n" \
"s_waitcnt lgkmcnt(4)\n" \
"v_rcp_f32 v70, v70\n" \
"v_mfma_f32_16x16x32_bf16 v[48:51], a[64:67], v[16:19], v[48:51]\n" \
"s_min_u32 s61, s56, s60\n" \
"s_add_u32 s61, s61, 16384\n" \
"s_add_u32 s56, s56, 0x10000\n" \
"s_add_u32 m0, s55, 98304\n" \
"s_nop 0\n" \
"buffer_load_dwordx4 v4, s[36:39], s61 offen offset:0 sc1 lds\n" \
"v_mfma_f32_16x16x32_bf16 v[52:55], a[64:67], v[20:23], v[52:55]\n" \
"ds_read_b128 v[40:43], v10 offset:35840\n" \
"v_mfma_f32_16x16x32_bf16 v[56:59], a[192:195], v[16:19], v[56:59]\n" \
"ds_read_b128 v[44:47], v10 offset:52224\n" \
"v_mfma_f32_16x16x32_bf16 v[60:63], a[192:195], v[20:23], v[60:63]\n" \
"s_waitcnt lgkmcnt(4)\n" \
"v_rcp_f32 v71, v71\n" \
"v_mfma_f32_16x16x32_bf16 v[48:51], a[68:71], v[24:27], v[48:51]\n" \
"buffer_load_dwordx4 v4, s[36:39], s61 offen offset:1024 sc1 lds\n" \
"v_mfma_f32_16x16x32_bf16 v[52:55], a[68:71], v[28:31], v[52:55]\n" \
"ds_read_b128 v[16:19], v10 offset:36864\n" \
"v_mul_f32_e64 v68, v68, v76\n" \
"v_mfma_f32_16x16x32_bf16 v[56:59], a[196:199], v[24:27], v[56:59]\n" \
"ds_read_b128 v[20:23], v10 offset:53248\n" \
"v_mul_f32_e64 v69, v69, v77\n" \
"v_mfma_f32_16x16x32_bf16 v[60:63], a[196:199], v[28:31], v[60:63]\n" \
"s_waitcnt lgkmcnt(4)\n" \
"v_mul_f32 v70, v70, v78\n" \
"v_mul_f32_e64 v71, v71, v79\n" \
"v_mfma_f32_16x16x32_bf16 v[48:51], a[72:75], v[32:35], v[48:51]\n" \
"v_cvt_pk_bf16_f32 v82, v68, v69\n" \
"v_cvt_pk_bf16_f32 v83, v70, v71\n" \
"v_mfma_f32_16x16x32_bf16 v[52:55], a[72:75], v[36:39], v[52:55]\n" \
"ds_read_b128 v[24:27], v10 offset:37888\n" \
"s_nop 0\n" \
"v_mfma_f32_16x16x32_bf16 v[56:59], a[200:203], v[32:35], v[56:59]\n" \
"ds_read_b128 v[28:31], v10 offset:54272\n" \
"s_nop 0\n" \
"v_mfma_f32_16x16x32_bf16 v[60:63], a[200:203], v[36:39], v[60:63]\n" \
"s_waitcnt lgkmcnt(4)\n" \
"v_permlane16_swap_b32 v80, v82\n" \
"v_permlane16_swap_b32 v81, v83\n" \
"v_mfma_f32_16x16x32_bf16 v[48:51], a[76:79], v[40:43], v[48:51]\n" \
"buffer_load_dwordx4 v4, s[36:39], s61 offen offset:2048 sc1 lds\n" \
"v_mfma_f32_16x16x32_bf16 v[52:55], a[76:79], v[44:47], v[52:55]\n" \
"ds_read_b128 v[32:35], v10 offset:38912\n" \
"buffer_store_dwordx4 v[80:83], v5, s[36:39], s57 offen sc1\n" \
"v_mfma_f32_16x16x32_bf16 v[56:59], a[204:207], v[40:43], v[56:59]\n" \
"ds_read_b128 v[36:39], v10 offset:55296\n" \
"v_mfma_f32_16x16x32_bf16 v[60:63], a[204:207], v[44:47], v[60:63]\n" \
"s_waitcnt lgkmcnt(4)\n" \
"v_mfma_f32_16x16x32_bf16 v[48:51], a[80:83], v[16:19], v[48:51]\n" \
"buffer_load_dwordx4 v4, s[36:39], s61 offen offset:3072 sc1 lds\n" \
"v_mfma_f32_16x16x32_bf16 v[52:55], a[80:83], v[20:23], v[52:55]\n" \
"ds_read_b128 v[40:43], v10 offset:39936\n" \
"v_mfma_f32_16x16x32_bf16 v[56:59], a[208:211], v[16:19], v[56:59]\n" \
"ds_read_b128 v[44:47], v10 offset:56320\n" \
"v_mfma_f32_16x16x32_bf16 v[60:63], a[208:211], v[20:23], v[60:63]\n" \
"s_waitcnt lgkmcnt(4)\n" \
"v_mfma_f32_16x16x32_bf16 v[48:51], a[84:87], v[24:27], v[48:51]\n" \
"v_mfma_f32_16x16x32_bf16 v[52:55], a[84:87], v[28:31], v[52:55]\n" \
"ds_read_b128 v[16:19], v10 offset:40960\n" \
"v_mfma_f32_16x16x32_bf16 v[56:59], a[212:215], v[24:27], v[56:59]\n" \
"ds_read_b128 v[20:23], v10 offset:57344\n" \
"v_mfma_f32_16x16x32_bf16 v[60:63], a[212:215], v[28:31], v[60:63]\n" \
"s_waitcnt lgkmcnt(4)\n" \
"v_mfma_f32_16x16x32_bf16 v[48:51], a[88:91], v[32:35], v[48:51]\n" \
"s_add_u32 m0, s55, 102400\n" \
"s_add_u32 s61, s61, 4096\n" \
"buffer_load_dwordx4 v4, s[36:39], s61 offen offset:0 sc1 lds\n" \
"v_mfma_f32_16x16x32_bf16 v[52:55], a[88:91], v[36:39], v[52:55]\n" \
"ds_read_b128 v[24:27], v10 offset:41984\n" \
"v_mfma_f32_16x16x32_bf16 v[56:59], a[216:219], v[32:35], v[56:59]\n" \
"ds_read_b128 v[28:31], v10 offset:58368\n" \
"v_mfma_f32_16x16x32_bf16 v[60:63], a[216:219], v[36:39], v[60:63]\n" \
"s_waitcnt lgkmcnt(4)\n" \
"v_mfma_f32_16x16x32_bf16 v[48:51], a[92:95], v[40:43], v[48:51]\n" \
"buffer_load_dwordx4 v4, s[36:39], s61 offen offset:1024 sc1 lds\n" \
"v_mfma_f32_16x16x32_bf16 v[52:55], a[92:95], v[44:47], v[52:55]\n" \
"ds_read_b128 v[32:35], v10 offset:43008\n" \
"v_mfma_f32_16x16x32_bf16 v[56:59], a[220:223], v[40:43], v[56:59]\n" \
"ds_read_b128 v[36:39], v10 offset:59392\n" \
"v_mfma_f32_16x16x32_bf16 v[60:63], a[220:223], v[44:47], v[60:63]\n" \
"s_waitcnt lgkmcnt(4)\n" \
"v_mfma_f32_16x16x32_bf16 v[48:51], a[96:99], v[16:19], v[48:51]\n" \
"v_mfma_f32_16x16x32_bf16 v[52:55], a[96:99], v[20:23], v[52:55]\n" \
"ds_read_b128 v[40:43], v10 offset:44032\n" \
"v_mfma_f32_16x16x32_bf16 v[56:59], a[224:227], v[16:19], v[56:59]\n" \
"ds_read_b128 v[44:47], v10 offset:60416\n" \
"v_mfma_f32_16x16x32_bf16 v[60:63], a[224:227], v[20:23], v[60:63]\n" \
"s_waitcnt lgkmcnt(4)\n" \
"v_mfma_f32_16x16x32_bf16 v[48:51], a[100:103], v[24:27], v[48:51]\n" \
"buffer_load_dwordx4 v4, s[36:39], s61 offen offset:2048 sc1 lds\n" \
"v_mfma_f32_16x16x32_bf16 v[52:55], a[100:103], v[28:31], v[52:55]\n" \
"ds_read_b128 v[16:19], v10 offset:45056\n" \
"v_mfma_f32_16x16x32_bf16 v[56:59], a[228:231], v[24:27], v[56:59]\n" \
"ds_read_b128 v[20:23], v10 offset:61440\n" \
"v_mfma_f32_16x16x32_bf16 v[60:63], a[228:231], v[28:31], v[60:63]\n" \
"s_waitcnt lgkmcnt(4)\n" \
"v_mfma_f32_16x16x32_bf16 v[48:51], a[104:107], v[32:35], v[48:51]\n" \
"buffer_load_dwordx4 v4, s[36:39], s61 offen offset:3072 sc1 lds\n" \
"v_mfma_f32_16x16x32_bf16 v[52:55], a[104:107], v[36:39], v[52:55]\n" \
"ds_read_b128 v[24:27], v10 offset:46080\n" \
"v_mfma_f32_16x16x32_bf16 v[56:59], a[232:235], v[32:35], v[56:59]\n" \
"ds_read_b128 v[28:31], v10 offset:62464\n" \
"v_mfma_f32_16x16x32_bf16 v[60:63], a[232:235], v[36:39], v[60:63]\n" \
"s_waitcnt lgkmcnt(4)\n" \
"v_mfma_f32_16x16x32_bf16 v[48:51], a[108:111], v[40:43], v[48:51]\n" \
"v_mfma_f32_16x16x32_bf16 v[52:55], a[108:111], v[44:47], v[52:55]\n" \
"ds_read_b128 v[32:35], v10 offset:47104\n" \
"v_mfma_f32_16x16x32_bf16 v[56:59], a[236:239], v[40:43], v[56:59]\n" \
"ds_read_b128 v[36:39], v10 offset:63488\n" \
"v_mfma_f32_16x16x32_bf16 v[60:63], a[236:239], v[44:47], v[60:63]\n" \
"s_waitcnt lgkmcnt(4)\n" \
"v_mfma_f32_16x16x32_bf16 v[48:51], a[112:115], v[16:19], v[48:51]\n" \
"v_mfma_f32_16x16x32_bf16 v[52:55], a[112:115], v[20:23], v[52:55]\n" \
"ds_read_b128 v[40:43], v10 offset:48128\n" \
"v_mfma_f32_16x16x32_bf16 v[56:59], a[240:243], v[16:19], v[56:59]\n" \
"ds_read_b128 v[44:47], v10 offset:64512\n" \
"v_mfma_f32_16x16x32_bf16 v[60:63], a[240:243], v[20:23], v[60:63]\n" \
"s_waitcnt lgkmcnt(4)\n" \
"v_mfma_f32_16x16x32_bf16 v[48:51], a[116:119], v[24:27], v[48:51]\n" \
"s_waitcnt vmcnt(10)\n" \
"s_barrier\n" \
"v_mfma_f32_16x16x32_bf16 v[52:55], a[116:119], v[28:31], v[52:55]\n" \
"ds_read_b128 v[16:19], v12 offset:0\n" \
"v_mfma_f32_16x16x32_bf16 v[56:59], a[244:247], v[24:27], v[56:59]\n" \
"ds_read_b128 v[20:23], v12 offset:16384\n" \
"v_mfma_f32_16x16x32_bf16 v[60:63], a[244:247], v[28:31], v[60:63]\n" \
"s_waitcnt lgkmcnt(4)\n" \
"v_mfma_f32_16x16x32_bf16 v[48:51], a[120:123], v[32:35], v[48:51]\n" \
"v_cmp_gt_u32_e64 vcc, 16, v15\n" \
"v_mfma_f32_16x16x32_bf16 v[52:55], a[120:123], v[36:39], v[52:55]\n" \
"ds_read_b128 v[24:27], v12 offset:1024\n" \
"v_mfma_f32_16x16x32_bf16 v[56:59], a[248:251], v[32:35], v[56:59]\n" \
"ds_read_b128 v[28:31], v12 offset:17408\n" \
"v_mfma_f32_16x16x32_bf16 v[60:63], a[248:251], v[36:39], v[60:63]\n" \
"s_waitcnt lgkmcnt(4)\n" \
"v_mfma_f32_16x16x32_bf16 v[48:51], a[124:127], v[40:43], v[48:51]\n" \
"s_cbranch_vccnz .Lp1g1_m0_%=\n" \
".Lp1g1_m0k_%=:\n" \
"v_mfma_f32_16x16x32_bf16 v[52:55], a[124:127], v[44:47], v[52:55]\n" \
"ds_read_b128 v[32:35], v12 offset:2048\n" \
"v_mfma_f32_16x16x32_bf16 v[56:59], a[252:255], v[40:43], v[56:59]\n" \
"ds_read_b128 v[36:39], v12 offset:18432\n" \
"v_mfma_f32_16x16x32_bf16 v[60:63], a[252:255], v[44:47], v[60:63]\n" \
"s_waitcnt lgkmcnt(4)\n" \
"s_add_u32 s57, s57, s49\n" \
"s_add_u32 s58, s58, s49\n" \
"s_add_u32 s59, s59, 1\n" \
"v_mov_b32 v5, v7\n" \
"v_mov_b32 v6, v11\n" \
"s_cmp_ge_u32 s59, s47\n" \
"s_cbranch_scc1 .Lp1g1_out0_%=\n" \
"v_mfma_f32_16x16x32_bf16 v[64:67], a[0:3], v[16:19], 0\n" \
"s_min_u32 s61, s56, s60\n" \
"s_add_u32 m0, s55, 0\n" \
"s_nop 0\n" \
"buffer_load_dwordx4 v4, s[36:39], s61 offen offset:0 sc1 lds\n" \
"v_add_u32_e64 v13, -1, s59\n" \
"v_mfma_f32_16x16x32_bf16 v[68:71], a[0:3], v[20:23], 0\n" \
"ds_read_b128 v[40:43], v12 offset:3072\n" \
"s_nop 0\n" \
"v_mfma_f32_16x16x32_bf16 v[72:75], a[128:131], v[16:19], 0\n" \
"ds_read_b128 v[44:47], v12 offset:19456\n" \
"s_nop 0\n" \
"v_mfma_f32_16x16x32_bf16 v[76:79], a[128:131], v[20:23], 0\n" \
"s_waitcnt lgkmcnt(4)\n" \
"v_mul_f32 v48, 0xbfb8aa3b, v48\n" \
"v_mul_f32 v49, 0xbfb8aa3b, v49\n" \
"v_mfma_f32_16x16x32_bf16 v[64:67], a[4:7], v[24:27], v[64:67]\n" \
"buffer_load_dwordx4 v4, s[36:39], s61 offen offset:1024 sc1 lds\n" \
"v_mfma_f32_16x16x32_bf16 v[68:71], a[4:7], v[28:31], v[68:71]\n" \
"ds_read_b128 v[16:19], v12 offset:4096\n" \
"v_mul_f32 v50, 0xbfb8aa3b, v50\n" \
"v_mfma_f32_16x16x32_bf16 v[72:75], a[132:135], v[24:27], v[72:75]\n" \
"ds_read_b128 v[20:23], v12 offset:20480\n" \
"v_mul_f32 v51, 0xbfb8aa3b, v51\n" \
"v_mfma_f32_16x16x32_bf16 v[76:79], a[132:135], v[28:31], v[76:79]\n" \
"s_waitcnt lgkmcnt(4)\n" \
"v_exp_f32 v48, v48\n" \
"v_mfma_f32_16x16x32_bf16 v[64:67], a[8:11], v[32:35], v[64:67]\n" \
"s_add_u32 s63, s59, 2\n" \
"s_min_u32 s63, s63, 63\n" \
"v_mfma_f32_16x16x32_bf16 v[68:71], a[8:11], v[36:39], v[68:71]\n" \
"ds_read_b128 v[24:27], v12 offset:5120\n" \
"s_lshr_b32 s63, s63, 3\n" \
"s_lshl_b32 s63, s63, 7\n" \
"v_mfma_f32_16x16x32_bf16 v[72:75], a[136:139], v[32:35], v[72:75]\n" \
"ds_read_b128 v[28:31], v12 offset:21504\n" \
"v_mfma_f32_16x16x32_bf16 v[76:79], a[136:139], v[36:39], v[76:79]\n" \
"s_waitcnt lgkmcnt(4)\n" \
"v_exp_f32 v49, v49\n" \
"v_mfma_f32_16x16x32_bf16 v[64:67], a[12:15], v[40:43], v[64:67]\n" \
"buffer_load_dwordx4 v4, s[36:39], s61 offen offset:2048 sc1 lds\n" \
"v_mfma_f32_16x16x32_bf16 v[68:71], a[12:15], v[44:47], v[68:71]\n" \
"ds_read_b128 v[32:35], v12 offset:6144\n" \
"v_mfma_f32_16x16x32_bf16 v[72:75], a[140:143], v[40:43], v[72:75]\n" \
"ds_read_b128 v[36:39], v12 offset:22528\n" \
"v_mfma_f32_16x16x32_bf16 v[76:79], a[140:143], v[44:47], v[76:79]\n" \
"s_waitcnt lgkmcnt(4)\n" \
"v_exp_f32 v50, v50\n" \
"v_mfma_f32_16x16x32_bf16 v[64:67], a[16:19], v[16:19], v[64:67]\n" \
"buffer_load_dwordx4 v4, s[36:39], s61 offen offset:3072 sc1 lds\n" \
"v_mfma_f32_16x16x32_bf16 v[68:71], a[16:19], v[20:23], v[68:71]\n" \
"ds_read_b128 v[40:43], v12 offset:7168\n" \
"v_mfma_f32_16x16x32_bf16 v[72:75], a[144:147], v[16:19], v[72:75]\n" \
"ds_read_b128 v[44:47], v12 offset:23552\n" \
"v_mfma_f32_16x16x32_bf16 v[76:79], a[144:147], v[20:23], v[76:79]\n" \
"s_waitcnt lgkmcnt(4)\n" \
"v_exp_f32 v51, v51\n" \
"v_mfma_f32_16x16x32_bf16 v[64:67], a[20:23], v[24:27], v[64:67]\n" \
"buffer_load_dword v15, v14, s[36:39], s63 offen sc1\n" \
"v_mfma_f32_16x16x32_bf16 v[68:71], a[20:23], v[28:31], v[68:71]\n" \
"ds_read_b128 v[16:19], v12 offset:8192\n" \
"v_add_f32_e64 v48, 1.0, v48\n" \
"v_mfma_f32_16x16x32_bf16 v[72:75], a[148:151], v[24:27], v[72:75]\n" \
"ds_read_b128 v[20:23], v12 offset:24576\n" \
"v_add_f32_e64 v49, 1.0, v49\n" \
"v_mfma_f32_16x16x32_bf16 v[76:79], a[148:151], v[28:31], v[76:79]\n" \
"s_waitcnt lgkmcnt(4)\n" \
"v_add_f32 v50, 1.0, v50\n" \
"v_add_f32_e64 v51, 1.0, v51\n" \
"v_mfma_f32_16x16x32_bf16 v[64:67], a[24:27], v[32:35], v[64:67]\n" \
"s_add_u32 m0, s55, 4096\n" \
"s_add_u32 s61, s61, 4096\n" \
"buffer_load_dwordx4 v4, s[36:39], s61 offen offset:0 sc1 lds\n" \
"v_mfma_f32_16x16x32_bf16 v[68:71], a[24:27], v[36:39], v[68:71]\n" \
"ds_read_b128 v[24:27], v12 offset:9216\n" \
"v_mfma_f32_16x16x32_bf16 v[72:75], a[152:155], v[32:35], v[72:75]\n" \
"ds_read_b128 v[28:31], v12 offset:25600\n" \
"v_mfma_f32_16x16x32_bf16 v[76:79], a[152:155], v[36:39], v[76:79]\n" \
"s_waitcnt lgkmcnt(4)\n" \
"v_rcp_f32 v48, v48\n" \
"v_mfma_f32_16x16x32_bf16 v[64:67], a[28:31], v[40:43], v[64:67]\n" \
"buffer_load_dwordx4 v4, s[36:39], s61 offen offset:1024 sc1 lds\n" \
"v_mfma_f32_16x16x32_bf16 v[68:71], a[28:31], v[44:47], v[68:71]\n" \
"ds_read_b128 v[32:35], v12 offset:10240\n" \
"v_mfma_f32_16x16x32_bf16 v[72:75], a[156:159], v[40:43], v[72:75]\n" \
"ds_read_b128 v[36:39], v12 offset:26624\n" \
"v_mfma_f32_16x16x32_bf16 v[76:79], a[156:159], v[44:47], v[76:79]\n" \
"s_waitcnt lgkmcnt(4)\n" \
"v_rcp_f32 v49, v49\n" \
"v_mfma_f32_16x16x32_bf16 v[64:67], a[32:35], v[16:19], v[64:67]\n" \
"v_rcp_f32_e64 v50, v50\n" \
"v_mfma_f32_16x16x32_bf16 v[68:71], a[32:35], v[20:23], v[68:71]\n" \
"ds_read_b128 v[40:43], v12 offset:11264\n" \
"v_mfma_f32_16x16x32_bf16 v[72:75], a[160:163], v[16:19], v[72:75]\n" \
"ds_read_b128 v[44:47], v12 offset:27648\n" \
"v_mfma_f32_16x16x32_bf16 v[76:79], a[160:163], v[20:23], v[76:79]\n" \
"s_waitcnt lgkmcnt(4)\n" \
"v_rcp_f32 v51, v51\n" \
"v_mfma_f32_16x16x32_bf16 v[64:67], a[36:39], v[24:27], v[64:67]\n" \
"buffer_load_dwordx4 v4, s[36:39], s61 offen offset:2048 sc1 lds\n" \
"v_mfma_f32_16x16x32_bf16 v[68:71], a[36:39], v[28:31], v[68:71]\n" \
"ds_read_b128 v[16:19], v12 offset:12288\n" \
"v_mul_f32_e64 v48, v48, v56\n" \
"v_mfma_f32_16x16x32_bf16 v[72:75], a[164:167], v[24:27], v[72:75]\n" \
"ds_read_b128 v[20:23], v12 offset:28672\n" \
"v_mul_f32_e64 v49, v49, v57\n" \
"v_mfma_f32_16x16x32_bf16 v[76:79], a[164:167], v[28:31], v[76:79]\n" \
"s_waitcnt lgkmcnt(4)\n" \
"v_mul_f32 v50, v50, v58\n" \
"v_mul_f32_e64 v51, v51, v59\n" \
"v_mfma_f32_16x16x32_bf16 v[64:67], a[40:43], v[32:35], v[64:67]\n" \
"buffer_load_dwordx4 v4, s[36:39], s61 offen offset:3072 sc1 lds\n" \
"v_mfma_f32_16x16x32_bf16 v[68:71], a[40:43], v[36:39], v[68:71]\n" \
"ds_read_b128 v[24:27], v12 offset:13312\n" \
"v_cvt_pk_bf16_f32 v80, v48, v49\n" \
"v_mfma_f32_16x16x32_bf16 v[72:75], a[168:171], v[32:35], v[72:75]\n" \
"ds_read_b128 v[28:31], v12 offset:29696\n" \
"v_cvt_pk_bf16_f32 v81, v50, v51\n" \
"v_mfma_f32_16x16x32_bf16 v[76:79], a[168:171], v[36:39], v[76:79]\n" \
"s_waitcnt lgkmcnt(4)\n" \
"v_mul_f32 v52, 0xbfb8aa3b, v52\n" \
"v_mul_f32 v53, 0xbfb8aa3b, v53\n" \
"v_mfma_f32_16x16x32_bf16 v[64:67], a[44:47], v[40:43], v[64:67]\n" \
"v_mul_f32 v54, 0xbfb8aa3b, v54\n" \
"v_mul_f32 v55, 0xbfb8aa3b, v55\n" \
"v_mfma_f32_16x16x32_bf16 v[68:71], a[44:47], v[44:47], v[68:71]\n" \
"ds_read_b128 v[32:35], v12 offset:14336\n" \
"v_mfma_f32_16x16x32_bf16 v[72:75], a[172:175], v[40:43], v[72:75]\n" \
"ds_read_b128 v[36:39], v12 offset:30720\n" \
"v_mfma_f32_16x16x32_bf16 v[76:79], a[172:175], v[44:47], v[76:79]\n" \
"s_waitcnt lgkmcnt(4)\n" \
"v_exp_f32_e64 v52, v52\n" \
"v_mfma_f32_16x16x32_bf16 v[64:67], a[48:51], v[16:19], v[64:67]\n" \
"v_exp_f32_e64 v53, v53\n" \
"v_mfma_f32_16x16x32_bf16 v[68:71], a[48:51], v[20:23], v[68:71]\n" \
"ds_read_b128 v[40:43], v12 offset:15360\n" \
"v_mfma_f32_16x16x32_bf16 v[72:75], a[176:179], v[16:19], v[72:75]\n" \
"ds_read_b128 v[44:47], v12 offset:31744\n" \
"v_mfma_f32_16x16x32_bf16 v[76:79], a[176:179], v[20:23], v[76:79]\n" \
"s_waitcnt lgkmcnt(4)\n" \
"v_exp_f32 v54, v54\n" \
"v_mfma_f32_16x16x32_bf16 v[64:67], a[52:55], v[24:27], v[64:67]\n" \
"s_waitcnt vmcnt(9)\n" \
"s_barrier\n" \
"v_mfma_f32_16x16x32_bf16 v[68:71], a[52:55], v[28:31], v[68:71]\n" \
"ds_read_b128 v[16:19], v12 offset:32768\n" \
"v_mfma_f32_16x16x32_bf16 v[72:75], a[180:183], v[24:27], v[72:75]\n" \
"ds_read_b128 v[20:23], v12 offset:49152\n" \
"v_mfma_f32_16x16x32_bf16 v[76:79], a[180:183], v[28:31], v[76:79]\n" \
"s_waitcnt lgkmcnt(4)\n" \
"buffer_store_dword v13, v6, s[36:39], 0 offen sc1\n" \
"v_exp_f32 v55, v55\n" \
"v_mfma_f32_16x16x32_bf16 v[64:67], a[56:59], v[32:35], v[64:67]\n" \
"v_add_f32 v52, 1.0, v52\n" \
"v_add_f32 v53, 1.0, v53\n" \
"v_mfma_f32_16x16x32_bf16 v[68:71], a[56:59], v[36:39], v[68:71]\n" \
"ds_read_b128 v[24:27], v12 offset:33792\n" \
"v_add_f32_e64 v54, 1.0, v54\n" \
"v_mfma_f32_16x16x32_bf16 v[72:75], a[184:187], v[32:35], v[72:75]\n" \
"ds_read_b128 v[28:31], v12 offset:50176\n" \
"v_add_f32_e64 v55, 1.0, v55\n" \
"v_mfma_f32_16x16x32_bf16 v[76:79], a[184:187], v[36:39], v[76:79]\n" \
"s_waitcnt lgkmcnt(4)\n" \
"v_rcp_f32 v52, v52\n" \
"v_mfma_f32_16x16x32_bf16 v[64:67], a[60:63], v[40:43], v[64:67]\n" \
"v_rcp_f32_e64 v53, v53\n" \
"v_mfma_f32_16x16x32_bf16 v[68:71], a[60:63], v[44:47], v[68:71]\n" \
"ds_read_b128 v[32:35], v12 offset:34816\n" \
"v_mfma_f32_16x16x32_bf16 v[72:75], a[188:191], v[40:43], v[72:75]\n" \
"ds_read_b128 v[36:39], v12 offset:51200\n" \
"v_mfma_f32_16x16x32_bf16 v[76:79], a[188:191], v[44:47], v[76:79]\n" \
"s_waitcnt lgkmcnt(4)\n" \
"v_rcp_f32 v54, v54\n" \
"v_mfma_f32_16x16x32_bf16 v[64:67], a[64:67], v[16:19], v[64:67]\n" \
"s_min_u32 s61, s56, s60\n" \
"s_add_u32 s61, s61, 16384\n" \
"s_add_u32 s56, s56, 0x10000\n" \
"s_add_u32 m0, s55, 32768\n" \
"s_nop 0\n" \
"buffer_load_dwordx4 v4, s[36:39], s61 offen offset:0 sc1 lds\n" \
"v_mfma_f32_16x16x32_bf16 v[68:71], a[64:67], v[20:23], v[68:71]\n" \
"ds_read_b128 v[40:43], v12 offset:35840\n" \
"v_mfma_f32_16x16x32_bf16 v[72:75], a[192:195], v[16:19], v[72:75]\n" \
"ds_read_b128 v[44:47], v12 offset:52224\n" \
"v_mfma_f32_16x16x32_bf16 v[76:79], a[192:195], v[20:23], v[76:79]\n" \
"s_waitcnt lgkmcnt(4)\n" \
"v_rcp_f32 v55, v55\n" \
"v_mfma_f32_16x16x32_bf16 v[64:67], a[68:71], v[24:27], v[64:67]\n" \
"buffer_load_dwordx4 v4, s[36:39], s61 offen offset:1024 sc1 lds\n" \
"v_mfma_f32_16x16x32_bf16 v[68:71], a[68:71], v[28:31], v[68:71]\n" \
"ds_read_b128 v[16:19], v12 offset:36864\n" \
"v_mul_f32_e64 v52, v52, v60\n" \
"v_mfma_f32_16x16x32_bf16 v[72:75], a[196:199], v[24:27], v[72:75]\n" \
"ds_read_b128 v[20:23], v12 offset:53248\n" \
"v_mul_f32_e64 v53, v53, v61\n" \
"v_mfma_f32_16x16x32_bf16 v[76:79], a[196:199], v[28:31], v[76:79]\n" \
"s_waitcnt lgkmcnt(4)\n" \
"v_mul_f32 v54, v54, v62\n" \
"v_mul_f32_e64 v55, v55, v63\n" \
"v_mfma_f32_16x16x32_bf16 v[64:67], a[72:75], v[32:35], v[64:67]\n" \
"v_cvt_pk_bf16_f32 v82, v52, v53\n" \
"v_cvt_pk_bf16_f32 v83, v54, v55\n" \
"v_mfma_f32_16x16x32_bf16 v[68:71], a[72:75], v[36:39], v[68:71]\n" \
"ds_read_b128 v[24:27], v12 offset:37888\n" \
"s_nop 0\n" \
"v_mfma_f32_16x16x32_bf16 v[72:75], a[200:203], v[32:35], v[72:75]\n" \
"ds_read_b128 v[28:31], v12 offset:54272\n" \
"s_nop 0\n" \
"v_mfma_f32_16x16x32_bf16 v[76:79], a[200:203], v[36:39], v[76:79]\n" \
"s_waitcnt lgkmcnt(4)\n" \
"v_permlane16_swap_b32 v80, v82\n" \
"v_permlane16_swap_b32 v81, v83\n" \
"v_mfma_f32_16x16x32_bf16 v[64:67], a[76:79], v[40:43], v[64:67]\n" \
"buffer_load_dwordx4 v4, s[36:39], s61 offen offset:2048 sc1 lds\n" \
"v_mfma_f32_16x16x32_bf16 v[68:71], a[76:79], v[44:47], v[68:71]\n" \
"ds_read_b128 v[32:35], v12 offset:38912\n" \
"buffer_store_dwordx4 v[80:83], v5, s[36:39], s57 offen sc1\n" \
"v_mfma_f32_16x16x32_bf16 v[72:75], a[204:207], v[40:43], v[72:75]\n" \
"ds_read_b128 v[36:39], v12 offset:55296\n" \
"v_mfma_f32_16x16x32_bf16 v[76:79], a[204:207], v[44:47], v[76:79]\n" \
"s_waitcnt lgkmcnt(4)\n" \
"v_mfma_f32_16x16x32_bf16 v[64:67], a[80:83], v[16:19], v[64:67]\n" \
"buffer_load_dwordx4 v4, s[36:39], s61 offen offset:3072 sc1 lds\n" \
"v_mfma_f32_16x16x32_bf16 v[68:71], a[80:83], v[20:23], v[68:71]\n" \
"ds_read_b128 v[40:43], v12 offset:39936\n" \
"v_mfma_f32_16x16x32_bf16 v[72:75], a[208:211], v[16:19], v[72:75]\n" \
"ds_read_b128 v[44:47], v12 offset:56320\n" \
"v_mfma_f32_16x16x32_bf16 v[76:79], a[208:211], v[20:23], v[76:79]\n" \
"s_waitcnt lgkmcnt(4)\n" \
"v_mfma_f32_16x16x32_bf16 v[64:67], a[84:87], v[24:27], v[64:67]\n" \
"v_mfma_f32_16x16x32_bf16 v[68:71], a[84:87], v[28:31], v[68:71]\n" \
"ds_read_b128 v[16:19], v12 offset:40960\n" \
"v_mfma_f32_16x16x32_bf16 v[72:75], a[212:215], v[24:27], v[72:75]\n" \
"ds_read_b128 v[20:23], v12 offset:57344\n" \
"v_mfma_f32_16x16x32_bf16 v[76:79], a[212:215], v[28:31], v[76:79]\n" \
"s_waitcnt lgkmcnt(4)\n" \
"v_mfma_f32_16x16x32_bf16 v[64:67], a[88:91], v[32:35], v[64:67]\n" \
"s_add_u32 m0, s55, 36864\n" \
"s_add_u32 s61, s61, 4096\n" \
"buffer_load_dwordx4 v4, s[36:39], s61 offen offset:0 sc1 lds\n" \
"v_mfma_f32_16x16x32_bf16 v[68:71], a[88:91], v[36:39], v[68:71]\n" \
"ds_read_b128 v[24:27], v12 offset:41984\n" \
"v_mfma_f32_16x16x32_bf16 v[72:75], a[216:219], v[32:35], v[72:75]\n" \
"ds_read_b128 v[28:31], v12 offset:58368\n" \
"v_mfma_f32_16x16x32_bf16 v[76:79], a[216:219], v[36:39], v[76:79]\n" \
"s_waitcnt lgkmcnt(4)\n" \
"v_mfma_f32_16x16x32_bf16 v[64:67], a[92:95], v[40:43], v[64:67]\n" \
"buffer_load_dwordx4 v4, s[36:39], s61 offen offset:1024 sc1 lds\n" \
"v_mfma_f32_16x16x32_bf16 v[68:71], a[92:95], v[44:47], v[68:71]\n" \
"ds_read_b128 v[32:35], v12 offset:43008\n" \
"v_mfma_f32_16x16x32_bf16 v[72:75], a[220:223], v[40:43], v[72:75]\n" \
"ds_read_b128 v[36:39], v12 offset:59392\n" \
"v_mfma_f32_16x16x32_bf16 v[76:79], a[220:223], v[44:47], v[76:79]\n" \
"s_waitcnt lgkmcnt(4)\n" \
"v_mfma_f32_16x16x32_bf16 v[64:67], a[96:99], v[16:19], v[64:67]\n" \
"v_mfma_f32_16x16x32_bf16 v[68:71], a[96:99], v[20:23], v[68:71]\n" \
"ds_read_b128 v[40:43], v12 offset:44032\n" \
"v_mfma_f32_16x16x32_bf16 v[72:75], a[224:227], v[16:19], v[72:75]\n" \
"ds_read_b128 v[44:47], v12 offset:60416\n" \
"v_mfma_f32_16x16x32_bf16 v[76:79], a[224:227], v[20:23], v[76:79]\n" \
"s_waitcnt lgkmcnt(4)\n" \
"v_mfma_f32_16x16x32_bf16 v[64:67], a[100:103], v[24:27], v[64:67]\n" \
"buffer_load_dwordx4 v4, s[36:39], s61 offen offset:2048 sc1 lds\n" \
"v_mfma_f32_16x16x32_bf16 v[68:71], a[100:103], v[28:31], v[68:71]\n" \
"ds_read_b128 v[16:19], v12 offset:45056\n" \
"v_mfma_f32_16x16x32_bf16 v[72:75], a[228:231], v[24:27], v[72:75]\n" \
"ds_read_b128 v[20:23], v12 offset:61440\n" \
"v_mfma_f32_16x16x32_bf16 v[76:79], a[228:231], v[28:31], v[76:79]\n" \
"s_waitcnt lgkmcnt(4)\n" \
"v_mfma_f32_16x16x32_bf16 v[64:67], a[104:107], v[32:35], v[64:67]\n" \
"buffer_load_dwordx4 v4, s[36:39], s61 offen offset:3072 sc1 lds\n" \
"v_mfma_f32_16x16x32_bf16 v[68:71], a[104:107], v[36:39], v[68:71]\n" \
"ds_read_b128 v[24:27], v12 offset:46080\n" \
"v_mfma_f32_16x16x32_bf16 v[72:75], a[232:235], v[32:35], v[72:75]\n" \
"ds_read_b128 v[28:31], v12 offset:62464\n" \
"v_mfma_f32_16x16x32_bf16 v[76:79], a[232:235], v[36:39], v[76:79]\n" \
"s_waitcnt lgkmcnt(4)\n" \
"v_mfma_f32_16x16x32_bf16 v[64:67], a[108:111], v[40:43], v[64:67]\n" \
"v_mfma_f32_16x16x32_bf16 v[68:71], a[108:111], v[44:47], v[68:71]\n" \
"ds_read_b128 v[32:35], v12 offset:47104\n" \
"v_mfma_f32_16x16x32_bf16 v[72:75], a[236:239], v[40:43], v[72:75]\n" \
"ds_read_b128 v[36:39], v12 offset:63488\n" \
"v_mfma_f32_16x16x32_bf16 v[76:79], a[236:239], v[44:47], v[76:79]\n" \
"s_waitcnt lgkmcnt(4)\n" \
"v_mfma_f32_16x16x32_bf16 v[64:67], a[112:115], v[16:19], v[64:67]\n" \
"v_mfma_f32_16x16x32_bf16 v[68:71], a[112:115], v[20:23], v[68:71]\n" \
"ds_read_b128 v[40:43], v12 offset:48128\n" \
"v_mfma_f32_16x16x32_bf16 v[72:75], a[240:243], v[16:19], v[72:75]\n" \
"ds_read_b128 v[44:47], v12 offset:64512\n" \
"v_mfma_f32_16x16x32_bf16 v[76:79], a[240:243], v[20:23], v[76:79]\n" \
"s_waitcnt lgkmcnt(4)\n" \
"v_mfma_f32_16x16x32_bf16 v[64:67], a[116:119], v[24:27], v[64:67]\n" \
"s_waitcnt vmcnt(10)\n" \
"s_barrier\n" \
"v_mfma_f32_16x16x32_bf16 v[68:71], a[116:119], v[28:31], v[68:71]\n" \
"ds_read_b128 v[16:19], v10 offset:0\n" \
"v_mfma_f32_16x16x32_bf16 v[72:75], a[244:247], v[24:27], v[72:75]\n" \
"ds_read_b128 v[20:23], v10 offset:16384\n" \
"v_mfma_f32_16x16x32_bf16 v[76:79], a[244:247], v[28:31], v[76:79]\n" \
"s_waitcnt lgkmcnt(4)\n" \
"v_mfma_f32_16x16x32_bf16 v[64:67], a[120:123], v[32:35], v[64:67]\n" \
"v_cmp_gt_u32_e64 vcc, 16, v15\n" \
"v_mfma_f32_16x16x32_bf16 v[68:71], a[120:123], v[36:39], v[68:71]\n" \
"ds_read_b128 v[24:27], v10 offset:1024\n" \
"v_mfma_f32_16x16x32_bf16 v[72:75], a[248:251], v[32:35], v[72:75]\n" \
"ds_read_b128 v[28:31], v10 offset:17408\n" \
"v_mfma_f32_16x16x32_bf16 v[76:79], a[248:251], v[36:39], v[76:79]\n" \
"s_waitcnt lgkmcnt(4)\n" \
"v_mfma_f32_16x16x32_bf16 v[64:67], a[124:127], v[40:43], v[64:67]\n" \
"s_cbranch_vccnz .Lp1g1_m1_%=\n" \
".Lp1g1_m1k_%=:\n" \
"v_mfma_f32_16x16x32_bf16 v[68:71], a[124:127], v[44:47], v[68:71]\n" \
"ds_read_b128 v[32:35], v10 offset:2048\n" \
"v_mfma_f32_16x16x32_bf16 v[72:75], a[252:255], v[40:43], v[72:75]\n" \
"ds_read_b128 v[36:39], v10 offset:18432\n" \
"v_mfma_f32_16x16x32_bf16 v[76:79], a[252:255], v[44:47], v[76:79]\n" \
"s_waitcnt lgkmcnt(4)\n" \
"s_add_u32 s57, s57, s49\n" \
"s_add_u32 s58, s58, s49\n" \
"s_add_u32 s59, s59, 1\n" \
"v_mov_b32_e64 v5, v7\n" \
"v_mov_b32_e64 v6, v11\n" \
"s_cmp_ge_u32 s59, s47\n" \
"s_nop 0\n" \
"s_cbranch_scc0 .Lp1g1_loop_%=\n" \
".Lp1g1_out1_%=:\n" \
"s_nop 7\n" \
"s_nop 7\n" \
"v_mul_f32 v64, 0xbfb8aa3b, v64\n" \
"v_mul_f32 v65, 0xbfb8aa3b, v65\n" \
"v_mul_f32 v66, 0xbfb8aa3b, v66\n" \
"v_mul_f32 v67, 0xbfb8aa3b, v67\n" \
"v_exp_f32 v64, v64\n" \
"v_exp_f32 v65, v65\n" \
"v_exp_f32 v66, v66\n" \
"v_exp_f32 v67, v67\n" \
"v_add_f32 v64, 1.0, v64\n" \
"v_add_f32 v65, 1.0, v65\n" \
"v_add_f32 v66, 1.0, v66\n" \
"v_add_f32 v67, 1.0, v67\n" \
"v_rcp_f32 v64, v64\n" \
"v_rcp_f32 v65, v65\n" \
"v_rcp_f32 v66, v66\n" \
"v_rcp_f32 v67, v67\n" \
"v_mul_f32 v64, v64, v72\n" \
"v_mul_f32 v65, v65, v73\n" \
"v_mul_f32 v66, v66, v74\n" \
"v_mul_f32 v67, v67, v75\n" \
"v_cvt_pk_bf16_f32 v80, v64, v65\n" \
"v_cvt_pk_bf16_f32 v81, v66, v67\n" \
"v_mul_f32 v68, 0xbfb8aa3b, v68\n" \
"v_mul_f32 v69, 0xbfb8aa3b, v69\n" \
"v_mul_f32 v70, 0xbfb8aa3b, v70\n" \
"v_mul_f32 v71, 0xbfb8aa3b, v71\n" \
"v_exp_f32 v68, v68\n" \
"v_exp_f32 v69, v69\n" \
"v_exp_f32 v70, v70\n" \
"v_exp_f32 v71, v71\n" \
"v_add_f32 v68, 1.0, v68\n" \
"v_add_f32 v69, 1.0, v69\n" \
"v_add_f32 v70, 1.0, v70\n" \
"v_add_f32 v71, 1.0, v71\n" \
"v_rcp_f32 v68, v68\n" \
"v_rcp_f32 v69, v69\n" \
"v_rcp_f32 v70, v70\n" \
"v_rcp_f32 v71, v71\n" \
"v_mul_f32 v68, v68, v76\n" \
"v_mul_f32 v69, v69, v77\n" \
"v_mul_f32 v70, v70, v78\n" \
"v_mul_f32 v71, v71, v79\n" \
"v_cvt_pk_bf16_f32 v82, v68, v69\n" \
"v_cvt_pk_bf16_f32 v83, v70, v71\n" \
"s_nop 0\n" \
"s_nop 0\n" \
"v_permlane16_swap_b32 v80, v82\n" \
"v_permlane16_swap_b32 v81, v83\n" \
"buffer_store_dwordx4 v[80:83], v5, s[36:39], s57 offen sc1\n" \
"s_branch .Lp1g1_end_%=\n" \
".Lp1g1_out0_%=:\n" \
"s_nop 7\n" \
"s_nop 7\n" \
"v_mul_f32 v48, 0xbfb8aa3b, v48\n" \
"v_mul_f32 v49, 0xbfb8aa3b, v49\n" \
"v_mul_f32 v50, 0xbfb8aa3b, v50\n" \
"v_mul_f32 v51, 0xbfb8aa3b, v51\n" \
"v_exp_f32 v48, v48\n" \
"v_exp_f32 v49, v49\n" \
"v_exp_f32 v50, v50\n" \
"v_exp_f32 v51, v51\n" \
"v_add_f32 v48, 1.0, v48\n" \
"v_add_f32 v49, 1.0, v49\n" \
"v_add_f32 v50, 1.0, v50\n" \
"v_add_f32 v51, 1.0, v51\n" \
"v_rcp_f32 v48, v48\n" \
"v_rcp_f32 v49, v49\n" \
"v_rcp_f32 v50, v50\n" \
"v_rcp_f32 v51, v51\n" \
"v_mul_f32 v48, v48, v56\n" \
"v_mul_f32 v49, v49, v57\n" \
"v_mul_f32 v50, v50, v58\n" \
"v_mul_f32 v51, v51, v59\n" \
"v_cvt_pk_bf16_f32 v80, v48, v49\n" \
"v_cvt_pk_bf16_f32 v81, v50, v51\n" \
"v_mul_f32 v52, 0xbfb8aa3b, v52\n" \
"v_mul_f32 v53, 0xbfb8aa3b, v53\n" \
"v_mul_f32 v54, 0xbfb8aa3b, v54\n" \
"v_mul_f32 v55, 0xbfb8aa3b, v55\n" \
"v_exp_f32 v52, v52\n" \
"v_exp_f32 v53, v53\n" \
"v_exp_f32 v54, v54\n" \
"v_exp_f32 v55, v55\n" \
"v_add_f32 v52, 1.0, v52\n" \
"v_add_f32 v53, 1.0, v53\n" \
"v_add_f32 v54, 1.0, v54\n" \
"v_add_f32 v55, 1.0, v55\n" \
"v_rcp_f32 v52, v52\n" \
"v_rcp_f32 v53, v53\n" \
"v_rcp_f32 v54, v54\n" \
"v_rcp_f32 v55, v55\n" \
"v_mul_f32 v52, v52, v60\n" \
"v_mul_f32 v53, v53, v61\n" \
"v_mul_f32 v54, v54, v62\n" \
"v_mul_f32 v55, v55, v63\n" \
"v_cvt_pk_bf16_f32 v82, v52, v53\n" \
"v_cvt_pk_bf16_f32 v83, v54, v55\n" \
"s_nop 0\n" \
"s_nop 0\n" \
"v_permlane16_swap_b32 v80, v82\n" \
"v_permlane16_swap_b32 v81, v83\n" \
"buffer_store_dwordx4 v[80:83], v5, s[36:39], s57 offen sc1\n" \
".Lp1g1_end_%=:\n" \
"s_waitcnt vmcnt(0) lgkmcnt(0)\n" \
"s_barrier\n" \
"v_mov_b32 v13, s59\n" \
"buffer_store_dword v13, v6, s[36:39], 0 offen sc1\n" \
"s_waitcnt vmcnt(0) lgkmcnt(0)\n" \
"s_branch .Lp1g1_x_%=\n" \
".Lp1g1_l0_%=:\n" \
"s_nop 7\n" \
"s_nop 7\n" \
"s_waitcnt vmcnt(0) lgkmcnt(0)\n" \
"s_mov_b32 s62, 0\n" \
".Lp1g1_l0a_%=:\n" \
"buffer_load_dword v15, v14, s[36:39], s63 offen sc1\n" \
"s_waitcnt vmcnt(0)\n" \
"v_cmp_gt_u32 vcc, 16, v15\n" \
"s_nop 4\n" \
"s_cbranch_vccz .Lp1g1_l0k_%=\n" \
"s_sleep 4\n" \
"s_add_u32 s62, s62, 1\n" \
"s_cmp_lt_u32 s62, 0x10000\n" \
"s_cbranch_scc1 .Lp1g1_l0a_%=\n" \
"v_mov_b32 v15, 256\n" \
"buffer_store_dword v15, v15, s[36:39], 0 offen sc1\n" \
"s_waitcnt vmcnt(0) lgkmcnt(0)\n" \
"s_branch .Lp1g1_l0k_%=\n" \
".Lp1g1_l1_%=:\n" \
"s_nop 7\n" \
"s_nop 7\n" \
"s_waitcnt vmcnt(0) lgkmcnt(0)\n" \
"s_mov_b32 s62, 0\n" \
".Lp1g1_l1a_%=:\n" \
"buffer_load_dword v15, v14, s[36:39], s63 offen sc1\n" \
"s_waitcnt vmcnt(0)\n" \
"v_cmp_gt_u32 vcc, 16, v15\n" \
"s_nop 4\n" \
"s_cbranch_vccz .Lp1g1_l1k_%=\n" \
"s_sleep 4\n" \
"s_add_u32 s62, s62, 1\n" \
"s_cmp_lt_u32 s62, 0x10000\n" \
"s_cbranch_scc1 .Lp1g1_l1a_%=\n" \
"v_mov_b32 v15, 256\n" \
"buffer_store_dword v15, v15, s[36:39], 0 offen sc1\n" \
"s_waitcnt vmcnt(0) lgkmcnt(0)\n" \
"s_branch .Lp1g1_l1k_%=\n" \
".Lp1g1_m0_%=:\n" \
"s_nop 7\n" \
"s_nop 7\n" \
"s_waitcnt vmcnt(0) lgkmcnt(0)\n" \
"s_mov_b32 s62, 0\n" \
".Lp1g1_m0a_%=:\n" \
"buffer_load_dword v15, v14, s[36:39], s63 offen sc1\n" \
"s_waitcnt vmcnt(0)\n" \
"v_cmp_gt_u32 vcc, 16, v15\n" \
"s_nop 4\n" \
"s_cbranch_vccz .Lp1g1_m0k_%=\n" \
"s_sleep 4\n" \
"s_add_u32 s62, s62, 1\n" \
"s_cmp_lt_u32 s62, 0x10000\n" \
"s_cbranch_scc1 .Lp1g1_m0a_%=\n" \
"v_mov_b32 v15, 256\n" \
"buffer_store_dword v15, v15, s[36:39], 0 offen sc1\n" \
"s_waitcnt vmcnt(0) lgkmcnt(0)\n" \
"s_branch .Lp1g1_m0k_%=\n" \
".Lp1g1_m1_%=:\n" \
"s_nop 7\n" \
"s_nop 7\n" \
"s_waitcnt vmcnt(0) lgkmcnt(0)\n" \
"s_mov_b32 s62, 0\n" \
".Lp1g1_m1a_%=:\n" \
"buffer_load_dword v15, v14, s[36:39], s63 offen sc1\n" \
"s_waitcnt vmcnt(0)\n" \
"v_cmp_gt_u32 vcc, 16, v15\n" \
"s_nop 4\n" \
"s_cbranch_vccz .Lp1g1_m1k_%=\n" \
"s_sleep 4\n" \
"s_add_u32 s62, s62, 1\n" \
"s_cmp_lt_u32 s62, 0x10000\n" \
"s_cbranch_scc1 .Lp1g1_m1a_%=\n" \
"v_mov_b32 v15, 256\n" \
"buffer_store_dword v15, v15, s[36:39], 0 offen sc1\n" \
"s_waitcnt vmcnt(0) lgkmcnt(0)\n" \
"s_branch .Lp1g1_m1k_%=\n" \
".Lp1g1_x_%=:\n"
#define CLOB_P1 \
"a0", "a1", "a2", "a3", "a4", "a5", "a6", "a7", "a8", "a9", "a10", "a11", "a12", "a13", "a14", "a15", "a16", "a17", "a18", "a19", "a20", "a21", "a22", "a23", "a24", "a25", "a26", "a27", "a28", "a29", "a30", "a31", "a32", "a33", "a34", "a35", "a36", "a37", "a38", "a39", "a40", "a41", "a42", "a43", "a44", "a45", "a46", "a47", "a48", "a49", "a50", "a51", "a52", "a53", "a54", "a55", "a56", "a57", "a58", "a59", "a60", "a61", "a62", "a63", "a64", "a65", "a66", "a67", "a68", "a69", "a70", "a71", "a72", "a73", "a74", "a75", "a76", "a77", "a78", "a79", "a80", "a81", "a82", "a83", "a84", "a85", "a86", "a87", "a88", "a89", "a90", "a91", "a92", "a93", "a94", "a95", "a96", "a97", "a98", "a99", "a100", "a101", "a102", "a103", "a104", "a105", "a106", "a107", "a108", "a109", "a110", "a111", "a112", "a113", "a114", "a115", "a116", "a117", "a118", "a119", "a120", "a121", "a122", "a123", "a124", "a125", "a126", "a127", "a128", "a129", "a130", "a131", "a132", "a133", "a134", "a135", "a136", "a137", "a138", "a139", "a140", "a141", "a142", "a143", "a144", "a145", "a146", "a147", "a148", "a149", "a150", "a151", "a152", "a153", "a154", "a155", "a156", "a157", "a158", "a159", "a160", "a161", "a162", "a163", "a164", "a165", "a166", "a167", "a168", "a169", "a170", "a171", "a172", "a173", "a174", "a175", "a176", "a177", "a178", "a179", "a180", "a181", "a182", "a183", "a184", "a185", "a186", "a187", "a188", "a189", "a190", "a191", "a192", "a193", "a194", "a195", "a196", "a197", "a198", "a199", "a200", "a201", "a202", "a203", "a204", "a205", "a206", "a207", "a208", "a209", "a210", "a211", "a212", "a213", "a214", "a215", "a216", "a217", "a218", "a219", "a220", "a221", "a222", "a223", "a224", "a225", "a226", "a227", "a228", "a229", "a230", "a231", "a232", "a233", "a234", "a235", "a236", "a237", "a238", "a239", "a240", "a241", "a242", "a243", "a244", "a245", "a246", "a247", "a248", "a249", "a250", "a251", "a252", "a253", "a254", "a255", "s52", "s53", "s54", "s55", "s56", "s57", "s58", "s59", "s60", "s61", "s62", "s63", "v7", "v8", "v9", "v10", "v11", "v12", "v13", "v14", "v15", "v16", "v17", "v18", "v19", "v20", "v21", "v22", "v23", "v24", "v25", "v26", "v27", "v28", "v29", "v30", "v31", "v32", "v33", "v34", "v35", "v36", "v37", "v38", "v39", "v40", "v41", "v42", "v43", "v44", "v45", "v46", "v47", "v48", "v49", "v50", "v51", "v52", "v53", "v54", "v55", "v56", "v57", "v58", "v59", "v60", "v61", "v62", "v63", "v64", "v65", "v66", "v67", "v68", "v69", "v70", "v71", "v72", "v73", "v74", "v75", "v76", "v77", "v78", "v79", "v80", "v81", "v82", "v83", "v84", "v85", "v86", "v87", "vcc", "scc", "memory"
#define ASM_QK \
"s_nop 7\n" \
"s_nop 7\n" \
"v_add_u32 v36, 0x10000, v34\n" \
"v_add_u32 v38, 0x20000, v34\n" \
"v_add_u32 v37, 0x10000, v35\n" \
"v_add_u32 v39, 0x20000, v35\n" \
"s_lshl_b32 s62, s48, 12\n" \
"s_add_u32 m0, s62, 147456\n" \
"s_xor_b32 s54, s15, 524288\n" \
"buffer_load_dwordx4 v32, s[40:43], s54 offen nt lds\n" \
"s_add_u32 m0, s62, 148480\n" \
"s_xor_b32 s54, s15, 524288\n" \
"buffer_load_dwordx4 v33, s[40:43], s54 offen nt lds\n" \
"s_add_u32 m0, s62, 149504\n" \
"s_xor_b32 s54, s15, 655360\n" \
"buffer_load_dwordx4 v32, s[40:43], s54 offen nt lds\n" \
"s_add_u32 m0, s62, 150528\n" \
"s_xor_b32 s54, s15, 655360\n" \
"buffer_load_dwordx4 v33, s[40:43], s54 offen nt lds\n" \
"s_add_u32 m0, s62, 131072\n" \
"s_xor_b32 s54, s15, 786432\n" \
"buffer_load_dwordx4 v32, s[40:43], s54 offen nt lds\n" \
"s_add_u32 m0, s62, 132096\n" \
"s_xor_b32 s54, s15, 786432\n" \
"buffer_load_dwordx4 v33, s[40:43], s54 offen nt lds\n" \
"s_add_u32 m0, s62, 133120\n" \
"s_xor_b32 s54, s15, 917504\n" \
"buffer_load_dwordx4 v32, s[40:43], s54 offen nt lds\n" \
"s_add_u32 m0, s62, 134144\n" \
"s_xor_b32 s54, s15, 917504\n" \
"buffer_load_dwordx4 v33, s[40:43], s54 offen nt lds\n" \
"s_add_u32 m0, s62, 114688\n" \
"s_xor_b32 s54, s15, 1572864\n" \
"buffer_load_dwordx4 v32, s[40:43], s54 offen nt lds\n" \
"s_add_u32 m0, s62, 115712\n" \
"s_xor_b32 s54, s15, 1572864\n" \
"buffer_load_dwordx4 v33, s[40:43], s54 offen nt lds\n" \
"s_add_u32 m0, s62, 116736\n" \
"s_xor_b32 s54, s15, 1703936\n" \
"buffer_load_dwordx4 v32, s[40:43], s54 offen nt lds\n" \
"s_add_u32 m0, s62, 117760\n" \
"s_xor_b32 s54, s15, 1703936\n" \
"buffer_load_dwordx4 v33, s[40:43], s54 offen nt lds\n" \
"s_add_u32 m0, s62, 98304\n" \
"s_xor_b32 s54, s15, 1835008\n" \
"buffer_load_dwordx4 v32, s[40:43], s54 offen nt lds\n" \
"s_add_u32 m0, s62, 99328\n" \
"s_xor_b32 s54, s15, 1835008\n" \
"buffer_load_dwordx4 v33, s[40:43], s54 offen nt lds\n" \
"s_add_u32 m0, s62, 100352\n" \
"s_xor_b32 s54, s15, 1966080\n" \
"buffer_load_dwordx4 v32, s[40:43], s54 offen nt lds\n" \
"s_add_u32 m0, s62, 101376\n" \
"s_xor_b32 s54, s15, 1966080\n" \
"buffer_load_dwordx4 v33, s[40:43], s54 offen nt lds\n" \
"s_add_u32 m0, s62, 81920\n" \
"s_xor_b32 s54, s15, 2621440\n" \
"buffer_load_dwordx4 v32, s[40:43], s54 offen nt lds\n" \
"s_add_u32 m0, s62, 82944\n" \
"s_xor_b32 s54, s15, 2621440\n" \
"buffer_load_dwordx4 v33, s[40:43], s54 offen nt lds\n" \
"s_add_u32 m0, s62, 83968\n" \
"s_xor_b32 s54, s15, 2752512\n" \
"buffer_load_dwordx4 v32, s[40:43], s54 offen nt lds\n" \
"s_add_u32 m0, s62, 84992\n" \
"s_xor_b32 s54, s15, 2752512\n" \
"buffer_load_dwordx4 v33, s[40:43], s54 offen nt lds\n" \
"s_add_u32 m0, s62, 65536\n" \
"s_xor_b32 s54, s15, 2883584\n" \
"buffer_load_dwordx4 v32, s[40:43], s54 offen nt lds\n" \
"s_add_u32 m0, s62, 66560\n" \
"s_xor_b32 s54, s15, 2883584\n" \
"buffer_load_dwordx4 v33, s[40:43], s54 offen nt lds\n" \
"s_add_u32 m0, s62, 67584\n" \
"s_xor_b32 s54, s15, 3014656\n" \
"buffer_load_dwordx4 v32, s[40:43], s54 offen nt lds\n" \
"s_add_u32 m0, s62, 68608\n" \
"s_xor_b32 s54, s15, 3014656\n" \
"buffer_load_dwordx4 v33, s[40:43], s54 offen nt lds\n" \
"s_add_u32 m0, s62, 49152\n" \
"s_xor_b32 s54, s15, 3670016\n" \
"buffer_load_dwordx4 v32, s[40:43], s54 offen nt lds\n" \
"s_add_u32 m0, s62, 50176\n" \
"s_xor_b32 s54, s15, 3670016\n" \
"buffer_load_dwordx4 v33, s[40:43], s54 offen nt lds\n" \
"s_add_u32 m0, s62, 51200\n" \
"s_xor_b32 s54, s15, 3801088\n" \
"buffer_load_dwordx4 v32, s[40:43], s54 offen nt lds\n" \
"s_add_u32 m0, s62, 52224\n" \
"s_xor_b32 s54, s15, 3801088\n" \
"buffer_load_dwordx4 v33, s[40:43], s54 offen nt lds\n" \
"s_add_u32 m0, s62, 32768\n" \
"s_xor_b32 s54, s15, 3932160\n" \
"buffer_load_dwordx4 v32, s[40:43], s54 offen nt lds\n" \
"s_add_u32 m0, s62, 33792\n" \
"s_xor_b32 s54, s15, 3932160\n" \
"buffer_load_dwordx4 v33, s[40:43], s54 offen nt lds\n" \
"s_add_u32 m0, s62, 34816\n" \
"s_xor_b32 s54, s15, 4063232\n" \
"buffer_load_dwordx4 v32, s[40:43], s54 offen nt lds\n" \
"s_add_u32 m0, s62, 35840\n" \
"s_xor_b32 s54, s15, 4063232\n" \
"buffer_load_dwordx4 v33, s[40:43], s54 offen nt lds\n" \
"s_add_u32 m0, s62, 16384\n" \
"s_xor_b32 s54, s15, 4718592\n" \
"buffer_load_dwordx4 v32, s[40:43], s54 offen nt lds\n" \
"s_add_u32 m0, s62, 17408\n" \
"s_xor_b32 s54, s15, 4718592\n" \
"buffer_load_dwordx4 v33, s[40:43], s54 offen nt lds\n" \
"s_add_u32 m0, s62, 18432\n" \
"s_xor_b32 s54, s15, 4849664\n" \
"buffer_load_dwordx4 v32, s[40:43], s54 offen nt lds\n" \
"s_add_u32 m0, s62, 19456\n" \
"s_xor_b32 s54, s15, 4849664\n" \
"buffer_load_dwordx4 v33, s[40:43], s54 offen nt lds\n" \
"s_add_u32 m0, s62, 0\n" \
"s_xor_b32 s54, s15, 4980736\n" \
"buffer_load_dwordx4 v32, s[40:43], s54 offen nt lds\n" \
"s_add_u32 m0, s62, 1024\n" \
"s_xor_b32 s54, s15, 4980736\n" \
"buffer_load_dwordx4 v33, s[40:43], s54 offen nt lds\n" \
"s_add_u32 m0, s62, 2048\n" \
"s_xor_b32 s54, s15, 5111808\n" \
"buffer_load_dwordx4 v32, s[40:43], s54 offen nt lds\n" \
"s_add_u32 m0, s62, 3072\n" \
"s_xor_b32 s54, s15, 5111808\n" \
"buffer_load_dwordx4 v33, s[40:43], s54 offen nt lds\n" \
"s_waitcnt vmcnt(32)\n" \
"s_barrier\n" \
"ds_read_b64_tr_b16 a[16:17], v38 offset:16384\n" \
"ds_read_b64_tr_b16 a[18:19], v38 offset:16896\n" \
"ds_read_b64_tr_b16 a[24:25], v39 offset:16384\n" \
"ds_read_b64_tr_b16 a[26:27], v39 offset:16896\n" \
"ds_read_b64_tr_b16 v[112:113], v38 offset:24576\n" \
"ds_read_b64_tr_b16 v[114:115], v38 offset:25088\n" \
"ds_read_b64_tr_b16 v[120:121], v39 offset:24576\n" \
"ds_read_b64_tr_b16 v[122:123], v39 offset:25088\n" \
"ds_read_b64_tr_b16 a[20:21], v38 offset:0\n" \
"ds_read_b64_tr_b16 a[22:23], v38 offset:512\n" \
"ds_read_b64_tr_b16 a[28:29], v39 offset:0\n" \
"ds_read_b64_tr_b16 a[30:31], v39 offset:512\n" \
"ds_read_b64_tr_b16 v[116:117], v38 offset:8192\n" \
"ds_read_b64_tr_b16 v[118:119], v38 offset:8704\n" \
"ds_read_b64_tr_b16 v[124:125], v39 offset:8192\n" \
"ds_read_b64_tr_b16 v[126:127], v39 offset:8704\n" \
"s_waitcnt lgkmcnt(0)\n" \
"s_barrier\n" \
"s_add_u32 m0, s62, 147456\n" \
"s_xor_b32 s54, s15, 5767168\n" \
"buffer_load_dwordx4 v32, s[40:43], s54 offen nt lds\n" \
"s_add_u32 m0, s62, 148480\n" \
"s_xor_b32 s54, s15, 5767168\n" \
"buffer_load_dwordx4 v33, s[40:43], s54 offen nt lds\n" \
"s_add_u32 m0, s62, 149504\n" \
"s_xor_b32 s54, s15, 5898240\n" \
"buffer_load_dwordx4 v32, s[40:43], s54 offen nt lds\n" \
"s_add_u32 m0, s62, 150528\n" \
"s_xor_b32 s54, s15, 5898240\n" \
"buffer_load_dwordx4 v33, s[40:43], s54 offen nt lds\n" \
"s_add_u32 m0, s62, 131072\n" \
"s_xor_b32 s54, s15, 6029312\n" \
"buffer_load_dwordx4 v32, s[40:43], s54 offen nt lds\n" \
"s_add_u32 m0, s62, 132096\n" \
"s_xor_b32 s54, s15, 6029312\n" \
"buffer_load_dwordx4 v33, s[40:43], s54 offen nt lds\n" \
"s_add_u32 m0, s62, 133120\n" \
"s_xor_b32 s54, s15, 6160384\n" \
"buffer_load_dwordx4 v32, s[40:43], s54 offen nt lds\n" \
"s_add_u32 m0, s62, 134144\n" \
"s_xor_b32 s54, s15, 6160384\n" \
"buffer_load_dwordx4 v33, s[40:43], s54 offen nt lds\n" \
"s_waitcnt vmcnt(32)\n" \
"s_barrier\n" \
"ds_read_b64_tr_b16 a[48:49], v36 offset:49152\n" \
"ds_read_b64_tr_b16 a[50:51], v36 offset:49664\n" \
"ds_read_b64_tr_b16 a[56:57], v37 offset:49152\n" \
"ds_read_b64_tr_b16 a[58:59], v37 offset:49664\n" \
"ds_read_b64_tr_b16 v[128:129], v36 offset:57344\n" \
"ds_read_b64_tr_b16 v[130:131], v36 offset:57856\n" \
"ds_read_b64_tr_b16 v[136:137], v37 offset:57344\n" \
"ds_read_b64_tr_b16 v[138:139], v37 offset:57856\n" \
"ds_read_b64_tr_b16 a[52:53], v36 offset:32768\n" \
"ds_read_b64_tr_b16 a[54:55], v36 offset:33280\n" \
"ds_read_b64_tr_b16 a[60:61], v37 offset:32768\n" \
"ds_read_b64_tr_b16 a[62:63], v37 offset:33280\n" \
"ds_read_b64_tr_b16 v[132:133], v36 offset:40960\n" \
"ds_read_b64_tr_b16 v[134:135], v36 offset:41472\n" \
"ds_read_b64_tr_b16 v[140:141], v37 offset:40960\n" \
"ds_read_b64_tr_b16 v[142:143], v37 offset:41472\n" \
"s_waitcnt lgkmcnt(0)\n" \
"s_barrier\n" \
"s_add_u32 m0, s62, 114688\n" \
"s_xor_b32 s54, s15, 6815744\n" \
"buffer_load_dwordx4 v32, s[40:43], s54 offen nt lds\n" \
"s_add_u32 m0, s62, 115712\n" \
"s_xor_b32 s54, s15, 6815744\n" \
"buffer_load_dwordx4 v33, s[40:43], s54 offen nt lds\n" \
"s_add_u32 m0, s62, 116736\n" \
"s_xor_b32 s54, s15, 6946816\n" \
"buffer_load_dwordx4 v32, s[40:43], s54 offen nt lds\n" \
"s_add_u32 m0, s62, 117760\n" \
"s_xor_b32 s54, s15, 6946816\n" \
"buffer_load_dwordx4 v33, s[40:43], s54 offen nt lds\n" \
"s_add_u32 m0, s62, 98304\n" \
"s_xor_b32 s54, s15, 7077888\n" \
"buffer_load_dwordx4 v32, s[40:43], s54 offen nt lds\n" \
"s_add_u32 m0, s62, 99328\n" \
"s_xor_b32 s54, s15, 7077888\n" \
"buffer_load_dwordx4 v33, s[40:43], s54 offen nt lds\n" \
"s_add_u32 m0, s62, 100352\n" \
"s_xor_b32 s54, s15, 7208960\n" \
"buffer_load_dwordx4 v32, s[40:43], s54 offen nt lds\n" \
"s_add_u32 m0, s62, 101376\n" \
"s_xor_b32 s54, s15, 7208960\n" \
"buffer_load_dwordx4 v33, s[40:43], s54 offen nt lds\n" \
"s_waitcnt vmcnt(32)\n" \
"s_barrier\n" \
"ds_read_b64_tr_b16 a[80:81], v36 offset:16384\n" \
"ds_read_b64_tr_b16 a[82:83], v36 offset:16896\n" \
"ds_read_b64_tr_b16 a[88:89], v37 offset:16384\n" \
"ds_read_b64_tr_b16 a[90:91], v37 offset:16896\n" \
"ds_read_b64_tr_b16 v[144:145], v36 offset:24576\n" \
"ds_read_b64_tr_b16 v[146:147], v36 offset:25088\n" \
"ds_read_b64_tr_b16 v[152:153], v37 offset:24576\n" \
"ds_read_b64_tr_b16 v[154:155], v37 offset:25088\n" \
"ds_read_b64_tr_b16 a[84:85], v36 offset:0\n" \
"ds_read_b64_tr_b16 a[86:87], v36 offset:512\n" \
"ds_read_b64_tr_b16 a[92:93], v37 offset:0\n" \
"ds_read_b64_tr_b16 a[94:95], v37 offset:512\n" \
"ds_read_b64_tr_b16 v[148:149], v36 offset:8192\n" \
"ds_read_b64_tr_b16 v[150:151], v36 offset:8704\n" \
"ds_read_b64_tr_b16 v[156:157], v37 offset:8192\n" \
"ds_read_b64_tr_b16 v[158:159], v37 offset:8704\n" \
"s_waitcnt lgkmcnt(0)\n" \
"s_barrier\n" \
"s_add_u32 m0, s62, 81920\n" \
"s_xor_b32 s54, s15, 7340032\n" \
"buffer_load_dwordx4 v32, s[40:43], s54 offen nt lds\n" \
"s_add_u32 m0, s62, 82944\n" \
"s_xor_b32 s54, s15, 7340032\n" \
"buffer_load_dwordx4 v33, s[40:43], s54 offen nt lds\n" \
"s_add_u32 m0, s62, 83968\n" \
"s_xor_b32 s54, s15, 7471104\n" \
"buffer_load_dwordx4 v32, s[40:43], s54 offen nt lds\n" \
"s_add_u32 m0, s62, 84992\n" \
"s_xor_b32 s54, s15, 7471104\n" \
"buffer_load_dwordx4 v33, s[40:43], s54 offen nt lds\n" \
"s_add_u32 m0, s62, 65536\n" \
"s_xor_b32 s54, s15, 7602176\n" \
"buffer_load_dwordx4 v32, s[40:43], s54 offen nt lds\n" \
"s_add_u32 m0, s62, 66560\n" \
"s_xor_b32 s54, s15, 7602176\n" \
"buffer_load_dwordx4 v33, s[40:43], s54 offen nt lds\n" \
"s_add_u32 m0, s62, 67584\n" \
"s_xor_b32 s54, s15, 7733248\n" \
"buffer_load_dwordx4 v32, s[40:43], s54 offen nt lds\n" \
"s_add_u32 m0, s62, 68608\n" \
"s_xor_b32 s54, s15, 7733248\n" \
"buffer_load_dwordx4 v33, s[40:43], s54 offen nt lds\n" \
"s_waitcnt vmcnt(32)\n" \
"s_barrier\n" \
"ds_read_b64_tr_b16 a[112:113], v34 offset:49152\n" \
"ds_read_b64_tr_b16 a[114:115], v34 offset:49664\n" \
"ds_read_b64_tr_b16 a[120:121], v35 offset:49152\n" \
"ds_read_b64_tr_b16 a[122:123], v35 offset:49664\n" \
"ds_read_b64_tr_b16 v[160:161], v34 offset:57344\n" \
"ds_read_b64_tr_b16 v[162:163], v34 offset:57856\n" \
"ds_read_b64_tr_b16 v[168:169], v35 offset:57344\n" \
"ds_read_b64_tr_b16 v[170:171], v35 offset:57856\n" \
"ds_read_b64_tr_b16 a[116:117], v34 offset:32768\n" \
"ds_read_b64_tr_b16 a[118:119], v34 offset:33280\n" \
"ds_read_b64_tr_b16 a[124:125], v35 offset:32768\n" \
"ds_read_b64_tr_b16 a[126:127], v35 offset:33280\n" \
"ds_read_b64_tr_b16 v[164:165], v34 offset:40960\n" \
"ds_read_b64_tr_b16 v[166:167], v34 offset:41472\n" \
"ds_read_b64_tr_b16 v[172:173], v35 offset:40960\n" \
"ds_read_b64_tr_b16 v[174:175], v35 offset:41472\n" \
"s_waitcnt lgkmcnt(0)\n" \
"s_barrier\n" \
"s_add_u32 m0, s62, 49152\n" \
"s_xor_b32 s54, s15, 7864320\n" \
"buffer_load_dwordx4 v32, s[40:43], s54 offen nt lds\n" \
"s_add_u32 m0, s62, 50176\n" \
"s_xor_b32 s54, s15, 7864320\n" \
"buffer_load_dwordx4 v33, s[40:43], s54 offen nt lds\n" \
"s_add_u32 m0, s62, 51200\n" \
"s_xor_b32 s54, s15, 7995392\n" \
"buffer_load_dwordx4 v32, s[40:43], s54 offen nt lds\n" \
"s_add_u32 m0, s62, 52224\n" \
"s_xor_b32 s54, s15, 7995392\n" \
"buffer_load_dwordx4 v33, s[40:43], s54 offen nt lds\n" \
"s_add_u32 m0, s62, 32768\n" \
"s_xor_b32 s54, s15, 8126464\n" \
"buffer_load_dwordx4 v32, s[40:43], s54 offen nt lds\n" \
"s_add_u32 m0, s62, 33792\n" \
"s_xor_b32 s54, s15, 8126464\n" \
"buffer_load_dwordx4 v33, s[40:43], s54 offen nt lds\n" \
"s_add_u32 m0, s62, 34816\n" \
"s_xor_b32 s54, s15, 8257536\n" \
"buffer_load_dwordx4 v32, s[40:43], s54 offen nt lds\n" \
"s_add_u32 m0, s62, 35840\n" \
"s_xor_b32 s54, s15, 8257536\n" \
"buffer_load_dwordx4 v33, s[40:43], s54 offen nt lds\n" \
"s_waitcnt vmcnt(32)\n" \
"s_barrier\n" \
"ds_read_b64_tr_b16 a[144:145], v34 offset:16384\n" \
"ds_read_b64_tr_b16 a[146:147], v34 offset:16896\n" \
"ds_read_b64_tr_b16 a[152:153], v35 offset:16384\n" \
"ds_read_b64_tr_b16 a[154:155], v35 offset:16896\n" \
"ds_read_b64_tr_b16 v[176:177], v34 offset:24576\n" \
"ds_read_b64_tr_b16 v[178:179], v34 offset:25088\n" \
"ds_read_b64_tr_b16 v[184:185], v35 offset:24576\n" \
"ds_read_b64_tr_b16 v[186:187], v35 offset:25088\n" \
"ds_read_b64_tr_b16 a[148:149], v34 offset:0\n" \
"ds_read_b64_tr_b16 a[150:151], v34 offset:512\n" \
"ds_read_b64_tr_b16 a[156:157], v35 offset:0\n" \
"ds_read_b64_tr_b16 a[158:159], v35 offset:512\n" \
"ds_read_b64_tr_b16 v[180:181], v34 offset:8192\n" \
"ds_read_b64_tr_b16 v[182:183], v34 offset:8704\n" \
"ds_read_b64_tr_b16 v[188:189], v35 offset:8192\n" \
"ds_read_b64_tr_b16 v[190:191], v35 offset:8704\n" \
"s_waitcnt lgkmcnt(0)\n" \
"s_barrier\n" \
"s_add_u32 m0, s62, 0\n" \
"s_xor_b32 s54, s15, 0\n" \
"buffer_load_dwordx4 v32, s[40:43], s54 offen nt lds\n" \
"s_add_u32 m0, s62, 1024\n" \
"s_xor_b32 s54, s15, 0\n" \
"buffer_load_dwordx4 v33, s[40:43], s54 offen nt lds\n" \
"s_add_u32 m0, s62, 2048\n" \
"s_xor_b32 s54, s15, 131072\n" \
"buffer_load_dwordx4 v32, s[40:43], s54 offen nt lds\n" \
"s_add_u32 m0, s62, 3072\n" \
"s_xor_b32 s54, s15, 131072\n" \
"buffer_load_dwordx4 v33, s[40:43], s54 offen nt lds\n" \
"s_add_u32 m0, s62, 16384\n" \
"s_xor_b32 s54, s15, 262144\n" \
"buffer_load_dwordx4 v32, s[40:43], s54 offen nt lds\n" \
"s_add_u32 m0, s62, 17408\n" \
"s_xor_b32 s54, s15, 262144\n" \
"buffer_load_dwordx4 v33, s[40:43], s54 offen nt lds\n" \
"s_add_u32 m0, s62, 18432\n" \
"s_xor_b32 s54, s15, 393216\n" \
"buffer_load_dwordx4 v32, s[40:43], s54 offen nt lds\n" \
"s_add_u32 m0, s62, 19456\n" \
"s_xor_b32 s54, s15, 393216\n" \
"buffer_load_dwordx4 v33, s[40:43], s54 offen nt lds\n" \
"s_waitcnt vmcnt(32)\n" \
"s_barrier\n" \
"ds_read_b64_tr_b16 a[176:177], v38 offset:16384\n" \
"ds_read_b64_tr_b16 a[178:179], v38 offset:16896\n" \
"ds_read_b64_tr_b16 a[184:185], v39 offset:16384\n" \
"ds_read_b64_tr_b16 a[186:187], v39 offset:16896\n" \
"ds_read_b64_tr_b16 v[192:193], v38 offset:24576\n" \
"ds_read_b64_tr_b16 v[194:195], v38 offset:25088\n" \
"ds_read_b64_tr_b16 v[200:201], v39 offset:24576\n" \
"ds_read_b64_tr_b16 v[202:203], v39 offset:25088\n" \
"ds_read_b64_tr_b16 a[180:181], v38 offset:0\n" \
"ds_read_b64_tr_b16 a[182:183], v38 offset:512\n" \
"ds_read_b64_tr_b16 a[188:189], v39 offset:0\n" \
"ds_read_b64_tr_b16 a[190:191], v39 offset:512\n" \
"ds_read_b64_tr_b16 v[196:197], v38 offset:8192\n" \
"ds_read_b64_tr_b16 v[198:199], v38 offset:8704\n" \
"ds_read_b64_tr_b16 v[204:205], v39 offset:8192\n" \
"ds_read_b64_tr_b16 v[206:207], v39 offset:8704\n" \
"s_waitcnt lgkmcnt(0)\n" \
"s_barrier\n" \
"s_waitcnt vmcnt(24)\n" \
"s_barrier\n" \
"ds_read_b64_tr_b16 a[208:209], v36 offset:49152\n" \
"ds_read_b64_tr_b16 a[210:211], v36 offset:49664\n" \
"ds_read_b64_tr_b16 a[216:217], v37 offset:49152\n" \
"ds_read_b64_tr_b16 a[218:219], v37 offset:49664\n" \
"ds_read_b64_tr_b16 v[208:209], v36 offset:57344\n" \
"ds_read_b64_tr_b16 v[210:211], v36 offset:57856\n" \
"ds_read_b64_tr_b16 v[216:217], v37 offset:57344\n" \
"ds_read_b64_tr_b16 v[218:219], v37 offset:57856\n" \
"ds_read_b64_tr_b16 a[212:213], v36 offset:32768\n" \
"ds_read_b64_tr_b16 a[214:215], v36 offset:33280\n" \
"ds_read_b64_tr_b16 a[220:221], v37 offset:32768\n" \
"ds_read_b64_tr_b16 a[222:223], v37 offset:33280\n" \
"ds_read_b64_tr_b16 v[212:213], v36 offset:40960\n" \
"ds_read_b64_tr_b16 v[214:215], v36 offset:41472\n" \
"ds_read_b64_tr_b16 v[220:221], v37 offset:40960\n" \
"ds_read_b64_tr_b16 v[222:223], v37 offset:41472\n" \
"s_waitcnt lgkmcnt(0)\n" \
"s_barrier\n" \
"s_add_u32 m0, s62, 98304\n" \
"s_xor_b32 s54, s15, 1048576\n" \
"buffer_load_dwordx4 v32, s[40:43], s54 offen nt lds\n" \
"s_add_u32 m0, s62, 99328\n" \
"s_xor_b32 s54, s15, 1048576\n" \
"buffer_load_dwordx4 v33, s[40:43], s54 offen nt lds\n" \
"s_add_u32 m0, s62, 100352\n" \
"s_xor_b32 s54, s15, 1179648\n" \
"buffer_load_dwordx4 v32, s[40:43], s54 offen nt lds\n" \
"s_add_u32 m0, s62, 101376\n" \
"s_xor_b32 s54, s15, 1179648\n" \
"buffer_load_dwordx4 v33, s[40:43], s54 offen nt lds\n" \
"s_add_u32 m0, s62, 147456\n" \
"s_xor_b32 s54, s15, 1310720\n" \
"buffer_load_dwordx4 v32, s[40:43], s54 offen nt lds\n" \
"s_add_u32 m0, s62, 148480\n" \
"s_xor_b32 s54, s15, 1310720\n" \
"buffer_load_dwordx4 v33, s[40:43], s54 offen nt lds\n" \
"s_add_u32 m0, s62, 149504\n" \
"s_xor_b32 s54, s15, 1441792\n" \
"buffer_load_dwordx4 v32, s[40:43], s54 offen nt lds\n" \
"s_add_u32 m0, s62, 150528\n" \
"s_xor_b32 s54, s15, 1441792\n" \
"buffer_load_dwordx4 v33, s[40:43], s54 offen nt lds\n" \
"s_waitcnt vmcnt(24)\n" \
"s_barrier\n" \
"ds_read_b64_tr_b16 a[224:225], v36 offset:16384\n" \
"ds_read_b64_tr_b16 a[226:227], v36 offset:16896\n" \
"ds_read_b64_tr_b16 a[232:233], v37 offset:16384\n" \
"ds_read_b64_tr_b16 a[234:235], v37 offset:16896\n" \
"ds_read_b64_tr_b16 v[224:225], v36 offset:24576\n" \
"ds_read_b64_tr_b16 v[226:227], v36 offset:25088\n" \
"ds_read_b64_tr_b16 v[232:233], v37 offset:24576\n" \
"ds_read_b64_tr_b16 v[234:235], v37 offset:25088\n" \
"ds_read_b64_tr_b16 a[228:229], v36 offset:0\n" \
"ds_read_b64_tr_b16 a[230:231], v36 offset:512\n" \
"ds_read_b64_tr_b16 a[236:237], v37 offset:0\n" \
"ds_read_b64_tr_b16 a[238:239], v37 offset:512\n" \
"ds_read_b64_tr_b16 v[228:229], v36 offset:8192\n" \
"ds_read_b64_tr_b16 v[230:231], v36 offset:8704\n" \
"ds_read_b64_tr_b16 v[236:237], v37 offset:8192\n" \
"ds_read_b64_tr_b16 v[238:239], v37 offset:8704\n" \
"s_waitcnt lgkmcnt(0)\n" \
"s_barrier\n" \
"s_add_u32 m0, s62, 65536\n" \
"s_xor_b32 s54, s15, 2097152\n" \
"buffer_load_dwordx4 v32, s[40:43], s54 offen nt lds\n" \
"s_add_u32 m0, s62, 66560\n" \
"s_xor_b32 s54, s15, 2097152\n" \
"buffer_load_dwordx4 v33, s[40:43], s54 offen nt lds\n" \
"s_add_u32 m0, s62, 67584\n" \
"s_xor_b32 s54, s15, 2228224\n" \
"buffer_load_dwordx4 v32, s[40:43], s54 offen nt lds\n" \
"s_add_u32 m0, s62, 68608\n" \
"s_xor_b32 s54, s15, 2228224\n" \
"buffer_load_dwordx4 v33, s[40:43], s54 offen nt lds\n" \
"s_add_u32 m0, s62, 131072\n" \
"s_xor_b32 s54, s15, 2359296\n" \
"buffer_load_dwordx4 v32, s[40:43], s54 offen nt lds\n" \
"s_add_u32 m0, s62, 132096\n" \
"s_xor_b32 s54, s15, 2359296\n" \
"buffer_load_dwordx4 v33, s[40:43], s54 offen nt lds\n" \
"s_add_u32 m0, s62, 133120\n" \
"s_xor_b32 s54, s15, 2490368\n" \
"buffer_load_dwordx4 v32, s[40:43], s54 offen nt lds\n" \
"s_add_u32 m0, s62, 134144\n" \
"s_xor_b32 s54, s15, 2490368\n" \
"buffer_load_dwordx4 v33, s[40:43], s54 offen nt lds\n" \
"s_add_u32 m0, s62, 81920\n" \
"s_xor_b32 s54, s15, 3145728\n" \
"buffer_load_dwordx4 v32, s[40:43], s54 offen nt lds\n" \
"s_add_u32 m0, s62, 82944\n" \
"s_xor_b32 s54, s15, 3145728\n" \
"buffer_load_dwordx4 v33, s[40:43], s54 offen nt lds\n" \
"s_add_u32 m0, s62, 83968\n" \
"s_xor_b32 s54, s15, 3276800\n" \
"buffer_load_dwordx4 v32, s[40:43], s54 offen nt lds\n" \
"s_add_u32 m0, s62, 84992\n" \
"s_xor_b32 s54, s15, 3276800\n" \
"buffer_load_dwordx4 v33, s[40:43], s54 offen nt lds\n" \
"s_add_u32 m0, s62, 114688\n" \
"s_xor_b32 s54, s15, 3407872\n" \
"buffer_load_dwordx4 v32, s[40:43], s54 offen nt lds\n" \
"s_add_u32 m0, s62, 115712\n" \
"s_xor_b32 s54, s15, 3407872\n" \
"buffer_load_dwordx4 v33, s[40:43], s54 offen nt lds\n" \
"s_add_u32 m0, s62, 116736\n" \
"s_xor_b32 s54, s15, 3538944\n" \
"buffer_load_dwordx4 v32, s[40:43], s54 offen nt lds\n" \
"s_add_u32 m0, s62, 117760\n" \
"s_xor_b32 s54, s15, 3538944\n" \
"buffer_load_dwordx4 v33, s[40:43], s54 offen nt lds\n" \
"s_waitcnt vmcnt(32)\n" \
"s_barrier\n" \
"ds_read_b64_tr_b16 a[240:241], v34 offset:49152\n" \
"ds_read_b64_tr_b16 a[242:243], v34 offset:49664\n" \
"ds_read_b64_tr_b16 a[248:249], v35 offset:49152\n" \
"ds_read_b64_tr_b16 a[250:251], v35 offset:49664\n" \
"ds_read_b64_tr_b16 v[240:241], v34 offset:57344\n" \
"ds_read_b64_tr_b16 v[242:243], v34 offset:57856\n" \
"ds_read_b64_tr_b16 v[248:249], v35 offset:57344\n" \
"ds_read_b64_tr_b16 v[250:251], v35 offset:57856\n" \
"ds_read_b64_tr_b16 a[244:245], v34 offset:32768\n" \
"ds_read_b64_tr_b16 a[246:247], v34 offset:33280\n" \
"ds_read_b64_tr_b16 a[252:253], v35 offset:32768\n" \
"ds_read_b64_tr_b16 a[254:255], v35 offset:33280\n" \
"ds_read_b64_tr_b16 v[244:245], v34 offset:40960\n" \
"ds_read_b64_tr_b16 v[246:247], v34 offset:41472\n" \
"ds_read_b64_tr_b16 v[252:253], v35 offset:40960\n" \
"ds_read_b64_tr_b16 v[254:255], v35 offset:41472\n" \
"s_waitcnt lgkmcnt(0)\n" \
"s_barrier\n" \
"s_add_u32 m0, s62, 32768\n" \
"s_xor_b32 s54, s15, 4194304\n" \
"buffer_load_dwordx4 v32, s[40:43], s54 offen nt lds\n" \
"s_add_u32 m0, s62, 33792\n" \
"s_xor_b32 s54, s15, 4194304\n" \
"buffer_load_dwordx4 v33, s[40:43], s54 offen nt lds\n" \
"s_add_u32 m0, s62, 34816\n" \
"s_xor_b32 s54, s15, 4325376\n" \
"buffer_load_dwordx4 v32, s[40:43], s54 offen nt lds\n" \
"s_add_u32 m0, s62, 35840\n" \
"s_xor_b32 s54, s15, 4325376\n" \
"buffer_load_dwordx4 v33, s[40:43], s54 offen nt lds\n" \
"s_add_u32 m0, s62, 49152\n" \
"s_xor_b32 s54, s15, 4456448\n" \
"buffer_load_dwordx4 v32, s[40:43], s54 offen nt lds\n" \
"s_add_u32 m0, s62, 50176\n" \
"s_xor_b32 s54, s15, 4456448\n" \
"buffer_load_dwordx4 v33, s[40:43], s54 offen nt lds\n" \
"s_add_u32 m0, s62, 51200\n" \
"s_xor_b32 s54, s15, 4587520\n" \
"buffer_load_dwordx4 v32, s[40:43], s54 offen nt lds\n" \
"s_add_u32 m0, s62, 52224\n" \
"s_xor_b32 s54, s15, 4587520\n" \
"buffer_load_dwordx4 v33, s[40:43], s54 offen nt lds\n" \
"s_waitcnt vmcnt(32)\n" \
"s_barrier\n" \
"ds_read_b64_tr_b16 a[0:1], v34 offset:0\n" \
"ds_read_b64_tr_b16 a[2:3], v34 offset:512\n" \
"ds_read_b64_tr_b16 a[8:9], v35 offset:0\n" \
"ds_read_b64_tr_b16 a[10:11], v35 offset:512\n" \
"ds_read_b64_tr_b16 v[48:49], v34 offset:8192\n" \
"ds_read_b64_tr_b16 v[50:51], v34 offset:8704\n" \
"ds_read_b64_tr_b16 v[56:57], v35 offset:8192\n" \
"ds_read_b64_tr_b16 v[58:59], v35 offset:8704\n" \
"ds_read_b64_tr_b16 a[4:5], v34 offset:16384\n" \
"ds_read_b64_tr_b16 a[6:7], v34 offset:16896\n" \
"ds_read_b64_tr_b16 a[12:13], v35 offset:16384\n" \
"ds_read_b64_tr_b16 a[14:15], v35 offset:16896\n" \
"ds_read_b64_tr_b16 v[52:53], v34 offset:24576\n" \
"ds_read_b64_tr_b16 v[54:55], v34 offset:25088\n" \
"ds_read_b64_tr_b16 v[60:61], v35 offset:24576\n" \
"ds_read_b64_tr_b16 v[62:63], v35 offset:25088\n" \
"s_waitcnt lgkmcnt(0)\n" \
"s_barrier\n" \
"ds_write_b128 v1, v[48:51] offset:0\n" \
"ds_write_b128 v1, v[52:55] offset:1024\n" \
"ds_write_b128 v1, v[56:59] offset:2048\n" \
"ds_write_b128 v1, v[60:63] offset:3072\n" \
"s_add_u32 m0, s62, 16384\n" \
"s_xor_b32 s54, s15, 5242880\n" \
"buffer_load_dwordx4 v32, s[40:43], s54 offen nt lds\n" \
"s_add_u32 m0, s62, 17408\n" \
"s_xor_b32 s54, s15, 5242880\n" \
"buffer_load_dwordx4 v33, s[40:43], s54 offen nt lds\n" \
"s_add_u32 m0, s62, 18432\n" \
"s_xor_b32 s54, s15, 5373952\n" \
"buffer_load_dwordx4 v32, s[40:43], s54 offen nt lds\n" \
"s_add_u32 m0, s62, 19456\n" \
"s_xor_b32 s54, s15, 5373952\n" \
"buffer_load_dwordx4 v33, s[40:43], s54 offen nt lds\n" \
"s_waitcnt vmcnt(28)\n" \
"s_barrier\n" \
"ds_read_b64_tr_b16 a[32:33], v36 offset:32768\n" \
"ds_read_b64_tr_b16 a[34:35], v36 offset:33280\n" \
"ds_read_b64_tr_b16 a[40:41], v37 offset:32768\n" \
"ds_read_b64_tr_b16 a[42:43], v37 offset:33280\n" \
"ds_read_b64_tr_b16 v[48:49], v36 offset:40960\n" \
"ds_read_b64_tr_b16 v[50:51], v36 offset:41472\n" \
"ds_read_b64_tr_b16 v[56:57], v37 offset:40960\n" \
"ds_read_b64_tr_b16 v[58:59], v37 offset:41472\n" \
"ds_read_b64_tr_b16 a[36:37], v38 offset:16384\n" \
"ds_read_b64_tr_b16 a[38:39], v38 offset:16896\n" \
"ds_read_b64_tr_b16 a[44:45], v39 offset:16384\n" \
"ds_read_b64_tr_b16 a[46:47], v39 offset:16896\n" \
"ds_read_b64_tr_b16 v[52:53], v38 offset:24576\n" \
"ds_read_b64_tr_b16 v[54:55], v38 offset:25088\n" \
"ds_read_b64_tr_b16 v[60:61], v39 offset:24576\n" \
"ds_read_b64_tr_b16 v[62:63], v39 offset:25088\n" \
"s_waitcnt lgkmcnt(0)\n" \
"s_barrier\n" \
"ds_write_b128 v2, v[48:51] offset:32768\n" \
"ds_write_b128 v2, v[52:55] offset:33792\n" \
"ds_write_b128 v2, v[56:59] offset:34816\n" \
"ds_write_b128 v2, v[60:63] offset:35840\n" \
"s_add_u32 m0, s62, 147456\n" \
"s_xor_b32 s54, s15, 5505024\n" \
"buffer_load_dwordx4 v32, s[40:43], s54 offen nt lds\n" \
"s_add_u32 m0, s62, 148480\n" \
"s_xor_b32 s54, s15, 5505024\n" \
"buffer_load_dwordx4 v33, s[40:43], s54 offen nt lds\n" \
"s_add_u32 m0, s62, 149504\n" \
"s_xor_b32 s54, s15, 5636096\n" \
"buffer_load_dwordx4 v32, s[40:43], s54 offen nt lds\n" \
"s_add_u32 m0, s62, 150528\n" \
"s_xor_b32 s54, s15, 5636096\n" \
"buffer_load_dwordx4 v33, s[40:43], s54 offen nt lds\n" \
"s_waitcnt vmcnt(24)\n" \
"s_barrier\n" \
"ds_read_b64_tr_b16 a[64:65], v36 offset:0\n" \
"ds_read_b64_tr_b16 a[66:67], v36 offset:512\n" \
"ds_read_b64_tr_b16 a[72:73], v37 offset:0\n" \
"ds_read_b64_tr_b16 a[74:75], v37 offset:512\n" \
"ds_read_b64_tr_b16 v[48:49], v36 offset:8192\n" \
"ds_read_b64_tr_b16 v[50:51], v36 offset:8704\n" \
"ds_read_b64_tr_b16 v[56:57], v37 offset:8192\n" \
"ds_read_b64_tr_b16 v[58:59], v37 offset:8704\n" \
"ds_read_b64_tr_b16 a[68:69], v38 offset:0\n" \
"ds_read_b64_tr_b16 a[70:71], v38 offset:512\n" \
"ds_read_b64_tr_b16 a[76:77], v39 offset:0\n" \
"ds_read_b64_tr_b16 a[78:79], v39 offset:512\n" \
"ds_read_b64_tr_b16 v[52:53], v38 offset:8192\n" \
"ds_read_b64_tr_b16 v[54:55], v38 offset:8704\n" \
"ds_read_b64_tr_b16 v[60:61], v39 offset:8192\n" \
"ds_read_b64_tr_b16 v[62:63], v39 offset:8704\n" \
"s_waitcnt lgkmcnt(0)\n" \
"s_barrier\n" \
"ds_write_b128 v2, v[48:51] offset:0\n" \
"ds_write_b128 v2, v[52:55] offset:1024\n" \
"ds_write_b128 v2, v[56:59] offset:2048\n" \
"ds_write_b128 v2, v[60:63] offset:3072\n" \
"s_waitcnt vmcnt(16)\n" \
"s_barrier\n" \
"ds_read_b64_tr_b16 a[96:97], v36 offset:16384\n" \
"ds_read_b64_tr_b16 a[98:99], v36 offset:16896\n" \
"ds_read_b64_tr_b16 a[104:105], v37 offset:16384\n" \
"ds_read_b64_tr_b16 a[106:107], v37 offset:16896\n" \
"ds_read_b64_tr_b16 v[48:49], v36 offset:24576\n" \
"ds_read_b64_tr_b16 v[50:51], v36 offset:25088\n" \
"ds_read_b64_tr_b16 v[56:57], v37 offset:24576\n" \
"ds_read_b64_tr_b16 v[58:59], v37 offset:25088\n" \
"ds_read_b64_tr_b16 a[100:101], v36 offset:49152\n" \
"ds_read_b64_tr_b16 a[102:103], v36 offset:49664\n" \
"ds_read_b64_tr_b16 a[108:109], v37 offset:49152\n" \
"ds_read_b64_tr_b16 a[110:111], v37 offset:49664\n" \
"ds_read_b64_tr_b16 v[52:53], v36 offset:57344\n" \
"ds_read_b64_tr_b16 v[54:55], v36 offset:57856\n" \
"ds_read_b64_tr_b16 v[60:61], v37 offset:57344\n" \
"ds_read_b64_tr_b16 v[62:63], v37 offset:57856\n" \
"s_waitcnt lgkmcnt(0)\n" \
"s_barrier\n" \
"ds_write_b128 v2, v[48:51] offset:16384\n" \
"ds_write_b128 v2, v[52:55] offset:17408\n" \
"ds_write_b128 v2, v[56:59] offset:18432\n" \
"ds_write_b128 v2, v[60:63] offset:19456\n" \
"s_waitcnt vmcnt(8)\n" \
"s_barrier\n" \
"ds_read_b64_tr_b16 a[128:129], v34 offset:32768\n" \
"ds_read_b64_tr_b16 a[130:131], v34 offset:33280\n" \
"ds_read_b64_tr_b16 a[136:137], v35 offset:32768\n" \
"ds_read_b64_tr_b16 a[138:139], v35 offset:33280\n" \
"ds_read_b64_tr_b16 v[48:49], v34 offset:40960\n" \
"ds_read_b64_tr_b16 v[50:51], v34 offset:41472\n" \
"ds_read_b64_tr_b16 v[56:57], v35 offset:40960\n" \
"ds_read_b64_tr_b16 v[58:59], v35 offset:41472\n" \
"ds_read_b64_tr_b16 a[132:133], v34 offset:49152\n" \
"ds_read_b64_tr_b16 a[134:135], v34 offset:49664\n" \
"ds_read_b64_tr_b16 a[140:141], v35 offset:49152\n" \
"ds_read_b64_tr_b16 a[142:143], v35 offset:49664\n" \
"ds_read_b64_tr_b16 v[52:53], v34 offset:57344\n" \
"ds_read_b64_tr_b16 v[54:55], v34 offset:57856\n" \
"ds_read_b64_tr_b16 v[60:61], v35 offset:57344\n" \
"ds_read_b64_tr_b16 v[62:63], v35 offset:57856\n" \
"s_waitcnt lgkmcnt(0)\n" \
"s_barrier\n" \
"ds_write_b128 v1, v[48:51] offset:32768\n" \
"ds_write_b128 v1, v[52:55] offset:33792\n" \
"ds_write_b128 v1, v[56:59] offset:34816\n" \
"ds_write_b128 v1, v[60:63] offset:35840\n" \
"s_add_u32 m0, s62, 49152\n" \
"s_xor_b32 s54, s15, 6291456\n" \
"buffer_load_dwordx4 v32, s[40:43], s54 offen nt lds\n" \
"s_add_u32 m0, s62, 50176\n" \
"s_xor_b32 s54, s15, 6291456\n" \
"buffer_load_dwordx4 v33, s[40:43], s54 offen nt lds\n" \
"s_add_u32 m0, s62, 51200\n" \
"s_xor_b32 s54, s15, 6422528\n" \
"buffer_load_dwordx4 v32, s[40:43], s54 offen nt lds\n" \
"s_add_u32 m0, s62, 52224\n" \
"s_xor_b32 s54, s15, 6422528\n" \
"buffer_load_dwordx4 v33, s[40:43], s54 offen nt lds\n" \
"s_add_u32 m0, s62, 131072\n" \
"s_xor_b32 s54, s15, 6553600\n" \
"buffer_load_dwordx4 v32, s[40:43], s54 offen nt lds\n" \
"s_add_u32 m0, s62, 132096\n" \
"s_xor_b32 s54, s15, 6553600\n" \
"buffer_load_dwordx4 v33, s[40:43], s54 offen nt lds\n" \
"s_add_u32 m0, s62, 133120\n" \
"s_xor_b32 s54, s15, 6684672\n" \
"buffer_load_dwordx4 v32, s[40:43], s54 offen nt lds\n" \
"s_add_u32 m0, s62, 134144\n" \
"s_xor_b32 s54, s15, 6684672\n" \
"buffer_load_dwordx4 v33, s[40:43], s54 offen nt lds\n" \
"s_waitcnt vmcnt(8)\n" \
"s_barrier\n" \
"ds_read_b64_tr_b16 a[160:161], v34 offset:16384\n" \
"ds_read_b64_tr_b16 a[162:163], v34 offset:16896\n" \
"ds_read_b64_tr_b16 a[168:169], v35 offset:16384\n" \
"ds_read_b64_tr_b16 a[170:171], v35 offset:16896\n" \
"ds_read_b64_tr_b16 v[48:49], v34 offset:24576\n" \
"ds_read_b64_tr_b16 v[50:51], v34 offset:25088\n" \
"ds_read_b64_tr_b16 v[56:57], v35 offset:24576\n" \
"ds_read_b64_tr_b16 v[58:59], v35 offset:25088\n" \
"ds_read_b64_tr_b16 a[164:165], v38 offset:16384\n" \
"ds_read_b64_tr_b16 a[166:167], v38 offset:16896\n" \
"ds_read_b64_tr_b16 a[172:173], v39 offset:16384\n" \
"ds_read_b64_tr_b16 a[174:175], v39 offset:16896\n" \
"ds_read_b64_tr_b16 v[52:53], v38 offset:24576\n" \
"ds_read_b64_tr_b16 v[54:55], v38 offset:25088\n" \
"ds_read_b64_tr_b16 v[60:61], v39 offset:24576\n" \
"ds_read_b64_tr_b16 v[62:63], v39 offset:25088\n" \
"s_waitcnt lgkmcnt(0)\n" \
"s_barrier\n" \
"ds_write_b128 v1, v[48:51] offset:16384\n" \
"ds_write_b128 v1, v[52:55] offset:17408\n" \
"ds_write_b128 v1, v[56:59] offset:18432\n" \
"ds_write_b128 v1, v[60:63] offset:19456\n" \
"s_waitcnt vmcnt(0)\n" \
"s_barrier\n" \
"ds_read_b64_tr_b16 a[192:193], v34 offset:49152\n" \
"ds_read_b64_tr_b16 a[194:195], v34 offset:49664\n" \
"ds_read_b64_tr_b16 a[200:201], v35 offset:49152\n" \
"ds_read_b64_tr_b16 a[202:203], v35 offset:49664\n" \
"ds_read_b64_tr_b16 v[48:49], v34 offset:57344\n" \
"ds_read_b64_tr_b16 v[50:51], v34 offset:57856\n" \
"ds_read_b64_tr_b16 v[56:57], v35 offset:57344\n" \
"ds_read_b64_tr_b16 v[58:59], v35 offset:57856\n" \
"ds_read_b64_tr_b16 a[196:197], v38 offset:0\n" \
"ds_read_b64_tr_b16 a[198:199], v38 offset:512\n" \
"ds_read_b64_tr_b16 a[204:205], v39 offset:0\n" \
"ds_read_b64_tr_b16 a[206:207], v39 offset:512\n" \
"ds_read_b64_tr_b16 v[52:53], v38 offset:8192\n" \
"ds_read_b64_tr_b16 v[54:55], v38 offset:8704\n" \
"ds_read_b64_tr_b16 v[60:61], v39 offset:8192\n" \
"ds_read_b64_tr_b16 v[62:63], v39 offset:8704\n" \
"s_waitcnt lgkmcnt(0)\n" \
"s_barrier\n" \
"ds_write_b128 v1, v[48:51] offset:49152\n" \
"ds_write_b128 v1, v[52:55] offset:50176\n" \
"ds_write_b128 v1, v[56:59] offset:51200\n" \
"ds_write_b128 v1, v[60:63] offset:52224\n" \
"s_waitcnt vmcnt(0) lgkmcnt(0)\n" \
"s_barrier\n" \
"s_lshl_b32 s62, s48, 11\n" \
"s_add_u32 s63, s63, s62\n" \
"s_mov_b32 s49, 0\n" \
"s_mov_b32 s52, 0\n" \
"s_mov_b32 s81, 0\n" \
"s_mov_b32 s88, 0\n" \
"s_mov_b32 s95, 0x7ffffff0\n" \
"s_mov_b32 s98, 0x3e0293ee\n" \
"s_mov_b32 s99, 0x3e0293ee\n" \
"v_mov_b32 v20, 0x3f803f80\n" \
"v_mov_b32 v21, 0x3f803f80\n" \
"v_mov_b32 v22, 0x3f803f80\n" \
"v_mov_b32 v23, 0x3f803f80\n" \
"v_mov_b32 v24, 0\n" \
"v_mov_b32 v25, 0\n" \
"v_mov_b32 v26, 0\n" \
"v_mov_b32 v27, 0\n" \
"v_mov_b32 v28, 0\n" \
"v_mov_b32 v29, 0\n" \
"v_mov_b32 v104, 0\n" \
"v_mov_b32 v105, 0\n" \
"v_mov_b32 v106, 0\n" \
"v_mov_b32 v107, 0\n" \
"v_mov_b32 v108, 0\n" \
"v_mov_b32 v109, 0\n" \
"v_mov_b32 v110, 0\n" \
"v_mov_b32 v111, 0\n" \
"v_cmp_le_i32_e64 s[64:65], 0, v14\n" \
"v_cmp_le_i32_e64 s[66:67], 1, v14\n" \
"v_cmp_le_i32_e64 s[68:69], 2, v14\n" \
"v_cmp_le_i32_e64 s[70:71], 3, v14\n" \
"v_cmp_le_i32_e64 s[72:73], 16, v14\n" \
"v_cmp_le_i32_e64 s[74:75], 17, v14\n" \
"v_cmp_le_i32_e64 s[76:77], 18, v14\n" \
"v_cmp_le_i32_e64 s[78:79], 19, v14\n" \
"s_mov_b32 s82, -1\n" \
"s_mov_b32 s83, 0\n" \
"s_mov_b32 s22, 0xfffffc00\n" \
"s_mov_b32 s23, -1\n" \
"s_mul_i32 s32, s46, 0x1010101\n" \
"s_mov_b32 s33, s32\n" \
"s_mov_b32 s34, s46\n" \
"s_mov_b32 s35, s17\n" \
"s_mov_b32 s41, s10\n" \
"s_lshl_b32 s42, s48, 10\n" \
"s_add_u32 s42, s42, 114688\n" \
"s_and_b32 s43, s48, 1\n" \
"s_lshl_b32 s43, s43, 12\n" \
"s_mov_b32 s54, 0\n" \
"buffer_store_dwordx4 v[24:27], v7, s[36:39], s54 offen sc1\n" \
"s_mov_b32 s54, 131072\n" \
"buffer_store_dwordx4 v[24:27], v7, s[36:39], s54 offen sc1\n" \
"s_add_u32 s54, s94, 65536\n" \
"buffer_store_dword v24, v10, s[36:39], s54 offen sc1\n" \
"s_xor_b32 s54, s54, 0x4800\n" \
"buffer_store_dword v24, v10, s[36:39], s54 offen sc1\n" \
"s_mov_b32 s54, 262144\n" \
"buffer_store_dwordx4 v[24:27], v7, s[36:39], s54 offen sc1\n" \
"s_add_u32 s54, s94, 131072\n" \
"buffer_store_dword v24, v10, s[36:39], s54 offen sc1\n" \
"s_xor_b32 s54, s54, 0x4800\n" \
"buffer_store_dword v24, v10, s[36:39], s54 offen sc1\n" \
"s_mov_b32 s54, 393216\n" \
"buffer_store_dwordx4 v[24:27], v7, s[36:39], s54 offen sc1\n" \
"s_add_u32 s54, s94, 196608\n" \
"buffer_store_dword v24, v10, s[36:39], s54 offen sc1\n" \
"s_xor_b32 s54, s54, 0x4800\n" \
"buffer_store_dword v24, v10, s[36:39], s54 offen sc1\n" \
"s_cmp_eq_u32 s48, 0\n" \
"s_cselect_b32 s9, 7, 0\n" \
"buffer_load_dword v30, v15, s[36:39], s80 offen sc1\n" \
"s_waitcnt vmcnt(0)\n" \
"v_cmp_ge_u32_e64 s[96:97], s81, v30\n" \
"s_nop 4\n" \
"s_and_b32 s96, s96, s9\n" \
"s_cbranch_scc1 .Lq_f0_cold_%=\n" \
".Lq_f0_ok_%=:\n" \
"s_barrier\n" \
"s_add_u32 m0, s62, 131072\n" \
"s_add_u32 s15, s63, 10371072\n" \
"buffer_load_dwordx4 v3, s[36:39], s15 offen offset:0 sc1 lds\n" \
"buffer_load_dwordx4 v3, s[36:39], s15 offen offset:1024 sc1 lds\n" \
"s_add_u32 m0, s62, 139264\n" \
"s_add_u32 s15, s63, 10379264\n" \
"buffer_load_dwordx4 v3, s[36:39], s15 offen offset:0 sc1 lds\n" \
"buffer_load_dwordx4 v3, s[36:39], s15 offen offset:1024 sc1 lds\n" \
"s_add_u32 m0, s62, 147456\n" \
"s_add_u32 s15, s63, 10387456\n" \
"buffer_load_dwordx4 v3, s[36:39], s15 offen offset:0 sc1 lds\n" \
"buffer_load_dwordx4 v3, s[36:39], s15 offen offset:1024 sc1 lds\n" \
"s_memrealtime s[56:57]\n" \
"s_waitcnt lgkmcnt(0)\n" \
"s_sub_u32 s50, s56, 20\n" \
"s_waitcnt vmcnt(0) lgkmcnt(0)\n" \
"s_add_u32 s54, s49, 0\n" \
"s_and_b32 s54, s54, 3\n" \
"s_mul_i32 s58, s54, 131072\n" \
"s_add_u32 s54, s49, 1\n" \
"s_and_b32 s54, s54, 3\n" \
"s_mul_i32 s59, s54, 131072\n" \
"s_add_u32 s54, s49, 2\n" \
"s_and_b32 s54, s54, 3\n" \
"s_mul_i32 s60, s54, 131072\n" \
"s_add_u32 s54, s49, 3\n" \
"s_and_b32 s54, s54, 3\n" \
"s_mul_i32 s61, s54, 131072\n" \
"s_lshr_b32 s53, s58, 1\n" \
"s_add_u32 s92, s53, s94\n" \
"s_add_u32 s55, s53, s41\n" \
"s_xor_b32 s89, s55, 0x4800\n" \
"s_xor_b32 s90, s55, 0x800\n" \
"s_xor_b32 s91, s55, 0x4000\n" \
"s_xor_b32 s93, s92, 0x20000\n" \
"s_add_u32 s81, s49, 1\n" \
"s_min_u32 s81, s81, 63\n" \
"ds_read_b128 v[32:35], v1 offset:0\n" \
"ds_read_b128 v[36:39], v1 offset:1024\n" \
"ds_read_b128 v[40:43], v1 offset:2048\n" \
"ds_read_b128 v[44:47], v1 offset:3072\n" \
"ds_read_b128 v[48:51], v2 offset:32768\n" \
"ds_read_b128 v[52:55], v2 offset:33792\n" \
"ds_read_b128 v[56:59], v2 offset:34816\n" \
"ds_read_b128 v[60:63], v2 offset:35840\n" \
"v_mov_b64 v[80:81], 0\n" \
"v_mov_b64 v[82:83], 0\n" \
"v_mov_b64 v[84:85], 0\n" \
"v_mov_b64 v[86:87], 0\n" \
"v_mov_b64 v[88:89], 0\n" \
"v_mov_b64 v[90:91], 0\n" \
"v_mov_b64 v[92:93], 0\n" \
"v_mov_b64 v[94:95], 0\n" \
"v_mov_b32 v13, -1\n" \
"v_add_u32 v14, s62, v3\n" \
"v_xor_b32 v31, 0x400, v11\n" \
"s_max_u32 s53, s49, 1\n" \
"s_sub_u32 s53, s53, 1\n" \
"v_mov_b32 v30, s53\n" \
"s_mov_b32 m0, s42\n" \
"s_waitcnt vmcnt(0) lgkmcnt(0)\n" \
"s_nop 7\n" \
"s_nop 7\n" \
".p2align 6\n" \
".Lq_loop_%=:\n" \
"s_getpc_b64 s[18:19]\n" \
".Lq_wp_%=:\n" \
"s_add_u32 s18, s18, .Lq_ws_%=-.Lq_wp_%=\n" \
"s_addc_u32 s19, s19, 0\n" \
"s_sub_u32 s21, s50, 5\n" \
"s_nop 0\n" \
".Lq_w_%=:\n" \
"s_memrealtime s[56:57]\n" \
"s_waitcnt lgkmcnt(0)\n" \
"s_sub_u32 s54, s56, s21\n" \
"s_cmp_gt_u32 s54, s22\n" \
"s_cbranch_scc1 .Lq_w_%=\n" \
"s_min_u32 s21, s54, 3\n" \
"s_lshl2_add_u32 s18, s21, s18\n" \
"s_setpc_b64 s[18:19]\n" \
"s_nop 0\n" \
".Lq_ws_%=:\n" \
"s_nop 4\n" \
"s_nop 5\n" \
"s_nop 4\n" \
".Lq_w4_%=:\n" \
"s_barrier\n" \
".Lq_go_%=:\n" \
"buffer_load_dwordx4 v4, s[36:39], s55 offen sc1 lds\n" \
"s_cmp_lt_u32 s54, 8\n" \
"s_cselect_b32 s51, s50, s56\n" \
"s_cselect_b32 s23, s23, s49\n" \
"s_cselect_b32 s53, 0, s14\n" \
"s_add_u32 s53, s53, s51\n" \
"s_add_u32 s53, s53, s34\n" \
"s_max_u32 s53, s53, 1\n" \
"v_mov_b32 v16, s53\n" \
"v_mov_b32 v17, s35\n" \
"s_nop 7\n" \
"s_nop 7\n" \
"s_add_u32 m0, s42, 4096\n" \
"s_nop 0\n" \
"buffer_load_dwordx4 v4, s[36:39], s89 offen sc1 lds\n" \
"s_nop 7\n" \
"s_nop 7\n" \
"s_nop 7\n" \
"s_nop 7\n" \
"s_nop 7\n" \
"s_add_u32 m0, s42, 8192\n" \
"s_nop 0\n" \
"buffer_load_dwordx4 v4, s[36:39], s90 offen sc1 lds\n" \
"s_nop 7\n" \
"s_add_u32 m0, s42, 12288\n" \
"s_nop 0\n" \
"buffer_load_dwordx4 v4, s[36:39], s91 offen sc1 lds\n" \
"s_waitcnt vmcnt(3)\n" \
"s_barrier\n" \
"ds_read_b128 v[64:67], v3 offset:0\n" \
"ds_read_b128 v[68:71], v3 offset:1024\n" \
"ds_read_b128 v[72:75], v3 offset:2048\n" \
"ds_read_b128 v[76:79], v3 offset:3072\n" \
"s_waitcnt lgkmcnt(3)\n" \
"v_smfmac_f32_16x16x64_bf16 v[80:83], v[64:67], a[0:7], v12\n" \
"v_min3_u32 v13, v13, v64, v66\n" \
"v_smfmac_f32_16x16x64_bf16 v[84:87], v[64:67], a[8:15], v12\n" \
"s_waitcnt lgkmcnt(2)\n" \
"v_min3_u32 v13, v13, v68, v70\n" \
"v_smfmac_f32_16x16x64_bf16 v[88:91], v[64:67], v[32:39], v12\n" \
"s_waitcnt lgkmcnt(1)\n" \
"v_min3_u32 v13, v13, v72, v74\n" \
"v_smfmac_f32_16x16x64_bf16 v[92:95], v[64:67], v[40:47], v12\n" \
"s_waitcnt lgkmcnt(0)\n" \
"v_min3_u32 v13, v13, v76, v78\n" \
"v_smfmac_f32_16x16x64_bf16 v[80:83], v[68:71], a[16:23], v12\n" \
"v_cmp_eq_u32_e64 vcc, 0, v13\n" \
"v_smfmac_f32_16x16x64_bf16 v[84:87], v[68:71], a[24:31], v12\n" \
"ds_read_b128 v[32:35], v2 offset:0\n" \
"v_smfmac_f32_16x16x64_bf16 v[88:91], v[68:71], v[112:119], v12\n" \
"ds_read_b128 v[36:39], v2 offset:1024\n" \
"v_smfmac_f32_16x16x64_bf16 v[92:95], v[68:71], v[120:127], v12\n" \
"s_cbranch_vccnz .Lq_retry_%=\n" \
"s_waitcnt vmcnt(2)\n" \
"s_barrier\n" \
"ds_read_b128 v[64:67], v3 offset:4096\n" \
"v_smfmac_f32_16x16x64_bf16 v[80:83], v[72:75], a[32:39], v12\n" \
"ds_read_b128 v[68:71], v3 offset:5120\n" \
"v_smfmac_f32_16x16x64_bf16 v[84:87], v[72:75], a[40:47], v12\n" \
"ds_read_b128 v[40:43], v2 offset:2048\n" \
"v_smfmac_f32_16x16x64_bf16 v[88:91], v[72:75], v[48:55], v12\n" \
"ds_read_b128 v[44:47], v2 offset:3072\n" \
"v_smfmac_f32_16x16x64_bf16 v[92:95], v[72:75], v[56:63], v12\n" \
"ds_read_b128 v[72:75], v3 offset:6144\n" \
"v_smfmac_f32_16x16x64_bf16 v[80:83], v[76:79], a[48:55], v12\n" \
"buffer_store_dwordx2 v[16:17], v10, s[36:39], s92 offen sc1\n" \
"v_smfmac_f32_16x16x64_bf16 v[84:87], v[76:79], a[56:63], v12\n" \
"ds_read_b128 v[48:51], v2 offset:16384\n" \
"v_smfmac_f32_16x16x64_bf16 v[88:91], v[76:79], v[128:135], v12\n" \
"ds_read_b128 v[52:55], v2 offset:17408\n" \
"v_smfmac_f32_16x16x64_bf16 v[92:95], v[76:79], v[136:143], v12\n" \
"ds_read_b128 v[76:79], v3 offset:7168\n" \
"s_waitcnt lgkmcnt(7)\n" \
"v_smfmac_f32_16x16x64_bf16 v[80:83], v[64:67], a[64:71], v12\n" \
"v_min3_u32 v13, v13, v64, v66\n" \
"v_smfmac_f32_16x16x64_bf16 v[84:87], v[64:67], a[72:79], v12\n" \
"ds_read_b128 v[56:59], v2 offset:18432\n" \
"v_smfmac_f32_16x16x64_bf16 v[88:91], v[64:67], v[32:39], v12\n" \
"ds_read_b128 v[60:63], v2 offset:19456\n" \
"s_waitcnt lgkmcnt(6)\n" \
"v_smfmac_f32_16x16x64_bf16 v[92:95], v[64:67], v[40:47], v12\n" \
"v_smfmac_f32_16x16x64_bf16 v[80:83], v[68:71], a[80:87], v12\n" \
"v_min3_u32 v13, v13, v68, v70\n" \
"v_smfmac_f32_16x16x64_bf16 v[84:87], v[68:71], a[88:95], v12\n" \
"ds_read_b128 v[32:35], v1 offset:32768\n" \
"v_smfmac_f32_16x16x64_bf16 v[88:91], v[68:71], v[144:151], v12\n" \
"ds_read_b128 v[36:39], v1 offset:33792\n" \
"v_smfmac_f32_16x16x64_bf16 v[92:95], v[68:71], v[152:159], v12\n" \
"s_waitcnt lgkmcnt(7)\n" \
"v_smfmac_f32_16x16x64_bf16 v[80:83], v[72:75], a[96:103], v12\n" \
"v_min3_u32 v13, v13, v72, v74\n" \
"v_smfmac_f32_16x16x64_bf16 v[84:87], v[72:75], a[104:111], v12\n" \
"ds_read_b128 v[40:43], v1 offset:34816\n" \
"s_waitcnt lgkmcnt(6)\n" \
"v_smfmac_f32_16x16x64_bf16 v[88:91], v[72:75], v[48:55], v12\n" \
"ds_read_b128 v[44:47], v1 offset:35840\n" \
"s_waitcnt lgkmcnt(4)\n" \
"v_smfmac_f32_16x16x64_bf16 v[92:95], v[72:75], v[56:63], v12\n" \
"s_waitcnt vmcnt(1)\n" \
"s_barrier\n" \
"ds_read_b128 v[64:67], v3 offset:8192\n" \
"v_smfmac_f32_16x16x64_bf16 v[80:83], v[76:79], a[112:119], v12\n" \
"ds_read_b128 v[68:71], v3 offset:9216\n" \
"v_min3_u32 v13, v13, v76, v78\n" \
"v_smfmac_f32_16x16x64_bf16 v[84:87], v[76:79], a[120:127], v12\n" \
"ds_read_b128 v[48:51], v1 offset:16384\n" \
"v_smfmac_f32_16x16x64_bf16 v[88:91], v[76:79], v[160:167], v12\n" \
"ds_read_b128 v[52:55], v1 offset:17408\n" \
"v_smfmac_f32_16x16x64_bf16 v[92:95], v[76:79], v[168:175], v12\n" \
"ds_read_b128 v[72:75], v3 offset:10240\n" \
"ds_read_b128 v[76:79], v3 offset:11264\n" \
"s_waitcnt lgkmcnt(5)\n" \
"v_smfmac_f32_16x16x64_bf16 v[80:83], v[64:67], a[128:135], v12\n" \
"buffer_store_dwordx4 v[96:99], v11, s[36:39], s95 offen sc1\n" \
"v_min3_u32 v13, v13, v64, v66\n" \
"v_smfmac_f32_16x16x64_bf16 v[84:87], v[64:67], a[136:143], v12\n" \
"ds_read_b128 v[56:59], v1 offset:18432\n" \
"v_smfmac_f32_16x16x64_bf16 v[88:91], v[64:67], v[32:39], v12\n" \
"ds_read_b128 v[60:63], v1 offset:19456\n" \
"v_smfmac_f32_16x16x64_bf16 v[92:95], v[64:67], v[40:47], v12\n" \
"ds_read_b128 v[64:67], v3 offset:12288\n" \
"s_waitcnt lgkmcnt(7)\n" \
"v_smfmac_f32_16x16x64_bf16 v[80:83], v[68:71], a[144:151], v12\n" \
"v_min3_u32 v13, v13, v68, v70\n" \
"v_smfmac_f32_16x16x64_bf16 v[84:87], v[68:71], a[152:159], v12\n" \
"ds_read_b128 v[32:35], v1 offset:49152\n" \
"v_smfmac_f32_16x16x64_bf16 v[88:91], v[68:71], v[176:183], v12\n" \
"ds_read_b128 v[36:39], v1 offset:50176\n" \
"v_smfmac_f32_16x16x64_bf16 v[92:95], v[68:71], v[184:191], v12\n" \
"ds_read_b128 v[68:71], v3 offset:13312\n" \
"s_waitcnt lgkmcnt(7)\n" \
"v_smfmac_f32_16x16x64_bf16 v[80:83], v[72:75], a[160:167], v12\n" \
"v_min3_u32 v13, v13, v72, v74\n" \
"v_smfmac_f32_16x16x64_bf16 v[84:87], v[72:75], a[168:175], v12\n" \
"ds_read_b128 v[40:43], v1 offset:51200\n" \
"v_smfmac_f32_16x16x64_bf16 v[88:91], v[72:75], v[48:55], v12\n" \
"ds_read_b128 v[44:47], v1 offset:52224\n" \
"s_waitcnt lgkmcnt(6)\n" \
"v_smfmac_f32_16x16x64_bf16 v[92:95], v[72:75], v[56:63], v12\n" \
"ds_read_b128 v[72:75], v3 offset:14336\n" \
"v_smfmac_f32_16x16x64_bf16 v[80:83], v[76:79], a[176:183], v12\n" \
"buffer_store_dwordx4 v[100:103], v31, s[36:39], s95 offen sc1\n" \
"v_min3_u32 v13, v13, v76, v78\n" \
"v_smfmac_f32_16x16x64_bf16 v[84:87], v[76:79], a[184:191], v12\n" \
"ds_read_b128 v[48:51], v3 offset:20480\n" \
"v_smfmac_f32_16x16x64_bf16 v[88:91], v[76:79], v[192:199], v12\n" \
"ds_read_b128 v[52:55], v3 offset:21504\n" \
"v_smfmac_f32_16x16x64_bf16 v[92:95], v[76:79], v[200:207], v12\n" \
"ds_read_b128 v[76:79], v3 offset:15360\n" \
"s_waitcnt lgkmcnt(9)\n" \
"v_smfmac_f32_16x16x64_bf16 v[80:83], v[64:67], a[192:199], v12\n" \
"s_mov_b32 s95, 0x7ffffff0\n" \
"v_min3_u32 v13, v13, v64, v66\n" \
"v_smfmac_f32_16x16x64_bf16 v[84:87], v[64:67], a[200:207], v12\n" \
"ds_read_b128 v[56:59], v3 offset:22528\n" \
"s_waitcnt lgkmcnt(8)\n" \
"v_smfmac_f32_16x16x64_bf16 v[88:91], v[64:67], v[32:39], v12\n" \
"ds_read_b128 v[60:63], v3 offset:23552\n" \
"s_waitcnt lgkmcnt(6)\n" \
"v_smfmac_f32_16x16x64_bf16 v[92:95], v[64:67], v[40:47], v12\n" \
"v_smfmac_f32_16x16x64_bf16 v[80:83], v[68:71], a[208:215], v12\n" \
"ds_read_b128 v[96:99], v14 offset:32768\n" \
"v_min3_u32 v13, v13, v68, v70\n" \
"v_smfmac_f32_16x16x64_bf16 v[84:87], v[68:71], a[216:223], v12\n" \
"ds_read_b128 v[32:35], v3 offset:16384\n" \
"v_smfmac_f32_16x16x64_bf16 v[88:91], v[68:71], v[208:215], v12\n" \
"ds_read_b128 v[36:39], v3 offset:17408\n" \
"v_smfmac_f32_16x16x64_bf16 v[92:95], v[68:71], v[216:223], v12\n" \
"s_waitcnt lgkmcnt(8)\n" \
"v_smfmac_f32_16x16x64_bf16 v[80:83], v[72:75], a[224:231], v12\n" \
"ds_read_b128 v[100:103], v14 offset:33792\n" \
"v_min3_u32 v13, v13, v72, v74\n" \
"v_smfmac_f32_16x16x64_bf16 v[84:87], v[72:75], a[232:239], v12\n" \
"ds_read_b128 v[40:43], v3 offset:18432\n" \
"s_waitcnt lgkmcnt(7)\n" \
"v_smfmac_f32_16x16x64_bf16 v[80:83], v[76:79], a[240:247], v12\n" \
"ds_read_b128 v[44:47], v3 offset:19456\n" \
"v_min3_u32 v13, v13, v76, v78\n" \
"v_smfmac_f32_16x16x64_bf16 v[84:87], v[76:79], a[248:255], v12\n" \
"v_cmp_eq_u32 vcc, 0, v13\n" \
"v_smfmac_f32_16x16x64_bf16 v[88:91], v[72:75], v[224:231], v12\n" \
"v_add_f32_e64 v80, v80, v82\n" \
"v_smfmac_f32_16x16x64_bf16 v[92:95], v[72:75], v[232:239], v12\n" \
"v_add_f32_e64 v81, v81, v83\n" \
"v_smfmac_f32_16x16x64_bf16 v[88:91], v[76:79], v[240:247], v12\n" \
"s_cbranch_vccnz .Lq_retry_%=\n" \
"v_add_f32 v84, v84, v86\n" \
"v_smfmac_f32_16x16x64_bf16 v[92:95], v[76:79], v[248:255], v12\n" \
"v_add_f32_e64 v85, v85, v87\n" \
"v_pk_mul_f32 v[80:81], v[80:81], s[98:99]\n" \
"v_pk_mul_f32 v[84:85], v[84:85], s[98:99]\n" \
"v_cvt_pk_bf16_f32 v80, v80, v81\n" \
"v_cvt_pk_bf16_f32 v84, v84, v85\n" \
"v_pk_add_f32 v[88:89], v[88:89], v[90:91]\n" \
"v_cvt_pk_bf16_f32 v81, v88, v89\n" \
"s_nop 0\n" \
"v_pk_add_f32 v[92:93], v[92:93], v[94:95]\n" \
"v_cvt_pk_bf16_f32 v85, v92, v93\n" \
"ds_write2_b64 v5, v[80:81], v[84:85] offset1:64\n" \
"buffer_load_dword v104, v8, s[36:39], s58 offen offset:0 sc1\n" \
"buffer_load_dword v106, v8, s[36:39], s58 offen offset:256 sc1\n" \
"s_cmp_eq_u32 s49, 63\n" \
"s_waitcnt lgkmcnt(0)\n" \
"s_barrier\n" \
"ds_read_b64_tr_b16 v[64:65], v6 offset:0\n" \
"ds_read_b64_tr_b16 v[66:67], v6 offset:512\n" \
"ds_read_b64_tr_b16 v[68:69], v6 offset:1024\n" \
"ds_read_b64_tr_b16 v[70:71], v6 offset:1536\n" \
"ds_read_b64_tr_b16 v[72:73], v6 offset:2048\n" \
"ds_read_b64_tr_b16 v[74:75], v6 offset:2560\n" \
"ds_read_b64_tr_b16 v[76:77], v6 offset:3072\n" \
"ds_read_b64_tr_b16 v[78:79], v6 offset:3584\n" \
"buffer_store_dword v30, v10, s[36:39], s87 offen sc1\n" \
"buffer_load_dword v30, v15, s[36:39], s80 offen sc1\n" \
"s_cbranch_scc1 .Lq_last_%=\n" \
"s_waitcnt lgkmcnt(6)\n" \
"v_mfma_f32_16x16x32_bf16 v[88:91], v[64:67], v[64:67], 0\n" \
"v_mfma_f32_16x16x32_bf16 v[80:83], v[32:35], v[64:67], 0\n" \
"s_waitcnt lgkmcnt(4)\n" \
"v_mfma_f32_16x16x32_bf16 v[88:91], v[68:71], v[68:71], v[88:91]\n" \
"v_mfma_f32_16x16x32_bf16 v[80:83], v[36:39], v[68:71], v[80:83]\n" \
"s_waitcnt lgkmcnt(2)\n" \
"v_mfma_f32_16x16x32_bf16 v[88:91], v[72:75], v[72:75], v[88:91]\n" \
"v_mfma_f32_16x16x32_bf16 v[80:83], v[40:43], v[72:75], v[80:83]\n" \
"s_waitcnt lgkmcnt(0)\n" \
"v_mfma_f32_16x16x32_bf16 v[88:91], v[76:79], v[76:79], v[88:91]\n" \
"v_mfma_f32_16x16x32_bf16 v[80:83], v[44:47], v[76:79], v[80:83]\n" \
"v_mfma_f32_16x16x32_bf16 v[84:87], v[48:51], v[64:67], 0\n" \
"v_mfma_f32_16x16x32_bf16 v[84:87], v[52:55], v[68:71], v[84:87]\n" \
"v_mfma_f32_16x16x32_bf16 v[84:87], v[56:59], v[72:75], v[84:87]\n" \
"v_max3_f32 v13, v90, v91, v80\n" \
"v_mfma_f32_16x16x32_bf16 v[84:87], v[60:63], v[76:79], v[84:87]\n" \
"v_max3_f32 v14, v81, v82, v83\n" \
"s_nop 6\n" \
"v_max3_f32 v14, v14, v84, v85\n" \
"v_max3_f32 v13, v13, v86, v87\n" \
"v_max_f32 v13, v13, v14\n" \
"v_max_f32 v14, v13, v14\n" \
"s_nop 1\n" \
"v_permlane16_swap_b32 v13, v14\n" \
"v_max_f32 v13, v13, v14\n" \
"v_max_f32 v14, v13, v14\n" \
"s_nop 1\n" \
"v_permlane32_swap_b32 v13, v14\n" \
"v_max_f32_e64 v14, v13, v14\n" \
"v_pk_add_f32 v[80:81], v[80:81], v[14:15] op_sel_hi:[1,0] neg_lo:[0,1] neg_hi:[0,1]\n" \
"v_pk_add_f32 v[82:83], v[82:83], v[14:15] op_sel_hi:[1,0] neg_lo:[0,1] neg_hi:[0,1]\n" \
"v_pk_add_f32 v[84:85], v[84:85], v[14:15] op_sel_hi:[1,0] neg_lo:[0,1] neg_hi:[0,1]\n" \
"v_pk_add_f32 v[86:87], v[86:87], v[14:15] op_sel_hi:[1,0] neg_lo:[0,1] neg_hi:[0,1]\n" \
"v_pk_add_f32 v[90:91], v[90:91], v[14:15] op_sel_hi:[1,0] neg_lo:[0,1] neg_hi:[0,1]\n" \
"v_exp_f32 v80, v80\n" \
"v_exp_f32 v81, v81\n" \
"v_exp_f32 v82, v82\n" \
"v_exp_f32 v83, v83\n" \
"v_exp_f32 v84, v84\n" \
"v_exp_f32 v85, v85\n" \
"v_exp_f32 v86, v86\n" \
"v_exp_f32 v87, v87\n" \
"v_cvt_pk_bf16_f32 v80, v80, v81\n" \
"v_cvt_pk_bf16_f32 v81, v82, v83\n" \
"v_cvt_pk_bf16_f32 v82, v84, v85\n" \
"v_cvt_pk_bf16_f32 v83, v86, v87\n" \
"v_exp_f32 v90, v90\n" \
"s_nop 0\n" \
"v_mfma_f32_16x16x32_bf16 v[84:87], v[96:99], v[80:83], 0\n" \
"v_exp_f32_e64 v91, v91\n" \
"v_mfma_f32_16x16x32_bf16 v[92:95], v[100:103], v[80:83], 0\n" \
"v_cvt_pk_bf16_f32 v26, v90, v91\n" \
"v_mfma_f32_16x16x32_bf16 v[16:19], v[20:23], v[80:83], 0\n" \
"s_nop 0\n" \
"v_mfma_f32_16x16x32_bf16 v[16:19], v[20:23], v[26:29], v[16:19]\n" \
"s_waitcnt vmcnt(2)\n" \
"v_mfma_f32_16x16x32_bf16 v[84:87], v[104:107], v[26:29], v[84:87]\n" \
"v_mfma_f32_16x16x32_bf16 v[92:95], v[104:107], v[24:27], v[92:95]\n" \
"v_min_u32 v13, v104, v106\n" \
"v_cmp_eq_u32_e64 vcc, 0, v13\n" \
"v_rcp_f32_e64 v14, v16\n" \
"s_nop 1\n" \
"s_cbranch_vccnz .Lq_vr_%=\n" \
".Lq_vr2_%=:\n" \
"v_pk_mul_f32 v[84:85], v[84:85], v[14:15] op_sel_hi:[1,0]\n" \
"v_pk_mul_f32 v[86:87], v[86:87], v[14:15] op_sel_hi:[1,0]\n" \
"v_cvt_pk_bf16_f32 v80, v84, v85\n" \
"v_cvt_pk_bf16_f32 v81, v86, v87\n" \
"v_max_u32 v80, 1, v80\n" \
"v_pk_mul_f32 v[92:93], v[92:93], v[14:15] op_sel_hi:[1,0]\n" \
"v_pk_mul_f32 v[94:95], v[94:95], v[14:15] op_sel_hi:[1,0]\n" \
"v_cvt_pk_bf16_f32 v82, v92, v93\n" \
"v_cvt_pk_bf16_f32 v83, v94, v95\n" \
"v_max_u32_e64 v82, 1, v82\n" \
"buffer_store_dwordx4 v[80:83], v7, s[36:39], s58 offen sc1\n" \
".Lq_tok_%=:\n" \
"s_memrealtime s[24:25]\n" \
"v_add_u32_e64 v14, s43, v3\n" \
"ds_read_b128 v[96:99], v14 offset:24576\n" \
"ds_read_b128 v[100:103], v14 offset:25600\n" \
"ds_read_b128 v[92:95], v14 offset:26624\n" \
"s_memrealtime s[26:27]\n" \
"ds_read_b128 v[16:19], v14 offset:27648\n" \
"v_xor_b32 v0, 512, v8\n" \
"v_mov_b32 v26, 0\n" \
"s_nop 1\n" \
"s_memrealtime s[28:29]\n" \
"s_nop 4\n" \
"s_memrealtime s[30:31]\n" \
"s_waitcnt lgkmcnt(0)\n" \
"v_mfma_f32_16x16x32_bf16 v[88:91], v[64:67], v[96:99], 0\n" \
"s_add_u32 s25, s24, 3\n" \
"s_cmp_eq_u32 s26, s24\n" \
"s_subb_u32 s25, s25, 0\n" \
"v_mfma_f32_16x16x32_bf16 v[80:83], v[32:35], v[96:99], 0\n" \
"s_cmp_eq_u32 s28, s24\n" \
"s_subb_u32 s25, s25, 0\n" \
"v_mfma_f32_16x16x32_bf16 v[88:91], v[68:71], v[100:103], v[88:91]\n" \
"s_cmp_eq_u32 s30, s24\n" \
"s_subb_u32 s25, s25, 0\n" \
"s_max_u32 s16, s25, 1\n" \
"v_mov_b32_e64 v13, s16\n" \
"v_mfma_f32_16x16x32_bf16 v[80:83], v[36:39], v[100:103], v[80:83]\n" \
"s_xor_b32 s53, s92, 0x4800\n" \
"buffer_store_dword v13, v10, s[36:39], s53 offen sc1\n" \
"v_mfma_f32_16x16x32_bf16 v[88:91], v[72:75], v[92:95], v[88:91]\n" \
"s_sub_u32 s24, s25, s51\n" \
"s_min_u32 s24, s24, 255\n" \
"v_mfma_f32_16x16x32_bf16 v[80:83], v[40:43], v[92:95], v[80:83]\n" \
"s_cmp_eq_u32 s23, s49\n" \
"s_cselect_b32 s24, s46, s24\n" \
"buffer_load_dword v108, v0, s[36:39], s58 offen offset:0 sc1\n" \
"v_mfma_f32_16x16x32_bf16 v[88:91], v[76:79], v[16:19], v[88:91]\n" \
"s_lshl_b64 s[32:33], s[32:33], 8\n" \
"s_or_b32 s32, s32, s24\n" \
"buffer_load_dword v110, v0, s[36:39], s58 offen offset:256 sc1\n" \
"v_mfma_f32_16x16x32_bf16 v[80:83], v[44:47], v[16:19], v[80:83]\n" \
"s_bfe_u32 s24, s32, 524312\n" \
"s_bfe_u32 s25, s33, 524312\n" \
"s_min_u32 s34, s24, s25\n" \
"v_add_u32_e64 v14, s62, v3\n" \
"v_mfma_f32_16x16x32_bf16 v[84:87], v[48:51], v[96:99], 0\n" \
"buffer_store_dwordx4 v[24:27], v7, s[36:39], s60 offen sc1\n" \
"v_xor_b32 v0, 0x800, v14\n" \
"v_mfma_f32_16x16x32_bf16 v[84:87], v[52:55], v[100:103], v[84:87]\n" \
"ds_read_b128 v[32:35], v14 offset:32768\n" \
"ds_read_b128 v[36:39], v14 offset:33792\n" \
"buffer_store_dword v24, v10, s[36:39], s93 offen sc1\n" \
"s_xor_b32 s53, s93, 0x4800\n" \
"v_mov_b32 v13, 0xceabdc6e\n" \
"v_mfma_f32_16x16x32_bf16 v[84:87], v[56:59], v[92:95], v[84:87]\n" \
"ds_read_b128 v[40:43], v0 offset:32768\n" \
"ds_read_b128 v[44:47], v0 offset:33792\n" \
"buffer_store_dword v24, v10, s[36:39], s53 offen sc1\n" \
"v_mfma_f32_16x16x32_bf16 v[84:87], v[60:63], v[16:19], v[84:87]\n" \
"s_cmp_lt_u32 s49, 63\n" \
"s_cselect_b32 s54, 24576, 0\n" \
"s_add_u32 s63, s63, s54\n" \
"v_cndmask_b32_e64 v80, v13, v80, s[64:65]\n" \
"v_cndmask_b32_e64 v81, v13, v81, s[66:67]\n" \
"v_cndmask_b32_e64 v82, v13, v82, s[68:69]\n" \
"v_cndmask_b32_e64 v83, v13, v83, s[70:71]\n" \
"s_waitcnt vmcnt(7)\n" \
"v_cmp_ge_u32_e64 s[96:97], s81, v30\n" \
"v_readlane_b32 s35, v30, 3\n" \
"v_readlane_b32 s53, v30, 4\n" \
"v_cndmask_b32_e64 v84, v13, v84, s[72:73]\n" \
"v_cndmask_b32_e64 v85, v13, v85, s[74:75]\n" \
"v_cndmask_b32_e64 v86, v13, v86, s[76:77]\n" \
"v_cndmask_b32_e64 v87, v13, v87, s[78:79]\n" \
"s_max_u32 s35, s35, s53\n" \
"s_cmp_eq_u32 s35, 0\n" \
"s_cselect_b32 s35, s17, s35\n" \
"s_and_b32 s96, s96, s9\n" \
"s_cbranch_scc1 .Lq_f_cold_%=\n" \
".Lq_f_ok_%=:\n" \
"s_waitcnt lgkmcnt(0)\n" \
"s_barrier\n" \
"s_lshr_b32 s11, s58, 1\n" \
"s_mov_b32 s54, s58\n" \
"s_mov_b32 s58, s59\n" \
"s_mov_b32 s59, s60\n" \
"s_mov_b32 s60, s61\n" \
"s_mov_b32 s61, s54\n" \
"buffer_load_dwordx2 v[64:65], v9, s[36:39], s11 offen sc1\n" \
"v_max3_f32 v13, v90, v91, v80\n" \
"v_max3_f32 v14, v81, v82, v83\n" \
"v_max3_f32 v14, v14, v84, v85\n" \
"v_max3_f32 v13, v13, v86, v87\n" \
"v_max_f32 v13, v13, v14\n" \
"v_max_f32 v14, v13, v14\n" \
"s_add_u32 m0, s62, 131072\n" \
"s_add_u32 s15, s63, 10371072\n" \
"buffer_load_dwordx4 v3, s[36:39], s15 offen offset:0 sc1 lds\n" \
"v_permlane16_swap_b32 v13, v14\n" \
"v_max_f32 v13, v13, v14\n" \
"v_max_f32_e64 v14, v13, v14\n" \
"buffer_load_dwordx4 v3, s[36:39], s15 offen offset:1024 sc1 lds\n" \
"s_nop 0\n" \
"v_permlane32_swap_b32 v13, v14\n" \
"v_max_f32_e64 v14, v13, v14\n" \
"v_pk_add_f32 v[80:81], v[80:81], v[14:15] op_sel_hi:[1,0] neg_lo:[0,1] neg_hi:[0,1]\n" \
"v_pk_add_f32 v[82:83], v[82:83], v[14:15] op_sel_hi:[1,0] neg_lo:[0,1] neg_hi:[0,1]\n" \
"v_pk_add_f32 v[84:85], v[84:85], v[14:15] op_sel_hi:[1,0] neg_lo:[0,1] neg_hi:[0,1]\n" \
"v_pk_add_f32 v[86:87], v[86:87], v[14:15] op_sel_hi:[1,0] neg_lo:[0,1] neg_hi:[0,1]\n" \
"v_pk_add_f32 v[90:91], v[90:91], v[14:15] op_sel_hi:[1,0] neg_lo:[0,1] neg_hi:[0,1]\n" \
"v_exp_f32_e64 v80, v80\n" \
"s_add_u32 m0, s62, 139264\n" \
"s_add_u32 s15, s63, 10379264\n" \
"buffer_load_dwordx4 v3, s[36:39], s15 offen offset:0 sc1 lds\n" \
"v_exp_f32 v81, v81\n" \
"v_exp_f32 v82, v82\n" \
"v_exp_f32_e64 v83, v83\n" \
"buffer_load_dwordx4 v3, s[36:39], s15 offen offset:1024 sc1 lds\n" \
"v_exp_f32 v84, v84\n" \
"v_exp_f32 v85, v85\n" \
"v_exp_f32_e64 v86, v86\n" \
"s_add_u32 m0, s62, 147456\n" \
"s_add_u32 s15, s63, 10387456\n" \
"buffer_load_dwordx4 v3, s[36:39], s15 offen offset:0 sc1 lds\n" \
"v_exp_f32_e64 v87, v87\n" \
"v_cvt_pk_bf16_f32 v80, v80, v81\n" \
"v_cvt_pk_bf16_f32 v81, v82, v83\n" \
"v_cvt_pk_bf16_f32 v82, v84, v85\n" \
"v_cvt_pk_bf16_f32 v83, v86, v87\n" \
"buffer_load_dwordx4 v3, s[36:39], s15 offen offset:1024 sc1 lds\n" \
"v_exp_f32_e64 v90, v90\n" \
"v_mfma_f32_16x16x32_bf16 v[48:51], v[32:35], v[80:83], 0\n" \
"v_exp_f32_e64 v91, v91\n" \
"v_mfma_f32_16x16x32_bf16 v[52:55], v[36:39], v[80:83], 0\n" \
"v_cvt_pk_bf16_f32 v26, v90, v91\n" \
"v_mfma_f32_16x16x32_bf16 v[56:59], v[40:43], v[80:83], 0\n" \
"v_mfma_f32_16x16x32_bf16 v[60:63], v[44:47], v[80:83], 0\n" \
"v_mfma_f32_16x16x32_bf16 v[92:95], v[20:23], v[80:83], 0\n" \
"s_waitcnt vmcnt(11)\n" \
"v_min3_u32 v13, v104, v106, v108\n" \
"s_waitcnt vmcnt(10)\n" \
"v_min3_u32 v13, v13, v110, v110\n" \
"v_cmp_eq_u32 vcc, 0, v13\n" \
"s_nop 0\n" \
"s_cbranch_vccnz .Lq_v2_cold_%=\n" \
".Lq_v2_ok_%=:\n" \
"v_mfma_f32_16x16x32_bf16 v[92:95], v[20:23], v[26:29], v[92:95]\n" \
"v_mfma_f32_16x16x32_bf16 v[48:51], v[104:107], v[26:29], v[48:51]\n" \
"ds_read_b128 v[32:35], v1 offset:0\n" \
"s_lshl_b32 s95, s49, 13\n" \
"s_add_u32 s49, s49, 1\n" \
"v_mfma_f32_16x16x32_bf16 v[52:55], v[104:107], v[24:27], v[52:55]\n" \
"ds_read_b128 v[36:39], v1 offset:1024\n" \
"s_lshr_b32 s53, s58, 1\n" \
"s_add_u32 s92, s53, s94\n" \
"v_mfma_f32_16x16x32_bf16 v[56:59], v[108:111], v[26:29], v[56:59]\n" \
"ds_read_b128 v[40:43], v1 offset:2048\n" \
"s_add_u32 s55, s53, s41\n" \
"s_xor_b32 s89, s55, 0x4800\n" \
"v_mfma_f32_16x16x32_bf16 v[60:63], v[108:111], v[24:27], v[60:63]\n" \
"ds_read_b128 v[44:47], v1 offset:3072\n" \
"s_xor_b32 s90, s55, 0x800\n" \
"s_xor_b32 s91, s55, 0x4000\n" \
"s_xor_b32 s93, s92, 0x20000\n" \
"s_add_u32 s81, s49, 1\n" \
"s_min_u32 s81, s81, 63\n" \
"v_rcp_f32 v14, v92\n" \
"s_nop 0\n" \
"v_pk_mul_f32 v[48:49], v[48:49], v[14:15] op_sel_hi:[1,0]\n" \
"v_pk_mul_f32 v[50:51], v[50:51], v[14:15] op_sel_hi:[1,0]\n" \
"v_pk_mul_f32 v[52:53], v[52:53], v[14:15] op_sel_hi:[1,0]\n" \
"v_pk_mul_f32 v[54:55], v[54:55], v[14:15] op_sel_hi:[1,0]\n" \
"v_cvt_pk_bf16_f32 v96, v48, v49\n" \
"v_cvt_pk_bf16_f32 v97, v50, v51\n" \
"v_cvt_pk_bf16_f32 v98, v52, v53\n" \
"v_cvt_pk_bf16_f32 v99, v54, v55\n" \
"ds_read_b128 v[48:51], v2 offset:32768\n" \
"ds_read_b128 v[52:55], v2 offset:33792\n" \
"v_pk_mul_f32 v[56:57], v[56:57], v[14:15] op_sel_hi:[1,0]\n" \
"v_pk_mul_f32 v[58:59], v[58:59], v[14:15] op_sel_hi:[1,0]\n" \
"v_pk_mul_f32 v[60:61], v[60:61], v[14:15] op_sel_hi:[1,0]\n" \
"v_pk_mul_f32 v[62:63], v[62:63], v[14:15] op_sel_hi:[1,0]\n" \
"v_cvt_pk_bf16_f32 v100, v56, v57\n" \
"v_cvt_pk_bf16_f32 v101, v58, v59\n" \
"v_cvt_pk_bf16_f32 v102, v60, v61\n" \
"v_cvt_pk_bf16_f32 v103, v62, v63\n" \
"ds_read_b128 v[56:59], v2 offset:34816\n" \
"ds_read_b128 v[60:63], v2 offset:35840\n" \
"s_cmp_eq_u32 s49, 64\n" \
"s_cbranch_scc1 .Lq_end_%=\n" \
"v_mov_b64 v[80:81], 0\n" \
"v_mov_b64 v[82:83], 0\n" \
"v_mov_b64 v[84:85], 0\n" \
"v_mov_b64 v[86:87], 0\n" \
"v_mov_b64 v[88:89], 0\n" \
"v_mov_b64 v[90:91], 0\n" \
"v_mov_b64 v[92:93], 0\n" \
"v_mov_b64 v[94:95], 0\n" \
"v_mov_b32 v13, -1\n" \
"v_add_u32 v14, s62, v3\n" \
"v_xor_b32 v31, 0x400, v11\n" \
"s_max_u32 s53, s49, 1\n" \
"s_sub_u32 s53, s53, 1\n" \
"v_mov_b32_e64 v30, s53\n" \
"s_mov_b32 m0, s42\n" \
"s_waitcnt lgkmcnt(0)\n" \
"s_mov_b32 s54, 0\n" \
"s_cmp_lg_u32 s48, 0\n" \
"s_nop 0\n" \
"s_nop 0\n" \
"s_cbranch_scc1 .Lq_w4_%=\n" \
"s_waitcnt vmcnt(6)\n" \
"v_cmp_eq_u32_e64 vcc, 0, v64\n" \
"s_nop 0\n" \
"s_and_b64 vcc, vcc, s[82:83]\n" \
"s_nop 0\n" \
"s_nop 0\n" \
"s_cbranch_vccnz .Lq_h_cold_%=\n" \
".Lq_h_ok_%=:\n" \
"s_add_u32 s53, s16, 20\n" \
"v_mov_b32 v0, s53\n" \
"v_cmp_eq_u32 vcc, 0, v64\n" \
"s_nop 1\n" \
"v_cndmask_b32 v64, v64, v0, vcc\n" \
"v_subrev_u32_e64 v64, s51, v64\n" \
"v_max_i32_dpp v65, v65, v65 row_ror:8 row_mask:0xf bank_mask:0xf\n" \
"s_nop 0\n" \
"v_max_i32_dpp v64, v64, v64 row_ror:8 row_mask:0xf bank_mask:0xf\n" \
"v_max_i32_dpp v65, v65, v65 row_ror:4 row_mask:0xf bank_mask:0xf\n" \
"s_nop 0\n" \
"v_max_i32_dpp v64, v64, v64 row_ror:4 row_mask:0xf bank_mask:0xf\n" \
"v_max_i32_dpp v65, v65, v65 row_ror:2 row_mask:0xf bank_mask:0xf\n" \
"s_nop 0\n" \
"v_max_i32_dpp v64, v64, v64 row_ror:2 row_mask:0xf bank_mask:0xf\n" \
"v_max_i32_dpp v65, v65, v65 row_ror:1 row_mask:0xf bank_mask:0xf\n" \
"s_nop 0\n" \
"v_max_i32_dpp v64, v64, v64 row_ror:1 row_mask:0xf bank_mask:0xf\n" \
"v_readlane_b32 s53, v65, 0\n" \
"v_readlane_b32 s50, v65, 16\n" \
"v_readlane_b32 s54, v64, 0\n" \
"v_readlane_b32 s40, v64, 16\n" \
"s_max_i32 s50, s50, s53\n" \
"s_max_i32 s40, s40, s54\n" \
"s_add_u32 s40, s40, s51\n" \
"s_add_u32 s40, s40, s44\n" \
"s_add_u32 s50, s50, s40\n" \
"s_add_u32 s50, s50, s45\n" \
"v_readlane_b32 s53, v64, 32\n" \
"v_readlane_b32 s54, v64, 48\n" \
"s_nop 0\n" \
"s_max_i32 s53, s53, s54\n" \
"s_add_u32 s53, s53, s51\n" \
"s_sub_u32 s53, s53, s40\n" \
"s_add_u32 s53, s53, s12\n" \
"s_add_u32 s54, s53, s13\n" \
"s_cmp_gt_i32 s53, 0\n" \
"s_cselect_b32 s54, s54, 0\n" \
"s_add_u32 s50, s50, s54\n" \
"s_branch .Lq_loop_%=\n" \
".Lq_f0_cold_%=:\n" \
"s_nop 7\n" \
"s_nop 7\n" \
"s_waitcnt vmcnt(0) lgkmcnt(0)\n" \
"s_mov_b32 s53, 0\n" \
".Lq_f0_again_%=:\n" \
"s_cmp_lg_u32 s52, 0\n" \
"s_cbranch_scc1 .Lq_f0_ok_%=\n" \
"s_sleep 4\n" \
"buffer_load_dword v30, v15, s[36:39], s80 offen sc1\n" \
"s_waitcnt vmcnt(0)\n" \
"v_cmp_ge_u32_e64 s[96:97], s81, v30\n" \
"s_nop 4\n" \
"s_and_b32 s96, s96, s9\n" \
"s_cbranch_scc0 .Lq_f0_ok_%=\n" \
"s_add_u32 s53, s53, 1\n" \
"s_cmp_lt_u32 s53, 32768\n" \
"s_cbranch_scc1 .Lq_f0_again_%=\n" \
"s_mov_b32 s52, 1\n" \
"v_mov_b32 v30, 256\n" \
"v_mov_b32 v31, 1\n" \
"buffer_store_dword v31, v30, s[36:39], 0 offen sc1\n" \
"s_waitcnt vmcnt(0) lgkmcnt(0)\n" \
"s_branch .Lq_f0_ok_%=\n" \
".Lq_vr_%=:\n" \
"s_nop 7\n" \
"s_nop 7\n" \
"s_waitcnt vmcnt(0) lgkmcnt(0)\n" \
"buffer_load_dword v104, v8, s[36:39], s58 offen offset:0 sc1\n" \
"buffer_load_dword v106, v8, s[36:39], s58 offen offset:256 sc1\n" \
"s_waitcnt vmcnt(0)\n" \
"v_min3_u32 v13, v104, v106, v106\n" \
"v_cmp_eq_u32 vcc, 0, v13\n" \
"s_nop 0\n" \
"s_cbranch_vccnz .Lq_v_cold_%=\n" \
".Lq_v_ok_%=:\n" \
"s_mov_b32 s23, s49\n" \
"v_mfma_f32_16x16x32_bf16 v[84:87], v[96:99], v[80:83], 0\n" \
"v_mfma_f32_16x16x32_bf16 v[92:95], v[100:103], v[80:83], 0\n" \
"v_mfma_f32_16x16x32_bf16 v[84:87], v[104:107], v[26:29], v[84:87]\n" \
"v_mfma_f32_16x16x32_bf16 v[92:95], v[104:107], v[24:27], v[92:95]\n" \
"s_nop 7\n" \
"s_nop 7\n" \
"s_branch .Lq_vr2_%=\n" \
".Lq_v_cold_%=:\n" \
"s_nop 7\n" \
"s_nop 7\n" \
"s_waitcnt vmcnt(0) lgkmcnt(0)\n" \
"s_mov_b32 s53, 0\n" \
".Lq_v_again_%=:\n" \
"s_cmp_lg_u32 s52, 0\n" \
"s_cbranch_scc1 .Lq_v_ok_%=\n" \
"s_sleep 1\n" \
"buffer_load_dword v104, v8, s[36:39], s58 offen offset:0 sc1\n" \
"buffer_load_dword v106, v8, s[36:39], s58 offen offset:256 sc1\n" \
"s_waitcnt vmcnt(0)\n" \
"v_min3_u32 v13, v104, v106, v106\n" \
"v_cmp_eq_u32 vcc, 0, v13\n" \
"s_nop 4\n" \
"s_cbranch_vccz .Lq_v_ok_%=\n" \
"s_add_u32 s53, s53, 1\n" \
"s_cmp_lt_u32 s53, 32768\n" \
"s_cbranch_scc1 .Lq_v_again_%=\n" \
"s_mov_b32 s52, 1\n" \
"v_mov_b32 v13, 256\n" \
"v_mov_b32 v14, 1\n" \
"buffer_store_dword v14, v13, s[36:39], 0 offen sc1\n" \
"s_waitcnt vmcnt(0) lgkmcnt(0)\n" \
"s_branch .Lq_v_ok_%=\n" \
".Lq_f_cold_%=:\n" \
"s_nop 7\n" \
"s_nop 7\n" \
"s_waitcnt vmcnt(0) lgkmcnt(0)\n" \
"s_mov_b32 s53, 0\n" \
".Lq_f_again_%=:\n" \
"s_cmp_lg_u32 s52, 0\n" \
"s_cbranch_scc1 .Lq_f_ok_%=\n" \
"s_sleep 4\n" \
"buffer_load_dword v30, v15, s[36:39], s80 offen sc1\n" \
"s_waitcnt vmcnt(0)\n" \
"v_cmp_ge_u32_e64 s[96:97], s81, v30\n" \
"s_nop 4\n" \
"s_and_b32 s96, s96, s9\n" \
"s_cbranch_scc0 .Lq_f_ok_%=\n" \
"s_add_u32 s53, s53, 1\n" \
"s_cmp_lt_u32 s53, 32768\n" \
"s_cbranch_scc1 .Lq_f_again_%=\n" \
"s_mov_b32 s52, 1\n" \
"v_mov_b32 v30, 256\n" \
"v_mov_b32 v31, 1\n" \
"buffer_store_dword v31, v30, s[36:39], 0 offen sc1\n" \
"s_waitcnt vmcnt(0) lgkmcnt(0)\n" \
"s_branch .Lq_f_ok_%=\n" \
".Lq_v2_cold_%=:\n" \
"s_nop 7\n" \
"s_nop 7\n" \
"s_waitcnt vmcnt(0) lgkmcnt(0)\n" \
"s_mov_b32 s53, 0\n" \
".Lq_v2_again_%=:\n" \
"s_cmp_lg_u32 s52, 0\n" \
"s_cbranch_scc1 .Lq_v2_ok_%=\n" \
"s_sleep 1\n" \
"v_xor_b32 v31, 512, v8\n" \
"buffer_load_dword v104, v8, s[36:39], s61 offen offset:0 sc1\n" \
"buffer_load_dword v106, v8, s[36:39], s61 offen offset:256 sc1\n" \
"buffer_load_dword v108, v31, s[36:39], s61 offen offset:0 sc1\n" \
"buffer_load_dword v110, v31, s[36:39], s61 offen offset:256 sc1\n" \
"s_waitcnt vmcnt(1)\n" \
"v_min3_u32 v13, v104, v106, v108\n" \
"s_waitcnt vmcnt(0)\n" \
"v_min3_u32 v13, v13, v110, v110\n" \
"v_cmp_eq_u32 vcc, 0, v13\n" \
"s_nop 4\n" \
"s_cbranch_vccz .Lq_v2_ok_%=\n" \
"s_add_u32 s53, s53, 1\n" \
"s_cmp_lt_u32 s53, 32768\n" \
"s_cbranch_scc1 .Lq_v2_again_%=\n" \
"s_mov_b32 s52, 1\n" \
"v_mov_b32 v13, 256\n" \
"v_mov_b32 v14, 1\n" \
"buffer_store_dword v14, v13, s[36:39], 0 offen sc1\n" \
"s_waitcnt vmcnt(0) lgkmcnt(0)\n" \
"s_branch .Lq_v2_ok_%=\n" \
".Lq_h_cold_%=:\n" \
"s_nop 7\n" \
"s_nop 7\n" \
"s_waitcnt vmcnt(0) lgkmcnt(0)\n" \
"s_mov_b32 s53, 0\n" \
".Lq_h_again_%=:\n" \
"s_cmp_lg_u32 s52, 0\n" \
"s_cbranch_scc1 .Lq_h_ok_%=\n" \
"s_sleep 1\n" \
"buffer_load_dwordx2 v[64:65], v9, s[36:39], s11 offen sc1\n" \
"s_waitcnt vmcnt(0)\n" \
"v_cmp_eq_u32 vcc, 0, v64\n" \
"s_nop 0\n" \
"s_and_b64 vcc, vcc, s[82:83]\n" \
"s_nop 4\n" \
"s_cbranch_vccz .Lq_h_ok_%=\n" \
"s_add_u32 s53, s53, 1\n" \
"s_cmp_lt_u32 s53, 32768\n" \
"s_cbranch_scc1 .Lq_h_again_%=\n" \
"s_mov_b32 s52, 1\n" \
"v_mov_b32 v18, 256\n" \
"v_mov_b32 v19, 1\n" \
"buffer_store_dword v19, v18, s[36:39], 0 offen sc1\n" \
"s_waitcnt vmcnt(0) lgkmcnt(0)\n" \
"s_branch .Lq_h_ok_%=\n" \
".Lq_retry_%=:\n" \
"s_nop 7\n" \
"s_nop 7\n" \
"s_waitcnt vmcnt(0) lgkmcnt(0)\n" \
"ds_read_b128 v[32:35], v1 offset:0\n" \
"ds_read_b128 v[36:39], v1 offset:1024\n" \
"ds_read_b128 v[40:43], v1 offset:2048\n" \
"ds_read_b128 v[44:47], v1 offset:3072\n" \
"ds_read_b128 v[48:51], v2 offset:32768\n" \
"ds_read_b128 v[52:55], v2 offset:33792\n" \
"ds_read_b128 v[56:59], v2 offset:34816\n" \
"ds_read_b128 v[60:63], v2 offset:35840\n" \
"v_mov_b64 v[80:81], 0\n" \
"v_mov_b64 v[82:83], 0\n" \
"v_mov_b64 v[84:85], 0\n" \
"v_mov_b64 v[86:87], 0\n" \
"v_mov_b64 v[88:89], 0\n" \
"v_mov_b64 v[90:91], 0\n" \
"v_mov_b64 v[92:93], 0\n" \
"v_mov_b64 v[94:95], 0\n" \
"v_mov_b32 v13, -1\n" \
"v_add_u32 v14, s62, v3\n" \
"v_xor_b32 v31, 0x400, v11\n" \
"s_max_u32 s53, s49, 1\n" \
"s_sub_u32 s53, s53, 1\n" \
"v_mov_b32 v30, s53\n" \
"s_mov_b32 m0, s42\n" \
"s_memrealtime s[56:57]\n" \
"s_mov_b32 s54, 100\n" \
"s_waitcnt vmcnt(0) lgkmcnt(0)\n" \
"s_barrier\n" \
"s_add_u32 s88, s88, 1\n" \
"s_cmp_lt_u32 s88, 32768\n" \
"s_cbranch_scc1 .Lq_r1_%=\n" \
"s_mov_b32 s52, 1\n" \
"v_mov_b32 v13, 256\n" \
"v_mov_b32 v14, 1\n" \
"buffer_store_dword v14, v13, s[36:39], 0 offen sc1\n" \
"s_waitcnt vmcnt(0) lgkmcnt(0)\n" \
"s_endpgm\n" \
".Lq_r1_%=:\n" \
"s_cmp_lg_u32 s49, 0\n" \
"s_cbranch_scc1 .Lq_r2_%=\n" \
"s_sleep 8\n" \
".Lq_r2_%=:\n" \
"s_branch .Lq_go_%=\n" \
".Lq_last_%=:\n" \
"s_waitcnt vmcnt(0) lgkmcnt(0)\n" \
"s_nop 7\n" \
"s_nop 7\n" \
"s_branch .Lq_tok_%=\n" \
".Lq_end_%=:\n" \
"s_nop 7\n" \
"s_nop 7\n" \
"v_xor_b32 v31, 0x400, v11\n" \
"buffer_store_dwordx4 v[96:99], v11, s[36:39], s95 offen sc1\n" \
"buffer_store_dwordx4 v[100:103], v31, s[36:39], s95 offen sc1\n" \
"s_waitcnt vmcnt(0) lgkmcnt(0)\n" \
"s_barrier\n" \
"v_mov_b32 v31, 64\n" \
"buffer_store_dword v31, v10, s[36:39], s87 offen sc1\n" \
"s_waitcnt vmcnt(0) lgkmcnt(0)\n" \
"s_endpgm\n"
#define ASM_VO \
"s_nop 7\n" \
"s_nop 7\n" \
"buffer_load_dword v20, v13, s[68:71], 0 offen offset:0\n" \
"buffer_load_dword v21, v13, s[68:71], 0 offen offset:8\n" \
"buffer_load_dword v22, v13, s[68:71], 0 offen offset:16\n" \
"buffer_load_dword v23, v13, s[68:71], 0 offen offset:24\n" \
"v_mov_b32 v16, 0\n" \
"v_mov_b32 v17, 0\n" \
"v_mov_b32 v18, 0\n" \
"v_mov_b32 v19, 0\n" \
"s_mov_b32 s54, 0\n" \
"buffer_store_dword v16, v7, s[36:39], s54 offen sc1\n" \
"s_mov_b32 s54, 131072\n" \
"buffer_store_dword v16, v7, s[36:39], s54 offen sc1\n" \
"s_mov_b32 s54, 65536\n" \
"buffer_store_dwordx4 v[16:19], v8, s[36:39], s54 offen sc1\n" \
"s_mov_b32 s54, 262144\n" \
"buffer_store_dword v16, v7, s[36:39], s54 offen sc1\n" \
"s_mov_b32 s54, 131072\n" \
"buffer_store_dwordx4 v[16:19], v8, s[36:39], s54 offen sc1\n" \
"s_mov_b32 s54, 393216\n" \
"buffer_store_dword v16, v7, s[36:39], s54 offen sc1\n" \
"s_mov_b32 s54, 196608\n" \
"buffer_store_dwordx4 v[16:19], v8, s[36:39], s54 offen sc1\n" \
"s_mul_i32 s62, s48, 40960\n" \
"v_add_u32 v32, 40960, v3\n" \
"v_add_u32 v33, 81920, v3\n" \
"v_add_u32 v34, 122880, v3\n" \
"s_add_u32 m0, s62, 0\n" \
"s_mov_b32 s54, 0\n" \
"buffer_load_dwordx4 v1, s[40:43], s54 offen nt lds\n" \
"s_add_u32 m0, s62, 1024\n" \
"s_mov_b32 s54, 65536\n" \
"buffer_load_dwordx4 v1, s[40:43], s54 offen nt lds\n" \
"s_add_u32 m0, s62, 2048\n" \
"s_mov_b32 s54, 131072\n" \
"buffer_load_dwordx4 v1, s[40:43], s54 offen nt lds\n" \
"s_add_u32 m0, s62, 3072\n" \
"s_mov_b32 s54, 196608\n" \
"buffer_load_dwordx4 v1, s[40:43], s54 offen nt lds\n" \
"s_add_u32 m0, s62, 4096\n" \
"s_mov_b32 s54, 262144\n" \
"buffer_load_dwordx4 v1, s[40:43], s54 offen nt lds\n" \
"s_add_u32 m0, s62, 5120\n" \
"s_mov_b32 s54, 327680\n" \
"buffer_load_dwordx4 v1, s[40:43], s54 offen nt lds\n" \
"s_add_u32 m0, s62, 6144\n" \
"s_mov_b32 s54, 393216\n" \
"buffer_load_dwordx4 v1, s[40:43], s54 offen nt lds\n" \
"s_add_u32 m0, s62, 7168\n" \
"s_mov_b32 s54, 458752\n" \
"buffer_load_dwordx4 v1, s[40:43], s54 offen nt lds\n" \
"s_add_u32 m0, s62, 8192\n" \
"s_mov_b32 s54, 524288\n" \
"buffer_load_dwordx4 v1, s[40:43], s54 offen nt lds\n" \
"s_add_u32 m0, s62, 9216\n" \
"s_mov_b32 s54, 589824\n" \
"buffer_load_dwordx4 v1, s[40:43], s54 offen nt lds\n" \
"s_add_u32 m0, s62, 10240\n" \
"s_mov_b32 s54, 655360\n" \
"buffer_load_dwordx4 v1, s[40:43], s54 offen nt lds\n" \
"s_add_u32 m0, s62, 11264\n" \
"s_mov_b32 s54, 720896\n" \
"buffer_load_dwordx4 v1, s[40:43], s54 offen nt lds\n" \
"s_add_u32 m0, s62, 12288\n" \
"s_mov_b32 s54, 786432\n" \
"buffer_load_dwordx4 v1, s[40:43], s54 offen nt lds\n" \
"s_add_u32 m0, s62, 13312\n" \
"s_mov_b32 s54, 851968\n" \
"buffer_load_dwordx4 v1, s[40:43], s54 offen nt lds\n" \
"s_add_u32 m0, s62, 14336\n" \
"s_mov_b32 s54, 917504\n" \
"buffer_load_dwordx4 v1, s[40:43], s54 offen nt lds\n" \
"s_add_u32 m0, s62, 15360\n" \
"s_mov_b32 s54, 983040\n" \
"buffer_load_dwordx4 v1, s[40:43], s54 offen nt lds\n" \
"s_add_u32 m0, s62, 16384\n" \
"s_mov_b32 s54, 1048576\n" \
"buffer_load_dwordx4 v1, s[40:43], s54 offen nt lds\n" \
"s_add_u32 m0, s62, 17408\n" \
"s_mov_b32 s54, 1114112\n" \
"buffer_load_dwordx4 v1, s[40:43], s54 offen nt lds\n" \
"s_add_u32 m0, s62, 18432\n" \
"s_mov_b32 s54, 1179648\n" \
"buffer_load_dwordx4 v1, s[40:43], s54 offen nt lds\n" \
"s_add_u32 m0, s62, 19456\n" \
"s_mov_b32 s54, 1245184\n" \
"buffer_load_dwordx4 v1, s[40:43], s54 offen nt lds\n" \
"s_add_u32 m0, s62, 20480\n" \
"s_mov_b32 s54, 1310720\n" \
"buffer_load_dwordx4 v1, s[40:43], s54 offen nt lds\n" \
"s_add_u32 m0, s62, 21504\n" \
"s_mov_b32 s54, 1376256\n" \
"buffer_load_dwordx4 v1, s[40:43], s54 offen nt lds\n" \
"s_add_u32 m0, s62, 22528\n" \
"s_mov_b32 s54, 1441792\n" \
"buffer_load_dwordx4 v1, s[40:43], s54 offen nt lds\n" \
"s_add_u32 m0, s62, 23552\n" \
"s_mov_b32 s54, 1507328\n" \
"buffer_load_dwordx4 v1, s[40:43], s54 offen nt lds\n" \
"s_add_u32 m0, s62, 24576\n" \
"s_mov_b32 s54, 1572864\n" \
"buffer_load_dwordx4 v1, s[40:43], s54 offen nt lds\n" \
"s_add_u32 m0, s62, 25600\n" \
"s_mov_b32 s54, 1638400\n" \
"buffer_load_dwordx4 v1, s[40:43], s54 offen nt lds\n" \
"s_add_u32 m0, s62, 26624\n" \
"s_mov_b32 s54, 1703936\n" \
"buffer_load_dwordx4 v1, s[40:43], s54 offen nt lds\n" \
"s_add_u32 m0, s62, 27648\n" \
"s_mov_b32 s54, 1769472\n" \
"buffer_load_dwordx4 v1, s[40:43], s54 offen nt lds\n" \
"s_add_u32 m0, s62, 28672\n" \
"s_mov_b32 s54, 1835008\n" \
"buffer_load_dwordx4 v1, s[40:43], s54 offen nt lds\n" \
"s_add_u32 m0, s62, 29696\n" \
"s_mov_b32 s54, 1900544\n" \
"buffer_load_dwordx4 v1, s[40:43], s54 offen nt lds\n" \
"s_add_u32 m0, s62, 30720\n" \
"s_mov_b32 s54, 1966080\n" \
"buffer_load_dwordx4 v1, s[40:43], s54 offen nt lds\n" \
"s_add_u32 m0, s62, 31744\n" \
"s_mov_b32 s54, 2031616\n" \
"buffer_load_dwordx4 v1, s[40:43], s54 offen nt lds\n" \
"s_add_u32 m0, s62, 32768\n" \
"s_mov_b32 s54, 0\n" \
"buffer_load_dwordx4 v2, s[64:67], s54 offen lds\n" \
"s_add_u32 m0, s62, 33792\n" \
"s_mov_b32 s54, 32768\n" \
"buffer_load_dwordx4 v2, s[64:67], s54 offen lds\n" \
"s_add_u32 m0, s62, 34816\n" \
"s_mov_b32 s54, 65536\n" \
"buffer_load_dwordx4 v2, s[64:67], s54 offen lds\n" \
"s_add_u32 m0, s62, 35840\n" \
"s_mov_b32 s54, 98304\n" \
"buffer_load_dwordx4 v2, s[64:67], s54 offen lds\n" \
"s_add_u32 m0, s62, 36864\n" \
"s_mov_b32 s54, 131072\n" \
"buffer_load_dwordx4 v2, s[64:67], s54 offen lds\n" \
"s_add_u32 m0, s62, 37888\n" \
"s_mov_b32 s54, 163840\n" \
"buffer_load_dwordx4 v2, s[64:67], s54 offen lds\n" \
"s_add_u32 m0, s62, 38912\n" \
"s_mov_b32 s54, 196608\n" \
"buffer_load_dwordx4 v2, s[64:67], s54 offen lds\n" \
"s_add_u32 m0, s62, 39936\n" \
"s_mov_b32 s54, 229376\n" \
"buffer_load_dwordx4 v2, s[64:67], s54 offen lds\n" \
"s_waitcnt vmcnt(32)\n" \
"s_barrier\n" \
"ds_read_b64_tr_b16 v[128:129], v3 offset:0\n" \
"ds_read_b64_tr_b16 v[130:131], v3 offset:512\n" \
"ds_read_b64_tr_b16 v[132:133], v3 offset:4096\n" \
"ds_read_b64_tr_b16 v[134:135], v3 offset:4608\n" \
"ds_read_b64_tr_b16 v[160:161], v32 offset:0\n" \
"ds_read_b64_tr_b16 v[162:163], v32 offset:512\n" \
"ds_read_b64_tr_b16 v[164:165], v32 offset:4096\n" \
"ds_read_b64_tr_b16 v[166:167], v32 offset:4608\n" \
"ds_read_b64_tr_b16 v[192:193], v33 offset:0\n" \
"ds_read_b64_tr_b16 v[194:195], v33 offset:512\n" \
"ds_read_b64_tr_b16 v[196:197], v33 offset:4096\n" \
"ds_read_b64_tr_b16 v[198:199], v33 offset:4608\n" \
"ds_read_b64_tr_b16 v[224:225], v34 offset:0\n" \
"ds_read_b64_tr_b16 v[226:227], v34 offset:512\n" \
"ds_read_b64_tr_b16 v[228:229], v34 offset:4096\n" \
"ds_read_b64_tr_b16 v[230:231], v34 offset:4608\n" \
"s_waitcnt lgkmcnt(0)\n" \
"s_barrier\n" \
"s_add_u32 m0, s62, 0\n" \
"s_mov_b32 s54, 262144\n" \
"buffer_load_dwordx4 v2, s[64:67], s54 offen lds\n" \
"s_add_u32 m0, s62, 1024\n" \
"s_mov_b32 s54, 294912\n" \
"buffer_load_dwordx4 v2, s[64:67], s54 offen lds\n" \
"s_add_u32 m0, s62, 2048\n" \
"s_mov_b32 s54, 327680\n" \
"buffer_load_dwordx4 v2, s[64:67], s54 offen lds\n" \
"s_add_u32 m0, s62, 3072\n" \
"s_mov_b32 s54, 360448\n" \
"buffer_load_dwordx4 v2, s[64:67], s54 offen lds\n" \
"s_add_u32 m0, s62, 4096\n" \
"s_mov_b32 s54, 393216\n" \
"buffer_load_dwordx4 v2, s[64:67], s54 offen lds\n" \
"s_add_u32 m0, s62, 5120\n" \
"s_mov_b32 s54, 425984\n" \
"buffer_load_dwordx4 v2, s[64:67], s54 offen lds\n" \
"s_add_u32 m0, s62, 6144\n" \
"s_mov_b32 s54, 458752\n" \
"buffer_load_dwordx4 v2, s[64:67], s54 offen lds\n" \
"s_add_u32 m0, s62, 7168\n" \
"s_mov_b32 s54, 491520\n" \
"buffer_load_dwordx4 v2, s[64:67], s54 offen lds\n" \
"s_waitcnt vmcnt(32)\n" \
"s_barrier\n" \
"ds_read_b64_tr_b16 v[136:137], v3 offset:8192\n" \
"ds_read_b64_tr_b16 v[138:139], v3 offset:8704\n" \
"ds_read_b64_tr_b16 v[140:141], v3 offset:12288\n" \
"ds_read_b64_tr_b16 v[142:143], v3 offset:12800\n" \
"ds_read_b64_tr_b16 v[168:169], v32 offset:8192\n" \
"ds_read_b64_tr_b16 v[170:171], v32 offset:8704\n" \
"ds_read_b64_tr_b16 v[172:173], v32 offset:12288\n" \
"ds_read_b64_tr_b16 v[174:175], v32 offset:12800\n" \
"ds_read_b64_tr_b16 v[200:201], v33 offset:8192\n" \
"ds_read_b64_tr_b16 v[202:203], v33 offset:8704\n" \
"ds_read_b64_tr_b16 v[204:205], v33 offset:12288\n" \
"ds_read_b64_tr_b16 v[206:207], v33 offset:12800\n" \
"ds_read_b64_tr_b16 v[232:233], v34 offset:8192\n" \
"ds_read_b64_tr_b16 v[234:235], v34 offset:8704\n" \
"ds_read_b64_tr_b16 v[236:237], v34 offset:12288\n" \
"ds_read_b64_tr_b16 v[238:239], v34 offset:12800\n" \
"s_waitcnt lgkmcnt(0)\n" \
"s_barrier\n" \
"s_add_u32 m0, s62, 8192\n" \
"s_mov_b32 s54, 524288\n" \
"buffer_load_dwordx4 v2, s[64:67], s54 offen lds\n" \
"s_add_u32 m0, s62, 9216\n" \
"s_mov_b32 s54, 557056\n" \
"buffer_load_dwordx4 v2, s[64:67], s54 offen lds\n" \
"s_add_u32 m0, s62, 10240\n" \
"s_mov_b32 s54, 589824\n" \
"buffer_load_dwordx4 v2, s[64:67], s54 offen lds\n" \
"s_add_u32 m0, s62, 11264\n" \
"s_mov_b32 s54, 622592\n" \
"buffer_load_dwordx4 v2, s[64:67], s54 offen lds\n" \
"s_add_u32 m0, s62, 12288\n" \
"s_mov_b32 s54, 655360\n" \
"buffer_load_dwordx4 v2, s[64:67], s54 offen lds\n" \
"s_add_u32 m0, s62, 13312\n" \
"s_mov_b32 s54, 688128\n" \
"buffer_load_dwordx4 v2, s[64:67], s54 offen lds\n" \
"s_add_u32 m0, s62, 14336\n" \
"s_mov_b32 s54, 720896\n" \
"buffer_load_dwordx4 v2, s[64:67], s54 offen lds\n" \
"s_add_u32 m0, s62, 15360\n" \
"s_mov_b32 s54, 753664\n" \
"buffer_load_dwordx4 v2, s[64:67], s54 offen lds\n" \
"s_waitcnt vmcnt(32)\n" \
"s_barrier\n" \
"ds_read_b64_tr_b16 v[144:145], v3 offset:16384\n" \
"ds_read_b64_tr_b16 v[146:147], v3 offset:16896\n" \
"ds_read_b64_tr_b16 v[148:149], v3 offset:20480\n" \
"ds_read_b64_tr_b16 v[150:151], v3 offset:20992\n" \
"ds_read_b64_tr_b16 v[176:177], v32 offset:16384\n" \
"ds_read_b64_tr_b16 v[178:179], v32 offset:16896\n" \
"ds_read_b64_tr_b16 v[180:181], v32 offset:20480\n" \
"ds_read_b64_tr_b16 v[182:183], v32 offset:20992\n" \
"ds_read_b64_tr_b16 v[208:209], v33 offset:16384\n" \
"ds_read_b64_tr_b16 v[210:211], v33 offset:16896\n" \
"ds_read_b64_tr_b16 v[212:213], v33 offset:20480\n" \
"ds_read_b64_tr_b16 v[214:215], v33 offset:20992\n" \
"ds_read_b64_tr_b16 v[240:241], v34 offset:16384\n" \
"ds_read_b64_tr_b16 v[242:243], v34 offset:16896\n" \
"ds_read_b64_tr_b16 v[244:245], v34 offset:20480\n" \
"ds_read_b64_tr_b16 v[246:247], v34 offset:20992\n" \
"s_waitcnt lgkmcnt(0)\n" \
"s_barrier\n" \
"s_add_u32 m0, s62, 16384\n" \
"s_mov_b32 s54, 786432\n" \
"buffer_load_dwordx4 v2, s[64:67], s54 offen lds\n" \
"s_add_u32 m0, s62, 17408\n" \
"s_mov_b32 s54, 819200\n" \
"buffer_load_dwordx4 v2, s[64:67], s54 offen lds\n" \
"s_add_u32 m0, s62, 18432\n" \
"s_mov_b32 s54, 851968\n" \
"buffer_load_dwordx4 v2, s[64:67], s54 offen lds\n" \
"s_add_u32 m0, s62, 19456\n" \
"s_mov_b32 s54, 884736\n" \
"buffer_load_dwordx4 v2, s[64:67], s54 offen lds\n" \
"s_add_u32 m0, s62, 20480\n" \
"s_mov_b32 s54, 917504\n" \
"buffer_load_dwordx4 v2, s[64:67], s54 offen lds\n" \
"s_add_u32 m0, s62, 21504\n" \
"s_mov_b32 s54, 950272\n" \
"buffer_load_dwordx4 v2, s[64:67], s54 offen lds\n" \
"s_add_u32 m0, s62, 22528\n" \
"s_mov_b32 s54, 983040\n" \
"buffer_load_dwordx4 v2, s[64:67], s54 offen lds\n" \
"s_add_u32 m0, s62, 23552\n" \
"s_mov_b32 s54, 1015808\n" \
"buffer_load_dwordx4 v2, s[64:67], s54 offen lds\n" \
"s_waitcnt vmcnt(32)\n" \
"s_barrier\n" \
"ds_read_b64_tr_b16 v[152:153], v3 offset:24576\n" \
"ds_read_b64_tr_b16 v[154:155], v3 offset:25088\n" \
"ds_read_b64_tr_b16 v[156:157], v3 offset:28672\n" \
"ds_read_b64_tr_b16 v[158:159], v3 offset:29184\n" \
"ds_read_b64_tr_b16 v[184:185], v32 offset:24576\n" \
"ds_read_b64_tr_b16 v[186:187], v32 offset:25088\n" \
"ds_read_b64_tr_b16 v[188:189], v32 offset:28672\n" \
"ds_read_b64_tr_b16 v[190:191], v32 offset:29184\n" \
"ds_read_b64_tr_b16 v[216:217], v33 offset:24576\n" \
"ds_read_b64_tr_b16 v[218:219], v33 offset:25088\n" \
"ds_read_b64_tr_b16 v[220:221], v33 offset:28672\n" \
"ds_read_b64_tr_b16 v[222:223], v33 offset:29184\n" \
"ds_read_b64_tr_b16 v[248:249], v34 offset:24576\n" \
"ds_read_b64_tr_b16 v[250:251], v34 offset:25088\n" \
"ds_read_b64_tr_b16 v[252:253], v34 offset:28672\n" \
"ds_read_b64_tr_b16 v[254:255], v34 offset:29184\n" \
"s_waitcnt lgkmcnt(0)\n" \
"s_barrier\n" \
"s_add_u32 m0, s62, 24576\n" \
"s_mov_b32 s54, 1048576\n" \
"buffer_load_dwordx4 v2, s[64:67], s54 offen lds\n" \
"s_add_u32 m0, s62, 25600\n" \
"s_mov_b32 s54, 1081344\n" \
"buffer_load_dwordx4 v2, s[64:67], s54 offen lds\n" \
"s_add_u32 m0, s62, 26624\n" \
"s_mov_b32 s54, 1114112\n" \
"buffer_load_dwordx4 v2, s[64:67], s54 offen lds\n" \
"s_add_u32 m0, s62, 27648\n" \
"s_mov_b32 s54, 1146880\n" \
"buffer_load_dwordx4 v2, s[64:67], s54 offen lds\n" \
"s_add_u32 m0, s62, 28672\n" \
"s_mov_b32 s54, 1179648\n" \
"buffer_load_dwordx4 v2, s[64:67], s54 offen lds\n" \
"s_add_u32 m0, s62, 29696\n" \
"s_mov_b32 s54, 1212416\n" \
"buffer_load_dwordx4 v2, s[64:67], s54 offen lds\n" \
"s_add_u32 m0, s62, 30720\n" \
"s_mov_b32 s54, 1245184\n" \
"buffer_load_dwordx4 v2, s[64:67], s54 offen lds\n" \
"s_add_u32 m0, s62, 31744\n" \
"s_mov_b32 s54, 1277952\n" \
"buffer_load_dwordx4 v2, s[64:67], s54 offen lds\n" \
"s_waitcnt vmcnt(36)\n" \
"ds_read_b64_tr_b16 a[0:1], v4 offset:32768\n" \
"ds_read_b64_tr_b16 a[2:3], v4 offset:32896\n" \
"ds_read_b64_tr_b16 a[4:5], v4 offset:34816\n" \
"ds_read_b64_tr_b16 a[6:7], v4 offset:34944\n" \
"ds_read_b64_tr_b16 a[8:9], v4 offset:32800\n" \
"ds_read_b64_tr_b16 a[10:11], v4 offset:32928\n" \
"ds_read_b64_tr_b16 a[12:13], v4 offset:34848\n" \
"ds_read_b64_tr_b16 a[14:15], v4 offset:34976\n" \
"s_waitcnt lgkmcnt(0)\n" \
"s_add_u32 m0, s62, 32768\n" \
"s_mov_b32 s54, 1310720\n" \
"buffer_load_dwordx4 v2, s[64:67], s54 offen lds\n" \
"s_add_u32 m0, s62, 33792\n" \
"s_mov_b32 s54, 1343488\n" \
"buffer_load_dwordx4 v2, s[64:67], s54 offen lds\n" \
"s_add_u32 m0, s62, 34816\n" \
"s_mov_b32 s54, 1376256\n" \
"buffer_load_dwordx4 v2, s[64:67], s54 offen lds\n" \
"s_add_u32 m0, s62, 35840\n" \
"s_mov_b32 s54, 1409024\n" \
"buffer_load_dwordx4 v2, s[64:67], s54 offen lds\n" \
"s_waitcnt vmcnt(36)\n" \
"ds_read_b64_tr_b16 a[16:17], v4 offset:36864\n" \
"ds_read_b64_tr_b16 a[18:19], v4 offset:36992\n" \
"ds_read_b64_tr_b16 a[20:21], v4 offset:38912\n" \
"ds_read_b64_tr_b16 a[22:23], v4 offset:39040\n" \
"ds_read_b64_tr_b16 a[24:25], v4 offset:36896\n" \
"ds_read_b64_tr_b16 a[26:27], v4 offset:37024\n" \
"ds_read_b64_tr_b16 a[28:29], v4 offset:38944\n" \
"ds_read_b64_tr_b16 a[30:31], v4 offset:39072\n" \
"s_waitcnt lgkmcnt(0)\n" \
"s_add_u32 m0, s62, 36864\n" \
"s_mov_b32 s54, 1441792\n" \
"buffer_load_dwordx4 v2, s[64:67], s54 offen lds\n" \
"s_add_u32 m0, s62, 37888\n" \
"s_mov_b32 s54, 1474560\n" \
"buffer_load_dwordx4 v2, s[64:67], s54 offen lds\n" \
"s_add_u32 m0, s62, 38912\n" \
"s_mov_b32 s54, 1507328\n" \
"buffer_load_dwordx4 v2, s[64:67], s54 offen lds\n" \
"s_add_u32 m0, s62, 39936\n" \
"s_mov_b32 s54, 1540096\n" \
"buffer_load_dwordx4 v2, s[64:67], s54 offen lds\n" \
"s_waitcnt vmcnt(36)\n" \
"ds_read_b64_tr_b16 a[32:33], v4 offset:0\n" \
"ds_read_b64_tr_b16 a[34:35], v4 offset:128\n" \
"ds_read_b64_tr_b16 a[36:37], v4 offset:2048\n" \
"ds_read_b64_tr_b16 a[38:39], v4 offset:2176\n" \
"ds_read_b64_tr_b16 a[40:41], v4 offset:32\n" \
"ds_read_b64_tr_b16 a[42:43], v4 offset:160\n" \
"ds_read_b64_tr_b16 a[44:45], v4 offset:2080\n" \
"ds_read_b64_tr_b16 a[46:47], v4 offset:2208\n" \
"s_waitcnt lgkmcnt(0)\n" \
"s_add_u32 m0, s62, 0\n" \
"s_mov_b32 s54, 1572864\n" \
"buffer_load_dwordx4 v2, s[64:67], s54 offen lds\n" \
"s_add_u32 m0, s62, 1024\n" \
"s_mov_b32 s54, 1605632\n" \
"buffer_load_dwordx4 v2, s[64:67], s54 offen lds\n" \
"s_add_u32 m0, s62, 2048\n" \
"s_mov_b32 s54, 1638400\n" \
"buffer_load_dwordx4 v2, s[64:67], s54 offen lds\n" \
"s_add_u32 m0, s62, 3072\n" \
"s_mov_b32 s54, 1671168\n" \
"buffer_load_dwordx4 v2, s[64:67], s54 offen lds\n" \
"s_waitcnt vmcnt(36)\n" \
"ds_read_b64_tr_b16 a[48:49], v4 offset:4096\n" \
"ds_read_b64_tr_b16 a[50:51], v4 offset:4224\n" \
"ds_read_b64_tr_b16 a[52:53], v4 offset:6144\n" \
"ds_read_b64_tr_b16 a[54:55], v4 offset:6272\n" \
"ds_read_b64_tr_b16 a[56:57], v4 offset:4128\n" \
"ds_read_b64_tr_b16 a[58:59], v4 offset:4256\n" \
"ds_read_b64_tr_b16 a[60:61], v4 offset:6176\n" \
"ds_read_b64_tr_b16 a[62:63], v4 offset:6304\n" \
"s_waitcnt lgkmcnt(0)\n" \
"s_add_u32 m0, s62, 4096\n" \
"s_mov_b32 s54, 1703936\n" \
"buffer_load_dwordx4 v2, s[64:67], s54 offen lds\n" \
"s_add_u32 m0, s62, 5120\n" \
"s_mov_b32 s54, 1736704\n" \
"buffer_load_dwordx4 v2, s[64:67], s54 offen lds\n" \
"s_add_u32 m0, s62, 6144\n" \
"s_mov_b32 s54, 1769472\n" \
"buffer_load_dwordx4 v2, s[64:67], s54 offen lds\n" \
"s_add_u32 m0, s62, 7168\n" \
"s_mov_b32 s54, 1802240\n" \
"buffer_load_dwordx4 v2, s[64:67], s54 offen lds\n" \
"s_waitcnt vmcnt(36)\n" \
"ds_read_b64_tr_b16 a[64:65], v4 offset:8192\n" \
"ds_read_b64_tr_b16 a[66:67], v4 offset:8320\n" \
"ds_read_b64_tr_b16 a[68:69], v4 offset:10240\n" \
"ds_read_b64_tr_b16 a[70:71], v4 offset:10368\n" \
"ds_read_b64_tr_b16 a[72:73], v4 offset:8224\n" \
"ds_read_b64_tr_b16 a[74:75], v4 offset:8352\n" \
"ds_read_b64_tr_b16 a[76:77], v4 offset:10272\n" \
"ds_read_b64_tr_b16 a[78:79], v4 offset:10400\n" \
"s_waitcnt lgkmcnt(0)\n" \
"s_add_u32 m0, s62, 8192\n" \
"s_mov_b32 s54, 1835008\n" \
"buffer_load_dwordx4 v2, s[64:67], s54 offen lds\n" \
"s_add_u32 m0, s62, 9216\n" \
"s_mov_b32 s54, 1867776\n" \
"buffer_load_dwordx4 v2, s[64:67], s54 offen lds\n" \
"s_add_u32 m0, s62, 10240\n" \
"s_mov_b32 s54, 1900544\n" \
"buffer_load_dwordx4 v2, s[64:67], s54 offen lds\n" \
"s_add_u32 m0, s62, 11264\n" \
"s_mov_b32 s54, 1933312\n" \
"buffer_load_dwordx4 v2, s[64:67], s54 offen lds\n" \
"s_waitcnt vmcnt(36)\n" \
"ds_read_b64_tr_b16 a[80:81], v4 offset:12288\n" \
"ds_read_b64_tr_b16 a[82:83], v4 offset:12416\n" \
"ds_read_b64_tr_b16 a[84:85], v4 offset:14336\n" \
"ds_read_b64_tr_b16 a[86:87], v4 offset:14464\n" \
"ds_read_b64_tr_b16 a[88:89], v4 offset:12320\n" \
"ds_read_b64_tr_b16 a[90:91], v4 offset:12448\n" \
"ds_read_b64_tr_b16 a[92:93], v4 offset:14368\n" \
"ds_read_b64_tr_b16 a[94:95], v4 offset:14496\n" \
"s_waitcnt lgkmcnt(0)\n" \
"s_add_u32 m0, s62, 12288\n" \
"s_mov_b32 s54, 1966080\n" \
"buffer_load_dwordx4 v2, s[64:67], s54 offen lds\n" \
"s_add_u32 m0, s62, 13312\n" \
"s_mov_b32 s54, 1998848\n" \
"buffer_load_dwordx4 v2, s[64:67], s54 offen lds\n" \
"s_add_u32 m0, s62, 14336\n" \
"s_mov_b32 s54, 2031616\n" \
"buffer_load_dwordx4 v2, s[64:67], s54 offen lds\n" \
"s_add_u32 m0, s62, 15360\n" \
"s_mov_b32 s54, 2064384\n" \
"buffer_load_dwordx4 v2, s[64:67], s54 offen lds\n" \
"s_waitcnt vmcnt(36)\n" \
"ds_read_b64_tr_b16 a[96:97], v4 offset:16384\n" \
"ds_read_b64_tr_b16 a[98:99], v4 offset:16512\n" \
"ds_read_b64_tr_b16 a[100:101], v4 offset:18432\n" \
"ds_read_b64_tr_b16 a[102:103], v4 offset:18560\n" \
"ds_read_b64_tr_b16 a[104:105], v4 offset:16416\n" \
"ds_read_b64_tr_b16 a[106:107], v4 offset:16544\n" \
"ds_read_b64_tr_b16 a[108:109], v4 offset:18464\n" \
"ds_read_b64_tr_b16 a[110:111], v4 offset:18592\n" \
"s_waitcnt vmcnt(32)\n" \
"ds_read_b64_tr_b16 a[112:113], v4 offset:20480\n" \
"ds_read_b64_tr_b16 a[114:115], v4 offset:20608\n" \
"ds_read_b64_tr_b16 a[116:117], v4 offset:22528\n" \
"ds_read_b64_tr_b16 a[118:119], v4 offset:22656\n" \
"ds_read_b64_tr_b16 a[120:121], v4 offset:20512\n" \
"ds_read_b64_tr_b16 a[122:123], v4 offset:20640\n" \
"ds_read_b64_tr_b16 a[124:125], v4 offset:22560\n" \
"ds_read_b64_tr_b16 a[126:127], v4 offset:22688\n" \
"s_waitcnt vmcnt(28)\n" \
"ds_read_b64_tr_b16 a[128:129], v4 offset:24576\n" \
"ds_read_b64_tr_b16 a[130:131], v4 offset:24704\n" \
"ds_read_b64_tr_b16 a[132:133], v4 offset:26624\n" \
"ds_read_b64_tr_b16 a[134:135], v4 offset:26752\n" \
"ds_read_b64_tr_b16 a[136:137], v4 offset:24608\n" \
"ds_read_b64_tr_b16 a[138:139], v4 offset:24736\n" \
"ds_read_b64_tr_b16 a[140:141], v4 offset:26656\n" \
"ds_read_b64_tr_b16 a[142:143], v4 offset:26784\n" \
"s_waitcnt vmcnt(24)\n" \
"ds_read_b64_tr_b16 a[144:145], v4 offset:28672\n" \
"ds_read_b64_tr_b16 a[146:147], v4 offset:28800\n" \
"ds_read_b64_tr_b16 a[148:149], v4 offset:30720\n" \
"ds_read_b64_tr_b16 a[150:151], v4 offset:30848\n" \
"ds_read_b64_tr_b16 a[152:153], v4 offset:28704\n" \
"ds_read_b64_tr_b16 a[154:155], v4 offset:28832\n" \
"ds_read_b64_tr_b16 a[156:157], v4 offset:30752\n" \
"ds_read_b64_tr_b16 a[158:159], v4 offset:30880\n" \
"s_waitcnt vmcnt(20)\n" \
"ds_read_b64_tr_b16 a[160:161], v4 offset:32768\n" \
"ds_read_b64_tr_b16 a[162:163], v4 offset:32896\n" \
"ds_read_b64_tr_b16 a[164:165], v4 offset:34816\n" \
"ds_read_b64_tr_b16 a[166:167], v4 offset:34944\n" \
"ds_read_b64_tr_b16 a[168:169], v4 offset:32800\n" \
"ds_read_b64_tr_b16 a[170:171], v4 offset:32928\n" \
"ds_read_b64_tr_b16 a[172:173], v4 offset:34848\n" \
"ds_read_b64_tr_b16 a[174:175], v4 offset:34976\n" \
"s_waitcnt vmcnt(16)\n" \
"ds_read_b64_tr_b16 a[176:177], v4 offset:36864\n" \
"ds_read_b64_tr_b16 a[178:179], v4 offset:36992\n" \
"ds_read_b64_tr_b16 a[180:181], v4 offset:38912\n" \
"ds_read_b64_tr_b16 a[182:183], v4 offset:39040\n" \
"ds_read_b64_tr_b16 a[184:185], v4 offset:36896\n" \
"ds_read_b64_tr_b16 a[186:187], v4 offset:37024\n" \
"ds_read_b64_tr_b16 a[188:189], v4 offset:38944\n" \
"ds_read_b64_tr_b16 a[190:191], v4 offset:39072\n" \
"s_waitcnt vmcnt(12)\n" \
"ds_read_b64_tr_b16 a[192:193], v4 offset:0\n" \
"ds_read_b64_tr_b16 a[194:195], v4 offset:128\n" \
"ds_read_b64_tr_b16 a[196:197], v4 offset:2048\n" \
"ds_read_b64_tr_b16 a[198:199], v4 offset:2176\n" \
"ds_read_b64_tr_b16 a[200:201], v4 offset:32\n" \
"ds_read_b64_tr_b16 a[202:203], v4 offset:160\n" \
"ds_read_b64_tr_b16 a[204:205], v4 offset:2080\n" \
"ds_read_b64_tr_b16 a[206:207], v4 offset:2208\n" \
"s_waitcnt vmcnt(8)\n" \
"ds_read_b64_tr_b16 a[208:209], v4 offset:4096\n" \
"ds_read_b64_tr_b16 a[210:211], v4 offset:4224\n" \
"ds_read_b64_tr_b16 a[212:213], v4 offset:6144\n" \
"ds_read_b64_tr_b16 a[214:215], v4 offset:6272\n" \
"ds_read_b64_tr_b16 a[216:217], v4 offset:4128\n" \
"ds_read_b64_tr_b16 a[218:219], v4 offset:4256\n" \
"ds_read_b64_tr_b16 a[220:221], v4 offset:6176\n" \
"ds_read_b64_tr_b16 a[222:223], v4 offset:6304\n" \
"s_waitcnt vmcnt(4)\n" \
"ds_read_b64_tr_b16 a[224:225], v4 offset:8192\n" \
"ds_read_b64_tr_b16 a[226:227], v4 offset:8320\n" \
"ds_read_b64_tr_b16 a[228:229], v4 offset:10240\n" \
"ds_read_b64_tr_b16 a[230:231], v4 offset:10368\n" \
"ds_read_b64_tr_b16 a[232:233], v4 offset:8224\n" \
"ds_read_b64_tr_b16 a[234:235], v4 offset:8352\n" \
"ds_read_b64_tr_b16 a[236:237], v4 offset:10272\n" \
"ds_read_b64_tr_b16 a[238:239], v4 offset:10400\n" \
"s_waitcnt vmcnt(0)\n" \
"ds_read_b64_tr_b16 a[240:241], v4 offset:12288\n" \
"ds_read_b64_tr_b16 a[242:243], v4 offset:12416\n" \
"ds_read_b64_tr_b16 a[244:245], v4 offset:14336\n" \
"ds_read_b64_tr_b16 a[246:247], v4 offset:14464\n" \
"ds_read_b64_tr_b16 a[248:249], v4 offset:12320\n" \
"ds_read_b64_tr_b16 a[250:251], v4 offset:12448\n" \
"ds_read_b64_tr_b16 a[252:253], v4 offset:14368\n" \
"ds_read_b64_tr_b16 a[254:255], v4 offset:14496\n" \
"s_waitcnt vmcnt(0) lgkmcnt(0)\n" \
"s_barrier\n" \
"s_mov_b32 s49, 0\n" \
"s_mov_b32 s52, 0\n" \
"s_mov_b32 s88, 0\n" \
"s_mov_b32 s76, 0\n" \
"s_mov_b32 s22, 0xfffffc00\n" \
"s_mov_b32 s23, -1\n" \
"s_mul_i32 s32, s17, 0x1010101\n" \
"s_mov_b32 s33, s32\n" \
"s_mov_b32 s34, s17\n" \
"v_max_u32 v20, 1, v20\n" \
"v_max_u32 v21, 1, v21\n" \
"v_max_u32 v22, 1, v22\n" \
"v_max_u32 v23, 1, v23\n" \
"buffer_store_dwordx4 v[20:23], v8, s[36:39], 0 offen sc1\n" \
"s_mov_b32 s72, 0\n" \
"s_mov_b32 s73, -1\n" \
"v_lshlrev_b32 v31, 1, v0\n" \
"v_add_u32 v31, 16384, v31\n" \
"s_lshl_b32 s79, s48, 10\n" \
"s_add_u32 s79, s79, 0\n" \
"v_xor_b32 v30, 0x1000, v6\n" \
"s_memrealtime s[56:57]\n" \
"s_waitcnt lgkmcnt(0)\n" \
"s_mov_b32 s50, s56\n" \
"s_waitcnt vmcnt(0) lgkmcnt(0)\n" \
"s_add_u32 s54, s49, 0\n" \
"s_and_b32 s54, s54, 3\n" \
"s_mul_i32 s58, s54, 131072\n" \
"s_add_u32 s54, s49, 1\n" \
"s_and_b32 s54, s54, 3\n" \
"s_mul_i32 s59, s54, 131072\n" \
"s_add_u32 s54, s49, 2\n" \
"s_and_b32 s54, s54, 3\n" \
"s_mul_i32 s60, s54, 131072\n" \
"s_add_u32 s54, s49, 3\n" \
"s_and_b32 s54, s54, 3\n" \
"s_mul_i32 s61, s54, 131072\n" \
"s_mov_b32 s77, 0\n" \
"s_add_u32 s78, s77, s63\n" \
"s_xor_b32 s89, s78, 0x4800\n" \
"s_xor_b32 s90, s78, 0x800\n" \
"s_xor_b32 s91, s78, 0x4000\n" \
"s_mov_b32 m0, s79\n" \
"s_add_u32 s51, s50, s46\n" \
"v_mov_b64 v[80:81], 0\n" \
"v_mov_b64 v[82:83], 0\n" \
"v_mov_b32 v13, -1\n" \
"s_mov_b32 s23, -1\n" \
"s_cmp_eq_u32 s48, 0\n" \
"s_cselect_b32 s80, -1, 0\n" \
"s_mov_b32 s81, 0\n" \
"s_nop 7\n" \
"s_nop 7\n" \
".p2align 6\n" \
".Lv_loop_%=:\n" \
"s_getpc_b64 s[18:19]\n" \
".Lv_wp_%=:\n" \
"s_add_u32 s18, s18, .Lv_ws_%=-.Lv_wp_%=\n" \
"s_addc_u32 s19, s19, 0\n" \
"s_sub_u32 s21, s51, 5\n" \
"s_nop 0\n" \
".Lv_w_%=:\n" \
"s_memrealtime s[56:57]\n" \
"s_waitcnt lgkmcnt(0)\n" \
"s_sub_u32 s54, s56, s21\n" \
"s_cmp_gt_u32 s54, s22\n" \
"s_cbranch_scc1 .Lv_w_%=\n" \
"s_min_u32 s21, s54, 3\n" \
"s_lshl2_add_u32 s18, s21, s18\n" \
"s_setpc_b64 s[18:19]\n" \
"s_nop 0\n" \
".Lv_ws_%=:\n" \
"s_nop 4\n" \
"s_nop 5\n" \
"s_nop 4\n" \
".Lv_w4_%=:\n" \
"s_barrier\n" \
".Lv_go_%=:\n" \
"buffer_load_dwordx4 v5, s[36:39], s78 offen sc1 lds\n" \
"s_nop 7\n" \
"s_add_u32 m0, s79, 4096\n" \
"s_nop 0\n" \
"buffer_load_dwordx4 v5, s[36:39], s89 offen sc1 lds\n" \
"s_nop 7\n" \
"s_add_u32 m0, s79, 8192\n" \
"s_nop 0\n" \
"buffer_load_dwordx4 v5, s[36:39], s90 offen sc1 lds\n" \
"s_nop 7\n" \
"s_add_u32 m0, s79, 12288\n" \
"s_nop 0\n" \
"buffer_load_dwordx4 v5, s[36:39], s91 offen sc1 lds\n" \
".Lv_x_%=:\n" \
"s_waitcnt vmcnt(2)\n" \
"s_barrier\n" \
"ds_read_b128 v[32:35], v0 offset:0\n" \
"ds_read_b128 v[36:39], v0 offset:1024\n" \
"ds_read_b128 v[40:43], v0 offset:2048\n" \
"ds_read_b128 v[44:47], v0 offset:3072\n" \
"ds_read_b128 v[48:51], v0 offset:4096\n" \
"s_waitcnt lgkmcnt(4)\n" \
"v_smfmac_f32_16x16x64_bf16 v[80:83], v[32:35], v[128:135], v12\n" \
"ds_read_b128 v[52:55], v0 offset:5120\n" \
"s_waitcnt lgkmcnt(4)\n" \
"v_min3_u32 v13, v13, v32, v34\n" \
"v_smfmac_f32_16x16x64_bf16 v[80:83], v[36:39], v[136:143], v12\n" \
"ds_read_b128 v[56:59], v0 offset:6144\n" \
"s_waitcnt lgkmcnt(4)\n" \
"v_min3_u32 v13, v13, v36, v38\n" \
"v_smfmac_f32_16x16x64_bf16 v[80:83], v[40:43], v[144:151], v12\n" \
"ds_read_b128 v[60:63], v0 offset:7168\n" \
"s_waitcnt lgkmcnt(4)\n" \
"v_min3_u32 v13, v13, v40, v42\n" \
"v_smfmac_f32_16x16x64_bf16 v[80:83], v[44:47], v[152:159], v12\n" \
"s_waitcnt vmcnt(0)\n" \
"s_barrier\n" \
"ds_read_b128 v[32:35], v0 offset:8192\n" \
"s_waitcnt lgkmcnt(4)\n" \
"v_min3_u32 v13, v13, v44, v46\n" \
"v_smfmac_f32_16x16x64_bf16 v[80:83], v[48:51], v[160:167], v12\n" \
"ds_read_b128 v[36:39], v0 offset:9216\n" \
"s_waitcnt lgkmcnt(4)\n" \
"v_min3_u32 v13, v13, v48, v50\n" \
"v_cmp_eq_u32_e64 vcc, 0, v13\n" \
"v_smfmac_f32_16x16x64_bf16 v[80:83], v[52:55], v[168:175], v12\n" \
"ds_read_b128 v[40:43], v0 offset:10240\n" \
"s_waitcnt lgkmcnt(4)\n" \
"v_min3_u32 v13, v13, v52, v54\n" \
"v_smfmac_f32_16x16x64_bf16 v[80:83], v[56:59], v[176:183], v12\n" \
"ds_read_b128 v[44:47], v0 offset:11264\n" \
"s_waitcnt lgkmcnt(4)\n" \
"v_min3_u32 v13, v13, v56, v58\n" \
"v_smfmac_f32_16x16x64_bf16 v[80:83], v[60:63], v[184:191], v12\n" \
"ds_read_b128 v[48:51], v0 offset:12288\n" \
"s_waitcnt lgkmcnt(4)\n" \
"s_nop 0\n" \
"s_cbranch_vccnz .Lv_mretry_%=\n" \
"v_min3_u32 v13, v13, v60, v62\n" \
"v_smfmac_f32_16x16x64_bf16 v[80:83], v[32:35], v[192:199], v12\n" \
"ds_read_b128 v[52:55], v0 offset:13312\n" \
"s_waitcnt lgkmcnt(4)\n" \
"v_min3_u32 v13, v13, v32, v34\n" \
"v_smfmac_f32_16x16x64_bf16 v[80:83], v[36:39], v[200:207], v12\n" \
"ds_read_b128 v[56:59], v0 offset:14336\n" \
"s_waitcnt lgkmcnt(4)\n" \
"v_min3_u32 v13, v13, v36, v38\n" \
"v_smfmac_f32_16x16x64_bf16 v[80:83], v[40:43], v[208:215], v12\n" \
"ds_read_b128 v[60:63], v0 offset:15360\n" \
"s_waitcnt lgkmcnt(4)\n" \
"v_min3_u32 v13, v13, v40, v42\n" \
"v_smfmac_f32_16x16x64_bf16 v[80:83], v[44:47], v[216:223], v12\n" \
"s_waitcnt lgkmcnt(3)\n" \
"v_min3_u32 v13, v13, v44, v46\n" \
"v_smfmac_f32_16x16x64_bf16 v[80:83], v[48:51], v[224:231], v12\n" \
"s_waitcnt lgkmcnt(2)\n" \
"v_min3_u32 v13, v13, v48, v50\n" \
"v_smfmac_f32_16x16x64_bf16 v[80:83], v[52:55], v[232:239], v12\n" \
"s_waitcnt lgkmcnt(1)\n" \
"v_min3_u32 v13, v13, v52, v54\n" \
"v_smfmac_f32_16x16x64_bf16 v[80:83], v[56:59], v[240:247], v12\n" \
"s_waitcnt lgkmcnt(0)\n" \
"v_min3_u32 v13, v13, v56, v58\n" \
"v_smfmac_f32_16x16x64_bf16 v[80:83], v[60:63], v[248:255], v12\n" \
"v_min3_u32 v13, v13, v60, v62\n" \
"v_cmp_eq_u32 vcc, 0, v13\n" \
"s_nop 5\n" \
"v_pk_add_f32 v[80:81], v[80:81], v[82:83]\n" \
"s_cbranch_vccnz .Lv_mretry_%=\n" \
"v_cvt_pk_bf16_f32 v84, v80, v81\n" \
"v_max_u32 v84, 1, v84\n" \
"buffer_store_dword v84, v7, s[36:39], s58 offen sc1\n" \
"s_mov_b32 s75, s77\n" \
"s_lshr_b32 s77, s59, 1\n" \
"buffer_load_dwordx2 v[64:65], v9, s[36:39], s75 offen sc1\n" \
"s_sub_u32 s53, s49, 1\n" \
"s_add_u32 s25, s24, 3\n" \
"s_cmp_eq_u32 s26, s24\n" \
"s_subb_u32 s25, s25, 0\n" \
"s_cmp_eq_u32 s28, s24\n" \
"s_subb_u32 s25, s25, 0\n" \
"s_cmp_eq_u32 s30, s24\n" \
"s_subb_u32 s25, s25, 0\n" \
"s_max_u32 s16, s25, 1\n" \
"s_sub_u32 s24, s25, s55\n" \
"s_min_u32 s24, s24, 255\n" \
"s_cmp_eq_u32 s23, s53\n" \
"s_cselect_b32 s24, s17, s24\n" \
"s_lshl_b64 s[32:33], s[32:33], 8\n" \
"s_or_b32 s32, s32, s24\n" \
"s_bfe_u32 s24, s32, 524296\n" \
"s_bfe_u32 s25, s33, 524296\n" \
"s_min_u32 s34, s24, s25\n" \
"s_cmp_eq_u32 s49, 63\n" \
"s_cbranch_scc1 .Lv_end_%=\n" \
"v_mov_b64 v[80:81], 0\n" \
"v_mov_b64 v[82:83], 0\n" \
"v_mov_b64 v[84:85], 0\n" \
"v_mov_b64 v[86:87], 0\n" \
"v_mov_b64 v[88:89], 0\n" \
"v_mov_b64 v[90:91], 0\n" \
"v_mov_b64 v[92:93], 0\n" \
"v_mov_b64 v[94:95], 0\n" \
"buffer_store_dword v16, v7, s[36:39], s60 offen sc1\n" \
"s_lshr_b32 s54, s61, 1\n" \
"buffer_store_dwordx4 v[16:19], v8, s[36:39], s54 offen sc1\n" \
"s_sub_u32 s54, s34, s74\n" \
"v_mov_b32 v13, s54\n" \
"buffer_store_dword v13, v28, s[36:39], 0 offen sc1\n" \
"v_mov_b32_e64 v13, -1\n" \
"s_mov_b32 s54, 0\n" \
"s_cmp_lg_u32 s48, 0\n" \
"s_cbranch_scc1 .Lv_wo4_%=\n" \
"s_waitcnt vmcnt(3)\n" \
"v_cmp_eq_u32_e64 vcc, 0, v64\n" \
"s_nop 0\n" \
"s_and_b64 vcc, vcc, s[80:81]\n" \
"s_cbranch_vccnz .Lv_h_cold_%=\n" \
".Lv_h_ok_%=:\n" \
"v_subrev_u32 v64, s51, v64\n" \
"v_max_i32_dpp v65, v65, v65 row_ror:8 row_mask:0xf bank_mask:0xf\n" \
"s_nop 0\n" \
"v_max_i32_dpp v64, v64, v64 row_ror:8 row_mask:0xf bank_mask:0xf\n" \
"v_max_i32_dpp v65, v65, v65 row_ror:4 row_mask:0xf bank_mask:0xf\n" \
"s_nop 0\n" \
"v_max_i32_dpp v64, v64, v64 row_ror:4 row_mask:0xf bank_mask:0xf\n" \
"v_max_i32_dpp v65, v65, v65 row_ror:2 row_mask:0xf bank_mask:0xf\n" \
"s_nop 0\n" \
"v_max_i32_dpp v64, v64, v64 row_ror:2 row_mask:0xf bank_mask:0xf\n" \
"v_max_i32_dpp v65, v65, v65 row_ror:1 row_mask:0xf bank_mask:0xf\n" \
"s_nop 0\n" \
"v_max_i32_dpp v64, v64, v64 row_ror:1 row_mask:0xf bank_mask:0xf\n" \
"v_readlane_b32 s53, v65, 0\n" \
"v_readlane_b32 s50, v65, 16\n" \
"v_readlane_b32 s54, v64, 0\n" \
"v_readlane_b32 s55, v64, 16\n" \
"s_max_i32 s50, s50, s53\n" \
"s_max_i32 s55, s55, s54\n" \
"s_add_u32 s55, s55, s51\n" \
"s_add_u32 s55, s55, s44\n" \
"s_add_u32 s50, s50, s55\n" \
"s_add_u32 s50, s50, s45\n" \
"s_getpc_b64 s[18:19]\n" \
".Lv_wop_%=:\n" \
"s_add_u32 s18, s18, .Lv_wos_%=-.Lv_wop_%=\n" \
"s_addc_u32 s19, s19, 0\n" \
"s_sub_u32 s21, s55, 5\n" \
"s_nop 0\n" \
"s_nop 0\n" \
"s_nop 0\n" \
"s_nop 0\n" \
"s_nop 0\n" \
".Lv_wo_%=:\n" \
"s_memrealtime s[56:57]\n" \
"s_waitcnt lgkmcnt(0)\n" \
"s_sub_u32 s54, s56, s21\n" \
"s_cmp_gt_u32 s54, s22\n" \
"s_cbranch_scc1 .Lv_wo_%=\n" \
"s_min_u32 s21, s54, 3\n" \
"s_lshl2_add_u32 s18, s21, s18\n" \
"s_setpc_b64 s[18:19]\n" \
"s_nop 0\n" \
".Lv_wos_%=:\n" \
"s_nop 4\n" \
"s_nop 5\n" \
"s_nop 4\n" \
".Lv_wo4_%=:\n" \
"s_barrier\n" \
".Lv_o_%=:\n" \
"buffer_load_dwordx4 v[32:35], v6, s[36:39], s58 offen offset:0 sc1\n" \
"s_cmp_lt_u32 s54, 8\n" \
"s_cselect_b32 s23, s23, s49\n" \
"s_nop 8\n" \
"buffer_load_dwordx4 v[36:39], v6, s[36:39], s58 offen offset:1024 sc1\n" \
"s_nop 10\n" \
"buffer_load_dwordx4 v[40:43], v6, s[36:39], s58 offen offset:2048 sc1\n" \
"s_nop 10\n" \
"buffer_load_dwordx4 v[44:47], v6, s[36:39], s58 offen offset:3072 sc1\n" \
"s_nop 10\n" \
"buffer_load_dwordx4 v[48:51], v30, s[36:39], s58 offen offset:0 sc1\n" \
"s_nop 10\n" \
"buffer_load_dwordx4 v[52:55], v30, s[36:39], s58 offen offset:1024 sc1\n" \
"s_nop 10\n" \
"buffer_load_dwordx4 v[56:59], v30, s[36:39], s58 offen offset:2048 sc1\n" \
"s_nop 10\n" \
"buffer_load_dwordx4 v[60:63], v30, s[36:39], s58 offen offset:3072 sc1\n" \
".Lv_y_%=:\n" \
"s_waitcnt vmcnt(7)\n" \
"v_smfmac_f32_16x16x64_bf16 v[80:83], v[32:35], a[0:7], v12\n" \
"v_min3_u32 v13, v13, v32, v34\n" \
"v_smfmac_f32_16x16x64_bf16 v[84:87], v[32:35], a[8:15], v12\n" \
"v_smfmac_f32_16x16x64_bf16 v[88:91], v[32:35], a[16:23], v12\n" \
"v_smfmac_f32_16x16x64_bf16 v[92:95], v[32:35], a[24:31], v12\n" \
"s_waitcnt vmcnt(6)\n" \
"v_smfmac_f32_16x16x64_bf16 v[80:83], v[36:39], a[32:39], v12\n" \
"v_min3_u32 v13, v13, v36, v38\n" \
"v_smfmac_f32_16x16x64_bf16 v[84:87], v[36:39], a[40:47], v12\n" \
"v_cmp_eq_u32 vcc, 0, v13\n" \
"v_smfmac_f32_16x16x64_bf16 v[88:91], v[36:39], a[48:55], v12\n" \
"v_smfmac_f32_16x16x64_bf16 v[92:95], v[36:39], a[56:63], v12\n" \
"s_cbranch_vccnz .Lv_retry_%=\n" \
"s_waitcnt vmcnt(5)\n" \
"v_smfmac_f32_16x16x64_bf16 v[80:83], v[40:43], a[64:71], v12\n" \
"v_min3_u32 v13, v13, v40, v42\n" \
"v_smfmac_f32_16x16x64_bf16 v[84:87], v[40:43], a[72:79], v12\n" \
"v_smfmac_f32_16x16x64_bf16 v[88:91], v[40:43], a[80:87], v12\n" \
"v_smfmac_f32_16x16x64_bf16 v[92:95], v[40:43], a[88:95], v12\n" \
"s_waitcnt vmcnt(4)\n" \
"v_smfmac_f32_16x16x64_bf16 v[80:83], v[44:47], a[96:103], v12\n" \
"v_min3_u32 v13, v13, v44, v46\n" \
"v_smfmac_f32_16x16x64_bf16 v[84:87], v[44:47], a[104:111], v12\n" \
"v_smfmac_f32_16x16x64_bf16 v[88:91], v[44:47], a[112:119], v12\n" \
"v_smfmac_f32_16x16x64_bf16 v[92:95], v[44:47], a[120:127], v12\n" \
"s_waitcnt vmcnt(3)\n" \
"v_smfmac_f32_16x16x64_bf16 v[80:83], v[48:51], a[128:135], v12\n" \
"v_min3_u32 v13, v13, v48, v50\n" \
"v_smfmac_f32_16x16x64_bf16 v[84:87], v[48:51], a[136:143], v12\n" \
"v_smfmac_f32_16x16x64_bf16 v[88:91], v[48:51], a[144:151], v12\n" \
"v_smfmac_f32_16x16x64_bf16 v[92:95], v[48:51], a[152:159], v12\n" \
"s_waitcnt vmcnt(2)\n" \
"v_smfmac_f32_16x16x64_bf16 v[80:83], v[52:55], a[160:167], v12\n" \
"v_min3_u32 v13, v13, v52, v54\n" \
"v_smfmac_f32_16x16x64_bf16 v[84:87], v[52:55], a[168:175], v12\n" \
"v_smfmac_f32_16x16x64_bf16 v[88:91], v[52:55], a[176:183], v12\n" \
"v_smfmac_f32_16x16x64_bf16 v[92:95], v[52:55], a[184:191], v12\n" \
"s_waitcnt vmcnt(1)\n" \
"v_smfmac_f32_16x16x64_bf16 v[80:83], v[56:59], a[192:199], v12\n" \
"v_min3_u32 v13, v13, v56, v58\n" \
"v_smfmac_f32_16x16x64_bf16 v[84:87], v[56:59], a[200:207], v12\n" \
"v_smfmac_f32_16x16x64_bf16 v[88:91], v[56:59], a[208:215], v12\n" \
"v_smfmac_f32_16x16x64_bf16 v[92:95], v[56:59], a[216:223], v12\n" \
"s_waitcnt vmcnt(0)\n" \
"v_smfmac_f32_16x16x64_bf16 v[80:83], v[60:63], a[224:231], v12\n" \
"v_min3_u32 v13, v13, v60, v62\n" \
"v_smfmac_f32_16x16x64_bf16 v[84:87], v[60:63], a[232:239], v12\n" \
"v_cmp_eq_u32 vcc, 0, v13\n" \
"v_smfmac_f32_16x16x64_bf16 v[88:91], v[60:63], a[240:247], v12\n" \
"v_smfmac_f32_16x16x64_bf16 v[92:95], v[60:63], a[248:255], v12\n" \
"s_cbranch_vccnz .Lv_retry_%=\n" \
".Lv_y2_%=:\n" \
"v_pk_add_f32 v[80:81], v[80:81], v[82:83]\n" \
"v_pk_add_f32 v[84:85], v[84:85], v[86:87]\n" \
"s_nop 0\n" \
"v_pk_add_f32 v[88:89], v[88:89], v[90:91]\n" \
"s_nop 2\n" \
"v_pk_add_f32 v[92:93], v[92:93], v[94:95]\n" \
"v_cndmask_b32_e64 v80, v80, v88, s[72:73]\n" \
"v_cndmask_b32_e64 v81, v81, v89, s[72:73]\n" \
"v_cndmask_b32_e64 v84, v84, v92, s[72:73]\n" \
"v_cndmask_b32_e64 v85, v85, v93, s[72:73]\n" \
"s_nop 0\n" \
"v_permlane32_swap_b32 v80, v84\n" \
"v_permlane32_swap_b32 v81, v85\n" \
"v_pk_add_f32 v[80:81], v[80:81], v[84:85]\n" \
"ds_write2_b32 v15, v80, v81 offset1:32\n" \
"s_cmp_lg_u32 s48, 0\n" \
"s_waitcnt lgkmcnt(0)\n" \
"s_barrier\n" \
"s_nop 0\n" \
"s_nop 0\n" \
"s_nop 0\n" \
"s_cbranch_scc1 .Lv_skip_%=\n" \
"ds_read_b128 v[64:67], v31\n" \
"ds_read_b128 v[68:71], v31 offset:16\n" \
"s_waitcnt lgkmcnt(1)\n" \
"v_pk_add_f32 v[64:65], v[64:65], v[66:67]\n" \
"s_waitcnt lgkmcnt(0)\n" \
"v_pk_add_f32 v[68:69], v[68:69], v[70:71]\n" \
"v_pk_add_f32 v[64:65], v[64:65], v[68:69]\n" \
"v_cvt_pk_bf16_f32 v72, v64, v65\n" \
"v_max_u32 v72, 1, v72\n" \
"s_nop 1\n" \
"v_mov_b32_dpp v73, v72 quad_perm:[1,1,1,1] row_mask:0xf bank_mask:0xf\n" \
"v_mov_b32_dpp v74, v72 quad_perm:[2,2,2,2] row_mask:0xf bank_mask:0xf\n" \
"v_mov_b32_dpp v75, v72 quad_perm:[3,3,3,3] row_mask:0xf bank_mask:0xf\n" \
".Lv_skip_%=:\n" \
"buffer_store_dwordx4 v[72:75], v8, s[36:39], s77 offen sc1\n" \
"s_memrealtime s[24:25]\n" \
"s_mov_b32 s54, s58\n" \
"s_mov_b32 s58, s59\n" \
"s_mov_b32 s59, s60\n" \
"s_mov_b32 s60, s61\n" \
"s_mov_b32 s61, s54\n" \
"s_memrealtime s[26:27]\n" \
"s_add_u32 s78, s77, s63\n" \
"s_xor_b32 s89, s78, 0x4800\n" \
"s_xor_b32 s90, s78, 0x800\n" \
"s_xor_b32 s91, s78, 0x4000\n" \
"s_mov_b32 m0, s79\n" \
"s_memrealtime s[28:29]\n" \
"s_add_u32 s51, s50, s46\n" \
"v_mov_b64 v[80:81], 0\n" \
"v_mov_b64 v[82:83], 0\n" \
"v_mov_b32 v13, -1\n" \
"s_memrealtime s[30:31]\n" \
"s_add_u32 s49, s49, 1\n" \
"s_waitcnt lgkmcnt(0)\n" \
"s_cmp_lg_u32 s48, 0\n" \
"s_nop 0\n" \
"s_nop 0\n" \
"s_nop 0\n" \
"s_cbranch_scc1 .Lv_w4_%=\n" \
"s_branch .Lv_loop_%=\n" \
".Lv_h_cold_%=:\n" \
"s_nop 7\n" \
"s_nop 7\n" \
"s_waitcnt vmcnt(0) lgkmcnt(0)\n" \
"s_mov_b32 s53, 0\n" \
".Lv_h_again_%=:\n" \
"s_cmp_lg_u32 s52, 0\n" \
"s_cbranch_scc1 .Lv_h_ok_%=\n" \
"s_sleep 1\n" \
"buffer_load_dwordx2 v[64:65], v9, s[36:39], s75 offen sc1\n" \
"s_waitcnt vmcnt(0)\n" \
"v_cmp_eq_u32 vcc, 0, v64\n" \
"s_nop 0\n" \
"s_and_b64 vcc, vcc, s[80:81]\n" \
"s_nop 4\n" \
"s_cbranch_vccz .Lv_h_ok_%=\n" \
"s_add_u32 s53, s53, 1\n" \
"s_cmp_lt_u32 s53, 32768\n" \
"s_cbranch_scc1 .Lv_h_again_%=\n" \
"s_mov_b32 s52, 1\n" \
"v_mov_b32 v10, 256\n" \
"v_mov_b32 v11, 1\n" \
"buffer_store_dword v11, v10, s[36:39], 0 offen sc1\n" \
"s_waitcnt vmcnt(0) lgkmcnt(0)\n" \
"s_branch .Lv_h_ok_%=\n" \
".Lv_retry_%=:\n" \
"s_nop 7\n" \
"s_nop 7\n" \
"s_cmp_lg_u32 s52, 0\n" \
"s_cbranch_scc1 .Lv_dead_%=\n" \
"v_mov_b64 v[80:81], 0\n" \
"v_mov_b64 v[82:83], 0\n" \
"v_mov_b64 v[84:85], 0\n" \
"v_mov_b64 v[86:87], 0\n" \
"v_mov_b64 v[88:89], 0\n" \
"v_mov_b64 v[90:91], 0\n" \
"v_mov_b64 v[92:93], 0\n" \
"v_mov_b64 v[94:95], 0\n" \
"v_mov_b32 v13, -1\n" \
"s_mov_b32 s54, 100\n" \
"s_add_u32 s76, s76, 1\n" \
"s_cmp_lt_u32 s76, 32768\n" \
"s_cbranch_scc1 .Lv_o_%=\n" \
"s_mov_b32 s52, 1\n" \
"v_mov_b32 v13, 256\n" \
"v_mov_b32 v14, 1\n" \
"buffer_store_dword v14, v13, s[36:39], 0 offen sc1\n" \
".Lv_dead_%=:\n" \
"s_waitcnt vmcnt(0) lgkmcnt(0)\n" \
"s_branch .Lv_y2_%=\n" \
".Lv_mretry_%=:\n" \
"s_nop 7\n" \
"s_nop 7\n" \
"v_mov_b64 v[80:81], 0\n" \
"v_mov_b64 v[82:83], 0\n" \
"v_mov_b32 v13, -1\n" \
"s_mov_b32 m0, s79\n" \
"s_waitcnt vmcnt(0) lgkmcnt(0)\n" \
"s_barrier\n" \
"s_add_u32 s88, s88, 1\n" \
"s_cmp_lt_u32 s88, 32768\n" \
"s_cbranch_scc1 .Lv_r1_%=\n" \
"s_mov_b32 s52, 1\n" \
"v_mov_b32 v13, 256\n" \
"v_mov_b32 v14, 1\n" \
"buffer_store_dword v14, v13, s[36:39], 0 offen sc1\n" \
"s_waitcnt vmcnt(0) lgkmcnt(0)\n" \
"s_endpgm\n" \
".Lv_r1_%=:\n" \
"s_cmp_lg_u32 s49, 0\n" \
"s_cbranch_scc1 .Lv_r2_%=\n" \
"s_sleep 8\n" \
".Lv_r2_%=:\n" \
"s_branch .Lv_go_%=\n" \
".Lv_end_%=:\n" \
"s_nop 7\n" \
"s_nop 7\n" \
"s_waitcnt vmcnt(0) lgkmcnt(0)\n" \
"s_waitcnt vmcnt(0) lgkmcnt(0)\n" \
"s_endpgm\n"
#define ASM_S64_WO \
"s_nop 7\n" \
"s_nop 7\n" \
"s_lshl_b32 s52, s48, 15\n" \
"s_lshl_b32 s53, s48, 13\n" \
"s_add_u32 s53, s53, 98304\n" \
"s_sub_u32 s54, s53, s52\n" \
"v_add_u32 v9, s54, v1\n" \
"s_mov_b32 s54, s85\n" \
"s_mul_i32 s86, s54, 262144\n" \
"s_add_u32 s54, s54, 1\n" \
"s_cmp_eq_u32 s54, 8\n" \
"s_cselect_b32 s54, 0, s54\n" \
"s_mul_i32 s87, s54, 262144\n" \
"s_add_u32 s54, s54, 1\n" \
"s_cmp_eq_u32 s54, 8\n" \
"s_cselect_b32 s54, 0, s54\n" \
"s_mul_i32 s88, s54, 262144\n" \
"s_add_u32 s54, s54, 1\n" \
"s_cmp_eq_u32 s54, 8\n" \
"s_cselect_b32 s54, 0, s54\n" \
"s_mul_i32 s89, s54, 262144\n" \
"s_add_u32 s54, s54, 1\n" \
"s_cmp_eq_u32 s54, 8\n" \
"s_cselect_b32 s54, 0, s54\n" \
"s_mul_i32 s90, s54, 262144\n" \
"s_add_u32 s54, s54, 1\n" \
"s_cmp_eq_u32 s54, 8\n" \
"s_cselect_b32 s54, 0, s54\n" \
"s_mul_i32 s91, s54, 262144\n" \
"s_add_u32 s54, s54, 1\n" \
"s_cmp_eq_u32 s54, 8\n" \
"s_cselect_b32 s54, 0, s54\n" \
"s_mul_i32 s92, s54, 262144\n" \
"s_add_u32 s54, s54, 1\n" \
"s_cmp_eq_u32 s54, 8\n" \
"s_cselect_b32 s54, 0, s54\n" \
"s_mul_i32 s93, s54, 262144\n" \
"s_add_u32 s54, s54, 1\n" \
"s_cmp_eq_u32 s54, 8\n" \
"s_cselect_b32 s54, 0, s54\n" \
"s_add_u32 m0, s52, 0\n" \
"s_add_u32 s54, s86, 0\n" \
"buffer_load_dwordx4 v0, s[40:43], s54 offen lds\n" \
"s_add_u32 m0, s52, 1024\n" \
"s_add_u32 s54, s86, 16384\n" \
"buffer_load_dwordx4 v0, s[40:43], s54 offen lds\n" \
"s_add_u32 m0, s52, 2048\n" \
"s_add_u32 s54, s86, 32768\n" \
"buffer_load_dwordx4 v0, s[40:43], s54 offen lds\n" \
"s_add_u32 m0, s52, 3072\n" \
"s_add_u32 s54, s86, 49152\n" \
"buffer_load_dwordx4 v0, s[40:43], s54 offen lds\n" \
"s_mov_b32 s54, s99\n" \
".Lswo_z0_%=:\n" \
"s_sleep 1\n" \
"s_sub_u32 s54, s54, 1\n" \
"s_cbranch_scc0 .Lswo_z0_%=\n" \
"s_add_u32 m0, s52, 4096\n" \
"s_add_u32 s54, s86, 65536\n" \
"buffer_load_dwordx4 v0, s[40:43], s54 offen lds\n" \
"s_add_u32 m0, s52, 5120\n" \
"s_add_u32 s54, s86, 81920\n" \
"buffer_load_dwordx4 v0, s[40:43], s54 offen lds\n" \
"s_add_u32 m0, s52, 6144\n" \
"s_add_u32 s54, s86, 98304\n" \
"buffer_load_dwordx4 v0, s[40:43], s54 offen lds\n" \
"s_add_u32 m0, s52, 7168\n" \
"s_add_u32 s54, s86, 114688\n" \
"buffer_load_dwordx4 v0, s[40:43], s54 offen lds\n" \
"s_mov_b32 s54, s99\n" \
".Lswo_z1_%=:\n" \
"s_sleep 1\n" \
"s_sub_u32 s54, s54, 1\n" \
"s_cbranch_scc0 .Lswo_z1_%=\n" \
"s_add_u32 m0, s52, 8192\n" \
"s_add_u32 s54, s86, 131072\n" \
"buffer_load_dwordx4 v0, s[40:43], s54 offen lds\n" \
"s_add_u32 m0, s52, 9216\n" \
"s_add_u32 s54, s86, 147456\n" \
"buffer_load_dwordx4 v0, s[40:43], s54 offen lds\n" \
"s_add_u32 m0, s52, 10240\n" \
"s_add_u32 s54, s86, 163840\n" \
"buffer_load_dwordx4 v0, s[40:43], s54 offen lds\n" \
"s_add_u32 m0, s52, 11264\n" \
"s_add_u32 s54, s86, 180224\n" \
"buffer_load_dwordx4 v0, s[40:43], s54 offen lds\n" \
"s_mov_b32 s54, s99\n" \
".Lswo_z2_%=:\n" \
"s_sleep 1\n" \
"s_sub_u32 s54, s54, 1\n" \
"s_cbranch_scc0 .Lswo_z2_%=\n" \
"s_add_u32 m0, s52, 12288\n" \
"s_add_u32 s54, s86, 196608\n" \
"buffer_load_dwordx4 v0, s[40:43], s54 offen lds\n" \
"s_add_u32 m0, s52, 13312\n" \
"s_add_u32 s54, s86, 212992\n" \
"buffer_load_dwordx4 v0, s[40:43], s54 offen lds\n" \
"s_add_u32 m0, s52, 14336\n" \
"s_add_u32 s54, s86, 229376\n" \
"buffer_load_dwordx4 v0, s[40:43], s54 offen lds\n" \
"s_add_u32 m0, s52, 15360\n" \
"s_add_u32 s54, s86, 245760\n" \
"buffer_load_dwordx4 v0, s[40:43], s54 offen lds\n" \
"s_mov_b32 s54, s99\n" \
".Lswo_z3_%=:\n" \
"s_sleep 1\n" \
"s_sub_u32 s54, s54, 1\n" \
"s_cbranch_scc0 .Lswo_z3_%=\n" \
"s_add_u32 m0, s52, 16384\n" \
"s_add_u32 s54, s87, 0\n" \
"buffer_load_dwordx4 v0, s[40:43], s54 offen lds\n" \
"s_add_u32 m0, s52, 17408\n" \
"s_add_u32 s54, s87, 16384\n" \
"buffer_load_dwordx4 v0, s[40:43], s54 offen lds\n" \
"s_add_u32 m0, s52, 18432\n" \
"s_add_u32 s54, s87, 32768\n" \
"buffer_load_dwordx4 v0, s[40:43], s54 offen lds\n" \
"s_add_u32 m0, s52, 19456\n" \
"s_add_u32 s54, s87, 49152\n" \
"buffer_load_dwordx4 v0, s[40:43], s54 offen lds\n" \
"s_mov_b32 s54, s99\n" \
".Lswo_z4_%=:\n" \
"s_sleep 1\n" \
"s_sub_u32 s54, s54, 1\n" \
"s_cbranch_scc0 .Lswo_z4_%=\n" \
"s_add_u32 m0, s52, 20480\n" \
"s_add_u32 s54, s87, 65536\n" \
"buffer_load_dwordx4 v0, s[40:43], s54 offen lds\n" \
"s_add_u32 m0, s52, 21504\n" \
"s_add_u32 s54, s87, 81920\n" \
"buffer_load_dwordx4 v0, s[40:43], s54 offen lds\n" \
"s_add_u32 m0, s52, 22528\n" \
"s_add_u32 s54, s87, 98304\n" \
"buffer_load_dwordx4 v0, s[40:43], s54 offen lds\n" \
"s_add_u32 m0, s52, 23552\n" \
"s_add_u32 s54, s87, 114688\n" \
"buffer_load_dwordx4 v0, s[40:43], s54 offen lds\n" \
"s_mov_b32 s54, s99\n" \
".Lswo_z5_%=:\n" \
"s_sleep 1\n" \
"s_sub_u32 s54, s54, 1\n" \
"s_cbranch_scc0 .Lswo_z5_%=\n" \
"s_add_u32 m0, s52, 24576\n" \
"s_add_u32 s54, s87, 131072\n" \
"buffer_load_dwordx4 v0, s[40:43], s54 offen lds\n" \
"s_add_u32 m0, s52, 25600\n" \
"s_add_u32 s54, s87, 147456\n" \
"buffer_load_dwordx4 v0, s[40:43], s54 offen lds\n" \
"s_add_u32 m0, s52, 26624\n" \
"s_add_u32 s54, s87, 163840\n" \
"buffer_load_dwordx4 v0, s[40:43], s54 offen lds\n" \
"s_add_u32 m0, s52, 27648\n" \
"s_add_u32 s54, s87, 180224\n" \
"buffer_load_dwordx4 v0, s[40:43], s54 offen lds\n" \
"s_mov_b32 s54, s99\n" \
".Lswo_z6_%=:\n" \
"s_sleep 1\n" \
"s_sub_u32 s54, s54, 1\n" \
"s_cbranch_scc0 .Lswo_z6_%=\n" \
"s_add_u32 m0, s52, 28672\n" \
"s_add_u32 s54, s87, 196608\n" \
"buffer_load_dwordx4 v0, s[40:43], s54 offen lds\n" \
"s_add_u32 m0, s52, 29696\n" \
"s_add_u32 s54, s87, 212992\n" \
"buffer_load_dwordx4 v0, s[40:43], s54 offen lds\n" \
"s_add_u32 m0, s52, 30720\n" \
"s_add_u32 s54, s87, 229376\n" \
"buffer_load_dwordx4 v0, s[40:43], s54 offen lds\n" \
"s_add_u32 m0, s52, 31744\n" \
"s_add_u32 s54, s87, 245760\n" \
"buffer_load_dwordx4 v0, s[40:43], s54 offen lds\n" \
"s_mov_b32 s54, s99\n" \
".Lswo_z7_%=:\n" \
"s_sleep 1\n" \
"s_sub_u32 s54, s54, 1\n" \
"s_cbranch_scc0 .Lswo_z7_%=\n" \
"s_add_u32 m0, s53, 32768\n" \
"s_add_u32 s54, s88, 0\n" \
"buffer_load_dwordx4 v0, s[40:43], s54 offen lds\n" \
"s_add_u32 m0, s53, 33792\n" \
"s_add_u32 s54, s88, 16384\n" \
"buffer_load_dwordx4 v0, s[40:43], s54 offen lds\n" \
"s_add_u32 m0, s53, 34816\n" \
"s_add_u32 s54, s88, 32768\n" \
"buffer_load_dwordx4 v0, s[40:43], s54 offen lds\n" \
"s_add_u32 m0, s53, 35840\n" \
"s_add_u32 s54, s88, 49152\n" \
"buffer_load_dwordx4 v0, s[40:43], s54 offen lds\n" \
"s_mov_b32 s54, s99\n" \
".Lswo_z8_%=:\n" \
"s_sleep 1\n" \
"s_sub_u32 s54, s54, 1\n" \
"s_cbranch_scc0 .Lswo_z8_%=\n" \
"s_add_u32 m0, s53, 36864\n" \
"s_add_u32 s54, s88, 65536\n" \
"buffer_load_dwordx4 v0, s[40:43], s54 offen lds\n" \
"s_add_u32 m0, s53, 37888\n" \
"s_add_u32 s54, s88, 81920\n" \
"buffer_load_dwordx4 v0, s[40:43], s54 offen lds\n" \
"s_add_u32 m0, s53, 38912\n" \
"s_add_u32 s54, s88, 98304\n" \
"buffer_load_dwordx4 v0, s[40:43], s54 offen lds\n" \
"s_add_u32 m0, s53, 39936\n" \
"s_add_u32 s54, s88, 114688\n" \
"buffer_load_dwordx4 v0, s[40:43], s54 offen lds\n" \
"s_mov_b32 s54, s99\n" \
".Lswo_z9_%=:\n" \
"s_sleep 1\n" \
"s_sub_u32 s54, s54, 1\n" \
"s_cbranch_scc0 .Lswo_z9_%=\n" \
"s_waitcnt vmcnt(36)\n" \
"ds_read_b64_tr_b16 a[0:1], v1 offset:0\n" \
"ds_read_b64_tr_b16 a[2:3], v1 offset:2048\n" \
"ds_read_b64_tr_b16 a[128:129], v1 offset:32\n" \
"ds_read_b64_tr_b16 a[130:131], v1 offset:2080\n" \
"ds_read_b64_tr_b16 v[128:129], v1 offset:64\n" \
"ds_read_b64_tr_b16 v[130:131], v1 offset:2112\n" \
"ds_read_b64_tr_b16 v[16:17], v1 offset:96\n" \
"ds_read_b64_tr_b16 v[18:19], v1 offset:2144\n" \
"s_waitcnt lgkmcnt(0)\n" \
"s_add_u32 m0, s52, 0\n" \
"s_add_u32 s54, s88, 131072\n" \
"buffer_load_dwordx4 v0, s[40:43], s54 offen lds\n" \
"s_add_u32 m0, s52, 1024\n" \
"s_add_u32 s54, s88, 147456\n" \
"buffer_load_dwordx4 v0, s[40:43], s54 offen lds\n" \
"s_add_u32 m0, s52, 2048\n" \
"s_add_u32 s54, s88, 163840\n" \
"buffer_load_dwordx4 v0, s[40:43], s54 offen lds\n" \
"s_add_u32 m0, s52, 3072\n" \
"s_add_u32 s54, s88, 180224\n" \
"buffer_load_dwordx4 v0, s[40:43], s54 offen lds\n" \
"s_mov_b32 s54, s99\n" \
".Lswo_z10_%=:\n" \
"s_sleep 1\n" \
"s_sub_u32 s54, s54, 1\n" \
"s_cbranch_scc0 .Lswo_z10_%=\n" \
"s_waitcnt vmcnt(36)\n" \
"ds_read_b64_tr_b16 a[4:5], v1 offset:4096\n" \
"ds_read_b64_tr_b16 a[6:7], v1 offset:6144\n" \
"ds_read_b64_tr_b16 a[132:133], v1 offset:4128\n" \
"ds_read_b64_tr_b16 a[134:135], v1 offset:6176\n" \
"ds_read_b64_tr_b16 v[132:133], v1 offset:4160\n" \
"ds_read_b64_tr_b16 v[134:135], v1 offset:6208\n" \
"ds_read_b64_tr_b16 v[20:21], v1 offset:4192\n" \
"ds_read_b64_tr_b16 v[22:23], v1 offset:6240\n" \
"s_waitcnt lgkmcnt(0)\n" \
"s_add_u32 m0, s52, 4096\n" \
"s_add_u32 s54, s88, 196608\n" \
"buffer_load_dwordx4 v0, s[40:43], s54 offen lds\n" \
"s_add_u32 m0, s52, 5120\n" \
"s_add_u32 s54, s88, 212992\n" \
"buffer_load_dwordx4 v0, s[40:43], s54 offen lds\n" \
"s_add_u32 m0, s52, 6144\n" \
"s_add_u32 s54, s88, 229376\n" \
"buffer_load_dwordx4 v0, s[40:43], s54 offen lds\n" \
"s_add_u32 m0, s52, 7168\n" \
"s_add_u32 s54, s88, 245760\n" \
"buffer_load_dwordx4 v0, s[40:43], s54 offen lds\n" \
"s_mov_b32 s54, s99\n" \
".Lswo_z11_%=:\n" \
"s_sleep 1\n" \
"s_sub_u32 s54, s54, 1\n" \
"s_cbranch_scc0 .Lswo_z11_%=\n" \
"s_waitcnt vmcnt(36)\n" \
"ds_read_b64_tr_b16 a[8:9], v1 offset:8192\n" \
"ds_read_b64_tr_b16 a[10:11], v1 offset:10240\n" \
"ds_read_b64_tr_b16 a[136:137], v1 offset:8224\n" \
"ds_read_b64_tr_b16 a[138:139], v1 offset:10272\n" \
"ds_read_b64_tr_b16 v[136:137], v1 offset:8256\n" \
"ds_read_b64_tr_b16 v[138:139], v1 offset:10304\n" \
"ds_read_b64_tr_b16 v[24:25], v1 offset:8288\n" \
"ds_read_b64_tr_b16 v[26:27], v1 offset:10336\n" \
"s_waitcnt lgkmcnt(0)\n" \
"s_add_u32 m0, s52, 8192\n" \
"s_add_u32 s54, s89, 0\n" \
"buffer_load_dwordx4 v0, s[40:43], s54 offen lds\n" \
"s_add_u32 m0, s52, 9216\n" \
"s_add_u32 s54, s89, 16384\n" \
"buffer_load_dwordx4 v0, s[40:43], s54 offen lds\n" \
"s_add_u32 m0, s52, 10240\n" \
"s_add_u32 s54, s89, 32768\n" \
"buffer_load_dwordx4 v0, s[40:43], s54 offen lds\n" \
"s_add_u32 m0, s52, 11264\n" \
"s_add_u32 s54, s89, 49152\n" \
"buffer_load_dwordx4 v0, s[40:43], s54 offen lds\n" \
"s_mov_b32 s54, s99\n" \
".Lswo_z12_%=:\n" \
"s_sleep 1\n" \
"s_sub_u32 s54, s54, 1\n" \
"s_cbranch_scc0 .Lswo_z12_%=\n" \
"s_waitcnt vmcnt(36)\n" \
"ds_read_b64_tr_b16 a[12:13], v1 offset:12288\n" \
"ds_read_b64_tr_b16 a[14:15], v1 offset:14336\n" \
"ds_read_b64_tr_b16 a[140:141], v1 offset:12320\n" \
"ds_read_b64_tr_b16 a[142:143], v1 offset:14368\n" \
"ds_read_b64_tr_b16 v[140:141], v1 offset:12352\n" \
"ds_read_b64_tr_b16 v[142:143], v1 offset:14400\n" \
"ds_read_b64_tr_b16 v[28:29], v1 offset:12384\n" \
"ds_read_b64_tr_b16 v[30:31], v1 offset:14432\n" \
"s_waitcnt lgkmcnt(0)\n" \
"s_add_u32 m0, s52, 12288\n" \
"s_add_u32 s54, s89, 65536\n" \
"buffer_load_dwordx4 v0, s[40:43], s54 offen lds\n" \
"s_add_u32 m0, s52, 13312\n" \
"s_add_u32 s54, s89, 81920\n" \
"buffer_load_dwordx4 v0, s[40:43], s54 offen lds\n" \
"s_add_u32 m0, s52, 14336\n" \
"s_add_u32 s54, s89, 98304\n" \
"buffer_load_dwordx4 v0, s[40:43], s54 offen lds\n" \
"s_add_u32 m0, s52, 15360\n" \
"s_add_u32 s54, s89, 114688\n" \
"buffer_load_dwordx4 v0, s[40:43], s54 offen lds\n" \
"s_mov_b32 s54, s99\n" \
".Lswo_z13_%=:\n" \
"s_sleep 1\n" \
"s_sub_u32 s54, s54, 1\n" \
"s_cbranch_scc0 .Lswo_z13_%=\n" \
"s_waitcnt vmcnt(36)\n" \
"ds_read_b64_tr_b16 a[16:17], v1 offset:16384\n" \
"ds_read_b64_tr_b16 a[18:19], v1 offset:18432\n" \
"ds_read_b64_tr_b16 a[144:145], v1 offset:16416\n" \
"ds_read_b64_tr_b16 a[146:147], v1 offset:18464\n" \
"ds_read_b64_tr_b16 v[144:145], v1 offset:16448\n" \
"ds_read_b64_tr_b16 v[146:147], v1 offset:18496\n" \
"ds_read_b64_tr_b16 v[32:33], v1 offset:16480\n" \
"ds_read_b64_tr_b16 v[34:35], v1 offset:18528\n" \
"s_waitcnt lgkmcnt(0)\n" \
"s_add_u32 m0, s52, 16384\n" \
"s_add_u32 s54, s89, 131072\n" \
"buffer_load_dwordx4 v0, s[40:43], s54 offen lds\n" \
"s_add_u32 m0, s52, 17408\n" \
"s_add_u32 s54, s89, 147456\n" \
"buffer_load_dwordx4 v0, s[40:43], s54 offen lds\n" \
"s_add_u32 m0, s52, 18432\n" \
"s_add_u32 s54, s89, 163840\n" \
"buffer_load_dwordx4 v0, s[40:43], s54 offen lds\n" \
"s_add_u32 m0, s52, 19456\n" \
"s_add_u32 s54, s89, 180224\n" \
"buffer_load_dwordx4 v0, s[40:43], s54 offen lds\n" \
"s_mov_b32 s54, s99\n" \
".Lswo_z14_%=:\n" \
"s_sleep 1\n" \
"s_sub_u32 s54, s54, 1\n" \
"s_cbranch_scc0 .Lswo_z14_%=\n" \
"s_waitcnt vmcnt(36)\n" \
"ds_read_b64_tr_b16 a[20:21], v1 offset:20480\n" \
"ds_read_b64_tr_b16 a[22:23], v1 offset:22528\n" \
"ds_read_b64_tr_b16 a[148:149], v1 offset:20512\n" \
"ds_read_b64_tr_b16 a[150:151], v1 offset:22560\n" \
"ds_read_b64_tr_b16 v[148:149], v1 offset:20544\n" \
"ds_read_b64_tr_b16 v[150:151], v1 offset:22592\n" \
"ds_read_b64_tr_b16 v[36:37], v1 offset:20576\n" \
"ds_read_b64_tr_b16 v[38:39], v1 offset:22624\n" \
"s_waitcnt lgkmcnt(0)\n" \
"s_add_u32 m0, s52, 20480\n" \
"s_add_u32 s54, s89, 196608\n" \
"buffer_load_dwordx4 v0, s[40:43], s54 offen lds\n" \
"s_add_u32 m0, s52, 21504\n" \
"s_add_u32 s54, s89, 212992\n" \
"buffer_load_dwordx4 v0, s[40:43], s54 offen lds\n" \
"s_add_u32 m0, s52, 22528\n" \
"s_add_u32 s54, s89, 229376\n" \
"buffer_load_dwordx4 v0, s[40:43], s54 offen lds\n" \
"s_add_u32 m0, s52, 23552\n" \
"s_add_u32 s54, s89, 245760\n" \
"buffer_load_dwordx4 v0, s[40:43], s54 offen lds\n" \
"s_mov_b32 s54, s99\n" \
".Lswo_z15_%=:\n" \
"s_sleep 1\n" \
"s_sub_u32 s54, s54, 1\n" \
"s_cbranch_scc0 .Lswo_z15_%=\n" \
"s_waitcnt vmcnt(36)\n" \
"ds_read_b64_tr_b16 a[24:25], v1 offset:24576\n" \
"ds_read_b64_tr_b16 a[26:27], v1 offset:26624\n" \
"ds_read_b64_tr_b16 a[152:153], v1 offset:24608\n" \
"ds_read_b64_tr_b16 a[154:155], v1 offset:26656\n" \
"ds_read_b64_tr_b16 v[152:153], v1 offset:24640\n" \
"ds_read_b64_tr_b16 v[154:155], v1 offset:26688\n" \
"ds_read_b64_tr_b16 v[40:41], v1 offset:24672\n" \
"ds_read_b64_tr_b16 v[42:43], v1 offset:26720\n" \
"s_waitcnt lgkmcnt(0)\n" \
"s_add_u32 m0, s52, 24576\n" \
"s_add_u32 s54, s90, 0\n" \
"buffer_load_dwordx4 v0, s[40:43], s54 offen lds\n" \
"s_add_u32 m0, s52, 25600\n" \
"s_add_u32 s54, s90, 16384\n" \
"buffer_load_dwordx4 v0, s[40:43], s54 offen lds\n" \
"s_add_u32 m0, s52, 26624\n" \
"s_add_u32 s54, s90, 32768\n" \
"buffer_load_dwordx4 v0, s[40:43], s54 offen lds\n" \
"s_add_u32 m0, s52, 27648\n" \
"s_add_u32 s54, s90, 49152\n" \
"buffer_load_dwordx4 v0, s[40:43], s54 offen lds\n" \
"s_mov_b32 s54, s99\n" \
".Lswo_z16_%=:\n" \
"s_sleep 1\n" \
"s_sub_u32 s54, s54, 1\n" \
"s_cbranch_scc0 .Lswo_z16_%=\n" \
"s_waitcnt vmcnt(36)\n" \
"ds_read_b64_tr_b16 a[28:29], v1 offset:28672\n" \
"ds_read_b64_tr_b16 a[30:31], v1 offset:30720\n" \
"ds_read_b64_tr_b16 a[156:157], v1 offset:28704\n" \
"ds_read_b64_tr_b16 a[158:159], v1 offset:30752\n" \
"ds_read_b64_tr_b16 v[156:157], v1 offset:28736\n" \
"ds_read_b64_tr_b16 v[158:159], v1 offset:30784\n" \
"ds_read_b64_tr_b16 v[44:45], v1 offset:28768\n" \
"ds_read_b64_tr_b16 v[46:47], v1 offset:30816\n" \
"s_waitcnt lgkmcnt(0)\n" \
"s_add_u32 m0, s52, 28672\n" \
"s_add_u32 s54, s90, 65536\n" \
"buffer_load_dwordx4 v0, s[40:43], s54 offen lds\n" \
"s_add_u32 m0, s52, 29696\n" \
"s_add_u32 s54, s90, 81920\n" \
"buffer_load_dwordx4 v0, s[40:43], s54 offen lds\n" \
"s_add_u32 m0, s52, 30720\n" \
"s_add_u32 s54, s90, 98304\n" \
"buffer_load_dwordx4 v0, s[40:43], s54 offen lds\n" \
"s_add_u32 m0, s52, 31744\n" \
"s_add_u32 s54, s90, 114688\n" \
"buffer_load_dwordx4 v0, s[40:43], s54 offen lds\n" \
"s_mov_b32 s54, s99\n" \
".Lswo_z17_%=:\n" \
"s_sleep 1\n" \
"s_sub_u32 s54, s54, 1\n" \
"s_cbranch_scc0 .Lswo_z17_%=\n" \
"s_waitcnt vmcnt(36)\n" \
"ds_read_b64_tr_b16 a[32:33], v9 offset:32768\n" \
"ds_read_b64_tr_b16 a[34:35], v9 offset:34816\n" \
"ds_read_b64_tr_b16 a[160:161], v9 offset:32800\n" \
"ds_read_b64_tr_b16 a[162:163], v9 offset:34848\n" \
"ds_read_b64_tr_b16 v[160:161], v9 offset:32832\n" \
"ds_read_b64_tr_b16 v[162:163], v9 offset:34880\n" \
"ds_read_b64_tr_b16 v[48:49], v9 offset:32864\n" \
"ds_read_b64_tr_b16 v[50:51], v9 offset:34912\n" \
"s_waitcnt lgkmcnt(0)\n" \
"s_add_u32 m0, s53, 32768\n" \
"s_add_u32 s54, s90, 131072\n" \
"buffer_load_dwordx4 v0, s[40:43], s54 offen lds\n" \
"s_add_u32 m0, s53, 33792\n" \
"s_add_u32 s54, s90, 147456\n" \
"buffer_load_dwordx4 v0, s[40:43], s54 offen lds\n" \
"s_add_u32 m0, s53, 34816\n" \
"s_add_u32 s54, s90, 163840\n" \
"buffer_load_dwordx4 v0, s[40:43], s54 offen lds\n" \
"s_add_u32 m0, s53, 35840\n" \
"s_add_u32 s54, s90, 180224\n" \
"buffer_load_dwordx4 v0, s[40:43], s54 offen lds\n" \
"s_mov_b32 s54, s99\n" \
".Lswo_z18_%=:\n" \
"s_sleep 1\n" \
"s_sub_u32 s54, s54, 1\n" \
"s_cbranch_scc0 .Lswo_z18_%=\n" \
"s_waitcnt vmcnt(36)\n" \
"ds_read_b64_tr_b16 a[36:37], v9 offset:36864\n" \
"ds_read_b64_tr_b16 a[38:39], v9 offset:38912\n" \
"ds_read_b64_tr_b16 a[164:165], v9 offset:36896\n" \
"ds_read_b64_tr_b16 a[166:167], v9 offset:38944\n" \
"ds_read_b64_tr_b16 v[164:165], v9 offset:36928\n" \
"ds_read_b64_tr_b16 v[166:167], v9 offset:38976\n" \
"ds_read_b64_tr_b16 v[52:53], v9 offset:36960\n" \
"ds_read_b64_tr_b16 v[54:55], v9 offset:39008\n" \
"s_waitcnt lgkmcnt(0)\n" \
"s_add_u32 m0, s53, 36864\n" \
"s_add_u32 s54, s90, 196608\n" \
"buffer_load_dwordx4 v0, s[40:43], s54 offen lds\n" \
"s_add_u32 m0, s53, 37888\n" \
"s_add_u32 s54, s90, 212992\n" \
"buffer_load_dwordx4 v0, s[40:43], s54 offen lds\n" \
"s_add_u32 m0, s53, 38912\n" \
"s_add_u32 s54, s90, 229376\n" \
"buffer_load_dwordx4 v0, s[40:43], s54 offen lds\n" \
"s_add_u32 m0, s53, 39936\n" \
"s_add_u32 s54, s90, 245760\n" \
"buffer_load_dwordx4 v0, s[40:43], s54 offen lds\n" \
"s_mov_b32 s54, s99\n" \
".Lswo_z19_%=:\n" \
"s_sleep 1\n" \
"s_sub_u32 s54, s54, 1\n" \
"s_cbranch_scc0 .Lswo_z19_%=\n" \
"s_waitcnt vmcnt(36)\n" \
"ds_read_b64_tr_b16 a[40:41], v1 offset:0\n" \
"ds_read_b64_tr_b16 a[42:43], v1 offset:2048\n" \
"ds_read_b64_tr_b16 a[168:169], v1 offset:32\n" \
"ds_read_b64_tr_b16 a[170:171], v1 offset:2080\n" \
"ds_read_b64_tr_b16 v[168:169], v1 offset:64\n" \
"ds_read_b64_tr_b16 v[170:171], v1 offset:2112\n" \
"ds_read_b64_tr_b16 v[56:57], v1 offset:96\n" \
"ds_read_b64_tr_b16 v[58:59], v1 offset:2144\n" \
"s_waitcnt lgkmcnt(0)\n" \
"s_add_u32 m0, s52, 0\n" \
"s_add_u32 s54, s91, 0\n" \
"buffer_load_dwordx4 v0, s[40:43], s54 offen lds\n" \
"s_add_u32 m0, s52, 1024\n" \
"s_add_u32 s54, s91, 16384\n" \
"buffer_load_dwordx4 v0, s[40:43], s54 offen lds\n" \
"s_add_u32 m0, s52, 2048\n" \
"s_add_u32 s54, s91, 32768\n" \
"buffer_load_dwordx4 v0, s[40:43], s54 offen lds\n" \
"s_add_u32 m0, s52, 3072\n" \
"s_add_u32 s54, s91, 49152\n" \
"buffer_load_dwordx4 v0, s[40:43], s54 offen lds\n" \
"s_mov_b32 s54, s99\n" \
".Lswo_z20_%=:\n" \
"s_sleep 1\n" \
"s_sub_u32 s54, s54, 1\n" \
"s_cbranch_scc0 .Lswo_z20_%=\n" \
"s_waitcnt vmcnt(36)\n" \
"ds_read_b64_tr_b16 a[44:45], v1 offset:4096\n" \
"ds_read_b64_tr_b16 a[46:47], v1 offset:6144\n" \
"ds_read_b64_tr_b16 a[172:173], v1 offset:4128\n" \
"ds_read_b64_tr_b16 a[174:175], v1 offset:6176\n" \
"ds_read_b64_tr_b16 v[172:173], v1 offset:4160\n" \
"ds_read_b64_tr_b16 v[174:175], v1 offset:6208\n" \
"ds_read_b64_tr_b16 v[60:61], v1 offset:4192\n" \
"ds_read_b64_tr_b16 v[62:63], v1 offset:6240\n" \
"s_waitcnt lgkmcnt(0)\n" \
"s_add_u32 m0, s52, 4096\n" \
"s_add_u32 s54, s91, 65536\n" \
"buffer_load_dwordx4 v0, s[40:43], s54 offen lds\n" \
"s_add_u32 m0, s52, 5120\n" \
"s_add_u32 s54, s91, 81920\n" \
"buffer_load_dwordx4 v0, s[40:43], s54 offen lds\n" \
"s_add_u32 m0, s52, 6144\n" \
"s_add_u32 s54, s91, 98304\n" \
"buffer_load_dwordx4 v0, s[40:43], s54 offen lds\n" \
"s_add_u32 m0, s52, 7168\n" \
"s_add_u32 s54, s91, 114688\n" \
"buffer_load_dwordx4 v0, s[40:43], s54 offen lds\n" \
"s_mov_b32 s54, s99\n" \
".Lswo_z21_%=:\n" \
"s_sleep 1\n" \
"s_sub_u32 s54, s54, 1\n" \
"s_cbranch_scc0 .Lswo_z21_%=\n" \
"s_waitcnt vmcnt(36)\n" \
"ds_read_b64_tr_b16 a[48:49], v1 offset:8192\n" \
"ds_read_b64_tr_b16 a[50:51], v1 offset:10240\n" \
"ds_read_b64_tr_b16 a[176:177], v1 offset:8224\n" \
"ds_read_b64_tr_b16 a[178:179], v1 offset:10272\n" \
"ds_read_b64_tr_b16 v[176:177], v1 offset:8256\n" \
"ds_read_b64_tr_b16 v[178:179], v1 offset:10304\n" \
"ds_read_b64_tr_b16 v[64:65], v1 offset:8288\n" \
"ds_read_b64_tr_b16 v[66:67], v1 offset:10336\n" \
"s_waitcnt lgkmcnt(0)\n" \
"s_add_u32 m0, s52, 8192\n" \
"s_add_u32 s54, s91, 131072\n" \
"buffer_load_dwordx4 v0, s[40:43], s54 offen lds\n" \
"s_add_u32 m0, s52, 9216\n" \
"s_add_u32 s54, s91, 147456\n" \
"buffer_load_dwordx4 v0, s[40:43], s54 offen lds\n" \
"s_add_u32 m0, s52, 10240\n" \
"s_add_u32 s54, s91, 163840\n" \
"buffer_load_dwordx4 v0, s[40:43], s54 offen lds\n" \
"s_add_u32 m0, s52, 11264\n" \
"s_add_u32 s54, s91, 180224\n" \
"buffer_load_dwordx4 v0, s[40:43], s54 offen lds\n" \
"s_mov_b32 s54, s99\n" \
".Lswo_z22_%=:\n" \
"s_sleep 1\n" \
"s_sub_u32 s54, s54, 1\n" \
"s_cbranch_scc0 .Lswo_z22_%=\n" \
"s_waitcnt vmcnt(36)\n" \
"ds_read_b64_tr_b16 a[52:53], v1 offset:12288\n" \
"ds_read_b64_tr_b16 a[54:55], v1 offset:14336\n" \
"ds_read_b64_tr_b16 a[180:181], v1 offset:12320\n" \
"ds_read_b64_tr_b16 a[182:183], v1 offset:14368\n" \
"ds_read_b64_tr_b16 v[180:181], v1 offset:12352\n" \
"ds_read_b64_tr_b16 v[182:183], v1 offset:14400\n" \
"ds_read_b64_tr_b16 v[68:69], v1 offset:12384\n" \
"ds_read_b64_tr_b16 v[70:71], v1 offset:14432\n" \
"s_waitcnt lgkmcnt(0)\n" \
"s_add_u32 m0, s52, 12288\n" \
"s_add_u32 s54, s91, 196608\n" \
"buffer_load_dwordx4 v0, s[40:43], s54 offen lds\n" \
"s_add_u32 m0, s52, 13312\n" \
"s_add_u32 s54, s91, 212992\n" \
"buffer_load_dwordx4 v0, s[40:43], s54 offen lds\n" \
"s_add_u32 m0, s52, 14336\n" \
"s_add_u32 s54, s91, 229376\n" \
"buffer_load_dwordx4 v0, s[40:43], s54 offen lds\n" \
"s_add_u32 m0, s52, 15360\n" \
"s_add_u32 s54, s91, 245760\n" \
"buffer_load_dwordx4 v0, s[40:43], s54 offen lds\n" \
"s_mov_b32 s54, s99\n" \
".Lswo_z23_%=:\n" \
"s_sleep 1\n" \
"s_sub_u32 s54, s54, 1\n" \
"s_cbranch_scc0 .Lswo_z23_%=\n" \
"s_waitcnt vmcnt(36)\n" \
"ds_read_b64_tr_b16 a[56:57], v1 offset:16384\n" \
"ds_read_b64_tr_b16 a[58:59], v1 offset:18432\n" \
"ds_read_b64_tr_b16 a[184:185], v1 offset:16416\n" \
"ds_read_b64_tr_b16 a[186:187], v1 offset:18464\n" \
"ds_read_b64_tr_b16 v[184:185], v1 offset:16448\n" \
"ds_read_b64_tr_b16 v[186:187], v1 offset:18496\n" \
"ds_read_b64_tr_b16 v[72:73], v1 offset:16480\n" \
"ds_read_b64_tr_b16 v[74:75], v1 offset:18528\n" \
"s_waitcnt lgkmcnt(0)\n" \
"s_add_u32 m0, s52, 16384\n" \
"s_add_u32 s54, s92, 0\n" \
"buffer_load_dwordx4 v0, s[40:43], s54 offen lds\n" \
"s_add_u32 m0, s52, 17408\n" \
"s_add_u32 s54, s92, 16384\n" \
"buffer_load_dwordx4 v0, s[40:43], s54 offen lds\n" \
"s_add_u32 m0, s52, 18432\n" \
"s_add_u32 s54, s92, 32768\n" \
"buffer_load_dwordx4 v0, s[40:43], s54 offen lds\n" \
"s_add_u32 m0, s52, 19456\n" \
"s_add_u32 s54, s92, 49152\n" \
"buffer_load_dwordx4 v0, s[40:43], s54 offen lds\n" \
"s_mov_b32 s54, s99\n" \
".Lswo_z24_%=:\n" \
"s_sleep 1\n" \
"s_sub_u32 s54, s54, 1\n" \
"s_cbranch_scc0 .Lswo_z24_%=\n" \
"s_waitcnt vmcnt(36)\n" \
"ds_read_b64_tr_b16 a[60:61], v1 offset:20480\n" \
"ds_read_b64_tr_b16 a[62:63], v1 offset:22528\n" \
"ds_read_b64_tr_b16 a[188:189], v1 offset:20512\n" \
"ds_read_b64_tr_b16 a[190:191], v1 offset:22560\n" \
"ds_read_b64_tr_b16 v[188:189], v1 offset:20544\n" \
"ds_read_b64_tr_b16 v[190:191], v1 offset:22592\n" \
"ds_read_b64_tr_b16 v[76:77], v1 offset:20576\n" \
"ds_read_b64_tr_b16 v[78:79], v1 offset:22624\n" \
"s_waitcnt lgkmcnt(0)\n" \
"s_add_u32 m0, s52, 20480\n" \
"s_add_u32 s54, s92, 65536\n" \
"buffer_load_dwordx4 v0, s[40:43], s54 offen lds\n" \
"s_add_u32 m0, s52, 21504\n" \
"s_add_u32 s54, s92, 81920\n" \
"buffer_load_dwordx4 v0, s[40:43], s54 offen lds\n" \
"s_add_u32 m0, s52, 22528\n" \
"s_add_u32 s54, s92, 98304\n" \
"buffer_load_dwordx4 v0, s[40:43], s54 offen lds\n" \
"s_add_u32 m0, s52, 23552\n" \
"s_add_u32 s54, s92, 114688\n" \
"buffer_load_dwordx4 v0, s[40:43], s54 offen lds\n" \
"s_mov_b32 s54, s99\n" \
".Lswo_z25_%=:\n" \
"s_sleep 1\n" \
"s_sub_u32 s54, s54, 1\n" \
"s_cbranch_scc0 .Lswo_z25_%=\n" \
"s_waitcnt vmcnt(36)\n" \
"ds_read_b64_tr_b16 a[64:65], v1 offset:24576\n" \
"ds_read_b64_tr_b16 a[66:67], v1 offset:26624\n" \
"ds_read_b64_tr_b16 a[192:193], v1 offset:24608\n" \
"ds_read_b64_tr_b16 a[194:195], v1 offset:26656\n" \
"ds_read_b64_tr_b16 v[192:193], v1 offset:24640\n" \
"ds_read_b64_tr_b16 v[194:195], v1 offset:26688\n" \
"ds_read_b64_tr_b16 v[80:81], v1 offset:24672\n" \
"ds_read_b64_tr_b16 v[82:83], v1 offset:26720\n" \
"s_waitcnt lgkmcnt(0)\n" \
"s_add_u32 m0, s52, 24576\n" \
"s_add_u32 s54, s92, 131072\n" \
"buffer_load_dwordx4 v0, s[40:43], s54 offen lds\n" \
"s_add_u32 m0, s52, 25600\n" \
"s_add_u32 s54, s92, 147456\n" \
"buffer_load_dwordx4 v0, s[40:43], s54 offen lds\n" \
"s_add_u32 m0, s52, 26624\n" \
"s_add_u32 s54, s92, 163840\n" \
"buffer_load_dwordx4 v0, s[40:43], s54 offen lds\n" \
"s_add_u32 m0, s52, 27648\n" \
"s_add_u32 s54, s92, 180224\n" \
"buffer_load_dwordx4 v0, s[40:43], s54 offen lds\n" \
"s_mov_b32 s54, s99\n" \
".Lswo_z26_%=:\n" \
"s_sleep 1\n" \
"s_sub_u32 s54, s54, 1\n" \
"s_cbranch_scc0 .Lswo_z26_%=\n" \
"s_waitcnt vmcnt(36)\n" \
"ds_read_b64_tr_b16 a[68:69], v1 offset:28672\n" \
"ds_read_b64_tr_b16 a[70:71], v1 offset:30720\n" \
"ds_read_b64_tr_b16 a[196:197], v1 offset:28704\n" \
"ds_read_b64_tr_b16 a[198:199], v1 offset:30752\n" \
"ds_read_b64_tr_b16 v[196:197], v1 offset:28736\n" \
"ds_read_b64_tr_b16 v[198:199], v1 offset:30784\n" \
"ds_read_b64_tr_b16 v[84:85], v1 offset:28768\n" \
"ds_read_b64_tr_b16 v[86:87], v1 offset:30816\n" \
"s_waitcnt lgkmcnt(0)\n" \
"s_add_u32 m0, s52, 28672\n" \
"s_add_u32 s54, s92, 196608\n" \
"buffer_load_dwordx4 v0, s[40:43], s54 offen lds\n" \
"s_add_u32 m0, s52, 29696\n" \
"s_add_u32 s54, s92, 212992\n" \
"buffer_load_dwordx4 v0, s[40:43], s54 offen lds\n" \
"s_add_u32 m0, s52, 30720\n" \
"s_add_u32 s54, s92, 229376\n" \
"buffer_load_dwordx4 v0, s[40:43], s54 offen lds\n" \
"s_add_u32 m0, s52, 31744\n" \
"s_add_u32 s54, s92, 245760\n" \
"buffer_load_dwordx4 v0, s[40:43], s54 offen lds\n" \
"s_mov_b32 s54, s99\n" \
".Lswo_z27_%=:\n" \
"s_sleep 1\n" \
"s_sub_u32 s54, s54, 1\n" \
"s_cbranch_scc0 .Lswo_z27_%=\n" \
"s_waitcnt vmcnt(36)\n" \
"ds_read_b64_tr_b16 a[72:73], v9 offset:32768\n" \
"ds_read_b64_tr_b16 a[74:75], v9 offset:34816\n" \
"ds_read_b64_tr_b16 a[200:201], v9 offset:32800\n" \
"ds_read_b64_tr_b16 a[202:203], v9 offset:34848\n" \
"ds_read_b64_tr_b16 v[200:201], v9 offset:32832\n" \
"ds_read_b64_tr_b16 v[202:203], v9 offset:34880\n" \
"ds_read_b64_tr_b16 v[88:89], v9 offset:32864\n" \
"ds_read_b64_tr_b16 v[90:91], v9 offset:34912\n" \
"s_waitcnt lgkmcnt(0)\n" \
"s_add_u32 m0, s53, 32768\n" \
"s_add_u32 s54, s93, 0\n" \
"buffer_load_dwordx4 v0, s[40:43], s54 offen lds\n" \
"s_add_u32 m0, s53, 33792\n" \
"s_add_u32 s54, s93, 16384\n" \
"buffer_load_dwordx4 v0, s[40:43], s54 offen lds\n" \
"s_add_u32 m0, s53, 34816\n" \
"s_add_u32 s54, s93, 32768\n" \
"buffer_load_dwordx4 v0, s[40:43], s54 offen lds\n" \
"s_add_u32 m0, s53, 35840\n" \
"s_add_u32 s54, s93, 49152\n" \
"buffer_load_dwordx4 v0, s[40:43], s54 offen lds\n" \
"s_mov_b32 s54, s99\n" \
".Lswo_z28_%=:\n" \
"s_sleep 1\n" \
"s_sub_u32 s54, s54, 1\n" \
"s_cbranch_scc0 .Lswo_z28_%=\n" \
"s_waitcnt vmcnt(36)\n" \
"ds_read_b64_tr_b16 a[76:77], v9 offset:36864\n" \
"ds_read_b64_tr_b16 a[78:79], v9 offset:38912\n" \
"ds_read_b64_tr_b16 a[204:205], v9 offset:36896\n" \
"ds_read_b64_tr_b16 a[206:207], v9 offset:38944\n" \
"ds_read_b64_tr_b16 v[204:205], v9 offset:36928\n" \
"ds_read_b64_tr_b16 v[206:207], v9 offset:38976\n" \
"ds_read_b64_tr_b16 v[92:93], v9 offset:36960\n" \
"ds_read_b64_tr_b16 v[94:95], v9 offset:39008\n" \
"s_waitcnt lgkmcnt(0)\n" \
"s_add_u32 m0, s53, 36864\n" \
"s_add_u32 s54, s93, 65536\n" \
"buffer_load_dwordx4 v0, s[40:43], s54 offen lds\n" \
"s_add_u32 m0, s53, 37888\n" \
"s_add_u32 s54, s93, 81920\n" \
"buffer_load_dwordx4 v0, s[40:43], s54 offen lds\n" \
"s_add_u32 m0, s53, 38912\n" \
"s_add_u32 s54, s93, 98304\n" \
"buffer_load_dwordx4 v0, s[40:43], s54 offen lds\n" \
"s_add_u32 m0, s53, 39936\n" \
"s_add_u32 s54, s93, 114688\n" \
"buffer_load_dwordx4 v0, s[40:43], s54 offen lds\n" \
"s_mov_b32 s54, s99\n" \
".Lswo_z29_%=:\n" \
"s_sleep 1\n" \
"s_sub_u32 s54, s54, 1\n" \
"s_cbranch_scc0 .Lswo_z29_%=\n" \
"s_waitcnt vmcnt(36)\n" \
"ds_read_b64_tr_b16 a[80:81], v1 offset:0\n" \
"ds_read_b64_tr_b16 a[82:83], v1 offset:2048\n" \
"ds_read_b64_tr_b16 a[208:209], v1 offset:32\n" \
"ds_read_b64_tr_b16 a[210:211], v1 offset:2080\n" \
"ds_read_b64_tr_b16 v[208:209], v1 offset:64\n" \
"ds_read_b64_tr_b16 v[210:211], v1 offset:2112\n" \
"ds_read_b64_tr_b16 v[96:97], v1 offset:96\n" \
"ds_read_b64_tr_b16 v[98:99], v1 offset:2144\n" \
"s_waitcnt lgkmcnt(0)\n" \
"s_add_u32 m0, s52, 0\n" \
"s_add_u32 s54, s93, 131072\n" \
"buffer_load_dwordx4 v0, s[40:43], s54 offen lds\n" \
"s_add_u32 m0, s52, 1024\n" \
"s_add_u32 s54, s93, 147456\n" \
"buffer_load_dwordx4 v0, s[40:43], s54 offen lds\n" \
"s_add_u32 m0, s52, 2048\n" \
"s_add_u32 s54, s93, 163840\n" \
"buffer_load_dwordx4 v0, s[40:43], s54 offen lds\n" \
"s_add_u32 m0, s52, 3072\n" \
"s_add_u32 s54, s93, 180224\n" \
"buffer_load_dwordx4 v0, s[40:43], s54 offen lds\n" \
"s_mov_b32 s54, s99\n" \
".Lswo_z30_%=:\n" \
"s_sleep 1\n" \
"s_sub_u32 s54, s54, 1\n" \
"s_cbranch_scc0 .Lswo_z30_%=\n" \
"s_waitcnt vmcnt(36)\n" \
"ds_read_b64_tr_b16 a[84:85], v1 offset:4096\n" \
"ds_read_b64_tr_b16 a[86:87], v1 offset:6144\n" \
"ds_read_b64_tr_b16 a[212:213], v1 offset:4128\n" \
"ds_read_b64_tr_b16 a[214:215], v1 offset:6176\n" \
"ds_read_b64_tr_b16 v[212:213], v1 offset:4160\n" \
"ds_read_b64_tr_b16 v[214:215], v1 offset:6208\n" \
"ds_read_b64_tr_b16 v[100:101], v1 offset:4192\n" \
"ds_read_b64_tr_b16 v[102:103], v1 offset:6240\n" \
"s_waitcnt lgkmcnt(0)\n" \
"s_add_u32 m0, s52, 4096\n" \
"s_add_u32 s54, s93, 196608\n" \
"buffer_load_dwordx4 v0, s[40:43], s54 offen lds\n" \
"s_add_u32 m0, s52, 5120\n" \
"s_add_u32 s54, s93, 212992\n" \
"buffer_load_dwordx4 v0, s[40:43], s54 offen lds\n" \
"s_add_u32 m0, s52, 6144\n" \
"s_add_u32 s54, s93, 229376\n" \
"buffer_load_dwordx4 v0, s[40:43], s54 offen lds\n" \
"s_add_u32 m0, s52, 7168\n" \
"s_add_u32 s54, s93, 245760\n" \
"buffer_load_dwordx4 v0, s[40:43], s54 offen lds\n" \
"s_mov_b32 s54, s99\n" \
".Lswo_z31_%=:\n" \
"s_sleep 1\n" \
"s_sub_u32 s54, s54, 1\n" \
"s_cbranch_scc0 .Lswo_z31_%=\n" \
"s_waitcnt vmcnt(36)\n" \
"ds_read_b64_tr_b16 a[88:89], v1 offset:8192\n" \
"ds_read_b64_tr_b16 a[90:91], v1 offset:10240\n" \
"ds_read_b64_tr_b16 a[216:217], v1 offset:8224\n" \
"ds_read_b64_tr_b16 a[218:219], v1 offset:10272\n" \
"ds_read_b64_tr_b16 v[216:217], v1 offset:8256\n" \
"ds_read_b64_tr_b16 v[218:219], v1 offset:10304\n" \
"ds_read_b64_tr_b16 v[104:105], v1 offset:8288\n" \
"ds_read_b64_tr_b16 v[106:107], v1 offset:10336\n" \
"s_waitcnt lgkmcnt(0)\n" \
"ds_write_b128 v2, v[16:19] offset:8192\n" \
"ds_write_b128 v2, v[20:23] offset:9216\n" \
"ds_write_b128 v2, v[24:27] offset:10240\n" \
"ds_write_b128 v2, v[28:31] offset:11264\n" \
"s_waitcnt vmcnt(32)\n" \
"ds_read_b64_tr_b16 a[92:93], v1 offset:12288\n" \
"ds_read_b64_tr_b16 a[94:95], v1 offset:14336\n" \
"ds_read_b64_tr_b16 a[220:221], v1 offset:12320\n" \
"ds_read_b64_tr_b16 a[222:223], v1 offset:14368\n" \
"ds_read_b64_tr_b16 v[220:221], v1 offset:12352\n" \
"ds_read_b64_tr_b16 v[222:223], v1 offset:14400\n" \
"ds_read_b64_tr_b16 v[108:109], v1 offset:12384\n" \
"ds_read_b64_tr_b16 v[110:111], v1 offset:14432\n" \
"s_waitcnt lgkmcnt(0)\n" \
"ds_write_b128 v2, v[32:35] offset:12288\n" \
"ds_write_b128 v2, v[36:39] offset:13312\n" \
"ds_write_b128 v2, v[40:43] offset:14336\n" \
"ds_write_b128 v2, v[44:47] offset:15360\n" \
"s_waitcnt vmcnt(28)\n" \
"ds_read_b64_tr_b16 a[96:97], v1 offset:16384\n" \
"ds_read_b64_tr_b16 a[98:99], v1 offset:18432\n" \
"ds_read_b64_tr_b16 a[224:225], v1 offset:16416\n" \
"ds_read_b64_tr_b16 a[226:227], v1 offset:18464\n" \
"ds_read_b64_tr_b16 v[224:225], v1 offset:16448\n" \
"ds_read_b64_tr_b16 v[226:227], v1 offset:18496\n" \
"ds_read_b64_tr_b16 v[16:17], v1 offset:16480\n" \
"ds_read_b64_tr_b16 v[18:19], v1 offset:18528\n" \
"s_waitcnt lgkmcnt(0)\n" \
"ds_write_b128 v2, v[48:51] offset:16384\n" \
"ds_write_b128 v2, v[52:55] offset:17408\n" \
"ds_write_b128 v2, v[56:59] offset:18432\n" \
"ds_write_b128 v2, v[60:63] offset:19456\n" \
"s_waitcnt vmcnt(24)\n" \
"ds_read_b64_tr_b16 a[100:101], v1 offset:20480\n" \
"ds_read_b64_tr_b16 a[102:103], v1 offset:22528\n" \
"ds_read_b64_tr_b16 a[228:229], v1 offset:20512\n" \
"ds_read_b64_tr_b16 a[230:231], v1 offset:22560\n" \
"ds_read_b64_tr_b16 v[228:229], v1 offset:20544\n" \
"ds_read_b64_tr_b16 v[230:231], v1 offset:22592\n" \
"ds_read_b64_tr_b16 v[20:21], v1 offset:20576\n" \
"ds_read_b64_tr_b16 v[22:23], v1 offset:22624\n" \
"s_waitcnt lgkmcnt(0)\n" \
"ds_write_b128 v2, v[64:67] offset:20480\n" \
"ds_write_b128 v2, v[68:71] offset:21504\n" \
"ds_write_b128 v2, v[72:75] offset:22528\n" \
"ds_write_b128 v2, v[76:79] offset:23552\n" \
"s_waitcnt vmcnt(20)\n" \
"ds_read_b64_tr_b16 a[104:105], v1 offset:24576\n" \
"ds_read_b64_tr_b16 a[106:107], v1 offset:26624\n" \
"ds_read_b64_tr_b16 a[232:233], v1 offset:24608\n" \
"ds_read_b64_tr_b16 a[234:235], v1 offset:26656\n" \
"ds_read_b64_tr_b16 v[232:233], v1 offset:24640\n" \
"ds_read_b64_tr_b16 v[234:235], v1 offset:26688\n" \
"ds_read_b64_tr_b16 v[24:25], v1 offset:24672\n" \
"ds_read_b64_tr_b16 v[26:27], v1 offset:26720\n" \
"s_waitcnt lgkmcnt(0)\n" \
"ds_write_b128 v2, v[80:83] offset:24576\n" \
"ds_write_b128 v2, v[84:87] offset:25600\n" \
"ds_write_b128 v2, v[88:91] offset:26624\n" \
"ds_write_b128 v2, v[92:95] offset:27648\n" \
"s_waitcnt vmcnt(16)\n" \
"ds_read_b64_tr_b16 a[108:109], v1 offset:28672\n" \
"ds_read_b64_tr_b16 a[110:111], v1 offset:30720\n" \
"ds_read_b64_tr_b16 a[236:237], v1 offset:28704\n" \
"ds_read_b64_tr_b16 a[238:239], v1 offset:30752\n" \
"ds_read_b64_tr_b16 v[236:237], v1 offset:28736\n" \
"ds_read_b64_tr_b16 v[238:239], v1 offset:30784\n" \
"ds_read_b64_tr_b16 v[28:29], v1 offset:28768\n" \
"ds_read_b64_tr_b16 v[30:31], v1 offset:30816\n" \
"s_waitcnt lgkmcnt(0)\n" \
"ds_write_b128 v2, v[96:99] offset:28672\n" \
"ds_write_b128 v2, v[100:103] offset:29696\n" \
"ds_write_b128 v2, v[104:107] offset:30720\n" \
"ds_write_b128 v2, v[108:111] offset:31744\n" \
"s_waitcnt vmcnt(12)\n" \
"ds_read_b64_tr_b16 a[112:113], v9 offset:32768\n" \
"ds_read_b64_tr_b16 a[114:115], v9 offset:34816\n" \
"ds_read_b64_tr_b16 a[240:241], v9 offset:32800\n" \
"ds_read_b64_tr_b16 a[242:243], v9 offset:34848\n" \
"ds_read_b64_tr_b16 v[240:241], v9 offset:32832\n" \
"ds_read_b64_tr_b16 v[242:243], v9 offset:34880\n" \
"ds_read_b64_tr_b16 v[32:33], v9 offset:32864\n" \
"ds_read_b64_tr_b16 v[34:35], v9 offset:34912\n" \
"s_waitcnt lgkmcnt(0)\n" \
"s_waitcnt vmcnt(8)\n" \
"ds_read_b64_tr_b16 a[116:117], v9 offset:36864\n" \
"ds_read_b64_tr_b16 a[118:119], v9 offset:38912\n" \
"ds_read_b64_tr_b16 a[244:245], v9 offset:36896\n" \
"ds_read_b64_tr_b16 a[246:247], v9 offset:38944\n" \
"ds_read_b64_tr_b16 v[244:245], v9 offset:36928\n" \
"ds_read_b64_tr_b16 v[246:247], v9 offset:38976\n" \
"ds_read_b64_tr_b16 v[36:37], v9 offset:36960\n" \
"ds_read_b64_tr_b16 v[38:39], v9 offset:39008\n" \
"s_waitcnt lgkmcnt(0)\n" \
"s_waitcnt vmcnt(4)\n" \
"ds_read_b64_tr_b16 a[120:121], v1 offset:0\n" \
"ds_read_b64_tr_b16 a[122:123], v1 offset:2048\n" \
"ds_read_b64_tr_b16 a[248:249], v1 offset:32\n" \
"ds_read_b64_tr_b16 a[250:251], v1 offset:2080\n" \
"ds_read_b64_tr_b16 v[248:249], v1 offset:64\n" \
"ds_read_b64_tr_b16 v[250:251], v1 offset:2112\n" \
"ds_read_b64_tr_b16 v[40:41], v1 offset:96\n" \
"ds_read_b64_tr_b16 v[42:43], v1 offset:2144\n" \
"s_waitcnt lgkmcnt(0)\n" \
"ds_write_b128 v2, v[16:19] offset:0\n" \
"ds_write_b128 v2, v[20:23] offset:1024\n" \
"ds_write_b128 v2, v[24:27] offset:2048\n" \
"ds_write_b128 v2, v[28:31] offset:3072\n" \
"s_waitcnt vmcnt(0)\n" \
"ds_read_b64_tr_b16 a[124:125], v1 offset:4096\n" \
"ds_read_b64_tr_b16 a[126:127], v1 offset:6144\n" \
"ds_read_b64_tr_b16 a[252:253], v1 offset:4128\n" \
"ds_read_b64_tr_b16 a[254:255], v1 offset:6176\n" \
"ds_read_b64_tr_b16 v[252:253], v1 offset:4160\n" \
"ds_read_b64_tr_b16 v[254:255], v1 offset:6208\n" \
"ds_read_b64_tr_b16 v[44:45], v1 offset:4192\n" \
"ds_read_b64_tr_b16 v[46:47], v1 offset:6240\n" \
"s_waitcnt lgkmcnt(0)\n" \
"ds_write_b128 v2, v[32:35] offset:4096\n" \
"ds_write_b128 v2, v[36:39] offset:5120\n" \
"ds_write_b128 v2, v[40:43] offset:6144\n" \
"ds_write_b128 v2, v[44:47] offset:7168\n" \
"s_waitcnt vmcnt(0) lgkmcnt(0)\n" \
"s_barrier\n" \
"s_mov_b32 s54, s85\n" \
"s_mul_i32 s86, s54, 532480\n" \
"s_add_u32 s54, s54, 1\n" \
"s_cmp_eq_u32 s54, 8\n" \
"s_cselect_b32 s54, 0, s54\n" \
"s_mul_i32 s87, s54, 532480\n" \
"s_add_u32 s54, s54, 1\n" \
"s_cmp_eq_u32 s54, 8\n" \
"s_cselect_b32 s54, 0, s54\n" \
"s_mul_i32 s88, s54, 532480\n" \
"s_add_u32 s54, s54, 1\n" \
"s_cmp_eq_u32 s54, 8\n" \
"s_cselect_b32 s54, 0, s54\n" \
"s_mul_i32 s89, s54, 532480\n" \
"s_add_u32 s54, s54, 1\n" \
"s_cmp_eq_u32 s54, 8\n" \
"s_cselect_b32 s54, 0, s54\n" \
"s_mul_i32 s90, s54, 532480\n" \
"s_add_u32 s54, s54, 1\n" \
"s_cmp_eq_u32 s54, 8\n" \
"s_cselect_b32 s54, 0, s54\n" \
"s_mul_i32 s91, s54, 532480\n" \
"s_add_u32 s54, s54, 1\n" \
"s_cmp_eq_u32 s54, 8\n" \
"s_cselect_b32 s54, 0, s54\n" \
"s_mul_i32 s92, s54, 532480\n" \
"s_add_u32 s54, s54, 1\n" \
"s_cmp_eq_u32 s54, 8\n" \
"s_cselect_b32 s54, 0, s54\n" \
"s_mul_i32 s93, s54, 532480\n" \
"s_add_u32 s54, s54, 1\n" \
"s_cmp_eq_u32 s54, 8\n" \
"s_cselect_b32 s54, 0, s54\n" \
"s_mov_b32 s55, 0\n" \
"s_mov_b32 s60, 0\n" \
".Lswo_prime_%=:\n" \
"s_lshr_b32 s59, s44, 1\n" \
"s_add_u32 s59, s59, 1\n" \
"s_mul_i32 s56, s44, 4096\n" \
"buffer_load_dword v11, v4, s[36:39], 0 offen sc1\n" \
"s_waitcnt vmcnt(0)\n" \
"v_cmp_gt_u32_e64 s[62:63], s59, v11\n" \
"s_nop 4\n" \
"s_cmp_lg_u64 s[62:63], 0\n" \
"s_cbranch_scc1 .Lswo_w_%=\n" \
".Lswo_wk_%=:\n" \
"s_mov_b32 s78, 0x70000000\n" \
"s_mov_b32 s79, 0x70000000\n" \
"s_mov_b32 s81, 1\n" \
"s_mov_b32 s84, 0\n" \
"v_mov_b32 v10, 0\n" \
"s_waitcnt vmcnt(0) lgkmcnt(0)\n" \
"s_add_u32 s57, s56, s86\n" \
"buffer_load_dwordx4 v[16:19], v3, s[36:39], s57 offen offset:0 sc1\n" \
"buffer_load_dwordx4 v[20:23], v3, s[36:39], s57 offen offset:1024 sc1\n" \
"buffer_load_dwordx4 v[24:27], v3, s[36:39], s57 offen offset:2048 sc1\n" \
"buffer_load_dwordx4 v[28:31], v3, s[36:39], s57 offen offset:3072 sc1\n" \
"s_add_u32 s57, s56, s87\n" \
"buffer_load_dwordx4 v[32:35], v3, s[36:39], s57 offen offset:0 sc1\n" \
"buffer_load_dwordx4 v[36:39], v3, s[36:39], s57 offen offset:1024 sc1\n" \
"buffer_load_dwordx4 v[40:43], v3, s[36:39], s57 offen offset:2048 sc1\n" \
"buffer_load_dwordx4 v[44:47], v3, s[36:39], s57 offen offset:3072 sc1\n" \
"s_add_u32 s57, s56, s88\n" \
"buffer_load_dwordx4 v[48:51], v3, s[36:39], s57 offen offset:0 sc1\n" \
"buffer_load_dwordx4 v[52:55], v3, s[36:39], s57 offen offset:1024 sc1\n" \
"buffer_load_dwordx4 v[56:59], v3, s[36:39], s57 offen offset:2048 sc1\n" \
"buffer_load_dwordx4 v[60:63], v3, s[36:39], s57 offen offset:3072 sc1\n" \
"s_add_u32 s57, s56, s89\n" \
"buffer_load_dwordx4 v[64:67], v3, s[36:39], s57 offen offset:0 sc1\n" \
"buffer_load_dwordx4 v[68:71], v3, s[36:39], s57 offen offset:1024 sc1\n" \
"buffer_load_dwordx4 v[72:75], v3, s[36:39], s57 offen offset:2048 sc1\n" \
"buffer_load_dwordx4 v[76:79], v3, s[36:39], s57 offen offset:3072 sc1\n" \
"ds_read_b128 v[80:83], v2 offset:8192\n" \
"ds_read_b128 v[84:87], v2 offset:9216\n" \
"ds_read_b128 v[88:91], v2 offset:10240\n" \
"s_waitcnt lgkmcnt(0)\n" \
"v_mov_b32 v13, 0x7ffffff0\n" \
"buffer_store_dword v13, v13, s[36:39], 0 offen\n" \
"buffer_store_dword v13, v13, s[36:39], 0 offen\n" \
"buffer_store_dword v13, v13, s[36:39], 0 offen\n" \
"s_waitcnt vmcnt(18)\n" \
"s_nop 7\n" \
"s_nop 7\n" \
".p2align 6\n" \
".Lswo_loop_%=:\n" \
"v_mfma_f32_16x16x32_bf16 v[96:99], a[0:3], v[16:19], 0\n" \
"ds_write_b128 v7, v[104:107] offset:2048\n" \
"v_mfma_f32_16x16x32_bf16 v[100:103], a[128:131], v[16:19], 0\n" \
"ds_write_b128 v7, v[108:111] offset:3072\n" \
"ds_read_b128 v[92:95], v2 offset:11264\n" \
"v_mfma_f32_16x16x32_bf16 v[104:107], v[128:131], v[16:19], 0\n" \
"s_waitcnt vmcnt(17) lgkmcnt(7)\n" \
"v_mfma_f32_16x16x32_bf16 v[108:111], v[80:83], v[16:19], 0\n" \
"s_add_u32 s57, s56, s90\n" \
"buffer_load_dwordx4 v[16:19], v3, s[36:39], s57 offen offset:0 sc1\n" \
"v_mfma_f32_16x16x32_bf16 v[96:99], a[4:7], v[20:23], v[96:99]\n" \
"v_readfirstlane_b32 s96, v9\n" \
"v_mfma_f32_16x16x32_bf16 v[100:103], a[132:135], v[20:23], v[100:103]\n" \
"ds_read_b128 v[80:83], v2 offset:12288\n" \
"v_mfma_f32_16x16x32_bf16 v[104:107], v[132:135], v[20:23], v[104:107]\n" \
"s_waitcnt vmcnt(17) lgkmcnt(7)\n" \
"v_mfma_f32_16x16x32_bf16 v[108:111], v[84:87], v[20:23], v[108:111]\n" \
"buffer_load_dwordx4 v[20:23], v3, s[36:39], s57 offen offset:1024 sc1\n" \
"v_mfma_f32_16x16x32_bf16 v[96:99], a[8:11], v[24:27], v[96:99]\n" \
"s_waitcnt lgkmcnt(2)\n" \
"s_cmp_lg_u32 s81, 0\n" \
"s_cbranch_scc1 .Lswo_nb_%=\n" \
"s_barrier\n" \
".Lswo_nb_%=:\n" \
"v_mfma_f32_16x16x32_bf16 v[100:103], a[136:139], v[24:27], v[100:103]\n" \
"ds_read_b128 v[84:87], v2 offset:13312\n" \
"v_xor_b32_e64 v7, s84, v7\n" \
"v_mfma_f32_16x16x32_bf16 v[104:107], v[136:139], v[24:27], v[104:107]\n" \
"s_waitcnt vmcnt(17)\n" \
"v_mfma_f32_16x16x32_bf16 v[108:111], v[88:91], v[24:27], v[108:111]\n" \
"buffer_load_dwordx4 v[24:27], v3, s[36:39], s57 offen offset:2048 sc1\n" \
"v_mfma_f32_16x16x32_bf16 v[96:99], a[12:15], v[28:31], v[96:99]\n" \
"s_add_u32 s76, s78, 0\n" \
"s_add_u32 s80, s78, 4194304\n" \
"s_and_b32 s96, s96, 2\n" \
"v_mfma_f32_16x16x32_bf16 v[100:103], a[140:143], v[28:31], v[100:103]\n" \
"ds_read_b128 v[88:91], v2 offset:14336\n" \
"s_cselect_b32 s76, 0x7ffffff0, s76\n" \
"s_cselect_b32 s77, s78, 0x7ffffff0\n" \
"v_mfma_f32_16x16x32_bf16 v[104:107], v[140:143], v[28:31], v[104:107]\n" \
"s_waitcnt vmcnt(17) lgkmcnt(3)\n" \
"s_cselect_b32 s80, s80, 0x7ffffff0\n" \
"s_cselect_b32 s94, 0x7ffffff0, s79\n" \
"v_mfma_f32_16x16x32_bf16 v[108:111], v[92:95], v[28:31], v[108:111]\n" \
"buffer_load_dwordx4 v[28:31], v3, s[36:39], s57 offen offset:3072 sc1\n" \
"v_mfma_f32_16x16x32_bf16 v[96:99], a[16:19], v[32:35], v[96:99]\n" \
"s_cmp_lt_i32 s44, s50\n" \
"s_cselect_b32 s82, s45, s47\n" \
"v_mfma_f32_16x16x32_bf16 v[100:103], a[144:147], v[32:35], v[100:103]\n" \
"ds_read_b128 v[92:95], v2 offset:15360\n" \
"s_add_u32 s82, s82, s44\n" \
"v_mfma_f32_16x16x32_bf16 v[104:107], v[144:147], v[32:35], v[104:107]\n" \
"s_waitcnt vmcnt(17) lgkmcnt(3)\n" \
"s_lshr_b32 s59, s82, 1\n" \
"s_add_u32 s59, s59, 1\n" \
"v_mfma_f32_16x16x32_bf16 v[108:111], v[80:83], v[32:35], v[108:111]\n" \
"s_add_u32 s57, s56, s91\n" \
"buffer_load_dwordx4 v[32:35], v3, s[36:39], s57 offen offset:0 sc1\n" \
"v_mfma_f32_16x16x32_bf16 v[96:99], a[20:23], v[36:39], v[96:99]\n" \
"s_waitcnt vmcnt(7)\n" \
"v_readfirstlane_b32 s54, v10\n" \
"v_mfma_f32_16x16x32_bf16 v[100:103], a[148:151], v[36:39], v[100:103]\n" \
"ds_read_b128 v[80:83], v2 offset:16384\n" \
"v_mfma_f32_16x16x32_bf16 v[104:107], v[148:151], v[36:39], v[104:107]\n" \
"s_waitcnt lgkmcnt(3)\n" \
"s_and_b32 s54, s54, 2\n" \
"s_cbranch_scc1 .Lswo_p_%=\n" \
".Lswo_pk_%=:\n" \
"v_mfma_f32_16x16x32_bf16 v[108:111], v[84:87], v[36:39], v[108:111]\n" \
"buffer_load_dwordx4 v[36:39], v3, s[36:39], s57 offen offset:1024 sc1\n" \
"v_mfma_f32_16x16x32_bf16 v[96:99], a[24:27], v[40:43], v[96:99]\n" \
"buffer_load_dwordx2 v[14:15], v6, s[36:39], s80 offen sc1\n" \
"ds_read_b128 v[112:115], v8 offset:0\n" \
"v_mfma_f32_16x16x32_bf16 v[100:103], a[152:155], v[40:43], v[100:103]\n" \
"ds_read_b128 v[84:87], v2 offset:17408\n" \
"v_mfma_f32_16x16x32_bf16 v[104:107], v[152:155], v[40:43], v[104:107]\n" \
"s_waitcnt lgkmcnt(4)\n" \
"buffer_load_dwordx2 v[12:13], v5, s[64:67], s77 offen\n" \
"ds_read_b128 v[116:119], v8 offset:4096\n" \
"v_mfma_f32_16x16x32_bf16 v[108:111], v[88:91], v[40:43], v[108:111]\n" \
"buffer_load_dwordx4 v[40:43], v3, s[36:39], s57 offen offset:2048 sc1\n" \
"v_mfma_f32_16x16x32_bf16 v[96:99], a[28:31], v[44:47], v[96:99]\n" \
"ds_read_b128 v[120:123], v8 offset:8192\n" \
"v_mfma_f32_16x16x32_bf16 v[100:103], a[156:159], v[44:47], v[100:103]\n" \
"ds_read_b128 v[88:91], v2 offset:18432\n" \
"v_mfma_f32_16x16x32_bf16 v[104:107], v[156:159], v[44:47], v[104:107]\n" \
"s_waitcnt lgkmcnt(6)\n" \
"ds_read_b128 v[124:127], v8 offset:12288\n" \
"v_mfma_f32_16x16x32_bf16 v[108:111], v[92:95], v[44:47], v[108:111]\n" \
"buffer_load_dwordx4 v[44:47], v3, s[36:39], s57 offen offset:3072 sc1\n" \
"v_mfma_f32_16x16x32_bf16 v[96:99], a[32:35], v[48:51], v[96:99]\n" \
"v_mfma_f32_16x16x32_bf16 v[100:103], a[160:163], v[48:51], v[100:103]\n" \
"ds_read_b128 v[92:95], v2 offset:19456\n" \
"v_mfma_f32_16x16x32_bf16 v[104:107], v[160:163], v[48:51], v[104:107]\n" \
"s_waitcnt lgkmcnt(7)\n" \
"v_mfma_f32_16x16x32_bf16 v[108:111], v[80:83], v[48:51], v[108:111]\n" \
"s_add_u32 s57, s56, s92\n" \
"buffer_load_dwordx4 v[48:51], v3, s[36:39], s57 offen offset:0 sc1\n" \
"v_mfma_f32_16x16x32_bf16 v[96:99], a[36:39], v[52:55], v[96:99]\n" \
"s_waitcnt lgkmcnt(4)\n" \
"v_add_f32 v112, v112, v116\n" \
"v_add_f32_e64 v113, v113, v117\n" \
"v_mfma_f32_16x16x32_bf16 v[100:103], a[164:167], v[52:55], v[100:103]\n" \
"ds_read_b128 v[80:83], v2 offset:20480\n" \
"v_add_f32_e64 v114, v114, v118\n" \
"v_mfma_f32_16x16x32_bf16 v[104:107], v[164:167], v[52:55], v[104:107]\n" \
"v_add_f32_e64 v115, v115, v119\n" \
"v_mfma_f32_16x16x32_bf16 v[108:111], v[84:87], v[52:55], v[108:111]\n" \
"buffer_load_dwordx4 v[52:55], v3, s[36:39], s57 offen offset:1024 sc1\n" \
"s_waitcnt lgkmcnt(2)\n" \
"v_add_f32 v120, v120, v124\n" \
"v_mfma_f32_16x16x32_bf16 v[96:99], a[40:43], v[56:59], v[96:99]\n" \
"v_add_f32 v121, v121, v125\n" \
"v_add_f32 v122, v122, v126\n" \
"v_mfma_f32_16x16x32_bf16 v[100:103], a[168:171], v[56:59], v[100:103]\n" \
"ds_read_b128 v[84:87], v2 offset:21504\n" \
"v_add_f32_e64 v123, v123, v127\n" \
"v_mfma_f32_16x16x32_bf16 v[104:107], v[168:171], v[56:59], v[104:107]\n" \
"v_add_f32_e64 v112, v112, v120\n" \
"v_mfma_f32_16x16x32_bf16 v[108:111], v[88:91], v[56:59], v[108:111]\n" \
"buffer_load_dwordx4 v[56:59], v3, s[36:39], s57 offen offset:2048 sc1\n" \
"v_add_f32_e64 v113, v113, v121\n" \
"v_mfma_f32_16x16x32_bf16 v[96:99], a[44:47], v[60:63], v[96:99]\n" \
"v_add_f32 v114, v114, v122\n" \
"v_add_f32 v115, v115, v123\n" \
"v_mfma_f32_16x16x32_bf16 v[100:103], a[172:175], v[60:63], v[100:103]\n" \
"ds_read_b128 v[88:91], v2 offset:22528\n" \
"v_xor_b32_e64 v8, s84, v8\n" \
"v_mfma_f32_16x16x32_bf16 v[104:107], v[172:175], v[60:63], v[104:107]\n" \
"s_waitcnt lgkmcnt(3)\n" \
"v_cvt_pk_bf16_f32 v0, v112, v113\n" \
"v_mfma_f32_16x16x32_bf16 v[108:111], v[92:95], v[60:63], v[108:111]\n" \
"buffer_load_dwordx4 v[60:63], v3, s[36:39], s57 offen offset:3072 sc1\n" \
"v_cvt_pk_bf16_f32 v1, v114, v115\n" \
"v_mfma_f32_16x16x32_bf16 v[96:99], a[48:51], v[64:67], v[96:99]\n" \
"s_lshl_b32 s97, s44, 8\n" \
"s_add_u32 s97, s97, s49\n" \
"v_mfma_f32_16x16x32_bf16 v[100:103], a[176:179], v[64:67], v[100:103]\n" \
"ds_read_b128 v[92:95], v2 offset:23552\n" \
"buffer_store_dwordx2 v[0:1], v6, s[36:39], s76 offen sc1\n" \
"v_lshlrev_b32 v112, 16, v0\n" \
"v_mfma_f32_16x16x32_bf16 v[104:107], v[176:179], v[64:67], v[104:107]\n" \
"s_waitcnt lgkmcnt(3)\n" \
"v_and_b32 v113, 0xffff0000, v0\n" \
"v_mfma_f32_16x16x32_bf16 v[108:111], v[80:83], v[64:67], v[108:111]\n" \
"s_add_u32 s57, s56, s93\n" \
"buffer_load_dwordx4 v[64:67], v3, s[36:39], s57 offen offset:0 sc1\n" \
"v_lshlrev_b32_e64 v114, 16, v1\n" \
"v_mfma_f32_16x16x32_bf16 v[96:99], a[52:55], v[68:71], v[96:99]\n" \
"v_cmp_gt_u32_e64 s[62:63], s59, v11\n" \
"v_and_b32 v115, 0xffff0000, v1\n" \
"v_mfma_f32_16x16x32_bf16 v[100:103], a[180:183], v[68:71], v[100:103]\n" \
"ds_read_b128 v[80:83], v2 offset:24576\n" \
"v_mfma_f32_16x16x32_bf16 v[104:107], v[180:183], v[68:71], v[104:107]\n" \
"s_waitcnt vmcnt(17) lgkmcnt(3)\n" \
"v_mfma_f32_16x16x32_bf16 v[108:111], v[84:87], v[68:71], v[108:111]\n" \
"buffer_load_dwordx4 v[68:71], v3, s[36:39], s57 offen offset:1024 sc1\n" \
"v_mfma_f32_16x16x32_bf16 v[96:99], a[56:59], v[72:75], v[96:99]\n" \
"s_add_u32 s54, s55, 1\n" \
"s_cmp_lt_u32 s54, s46\n" \
"v_mfma_f32_16x16x32_bf16 v[100:103], a[184:187], v[72:75], v[100:103]\n" \
"ds_read_b128 v[84:87], v2 offset:25600\n" \
"s_cselect_b64 s[62:63], s[62:63], -1\n" \
"v_mfma_f32_16x16x32_bf16 v[104:107], v[184:187], v[72:75], v[104:107]\n" \
"s_waitcnt vmcnt(17) lgkmcnt(3)\n" \
"s_mul_i32 s83, s82, 4096\n" \
"v_mfma_f32_16x16x32_bf16 v[108:111], v[88:91], v[72:75], v[108:111]\n" \
"buffer_load_dwordx4 v[72:75], v3, s[36:39], s57 offen offset:2048 sc1\n" \
"v_mfma_f32_16x16x32_bf16 v[96:99], a[60:63], v[76:79], v[96:99]\n" \
"s_cmp_lg_u64 s[62:63], 0\n" \
"s_cselect_b32 s81, 1, 0\n" \
"v_mfma_f32_16x16x32_bf16 v[100:103], a[188:191], v[76:79], v[100:103]\n" \
"ds_read_b128 v[88:91], v2 offset:26624\n" \
"s_cselect_b32 s83, 0x70000000, s83\n" \
"v_mfma_f32_16x16x32_bf16 v[104:107], v[188:191], v[76:79], v[104:107]\n" \
"s_waitcnt vmcnt(17) lgkmcnt(3)\n" \
"v_mfma_f32_16x16x32_bf16 v[108:111], v[92:95], v[76:79], v[108:111]\n" \
"buffer_load_dwordx4 v[76:79], v3, s[36:39], s57 offen offset:3072 sc1\n" \
"v_mfma_f32_16x16x32_bf16 v[96:99], a[64:67], v[16:19], v[96:99]\n" \
"v_mfma_f32_16x16x32_bf16 v[100:103], a[192:195], v[16:19], v[100:103]\n" \
"ds_read_b128 v[92:95], v2 offset:27648\n" \
"v_mfma_f32_16x16x32_bf16 v[104:107], v[192:195], v[16:19], v[104:107]\n" \
"s_waitcnt vmcnt(17) lgkmcnt(3)\n" \
"s_mov_b32 s56, s83\n" \
"v_mfma_f32_16x16x32_bf16 v[108:111], v[80:83], v[16:19], v[108:111]\n" \
"s_add_u32 s57, s56, s86\n" \
"buffer_load_dwordx4 v[16:19], v3, s[36:39], s57 offen offset:0 sc1\n" \
"v_mfma_f32_16x16x32_bf16 v[96:99], a[68:71], v[20:23], v[96:99]\n" \
"buffer_load_dword v9, off, s[36:39], s97 sc1\n" \
"v_mfma_f32_16x16x32_bf16 v[100:103], a[196:199], v[20:23], v[100:103]\n" \
"ds_read_b128 v[80:83], v2 offset:28672\n" \
"v_mfma_f32_16x16x32_bf16 v[104:107], v[196:199], v[20:23], v[104:107]\n" \
"s_waitcnt vmcnt(18) lgkmcnt(3)\n" \
"v_mfma_f32_16x16x32_bf16 v[108:111], v[84:87], v[20:23], v[108:111]\n" \
"buffer_load_dwordx4 v[20:23], v3, s[36:39], s57 offen offset:1024 sc1\n" \
"v_mfma_f32_16x16x32_bf16 v[96:99], a[72:75], v[24:27], v[96:99]\n" \
"v_mfma_f32_16x16x32_bf16 v[100:103], a[200:203], v[24:27], v[100:103]\n" \
"ds_read_b128 v[84:87], v2 offset:29696\n" \
"v_mfma_f32_16x16x32_bf16 v[104:107], v[200:203], v[24:27], v[104:107]\n" \
"s_waitcnt vmcnt(18) lgkmcnt(3)\n" \
"v_mfma_f32_16x16x32_bf16 v[108:111], v[88:91], v[24:27], v[108:111]\n" \
"buffer_load_dwordx4 v[24:27], v3, s[36:39], s57 offen offset:2048 sc1\n" \
"v_mfma_f32_16x16x32_bf16 v[96:99], a[76:79], v[28:31], v[96:99]\n" \
"v_mfma_f32_16x16x32_bf16 v[100:103], a[204:207], v[28:31], v[100:103]\n" \
"ds_read_b128 v[88:91], v2 offset:30720\n" \
"v_mfma_f32_16x16x32_bf16 v[104:107], v[204:207], v[28:31], v[104:107]\n" \
"s_waitcnt vmcnt(18) lgkmcnt(3)\n" \
"v_mfma_f32_16x16x32_bf16 v[108:111], v[92:95], v[28:31], v[108:111]\n" \
"buffer_load_dwordx4 v[28:31], v3, s[36:39], s57 offen offset:3072 sc1\n" \
"v_mfma_f32_16x16x32_bf16 v[96:99], a[80:83], v[32:35], v[96:99]\n" \
"s_waitcnt vmcnt(17)\n" \
"v_lshlrev_b32 v120, 16, v14\n" \
"v_and_b32 v121, 0xffff0000, v14\n" \
"v_mfma_f32_16x16x32_bf16 v[100:103], a[208:211], v[32:35], v[100:103]\n" \
"ds_read_b128 v[92:95], v2 offset:31744\n" \
"s_waitcnt vmcnt(16)\n" \
"v_lshlrev_b32 v124, 16, v12\n" \
"v_mfma_f32_16x16x32_bf16 v[104:107], v[208:211], v[32:35], v[104:107]\n" \
"s_waitcnt lgkmcnt(3)\n" \
"v_and_b32 v125, 0xffff0000, v12\n" \
"v_mfma_f32_16x16x32_bf16 v[108:111], v[80:83], v[32:35], v[108:111]\n" \
"s_add_u32 s57, s56, s87\n" \
"buffer_load_dwordx4 v[32:35], v3, s[36:39], s57 offen offset:0 sc1\n" \
"v_lshlrev_b32_e64 v122, 16, v15\n" \
"v_mfma_f32_16x16x32_bf16 v[96:99], a[84:87], v[36:39], v[96:99]\n" \
"v_and_b32 v123, 0xffff0000, v15\n" \
"v_lshlrev_b32_e64 v126, 16, v13\n" \
"v_mfma_f32_16x16x32_bf16 v[100:103], a[212:215], v[36:39], v[100:103]\n" \
"ds_read_b128 v[80:83], v2 offset:0\n" \
"v_and_b32 v127, 0xffff0000, v13\n" \
"v_mfma_f32_16x16x32_bf16 v[104:107], v[212:215], v[36:39], v[104:107]\n" \
"s_waitcnt vmcnt(16) lgkmcnt(3)\n" \
"v_fmac_f32 v124, v112, v120\n" \
"v_mfma_f32_16x16x32_bf16 v[108:111], v[84:87], v[36:39], v[108:111]\n" \
"buffer_load_dwordx4 v[36:39], v3, s[36:39], s57 offen offset:1024 sc1\n" \
"v_fmac_f32_e64 v125, v113, v121\n" \
"v_mfma_f32_16x16x32_bf16 v[96:99], a[88:91], v[40:43], v[96:99]\n" \
"v_fmac_f32 v126, v114, v122\n" \
"v_fmac_f32 v127, v115, v123\n" \
"v_mfma_f32_16x16x32_bf16 v[100:103], a[216:219], v[40:43], v[100:103]\n" \
"ds_read_b128 v[84:87], v2 offset:1024\n" \
"v_cvt_pk_bf16_f32 v118, v124, v125\n" \
"v_mfma_f32_16x16x32_bf16 v[104:107], v[216:219], v[40:43], v[104:107]\n" \
"s_waitcnt vmcnt(16) lgkmcnt(3)\n" \
"v_cvt_pk_bf16_f32 v119, v126, v127\n" \
"v_mfma_f32_16x16x32_bf16 v[108:111], v[88:91], v[40:43], v[108:111]\n" \
"buffer_load_dwordx4 v[40:43], v3, s[36:39], s57 offen offset:2048 sc1\n" \
"v_mfma_f32_16x16x32_bf16 v[96:99], a[92:95], v[44:47], v[96:99]\n" \
"buffer_store_dwordx2 v[118:119], v5, s[68:71], s77 offen\n" \
"v_mov_b32 v10, 1\n" \
"v_mfma_f32_16x16x32_bf16 v[100:103], a[220:223], v[44:47], v[100:103]\n" \
"ds_read_b128 v[88:91], v2 offset:2048\n" \
"v_mfma_f32_16x16x32_bf16 v[104:107], v[220:223], v[44:47], v[104:107]\n" \
"s_waitcnt vmcnt(17) lgkmcnt(3)\n" \
"v_mfma_f32_16x16x32_bf16 v[108:111], v[92:95], v[44:47], v[108:111]\n" \
"buffer_load_dwordx4 v[44:47], v3, s[36:39], s57 offen offset:3072 sc1\n" \
"v_mfma_f32_16x16x32_bf16 v[96:99], a[96:99], v[48:51], v[96:99]\n" \
"v_mfma_f32_16x16x32_bf16 v[100:103], a[224:227], v[48:51], v[100:103]\n" \
"ds_read_b128 v[92:95], v2 offset:3072\n" \
"v_mfma_f32_16x16x32_bf16 v[104:107], v[224:227], v[48:51], v[104:107]\n" \
"s_waitcnt vmcnt(17) lgkmcnt(3)\n" \
"v_mfma_f32_16x16x32_bf16 v[108:111], v[80:83], v[48:51], v[108:111]\n" \
"s_add_u32 s57, s56, s88\n" \
"buffer_load_dwordx4 v[48:51], v3, s[36:39], s57 offen offset:0 sc1\n" \
"v_mfma_f32_16x16x32_bf16 v[96:99], a[100:103], v[52:55], v[96:99]\n" \
"v_mfma_f32_16x16x32_bf16 v[100:103], a[228:231], v[52:55], v[100:103]\n" \
"ds_read_b128 v[80:83], v2 offset:4096\n" \
"v_mfma_f32_16x16x32_bf16 v[104:107], v[228:231], v[52:55], v[104:107]\n" \
"s_waitcnt vmcnt(17) lgkmcnt(3)\n" \
"v_mfma_f32_16x16x32_bf16 v[108:111], v[84:87], v[52:55], v[108:111]\n" \
"buffer_load_dwordx4 v[52:55], v3, s[36:39], s57 offen offset:1024 sc1\n" \
"v_mfma_f32_16x16x32_bf16 v[96:99], a[104:107], v[56:59], v[96:99]\n" \
"v_mfma_f32_16x16x32_bf16 v[100:103], a[232:235], v[56:59], v[100:103]\n" \
"ds_read_b128 v[84:87], v2 offset:5120\n" \
"v_mfma_f32_16x16x32_bf16 v[104:107], v[232:235], v[56:59], v[104:107]\n" \
"s_waitcnt vmcnt(17) lgkmcnt(3)\n" \
"v_mfma_f32_16x16x32_bf16 v[108:111], v[88:91], v[56:59], v[108:111]\n" \
"buffer_load_dwordx4 v[56:59], v3, s[36:39], s57 offen offset:2048 sc1\n" \
"v_mfma_f32_16x16x32_bf16 v[96:99], a[108:111], v[60:63], v[96:99]\n" \
"v_mfma_f32_16x16x32_bf16 v[100:103], a[236:239], v[60:63], v[100:103]\n" \
"ds_read_b128 v[88:91], v2 offset:6144\n" \
"v_mfma_f32_16x16x32_bf16 v[104:107], v[236:239], v[60:63], v[104:107]\n" \
"s_waitcnt vmcnt(16) lgkmcnt(3)\n" \
"v_mfma_f32_16x16x32_bf16 v[108:111], v[92:95], v[60:63], v[108:111]\n" \
"buffer_load_dwordx4 v[60:63], v3, s[36:39], s57 offen offset:3072 sc1\n" \
"v_mfma_f32_16x16x32_bf16 v[96:99], a[112:115], v[64:67], v[96:99]\n" \
"v_mfma_f32_16x16x32_bf16 v[100:103], a[240:243], v[64:67], v[100:103]\n" \
"ds_read_b128 v[92:95], v2 offset:7168\n" \
"v_mfma_f32_16x16x32_bf16 v[104:107], v[240:243], v[64:67], v[104:107]\n" \
"s_waitcnt vmcnt(16) lgkmcnt(3)\n" \
"s_mov_b32 s84, 0x4000\n" \
"v_mfma_f32_16x16x32_bf16 v[108:111], v[80:83], v[64:67], v[108:111]\n" \
"s_add_u32 s57, s56, s89\n" \
"buffer_load_dwordx4 v[64:67], v3, s[36:39], s57 offen offset:0 sc1\n" \
"v_mfma_f32_16x16x32_bf16 v[96:99], a[116:119], v[68:71], v[96:99]\n" \
"buffer_load_dword v11, v4, s[36:39], 0 offen sc1\n" \
"v_mfma_f32_16x16x32_bf16 v[100:103], a[244:247], v[68:71], v[100:103]\n" \
"ds_read_b128 v[80:83], v2 offset:8192\n" \
"v_mfma_f32_16x16x32_bf16 v[104:107], v[244:247], v[68:71], v[104:107]\n" \
"s_waitcnt vmcnt(17) lgkmcnt(3)\n" \
"v_mfma_f32_16x16x32_bf16 v[108:111], v[84:87], v[68:71], v[108:111]\n" \
"buffer_load_dwordx4 v[68:71], v3, s[36:39], s57 offen offset:1024 sc1\n" \
"v_mfma_f32_16x16x32_bf16 v[96:99], a[120:123], v[72:75], v[96:99]\n" \
"v_mfma_f32_16x16x32_bf16 v[100:103], a[248:251], v[72:75], v[100:103]\n" \
"ds_read_b128 v[84:87], v2 offset:9216\n" \
"v_mfma_f32_16x16x32_bf16 v[104:107], v[248:251], v[72:75], v[104:107]\n" \
"s_waitcnt vmcnt(17) lgkmcnt(3)\n" \
"s_mov_b64 exec, 1\n" \
"buffer_atomic_or v10, off, s[36:39], s94 sc0\n" \
"s_mov_b64 exec, -1\n" \
"s_mov_b32 s95, s78\n" \
"v_mfma_f32_16x16x32_bf16 v[108:111], v[88:91], v[72:75], v[108:111]\n" \
"buffer_load_dwordx4 v[72:75], v3, s[36:39], s57 offen offset:2048 sc1\n" \
"v_mfma_f32_16x16x32_bf16 v[96:99], a[124:127], v[76:79], v[96:99]\n" \
"s_lshl_b32 s78, s44, 15\n" \
"s_mov_b32 s79, s97\n" \
"v_mfma_f32_16x16x32_bf16 v[100:103], a[252:255], v[76:79], v[100:103]\n" \
"ds_read_b128 v[88:91], v2 offset:10240\n" \
"s_mov_b32 s44, s82\n" \
"s_add_u32 s55, s55, 1\n" \
"v_mfma_f32_16x16x32_bf16 v[104:107], v[252:255], v[76:79], v[104:107]\n" \
"s_waitcnt vmcnt(18) lgkmcnt(3)\n" \
"ds_write_b128 v7, v[96:99] offset:0\n" \
"v_mfma_f32_16x16x32_bf16 v[108:111], v[92:95], v[76:79], v[108:111]\n" \
"buffer_load_dwordx4 v[76:79], v3, s[36:39], s57 offen offset:3072 sc1\n" \
"ds_write_b128 v7, v[100:103] offset:1024\n" \
"s_cmp_eq_u32 s81, 0\n" \
"s_cbranch_scc1 .Lswo_loop_%=\n" \
"s_nop 7\n" \
"s_nop 7\n" \
"s_waitcnt vmcnt(0) lgkmcnt(0)\n" \
"ds_write_b128 v7, v[104:107] offset:2048\n" \
"ds_write_b128 v7, v[108:111] offset:3072\n" \
"s_waitcnt lgkmcnt(0)\n" \
"s_barrier\n" \
"buffer_load_dword v9, off, s[36:39], s97 sc1\n" \
"v_readfirstlane_b32 s54, v10\n" \
"s_nop 0\n" \
"s_and_b32 s54, s54, 2\n" \
"s_cbranch_scc1 .Lswo_q_%=\n" \
".Lswo_qk_%=:\n" \
"s_waitcnt vmcnt(0)\n" \
"v_readfirstlane_b32 s96, v9\n" \
"s_add_u32 s76, s78, 0\n" \
"s_add_u32 s80, s78, 4194304\n" \
"s_and_b32 s96, s96, 2\n" \
"s_cselect_b32 s76, 0x7ffffff0, s76\n" \
"s_cselect_b32 s77, s78, 0x7ffffff0\n" \
"s_cselect_b32 s80, s80, 0x7ffffff0\n" \
"s_cselect_b32 s94, 0x7ffffff0, s79\n" \
"buffer_load_dwordx2 v[14:15], v6, s[36:39], s80 offen sc1\n" \
"buffer_load_dwordx2 v[12:13], v5, s[64:67], s77 offen\n" \
"ds_read_b128 v[112:115], v8 offset:0\n" \
"ds_read_b128 v[116:119], v8 offset:4096\n" \
"ds_read_b128 v[120:123], v8 offset:8192\n" \
"ds_read_b128 v[124:127], v8 offset:12288\n" \
"s_waitcnt lgkmcnt(2)\n" \
"v_add_f32 v112, v112, v116\n" \
"v_add_f32 v113, v113, v117\n" \
"v_add_f32 v114, v114, v118\n" \
"v_add_f32 v115, v115, v119\n" \
"s_waitcnt lgkmcnt(0)\n" \
"v_add_f32 v120, v120, v124\n" \
"v_add_f32 v121, v121, v125\n" \
"v_add_f32 v122, v122, v126\n" \
"v_add_f32 v123, v123, v127\n" \
"v_add_f32 v112, v112, v120\n" \
"v_add_f32 v113, v113, v121\n" \
"v_add_f32 v114, v114, v122\n" \
"v_add_f32 v115, v115, v123\n" \
"v_xor_b32 v8, s84, v8\n" \
"v_cvt_pk_bf16_f32 v0, v112, v113\n" \
"v_cvt_pk_bf16_f32 v1, v114, v115\n" \
"buffer_store_dwordx2 v[0:1], v6, s[36:39], s76 offen sc1\n" \
"v_lshlrev_b32 v112, 16, v0\n" \
"v_and_b32 v113, 0xffff0000, v0\n" \
"v_lshlrev_b32 v114, 16, v1\n" \
"v_and_b32 v115, 0xffff0000, v1\n" \
"s_waitcnt vmcnt(2)\n" \
"v_lshlrev_b32 v120, 16, v14\n" \
"v_and_b32 v121, 0xffff0000, v14\n" \
"s_waitcnt vmcnt(1)\n" \
"v_lshlrev_b32 v124, 16, v12\n" \
"v_and_b32 v125, 0xffff0000, v12\n" \
"v_lshlrev_b32 v122, 16, v15\n" \
"v_and_b32 v123, 0xffff0000, v15\n" \
"v_lshlrev_b32 v126, 16, v13\n" \
"v_and_b32 v127, 0xffff0000, v13\n" \
"v_fmac_f32 v124, v112, v120\n" \
"v_fmac_f32 v125, v113, v121\n" \
"v_fmac_f32 v126, v114, v122\n" \
"v_fmac_f32 v127, v115, v123\n" \
"v_cvt_pk_bf16_f32 v118, v124, v125\n" \
"v_cvt_pk_bf16_f32 v119, v126, v127\n" \
"buffer_store_dwordx2 v[118:119], v5, s[68:71], s77 offen\n" \
"v_mov_b32 v10, 1\n" \
"s_waitcnt vmcnt(1)\n" \
"s_mov_b64 exec, 1\n" \
"buffer_atomic_or v10, off, s[36:39], s94 sc0\n" \
"s_mov_b64 exec, -1\n" \
"s_mov_b32 s95, s78\n" \
"s_waitcnt vmcnt(0)\n" \
"v_readfirstlane_b32 s54, v10\n" \
"s_nop 0\n" \
"s_and_b32 s54, s54, 2\n" \
"s_cbranch_scc1 .Lswo_r_%=\n" \
".Lswo_rk_%=:\n" \
"v_xor_b32 v7, 0x4000, v7\n" \
"s_waitcnt vmcnt(0) lgkmcnt(0)\n" \
"s_cmp_lt_u32 s55, s46\n" \
"s_cbranch_scc1 .Lswo_prime_%=\n" \
"s_branch .Lswo_end_%=\n" \
".Lswo_w_%=:\n" \
"s_nop 7\n" \
"s_nop 7\n" \
"s_waitcnt vmcnt(0) lgkmcnt(0)\n" \
"s_mov_b32 s61, 0\n" \
".Lswo_wa_%=:\n" \
"s_cmp_lg_u32 s60, 0\n" \
"s_cbranch_scc1 .Lswo_wk_%=\n" \
"buffer_load_dword v11, v4, s[36:39], 0 offen sc1\n" \
"s_waitcnt vmcnt(0)\n" \
"v_cmp_gt_u32_e64 s[62:63], s59, v11\n" \
"s_nop 4\n" \
"s_cmp_eq_u64 s[62:63], 0\n" \
"s_cbranch_scc1 .Lswo_wk_%=\n" \
"s_sleep 2\n" \
"s_add_u32 s61, s61, 1\n" \
"s_cmp_lt_u32 s61, 65536\n" \
"s_cbranch_scc1 .Lswo_wa_%=\n" \
"s_mov_b32 s60, 1\n" \
"v_mov_b32 v11, 256\n" \
"v_mov_b32 v12, 1\n" \
"buffer_store_dword v12, v11, s[36:39], 0 offen sc1\n" \
"s_waitcnt vmcnt(0) lgkmcnt(0)\n" \
"s_branch .Lswo_wk_%=\n" \
".Lswo_p_%=:\n" \
"s_nop 7\n" \
"s_nop 7\n" \
"s_waitcnt vmcnt(0) lgkmcnt(0)\n" \
"s_add_u32 s54, s95, 4194304\n" \
"buffer_load_dwordx2 v[14:15], v6, s[36:39], s54 offen sc1\n" \
"buffer_load_dwordx2 v[12:13], v5, s[64:67], s95 offen\n" \
"v_lshlrev_b32 v112, 16, v0\n" \
"v_and_b32 v113, 0xffff0000, v0\n" \
"v_lshlrev_b32 v114, 16, v1\n" \
"v_and_b32 v115, 0xffff0000, v1\n" \
"s_waitcnt vmcnt(1)\n" \
"v_lshlrev_b32 v120, 16, v14\n" \
"v_and_b32 v121, 0xffff0000, v14\n" \
"s_waitcnt vmcnt(0)\n" \
"v_lshlrev_b32 v124, 16, v12\n" \
"v_and_b32 v125, 0xffff0000, v12\n" \
"v_lshlrev_b32 v122, 16, v15\n" \
"v_and_b32 v123, 0xffff0000, v15\n" \
"v_lshlrev_b32 v126, 16, v13\n" \
"v_and_b32 v127, 0xffff0000, v13\n" \
"v_fmac_f32 v124, v112, v120\n" \
"v_fmac_f32 v125, v113, v121\n" \
"v_fmac_f32 v126, v114, v122\n" \
"v_fmac_f32 v127, v115, v123\n" \
"v_cvt_pk_bf16_f32 v118, v124, v125\n" \
"v_cvt_pk_bf16_f32 v119, v126, v127\n" \
"buffer_store_dwordx2 v[118:119], v5, s[68:71], s95 offen\n" \
"s_waitcnt vmcnt(0) lgkmcnt(0)\n" \
"s_branch .Lswo_pk_%=\n" \
".Lswo_q_%=:\n" \
"s_nop 7\n" \
"s_nop 7\n" \
"s_waitcnt vmcnt(0) lgkmcnt(0)\n" \
"s_add_u32 s54, s95, 4194304\n" \
"buffer_load_dwordx2 v[14:15], v6, s[36:39], s54 offen sc1\n" \
"buffer_load_dwordx2 v[12:13], v5, s[64:67], s95 offen\n" \
"v_lshlrev_b32 v112, 16, v0\n" \
"v_and_b32 v113, 0xffff0000, v0\n" \
"v_lshlrev_b32 v114, 16, v1\n" \
"v_and_b32 v115, 0xffff0000, v1\n" \
"s_waitcnt vmcnt(1)\n" \
"v_lshlrev_b32 v120, 16, v14\n" \
"v_and_b32 v121, 0xffff0000, v14\n" \
"s_waitcnt vmcnt(0)\n" \
"v_lshlrev_b32 v124, 16, v12\n" \
"v_and_b32 v125, 0xffff0000, v12\n" \
"v_lshlrev_b32 v122, 16, v15\n" \
"v_and_b32 v123, 0xffff0000, v15\n" \
"v_lshlrev_b32 v126, 16, v13\n" \
"v_and_b32 v127, 0xffff0000, v13\n" \
"v_fmac_f32 v124, v112, v120\n" \
"v_fmac_f32 v125, v113, v121\n" \
"v_fmac_f32 v126, v114, v122\n" \
"v_fmac_f32 v127, v115, v123\n" \
"v_cvt_pk_bf16_f32 v118, v124, v125\n" \
"v_cvt_pk_bf16_f32 v119, v126, v127\n" \
"buffer_store_dwordx2 v[118:119], v5, s[68:71], s95 offen\n" \
"s_waitcnt vmcnt(0) lgkmcnt(0)\n" \
"s_branch .Lswo_qk_%=\n" \
".Lswo_r_%=:\n" \
"s_nop 7\n" \
"s_nop 7\n" \
"s_waitcnt vmcnt(0) lgkmcnt(0)\n" \
"s_add_u32 s54, s95, 4194304\n" \
"buffer_load_dwordx2 v[14:15], v6, s[36:39], s54 offen sc1\n" \
"buffer_load_dwordx2 v[12:13], v5, s[64:67], s95 offen\n" \
"v_lshlrev_b32 v112, 16, v0\n" \
"v_and_b32 v113, 0xffff0000, v0\n" \
"v_lshlrev_b32 v114, 16, v1\n" \
"v_and_b32 v115, 0xffff0000, v1\n" \
"s_waitcnt vmcnt(1)\n" \
"v_lshlrev_b32 v120, 16, v14\n" \
"v_and_b32 v121, 0xffff0000, v14\n" \
"s_waitcnt vmcnt(0)\n" \
"v_lshlrev_b32 v124, 16, v12\n" \
"v_and_b32 v125, 0xffff0000, v12\n" \
"v_lshlrev_b32 v122, 16, v15\n" \
"v_and_b32 v123, 0xffff0000, v15\n" \
"v_lshlrev_b32 v126, 16, v13\n" \
"v_and_b32 v127, 0xffff0000, v13\n" \
"v_fmac_f32 v124, v112, v120\n" \
"v_fmac_f32 v125, v113, v121\n" \
"v_fmac_f32 v126, v114, v122\n" \
"v_fmac_f32 v127, v115, v123\n" \
"v_cvt_pk_bf16_f32 v118, v124, v125\n" \
"v_cvt_pk_bf16_f32 v119, v126, v127\n" \
"buffer_store_dwordx2 v[118:119], v5, s[68:71], s95 offen\n" \
"s_waitcnt vmcnt(0) lgkmcnt(0)\n" \
"s_branch .Lswo_rk_%=\n" \
".Lswo_end_%=:\n" \
"s_endpgm\n"
#define ASM_S64_G2 \
"s_nop 7\n" \
"s_nop 7\n" \
"s_lshl_b32 s52, s48, 15\n" \
"s_lshl_b32 s53, s48, 13\n" \
"s_add_u32 s53, s53, 98304\n" \
"s_sub_u32 s54, s53, s52\n" \
"v_add_u32 v9, s54, v1\n" \
"s_mov_b32 s54, s85\n" \
"s_mul_i32 s86, s54, 262144\n" \
"s_add_u32 s54, s54, 1\n" \
"s_cmp_eq_u32 s54, 7\n" \
"s_cselect_b32 s54, 0, s54\n" \
"s_mul_i32 s87, s54, 262144\n" \
"s_add_u32 s54, s54, 1\n" \
"s_cmp_eq_u32 s54, 7\n" \
"s_cselect_b32 s54, 0, s54\n" \
"s_mul_i32 s88, s54, 262144\n" \
"s_add_u32 s54, s54, 1\n" \
"s_cmp_eq_u32 s54, 7\n" \
"s_cselect_b32 s54, 0, s54\n" \
"s_mul_i32 s89, s54, 262144\n" \
"s_add_u32 s54, s54, 1\n" \
"s_cmp_eq_u32 s54, 7\n" \
"s_cselect_b32 s54, 0, s54\n" \
"s_mul_i32 s90, s54, 262144\n" \
"s_add_u32 s54, s54, 1\n" \
"s_cmp_eq_u32 s54, 7\n" \
"s_cselect_b32 s54, 0, s54\n" \
"s_mul_i32 s91, s54, 262144\n" \
"s_add_u32 s54, s54, 1\n" \
"s_cmp_eq_u32 s54, 7\n" \
"s_cselect_b32 s54, 0, s54\n" \
"s_mul_i32 s92, s54, 262144\n" \
"s_add_u32 s54, s54, 1\n" \
"s_cmp_eq_u32 s54, 7\n" \
"s_cselect_b32 s54, 0, s54\n" \
"s_add_u32 m0, s52, 0\n" \
"s_add_u32 s54, s86, 0\n" \
"buffer_load_dwordx4 v0, s[40:43], s54 offen nt lds\n" \
"s_add_u32 m0, s52, 1024\n" \
"s_add_u32 s54, s86, 16384\n" \
"buffer_load_dwordx4 v0, s[40:43], s54 offen nt lds\n" \
"s_add_u32 m0, s52, 2048\n" \
"s_add_u32 s54, s86, 32768\n" \
"buffer_load_dwordx4 v0, s[40:43], s54 offen nt lds\n" \
"s_add_u32 m0, s52, 3072\n" \
"s_add_u32 s54, s86, 49152\n" \
"buffer_load_dwordx4 v0, s[40:43], s54 offen nt lds\n" \
"s_mov_b32 s54, s99\n" \
".Lsg2_z0_%=:\n" \
"s_sleep 1\n" \
"s_sub_u32 s54, s54, 1\n" \
"s_cbranch_scc0 .Lsg2_z0_%=\n" \
"s_add_u32 m0, s52, 4096\n" \
"s_add_u32 s54, s86, 65536\n" \
"buffer_load_dwordx4 v0, s[40:43], s54 offen nt lds\n" \
"s_add_u32 m0, s52, 5120\n" \
"s_add_u32 s54, s86, 81920\n" \
"buffer_load_dwordx4 v0, s[40:43], s54 offen nt lds\n" \
"s_add_u32 m0, s52, 6144\n" \
"s_add_u32 s54, s86, 98304\n" \
"buffer_load_dwordx4 v0, s[40:43], s54 offen nt lds\n" \
"s_add_u32 m0, s52, 7168\n" \
"s_add_u32 s54, s86, 114688\n" \
"buffer_load_dwordx4 v0, s[40:43], s54 offen nt lds\n" \
"s_mov_b32 s54, s99\n" \
".Lsg2_z1_%=:\n" \
"s_sleep 1\n" \
"s_sub_u32 s54, s54, 1\n" \
"s_cbranch_scc0 .Lsg2_z1_%=\n" \
"s_add_u32 m0, s52, 8192\n" \
"s_add_u32 s54, s86, 131072\n" \
"buffer_load_dwordx4 v0, s[40:43], s54 offen nt lds\n" \
"s_add_u32 m0, s52, 9216\n" \
"s_add_u32 s54, s86, 147456\n" \
"buffer_load_dwordx4 v0, s[40:43], s54 offen nt lds\n" \
"s_add_u32 m0, s52, 10240\n" \
"s_add_u32 s54, s86, 163840\n" \
"buffer_load_dwordx4 v0, s[40:43], s54 offen nt lds\n" \
"s_add_u32 m0, s52, 11264\n" \
"s_add_u32 s54, s86, 180224\n" \
"buffer_load_dwordx4 v0, s[40:43], s54 offen nt lds\n" \
"s_mov_b32 s54, s99\n" \
".Lsg2_z2_%=:\n" \
"s_sleep 1\n" \
"s_sub_u32 s54, s54, 1\n" \
"s_cbranch_scc0 .Lsg2_z2_%=\n" \
"s_add_u32 m0, s52, 12288\n" \
"s_add_u32 s54, s86, 196608\n" \
"buffer_load_dwordx4 v0, s[40:43], s54 offen nt lds\n" \
"s_add_u32 m0, s52, 13312\n" \
"s_add_u32 s54, s86, 212992\n" \
"buffer_load_dwordx4 v0, s[40:43], s54 offen nt lds\n" \
"s_add_u32 m0, s52, 14336\n" \
"s_add_u32 s54, s86, 229376\n" \
"buffer_load_dwordx4 v0, s[40:43], s54 offen nt lds\n" \
"s_add_u32 m0, s52, 15360\n" \
"s_add_u32 s54, s86, 245760\n" \
"buffer_load_dwordx4 v0, s[40:43], s54 offen nt lds\n" \
"s_mov_b32 s54, s99\n" \
".Lsg2_z3_%=:\n" \
"s_sleep 1\n" \
"s_sub_u32 s54, s54, 1\n" \
"s_cbranch_scc0 .Lsg2_z3_%=\n" \
"s_add_u32 m0, s52, 16384\n" \
"s_add_u32 s54, s87, 0\n" \
"buffer_load_dwordx4 v0, s[40:43], s54 offen nt lds\n" \
"s_add_u32 m0, s52, 17408\n" \
"s_add_u32 s54, s87, 16384\n" \
"buffer_load_dwordx4 v0, s[40:43], s54 offen nt lds\n" \
"s_add_u32 m0, s52, 18432\n" \
"s_add_u32 s54, s87, 32768\n" \
"buffer_load_dwordx4 v0, s[40:43], s54 offen nt lds\n" \
"s_add_u32 m0, s52, 19456\n" \
"s_add_u32 s54, s87, 49152\n" \
"buffer_load_dwordx4 v0, s[40:43], s54 offen nt lds\n" \
"s_mov_b32 s54, s99\n" \
".Lsg2_z4_%=:\n" \
"s_sleep 1\n" \
"s_sub_u32 s54, s54, 1\n" \
"s_cbranch_scc0 .Lsg2_z4_%=\n" \
"s_add_u32 m0, s52, 20480\n" \
"s_add_u32 s54, s87, 65536\n" \
"buffer_load_dwordx4 v0, s[40:43], s54 offen nt lds\n" \
"s_add_u32 m0, s52, 21504\n" \
"s_add_u32 s54, s87, 81920\n" \
"buffer_load_dwordx4 v0, s[40:43], s54 offen nt lds\n" \
"s_add_u32 m0, s52, 22528\n" \
"s_add_u32 s54, s87, 98304\n" \
"buffer_load_dwordx4 v0, s[40:43], s54 offen nt lds\n" \
"s_add_u32 m0, s52, 23552\n" \
"s_add_u32 s54, s87, 114688\n" \
"buffer_load_dwordx4 v0, s[40:43], s54 offen nt lds\n" \
"s_mov_b32 s54, s99\n" \
".Lsg2_z5_%=:\n" \
"s_sleep 1\n" \
"s_sub_u32 s54, s54, 1\n" \
"s_cbranch_scc0 .Lsg2_z5_%=\n" \
"s_add_u32 m0, s52, 24576\n" \
"s_add_u32 s54, s87, 131072\n" \
"buffer_load_dwordx4 v0, s[40:43], s54 offen nt lds\n" \
"s_add_u32 m0, s52, 25600\n" \
"s_add_u32 s54, s87, 147456\n" \
"buffer_load_dwordx4 v0, s[40:43], s54 offen nt lds\n" \
"s_add_u32 m0, s52, 26624\n" \
"s_add_u32 s54, s87, 163840\n" \
"buffer_load_dwordx4 v0, s[40:43], s54 offen nt lds\n" \
"s_add_u32 m0, s52, 27648\n" \
"s_add_u32 s54, s87, 180224\n" \
"buffer_load_dwordx4 v0, s[40:43], s54 offen nt lds\n" \
"s_mov_b32 s54, s99\n" \
".Lsg2_z6_%=:\n" \
"s_sleep 1\n" \
"s_sub_u32 s54, s54, 1\n" \
"s_cbranch_scc0 .Lsg2_z6_%=\n" \
"s_add_u32 m0, s52, 28672\n" \
"s_add_u32 s54, s87, 196608\n" \
"buffer_load_dwordx4 v0, s[40:43], s54 offen nt lds\n" \
"s_add_u32 m0, s52, 29696\n" \
"s_add_u32 s54, s87, 212992\n" \
"buffer_load_dwordx4 v0, s[40:43], s54 offen nt lds\n" \
"s_add_u32 m0, s52, 30720\n" \
"s_add_u32 s54, s87, 229376\n" \
"buffer_load_dwordx4 v0, s[40:43], s54 offen nt lds\n" \
"s_add_u32 m0, s52, 31744\n" \
"s_add_u32 s54, s87, 245760\n" \
"buffer_load_dwordx4 v0, s[40:43], s54 offen nt lds\n" \
"s_mov_b32 s54, s99\n" \
".Lsg2_z7_%=:\n" \
"s_sleep 1\n" \
"s_sub_u32 s54, s54, 1\n" \
"s_cbranch_scc0 .Lsg2_z7_%=\n" \
"s_add_u32 m0, s53, 32768\n" \
"s_add_u32 s54, s88, 0\n" \
"buffer_load_dwordx4 v0, s[40:43], s54 offen nt lds\n" \
"s_add_u32 m0, s53, 33792\n" \
"s_add_u32 s54, s88, 16384\n" \
"buffer_load_dwordx4 v0, s[40:43], s54 offen nt lds\n" \
"s_add_u32 m0, s53, 34816\n" \
"s_add_u32 s54, s88, 32768\n" \
"buffer_load_dwordx4 v0, s[40:43], s54 offen nt lds\n" \
"s_add_u32 m0, s53, 35840\n" \
"s_add_u32 s54, s88, 49152\n" \
"buffer_load_dwordx4 v0, s[40:43], s54 offen nt lds\n" \
"s_mov_b32 s54, s99\n" \
".Lsg2_z8_%=:\n" \
"s_sleep 1\n" \
"s_sub_u32 s54, s54, 1\n" \
"s_cbranch_scc0 .Lsg2_z8_%=\n" \
"s_add_u32 m0, s53, 36864\n" \
"s_add_u32 s54, s88, 65536\n" \
"buffer_load_dwordx4 v0, s[40:43], s54 offen nt lds\n" \
"s_add_u32 m0, s53, 37888\n" \
"s_add_u32 s54, s88, 81920\n" \
"buffer_load_dwordx4 v0, s[40:43], s54 offen nt lds\n" \
"s_add_u32 m0, s53, 38912\n" \
"s_add_u32 s54, s88, 98304\n" \
"buffer_load_dwordx4 v0, s[40:43], s54 offen nt lds\n" \
"s_add_u32 m0, s53, 39936\n" \
"s_add_u32 s54, s88, 114688\n" \
"buffer_load_dwordx4 v0, s[40:43], s54 offen nt lds\n" \
"s_mov_b32 s54, s99\n" \
".Lsg2_z9_%=:\n" \
"s_sleep 1\n" \
"s_sub_u32 s54, s54, 1\n" \
"s_cbranch_scc0 .Lsg2_z9_%=\n" \
"s_waitcnt vmcnt(36)\n" \
"ds_read_b64_tr_b16 a[0:1], v1 offset:0\n" \
"ds_read_b64_tr_b16 a[2:3], v1 offset:512\n" \
"ds_read_b64_tr_b16 a[128:129], v1 offset:32\n" \
"ds_read_b64_tr_b16 a[130:131], v1 offset:544\n" \
"ds_read_b64_tr_b16 v[128:129], v1 offset:64\n" \
"ds_read_b64_tr_b16 v[130:131], v1 offset:576\n" \
"ds_read_b64_tr_b16 v[16:17], v1 offset:96\n" \
"ds_read_b64_tr_b16 v[18:19], v1 offset:608\n" \
"s_waitcnt lgkmcnt(0)\n" \
"s_add_u32 m0, s52, 0\n" \
"s_add_u32 s54, s88, 131072\n" \
"buffer_load_dwordx4 v0, s[40:43], s54 offen nt lds\n" \
"s_add_u32 m0, s52, 1024\n" \
"s_add_u32 s54, s88, 147456\n" \
"buffer_load_dwordx4 v0, s[40:43], s54 offen nt lds\n" \
"s_add_u32 m0, s52, 2048\n" \
"s_add_u32 s54, s88, 163840\n" \
"buffer_load_dwordx4 v0, s[40:43], s54 offen nt lds\n" \
"s_add_u32 m0, s52, 3072\n" \
"s_add_u32 s54, s88, 180224\n" \
"buffer_load_dwordx4 v0, s[40:43], s54 offen nt lds\n" \
"s_mov_b32 s54, s99\n" \
".Lsg2_z10_%=:\n" \
"s_sleep 1\n" \
"s_sub_u32 s54, s54, 1\n" \
"s_cbranch_scc0 .Lsg2_z10_%=\n" \
"s_waitcnt vmcnt(36)\n" \
"ds_read_b64_tr_b16 a[4:5], v1 offset:4096\n" \
"ds_read_b64_tr_b16 a[6:7], v1 offset:4608\n" \
"ds_read_b64_tr_b16 a[132:133], v1 offset:4128\n" \
"ds_read_b64_tr_b16 a[134:135], v1 offset:4640\n" \
"ds_read_b64_tr_b16 v[132:133], v1 offset:4160\n" \
"ds_read_b64_tr_b16 v[134:135], v1 offset:4672\n" \
"ds_read_b64_tr_b16 v[20:21], v1 offset:4192\n" \
"ds_read_b64_tr_b16 v[22:23], v1 offset:4704\n" \
"s_waitcnt lgkmcnt(0)\n" \
"s_add_u32 m0, s52, 4096\n" \
"s_add_u32 s54, s88, 196608\n" \
"buffer_load_dwordx4 v0, s[40:43], s54 offen nt lds\n" \
"s_add_u32 m0, s52, 5120\n" \
"s_add_u32 s54, s88, 212992\n" \
"buffer_load_dwordx4 v0, s[40:43], s54 offen nt lds\n" \
"s_add_u32 m0, s52, 6144\n" \
"s_add_u32 s54, s88, 229376\n" \
"buffer_load_dwordx4 v0, s[40:43], s54 offen nt lds\n" \
"s_add_u32 m0, s52, 7168\n" \
"s_add_u32 s54, s88, 245760\n" \
"buffer_load_dwordx4 v0, s[40:43], s54 offen nt lds\n" \
"s_mov_b32 s54, s99\n" \
".Lsg2_z11_%=:\n" \
"s_sleep 1\n" \
"s_sub_u32 s54, s54, 1\n" \
"s_cbranch_scc0 .Lsg2_z11_%=\n" \
"s_waitcnt vmcnt(36)\n" \
"ds_read_b64_tr_b16 a[8:9], v1 offset:8192\n" \
"ds_read_b64_tr_b16 a[10:11], v1 offset:8704\n" \
"ds_read_b64_tr_b16 a[136:137], v1 offset:8224\n" \
"ds_read_b64_tr_b16 a[138:139], v1 offset:8736\n" \
"ds_read_b64_tr_b16 v[136:137], v1 offset:8256\n" \
"ds_read_b64_tr_b16 v[138:139], v1 offset:8768\n" \
"ds_read_b64_tr_b16 v[24:25], v1 offset:8288\n" \
"ds_read_b64_tr_b16 v[26:27], v1 offset:8800\n" \
"s_waitcnt lgkmcnt(0)\n" \
"s_add_u32 m0, s52, 8192\n" \
"s_add_u32 s54, s89, 0\n" \
"buffer_load_dwordx4 v0, s[40:43], s54 offen nt lds\n" \
"s_add_u32 m0, s52, 9216\n" \
"s_add_u32 s54, s89, 16384\n" \
"buffer_load_dwordx4 v0, s[40:43], s54 offen nt lds\n" \
"s_add_u32 m0, s52, 10240\n" \
"s_add_u32 s54, s89, 32768\n" \
"buffer_load_dwordx4 v0, s[40:43], s54 offen nt lds\n" \
"s_add_u32 m0, s52, 11264\n" \
"s_add_u32 s54, s89, 49152\n" \
"buffer_load_dwordx4 v0, s[40:43], s54 offen nt lds\n" \
"s_mov_b32 s54, s99\n" \
".Lsg2_z12_%=:\n" \
"s_sleep 1\n" \
"s_sub_u32 s54, s54, 1\n" \
"s_cbranch_scc0 .Lsg2_z12_%=\n" \
"s_waitcnt vmcnt(36)\n" \
"ds_read_b64_tr_b16 a[12:13], v1 offset:12288\n" \
"ds_read_b64_tr_b16 a[14:15], v1 offset:12800\n" \
"ds_read_b64_tr_b16 a[140:141], v1 offset:12320\n" \
"ds_read_b64_tr_b16 a[142:143], v1 offset:12832\n" \
"ds_read_b64_tr_b16 v[140:141], v1 offset:12352\n" \
"ds_read_b64_tr_b16 v[142:143], v1 offset:12864\n" \
"ds_read_b64_tr_b16 v[28:29], v1 offset:12384\n" \
"ds_read_b64_tr_b16 v[30:31], v1 offset:12896\n" \
"s_waitcnt lgkmcnt(0)\n" \
"s_add_u32 m0, s52, 12288\n" \
"s_add_u32 s54, s89, 65536\n" \
"buffer_load_dwordx4 v0, s[40:43], s54 offen nt lds\n" \
"s_add_u32 m0, s52, 13312\n" \
"s_add_u32 s54, s89, 81920\n" \
"buffer_load_dwordx4 v0, s[40:43], s54 offen nt lds\n" \
"s_add_u32 m0, s52, 14336\n" \
"s_add_u32 s54, s89, 98304\n" \
"buffer_load_dwordx4 v0, s[40:43], s54 offen nt lds\n" \
"s_add_u32 m0, s52, 15360\n" \
"s_add_u32 s54, s89, 114688\n" \
"buffer_load_dwordx4 v0, s[40:43], s54 offen nt lds\n" \
"s_mov_b32 s54, s99\n" \
".Lsg2_z13_%=:\n" \
"s_sleep 1\n" \
"s_sub_u32 s54, s54, 1\n" \
"s_cbranch_scc0 .Lsg2_z13_%=\n" \
"s_waitcnt vmcnt(36)\n" \
"ds_read_b64_tr_b16 a[16:17], v1 offset:16384\n" \
"ds_read_b64_tr_b16 a[18:19], v1 offset:16896\n" \
"ds_read_b64_tr_b16 a[144:145], v1 offset:16416\n" \
"ds_read_b64_tr_b16 a[146:147], v1 offset:16928\n" \
"ds_read_b64_tr_b16 v[144:145], v1 offset:16448\n" \
"ds_read_b64_tr_b16 v[146:147], v1 offset:16960\n" \
"ds_read_b64_tr_b16 v[32:33], v1 offset:16480\n" \
"ds_read_b64_tr_b16 v[34:35], v1 offset:16992\n" \
"s_waitcnt lgkmcnt(0)\n" \
"s_add_u32 m0, s52, 16384\n" \
"s_add_u32 s54, s89, 131072\n" \
"buffer_load_dwordx4 v0, s[40:43], s54 offen nt lds\n" \
"s_add_u32 m0, s52, 17408\n" \
"s_add_u32 s54, s89, 147456\n" \
"buffer_load_dwordx4 v0, s[40:43], s54 offen nt lds\n" \
"s_add_u32 m0, s52, 18432\n" \
"s_add_u32 s54, s89, 163840\n" \
"buffer_load_dwordx4 v0, s[40:43], s54 offen nt lds\n" \
"s_add_u32 m0, s52, 19456\n" \
"s_add_u32 s54, s89, 180224\n" \
"buffer_load_dwordx4 v0, s[40:43], s54 offen nt lds\n" \
"s_mov_b32 s54, s99\n" \
".Lsg2_z14_%=:\n" \
"s_sleep 1\n" \
"s_sub_u32 s54, s54, 1\n" \
"s_cbranch_scc0 .Lsg2_z14_%=\n" \
"s_waitcnt vmcnt(36)\n" \
"ds_read_b64_tr_b16 a[20:21], v1 offset:20480\n" \
"ds_read_b64_tr_b16 a[22:23], v1 offset:20992\n" \
"ds_read_b64_tr_b16 a[148:149], v1 offset:20512\n" \
"ds_read_b64_tr_b16 a[150:151], v1 offset:21024\n" \
"ds_read_b64_tr_b16 v[148:149], v1 offset:20544\n" \
"ds_read_b64_tr_b16 v[150:151], v1 offset:21056\n" \
"ds_read_b64_tr_b16 v[36:37], v1 offset:20576\n" \
"ds_read_b64_tr_b16 v[38:39], v1 offset:21088\n" \
"s_waitcnt lgkmcnt(0)\n" \
"s_add_u32 m0, s52, 20480\n" \
"s_add_u32 s54, s89, 196608\n" \
"buffer_load_dwordx4 v0, s[40:43], s54 offen nt lds\n" \
"s_add_u32 m0, s52, 21504\n" \
"s_add_u32 s54, s89, 212992\n" \
"buffer_load_dwordx4 v0, s[40:43], s54 offen nt lds\n" \
"s_add_u32 m0, s52, 22528\n" \
"s_add_u32 s54, s89, 229376\n" \
"buffer_load_dwordx4 v0, s[40:43], s54 offen nt lds\n" \
"s_add_u32 m0, s52, 23552\n" \
"s_add_u32 s54, s89, 245760\n" \
"buffer_load_dwordx4 v0, s[40:43], s54 offen nt lds\n" \
"s_mov_b32 s54, s99\n" \
".Lsg2_z15_%=:\n" \
"s_sleep 1\n" \
"s_sub_u32 s54, s54, 1\n" \
"s_cbranch_scc0 .Lsg2_z15_%=\n" \
"s_waitcnt vmcnt(36)\n" \
"ds_read_b64_tr_b16 a[24:25], v1 offset:24576\n" \
"ds_read_b64_tr_b16 a[26:27], v1 offset:25088\n" \
"ds_read_b64_tr_b16 a[152:153], v1 offset:24608\n" \
"ds_read_b64_tr_b16 a[154:155], v1 offset:25120\n" \
"ds_read_b64_tr_b16 v[152:153], v1 offset:24640\n" \
"ds_read_b64_tr_b16 v[154:155], v1 offset:25152\n" \
"ds_read_b64_tr_b16 v[40:41], v1 offset:24672\n" \
"ds_read_b64_tr_b16 v[42:43], v1 offset:25184\n" \
"s_waitcnt lgkmcnt(0)\n" \
"s_add_u32 m0, s52, 24576\n" \
"s_add_u32 s54, s90, 0\n" \
"buffer_load_dwordx4 v0, s[40:43], s54 offen nt lds\n" \
"s_add_u32 m0, s52, 25600\n" \
"s_add_u32 s54, s90, 16384\n" \
"buffer_load_dwordx4 v0, s[40:43], s54 offen nt lds\n" \
"s_add_u32 m0, s52, 26624\n" \
"s_add_u32 s54, s90, 32768\n" \
"buffer_load_dwordx4 v0, s[40:43], s54 offen nt lds\n" \
"s_add_u32 m0, s52, 27648\n" \
"s_add_u32 s54, s90, 49152\n" \
"buffer_load_dwordx4 v0, s[40:43], s54 offen nt lds\n" \
"s_mov_b32 s54, s99\n" \
".Lsg2_z16_%=:\n" \
"s_sleep 1\n" \
"s_sub_u32 s54, s54, 1\n" \
"s_cbranch_scc0 .Lsg2_z16_%=\n" \
"s_waitcnt vmcnt(36)\n" \
"ds_read_b64_tr_b16 a[28:29], v1 offset:28672\n" \
"ds_read_b64_tr_b16 a[30:31], v1 offset:29184\n" \
"ds_read_b64_tr_b16 a[156:157], v1 offset:28704\n" \
"ds_read_b64_tr_b16 a[158:159], v1 offset:29216\n" \
"ds_read_b64_tr_b16 v[156:157], v1 offset:28736\n" \
"ds_read_b64_tr_b16 v[158:159], v1 offset:29248\n" \
"ds_read_b64_tr_b16 v[44:45], v1 offset:28768\n" \
"ds_read_b64_tr_b16 v[46:47], v1 offset:29280\n" \
"s_waitcnt lgkmcnt(0)\n" \
"s_add_u32 m0, s52, 28672\n" \
"s_add_u32 s54, s90, 65536\n" \
"buffer_load_dwordx4 v0, s[40:43], s54 offen nt lds\n" \
"s_add_u32 m0, s52, 29696\n" \
"s_add_u32 s54, s90, 81920\n" \
"buffer_load_dwordx4 v0, s[40:43], s54 offen nt lds\n" \
"s_add_u32 m0, s52, 30720\n" \
"s_add_u32 s54, s90, 98304\n" \
"buffer_load_dwordx4 v0, s[40:43], s54 offen nt lds\n" \
"s_add_u32 m0, s52, 31744\n" \
"s_add_u32 s54, s90, 114688\n" \
"buffer_load_dwordx4 v0, s[40:43], s54 offen nt lds\n" \
"s_mov_b32 s54, s99\n" \
".Lsg2_z17_%=:\n" \
"s_sleep 1\n" \
"s_sub_u32 s54, s54, 1\n" \
"s_cbranch_scc0 .Lsg2_z17_%=\n" \
"s_waitcnt vmcnt(36)\n" \
"ds_read_b64_tr_b16 a[32:33], v9 offset:32768\n" \
"ds_read_b64_tr_b16 a[34:35], v9 offset:33280\n" \
"ds_read_b64_tr_b16 a[160:161], v9 offset:32800\n" \
"ds_read_b64_tr_b16 a[162:163], v9 offset:33312\n" \
"ds_read_b64_tr_b16 v[160:161], v9 offset:32832\n" \
"ds_read_b64_tr_b16 v[162:163], v9 offset:33344\n" \
"ds_read_b64_tr_b16 v[48:49], v9 offset:32864\n" \
"ds_read_b64_tr_b16 v[50:51], v9 offset:33376\n" \
"s_waitcnt lgkmcnt(0)\n" \
"s_add_u32 m0, s53, 32768\n" \
"s_add_u32 s54, s90, 131072\n" \
"buffer_load_dwordx4 v0, s[40:43], s54 offen nt lds\n" \
"s_add_u32 m0, s53, 33792\n" \
"s_add_u32 s54, s90, 147456\n" \
"buffer_load_dwordx4 v0, s[40:43], s54 offen nt lds\n" \
"s_add_u32 m0, s53, 34816\n" \
"s_add_u32 s54, s90, 163840\n" \
"buffer_load_dwordx4 v0, s[40:43], s54 offen nt lds\n" \
"s_add_u32 m0, s53, 35840\n" \
"s_add_u32 s54, s90, 180224\n" \
"buffer_load_dwordx4 v0, s[40:43], s54 offen nt lds\n" \
"s_mov_b32 s54, s99\n" \
".Lsg2_z18_%=:\n" \
"s_sleep 1\n" \
"s_sub_u32 s54, s54, 1\n" \
"s_cbranch_scc0 .Lsg2_z18_%=\n" \
"s_waitcnt vmcnt(36)\n" \
"ds_read_b64_tr_b16 a[36:37], v9 offset:36864\n" \
"ds_read_b64_tr_b16 a[38:39], v9 offset:37376\n" \
"ds_read_b64_tr_b16 a[164:165], v9 offset:36896\n" \
"ds_read_b64_tr_b16 a[166:167], v9 offset:37408\n" \
"ds_read_b64_tr_b16 v[164:165], v9 offset:36928\n" \
"ds_read_b64_tr_b16 v[166:167], v9 offset:37440\n" \
"ds_read_b64_tr_b16 v[52:53], v9 offset:36960\n" \
"ds_read_b64_tr_b16 v[54:55], v9 offset:37472\n" \
"s_waitcnt lgkmcnt(0)\n" \
"s_add_u32 m0, s53, 36864\n" \
"s_add_u32 s54, s90, 196608\n" \
"buffer_load_dwordx4 v0, s[40:43], s54 offen nt lds\n" \
"s_add_u32 m0, s53, 37888\n" \
"s_add_u32 s54, s90, 212992\n" \
"buffer_load_dwordx4 v0, s[40:43], s54 offen nt lds\n" \
"s_add_u32 m0, s53, 38912\n" \
"s_add_u32 s54, s90, 229376\n" \
"buffer_load_dwordx4 v0, s[40:43], s54 offen nt lds\n" \
"s_add_u32 m0, s53, 39936\n" \
"s_add_u32 s54, s90, 245760\n" \
"buffer_load_dwordx4 v0, s[40:43], s54 offen nt lds\n" \
"s_mov_b32 s54, s99\n" \
".Lsg2_z19_%=:\n" \
"s_sleep 1\n" \
"s_sub_u32 s54, s54, 1\n" \
"s_cbranch_scc0 .Lsg2_z19_%=\n" \
"s_waitcnt vmcnt(36)\n" \
"ds_read_b64_tr_b16 a[40:41], v1 offset:0\n" \
"ds_read_b64_tr_b16 a[42:43], v1 offset:512\n" \
"ds_read_b64_tr_b16 a[168:169], v1 offset:32\n" \
"ds_read_b64_tr_b16 a[170:171], v1 offset:544\n" \
"ds_read_b64_tr_b16 v[168:169], v1 offset:64\n" \
"ds_read_b64_tr_b16 v[170:171], v1 offset:576\n" \
"ds_read_b64_tr_b16 v[56:57], v1 offset:96\n" \
"ds_read_b64_tr_b16 v[58:59], v1 offset:608\n" \
"s_waitcnt lgkmcnt(0)\n" \
"s_add_u32 m0, s52, 0\n" \
"s_add_u32 s54, s91, 0\n" \
"buffer_load_dwordx4 v0, s[40:43], s54 offen nt lds\n" \
"s_add_u32 m0, s52, 1024\n" \
"s_add_u32 s54, s91, 16384\n" \
"buffer_load_dwordx4 v0, s[40:43], s54 offen nt lds\n" \
"s_add_u32 m0, s52, 2048\n" \
"s_add_u32 s54, s91, 32768\n" \
"buffer_load_dwordx4 v0, s[40:43], s54 offen nt lds\n" \
"s_add_u32 m0, s52, 3072\n" \
"s_add_u32 s54, s91, 49152\n" \
"buffer_load_dwordx4 v0, s[40:43], s54 offen nt lds\n" \
"s_mov_b32 s54, s99\n" \
".Lsg2_z20_%=:\n" \
"s_sleep 1\n" \
"s_sub_u32 s54, s54, 1\n" \
"s_cbranch_scc0 .Lsg2_z20_%=\n" \
"s_waitcnt vmcnt(36)\n" \
"ds_read_b64_tr_b16 a[44:45], v1 offset:4096\n" \
"ds_read_b64_tr_b16 a[46:47], v1 offset:4608\n" \
"ds_read_b64_tr_b16 a[172:173], v1 offset:4128\n" \
"ds_read_b64_tr_b16 a[174:175], v1 offset:4640\n" \
"ds_read_b64_tr_b16 v[172:173], v1 offset:4160\n" \
"ds_read_b64_tr_b16 v[174:175], v1 offset:4672\n" \
"ds_read_b64_tr_b16 v[60:61], v1 offset:4192\n" \
"ds_read_b64_tr_b16 v[62:63], v1 offset:4704\n" \
"s_waitcnt lgkmcnt(0)\n" \
"s_add_u32 m0, s52, 4096\n" \
"s_add_u32 s54, s91, 65536\n" \
"buffer_load_dwordx4 v0, s[40:43], s54 offen nt lds\n" \
"s_add_u32 m0, s52, 5120\n" \
"s_add_u32 s54, s91, 81920\n" \
"buffer_load_dwordx4 v0, s[40:43], s54 offen nt lds\n" \
"s_add_u32 m0, s52, 6144\n" \
"s_add_u32 s54, s91, 98304\n" \
"buffer_load_dwordx4 v0, s[40:43], s54 offen nt lds\n" \
"s_add_u32 m0, s52, 7168\n" \
"s_add_u32 s54, s91, 114688\n" \
"buffer_load_dwordx4 v0, s[40:43], s54 offen nt lds\n" \
"s_mov_b32 s54, s99\n" \
".Lsg2_z21_%=:\n" \
"s_sleep 1\n" \
"s_sub_u32 s54, s54, 1\n" \
"s_cbranch_scc0 .Lsg2_z21_%=\n" \
"s_waitcnt vmcnt(36)\n" \
"ds_read_b64_tr_b16 a[48:49], v1 offset:8192\n" \
"ds_read_b64_tr_b16 a[50:51], v1 offset:8704\n" \
"ds_read_b64_tr_b16 a[176:177], v1 offset:8224\n" \
"ds_read_b64_tr_b16 a[178:179], v1 offset:8736\n" \
"ds_read_b64_tr_b16 v[176:177], v1 offset:8256\n" \
"ds_read_b64_tr_b16 v[178:179], v1 offset:8768\n" \
"ds_read_b64_tr_b16 v[64:65], v1 offset:8288\n" \
"ds_read_b64_tr_b16 v[66:67], v1 offset:8800\n" \
"s_waitcnt lgkmcnt(0)\n" \
"s_add_u32 m0, s52, 8192\n" \
"s_add_u32 s54, s91, 131072\n" \
"buffer_load_dwordx4 v0, s[40:43], s54 offen nt lds\n" \
"s_add_u32 m0, s52, 9216\n" \
"s_add_u32 s54, s91, 147456\n" \
"buffer_load_dwordx4 v0, s[40:43], s54 offen nt lds\n" \
"s_add_u32 m0, s52, 10240\n" \
"s_add_u32 s54, s91, 163840\n" \
"buffer_load_dwordx4 v0, s[40:43], s54 offen nt lds\n" \
"s_add_u32 m0, s52, 11264\n" \
"s_add_u32 s54, s91, 180224\n" \
"buffer_load_dwordx4 v0, s[40:43], s54 offen nt lds\n" \
"s_mov_b32 s54, s99\n" \
".Lsg2_z22_%=:\n" \
"s_sleep 1\n" \
"s_sub_u32 s54, s54, 1\n" \
"s_cbranch_scc0 .Lsg2_z22_%=\n" \
"s_waitcnt vmcnt(36)\n" \
"ds_read_b64_tr_b16 a[52:53], v1 offset:12288\n" \
"ds_read_b64_tr_b16 a[54:55], v1 offset:12800\n" \
"ds_read_b64_tr_b16 a[180:181], v1 offset:12320\n" \
"ds_read_b64_tr_b16 a[182:183], v1 offset:12832\n" \
"ds_read_b64_tr_b16 v[180:181], v1 offset:12352\n" \
"ds_read_b64_tr_b16 v[182:183], v1 offset:12864\n" \
"ds_read_b64_tr_b16 v[68:69], v1 offset:12384\n" \
"ds_read_b64_tr_b16 v[70:71], v1 offset:12896\n" \
"s_waitcnt lgkmcnt(0)\n" \
"s_add_u32 m0, s52, 12288\n" \
"s_add_u32 s54, s91, 196608\n" \
"buffer_load_dwordx4 v0, s[40:43], s54 offen nt lds\n" \
"s_add_u32 m0, s52, 13312\n" \
"s_add_u32 s54, s91, 212992\n" \
"buffer_load_dwordx4 v0, s[40:43], s54 offen nt lds\n" \
"s_add_u32 m0, s52, 14336\n" \
"s_add_u32 s54, s91, 229376\n" \
"buffer_load_dwordx4 v0, s[40:43], s54 offen nt lds\n" \
"s_add_u32 m0, s52, 15360\n" \
"s_add_u32 s54, s91, 245760\n" \
"buffer_load_dwordx4 v0, s[40:43], s54 offen nt lds\n" \
"s_mov_b32 s54, s99\n" \
".Lsg2_z23_%=:\n" \
"s_sleep 1\n" \
"s_sub_u32 s54, s54, 1\n" \
"s_cbranch_scc0 .Lsg2_z23_%=\n" \
"s_waitcnt vmcnt(36)\n" \
"ds_read_b64_tr_b16 a[56:57], v1 offset:16384\n" \
"ds_read_b64_tr_b16 a[58:59], v1 offset:16896\n" \
"ds_read_b64_tr_b16 a[184:185], v1 offset:16416\n" \
"ds_read_b64_tr_b16 a[186:187], v1 offset:16928\n" \
"ds_read_b64_tr_b16 v[184:185], v1 offset:16448\n" \
"ds_read_b64_tr_b16 v[186:187], v1 offset:16960\n" \
"ds_read_b64_tr_b16 v[72:73], v1 offset:16480\n" \
"ds_read_b64_tr_b16 v[74:75], v1 offset:16992\n" \
"s_waitcnt lgkmcnt(0)\n" \
"s_add_u32 m0, s52, 16384\n" \
"s_add_u32 s54, s92, 0\n" \
"buffer_load_dwordx4 v0, s[40:43], s54 offen nt lds\n" \
"s_add_u32 m0, s52, 17408\n" \
"s_add_u32 s54, s92, 16384\n" \
"buffer_load_dwordx4 v0, s[40:43], s54 offen nt lds\n" \
"s_add_u32 m0, s52, 18432\n" \
"s_add_u32 s54, s92, 32768\n" \
"buffer_load_dwordx4 v0, s[40:43], s54 offen nt lds\n" \
"s_add_u32 m0, s52, 19456\n" \
"s_add_u32 s54, s92, 49152\n" \
"buffer_load_dwordx4 v0, s[40:43], s54 offen nt lds\n" \
"s_mov_b32 s54, s99\n" \
".Lsg2_z24_%=:\n" \
"s_sleep 1\n" \
"s_sub_u32 s54, s54, 1\n" \
"s_cbranch_scc0 .Lsg2_z24_%=\n" \
"s_waitcnt vmcnt(36)\n" \
"ds_read_b64_tr_b16 a[60:61], v1 offset:20480\n" \
"ds_read_b64_tr_b16 a[62:63], v1 offset:20992\n" \
"ds_read_b64_tr_b16 a[188:189], v1 offset:20512\n" \
"ds_read_b64_tr_b16 a[190:191], v1 offset:21024\n" \
"ds_read_b64_tr_b16 v[188:189], v1 offset:20544\n" \
"ds_read_b64_tr_b16 v[190:191], v1 offset:21056\n" \
"ds_read_b64_tr_b16 v[76:77], v1 offset:20576\n" \
"ds_read_b64_tr_b16 v[78:79], v1 offset:21088\n" \
"s_waitcnt lgkmcnt(0)\n" \
"s_add_u32 m0, s52, 20480\n" \
"s_add_u32 s54, s92, 65536\n" \
"buffer_load_dwordx4 v0, s[40:43], s54 offen nt lds\n" \
"s_add_u32 m0, s52, 21504\n" \
"s_add_u32 s54, s92, 81920\n" \
"buffer_load_dwordx4 v0, s[40:43], s54 offen nt lds\n" \
"s_add_u32 m0, s52, 22528\n" \
"s_add_u32 s54, s92, 98304\n" \
"buffer_load_dwordx4 v0, s[40:43], s54 offen nt lds\n" \
"s_add_u32 m0, s52, 23552\n" \
"s_add_u32 s54, s92, 114688\n" \
"buffer_load_dwordx4 v0, s[40:43], s54 offen nt lds\n" \
"s_mov_b32 s54, s99\n" \
".Lsg2_z25_%=:\n" \
"s_sleep 1\n" \
"s_sub_u32 s54, s54, 1\n" \
"s_cbranch_scc0 .Lsg2_z25_%=\n" \
"s_waitcnt vmcnt(36)\n" \
"ds_read_b64_tr_b16 a[64:65], v1 offset:24576\n" \
"ds_read_b64_tr_b16 a[66:67], v1 offset:25088\n" \
"ds_read_b64_tr_b16 a[192:193], v1 offset:24608\n" \
"ds_read_b64_tr_b16 a[194:195], v1 offset:25120\n" \
"ds_read_b64_tr_b16 v[192:193], v1 offset:24640\n" \
"ds_read_b64_tr_b16 v[194:195], v1 offset:25152\n" \
"ds_read_b64_tr_b16 v[80:81], v1 offset:24672\n" \
"ds_read_b64_tr_b16 v[82:83], v1 offset:25184\n" \
"s_waitcnt lgkmcnt(0)\n" \
"s_add_u32 m0, s52, 24576\n" \
"s_add_u32 s54, s92, 131072\n" \
"buffer_load_dwordx4 v0, s[40:43], s54 offen nt lds\n" \
"s_add_u32 m0, s52, 25600\n" \
"s_add_u32 s54, s92, 147456\n" \
"buffer_load_dwordx4 v0, s[40:43], s54 offen nt lds\n" \
"s_add_u32 m0, s52, 26624\n" \
"s_add_u32 s54, s92, 163840\n" \
"buffer_load_dwordx4 v0, s[40:43], s54 offen nt lds\n" \
"s_add_u32 m0, s52, 27648\n" \
"s_add_u32 s54, s92, 180224\n" \
"buffer_load_dwordx4 v0, s[40:43], s54 offen nt lds\n" \
"s_mov_b32 s54, s99\n" \
".Lsg2_z26_%=:\n" \
"s_sleep 1\n" \
"s_sub_u32 s54, s54, 1\n" \
"s_cbranch_scc0 .Lsg2_z26_%=\n" \
"s_waitcnt vmcnt(36)\n" \
"ds_read_b64_tr_b16 a[68:69], v1 offset:28672\n" \
"ds_read_b64_tr_b16 a[70:71], v1 offset:29184\n" \
"ds_read_b64_tr_b16 a[196:197], v1 offset:28704\n" \
"ds_read_b64_tr_b16 a[198:199], v1 offset:29216\n" \
"ds_read_b64_tr_b16 v[196:197], v1 offset:28736\n" \
"ds_read_b64_tr_b16 v[198:199], v1 offset:29248\n" \
"ds_read_b64_tr_b16 v[84:85], v1 offset:28768\n" \
"ds_read_b64_tr_b16 v[86:87], v1 offset:29280\n" \
"s_waitcnt lgkmcnt(0)\n" \
"s_add_u32 m0, s52, 28672\n" \
"s_add_u32 s54, s92, 196608\n" \
"buffer_load_dwordx4 v0, s[40:43], s54 offen nt lds\n" \
"s_add_u32 m0, s52, 29696\n" \
"s_add_u32 s54, s92, 212992\n" \
"buffer_load_dwordx4 v0, s[40:43], s54 offen nt lds\n" \
"s_add_u32 m0, s52, 30720\n" \
"s_add_u32 s54, s92, 229376\n" \
"buffer_load_dwordx4 v0, s[40:43], s54 offen nt lds\n" \
"s_add_u32 m0, s52, 31744\n" \
"s_add_u32 s54, s92, 245760\n" \
"buffer_load_dwordx4 v0, s[40:43], s54 offen nt lds\n" \
"s_mov_b32 s54, s99\n" \
".Lsg2_z27_%=:\n" \
"s_sleep 1\n" \
"s_sub_u32 s54, s54, 1\n" \
"s_cbranch_scc0 .Lsg2_z27_%=\n" \
"s_waitcnt vmcnt(36)\n" \
"ds_read_b64_tr_b16 a[72:73], v9 offset:32768\n" \
"ds_read_b64_tr_b16 a[74:75], v9 offset:33280\n" \
"ds_read_b64_tr_b16 a[200:201], v9 offset:32800\n" \
"ds_read_b64_tr_b16 a[202:203], v9 offset:33312\n" \
"ds_read_b64_tr_b16 v[200:201], v9 offset:32832\n" \
"ds_read_b64_tr_b16 v[202:203], v9 offset:33344\n" \
"ds_read_b64_tr_b16 v[88:89], v9 offset:32864\n" \
"ds_read_b64_tr_b16 v[90:91], v9 offset:33376\n" \
"s_waitcnt lgkmcnt(0)\n" \
"s_waitcnt vmcnt(32)\n" \
"ds_read_b64_tr_b16 a[76:77], v9 offset:36864\n" \
"ds_read_b64_tr_b16 a[78:79], v9 offset:37376\n" \
"ds_read_b64_tr_b16 a[204:205], v9 offset:36896\n" \
"ds_read_b64_tr_b16 a[206:207], v9 offset:37408\n" \
"ds_read_b64_tr_b16 v[204:205], v9 offset:36928\n" \
"ds_read_b64_tr_b16 v[206:207], v9 offset:37440\n" \
"ds_read_b64_tr_b16 v[92:93], v9 offset:36960\n" \
"ds_read_b64_tr_b16 v[94:95], v9 offset:37472\n" \
"s_waitcnt lgkmcnt(0)\n" \
"s_waitcnt vmcnt(28)\n" \
"ds_read_b64_tr_b16 a[80:81], v1 offset:0\n" \
"ds_read_b64_tr_b16 a[82:83], v1 offset:512\n" \
"ds_read_b64_tr_b16 a[208:209], v1 offset:32\n" \
"ds_read_b64_tr_b16 a[210:211], v1 offset:544\n" \
"ds_read_b64_tr_b16 v[208:209], v1 offset:64\n" \
"ds_read_b64_tr_b16 v[210:211], v1 offset:576\n" \
"ds_read_b64_tr_b16 v[96:97], v1 offset:96\n" \
"ds_read_b64_tr_b16 v[98:99], v1 offset:608\n" \
"s_waitcnt lgkmcnt(0)\n" \
"ds_write_b128 v2, v[16:19] offset:0\n" \
"ds_write_b128 v2, v[20:23] offset:1024\n" \
"ds_write_b128 v2, v[24:27] offset:2048\n" \
"ds_write_b128 v2, v[28:31] offset:3072\n" \
"s_waitcnt vmcnt(24)\n" \
"ds_read_b64_tr_b16 a[84:85], v1 offset:4096\n" \
"ds_read_b64_tr_b16 a[86:87], v1 offset:4608\n" \
"ds_read_b64_tr_b16 a[212:213], v1 offset:4128\n" \
"ds_read_b64_tr_b16 a[214:215], v1 offset:4640\n" \
"ds_read_b64_tr_b16 v[212:213], v1 offset:4160\n" \
"ds_read_b64_tr_b16 v[214:215], v1 offset:4672\n" \
"ds_read_b64_tr_b16 v[100:101], v1 offset:4192\n" \
"ds_read_b64_tr_b16 v[102:103], v1 offset:4704\n" \
"s_waitcnt lgkmcnt(0)\n" \
"ds_write_b128 v2, v[32:35] offset:4096\n" \
"ds_write_b128 v2, v[36:39] offset:5120\n" \
"ds_write_b128 v2, v[40:43] offset:6144\n" \
"ds_write_b128 v2, v[44:47] offset:7168\n" \
"s_waitcnt vmcnt(20)\n" \
"ds_read_b64_tr_b16 a[88:89], v1 offset:8192\n" \
"ds_read_b64_tr_b16 a[90:91], v1 offset:8704\n" \
"ds_read_b64_tr_b16 a[216:217], v1 offset:8224\n" \
"ds_read_b64_tr_b16 a[218:219], v1 offset:8736\n" \
"ds_read_b64_tr_b16 v[216:217], v1 offset:8256\n" \
"ds_read_b64_tr_b16 v[218:219], v1 offset:8768\n" \
"ds_read_b64_tr_b16 v[104:105], v1 offset:8288\n" \
"ds_read_b64_tr_b16 v[106:107], v1 offset:8800\n" \
"s_waitcnt lgkmcnt(0)\n" \
"ds_write_b128 v2, v[48:51] offset:8192\n" \
"ds_write_b128 v2, v[52:55] offset:9216\n" \
"ds_write_b128 v2, v[56:59] offset:10240\n" \
"ds_write_b128 v2, v[60:63] offset:11264\n" \
"s_waitcnt vmcnt(16)\n" \
"ds_read_b64_tr_b16 a[92:93], v1 offset:12288\n" \
"ds_read_b64_tr_b16 a[94:95], v1 offset:12800\n" \
"ds_read_b64_tr_b16 a[220:221], v1 offset:12320\n" \
"ds_read_b64_tr_b16 a[222:223], v1 offset:12832\n" \
"ds_read_b64_tr_b16 v[220:221], v1 offset:12352\n" \
"ds_read_b64_tr_b16 v[222:223], v1 offset:12864\n" \
"ds_read_b64_tr_b16 v[108:109], v1 offset:12384\n" \
"ds_read_b64_tr_b16 v[110:111], v1 offset:12896\n" \
"s_waitcnt lgkmcnt(0)\n" \
"ds_write_b128 v2, v[64:67] offset:12288\n" \
"ds_write_b128 v2, v[68:71] offset:13312\n" \
"ds_write_b128 v2, v[72:75] offset:14336\n" \
"ds_write_b128 v2, v[76:79] offset:15360\n" \
"s_waitcnt vmcnt(12)\n" \
"ds_read_b64_tr_b16 a[96:97], v1 offset:16384\n" \
"ds_read_b64_tr_b16 a[98:99], v1 offset:16896\n" \
"ds_read_b64_tr_b16 a[224:225], v1 offset:16416\n" \
"ds_read_b64_tr_b16 a[226:227], v1 offset:16928\n" \
"ds_read_b64_tr_b16 v[224:225], v1 offset:16448\n" \
"ds_read_b64_tr_b16 v[226:227], v1 offset:16960\n" \
"ds_read_b64_tr_b16 v[16:17], v1 offset:16480\n" \
"ds_read_b64_tr_b16 v[18:19], v1 offset:16992\n" \
"s_waitcnt lgkmcnt(0)\n" \
"ds_write_b128 v2, v[80:83] offset:16384\n" \
"ds_write_b128 v2, v[84:87] offset:17408\n" \
"ds_write_b128 v2, v[88:91] offset:18432\n" \
"ds_write_b128 v2, v[92:95] offset:19456\n" \
"s_waitcnt vmcnt(8)\n" \
"ds_read_b64_tr_b16 a[100:101], v1 offset:20480\n" \
"ds_read_b64_tr_b16 a[102:103], v1 offset:20992\n" \
"ds_read_b64_tr_b16 a[228:229], v1 offset:20512\n" \
"ds_read_b64_tr_b16 a[230:231], v1 offset:21024\n" \
"ds_read_b64_tr_b16 v[228:229], v1 offset:20544\n" \
"ds_read_b64_tr_b16 v[230:231], v1 offset:21056\n" \
"ds_read_b64_tr_b16 v[20:21], v1 offset:20576\n" \
"ds_read_b64_tr_b16 v[22:23], v1 offset:21088\n" \
"s_waitcnt lgkmcnt(0)\n" \
"ds_write_b128 v2, v[96:99] offset:20480\n" \
"ds_write_b128 v2, v[100:103] offset:21504\n" \
"ds_write_b128 v2, v[104:107] offset:22528\n" \
"ds_write_b128 v2, v[108:111] offset:23552\n" \
"s_waitcnt vmcnt(4)\n" \
"ds_read_b64_tr_b16 a[104:105], v1 offset:24576\n" \
"ds_read_b64_tr_b16 a[106:107], v1 offset:25088\n" \
"ds_read_b64_tr_b16 a[232:233], v1 offset:24608\n" \
"ds_read_b64_tr_b16 a[234:235], v1 offset:25120\n" \
"ds_read_b64_tr_b16 v[232:233], v1 offset:24640\n" \
"ds_read_b64_tr_b16 v[234:235], v1 offset:25152\n" \
"ds_read_b64_tr_b16 v[24:25], v1 offset:24672\n" \
"ds_read_b64_tr_b16 v[26:27], v1 offset:25184\n" \
"s_waitcnt lgkmcnt(0)\n" \
"ds_write_b128 v2, v[16:19] offset:24576\n" \
"ds_write_b128 v2, v[20:23] offset:25600\n" \
"ds_write_b128 v2, v[24:27] offset:26624\n" \
"s_waitcnt vmcnt(0)\n" \
"ds_read_b64_tr_b16 a[108:109], v1 offset:28672\n" \
"ds_read_b64_tr_b16 a[110:111], v1 offset:29184\n" \
"ds_read_b64_tr_b16 a[236:237], v1 offset:28704\n" \
"ds_read_b64_tr_b16 a[238:239], v1 offset:29216\n" \
"ds_read_b64_tr_b16 v[236:237], v1 offset:28736\n" \
"ds_read_b64_tr_b16 v[238:239], v1 offset:29248\n" \
"ds_read_b64_tr_b16 v[28:29], v1 offset:28768\n" \
"ds_read_b64_tr_b16 v[30:31], v1 offset:29280\n" \
"s_waitcnt lgkmcnt(0)\n" \
"ds_write_b128 v2, v[28:31] offset:28672\n" \
"s_waitcnt vmcnt(0) lgkmcnt(0)\n" \
"s_barrier\n" \
"s_mov_b32 s54, s85\n" \
"s_mul_i32 s86, s54, 4096\n" \
"s_add_u32 s54, s54, 1\n" \
"s_cmp_eq_u32 s54, 7\n" \
"s_cselect_b32 s54, 0, s54\n" \
"s_mul_i32 s87, s54, 4096\n" \
"s_add_u32 s54, s54, 1\n" \
"s_cmp_eq_u32 s54, 7\n" \
"s_cselect_b32 s54, 0, s54\n" \
"s_mul_i32 s88, s54, 4096\n" \
"s_add_u32 s54, s54, 1\n" \
"s_cmp_eq_u32 s54, 7\n" \
"s_cselect_b32 s54, 0, s54\n" \
"s_mul_i32 s89, s54, 4096\n" \
"s_add_u32 s54, s54, 1\n" \
"s_cmp_eq_u32 s54, 7\n" \
"s_cselect_b32 s54, 0, s54\n" \
"s_mul_i32 s90, s54, 4096\n" \
"s_add_u32 s54, s54, 1\n" \
"s_cmp_eq_u32 s54, 7\n" \
"s_cselect_b32 s54, 0, s54\n" \
"s_mul_i32 s91, s54, 4096\n" \
"s_add_u32 s54, s54, 1\n" \
"s_cmp_eq_u32 s54, 7\n" \
"s_cselect_b32 s54, 0, s54\n" \
"s_mul_i32 s92, s54, 4096\n" \
"s_add_u32 s54, s54, 1\n" \
"s_cmp_eq_u32 s54, 7\n" \
"s_cselect_b32 s54, 0, s54\n" \
"s_mov_b32 s55, 0\n" \
"s_mov_b32 s60, 0\n" \
".Lsg2_prime_%=:\n" \
"s_lshr_b32 s59, s44, 1\n" \
"s_add_u32 s59, s59, 1\n" \
"s_mul_i32 s56, s44, 114688\n" \
"buffer_load_dword v11, v4, s[36:39], 0 offen sc1\n" \
"s_waitcnt vmcnt(0)\n" \
"v_cmp_gt_u32_e64 s[62:63], s59, v11\n" \
"s_nop 4\n" \
"s_cmp_lg_u64 s[62:63], 0\n" \
"s_cbranch_scc1 .Lsg2_w_%=\n" \
".Lsg2_wk_%=:\n" \
"s_mov_b32 s78, 0x70000000\n" \
"s_mov_b32 s79, 0x70000000\n" \
"s_mov_b32 s81, 1\n" \
"s_mov_b32 s84, 0\n" \
"v_mov_b32 v10, 0\n" \
"s_waitcnt vmcnt(0) lgkmcnt(0)\n" \
"s_add_u32 s57, s56, s86\n" \
"buffer_load_dwordx4 v[16:19], v3, s[36:39], s57 offen offset:0 sc1\n" \
"buffer_load_dwordx4 v[20:23], v3, s[36:39], s57 offen offset:1024 sc1\n" \
"buffer_load_dwordx4 v[24:27], v3, s[36:39], s57 offen offset:2048 sc1\n" \
"buffer_load_dwordx4 v[28:31], v3, s[36:39], s57 offen offset:3072 sc1\n" \
"s_add_u32 s57, s56, s87\n" \
"buffer_load_dwordx4 v[32:35], v3, s[36:39], s57 offen offset:0 sc1\n" \
"buffer_load_dwordx4 v[36:39], v3, s[36:39], s57 offen offset:1024 sc1\n" \
"buffer_load_dwordx4 v[40:43], v3, s[36:39], s57 offen offset:2048 sc1\n" \
"buffer_load_dwordx4 v[44:47], v3, s[36:39], s57 offen offset:3072 sc1\n" \
"s_add_u32 s57, s56, s88\n" \
"buffer_load_dwordx4 v[48:51], v3, s[36:39], s57 offen offset:0 sc1\n" \
"buffer_load_dwordx4 v[52:55], v3, s[36:39], s57 offen offset:1024 sc1\n" \
"buffer_load_dwordx4 v[56:59], v3, s[36:39], s57 offen offset:2048 sc1\n" \
"buffer_load_dwordx4 v[60:63], v3, s[36:39], s57 offen offset:3072 sc1\n" \
"s_add_u32 s57, s56, s89\n" \
"buffer_load_dwordx4 v[64:67], v3, s[36:39], s57 offen offset:0 sc1\n" \
"buffer_load_dwordx4 v[68:71], v3, s[36:39], s57 offen offset:1024 sc1\n" \
"ds_read_b128 v[80:83], v2 offset:0\n" \
"ds_read_b128 v[84:87], v2 offset:1024\n" \
"ds_read_b128 v[88:91], v2 offset:2048\n" \
"s_waitcnt lgkmcnt(0)\n" \
"v_mov_b32 v13, 0x7ffffff0\n" \
"buffer_store_dword v13, v13, s[36:39], 0 offen\n" \
"buffer_store_dword v13, v13, s[36:39], 0 offen\n" \
"buffer_store_dword v13, v13, s[36:39], 0 offen\n" \
"s_waitcnt vmcnt(14)\n" \
"s_nop 7\n" \
"s_nop 7\n" \
".p2align 6\n" \
".Lsg2_loop_%=:\n" \
"v_mfma_f32_16x16x32_bf16 v[96:99], a[0:3], v[16:19], 0\n" \
"ds_write_b128 v7, v[104:107] offset:2048\n" \
"v_mfma_f32_16x16x32_bf16 v[100:103], a[128:131], v[16:19], 0\n" \
"ds_write_b128 v7, v[108:111] offset:3072\n" \
"ds_read_b128 v[92:95], v2 offset:3072\n" \
"v_mfma_f32_16x16x32_bf16 v[104:107], v[128:131], v[16:19], 0\n" \
"s_waitcnt lgkmcnt(7)\n" \
"v_mfma_f32_16x16x32_bf16 v[108:111], v[80:83], v[16:19], 0\n" \
"buffer_load_dwordx4 v[16:19], v3, s[36:39], s57 offen offset:2048 sc1\n" \
"v_mfma_f32_16x16x32_bf16 v[96:99], a[4:7], v[20:23], v[96:99]\n" \
"v_readfirstlane_b32 s96, v9\n" \
"v_mfma_f32_16x16x32_bf16 v[100:103], a[132:135], v[20:23], v[100:103]\n" \
"ds_read_b128 v[80:83], v2 offset:4096\n" \
"v_mfma_f32_16x16x32_bf16 v[104:107], v[132:135], v[20:23], v[104:107]\n" \
"s_waitcnt lgkmcnt(7)\n" \
"v_mfma_f32_16x16x32_bf16 v[108:111], v[84:87], v[20:23], v[108:111]\n" \
"buffer_load_dwordx4 v[20:23], v3, s[36:39], s57 offen offset:3072 sc1\n" \
"v_mfma_f32_16x16x32_bf16 v[96:99], a[8:11], v[24:27], v[96:99]\n" \
"s_waitcnt lgkmcnt(2)\n" \
"s_cmp_lg_u32 s81, 0\n" \
"s_cbranch_scc1 .Lsg2_nb_%=\n" \
"s_barrier\n" \
".Lsg2_nb_%=:\n" \
"v_mfma_f32_16x16x32_bf16 v[100:103], a[136:139], v[24:27], v[100:103]\n" \
"ds_read_b128 v[84:87], v2 offset:5120\n" \
"v_xor_b32 v7, s84, v7\n" \
"v_mfma_f32_16x16x32_bf16 v[104:107], v[136:139], v[24:27], v[104:107]\n" \
"s_waitcnt vmcnt(15)\n" \
"v_mfma_f32_16x16x32_bf16 v[108:111], v[88:91], v[24:27], v[108:111]\n" \
"s_add_u32 s57, s56, s90\n" \
"buffer_load_dwordx4 v[24:27], v3, s[36:39], s57 offen offset:0 sc1\n" \
"v_mfma_f32_16x16x32_bf16 v[96:99], a[12:15], v[28:31], v[96:99]\n" \
"s_add_u32 s76, s78, 4194304\n" \
"s_add_u32 s80, s78, 0\n" \
"s_and_b32 s96, s96, 1\n" \
"v_mfma_f32_16x16x32_bf16 v[100:103], a[140:143], v[28:31], v[100:103]\n" \
"ds_read_b128 v[88:91], v2 offset:6144\n" \
"s_cselect_b32 s76, 0x7ffffff0, s76\n" \
"s_cselect_b32 s77, s78, 0x7ffffff0\n" \
"v_mfma_f32_16x16x32_bf16 v[104:107], v[140:143], v[28:31], v[104:107]\n" \
"s_waitcnt vmcnt(15) lgkmcnt(3)\n" \
"s_cselect_b32 s80, s80, 0x7ffffff0\n" \
"s_cselect_b32 s94, 0x7ffffff0, s79\n" \
"v_mfma_f32_16x16x32_bf16 v[108:111], v[92:95], v[28:31], v[108:111]\n" \
"buffer_load_dwordx4 v[28:31], v3, s[36:39], s57 offen offset:1024 sc1\n" \
"v_mfma_f32_16x16x32_bf16 v[96:99], a[16:19], v[32:35], v[96:99]\n" \
"s_cmp_lt_i32 s44, s50\n" \
"s_cselect_b32 s82, s45, s47\n" \
"v_mfma_f32_16x16x32_bf16 v[100:103], a[144:147], v[32:35], v[100:103]\n" \
"ds_read_b128 v[92:95], v2 offset:7168\n" \
"s_add_u32 s82, s82, s44\n" \
"v_mfma_f32_16x16x32_bf16 v[104:107], v[144:147], v[32:35], v[104:107]\n" \
"s_waitcnt vmcnt(15) lgkmcnt(3)\n" \
"s_lshr_b32 s59, s82, 1\n" \
"s_add_u32 s59, s59, 1\n" \
"v_mfma_f32_16x16x32_bf16 v[108:111], v[80:83], v[32:35], v[108:111]\n" \
"buffer_load_dwordx4 v[32:35], v3, s[36:39], s57 offen offset:2048 sc1\n" \
"v_mfma_f32_16x16x32_bf16 v[96:99], a[20:23], v[36:39], v[96:99]\n" \
"s_waitcnt vmcnt(7)\n" \
"v_readfirstlane_b32 s54, v10\n" \
"v_mfma_f32_16x16x32_bf16 v[100:103], a[148:151], v[36:39], v[100:103]\n" \
"ds_read_b128 v[80:83], v2 offset:8192\n" \
"v_mfma_f32_16x16x32_bf16 v[104:107], v[148:151], v[36:39], v[104:107]\n" \
"s_waitcnt lgkmcnt(3)\n" \
"s_and_b32 s54, s54, 1\n" \
"s_cbranch_scc1 .Lsg2_p_%=\n" \
".Lsg2_pk_%=:\n" \
"v_mfma_f32_16x16x32_bf16 v[108:111], v[84:87], v[36:39], v[108:111]\n" \
"buffer_load_dwordx4 v[36:39], v3, s[36:39], s57 offen offset:3072 sc1\n" \
"v_mfma_f32_16x16x32_bf16 v[96:99], a[24:27], v[40:43], v[96:99]\n" \
"buffer_load_dwordx2 v[14:15], v6, s[36:39], s80 offen sc1\n" \
"ds_read_b128 v[112:115], v8 offset:0\n" \
"v_mfma_f32_16x16x32_bf16 v[100:103], a[152:155], v[40:43], v[100:103]\n" \
"ds_read_b128 v[84:87], v2 offset:9216\n" \
"v_mfma_f32_16x16x32_bf16 v[104:107], v[152:155], v[40:43], v[104:107]\n" \
"s_waitcnt lgkmcnt(4)\n" \
"buffer_load_dwordx2 v[12:13], v5, s[64:67], s77 offen\n" \
"ds_read_b128 v[116:119], v8 offset:4096\n" \
"v_mfma_f32_16x16x32_bf16 v[108:111], v[88:91], v[40:43], v[108:111]\n" \
"s_add_u32 s57, s56, s91\n" \
"buffer_load_dwordx4 v[40:43], v3, s[36:39], s57 offen offset:0 sc1\n" \
"v_mfma_f32_16x16x32_bf16 v[96:99], a[28:31], v[44:47], v[96:99]\n" \
"ds_read_b128 v[120:123], v8 offset:8192\n" \
"v_mfma_f32_16x16x32_bf16 v[100:103], a[156:159], v[44:47], v[100:103]\n" \
"ds_read_b128 v[88:91], v2 offset:10240\n" \
"v_mfma_f32_16x16x32_bf16 v[104:107], v[156:159], v[44:47], v[104:107]\n" \
"s_waitcnt lgkmcnt(6)\n" \
"ds_read_b128 v[124:127], v8 offset:12288\n" \
"v_mfma_f32_16x16x32_bf16 v[108:111], v[92:95], v[44:47], v[108:111]\n" \
"buffer_load_dwordx4 v[44:47], v3, s[36:39], s57 offen offset:1024 sc1\n" \
"v_mfma_f32_16x16x32_bf16 v[96:99], a[32:35], v[48:51], v[96:99]\n" \
"v_mfma_f32_16x16x32_bf16 v[100:103], a[160:163], v[48:51], v[100:103]\n" \
"ds_read_b128 v[92:95], v2 offset:11264\n" \
"v_mfma_f32_16x16x32_bf16 v[104:107], v[160:163], v[48:51], v[104:107]\n" \
"s_waitcnt lgkmcnt(7)\n" \
"v_mfma_f32_16x16x32_bf16 v[108:111], v[80:83], v[48:51], v[108:111]\n" \
"buffer_load_dwordx4 v[48:51], v3, s[36:39], s57 offen offset:2048 sc1\n" \
"v_mfma_f32_16x16x32_bf16 v[96:99], a[36:39], v[52:55], v[96:99]\n" \
"s_waitcnt lgkmcnt(4)\n" \
"v_add_f32 v112, v112, v116\n" \
"v_add_f32_e64 v113, v113, v117\n" \
"v_mfma_f32_16x16x32_bf16 v[100:103], a[164:167], v[52:55], v[100:103]\n" \
"ds_read_b128 v[80:83], v2 offset:12288\n" \
"v_add_f32_e64 v114, v114, v118\n" \
"v_mfma_f32_16x16x32_bf16 v[104:107], v[164:167], v[52:55], v[104:107]\n" \
"v_add_f32_e64 v115, v115, v119\n" \
"v_mfma_f32_16x16x32_bf16 v[108:111], v[84:87], v[52:55], v[108:111]\n" \
"buffer_load_dwordx4 v[52:55], v3, s[36:39], s57 offen offset:3072 sc1\n" \
"s_waitcnt lgkmcnt(2)\n" \
"v_add_f32 v120, v120, v124\n" \
"v_mfma_f32_16x16x32_bf16 v[96:99], a[40:43], v[56:59], v[96:99]\n" \
"s_lshl_b32 s97, s44, 8\n" \
"s_add_u32 s97, s97, s49\n" \
"v_add_f32 v121, v121, v125\n" \
"v_add_f32 v122, v122, v126\n" \
"v_mfma_f32_16x16x32_bf16 v[100:103], a[168:171], v[56:59], v[100:103]\n" \
"ds_read_b128 v[84:87], v2 offset:13312\n" \
"v_add_f32_e64 v123, v123, v127\n" \
"v_mfma_f32_16x16x32_bf16 v[104:107], v[168:171], v[56:59], v[104:107]\n" \
"v_add_f32_e64 v112, v112, v120\n" \
"v_mfma_f32_16x16x32_bf16 v[108:111], v[88:91], v[56:59], v[108:111]\n" \
"s_add_u32 s57, s56, s92\n" \
"buffer_load_dwordx4 v[56:59], v3, s[36:39], s57 offen offset:0 sc1\n" \
"v_add_f32 v113, v113, v121\n" \
"v_mfma_f32_16x16x32_bf16 v[96:99], a[44:47], v[60:63], v[96:99]\n" \
"v_cmp_gt_u32_e64 s[62:63], s59, v11\n" \
"v_add_f32 v114, v114, v122\n" \
"v_add_f32 v115, v115, v123\n" \
"v_mfma_f32_16x16x32_bf16 v[100:103], a[172:175], v[60:63], v[100:103]\n" \
"ds_read_b128 v[88:91], v2 offset:14336\n" \
"v_xor_b32_e64 v8, s84, v8\n" \
"v_mfma_f32_16x16x32_bf16 v[104:107], v[172:175], v[60:63], v[104:107]\n" \
"s_waitcnt vmcnt(14) lgkmcnt(3)\n" \
"v_mul_f32 v116, 0xbfb8aa3b, v112\n" \
"v_mfma_f32_16x16x32_bf16 v[108:111], v[92:95], v[60:63], v[108:111]\n" \
"buffer_load_dwordx4 v[60:63], v3, s[36:39], s57 offen offset:1024 sc1\n" \
"v_mul_f32 v117, 0xbfb8aa3b, v113\n" \
"v_mfma_f32_16x16x32_bf16 v[96:99], a[48:51], v[64:67], v[96:99]\n" \
"s_add_u32 s54, s55, 1\n" \
"s_cmp_lt_u32 s54, s46\n" \
"v_mul_f32 v118, 0xbfb8aa3b, v114\n" \
"v_mul_f32 v119, 0xbfb8aa3b, v115\n" \
"v_mfma_f32_16x16x32_bf16 v[100:103], a[176:179], v[64:67], v[100:103]\n" \
"ds_read_b128 v[92:95], v2 offset:15360\n" \
"s_cselect_b64 s[62:63], s[62:63], -1\n" \
"v_exp_f32_e64 v116, v116\n" \
"v_mfma_f32_16x16x32_bf16 v[104:107], v[176:179], v[64:67], v[104:107]\n" \
"s_waitcnt vmcnt(14) lgkmcnt(3)\n" \
"s_mul_i32 s83, s82, 114688\n" \
"v_exp_f32 v117, v117\n" \
"v_mfma_f32_16x16x32_bf16 v[108:111], v[80:83], v[64:67], v[108:111]\n" \
"buffer_load_dwordx4 v[64:67], v3, s[36:39], s57 offen offset:2048 sc1\n" \
"v_exp_f32_e64 v118, v118\n" \
"v_mfma_f32_16x16x32_bf16 v[96:99], a[52:55], v[68:71], v[96:99]\n" \
"s_cmp_lg_u64 s[62:63], 0\n" \
"s_cselect_b32 s81, 1, 0\n" \
"v_exp_f32_e64 v119, v119\n" \
"v_mfma_f32_16x16x32_bf16 v[100:103], a[180:183], v[68:71], v[100:103]\n" \
"ds_read_b128 v[80:83], v2 offset:16384\n" \
"s_cselect_b32 s83, 0x70000000, s83\n" \
"v_add_f32_e64 v116, 1.0, v116\n" \
"v_mfma_f32_16x16x32_bf16 v[104:107], v[180:183], v[68:71], v[104:107]\n" \
"s_waitcnt vmcnt(14) lgkmcnt(3)\n" \
"v_add_f32 v117, 1.0, v117\n" \
"v_mfma_f32_16x16x32_bf16 v[108:111], v[84:87], v[68:71], v[108:111]\n" \
"buffer_load_dwordx4 v[68:71], v3, s[36:39], s57 offen offset:3072 sc1\n" \
"v_add_f32_e64 v118, 1.0, v118\n" \
"v_mfma_f32_16x16x32_bf16 v[96:99], a[56:59], v[16:19], v[96:99]\n" \
"v_add_f32_e64 v119, 1.0, v119\n" \
"v_mfma_f32_16x16x32_bf16 v[100:103], a[184:187], v[16:19], v[100:103]\n" \
"ds_read_b128 v[84:87], v2 offset:17408\n" \
"v_rcp_f32_e64 v116, v116\n" \
"v_mfma_f32_16x16x32_bf16 v[104:107], v[184:187], v[16:19], v[104:107]\n" \
"s_waitcnt vmcnt(14) lgkmcnt(3)\n" \
"s_mov_b32 s56, s83\n" \
"v_rcp_f32_e64 v117, v117\n" \
"v_mfma_f32_16x16x32_bf16 v[108:111], v[88:91], v[16:19], v[108:111]\n" \
"s_add_u32 s57, s56, s86\n" \
"buffer_load_dwordx4 v[16:19], v3, s[36:39], s57 offen offset:0 sc1\n" \
"v_rcp_f32 v118, v118\n" \
"v_mfma_f32_16x16x32_bf16 v[96:99], a[60:63], v[20:23], v[96:99]\n" \
"buffer_load_dword v9, off, s[36:39], s97 sc1\n" \
"v_rcp_f32_e64 v119, v119\n" \
"v_mfma_f32_16x16x32_bf16 v[100:103], a[188:191], v[20:23], v[100:103]\n" \
"ds_read_b128 v[88:91], v2 offset:18432\n" \
"v_mul_f32_e64 v112, v112, v116\n" \
"v_mfma_f32_16x16x32_bf16 v[104:107], v[188:191], v[20:23], v[104:107]\n" \
"s_waitcnt vmcnt(15) lgkmcnt(3)\n" \
"v_mul_f32 v113, v113, v117\n" \
"v_mfma_f32_16x16x32_bf16 v[108:111], v[92:95], v[20:23], v[108:111]\n" \
"buffer_load_dwordx4 v[20:23], v3, s[36:39], s57 offen offset:1024 sc1\n" \
"v_mul_f32_e64 v114, v114, v118\n" \
"v_mfma_f32_16x16x32_bf16 v[96:99], a[64:67], v[24:27], v[96:99]\n" \
"v_mul_f32_e64 v115, v115, v119\n" \
"v_cvt_pk_bf16_f32 v0, v112, v113\n" \
"v_mfma_f32_16x16x32_bf16 v[100:103], a[192:195], v[24:27], v[100:103]\n" \
"ds_read_b128 v[92:95], v2 offset:19456\n" \
"v_cvt_pk_bf16_f32 v1, v114, v115\n" \
"v_mfma_f32_16x16x32_bf16 v[104:107], v[192:195], v[24:27], v[104:107]\n" \
"s_waitcnt vmcnt(15) lgkmcnt(3)\n" \
"v_mfma_f32_16x16x32_bf16 v[108:111], v[80:83], v[24:27], v[108:111]\n" \
"buffer_load_dwordx4 v[24:27], v3, s[36:39], s57 offen offset:2048 sc1\n" \
"buffer_store_dwordx2 v[0:1], v6, s[36:39], s76 offen sc1\n" \
"v_lshlrev_b32 v112, 16, v0\n" \
"v_mfma_f32_16x16x32_bf16 v[96:99], a[68:71], v[28:31], v[96:99]\n" \
"v_and_b32 v113, 0xffff0000, v0\n" \
"v_lshlrev_b32_e64 v114, 16, v1\n" \
"v_mfma_f32_16x16x32_bf16 v[100:103], a[196:199], v[28:31], v[100:103]\n" \
"ds_read_b128 v[80:83], v2 offset:20480\n" \
"v_and_b32 v115, 0xffff0000, v1\n" \
"v_mfma_f32_16x16x32_bf16 v[104:107], v[196:199], v[28:31], v[104:107]\n" \
"s_waitcnt vmcnt(16) lgkmcnt(3)\n" \
"v_mfma_f32_16x16x32_bf16 v[108:111], v[84:87], v[28:31], v[108:111]\n" \
"buffer_load_dwordx4 v[28:31], v3, s[36:39], s57 offen offset:3072 sc1\n" \
"v_mfma_f32_16x16x32_bf16 v[96:99], a[72:75], v[32:35], v[96:99]\n" \
"s_waitcnt vmcnt(15)\n" \
"v_lshlrev_b32_e64 v120, 16, v14\n" \
"v_and_b32 v121, 0xffff0000, v14\n" \
"v_mfma_f32_16x16x32_bf16 v[100:103], a[200:203], v[32:35], v[100:103]\n" \
"ds_read_b128 v[84:87], v2 offset:21504\n" \
"s_waitcnt vmcnt(14)\n" \
"v_lshlrev_b32 v124, 16, v12\n" \
"v_mfma_f32_16x16x32_bf16 v[104:107], v[200:203], v[32:35], v[104:107]\n" \
"s_waitcnt lgkmcnt(3)\n" \
"v_and_b32 v125, 0xffff0000, v12\n" \
"v_mfma_f32_16x16x32_bf16 v[108:111], v[88:91], v[32:35], v[108:111]\n" \
"s_add_u32 s57, s56, s87\n" \
"buffer_load_dwordx4 v[32:35], v3, s[36:39], s57 offen offset:0 sc1\n" \
"v_lshlrev_b32_e64 v122, 16, v15\n" \
"v_mfma_f32_16x16x32_bf16 v[96:99], a[76:79], v[36:39], v[96:99]\n" \
"v_and_b32 v123, 0xffff0000, v15\n" \
"v_lshlrev_b32_e64 v126, 16, v13\n" \
"v_mfma_f32_16x16x32_bf16 v[100:103], a[204:207], v[36:39], v[100:103]\n" \
"ds_read_b128 v[88:91], v2 offset:22528\n" \
"v_and_b32 v127, 0xffff0000, v13\n" \
"v_mfma_f32_16x16x32_bf16 v[104:107], v[204:207], v[36:39], v[104:107]\n" \
"s_waitcnt vmcnt(14) lgkmcnt(3)\n" \
"v_fmac_f32 v124, v112, v120\n" \
"v_mfma_f32_16x16x32_bf16 v[108:111], v[92:95], v[36:39], v[108:111]\n" \
"buffer_load_dwordx4 v[36:39], v3, s[36:39], s57 offen offset:1024 sc1\n" \
"v_fmac_f32_e64 v125, v113, v121\n" \
"v_mfma_f32_16x16x32_bf16 v[96:99], a[80:83], v[40:43], v[96:99]\n" \
"v_fmac_f32 v126, v114, v122\n" \
"v_fmac_f32 v127, v115, v123\n" \
"v_mfma_f32_16x16x32_bf16 v[100:103], a[208:211], v[40:43], v[100:103]\n" \
"ds_read_b128 v[92:95], v2 offset:23552\n" \
"v_cvt_pk_bf16_f32 v118, v124, v125\n" \
"v_mfma_f32_16x16x32_bf16 v[104:107], v[208:211], v[40:43], v[104:107]\n" \
"s_waitcnt vmcnt(14) lgkmcnt(3)\n" \
"v_cvt_pk_bf16_f32 v119, v126, v127\n" \
"v_mfma_f32_16x16x32_bf16 v[108:111], v[80:83], v[40:43], v[108:111]\n" \
"buffer_load_dwordx4 v[40:43], v3, s[36:39], s57 offen offset:2048 sc1\n" \
"v_mfma_f32_16x16x32_bf16 v[96:99], a[84:87], v[44:47], v[96:99]\n" \
"buffer_store_dwordx2 v[118:119], v5, s[68:71], s77 offen\n" \
"v_mov_b32 v10, 2\n" \
"v_mfma_f32_16x16x32_bf16 v[100:103], a[212:215], v[44:47], v[100:103]\n" \
"ds_read_b128 v[80:83], v2 offset:24576\n" \
"v_mfma_f32_16x16x32_bf16 v[104:107], v[212:215], v[44:47], v[104:107]\n" \
"s_waitcnt vmcnt(15) lgkmcnt(3)\n" \
"v_mfma_f32_16x16x32_bf16 v[108:111], v[84:87], v[44:47], v[108:111]\n" \
"buffer_load_dwordx4 v[44:47], v3, s[36:39], s57 offen offset:3072 sc1\n" \
"v_mfma_f32_16x16x32_bf16 v[96:99], a[88:91], v[48:51], v[96:99]\n" \
"v_mfma_f32_16x16x32_bf16 v[100:103], a[216:219], v[48:51], v[100:103]\n" \
"ds_read_b128 v[84:87], v2 offset:25600\n" \
"v_mfma_f32_16x16x32_bf16 v[104:107], v[216:219], v[48:51], v[104:107]\n" \
"s_waitcnt vmcnt(15) lgkmcnt(3)\n" \
"v_mfma_f32_16x16x32_bf16 v[108:111], v[88:91], v[48:51], v[108:111]\n" \
"s_add_u32 s57, s56, s88\n" \
"buffer_load_dwordx4 v[48:51], v3, s[36:39], s57 offen offset:0 sc1\n" \
"v_mfma_f32_16x16x32_bf16 v[96:99], a[92:95], v[52:55], v[96:99]\n" \
"v_mfma_f32_16x16x32_bf16 v[100:103], a[220:223], v[52:55], v[100:103]\n" \
"ds_read_b128 v[88:91], v2 offset:26624\n" \
"v_mfma_f32_16x16x32_bf16 v[104:107], v[220:223], v[52:55], v[104:107]\n" \
"s_waitcnt vmcnt(15) lgkmcnt(3)\n" \
"v_mfma_f32_16x16x32_bf16 v[108:111], v[92:95], v[52:55], v[108:111]\n" \
"buffer_load_dwordx4 v[52:55], v3, s[36:39], s57 offen offset:1024 sc1\n" \
"v_mfma_f32_16x16x32_bf16 v[96:99], a[96:99], v[56:59], v[96:99]\n" \
"v_mfma_f32_16x16x32_bf16 v[100:103], a[224:227], v[56:59], v[100:103]\n" \
"ds_read_b128 v[92:95], v2 offset:28672\n" \
"v_mfma_f32_16x16x32_bf16 v[104:107], v[224:227], v[56:59], v[104:107]\n" \
"s_waitcnt vmcnt(15) lgkmcnt(3)\n" \
"s_mov_b32 s84, 0x4000\n" \
"v_mfma_f32_16x16x32_bf16 v[108:111], v[80:83], v[56:59], v[108:111]\n" \
"buffer_load_dwordx4 v[56:59], v3, s[36:39], s57 offen offset:2048 sc1\n" \
"v_mfma_f32_16x16x32_bf16 v[96:99], a[100:103], v[60:63], v[96:99]\n" \
"buffer_load_dword v11, v4, s[36:39], 0 offen sc1\n" \
"v_mfma_f32_16x16x32_bf16 v[100:103], a[228:231], v[60:63], v[100:103]\n" \
"ds_read_b128 v[80:83], v2 offset:0\n" \
"v_mfma_f32_16x16x32_bf16 v[104:107], v[228:231], v[60:63], v[104:107]\n" \
"s_waitcnt vmcnt(16) lgkmcnt(3)\n" \
"v_mfma_f32_16x16x32_bf16 v[108:111], v[84:87], v[60:63], v[108:111]\n" \
"buffer_load_dwordx4 v[60:63], v3, s[36:39], s57 offen offset:3072 sc1\n" \
"v_mfma_f32_16x16x32_bf16 v[96:99], a[104:107], v[64:67], v[96:99]\n" \
"v_mfma_f32_16x16x32_bf16 v[100:103], a[232:235], v[64:67], v[100:103]\n" \
"ds_read_b128 v[84:87], v2 offset:1024\n" \
"v_mfma_f32_16x16x32_bf16 v[104:107], v[232:235], v[64:67], v[104:107]\n" \
"s_waitcnt vmcnt(16) lgkmcnt(3)\n" \
"s_waitcnt vmcnt(11)\n" \
"s_mov_b64 exec, 1\n" \
"buffer_atomic_or v10, off, s[36:39], s94 sc0\n" \
"s_mov_b64 exec, -1\n" \
"s_mov_b32 s95, s78\n" \
"v_mfma_f32_16x16x32_bf16 v[108:111], v[88:91], v[64:67], v[108:111]\n" \
"s_add_u32 s57, s56, s89\n" \
"buffer_load_dwordx4 v[64:67], v3, s[36:39], s57 offen offset:0 sc1\n" \
"v_mfma_f32_16x16x32_bf16 v[96:99], a[108:111], v[68:71], v[96:99]\n" \
"s_lshl_b32 s78, s44, 15\n" \
"s_mov_b32 s79, s97\n" \
"v_mfma_f32_16x16x32_bf16 v[100:103], a[236:239], v[68:71], v[100:103]\n" \
"ds_read_b128 v[88:91], v2 offset:2048\n" \
"s_mov_b32 s44, s82\n" \
"s_add_u32 s55, s55, 1\n" \
"v_mfma_f32_16x16x32_bf16 v[104:107], v[236:239], v[68:71], v[104:107]\n" \
"s_waitcnt lgkmcnt(3)\n" \
"ds_write_b128 v7, v[96:99] offset:0\n" \
"v_mfma_f32_16x16x32_bf16 v[108:111], v[92:95], v[68:71], v[108:111]\n" \
"buffer_load_dwordx4 v[68:71], v3, s[36:39], s57 offen offset:1024 sc1\n" \
"ds_write_b128 v7, v[100:103] offset:1024\n" \
"s_cmp_eq_u32 s81, 0\n" \
"s_cbranch_scc1 .Lsg2_loop_%=\n" \
"s_nop 7\n" \
"s_nop 7\n" \
"s_waitcnt vmcnt(0) lgkmcnt(0)\n" \
"ds_write_b128 v7, v[104:107] offset:2048\n" \
"ds_write_b128 v7, v[108:111] offset:3072\n" \
"s_waitcnt lgkmcnt(0)\n" \
"s_barrier\n" \
"buffer_load_dword v9, off, s[36:39], s97 sc1\n" \
"v_readfirstlane_b32 s54, v10\n" \
"s_nop 0\n" \
"s_and_b32 s54, s54, 1\n" \
"s_cbranch_scc1 .Lsg2_q_%=\n" \
".Lsg2_qk_%=:\n" \
"s_waitcnt vmcnt(0)\n" \
"v_readfirstlane_b32 s96, v9\n" \
"s_add_u32 s76, s78, 4194304\n" \
"s_add_u32 s80, s78, 0\n" \
"s_and_b32 s96, s96, 1\n" \
"s_cselect_b32 s76, 0x7ffffff0, s76\n" \
"s_cselect_b32 s77, s78, 0x7ffffff0\n" \
"s_cselect_b32 s80, s80, 0x7ffffff0\n" \
"s_cselect_b32 s94, 0x7ffffff0, s79\n" \
"buffer_load_dwordx2 v[14:15], v6, s[36:39], s80 offen sc1\n" \
"buffer_load_dwordx2 v[12:13], v5, s[64:67], s77 offen\n" \
"ds_read_b128 v[112:115], v8 offset:0\n" \
"ds_read_b128 v[116:119], v8 offset:4096\n" \
"ds_read_b128 v[120:123], v8 offset:8192\n" \
"ds_read_b128 v[124:127], v8 offset:12288\n" \
"s_waitcnt lgkmcnt(2)\n" \
"v_add_f32 v112, v112, v116\n" \
"v_add_f32 v113, v113, v117\n" \
"v_add_f32 v114, v114, v118\n" \
"v_add_f32 v115, v115, v119\n" \
"s_waitcnt lgkmcnt(0)\n" \
"v_add_f32 v120, v120, v124\n" \
"v_add_f32 v121, v121, v125\n" \
"v_add_f32 v122, v122, v126\n" \
"v_add_f32 v123, v123, v127\n" \
"v_add_f32 v112, v112, v120\n" \
"v_add_f32 v113, v113, v121\n" \
"v_add_f32 v114, v114, v122\n" \
"v_add_f32 v115, v115, v123\n" \
"v_xor_b32 v8, s84, v8\n" \
"v_mul_f32 v116, 0xbfb8aa3b, v112\n" \
"v_mul_f32 v117, 0xbfb8aa3b, v113\n" \
"v_mul_f32 v118, 0xbfb8aa3b, v114\n" \
"v_mul_f32 v119, 0xbfb8aa3b, v115\n" \
"v_exp_f32 v116, v116\n" \
"v_exp_f32 v117, v117\n" \
"v_exp_f32 v118, v118\n" \
"v_exp_f32 v119, v119\n" \
"v_add_f32 v116, 1.0, v116\n" \
"v_add_f32 v117, 1.0, v117\n" \
"v_add_f32 v118, 1.0, v118\n" \
"v_add_f32 v119, 1.0, v119\n" \
"v_rcp_f32 v116, v116\n" \
"v_rcp_f32 v117, v117\n" \
"v_rcp_f32 v118, v118\n" \
"v_rcp_f32 v119, v119\n" \
"v_mul_f32 v112, v112, v116\n" \
"v_mul_f32 v113, v113, v117\n" \
"v_mul_f32 v114, v114, v118\n" \
"v_mul_f32 v115, v115, v119\n" \
"v_cvt_pk_bf16_f32 v0, v112, v113\n" \
"v_cvt_pk_bf16_f32 v1, v114, v115\n" \
"buffer_store_dwordx2 v[0:1], v6, s[36:39], s76 offen sc1\n" \
"v_lshlrev_b32 v112, 16, v0\n" \
"v_and_b32 v113, 0xffff0000, v0\n" \
"v_lshlrev_b32 v114, 16, v1\n" \
"v_and_b32 v115, 0xffff0000, v1\n" \
"s_waitcnt vmcnt(2)\n" \
"v_lshlrev_b32 v120, 16, v14\n" \
"v_and_b32 v121, 0xffff0000, v14\n" \
"s_waitcnt vmcnt(1)\n" \
"v_lshlrev_b32 v124, 16, v12\n" \
"v_and_b32 v125, 0xffff0000, v12\n" \
"v_lshlrev_b32 v122, 16, v15\n" \
"v_and_b32 v123, 0xffff0000, v15\n" \
"v_lshlrev_b32 v126, 16, v13\n" \
"v_and_b32 v127, 0xffff0000, v13\n" \
"v_fmac_f32 v124, v112, v120\n" \
"v_fmac_f32 v125, v113, v121\n" \
"v_fmac_f32 v126, v114, v122\n" \
"v_fmac_f32 v127, v115, v123\n" \
"v_cvt_pk_bf16_f32 v118, v124, v125\n" \
"v_cvt_pk_bf16_f32 v119, v126, v127\n" \
"buffer_store_dwordx2 v[118:119], v5, s[68:71], s77 offen\n" \
"v_mov_b32 v10, 2\n" \
"s_waitcnt vmcnt(1)\n" \
"s_mov_b64 exec, 1\n" \
"buffer_atomic_or v10, off, s[36:39], s94 sc0\n" \
"s_mov_b64 exec, -1\n" \
"s_mov_b32 s95, s78\n" \
"s_waitcnt vmcnt(0)\n" \
"v_readfirstlane_b32 s54, v10\n" \
"s_nop 0\n" \
"s_and_b32 s54, s54, 1\n" \
"s_cbranch_scc1 .Lsg2_r_%=\n" \
".Lsg2_rk_%=:\n" \
"v_xor_b32 v7, 0x4000, v7\n" \
"s_waitcnt vmcnt(0) lgkmcnt(0)\n" \
"s_cmp_lt_u32 s55, s46\n" \
"s_cbranch_scc1 .Lsg2_prime_%=\n" \
"s_branch .Lsg2_end_%=\n" \
".Lsg2_w_%=:\n" \
"s_nop 7\n" \
"s_nop 7\n" \
"s_waitcnt vmcnt(0) lgkmcnt(0)\n" \
"s_mov_b32 s61, 0\n" \
".Lsg2_wa_%=:\n" \
"s_cmp_lg_u32 s60, 0\n" \
"s_cbranch_scc1 .Lsg2_wk_%=\n" \
"buffer_load_dword v11, v4, s[36:39], 0 offen sc1\n" \
"s_waitcnt vmcnt(0)\n" \
"v_cmp_gt_u32_e64 s[62:63], s59, v11\n" \
"s_nop 4\n" \
"s_cmp_eq_u64 s[62:63], 0\n" \
"s_cbranch_scc1 .Lsg2_wk_%=\n" \
"s_sleep 2\n" \
"s_add_u32 s61, s61, 1\n" \
"s_cmp_lt_u32 s61, 65536\n" \
"s_cbranch_scc1 .Lsg2_wa_%=\n" \
"s_mov_b32 s60, 1\n" \
"v_mov_b32 v11, 256\n" \
"v_mov_b32 v12, 1\n" \
"buffer_store_dword v12, v11, s[36:39], 0 offen sc1\n" \
"s_waitcnt vmcnt(0) lgkmcnt(0)\n" \
"s_branch .Lsg2_wk_%=\n" \
".Lsg2_p_%=:\n" \
"s_nop 7\n" \
"s_nop 7\n" \
"s_waitcnt vmcnt(0) lgkmcnt(0)\n" \
"s_add_u32 s54, s95, 0\n" \
"buffer_load_dwordx2 v[14:15], v6, s[36:39], s54 offen sc1\n" \
"buffer_load_dwordx2 v[12:13], v5, s[64:67], s95 offen\n" \
"v_lshlrev_b32 v112, 16, v0\n" \
"v_and_b32 v113, 0xffff0000, v0\n" \
"v_lshlrev_b32 v114, 16, v1\n" \
"v_and_b32 v115, 0xffff0000, v1\n" \
"s_waitcnt vmcnt(1)\n" \
"v_lshlrev_b32 v120, 16, v14\n" \
"v_and_b32 v121, 0xffff0000, v14\n" \
"s_waitcnt vmcnt(0)\n" \
"v_lshlrev_b32 v124, 16, v12\n" \
"v_and_b32 v125, 0xffff0000, v12\n" \
"v_lshlrev_b32 v122, 16, v15\n" \
"v_and_b32 v123, 0xffff0000, v15\n" \
"v_lshlrev_b32 v126, 16, v13\n" \
"v_and_b32 v127, 0xffff0000, v13\n" \
"v_fmac_f32 v124, v112, v120\n" \
"v_fmac_f32 v125, v113, v121\n" \
"v_fmac_f32 v126, v114, v122\n" \
"v_fmac_f32 v127, v115, v123\n" \
"v_cvt_pk_bf16_f32 v118, v124, v125\n" \
"v_cvt_pk_bf16_f32 v119, v126, v127\n" \
"buffer_store_dwordx2 v[118:119], v5, s[68:71], s95 offen\n" \
"s_waitcnt vmcnt(0) lgkmcnt(0)\n" \
"s_branch .Lsg2_pk_%=\n" \
".Lsg2_q_%=:\n" \
"s_nop 7\n" \
"s_nop 7\n" \
"s_waitcnt vmcnt(0) lgkmcnt(0)\n" \
"s_add_u32 s54, s95, 0\n" \
"buffer_load_dwordx2 v[14:15], v6, s[36:39], s54 offen sc1\n" \
"buffer_load_dwordx2 v[12:13], v5, s[64:67], s95 offen\n" \
"v_lshlrev_b32 v112, 16, v0\n" \
"v_and_b32 v113, 0xffff0000, v0\n" \
"v_lshlrev_b32 v114, 16, v1\n" \
"v_and_b32 v115, 0xffff0000, v1\n" \
"s_waitcnt vmcnt(1)\n" \
"v_lshlrev_b32 v120, 16, v14\n" \
"v_and_b32 v121, 0xffff0000, v14\n" \
"s_waitcnt vmcnt(0)\n" \
"v_lshlrev_b32 v124, 16, v12\n" \
"v_and_b32 v125, 0xffff0000, v12\n" \
"v_lshlrev_b32 v122, 16, v15\n" \
"v_and_b32 v123, 0xffff0000, v15\n" \
"v_lshlrev_b32 v126, 16, v13\n" \
"v_and_b32 v127, 0xffff0000, v13\n" \
"v_fmac_f32 v124, v112, v120\n" \
"v_fmac_f32 v125, v113, v121\n" \
"v_fmac_f32 v126, v114, v122\n" \
"v_fmac_f32 v127, v115, v123\n" \
"v_cvt_pk_bf16_f32 v118, v124, v125\n" \
"v_cvt_pk_bf16_f32 v119, v126, v127\n" \
"buffer_store_dwordx2 v[118:119], v5, s[68:71], s95 offen\n" \
"s_waitcnt vmcnt(0) lgkmcnt(0)\n" \
"s_branch .Lsg2_qk_%=\n" \
".Lsg2_r_%=:\n" \
"s_nop 7\n" \
"s_nop 7\n" \
"s_waitcnt vmcnt(0) lgkmcnt(0)\n" \
"s_add_u32 s54, s95, 0\n" \
"buffer_load_dwordx2 v[14:15], v6, s[36:39], s54 offen sc1\n" \
"buffer_load_dwordx2 v[12:13], v5, s[64:67], s95 offen\n" \
"v_lshlrev_b32 v112, 16, v0\n" \
"v_and_b32 v113, 0xffff0000, v0\n" \
"v_lshlrev_b32 v114, 16, v1\n" \
"v_and_b32 v115, 0xffff0000, v1\n" \
"s_waitcnt vmcnt(1)\n" \
"v_lshlrev_b32 v120, 16, v14\n" \
"v_and_b32 v121, 0xffff0000, v14\n" \
"s_waitcnt vmcnt(0)\n" \
"v_lshlrev_b32 v124, 16, v12\n" \
"v_and_b32 v125, 0xffff0000, v12\n" \
"v_lshlrev_b32 v122, 16, v15\n" \
"v_and_b32 v123, 0xffff0000, v15\n" \
"v_lshlrev_b32 v126, 16, v13\n" \
"v_and_b32 v127, 0xffff0000, v13\n" \
"v_fmac_f32 v124, v112, v120\n" \
"v_fmac_f32 v125, v113, v121\n" \
"v_fmac_f32 v126, v114, v122\n" \
"v_fmac_f32 v127, v115, v123\n" \
"v_cvt_pk_bf16_f32 v118, v124, v125\n" \
"v_cvt_pk_bf16_f32 v119, v126, v127\n" \
"buffer_store_dwordx2 v[118:119], v5, s[68:71], s95 offen\n" \
"s_waitcnt vmcnt(0) lgkmcnt(0)\n" \
"s_branch .Lsg2_rk_%=\n" \
".Lsg2_end_%=:\n" \
"s_endpgm\n"

typedef u32 v32u __attribute__((ext_vector_type(32)));
typedef float f32x2 __attribute__((ext_vector_type(2)));
__device__ __forceinline__ float xsum(float s) {
    auto a = __builtin_amdgcn_permlane16_swap(__builtin_bit_cast(u32, s), __builtin_bit_cast(u32, s), false, false);
    s = __builtin_bit_cast(float, (u32)a[0]) + __builtin_bit_cast(float, (u32)a[1]);
    auto b = __builtin_amdgcn_permlane32_swap(__builtin_bit_cast(u32, s), __builtin_bit_cast(u32, s), false, false);
    return __builtin_bit_cast(float, (u32)b[0]) + __builtin_bit_cast(float, (u32)b[1]);
}
__device__ __forceinline__ void ln_tile(const Params& p, const v4u (&xv)[8], int rt, int w, int lane) {
    const int n = lane & 15, g = lane >> 4;
    const rsrc_t rs = __builtin_amdgcn_make_buffer_rsrc(p.ws, 0, WS_END, 0x00020000);
    LAS float* red = (LAS float*)(uintptr_t)(136 * 1024);
    f32x2 xf[32], s2 = {0.f, 0.f};
#pragma unroll
    for (int i = 0; i < 32; ++i) { xf[i] = f32x2{bflo(xv[i >> 2][i & 3]), bfhi(xv[i >> 2][i & 3])}; s2 += xf[i]; }
    const float s = xsum(s2.x + s2.y);
    if (g == 0) red[n * 4 + w] = s;
    BAR();
    f32x4 t = *(LAS f32x4*)(red + n * 4);
    const float mean = ((t.x + t.y) + (t.z + t.w)) * (1.0f / DM);
    f32x2 q2 = {0.f, 0.f};
#pragma unroll
    for (int i = 0; i < 32; ++i) { const f32x2 a = xf[i] - mean; q2 += a * a; }
    const float q = xsum(q2.x + q2.y);
    if (g == 0) red[64 + n * 4 + w] = q;
    BAR();
    t = *(LAS f32x4*)(red + 64 + n * 4);
    const float var = ((t.x + t.y) + (t.z + t.w)) * (1.0f / DM);
    const float mb = rbf(mean);
    const float rd = 1.0f / rbf(sqrtf(rbf(rbf(var) + 1e-5f)));
#pragma unroll
    for (int i = 0; i < 8; ++i) {
        v4u o;
#pragma unroll
        for (int e = 0; e < 4; ++e) { const f32x2 a = xf[4 * i + e] - mb; const u32 d = pack2(a.x, a.y); const f32x2 y = f32x2{bflo(d), bfhi(d)} * rd; o[e] = pack2(y.x, y.y); }
        st128s(o, rs, WS_XF + rt * 32768 + (w * 8 + i) * 1024 + lane * 16);
    }
    WAIT_V0();
    __builtin_amdgcn_s_barrier(); CFENCE();
    if (w == 0 && lane == 0) __hip_atomic_fetch_add((GAS u32*)(p.ws + WS_CT) + CT_LN + (rt >> 4) * 32, 1u, RLXA);
}
__device__ __forceinline__ void ln_role(const Params& p, int c, int w) {
    const int lane = threadIdx.x & 63;
    const u16* xr = p.x + (size_t)(c * 16 + (lane & 15)) * DM + w * 256 + (lane >> 4) * 8;
    v4u xa[8], xb[8];
#pragma unroll
    for (int i = 0; i < 8; ++i) { xa[i] = *(const v4u*)(xr + i * 32); xb[i] = *(const v4u*)(xr + 128 * DM + i * 32); }
#pragma unroll 1
    for (int k = 0; k < 16; ++k) {
        v4u xc[8];
#pragma unroll
        for (int i = 0; i < 8; ++i) xc[i] = *(const v4u*)(xr + (size_t)min(k + 2, 15) * 128 * DM + i * 32);
        ln_tile(p, xa, k * 8 + c, w, lane);
#pragma unroll
        for (int i = 0; i < 8; ++i) { xa[i] = xb[i]; xb[i] = xc[i]; }
    }
}

__device__ __forceinline__ void p1_role(const Params& p, int b, int s0, int s1, int id, int w) {
    const int lane = __builtin_amdgcn_mbcnt_hi(~0u, __builtin_amdgcn_mbcnt_lo(~0u, 0u)), n = lane & 15, g = lane >> 4;
    const bool g1 = b >= 96;
    const int h = b & 31, kind = b >> 5;
    const int stride = g1 ? FF * 2 : 8192;
    const u16* src = g1 ? ((w >> 1) ? p.wu : p.wg) + (b - 96) * 64 : colsrc(p, kind == 0 ? 1 : (kind == 1 ? 0 : 2), 2 * h + (w >> 1));
    const v4u rs = mksrd(p.ws, WS_END), rw = mksrd(src, 1023 * stride + 128);
    const int v0 = lane * 16;
    const int v1 = (8 * (w & 1) + (lane >> 3)) * stride + ((lane & 7) ^ ((((lane >> 4) & 1) | ((w & 1) << 1)) << 1)) * 16;
    const int swz = ((n >> 3) | ((g & 1) << 1)), rowo = (8 * g + (n >> 2)) * 128 + (n & 3) * 8;
    const int v2 = (g1 ? 0 : (w >> 1) * 8192) + rowo + (((g1 ? w : 2 * (w & 1)) ^ swz) * 32);
    const int v3 = (g1 ? 8192 : (w >> 1) * 8192) + rowo + (((g1 ? w : 2 * (w & 1) + 1) ^ swz) * 32);
    const int v4 = WS_XF + (w >> 1) * 32768 + (w & 1) * 8192 + lane * 16;
    int v5, ostr, od;
    if (g1) { v5 = WS_HF + (g & 1) * (112 * 1024) + ((b - 96) * 2 + (w >> 1)) * 1024 + (2 * (w & 1) + (g >> 1)) * 256 + n * 16; ostr = 2 * 112 * 1024; od = 0; }
    else if (kind == 2) { v5 = WS_TK + h * TK_STR + 16384 + w * 2048 + lane * 16; ostr = 24576; od = 1024; }
    else { v5 = WS_TK + h * TK_STR + (kind == 0 ? 0 : 8192) + w * 1024 + lane * 16; ostr = 24576; od = 4096; }
    const int v6 = (w == 0 && lane == 0) ? WS_CT + (g1 ? CT_G1 + (b - 96) * 32 : CT_P1 + h * 32 + kind * 4) * 4 : OOB;
#define P1_IN "{v0}"(v0), "{v1}"(v1), "{v2}"(v2), "{v3}"(v3), "{v4}"(v4), "{s[36:39]}"(rs), "{s[40:43]}"(rw), "{s44}"(16 * stride), "{s46}"(s0), "{s47}"(s1), "{s48}"(w), "{s49}"(ostr), "{s50}"(od), "{s51}"(rfl(kind == 1 ? 0x3e0293ee : 0x3f800000))
    int a5 = v5, a6 = v6; u32 tc, tr;
    if (g1) asm volatile(ASM_P1_G1 : "+{v5}"(a5), "+{v6}"(a6), "={s70}"(tc), "={s72}"(tr) : P1_IN : CLOB_P1);
    else if (kind == 2) asm volatile(ASM_P1_V : "+{v5}"(a5), "+{v6}"(a6), "={s70}"(tc), "={s72}"(tr) : P1_IN : CLOB_P1);
    else asm volatile(ASM_P1_QK : "+{v5}"(a5), "+{v6}"(a6), "={s70}"(tc), "={s72}"(tr) : P1_IN : CLOB_P1);
}

#define TUNEV(i, c) (c)
#define C_MO 8
#define C_MM 8
#define C_X 136
#define C_Y 82
#define C_DS 40
#define C_VD 12
#define C_YL 8
#define C_LT -50
#define C_LA 30
#define C_PW 4
#define C_PW3 4
#define C_GD 0
#define C_OM 0
__device__ __forceinline__ int mlane(int n, int g) { return (g >> 1) * 512 + (n >> 3) * 256 + (g & 1) * 128 + (n & 7) * 16; }
__device__ __forceinline__ int mq(int j) { return ((j & 1) ? 0x4800 : 0) ^ ((j & 2) ? 0x800 : 0); }
__device__ __forceinline__ int hword(int i, int cls) { return WS_M + (cls ^ 0x1000 ^ ((i & 32) ? 0x4800 : 0)) + ((i >> 4) & 1) * 32768 + (i & 15) * 128; }
__device__ __forceinline__ int oblk(const Params& p, int rg, int q, int cls) { const int m = TUNEV(8, C_OM); return m == 0 ? rg * 32768 + q * 8192 : (q * 2 + (rg ^ ((cls >> 14) & 1) ^ (m & 1))) * 8192; }
__device__ __forceinline__ void qk_role(const Params& p, int h, int par, int rot) {
    const int lane = __builtin_amdgcn_mbcnt_hi(~0u, __builtin_amdgcn_mbcnt_lo(~0u, 0u)), w = rfl(threadIdx.x >> 6), n = lane & 15, g = lane >> 4;
    const v4u rs = mksrd(p.ws, WS_END), rw = mksrd(colsrc(p, w >> 1, 2 * h + (w & 1)), 1023 * 8192 + 128);
    const int l16 = lane * 16, v1 = l16 + w * 4096, v2 = v1 + 65536, v3 = l16 + 112 * 1024;
    const int v4 = WS_M + (w >> 1) * 32768 + (w & 1) * 1024 + mlane(n, g);
    const int v5 = 152 * 1024 + (32 * w + n) * 32 + (g ^ (n >> 2)) * 8;
    const int v6 = 152 * 1024 + (4 * g + (n >> 2)) * 32 + ((n & 3) ^ g) * 8;
    const int v15 = w == 0 ? min(l16, 64) : OOB;
    const int v7 = (n & 2) == 0 ? WS_O + amap(oblk(p, n >> 3, h >> 3, par) + (h & 7) * 1024 + w * 256 + (g >> 1) * 128 + ((n >> 2) & 1) * 64 + (g & 1) * 32 + (n & 1) * 16, par) : OOB;
    const int v8 = WS_V + amap(h * 2048 + w * 512 + lane * 4, par);
    const int v9 = w == 0 ? hword(lane, par) : OOB;
    const int v10 = (w == 0 && lane == 0) ? 0 : OOB;
    const int v11 = WS_OF + h * OF_STR + (w & 1) * 4096 + w * 1024 + l16;
    const int v12 = (n & 2) ? 0xEEEE : 0x4444;
    const int v14 = 16 * (w & 1) + n - 4 * g;
    const int r8 = lane >> 3;
    const int v32 = r8 * 8192 + ((lane & 7) ^ (((r8 >> 1) & 1) << 1)) * 16, v33 = (8 + r8) * 8192 + ((lane & 7) ^ ((((r8 >> 1) & 1) | 2) << 1)) * 16;
    const int swz = (n >> 3) | ((g & 1) << 1), rowo = (w >> 1) * 4096 + (8 * g + (n >> 2)) * 128 + (n & 3) * 8;
    const int v34 = rowo + ((2 * (w & 1)) ^ swz) * 32, v35 = rowo + ((2 * (w & 1) + 1) ^ swz) * 32;
    asm volatile(ASM_QK :: "{v1}"(v1), "{v2}"(v2), "{v3}"(v3), "{v4}"(v4), "{v5}"(v5), "{v6}"(v6), "{v7}"(v7), "{v8}"(v8), "{v9}"(v9), "{v10}"(v10), "{v11}"(v11), "{v12}"(v12),
        "{v14}"(v14), "{v15}"(v15), "{v[32:35]}"(v4u{(u32)v32, (u32)v33, (u32)v34, (u32)v35}), "{s[36:39]}"(rs), "{s[40:43]}"(rw), "{s[44:45]}"(v2u{(u32)TUNEV(0, C_MO), (u32)TUNEV(1, C_MM)}), "{s46}"(TUNEV(2, C_X)), "{s17}"(TUNEV(3, C_Y)), "{s[12:15]}"(v4u{(u32)TUNEV(10, C_LT), (u32)TUNEV(11, C_LA), (u32)TUNEV(4, C_DS), (u32)rot << 21}), "{s10}"(par ^ mq(rot)), "{s47}"(WS_DBG + 131072 + (h * 4 + w) * 2048), "{s48}"(w),
        "{s63}"(h * TK_STR), "{s80}"(WS_CT + (CT_P1 + h * 32) * 4), "{s87}"(WS_CT + (CT_AT + h * 32) * 4), "{s94}"(hword(h, par)) : "v255", "a255", "s99", "memory");
    __builtin_unreachable();
}
__device__ __forceinline__ void vo_role(const Params& p, int c, int par) {
    const int lane = __builtin_amdgcn_mbcnt_hi(~0u, __builtin_amdgcn_mbcnt_lo(~0u, 0u)), w = rfl(threadIdx.x >> 6), n = lane & 15, g = lane >> 4;
    const int rg = RGX ? c >> 5 : (c >> 4) & 1, cs = RGX ? c & 31 : (c & 15) + 16 * (c >> 5);
    const v4u rs = mksrd(p.ws, WS_END), rw = mksrd(colsrc(p, 2, c) + (size_t)(256 * w) * 4096, 255 * 8192 + 128), ro = mksrd(colsrc(p, 3, cs >> 1) + (size_t)(1024 * w) * 1024 + (cs & 1) * 32, 1023 * 2048 + 64), rm = mksrd(p.mem0, 16384);
    const int v0 = lane * 16, v1 = (lane >> 3) * 8192 + (lane & 7) * 16, v2 = (lane >> 2) * 2048 + (lane & 3) * 16;
    const int v3 = (8 * g + (n >> 2)) * 128 + w * 32 + (n & 3) * 8;
    const int v4 = w * 40960 + (16 * (g & 1) + 8 * (g >> 1) + 4 * (n >> 3) + ((n >> 2) & 1)) * 64 + (n & 3) * 8;
    const int v5 = WS_M + (w >> 1) * 32768 + (w & 1) * 1024 + mlane(n, g);
    const int v6 = WS_O + amap(oblk(p, rg, w, par) + (2 * (n >> 3) + (g >> 1)) * 256 + (g & 1) * 128 + (n & 7) * 16, par);
    const int v7 = WS_V + amap(c * 1024 + w * 256 + lane * 4, par);
    const int v8 = (w == 0 && (lane & 3) == 0) ? WS_M + (par ^ mq(cs >> 3)) + ((cs >> 2) & 1) * 32768 + ((cs & 3) * 2 + rg) * 256 + lane * 4 : OOB;
    const int v9 = (w == 0 && lane < 32) ? hword(lane, par) : OOB;
    const int v12 = (n & 2) ? 0xEEEE : 0x4444;
    const int v13 = (4 * rg + 2 * ((lane >> 4) & 1) + ((lane >> 2) & 1)) * 2048 + (32 * cs + 16 * (lane >> 5) + 2 * ((lane >> 3) & 1)) * 2;
    const int v15 = 16384 + (g * 16 + ((n >> 1) & 1) * 8 + (n >> 2)) * 32 + w * 8 + (n & 1) * 4;
    asm volatile(ASM_VO :: "{v0}"(v0), "{v1}"(v1), "{v2}"(v2), "{v3}"(v3), "{v4}"(v4), "{v5}"(v5), "{v6}"(v6), "{v7}"(v7), "{v8}"(v8), "{v9}"(v9), "{v12}"(v12),
        "{v13}"(v13), "{v15}"(v15), "{v28}"((w == 0 && lane == 0) ? WS_CT + (CT_P1 + (c >> 1) * 32 + 12 + (c & 1) * 4) * 4 : OOB), "{s[36:39]}"(rs), "{s[40:43]}"(rw), "{s[64:67]}"(ro), "{s[68:71]}"(rm), "{s[44:45]}"(v2u{(u32)TUNEV(0, C_MO), (u32)TUNEV(1, C_MM)}), "{s46}"(TUNEV(12, C_VD)), "{s17}"(TUNEV(3, C_Y)),
        "{s63}"(par), "{s74}"(cs >= 16 ? TUNEV(15, C_YL) : 0), "{s47}"(WS_DBG + 131072 + ((32 + c) * 4 + w) * 2048), "{s48}"(w) : "v255", "a255", "s99", "memory");
    __builtin_unreachable();
}
__device__ __forceinline__ void s64_role(const Params& p, int kind, int cb, int r, int id, int w) {
    const int lane = __builtin_amdgcn_mbcnt_hi(~0u, __builtin_amdgcn_mbcnt_lo(~0u, 0u)), n = lane & 15, g = lane >> 4;
    const int kr = kind ? 896 : 1024;
    const v4u rs = mksrd(p.ws, WS_END), rw = mksrd((kind ? p.wd + cb * 64 : colsrc(p, 3, cb)) + (size_t)(kr * w) * 1024, (kr - 1) * 2048 + 128), rx = mksrd(p.x, 2048 * 2048), ro = mksrd(p.out, 2048 * 2048);
    const int v2 = lane * 16 + w * 32768, v0 = (lane >> 3) * 2048 + (lane & 7) * 16;
    const int v1 = w * 32768 + ((kind ? 8 : 4) * g + (n >> 2)) * 128 + (n & 3) * 8;
    const int v3 = (kind ? WS_HF + w * 28 * 1024 : WS_OF + 8 * w * OF_STR) + lane * 16;
    const int v4 = WS_CT + (kind ? CT_G1 + (14 * w + min(n, 13)) * 32 : CT_AT + (8 * w + (n & 7)) * 32) * 4;
    const int v5 = n * 2048 + cb * 128 + w * 32 + g * 8, v6 = WS_GT + cb * 2048 + w * 512 + lane * 8;
    const int v7 = 131072 + w * 4096 + lane * 16, v8 = 131072 + w * 1024 + lane * 16;
    const int ha = TUNEV(6, S64_HEAD);
    const int nt = kind ? (r < 2 ? 22 : 21) : (128 - ha) / 4 + (r < 2 ? ha / 2 : 0), rt = kind ? 127 - r : (r < 2 ? r : ha + r);
#define S64_IO "{v7}"(v7), "{v8}"(v8), "{s44}"(rt), "{v0}"(v0), "{v1}"(v1), "{v2}"(v2), "{v3}"(v3), "{v4}"(v4), "{v5}"(v5), "{v6}"(v6), "{s[36:39]}"(rs), "{s[40:43]}"(rw), "{s[64:67]}"(rx), "{s[68:71]}"(ro), \
        "{s45}"(2), "{s47}"(kind ? -6 : 4), "{s50}"(kind ? -128 : ha), "{s46}"(nt), "{s48}"(w), "{s49}"(WS_CT + CT_S * 4 + cb * 16 + w * 4), "{s51}"(w ? OOB : WS_DBG + id * 128), "{s99}"(kind == 0 && r < 2 ? TUNEV(9, C_PW3) : TUNEV(5, C_PW)), "{s85}"(kind ? cb % 7 : cb & 7)
    if (kind) asm volatile(ASM_S64_G2 :: S64_IO : "v255", "a255", "memory");
    else asm volatile(ASM_S64_WO :: S64_IO : "v255", "a255", "memory");
    __builtin_unreachable();
}
#define MAIN_BODY \
int gx = (id >> 5) - 4, gr = id & 31; \
    if (id < 128) { \
        const int x = id >> 5, t = id & 31; \
        const int nq = PLACE == 0 ? (x < 2 ? 16 : 0) : (PLACE == 1 ? 8 : (x < 2 ? 16 : 0)), nv = PLACE == 2 ? (x < 2 ? 16 : (x == 2 ? 32 : 0)) : 16; \
        if (t < nq) qk_role(p, nq * x + t, par, x & TUNEV(14, 1)); \
        else if (t < nq + nv) vo_role(p, 16 * x + t - nq, par); \
        gx = -1; gr = PLACE == 0 ? (x - 2) * 16 + t - 16 : (PLACE == 1 ? x * 8 + t - 24 : t); \
        id = 96 + gr; \
        STAMP(0); \
    } \
    if (gx >= 0 && gr >= 16 && gr < 30) for (int i = TUNEV(7, C_GD); i > 0; --i) __builtin_amdgcn_s_sleep(32); \
    if (gx < 0 || gr < 30) p1_role(p, gx < 0 ? 64 + gr : (gr < 16 ? gx * 8 + (gr & 7) + (gr >> 3) * 32 : 96 + gx * 14 + gr - 16), 0, 64, id, wv_); \
    else ln_role(p, gx * 2 + gr - 30, wv_); \
    STAMP(2); \
    s64_role(p, gx < 1 ? 0 : 1, gr & 15, (gx < 1 ? 2 * gx + 2 : 2 * gx - 2) + (gr >> 4), id, wv_);
constexpr int LDS_BYTES = 160 * 1024;
__device__ __forceinline__ int take_role(const Params& p, LAS u32* L) {
    if (threadIdx.x == 0) {
        GAS u32* tk = (GAS u32*)(p.ws + WS_CT) + CT_TICK;
        const u32 x = xcc_id() & 7u;
        const u32 t = __hip_atomic_fetch_add(tk + 64 * x, 1u, RLXA);
        u32 id = 32 * x + t;
        if (t >= 32u) {
            u32 o = __hip_atomic_fetch_add(tk + 64 * 8, 1u, RLXA);
            for (u32 spins = 0; spins < (1u << 20); ++spins) {
                u32 tot = 0;
                for (int k = 0; k < 8; ++k) tot += __hip_atomic_load(tk + 64 * k, RLXA);
                if (tot >= 256u) break;
                __builtin_amdgcn_s_sleep(8);
            }
            id = 255u;
            for (int k = 0; k < 8; ++k) {
                const u32 ck = __hip_atomic_load(tk + 64 * k, RLXA);
                const u32 fr = ck < 32u ? 32u - ck : 0u;
                if (o < fr) { id = 32 * k + ck + o; break; }
                o -= fr;
            }
        }
        *L = id;
    }
    BAR();
    const int id = rfl(*L);
    BAR();
    return id;
}
#define STAMP(i) do {} while (0)
__global__ void __launch_bounds__(256, 1) k_main(Params p) {
    extern __shared__ __attribute__((aligned(16))) u8 smem[];
    int par = rfl(*(const GAS u32*)(p.ws + WS_CT + CT_PAR * 4)) & 1;
    par = (par << 13) | ((TUNEV(13, 0) & 3) << 11) | ((TUNEV(13, 0) & 4) << 12);
    int id = take_role(p, (LAS u32*)smem);
    const int wv_ = rfl(threadIdx.x >> 6);
    STAMP(0);
    MAIN_BODY
    STAMP(7);
}
__global__ void __launch_bounds__(256) k_zero(Params p) {
    __shared__ u32 vote[4];
    u8* ws = p.ws;
    v4u z = {0u, 0u, 0u, 0u};
    if (WARM && blockIdx.x != 0) {
        const int i = (blockIdx.x - 1) * 32 + (threadIdx.x >> 3), m2 = min(i & 7, 2);
        v4u t[5];
#pragma unroll
        for (int m = 0; m < 4; ++m) t[m] = __builtin_nontemporal_load((const v4u*)(wmat(p, m) + (size_t)i * 512 + slowpos(wmat(p, m)) * 64 + (threadIdx.x & 7) * 8));
        if ((i & 7) < 3) t[4] = __builtin_nontemporal_load((const v4u*)(wmat(p, m2) + (size_t)(i >> 3) * 4096 + slowpos2(wmat(p, m2)) * 64 + (threadIdx.x & 7) * 8));
#pragma unroll
        for (int m = 0; m < 4; ++m) *(v4u*)(ws + WS_WC + (size_t)i * 1024 + (m == 3 ? 4 : m) * 128 + (threadIdx.x & 7) * 16) = t[m];
        if ((i & 7) < 3) *(v4u*)(ws + WS_WC + (size_t)(i >> 3) * 8192 + (5 + m2) * 128 + (threadIdx.x & 7) * 16) = t[4];
    }
    if (blockIdx.x == 0) {
        const rsrc_t rs = __builtin_amdgcn_make_buffer_rsrc(ws, 0, 2 * MB, 0x00020000);
        const int lane = threadIdx.x & 63, w = threadIdx.x >> 6, base = 2 * MB - 32768 + (w & 1) * 16384 + (w >> 1) * 2048 + (lane & 31) * 4;
        st32s(0u, rs, base + 1024); st32s(0u, rs, base + 8192 + 1024);
        u32 a = 65535u, b = 65535u;
#pragma unroll 1
        for (int k = 0; k < 8; ++k) {
            u64 t0, t1, t2;
            asm volatile("s_waitcnt vmcnt(0)\n\ts_memtime %0\n\ts_waitcnt lgkmcnt(0)" : "=s"(t0) :: "memory");
            st32s(0u, rs, base);
            asm volatile("s_waitcnt vmcnt(0)\n\ts_memtime %0\n\ts_waitcnt lgkmcnt(0)" : "=s"(t1) :: "memory");
            st32s(0u, rs, base + 8192);
            asm volatile("s_waitcnt vmcnt(0)\n\ts_memtime %0\n\ts_waitcnt lgkmcnt(0)" : "=s"(t2) :: "memory");
            a = min(a, (u32)(t1 - t0)); b = min(b, (u32)(t2 - t1));
            if (2 * min(a, b) > max(a, b) && 8 * max(a, b) > 9 * min(a, b) && max(a, b) < 1000u) break;
        }
        vote[w] = (a << 16) | b;
        __syncthreads();
        if (threadIdx.x == 0) {
            for (int k = 0; k < 4; ++k) { a = min(a, vote[k] >> 16); b = min(b, vote[k] & 65535u); }
            z.x = (b < a ? 1u : 0u) ^ (xcc_id() >= 4 ? 1u : 0u);
        }
    }
    if (blockIdx.x < 32) ((v4u*)(ws + (blockIdx.x >> 4) * WS_M))[(blockIdx.x & 15) * 256 + threadIdx.x] = z;
}

extern "C" void kernel_launch(void* const* d_in, const int* in_sizes, int n_in, void* d_out, int out_size, void* d_ws, size_t ws_size, hipStream_t stream) {
    Params p;
    p.x = (const u16*)d_in[0]; p.mem0 = (const u16*)d_in[1]; p.wq = (const u16*)d_in[2]; p.wk = (const u16*)d_in[3]; p.wv = (const u16*)d_in[4]; p.wo = (const u16*)d_in[5];
    p.wg = (const u16*)d_in[8]; p.wu = (const u16*)d_in[9]; p.wd = (const u16*)d_in[10];
    p.out = (u16*)d_out;
    p.ws = (u8*)(((uintptr_t)d_ws + (2u * MB - 1)) & ~(uintptr_t)(2u * MB - 1));
    (void)hipFuncSetAttribute((const void*)k_main, hipFuncAttributeMaxDynamicSharedMemorySize, LDS_BYTES);
    hipLaunchKernelGGL(k_zero, dim3(257), dim3(256), 0, stream, p);
    hipLaunchKernelGGL(k_main, dim3(256), dim3(256), LDS_BYTES, stream, p);
}
```

```cpp
#define C_MO 6
#define C_MM 6
#define C_PW 10
#pragma clang diagnostic ignored "-Wint-to-pointer-cast"
#pragma clang diagnostic ignored "-Winline-asm"
#include <hip/hip_runtime.h>
#include <hip/hip_bf16.h>
#include <stdint.h>

#define LAS __attribute__((address_space(3)))
#define GAS __attribute__((address_space(1)))
typedef unsigned u32;
typedef unsigned char u8;
typedef unsigned short u16;
typedef unsigned long long u64;
typedef u32 v2u __attribute__((ext_vector_type(2)));
typedef u32 v4u __attribute__((ext_vector_type(4)));
typedef float f32x4 __attribute__((ext_vector_type(4)));
typedef __bf16 bf16x2 __attribute__((ext_vector_type(2)));
typedef __amdgpu_buffer_rsrc_t rsrc_t;

constexpr int NSEG = 64, DM = 1024, NH = 32, FF = 3584;
constexpr int MB = 1 << 20;
constexpr int WS_CT = 0;
constexpr int WS_M = 262144;
constexpr int WS_O = 524288, WS_V = 1048576;
constexpr int WS_DBG = 4 * MB;
constexpr int WS_XF = 6 * MB;
constexpr int WS_TK = 10 * MB;
constexpr int WS_OF = 66 * MB;
constexpr int WS_HF = 84 * MB;
constexpr int WS_GT = 100 * MB;
constexpr int WS_END = 108 * MB;
constexpr int TK_STR = 64 * 24576 + 8192, OF_STR = 524288 + 8192;
constexpr int CT_PAR = 0, CT_TMO = 64, CT_TICK = 256;
constexpr int CT_LN = 1024;
constexpr int CT_WD = 1536;
constexpr int CT_P1 = 2048;
constexpr int CT_G1 = 4096;
constexpr int CT_AT = 3072;
constexpr int CT_S = 8192;
#define PLACE 2
#define RGX 0
#define WARM 1
#define S64_HEAD 28
constexpr int OOB = 0x7ffffff0;
constexpr int AUX_SC1 = 16;

struct Params {
    const u16 *x, *mem0, *wq, *wk, *wv, *wo, *wg, *wu, *wd;
    u16* out;
    u8* ws;
};
__device__ __forceinline__ u32 pack2(float a, float b) { bf16x2 v = {(__bf16)a, (__bf16)b}; return __builtin_bit_cast(u32, v); }
__device__ __forceinline__ float bflo(u32 d) { return __builtin_bit_cast(float, d << 16); }
__device__ __forceinline__ float bfhi(u32 d) { return __builtin_bit_cast(float, d & 0xFFFF0000u); }
__device__ __forceinline__ float rbf(float a) { return (float)(__bf16)a; }
__device__ __forceinline__ void st128s(v4u v, rsrc_t rs, int voff) { __builtin_amdgcn_raw_buffer_store_b128(v, rs, voff, 0, AUX_SC1); }
__device__ __forceinline__ void st32s(u32 v, rsrc_t rs, int voff) { __builtin_amdgcn_raw_buffer_store_b32(v, rs, voff, 0, AUX_SC1); }
__device__ __forceinline__ u32 ld32s(rsrc_t rs, int voff) { return __builtin_amdgcn_raw_buffer_load_b32(rs, voff, 0, AUX_SC1); }
__device__ __forceinline__ u32 xcc_id() { return (u32)__builtin_amdgcn_s_getreg(20 | (3 << 11)) & 0xfu; }
__device__ __forceinline__ int rfl(int x) { return __builtin_amdgcn_readfirstlane(x); }
__device__ __forceinline__ v4u mksrd(const void* p, u32 bytes) {
    const u64 a = (u64)p;
    return v4u{(u32)rfl((u32)a), (u32)rfl((u32)(a >> 32) & 0xFFFFu), (u32)rfl(bytes), 0x00020000u};
}
#define CFENCE() asm volatile("" ::: "memory")
#define WAIT_V0() asm volatile("s_waitcnt vmcnt(0)" ::: "memory")
#define BAR() do { asm volatile("s_waitcnt lgkmcnt(0)" ::: "memory"); __builtin_amdgcn_s_barrier(); asm volatile("" ::: "memory"); } while (0)
#define RLXA __ATOMIC_RELAXED, __HIP_MEMORY_SCOPE_AGENT
__device__ __forceinline__ int amap(int off, int cls) { return ((off >> 13) << 14) + (off & 8191) + (cls & 8192); }

constexpr int WS_WC = 112 * MB;
constexpr int WS_WD = 120 * MB, WD_STR = 2176;
__device__ __forceinline__ const u16* wmat(const Params& p, int m) { return m == 0 ? p.wq : (m == 1 ? p.wk : (m == 2 ? p.wv : p.wo)); }
__device__ __forceinline__ int slowpos(const void* b) { return (3 - (int)((u64)b >> 7)) & 7; }
__device__ __forceinline__ int slowpos2(const void* b) { return (63 - (int)((u64)b >> 7)) & 63; }
__device__ __forceinline__ const u16* colsrc(const Params& p, int m, int j) {
    const u16* b = wmat(p, m);
    if (WARM && m < 3 && j == slowpos2(b)) return (const u16*)(p.ws + WS_WC) + (5 + m) * 64;
    return (WARM && (j & 7) == slowpos(b)) ? (const u16*)(p.ws + WS_WC) + (j >> 3) * 512 + (m == 3 ? 4 : m) * 64 : b + j * 64;
}
#define ASM_P1_QK \
"s_nop 7\n" \
"s_nop 7\n" \
"s_lshr_b32 s52, s48, 1\n" \
"s_lshl_b32 s52, s52, 13\n" \
"s_and_b32 s53, s48, 1\n" \
"s_lshl_b32 s53, s53, 10\n" \
"s_add_u32 s52, s52, s53\n" \
"s_mov_b32 s54, 0\n" \
"s_add_u32 m0, s52, 0\n" \
"s_nop 0\n" \
"buffer_load_dwordx4 v1, s[40:43], s54 offen nt lds\n" \
"s_add_u32 s54, s54, s44\n" \
"s_add_u32 m0, s52, 2048\n" \
"s_nop 0\n" \
"buffer_load_dwordx4 v1, s[40:43], s54 offen nt lds\n" \
"s_add_u32 s54, s54, s44\n" \
"s_add_u32 m0, s52, 4096\n" \
"s_nop 0\n" \
"buffer_load_dwordx4 v1, s[40:43], s54 offen nt lds\n" \
"s_add_u32 s54, s54, s44\n" \
"s_add_u32 m0, s52, 6144\n" \
"s_nop 0\n" \
"buffer_load_dwordx4 v1, s[40:43], s54 offen nt lds\n" \
"s_add_u32 s54, s54, s44\n" \
"s_add_u32 m0, s52, 16384\n" \
"s_nop 0\n" \
"buffer_load_dwordx4 v1, s[40:43], s54 offen nt lds\n" \
"s_add_u32 s54, s54, s44\n" \
"s_add_u32 m0, s52, 18432\n" \
"s_nop 0\n" \
"buffer_load_dwordx4 v1, s[40:43], s54 offen nt lds\n" \
"s_add_u32 s54, s54, s44\n" \
"s_add_u32 m0, s52, 20480\n" \
"s_nop 0\n" \
"buffer_load_dwordx4 v1, s[40:43], s54 offen nt lds\n" \
"s_add_u32 s54, s54, s44\n" \
"s_add_u32 m0, s52, 22528\n" \
"s_nop 0\n" \
"buffer_load_dwordx4 v1, s[40:43], s54 offen nt lds\n" \
"s_add_u32 s54, s54, s44\n" \
"s_add_u32 m0, s52, 32768\n" \
"s_nop 0\n" \
"buffer_load_dwordx4 v1, s[40:43], s54 offen nt lds\n" \
"s_add_u32 s54, s54, s44\n" \
"s_add_u32 m0, s52, 34816\n" \
"s_nop 0\n" \
"buffer_load_dwordx4 v1, s[40:43], s54 offen nt lds\n" \
"s_add_u32 s54, s54, s44\n" \
"s_add_u32 m0, s52, 36864\n" \
"s_nop 0\n" \
"buffer_load_dwordx4 v1, s[40:43], s54 offen nt lds\n" \
"s_add_u32 s54, s54, s44\n" \
"s_add_u32 m0, s52, 38912\n" \
"s_nop 0\n" \
"buffer_load_dwordx4 v1, s[40:43], s54 offen nt lds\n" \
"s_add_u32 s54, s54, s44\n" \
"s_add_u32 m0, s52, 49152\n" \
"s_nop 0\n" \
"buffer_load_dwordx4 v1, s[40:43], s54 offen nt lds\n" \
"s_add_u32 s54, s54, s44\n" \
"s_add_u32 m0, s52, 51200\n" \
"s_nop 0\n" \
"buffer_load_dwordx4 v1, s[40:43], s54 offen nt lds\n" \
"s_add_u32 s54, s54, s44\n" \
"s_add_u32 m0, s52, 53248\n" \
"s_nop 0\n" \
"buffer_load_dwordx4 v1, s[40:43], s54 offen nt lds\n" \
"s_add_u32 s54, s54, s44\n" \
"s_add_u32 m0, s52, 55296\n" \
"s_nop 0\n" \
"buffer_load_dwordx4 v1, s[40:43], s54 offen nt lds\n" \
"s_add_u32 s54, s54, s44\n" \
"s_add_u32 m0, s52, 65536\n" \
"s_nop 0\n" \
"buffer_load_dwordx4 v1, s[40:43], s54 offen nt lds\n" \
"s_add_u32 s54, s54, s44\n" \
"s_add_u32 m0, s52, 67584\n" \
"s_nop 0\n" \
"buffer_load_dwordx4 v1, s[40:43], s54 offen nt lds\n" \
"s_add_u32 s54, s54, s44\n" \
"s_add_u32 m0, s52, 69632\n" \
"s_nop 0\n" \
"buffer_load_dwordx4 v1, s[40:43], s54 offen nt lds\n" \
"s_add_u32 s54, s54, s44\n" \
"s_add_u32 m0, s52, 71680\n" \
"s_nop 0\n" \
"buffer_load_dwordx4 v1, s[40:43], s54 offen nt lds\n" \
"s_add_u32 s54, s54, s44\n" \
"s_add_u32 m0, s52, 81920\n" \
"s_nop 0\n" \
"buffer_load_dwordx4 v1, s[40:43], s54 offen nt lds\n" \
"s_add_u32 s54, s54, s44\n" \
"s_add_u32 m0, s52, 83968\n" \
"s_nop 0\n" \
"buffer_load_dwordx4 v1, s[40:43], s54 offen nt lds\n" \
"s_add_u32 s54, s54, s44\n" \
"s_add_u32 m0, s52, 86016\n" \
"s_nop 0\n" \
"buffer_load_dwordx4 v1, s[40:43], s54 offen nt lds\n" \
"s_add_u32 s54, s54, s44\n" \
"s_add_u32 m0, s52, 88064\n" \
"s_nop 0\n" \
"buffer_load_dwordx4 v1, s[40:43], s54 offen nt lds\n" \
"s_add_u32 s54, s54, s44\n" \
"s_add_u32 m0, s52, 98304\n" \
"s_nop 0\n" \
"buffer_load_dwordx4 v1, s[40:43], s54 offen nt lds\n" \
"s_add_u32 s54, s54, s44\n" \
"s_add_u32 m0, s52, 100352\n" \
"s_nop 0\n" \
"buffer_load_dwordx4 v1, s[40:43], s54 offen nt lds\n" \
"s_add_u32 s54, s54, s44\n" \
"s_add_u32 m0, s52, 102400\n" \
"s_nop 0\n" \
"buffer_load_dwordx4 v1, s[40:43], s54 offen nt lds\n" \
"s_add_u32 s54, s54, s44\n" \
"s_add_u32 m0, s52, 104448\n" \
"s_nop 0\n" \
"buffer_load_dwordx4 v1, s[40:43], s54 offen nt lds\n" \
"s_add_u32 s54, s54, s44\n" \
"s_add_u32 m0, s52, 114688\n" \
"s_nop 0\n" \
"buffer_load_dwordx4 v1, s[40:43], s54 offen nt lds\n" \
"s_add_u32 s54, s54, s44\n" \
"s_add_u32 m0, s52, 116736\n" \
"s_nop 0\n" \
"buffer_load_dwordx4 v1, s[40:43], s54 offen nt lds\n" \
"s_add_u32 s54, s54, s44\n" \
"s_add_u32 m0, s52, 118784\n" \
"s_nop 0\n" \
"buffer_load_dwordx4 v1, s[40:43], s54 offen nt lds\n" \
"s_add_u32 s54, s54, s44\n" \
"s_add_u32 m0, s52, 120832\n" \
"s_nop 0\n" \
"buffer_load_dwordx4 v1, s[40:43], s54 offen nt lds\n" \
"s_add_u32 s54, s54, s44\n" \
"v_add_u32 v8, 0x10000, v2\n" \
"v_add_u32 v9, 0x10000, v3\n" \
"s_waitcnt vmcnt(28)\n" \
"s_barrier\n" \
"ds_read_b64_tr_b16 a[0:1], v2 offset:0\n" \
"ds_read_b64_tr_b16 a[2:3], v2 offset:512\n" \
"ds_read_b64_tr_b16 a[128:129], v3 offset:0\n" \
"ds_read_b64_tr_b16 a[130:131], v3 offset:512\n" \
"ds_read_b64_tr_b16 a[4:5], v2 offset:4096\n" \
"ds_read_b64_tr_b16 a[6:7], v2 offset:4608\n" \
"ds_read_b64_tr_b16 a[132:133], v3 offset:4096\n" \
"ds_read_b64_tr_b16 a[134:135], v3 offset:4608\n" \
"s_waitcnt lgkmcnt(0)\n" \
"s_barrier\n" \
"s_add_u32 m0, s52, 0\n" \
"s_nop 0\n" \
"buffer_load_dwordx4 v1, s[40:43], s54 offen nt lds\n" \
"s_add_u32 s54, s54, s44\n" \
"s_add_u32 m0, s52, 2048\n" \
"s_nop 0\n" \
"buffer_load_dwordx4 v1, s[40:43], s54 offen nt lds\n" \
"s_add_u32 s54, s54, s44\n" \
"s_add_u32 m0, s52, 4096\n" \
"s_nop 0\n" \
"buffer_load_dwordx4 v1, s[40:43], s54 offen nt lds\n" \
"s_add_u32 s54, s54, s44\n" \
"s_add_u32 m0, s52, 6144\n" \
"s_nop 0\n" \
"buffer_load_dwordx4 v1, s[40:43], s54 offen nt lds\n" \
"s_add_u32 s54, s54, s44\n" \
"s_waitcnt vmcnt(28)\n" \
"s_barrier\n" \
"ds_read_b64_tr_b16 a[8:9], v2 offset:16384\n" \
"ds_read_b64_tr_b16 a[10:11], v2 offset:16896\n" \
"ds_read_b64_tr_b16 a[136:137], v3 offset:16384\n" \
"ds_read_b64_tr_b16 a[138:139], v3 offset:16896\n" \
"ds_read_b64_tr_b16 a[12:13], v2 offset:20480\n" \
"ds_read_b64_tr_b16 a[14:15], v2 offset:20992\n" \
"ds_read_b64_tr_b16 a[140:141], v3 offset:20480\n" \
"ds_read_b64_tr_b16 a[142:143], v3 offset:20992\n" \
"s_waitcnt lgkmcnt(0)\n" \
"s_barrier\n" \
"s_add_u32 m0, s52, 16384\n" \
"s_nop 0\n" \
"buffer_load_dwordx4 v1, s[40:43], s54 offen nt lds\n" \
"s_add_u32 s54, s54, s44\n" \
"s_add_u32 m0, s52, 18432\n" \
"s_nop 0\n" \
"buffer_load_dwordx4 v1, s[40:43], s54 offen nt lds\n" \
"s_add_u32 s54, s54, s44\n" \
"s_add_u32 m0, s52, 20480\n" \
"s_nop 0\n" \
"buffer_load_dwordx4 v1, s[40:43], s54 offen nt lds\n" \
"s_add_u32 s54, s54, s44\n" \
"s_add_u32 m0, s52, 22528\n" \
"s_nop 0\n" \
"buffer_load_dwordx4 v1, s[40:43], s54 offen nt lds\n" \
"s_add_u32 s54, s54, s44\n" \
"s_waitcnt vmcnt(28)\n" \
"s_barrier\n" \
"ds_read_b64_tr_b16 a[16:17], v2 offset:32768\n" \
"ds_read_b64_tr_b16 a[18:19], v2 offset:33280\n" \
"ds_read_b64_tr_b16 a[144:145], v3 offset:32768\n" \
"ds_read_b64_tr_b16 a[146:147], v3 offset:33280\n" \
"ds_read_b64_tr_b16 a[20:21], v2 offset:36864\n" \
"ds_read_b64_tr_b16 a[22:23], v2 offset:37376\n" \
"ds_read_b64_tr_b16 a[148:149], v3 offset:36864\n" \
"ds_read_b64_tr_b16 a[150:151], v3 offset:37376\n" \
"s_waitcnt lgkmcnt(0)\n" \
"s_barrier\n" \
"s_add_u32 m0, s52, 32768\n" \
"s_nop 0\n" \
"buffer_load_dwordx4 v1, s[40:43], s54 offen nt lds\n" \
"s_add_u32 s54, s54, s44\n" \
"s_add_u32 m0, s52, 34816\n" \
"s_nop 0\n" \
"buffer_load_dwordx4 v1, s[40:43], s54 offen nt lds\n" \
"s_add_u32 s54, s54, s44\n" \
"s_add_u32 m0, s52, 36864\n" \
"s_nop 0\n" \
"buffer_load_dwordx4 v1, s[40:43], s54 offen nt lds\n" \
"s_add_u32 s54, s54, s44\n" \
"s_add_u32 m0, s52, 38912\n" \
"s_nop 0\n" \
"buffer_load_dwordx4 v1, s[40:43], s54 offen nt lds\n" \
"s_add_u32 s54, s54, s44\n" \
"s_waitcnt vmcnt(28)\n" \
"s_barrier\n" \
"ds_read_b64_tr_b16 a[24:25], v2 offset:49152\n" \
"ds_read_b64_tr_b16 a[26:27], v2 offset:49664\n" \
"ds_read_b64_tr_b16 a[152:153], v3 offset:49152\n" \
"ds_read_b64_tr_b16 a[154:155], v3 offset:49664\n" \
"ds_read_b64_tr_b16 a[28:29], v2 offset:53248\n" \
"ds_read_b64_tr_b16 a[30:31], v2 offset:53760\n" \
"ds_read_b64_tr_b16 a[156:157], v3 offset:53248\n" \
"ds_read_b64_tr_b16 a[158:159], v3 offset:53760\n" \
"s_waitcnt lgkmcnt(0)\n" \
"s_barrier\n" \
"s_add_u32 m0, s52, 49152\n" \
"s_nop 0\n" \
"buffer_load_dwordx4 v1, s[40:43], s54 offen nt lds\n" \
"s_add_u32 s54, s54, s44\n" \
"s_add_u32 m0, s52, 51200\n" \
"s_nop 0\n" \
"buffer_load_dwordx4 v1, s[40:43], s54 offen nt lds\n" \
"s_add_u32 s54, s54, s44\n" \
"s_add_u32 m0, s52, 53248\n" \
"s_nop 0\n" \
"buffer_load_dwordx4 v1, s[40:43], s54 offen nt lds\n" \
"s_add_u32 s54, s54, s44\n" \
"s_add_u32 m0, s52, 55296\n" \
"s_nop 0\n" \
"buffer_load_dwordx4 v1, s[40:43], s54 offen nt lds\n" \
"s_add_u32 s54, s54, s44\n" \
"s_waitcnt vmcnt(28)\n" \
"s_barrier\n" \
"ds_read_b64_tr_b16 a[32:33], v8 offset:0\n" \
"ds_read_b64_tr_b16 a[34:35], v8 offset:512\n" \
"ds_read_b64_tr_b16 a[160:161], v9 offset:0\n" \
"ds_read_b64_tr_b16 a[162:163], v9 offset:512\n" \
"ds_read_b64_tr_b16 a[36:37], v8 offset:4096\n" \
"ds_read_b64_tr_b16 a[38:39], v8 offset:4608\n" \
"ds_read_b64_tr_b16 a[164:165], v9 offset:4096\n" \
"ds_read_b64_tr_b16 a[166:167], v9 offset:4608\n" \
"s_waitcnt lgkmcnt(0)\n" \
"s_barrier\n" \
"s_add_u32 m0, s52, 65536\n" \
"s_nop 0\n" \
"buffer_load_dwordx4 v1, s[40:43], s54 offen nt lds\n" \
"s_add_u32 s54, s54, s44\n" \
"s_add_u32 m0, s52, 67584\n" \
"s_nop 0\n" \
"buffer_load_dwordx4 v1, s[40:43], s54 offen nt lds\n" \
"s_add_u32 s54, s54, s44\n" \
"s_add_u32 m0, s52, 69632\n" \
"s_nop 0\n" \
"buffer_load_dwordx4 v1, s[40:43], s54 offen nt lds\n" \
"s_add_u32 s54, s54, s44\n" \
"s_add_u32 m0, s52, 71680\n" \
"s_nop 0\n" \
"buffer_load_dwordx4 v1, s[40:43], s54 offen nt lds\n" \
"s_add_u32 s54, s54, s44\n" \
"s_waitcnt vmcnt(28)\n" \
"s_barrier\n" \
"ds_read_b64_tr_b16 a[40:41], v8 offset:16384\n" \
"ds_read_b64_tr_b16 a[42:43], v8 offset:16896\n" \
"ds_read_b64_tr_b16 a[168:169], v9 offset:16384\n" \
"ds_read_b64_tr_b16 a[170:171], v9 offset:16896\n" \
"ds_read_b64_tr_b16 a[44:45], v8 offset:20480\n" \
"ds_read_b64_tr_b16 a[46:47], v8 offset:20992\n" \
"ds_read_b64_tr_b16 a[172:173], v9 offset:20480\n" \
"ds_read_b64_tr_b16 a[174:175], v9 offset:20992\n" \
"s_waitcnt lgkmcnt(0)\n" \
"s_barrier\n" \
"s_add_u32 m0, s52, 81920\n" \
"s_nop 0\n" \
"buffer_load_dwordx4 v1, s[40:43], s54 offen nt lds\n" \
"s_add_u32 s54, s54, s44\n" \
"s_add_u32 m0, s52, 83968\n" \
"s_nop 0\n" \
"buffer_load_dwordx4 v1, s[40:43], s54 offen nt lds\n" \
"s_add_u32 s54, s54, s44\n" \
"s_add_u32 m0, s52, 86016\n" \
"s_nop 0\n" \
"buffer_load_dwordx4 v1, s[40:43], s54 offen nt lds\n" \
"s_add_u32 s54, s54, s44\n" \
"s_add_u32 m0, s52, 88064\n" \
"s_nop 0\n" \
"buffer_load_dwordx4 v1, s[40:43], s54 offen nt lds\n" \
"s_add_u32 s54, s54, s44\n" \
"s_waitcnt vmcnt(28)\n" \
"s_barrier\n" \
"ds_read_b64_tr_b16 a[48:49], v8 offset:32768\n" \
"ds_read_b64_tr_b16 a[50:51], v8 offset:33280\n" \
"ds_read_b64_tr_b16 a[176:177], v9 offset:32768\n" \
"ds_read_b64_tr_b16 a[178:179], v9 offset:33280\n" \
"ds_read_b64_tr_b16 a[52:53], v8 offset:36864\n" \
"ds_read_b64_tr_b16 a[54:55], v8 offset:37376\n" \
"ds_read_b64_tr_b16 a[180:181], v9 offset:36864\n" \
"ds_read_b64_tr_b16 a[182:183], v9 offset:37376\n" \
"s_waitcnt lgkmcnt(0)\n" \
"s_barrier\n" \
"s_add_u32 m0, s52, 98304\n" \
"s_nop 0\n" \
"buffer_load_dwordx4 v1, s[40:43], s54 offen nt lds\n" \
"s_add_u32 s54, s54, s44\n" \
"s_add_u32 m0, s52, 100352\n" \
"s_nop 0\n" \
"buffer_load_dwordx4 v1, s[40:43], s54 offen nt lds\n" \
"s_add_u32 s54, s54, s44\n" \
"s_add_u32 m0, s52, 102400\n" \
"s_nop 0\n" \
"buffer_load_dwordx4 v1, s[40:43], s54 offen nt lds\n" \
"s_add_u32 s54, s54, s44\n" \
"s_add_u32 m0, s52, 104448\n" \
"s_nop 0\n" \
"buffer_load_dwordx4 v1, s[40:43], s54 offen nt lds\n" \
"s_add_u32 s54, s54, s44\n" \
"s_waitcnt vmcnt(28)\n" \
"s_barrier\n" \
"ds_read_b64_tr_b16 a[56:57], v8 offset:49152\n" \
"ds_read_b64_tr_b16 a[58:59], v8 offset:49664\n" \
"ds_read_b64_tr_b16 a[184:185], v9 offset:49152\n" \
"ds_read_b64_tr_b16 a[186:187], v9 offset:49664\n" \
"ds_read_b64_tr_b16 a[60:61], v8 offset:53248\n" \
"ds_read_b64_tr_b16 a[62:63], v8 offset:53760\n" \
"ds_read_b64_tr_b16 a[188:189], v9 offset:53248\n" \
"ds_read_b64_tr_b16 a[190:191], v9 offset:53760\n" \
"s_waitcnt lgkmcnt(0)\n" \
"s_barrier\n" \
"s_add_u32 m0, s52, 114688\n" \
"s_nop 0\n" \
"buffer_load_dwordx4 v1, s[40:43], s54 offen nt lds\n" \
"s_add_u32 s54, s54, s44\n" \
"s_add_u32 m0, s52, 116736\n" \
"s_nop 0\n" \
"buffer_load_dwordx4 v1, s[40:43], s54 offen nt lds\n" \
"s_add_u32 s54, s54, s44\n" \
"s_add_u32 m0, s52, 118784\n" \
"s_nop 0\n" \
"buffer_load_dwordx4 v1, s[40:43], s54 offen nt lds\n" \
"s_add_u32 s54, s54, s44\n" \
"s_add_u32 m0, s52, 120832\n" \
"s_nop 0\n" \
"buffer_load_dwordx4 v1, s[40:43], s54 offen nt lds\n" \
"s_add_u32 s54, s54, s44\n" \
"s_waitcnt vmcnt(28)\n" \
"s_barrier\n" \
"ds_read_b64_tr_b16 a[64:65], v2 offset:0\n" \
"ds_read_b64_tr_b16 a[66:67], v2 offset:512\n" \
"ds_read_b64_tr_b16 a[192:193], v3 offset:0\n" \
"ds_read_b64_tr_b16 a[194:195], v3 offset:512\n" \
"ds_read_b64_tr_b16 a[68:69], v2 offset:4096\n" \
"ds_read_b64_tr_b16 a[70:71], v2 offset:4608\n" \
"ds_read_b64_tr_b16 a[196:197], v3 offset:4096\n" \
"ds_read_b64_tr_b16 a[198:199], v3 offset:4608\n" \
"s_waitcnt vmcnt(24)\n" \
"s_barrier\n" \
"ds_read_b64_tr_b16 a[72:73], v2 offset:16384\n" \
"ds_read_b64_tr_b16 a[74:75], v2 offset:16896\n" \
"ds_read_b64_tr_b16 a[200:201], v3 offset:16384\n" \
"ds_read_b64_tr_b16 a[202:203], v3 offset:16896\n" \
"ds_read_b64_tr_b16 a[76:77], v2 offset:20480\n" \
"ds_read_b64_tr_b16 a[78:79], v2 offset:20992\n" \
"ds_read_b64_tr_b16 a[204:205], v3 offset:20480\n" \
"ds_read_b64_tr_b16 a[206:207], v3 offset:20992\n" \
"s_waitcnt vmcnt(20)\n" \
"s_barrier\n" \
"ds_read_b64_tr_b16 a[80:81], v2 offset:32768\n" \
"ds_read_b64_tr_b16 a[82:83], v2 offset:33280\n" \
"ds_read_b64_tr_b16 a[208:209], v3 offset:32768\n" \
"ds_read_b64_tr_b16 a[210:211], v3 offset:33280\n" \
"ds_read_b64_tr_b16 a[84:85], v2 offset:36864\n" \
"ds_read_b64_tr_b16 a[86:87], v2 offset:37376\n" \
"ds_read_b64_tr_b16 a[212:213], v3 offset:36864\n" \
"ds_read_b64_tr_b16 a[214:215], v3 offset:37376\n" \
"s_waitcnt vmcnt(16)\n" \
"s_barrier\n" \
"ds_read_b64_tr_b16 a[88:89], v2 offset:49152\n" \
"ds_read_b64_tr_b16 a[90:91], v2 offset:49664\n" \
"ds_read_b64_tr_b16 a[216:217], v3 offset:49152\n" \
"ds_read_b64_tr_b16 a[218:219], v3 offset:49664\n" \
"ds_read_b64_tr_b16 a[92:93], v2 offset:53248\n" \
"ds_read_b64_tr_b16 a[94:95], v2 offset:53760\n" \
"ds_read_b64_tr_b16 a[220:221], v3 offset:53248\n" \
"ds_read_b64_tr_b16 a[222:223], v3 offset:53760\n" \
"s_waitcnt vmcnt(12)\n" \
"s_barrier\n" \
"ds_read_b64_tr_b16 a[96:97], v8 offset:0\n" \
"ds_read_b64_tr_b16 a[98:99], v8 offset:512\n" \
"ds_read_b64_tr_b16 a[224:225], v9 offset:0\n" \
"ds_read_b64_tr_b16 a[226:227], v9 offset:512\n" \
"ds_read_b64_tr_b16 a[100:101], v8 offset:4096\n" \
"ds_read_b64_tr_b16 a[102:103], v8 offset:4608\n" \
"ds_read_b64_tr_b16 a[228:229], v9 offset:4096\n" \
"ds_read_b64_tr_b16 a[230:231], v9 offset:4608\n" \
"s_waitcnt vmcnt(8)\n" \
"s_barrier\n" \
"ds_read_b64_tr_b16 a[104:105], v8 offset:16384\n" \
"ds_read_b64_tr_b16 a[106:107], v8 offset:16896\n" \
"ds_read_b64_tr_b16 a[232:233], v9 offset:16384\n" \
"ds_read_b64_tr_b16 a[234:235], v9 offset:16896\n" \
"ds_read_b64_tr_b16 a[108:109], v8 offset:20480\n" \
"ds_read_b64_tr_b16 a[110:111], v8 offset:20992\n" \
"ds_read_b64_tr_b16 a[236:237], v9 offset:20480\n" \
"ds_read_b64_tr_b16 a[238:239], v9 offset:20992\n" \
"s_waitcnt vmcnt(4)\n" \
"s_barrier\n" \
"ds_read_b64_tr_b16 a[112:113], v8 offset:32768\n" \
"ds_read_b64_tr_b16 a[114:115], v8 offset:33280\n" \
"ds_read_b64_tr_b16 a[240:241], v9 offset:32768\n" \
"ds_read_b64_tr_b16 a[242:243], v9 offset:33280\n" \
"ds_read_b64_tr_b16 a[116:117], v8 offset:36864\n" \
"ds_read_b64_tr_b16 a[118:119], v8 offset:37376\n" \
"ds_read_b64_tr_b16 a[244:245], v9 offset:36864\n" \
"ds_read_b64_tr_b16 a[246:247], v9 offset:37376\n" \
"s_waitcnt vmcnt(0)\n" \
"s_barrier\n" \
"ds_read_b64_tr_b16 a[120:121], v8 offset:49152\n" \
"ds_read_b64_tr_b16 a[122:123], v8 offset:49664\n" \
"ds_read_b64_tr_b16 a[248:249], v9 offset:49152\n" \
"ds_read_b64_tr_b16 a[250:251], v9 offset:49664\n" \
"ds_read_b64_tr_b16 a[124:125], v8 offset:53248\n" \
"ds_read_b64_tr_b16 a[126:127], v8 offset:53760\n" \
"ds_read_b64_tr_b16 a[252:253], v9 offset:53248\n" \
"ds_read_b64_tr_b16 a[254:255], v9 offset:53760\n" \
"s_waitcnt lgkmcnt(0)\n" \
"s_barrier\n" \
"v_mov_b32 v14, 4096\n" \
"s_mov_b32 s59, s46\n" \
"s_add_u32 s63, s59, 0\n" \
"s_min_u32 s63, s63, 63\n" \
"s_lshr_b32 s63, s63, 3\n" \
"s_lshl_b32 s63, s63, 7\n" \
"s_branch .Lp1qk_l0_%=\n" \
".Lp1qk_l0k_%=:\n" \
"s_add_u32 s63, s59, 1\n" \
"s_min_u32 s63, s63, 63\n" \
"s_lshr_b32 s63, s63, 3\n" \
"s_lshl_b32 s63, s63, 7\n" \
"s_branch .Lp1qk_l1_%=\n" \
".Lp1qk_l1k_%=:\n" \
"s_lshl_b32 s55, s48, 13\n" \
"s_lshl_b32 s56, s46, 16\n" \
"s_lshl_b32 s60, s47, 16\n" \
"s_sub_u32 s60, s60, 0x10000\n" \
"s_mul_i32 s57, s46, s49\n" \
"s_sub_u32 s57, s57, s49\n" \
"s_add_u32 s58, s57, s50\n" \
"v_mov_b32 v7, v5\n" \
"v_mov_b32 v5, 0x7ffffff0\n" \
"v_mov_b32 v10, v0\n" \
"v_mov_b32 v11, v6\n" \
"v_mov_b32 v6, 0x7ffffff0\n" \
"s_mov_b32 s59, s46\n" \
"s_min_u32 s61, s56, s60\n" \
"s_add_u32 m0, s55, 0\n" \
"s_nop 0\n" \
"buffer_load_dwordx4 v4, s[36:39], s61 offen offset:0 sc1 lds\n" \
"buffer_load_dwordx4 v4, s[36:39], s61 offen offset:1024 sc1 lds\n" \
"buffer_load_dwordx4 v4, s[36:39], s61 offen offset:2048 sc1 lds\n" \
"buffer_load_dwordx4 v4, s[36:39], s61 offen offset:3072 sc1 lds\n" \
"s_add_u32 m0, s55, 4096\n" \
"s_add_u32 s61, s61, 4096\n" \
"buffer_load_dwordx4 v4, s[36:39], s61 offen offset:0 sc1 lds\n" \
"buffer_load_dwordx4 v4, s[36:39], s61 offen offset:1024 sc1 lds\n" \
"buffer_load_dwordx4 v4, s[36:39], s61 offen offset:2048 sc1 lds\n" \
"buffer_load_dwordx4 v4, s[36:39], s61 offen offset:3072 sc1 lds\n" \
"s_min_u32 s61, s56, s60\n" \
"s_add_u32 s61, s61, 16384\n" \
"s_add_u32 s56, s56, 0x10000\n" \
"s_add_u32 m0, s55, 32768\n" \
"s_nop 0\n" \
"buffer_load_dwordx4 v4, s[36:39], s61 offen offset:0 sc1 lds\n" \
"buffer_load_dwordx4 v4, s[36:39], s61 offen offset:1024 sc1 lds\n" \
"buffer_load_dwordx4 v4, s[36:39], s61 offen offset:2048 sc1 lds\n" \
"buffer_load_dwordx4 v4, s[36:39], s61 offen offset:3072 sc1 lds\n" \
"s_add_u32 m0, s55, 36864\n" \
"s_add_u32 s61, s61, 4096\n" \
"buffer_load_dwordx4 v4, s[36:39], s61 offen offset:0 sc1 lds\n" \
"buffer_load_dwordx4 v4, s[36:39], s61 offen offset:1024 sc1 lds\n" \
"buffer_load_dwordx4 v4, s[36:39], s61 offen offset:2048 sc1 lds\n" \
"buffer_load_dwordx4 v4, s[36:39], s61 offen offset:3072 sc1 lds\n" \
"s_mov_b32 s70, 0\n" \
"s_mov_b32 s72, 0\n" \
"s_waitcnt vmcnt(0) lgkmcnt(0)\n" \
"s_barrier\n" \
"v_add_u32 v12, 0x10000, v10\n" \
"ds_read_b128 v[16:19], v10 offset:0\n" \
"ds_read_b128 v[20:23], v10 offset:16384\n" \
"ds_read_b128 v[24:27], v10 offset:1024\n" \
"ds_read_b128 v[28:31], v10 offset:17408\n" \
"ds_read_b128 v[32:35], v10 offset:2048\n" \
"ds_read_b128 v[36:39], v10 offset:18432\n" \
"s_waitcnt lgkmcnt(4)\n" \
"s_nop 7\n" \
"s_nop 7\n" \
".p2align 6\n" \
".Lp1qk_loop_%=:\n" \
"v_mfma_f32_16x16x32_bf16 v[48:51], a[0:3], v[16:19], 0\n" \
"s_min_u32 s61, s56, s60\n" \
"s_add_u32 m0, s55, 65536\n" \
"s_nop 0\n" \
"buffer_load_dwordx4 v4, s[36:39], s61 offen offset:0 sc1 lds\n" \
"v_mov_b32_e64 v13, s59\n" \
"v_mfma_f32_16x16x32_bf16 v[52:55], a[0:3], v[20:23], 0\n" \
"ds_read_b128 v[40:43], v10 offset:3072\n" \
"s_nop 0\n" \
"v_mfma_f32_16x16x32_bf16 v[56:59], a[128:131], v[16:19], 0\n" \
"ds_read_b128 v[44:47], v10 offset:19456\n" \
"s_nop 0\n" \
"v_mfma_f32_16x16x32_bf16 v[60:63], a[128:131], v[20:23], 0\n" \
"s_waitcnt lgkmcnt(4)\n" \
"v_mul_f32 v64, s51, v64\n" \
"v_mul_f32_e64 v65, s51, v65\n" \
"v_mfma_f32_16x16x32_bf16 v[48:51], a[4:7], v[24:27], v[48:51]\n" \
"buffer_load_dwordx4 v4, s[36:39], s61 offen offset:1024 sc1 lds\n" \
"v_mfma_f32_16x16x32_bf16 v[52:55], a[4:7], v[28:31], v[52:55]\n" \
"ds_read_b128 v[16:19], v10 offset:4096\n" \
"v_mul_f32_e64 v66, s51, v66\n" \
"v_mfma_f32_16x16x32_bf16 v[56:59], a[132:135], v[24:27], v[56:59]\n" \
"ds_read_b128 v[20:23], v10 offset:20480\n" \
"v_mul_f32_e64 v67, s51, v67\n" \
"v_mfma_f32_16x16x32_bf16 v[60:63], a[132:135], v[28:31], v[60:63]\n" \
"s_waitcnt lgkmcnt(4)\n" \
"v_mul_f32 v72, s51, v72\n" \
"v_mul_f32_e64 v73, s51, v73\n" \
"v_mfma_f32_16x16x32_bf16 v[48:51], a[8:11], v[32:35], v[48:51]\n" \
"s_add_u32 s63, s59, 2\n" \
"s_min_u32 s63, s63, 63\n" \
"v_mfma_f32_16x16x32_bf16 v[52:55], a[8:11], v[36:39], v[52:55]\n" \
"ds_read_b128 v[24:27], v10 offset:5120\n" \
"s_lshr_b32 s63, s63, 3\n" \
"s_lshl_b32 s63, s63, 7\n" \
"v_mul_f32_e64 v74, s51, v74\n" \
"v_mfma_f32_16x16x32_bf16 v[56:59], a[136:139], v[32:35], v[56:59]\n" \
"ds_read_b128 v[28:31], v10 offset:21504\n" \
"v_mul_f32_e64 v75, s51, v75\n" \
"v_mfma_f32_16x16x32_bf16 v[60:63], a[136:139], v[36:39], v[60:63]\n" \
"s_waitcnt lgkmcnt(4)\n" \
"v_cvt_pk_bf16_f32 v80, v64, v65\n" \
"v_cvt_pk_bf16_f32 v81, v66, v67\n" \
"v_mfma_f32_16x16x32_bf16 v[48:51], a[12:15], v[40:43], v[48:51]\n" \
"buffer_load_dwordx4 v4, s[36:39], s61 offen offset:2048 sc1 lds\n" \
"v_mfma_f32_16x16x32_bf16 v[52:55], a[12:15], v[44:47], v[52:55]\n" \
"ds_read_b128 v[32:35], v10 offset:6144\n" \
"v_cvt_pk_bf16_f32 v82, v72, v73\n" \
"v_mfma_f32_16x16x32_bf16 v[56:59], a[140:143], v[40:43], v[56:59]\n" \
"ds_read_b128 v[36:39], v10 offset:22528\n" \
"v_cvt_pk_bf16_f32 v83, v74, v75\n" \
"v_mfma_f32_16x16x32_bf16 v[60:63], a[140:143], v[44:47], v[60:63]\n" \
"s_waitcnt lgkmcnt(4)\n" \
"buffer_store_dwordx4 v[80:83], v5, s[36:39], s57 offen sc1\n" \
"v_mul_f32_e64 v68, s51, v68\n" \
"v_mfma_f32_16x16x32_bf16 v[48:51], a[16:19], v[16:19], v[48:51]\n" \
"buffer_load_dwordx4 v4, s[36:39], s61 offen offset:3072 sc1 lds\n" \
"v_mfma_f32_16x16x32_bf16 v[52:55], a[16:19], v[20:23], v[52:55]\n" \
"ds_read_b128 v[40:43], v10 offset:7168\n" \
"v_mul_f32_e64 v69, s51, v69\n" \
"v_mfma_f32_16x16x32_bf16 v[56:59], a[144:147], v[16:19], v[56:59]\n" \
"ds_read_b128 v[44:47], v10 offset:23552\n" \
"v_mul_f32_e64 v70, s51, v70\n" \
"v_mfma_f32_16x16x32_bf16 v[60:63], a[144:147], v[20:23], v[60:63]\n" \
"s_waitcnt lgkmcnt(4)\n" \
"v_mul_f32 v71, s51, v71\n" \
"v_mul_f32_e64 v76, s51, v76\n" \
"v_mfma_f32_16x16x32_bf16 v[48:51], a[20:23], v[24:27], v[48:51]\n" \
"buffer_load_dword v15, v14, s[36:39], s63 offen sc1\n" \
"v_mfma_f32_16x16x32_bf16 v[52:55], a[20:23], v[28:31], v[52:55]\n" \
"ds_read_b128 v[16:19], v10 offset:8192\n" \
"v_mul_f32_e64 v77, s51, v77\n" \
"v_mfma_f32_16x16x32_bf16 v[56:59], a[148:151], v[24:27], v[56:59]\n" \
"ds_read_b128 v[20:23], v10 offset:24576\n" \
"v_mul_f32_e64 v78, s51, v78\n" \
"v_mfma_f32_16x16x32_bf16 v[60:63], a[148:151], v[28:31], v[60:63]\n" \
"s_waitcnt lgkmcnt(4)\n" \
"v_mul_f32 v79, s51, v79\n" \
"v_cvt_pk_bf16_f32 v84, v68, v69\n" \
"v_mfma_f32_16x16x32_bf16 v[48:51], a[24:27], v[32:35], v[48:51]\n" \
"s_add_u32 m0, s55, 69632\n" \
"s_add_u32 s61, s61, 4096\n" \
"buffer_load_dwordx4 v4, s[36:39], s61 offen offset:0 sc1 lds\n" \
"v_mfma_f32_16x16x32_bf16 v[52:55], a[24:27], v[36:39], v[52:55]\n" \
"ds_read_b128 v[24:27], v10 offset:9216\n" \
"v_cvt_pk_bf16_f32 v85, v70, v71\n" \
"v_mfma_f32_16x16x32_bf16 v[56:59], a[152:155], v[32:35], v[56:59]\n" \
"ds_read_b128 v[28:31], v10 offset:25600\n" \
"v_cvt_pk_bf16_f32 v86, v76, v77\n" \
"v_mfma_f32_16x16x32_bf16 v[60:63], a[152:155], v[36:39], v[60:63]\n" \
"s_waitcnt lgkmcnt(4)\n" \
"v_cvt_pk_bf16_f32 v87, v78, v79\n" \
"buffer_store_dwordx4 v[84:87], v5, s[36:39], s58 offen sc1\n" \
"v_mfma_f32_16x16x32_bf16 v[48:51], a[28:31], v[40:43], v[48:51]\n" \
"buffer_load_dwordx4 v4, s[36:39], s61 offen offset:1024 sc1 lds\n" \
"v_mfma_f32_16x16x32_bf16 v[52:55], a[28:31], v[44:47], v[52:55]\n" \
"ds_read_b128 v[32:35], v10 offset:10240\n" \
"v_mfma_f32_16x16x32_bf16 v[56:59], a[156:159], v[40:43], v[56:59]\n" \
"ds_read_b128 v[36:39], v10 offset:26624\n" \
"v_mfma_f32_16x16x32_bf16 v[60:63], a[156:159], v[44:47], v[60:63]\n" \
"s_waitcnt lgkmcnt(4)\n" \
"v_mfma_f32_16x16x32_bf16 v[48:51], a[32:35], v[16:19], v[48:51]\n" \
"v_mfma_f32_16x16x32_bf16 v[52:55], a[32:35], v[20:23], v[52:55]\n" \
"ds_read_b128 v[40:43], v10 offset:11264\n" \
"v_mfma_f32_16x16x32_bf16 v[56:59], a[160:163], v[16:19], v[56:59]\n" \
"ds_read_b128 v[44:47], v10 offset:27648\n" \
"v_mfma_f32_16x16x32_bf16 v[60:63], a[160:163], v[20:23], v[60:63]\n" \
"s_waitcnt lgkmcnt(4)\n" \
"v_mfma_f32_16x16x32_bf16 v[48:51], a[36:39], v[24:27], v[48:51]\n" \
"buffer_load_dwordx4 v4, s[36:39], s61 offen offset:2048 sc1 lds\n" \
"v_mfma_f32_16x16x32_bf16 v[52:55], a[36:39], v[28:31], v[52:55]\n" \
"ds_read_b128 v[16:19], v10 offset:12288\n" \
"v_mfma_f32_16x16x32_bf16 v[56:59], a[164:167], v[24:27], v[56:59]\n" \
"ds_read_b128 v[20:23], v10 offset:28672\n" \
"v_mfma_f32_16x16x32_bf16 v[60:63], a[164:167], v[28:31], v[60:63]\n" \
"s_waitcnt lgkmcnt(4)\n" \
"v_mfma_f32_16x16x32_bf16 v[48:51], a[40:43], v[32:35], v[48:51]\n" \
"buffer_load_dwordx4 v4, s[36:39], s61 offen offset:3072 sc1 lds\n" \
"v_mfma_f32_16x16x32_bf16 v[52:55], a[40:43], v[36:39], v[52:55]\n" \
"ds_read_b128 v[24:27], v10 offset:13312\n" \
"v_mfma_f32_16x16x32_bf16 v[56:59], a[168:171], v[32:35], v[56:59]\n" \
"ds_read_b128 v[28:31], v10 offset:29696\n" \
"v_mfma_f32_16x16x32_bf16 v[60:63], a[168:171], v[36:39], v[60:63]\n" \
"s_waitcnt lgkmcnt(4)\n" \
"v_mfma_f32_16x16x32_bf16 v[48:51], a[44:47], v[40:43], v[48:51]\n" \
"v_mfma_f32_16x16x32_bf16 v[52:55], a[44:47], v[44:47], v[52:55]\n" \
"ds_read_b128 v[32:35], v10 offset:14336\n" \
"v_mfma_f32_16x16x32_bf16 v[56:59], a[172:175], v[40:43], v[56:59]\n" \
"ds_read_b128 v[36:39], v10 offset:30720\n" \
"v_mfma_f32_16x16x32_bf16 v[60:63], a[172:175], v[44:47], v[60:63]\n" \
"s_waitcnt lgkmcnt(4)\n" \
"v_mfma_f32_16x16x32_bf16 v[48:51], a[48:51], v[16:19], v[48:51]\n" \
"v_mfma_f32_16x16x32_bf16 v[52:55], a[48:51], v[20:23], v[52:55]\n" \
"ds_read_b128 v[40:43], v10 offset:15360\n" \
"v_mfma_f32_16x16x32_bf16 v[56:59], a[176:179], v[16:19], v[56:59]\n" \
"ds_read_b128 v[44:47], v10 offset:31744\n" \
"v_mfma_f32_16x16x32_bf16 v[60:63], a[176:179], v[20:23], v[60:63]\n" \
"s_waitcnt lgkmcnt(4)\n" \
"v_mfma_f32_16x16x32_bf16 v[48:51], a[52:55], v[24:27], v[48:51]\n" \
"s_waitcnt vmcnt(12)\n" \
"s_barrier\n" \
"v_mfma_f32_16x16x32_bf16 v[52:55], a[52:55], v[28:31], v[52:55]\n" \
"ds_read_b128 v[16:19], v10 offset:32768\n" \
"v_mfma_f32_16x16x32_bf16 v[56:59], a[180:183], v[24:27], v[56:59]\n" \
"ds_read_b128 v[20:23], v10 offset:49152\n" \
"v_mfma_f32_16x16x32_bf16 v[60:63], a[180:183], v[28:31], v[60:63]\n" \
"s_waitcnt lgkmcnt(4)\n" \
"v_mfma_f32_16x16x32_bf16 v[48:51], a[56:59], v[32:35], v[48:51]\n" \
"v_mfma_f32_16x16x32_bf16 v[52:55], a[56:59], v[36:39], v[52:55]\n" \
"ds_read_b128 v[24:27], v10 offset:33792\n" \
"v_mfma_f32_16x16x32_bf16 v[56:59], a[184:187], v[32:35], v[56:59]\n" \
"ds_read_b128 v[28:31], v10 offset:50176\n" \
"v_mfma_f32_16x16x32_bf16 v[60:63], a[184:187], v[36:39], v[60:63]\n" \
"s_waitcnt lgkmcnt(4)\n" \
"v_mfma_f32_16x16x32_bf16 v[48:51], a[60:63], v[40:43], v[48:51]\n" \
"v_mfma_f32_16x16x32_bf16 v[52:55], a[60:63], v[44:47], v[52:55]\n" \
"ds_read_b128 v[32:35], v10 offset:34816\n" \
"v_mfma_f32_16x16x32_bf16 v[56:59], a[188:191], v[40:43], v[56:59]\n" \
"ds_read_b128 v[36:39], v10 offset:51200\n" \
"v_mfma_f32_16x16x32_bf16 v[60:63], a[188:191], v[44:47], v[60:63]\n" \
"s_waitcnt lgkmcnt(4)\n" \
"v_mfma_f32_16x16x32_bf16 v[48:51], a[64:67], v[16:19], v[48:51]\n" \
"s_min_u32 s61, s56, s60\n" \
"s_add_u32 s61, s61, 16384\n" \
"s_add_u32 s56, s56, 0x10000\n" \
"s_add_u32 m0, s55, 98304\n" \
"s_nop 0\n" \
"buffer_load_dwordx4 v4, s[36:39], s61 offen offset:0 sc1 lds\n" \
"v_mfma_f32_16x16x32_bf16 v[52:55], a[64:67], v[20:23], v[52:55]\n" \
"ds_read_b128 v[40:43], v10 offset:35840\n" \
"v_mfma_f32_16x16x32_bf16 v[56:59], a[192:195], v[16:19], v[56:59]\n" \
"ds_read_b128 v[44:47], v10 offset:52224\n" \
"v_mfma_f32_16x16x32_bf16 v[60:63], a[192:195], v[20:23], v[60:63]\n" \
"s_waitcnt lgkmcnt(4)\n" \
"v_mfma_f32_16x16x32_bf16 v[48:51], a[68:71], v[24:27], v[48:51]\n" \
"buffer_load_dwordx4 v4, s[36:39], s61 offen offset:1024 sc1 lds\n" \
"v_mfma_f32_16x16x32_bf16 v[52:55], a[68:71], v[28:31], v[52:55]\n" \
"ds_read_b128 v[16:19], v10 offset:36864\n" \
"v_mfma_f32_16x16x32_bf16 v[56:59], a[196:199], v[24:27], v[56:59]\n" \
"ds_read_b128 v[20:23], v10 offset:53248\n" \
"v_mfma_f32_16x16x32_bf16 v[60:63], a[196:199], v[28:31], v[60:63]\n" \
"s_waitcnt lgkmcnt(4)\n" \
"v_mfma_f32_16x16x32_bf16 v[48:51], a[72:75], v[32:35], v[48:51]\n" \
"v_mfma_f32_16x16x32_bf16 v[52:55], a[72:75], v[36:39], v[52:55]\n" \
"ds_read_b128 v[24:27], v10 offset:37888\n" \
"v_mfma_f32_16x16x32_bf16 v[56:59], a[200:203], v[32:35], v[56:59]\n" \
"ds_read_b128 v[28:31], v10 offset:54272\n" \
"v_mfma_f32_16x16x32_bf16 v[60:63], a[200:203], v[36:39], v[60:63]\n" \
"s_waitcnt lgkmcnt(4)\n" \
"v_mfma_f32_16x16x32_bf16 v[48:51], a[76:79], v[40:43], v[48:51]\n" \
"buffer_load_dwordx4 v4, s[36:39], s61 offen offset:2048 sc1 lds\n" \
"v_mfma_f32_16x16x32_bf16 v[52:55], a[76:79], v[44:47], v[52:55]\n" \
"ds_read_b128 v[32:35], v10 offset:38912\n" \
"v_mfma_f32_16x16x32_bf16 v[56:59], a[204:207], v[40:43], v[56:59]\n" \
"ds_read_b128 v[36:39], v10 offset:55296\n" \
"v_mfma_f32_16x16x32_bf16 v[60:63], a[204:207], v[44:47], v[60:63]\n" \
"s_waitcnt lgkmcnt(4)\n" \
"v_mfma_f32_16x16x32_bf16 v[48:51], a[80:83], v[16:19], v[48:51]\n" \
"buffer_load_dwordx4 v4, s[36:39], s61 offen offset:3072 sc1 lds\n" \
"v_mfma_f32_16x16x32_bf16 v[52:55], a[80:83], v[20:23], v[52:55]\n" \
"ds_read_b128 v[40:43], v10 offset:39936\n" \
"v_mfma_f32_16x16x32_bf16 v[56:59], a[208:211], v[16:19], v[56:59]\n" \
"ds_read_b128 v[44:47], v10 offset:56320\n" \
"v_mfma_f32_16x16x32_bf16 v[60:63], a[208:211], v[20:23], v[60:63]\n" \
"s_waitcnt lgkmcnt(4)\n" \
"v_mfma_f32_16x16x32_bf16 v[48:51], a[84:87], v[24:27], v[48:51]\n" \
"v_mfma_f32_16x16x32_bf16 v[52:55], a[84:87], v[28:31], v[52:55]\n" \
"ds_read_b128 v[16:19], v10 offset:40960\n" \
"v_mfma_f32_16x16x32_bf16 v[56:59], a[212:215], v[24:27], v[56:59]\n" \
"ds_read_b128 v[20:23], v10 offset:57344\n" \
"v_mfma_f32_16x16x32_bf16 v[60:63], a[212:215], v[28:31], v[60:63]\n" \
"s_waitcnt lgkmcnt(4)\n" \
"v_mfma_f32_16x16x32_bf16 v[48:51], a[88:91], v[32:35], v[48:51]\n" \
"s_add_u32 m0, s55, 102400\n" \
"s_add_u32 s61, s61, 4096\n" \
"buffer_load_dwordx4 v4, s[36:39], s61 offen offset:0 sc1 lds\n" \
"v_mfma_f32_16x16x32_bf16 v[52:55], a[88:91], v[36:39], v[52:55]\n" \
"ds_read_b128 v[24:27], v10 offset:41984\n" \
"v_mfma_f32_16x16x32_bf16 v[56:59], a[216:219], v[32:35], v[56:59]\n" \
"ds_read_b128 v[28:31], v10 offset:58368\n" \
"v_mfma_f32_16x16x32_bf16 v[60:63], a[216:219], v[36:39], v[60:63]\n" \
"s_waitcnt lgkmcnt(4)\n" \
"v_mfma_f32_16x16x32_bf16 v[48:51], a[92:95], v[40:43], v[48:51]\n" \
"buffer_load_dwordx4 v4, s[36:39], s61 offen offset:1024 sc1 lds\n" \
"v_mfma_f32_16x16x32_bf16 v[52:55], a[92:95], v[44:47], v[52:55]\n" \
"ds_read_b128 v[32:35], v10 offset:43008\n" \
"v_mfma_f32_16x16x32_bf16 v[56:59], a[220:223], v[40:43], v[56:59]\n" \
"ds_read_b128 v[36:39], v10 offset:59392\n" \
"v_mfma_f32_16x16x32_bf16 v[60:63], a[220:223], v[44:47], v[60:63]\n" \
"s_waitcnt lgkmcnt(4)\n" \
"v_mfma_f32_16x16x32_bf16 v[48:51], a[96:99], v[16:19], v[48:51]\n" \
"v_mfma_f32_16x16x32_bf16 v[52:55], a[96:99], v[20:23], v[52:55]\n" \
"ds_read_b128 v[40:43], v10 offset:44032\n" \
"v_mfma_f32_16x16x32_bf16 v[56:59], a[224:227], v[16:19], v[56:59]\n" \
"ds_read_b128 v[44:47], v10 offset:60416\n" \
"v_mfma_f32_16x16x32_bf16 v[60:63], a[224:227], v[20:23], v[60:63]\n" \
"s_waitcnt lgkmcnt(4)\n" \
"v_mfma_f32_16x16x32_bf16 v[48:51], a[100:103], v[24:27], v[48:51]\n" \
"buffer_load_dwordx4 v4, s[36:39], s61 offen offset:2048 sc1 lds\n" \
"v_mfma_f32_16x16x32_bf16 v[52:55], a[100:103], v[28:31], v[52:55]\n" \
"ds_read_b128 v[16:19], v10 offset:45056\n" \
"v_mfma_f32_16x16x32_bf16 v[56:59], a[228:231], v[24:27], v[56:59]\n" \
"ds_read_b128 v[20:23], v10 offset:61440\n" \
"v_mfma_f32_16x16x32_bf16 v[60:63], a[228:231], v[28:31], v[60:63]\n" \
"s_waitcnt lgkmcnt(4)\n" \
"v_mfma_f32_16x16x32_bf16 v[48:51], a[104:107], v[32:35], v[48:51]\n" \
"buffer_load_dwordx4 v4, s[36:39], s61 offen offset:3072 sc1 lds\n" \
"v_mfma_f32_16x16x32_bf16 v[52:55], a[104:107], v[36:39], v[52:55]\n" \
"ds_read_b128 v[24:27], v10 offset:46080\n" \
"v_mfma_f32_16x16x32_bf16 v[56:59], a[232:235], v[32:35], v[56:59]\n" \
"ds_read_b128 v[28:31], v10 offset:62464\n" \
"v_mfma_f32_16x16x32_bf16 v[60:63], a[232:235], v[36:39], v[60:63]\n" \
"s_waitcnt lgkmcnt(4)\n" \
"v_mfma_f32_16x16x32_bf16 v[48:51], a[108:111], v[40:43], v[48:51]\n" \
"v_mfma_f32_16x16x32_bf16 v[52:55], a[108:111], v[44:47], v[52:55]\n" \
"ds_read_b128 v[32:35], v10 offset:47104\n" \
"v_mfma_f32_16x16x32_bf16 v[56:59], a[236:239], v[40:43], v[56:59]\n" \
"ds_read_b128 v[36:39], v10 offset:63488\n" \
"v_mfma_f32_16x16x32_bf16 v[60:63], a[236:239], v[44:47], v[60:63]\n" \
"s_waitcnt lgkmcnt(4)\n" \
"v_mfma_f32_16x16x32_bf16 v[48:51], a[112:115], v[16:19], v[48:51]\n" \
"v_mfma_f32_16x16x32_bf16 v[52:55], a[112:115], v[20:23], v[52:55]\n" \
"ds_read_b128 v[40:43], v10 offset:48128\n" \
"v_mfma_f32_16x16x32_bf16 v[56:59], a[240:243], v[16:19], v[56:59]\n" \
"ds_read_b128 v[44:47], v10 offset:64512\n" \
"v_mfma_f32_16x16x32_bf16 v[60:63], a[240:243], v[20:23], v[60:63]\n" \
"s_waitcnt lgkmcnt(4)\n" \
"v_mfma_f32_16x16x32_bf16 v[48:51], a[116:119], v[24:27], v[48:51]\n" \
"s_waitcnt vmcnt(8)\n" \
"s_barrier\n" \
"v_mfma_f32_16x16x32_bf16 v[52:55], a[116:119], v[28:31], v[52:55]\n" \
"ds_read_b128 v[16:19], v12 offset:0\n" \
"v_mfma_f32_16x16x32_bf16 v[56:59], a[244:247], v[24:27], v[56:59]\n" \
"ds_read_b128 v[20:23], v12 offset:16384\n" \
"v_mfma_f32_16x16x32_bf16 v[60:63], a[244:247], v[28:31], v[60:63]\n" \
"s_waitcnt lgkmcnt(4)\n" \
"buffer_store_dword v13, v6, s[36:39], 0 offen sc1\n" \
"v_mfma_f32_16x16x32_bf16 v[48:51], a[120:123], v[32:35], v[48:51]\n" \
"v_cmp_gt_u32_e64 vcc, 16, v15\n" \
"v_mfma_f32_16x16x32_bf16 v[52:55], a[120:123], v[36:39], v[52:55]\n" \
"ds_read_b128 v[24:27], v12 offset:1024\n" \
"v_mfma_f32_16x16x32_bf16 v[56:59], a[248:251], v[32:35], v[56:59]\n" \
"ds_read_b128 v[28:31], v12 offset:17408\n" \
"v_mfma_f32_16x16x32_bf16 v[60:63], a[248:251], v[36:39], v[60:63]\n" \
"s_waitcnt lgkmcnt(4)\n" \
"v_mfma_f32_16x16x32_bf16 v[48:51], a[124:127], v[40:43], v[48:51]\n" \
"s_cbranch_vccnz .Lp1qk_m0_%=\n" \
".Lp1qk_m0k_%=:\n" \
"v_mfma_f32_16x16x32_bf16 v[52:55], a[124:127], v[44:47], v[52:55]\n" \
"ds_read_b128 v[32:35], v12 offset:2048\n" \
"v_mfma_f32_16x16x32_bf16 v[56:59], a[252:255], v[40:43], v[56:59]\n" \
"ds_read_b128 v[36:39], v12 offset:18432\n" \
"v_mfma_f32_16x16x32_bf16 v[60:63], a[252:255], v[44:47], v[60:63]\n" \
"s_waitcnt lgkmcnt(4)\n" \
"s_add_u32 s57, s57, s49\n" \
"s_add_u32 s58, s58, s49\n" \
"s_add_u32 s59, s59, 1\n" \
"v_mov_b32_e64 v5, v7\n" \
"v_mov_b32_e64 v6, v11\n" \
"s_cmp_ge_u32 s59, s47\n" \
"s_nop 0\n" \
"s_cbranch_scc1 .Lp1qk_out0_%=\n" \
"v_mfma_f32_16x16x32_bf16 v[64:67], a[0:3], v[16:19], 0\n" \
"s_min_u32 s61, s56, s60\n" \
"s_add_u32 m0, s55, 0\n" \
"s_nop 0\n" \
"buffer_load_dwordx4 v4, s[36:39], s61 offen offset:0 sc1 lds\n" \
"v_mov_b32_e64 v13, s59\n" \
"v_mfma_f32_16x16x32_bf16 v[68:71], a[0:3], v[20:23], 0\n" \
"ds_read_b128 v[40:43], v12 offset:3072\n" \
"s_nop 0\n" \
"v_mfma_f32_16x16x32_bf16 v[72:75], a[128:131], v[16:19], 0\n" \
"ds_read_b128 v[44:47], v12 offset:19456\n" \
"s_nop 0\n" \
"v_mfma_f32_16x16x32_bf16 v[76:79], a[128:131], v[20:23], 0\n" \
"s_waitcnt lgkmcnt(4)\n" \
"v_mul_f32 v48, s51, v48\n" \
"v_mul_f32_e64 v49, s51, v49\n" \
"v_mfma_f32_16x16x32_bf16 v[64:67], a[4:7], v[24:27], v[64:67]\n" \
"buffer_load_dwordx4 v4, s[36:39], s61 offen offset:1024 sc1 lds\n" \
"v_mfma_f32_16x16x32_bf16 v[68:71], a[4:7], v[28:31], v[68:71]\n" \
"ds_read_b128 v[16:19], v12 offset:4096\n" \
"v_mul_f32_e64 v50, s51, v50\n" \
"v_mfma_f32_16x16x32_bf16 v[72:75], a[132:135], v[24:27], v[72:75]\n" \
"ds_read_b128 v[20:23], v12 offset:20480\n" \
"v_mul_f32_e64 v51, s51, v51\n" \
"v_mfma_f32_16x16x32_bf16 v[76:79], a[132:135], v[28:31], v[76:79]\n" \
"s_waitcnt lgkmcnt(4)\n" \
"v_mul_f32 v56, s51, v56\n" \
"v_mul_f32_e64 v57, s51, v57\n" \
"v_mfma_f32_16x16x32_bf16 v[64:67], a[8:11], v[32:35], v[64:67]\n" \
"s_add_u32 s63, s59, 2\n" \
"s_min_u32 s63, s63, 63\n" \
"v_mfma_f32_16x16x32_bf16 v[68:71], a[8:11], v[36:39], v[68:71]\n" \
"ds_read_b128 v[24:27], v12 offset:5120\n" \
"s_lshr_b32 s63, s63, 3\n" \
"s_lshl_b32 s63, s63, 7\n" \
"v_mul_f32_e64 v58, s51, v58\n" \
"v_mfma_f32_16x16x32_bf16 v[72:75], a[136:139], v[32:35], v[72:75]\n" \
"ds_read_b128 v[28:31], v12 offset:21504\n" \
"v_mul_f32_e64 v59, s51, v59\n" \
"v_mfma_f32_16x16x32_bf16 v[76:79], a[136:139], v[36:39], v[76:79]\n" \
"s_waitcnt lgkmcnt(4)\n" \
"v_cvt_pk_bf16_f32 v80, v48, v49\n" \
"v_cvt_pk_bf16_f32 v81, v50, v51\n" \
"v_mfma_f32_16x16x32_bf16 v[64:67], a[12:15], v[40:43], v[64:67]\n" \
"buffer_load_dwordx4 v4, s[36:39], s61 offen offset:2048 sc1 lds\n" \
"v_mfma_f32_16x16x32_bf16 v[68:71], a[12:15], v[44:47], v[68:71]\n" \
"ds_read_b128 v[32:35], v12 offset:6144\n" \
"v_cvt_pk_bf16_f32 v82, v56, v57\n" \
"v_mfma_f32_16x16x32_bf16 v[72:75], a[140:143], v[40:43], v[72:75]\n" \
"ds_read_b128 v[36:39], v12 offset:22528\n" \
"v_cvt_pk_bf16_f32 v83, v58, v59\n" \
"v_mfma_f32_16x16x32_bf16 v[76:79], a[140:143], v[44:47], v[76:79]\n" \
"s_waitcnt lgkmcnt(4)\n" \
"buffer_store_dwordx4 v[80:83], v5, s[36:39], s57 offen sc1\n" \
"v_mul_f32_e64 v52, s51, v52\n" \
"v_mfma_f32_16x16x32_bf16 v[64:67], a[16:19], v[16:19], v[64:67]\n" \
"buffer_load_dwordx4 v4, s[36:39], s61 offen offset:3072 sc1 lds\n" \
"v_mfma_f32_16x16x32_bf16 v[68:71], a[16:19], v[20:23], v[68:71]\n" \
"ds_read_b128 v[40:43], v12 offset:7168\n" \
"v_mul_f32_e64 v53, s51, v53\n" \
"v_mfma_f32_16x16x32_bf16 v[72:75], a[144:147], v[16:19], v[72:75]\n" \
"ds_read_b128 v[44:47], v12 offset:23552\n" \
"v_mul_f32_e64 v54, s51, v54\n" \
"v_mfma_f32_16x16x32_bf16 v[76:79], a[144:147], v[20:23], v[76:79]\n" \
"s_waitcnt lgkmcnt(4)\n" \
"v_mul_f32 v55, s51, v55\n" \
"v_mul_f32_e64 v60, s51, v60\n" \
"v_mfma_f32_16x16x32_bf16 v[64:67], a[20:23], v[24:27], v[64:67]\n" \
"buffer_load_dword v15, v14, s[36:39], s63 offen sc1\n" \
"v_mfma_f32_16x16x32_bf16 v[68:71], a[20:23], v[28:31], v[68:71]\n" \
"ds_read_b128 v[16:19], v12 offset:8192\n" \
"v_mul_f32_e64 v61, s51, v61\n" \
"v_mfma_f32_16x16x32_bf16 v[72:75], a[148:151], v[24:27], v[72:75]\n" \
"ds_read_b128 v[20:23], v12 offset:24576\n" \
"v_mul_f32_e64 v62, s51, v62\n" \
"v_mfma_f32_16x16x32_bf16 v[76:79], a[148:151], v[28:31], v[76:79]\n" \
"s_waitcnt lgkmcnt(4)\n" \
"v_mul_f32 v63, s51, v63\n" \
"v_cvt_pk_bf16_f32 v84, v52, v53\n" \
"v_mfma_f32_16x16x32_bf16 v[64:67], a[24:27], v[32:35], v[64:67]\n" \
"s_add_u32 m0, s55, 4096\n" \
"s_add_u32 s61, s61, 4096\n" \
"buffer_load_dwordx4 v4, s[36:39], s61 offen offset:0 sc1 lds\n" \
"v_mfma_f32_16x16x32_bf16 v[68:71], a[24:27], v[36:39], v[68:71]\n" \
"ds_read_b128 v[24:27], v12 offset:9216\n" \
"v_cvt_pk_bf16_f32 v85, v54, v55\n" \
"v_mfma_f32_16x16x32_bf16 v[72:75], a[152:155], v[32:35], v[72:75]\n" \
"ds_read_b128 v[28:31], v12 offset:25600\n" \
"v_cvt_pk_bf16_f32 v86, v60, v61\n" \
"v_mfma_f32_16x16x32_bf16 v[76:79], a[152:155], v[36:39], v[76:79]\n" \
"s_waitcnt lgkmcnt(4)\n" \
"v_cvt_pk_bf16_f32 v87, v62, v63\n" \
"buffer_store_dwordx4 v[84:87], v5, s[36:39], s58 offen sc1\n" \
"v_mfma_f32_16x16x32_bf16 v[64:67], a[28:31], v[40:43], v[64:67]\n" \
"buffer_load_dwordx4 v4, s[36:39], s61 offen offset:1024 sc1 lds\n" \
"v_mfma_f32_16x16x32_bf16 v[68:71], a[28:31], v[44:47], v[68:71]\n" \
"ds_read_b128 v[32:35], v12 offset:10240\n" \
"v_mfma_f32_16x16x32_bf16 v[72:75], a[156:159], v[40:43], v[72:75]\n" \
"ds_read_b128 v[36:39], v12 offset:26624\n" \
"v_mfma_f32_16x16x32_bf16 v[76:79], a[156:159], v[44:47], v[76:79]\n" \
"s_waitcnt lgkmcnt(4)\n" \
"v_mfma_f32_16x16x32_bf16 v[64:67], a[32:35], v[16:19], v[64:67]\n" \
"v_mfma_f32_16x16x32_bf16 v[68:71], a[32:35], v[20:23], v[68:71]\n" \
"ds_read_b128 v[40:43], v12 offset:11264\n" \
"v_mfma_f32_16x16x32_bf16 v[72:75], a[160:163], v[16:19], v[72:75]\n" \
"ds_read_b128 v[44:47], v12 offset:27648\n" \
"v_mfma_f32_16x16x32_bf16 v[76:79], a[160:163], v[20:23], v[76:79]\n" \
"s_waitcnt lgkmcnt(4)\n" \
"v_mfma_f32_16x16x32_bf16 v[64:67], a[36:39], v[24:27], v[64:67]\n" \
"buffer_load_dwordx4 v4, s[36:39], s61 offen offset:2048 sc1 lds\n" \
"v_mfma_f32_16x16x32_bf16 v[68:71], a[36:39], v[28:31], v[68:71]\n" \
"ds_read_b128 v[16:19], v12 offset:12288\n" \
"v_mfma_f32_16x16x32_bf16 v[72:75], a[164:167], v[24:27], v[72:75]\n" \
"ds_read_b128 v[20:23], v12 offset:28672\n" \
"v_mfma_f32_16x16x32_bf16 v[76:79], a[164:167], v[28:31], v[76:79]\n" \
"s_waitcnt lgkmcnt(4)\n" \
"v_mfma_f32_16x16x32_bf16 v[64:67], a[40:43], v[32:35], v[64:67]\n" \
"buffer_load_dwordx4 v4, s[36:39], s61 offen offset:3072 sc1 lds\n" \
"v_mfma_f32_16x16x32_bf16 v[68:71], a[40:43], v[36:39], v[68:71]\n" \
"ds_read_b128 v[24:27], v12 offset:13312\n" \
"v_mfma_f32_16x16x32_bf16 v[72:75], a[168:171], v[32:35], v[72:75]\n" \
"ds_read_b128 v[28:31], v12 offset:29696\n" \
"v_mfma_f32_16x16x32_bf16 v[76:79], a[168:171], v[36:39], v[76:79]\n" \
"s_waitcnt lgkmcnt(4)\n" \
"v_mfma_f32_16x16x32_bf16 v[64:67], a[44:47], v[40:43], v[64:67]\n" \
"v_mfma_f32_16x16x32_bf16 v[68:71], a[44:47], v[44:47], v[68:71]\n" \
"ds_read_b128 v[32:35], v12 offset:14336\n" \
"v_mfma_f32_16x16x32_bf16 v[72:75], a[172:175], v[40:43], v[72:75]\n" \
"ds_read_b128 v[36:39], v12 offset:30720\n" \
"v_mfma_f32_16x16x32_bf16 v[76:79], a[172:175], v[44:47], v[76:79]\n" \
"s_waitcnt lgkmcnt(4)\n" \
"v_mfma_f32_16x16x32_bf16 v[64:67], a[48:51], v[16:19], v[64:67]\n" \
"v_mfma_f32_16x16x32_bf16 v[68:71], a[48:51], v[20:23], v[68:71]\n" \
"ds_read_b128 v[40:43], v12 offset:15360\n" \
"v_mfma_f32_16x16x32_bf16 v[72:75], a[176:179], v[16:19], v[72:75]\n" \
"ds_read_b128 v[44:47], v12 offset:31744\n" \
"v_mfma_f32_16x16x32_bf16 v[76:79], a[176:179], v[20:23], v[76:79]\n" \
"s_waitcnt lgkmcnt(4)\n" \
"v_mfma_f32_16x16x32_bf16 v[64:67], a[52:55], v[24:27], v[64:67]\n" \
"s_waitcnt vmcnt(12)\n" \
"s_barrier\n" \
"v_mfma_f32_16x16x32_bf16 v[68:71], a[52:55], v[28:31], v[68:71]\n" \
"ds_read_b128 v[16:19], v12 offset:32768\n" \
"v_mfma_f32_16x16x32_bf16 v[72:75], a[180:183], v[24:27], v[72:75]\n" \
"ds_read_b128 v[20:23], v12 offset:49152\n" \
"v_mfma_f32_16x16x32_bf16 v[76:79], a[180:183], v[28:31], v[76:79]\n" \
"s_waitcnt lgkmcnt(4)\n" \
"v_mfma_f32_16x16x32_bf16 v[64:67], a[56:59], v[32:35], v[64:67]\n" \
"v_mfma_f32_16x16x32_bf16 v[68:71], a[56:59], v[36:39], v[68:71]\n" \
"ds_read_b128 v[24:27], v12 offset:33792\n" \
"v_mfma_f32_16x16x32_bf16 v[72:75], a[184:187], v[32:35], v[72:75]\n" \
"ds_read_b128 v[28:31], v12 offset:50176\n" \
"v_mfma_f32_16x16x32_bf16 v[76:79], a[184:187], v[36:39], v[76:79]\n" \
"s_waitcnt lgkmcnt(4)\n" \
"v_mfma_f32_16x16x32_bf16 v[64:67], a[60:63], v[40:43], v[64:67]\n" \
"v_mfma_f32_16x16x32_bf16 v[68:71], a[60:63], v[44:47], v[68:71]\n" \
"ds_read_b128 v[32:35], v12 offset:34816\n" \
"v_mfma_f32_16x16x32_bf16 v[72:75], a[188:191], v[40:43], v[72:75]\n" \
"ds_read_b128 v[36:39], v12 offset:51200\n" \
"v_mfma_f32_16x16x32_bf16 v[76:79], a[188:191], v[44:47], v[76:79]\n" \
"s_waitcnt lgkmcnt(4)\n" \
"v_mfma_f32_16x16x32_bf16 v[64:67], a[64:67], v[16:19], v[64:67]\n" \
"s_min_u32 s61, s56, s60\n" \
"s_add_u32 s61, s61, 16384\n" \
"s_add_u32 s56, s56, 0x10000\n" \
"s_add_u32 m0, s55, 32768\n" \
"s_nop 0\n" \
"buffer_load_dwordx4 v4, s[36:39], s61 offen offset:0 sc1 lds\n" \
"v_mfma_f32_16x16x32_bf16 v[68:71], a[64:67], v[20:23], v[68:71]\n" \
"ds_read_b128 v[40:43], v12 offset:35840\n" \
"v_mfma_f32_16x16x32_bf16 v[72:75], a[192:195], v[16:19], v[72:75]\n" \
"ds_read_b128 v[44:47], v12 offset:52224\n" \
"v_mfma_f32_16x16x32_bf16 v[76:79], a[192:195], v[20:23], v[76:79]\n" \
"s_waitcnt lgkmcnt(4)\n" \
"v_mfma_f32_16x16x32_bf16 v[64:67], a[68:71], v[24:27], v[64:67]\n" \
"buffer_load_dwordx4 v4, s[36:39], s61 offen offset:1024 sc1 lds\n" \
"v_mfma_f32_16x16x32_bf16 v[68:71], a[68:71], v[28:31], v[68:71]\n" \
"ds_read_b128 v[16:19], v12 offset:36864\n" \
"v_mfma_f32_16x16x32_bf16 v[72:75], a[196:199], v[24:27], v[72:75]\n" \
"ds_read_b128 v[20:23], v12 offset:53248\n" \
"v_mfma_f32_16x16x32_bf16 v[76:79], a[196:199], v[28:31], v[76:79]\n" \
"s_waitcnt lgkmcnt(4)\n" \
"v_mfma_f32_16x16x32_bf16 v[64:67], a[72:75], v[32:35], v[64:67]\n" \
"v_mfma_f32_16x16x32_bf16 v[68:71], a[72:75], v[36:39], v[68:71]\n" \
"ds_read_b128 v[24:27], v12 offset:37888\n" \
"v_mfma_f32_16x16x32_bf16 v[72:75], a[200:203], v[32:35], v[72:75]\n" \
"ds_read_b128 v[28:31], v12 offset:54272\n" \
"v_mfma_f32_16x16x32_bf16 v[76:79], a[200:203], v[36:39], v[76:79]\n" \
"s_waitcnt lgkmcnt(4)\n" \
"v_mfma_f32_16x16x32_bf16 v[64:67], a[76:79], v[40:43], v[64:67]\n" \
"buffer_load_dwordx4 v4, s[36:39], s61 offen offset:2048 sc1 lds\n" \
"v_mfma_f32_16x16x32_bf16 v[68:71], a[76:79], v[44:47], v[68:71]\n" \
"ds_read_b128 v[32:35], v12 offset:38912\n" \
"v_mfma_f32_16x16x32_bf16 v[72:75], a[204:207], v[40:43], v[72:75]\n" \
"ds_read_b128 v[36:39], v12 offset:55296\n" \
"v_mfma_f32_16x16x32_bf16 v[76:79], a[204:207], v[44:47], v[76:79]\n" \
"s_waitcnt lgkmcnt(4)\n" \
"v_mfma_f32_16x16x32_bf16 v[64:67], a[80:83], v[16:19], v[64:67]\n" \
"buffer_load_dwordx4 v4, s[36:39], s61 offen offset:3072 sc1 lds\n" \
"v_mfma_f32_16x16x32_bf16 v[68:71], a[80:83], v[20:23], v[68:71]\n" \
"ds_read_b128 v[40:43], v12 offset:39936\n" \
"v_mfma_f32_16x16x32_bf16 v[72:75], a[208:211], v[16:19], v[72:75]\n" \
"ds_read_b128 v[44:47], v12 offset:56320\n" \
"v_mfma_f32_16x16x32_bf16 v[76:79], a[208:211], v[20:23], v[76:79]\n" \
"s_waitcnt lgkmcnt(4)\n" \
"v_mfma_f32_16x16x32_bf16 v[64:67], a[84:87], v[24:27], v[64:67]\n" \
"v_mfma_f32_16x16x32_bf16 v[68:71], a[84:87], v[28:31], v[68:71]\n" \
"ds_read_b128 v[16:19], v12 offset:40960\n" \
"v_mfma_f32_16x16x32_bf16 v[72:75], a[212:215], v[24:27], v[72:75]\n" \
"ds_read_b128 v[20:23], v12 offset:57344\n" \
"v_mfma_f32_16x16x32_bf16 v[76:79], a[212:215], v[28:31], v[76:79]\n" \
"s_waitcnt lgkmcnt(4)\n" \
"v_mfma_f32_16x16x32_bf16 v[64:67], a[88:91], v[32:35], v[64:67]\n" \
"s_add_u32 m0, s55, 36864\n" \
"s_add_u32 s61, s61, 4096\n" \
"buffer_load_dwordx4 v4, s[36:39], s61 offen offset:0 sc1 lds\n" \
"v_mfma_f32_16x16x32_bf16 v[68:71], a[88:91], v[36:39], v[68:71]\n" \
"ds_read_b128 v[24:27], v12 offset:41984\n" \
"v_mfma_f32_16x16x32_bf16 v[72:75], a[216:219], v[32:35], v[72:75]\n" \
"ds_read_b128 v[28:31], v12 offset:58368\n" \
"v_mfma_f32_16x16x32_bf16 v[76:79], a[216:219], v[36:39], v[76:79]\n" \
"s_waitcnt lgkmcnt(4)\n" \
"v_mfma_f32_16x16x32_bf16 v[64:67], a[92:95], v[40:43], v[64:67]\n" \
"buffer_load_dwordx4 v4, s[36:39], s61 offen offset:1024 sc1 lds\n" \
"v_mfma_f32_16x16x32_bf16 v[68:71], a[92:95], v[44:47], v[68:71]\n" \
"ds_read_b128 v[32:35], v12 offset:43008\n" \
"v_mfma_f32_16x16x32_bf16 v[72:75], a[220:223], v[40:43], v[72:75]\n" \
"ds_read_b128 v[36:39], v12 offset:59392\n" \
"v_mfma_f32_16x16x32_bf16 v[76:79], a[220:223], v[44:47], v[76:79]\n" \
"s_waitcnt lgkmcnt(4)\n" \
"v_mfma_f32_16x16x32_bf16 v[64:67], a[96:99], v[16:19], v[64:67]\n" \
"v_mfma_f32_16x16x32_bf16 v[68:71], a[96:99], v[20:23], v[68:71]\n" \
"ds_read_b128 v[40:43], v12 offset:44032\n" \
"v_mfma_f32_16x16x32_bf16 v[72:75], a[224:227], v[16:19], v[72:75]\n" \
"ds_read_b128 v[44:47], v12 offset:60416\n" \
"v_mfma_f32_16x16x32_bf16 v[76:79], a[224:227], v[20:23], v[76:79]\n" \
"s_waitcnt lgkmcnt(4)\n" \
"v_mfma_f32_16x16x32_bf16 v[64:67], a[100:103], v[24:27], v[64:67]\n" \
"buffer_load_dwordx4 v4, s[36:39], s61 offen offset:2048 sc1 lds\n" \
"v_mfma_f32_16x16x32_bf16 v[68:71], a[100:103], v[28:31], v[68:71]\n" \
"ds_read_b128 v[16:19], v12 offset:45056\n" \
"v_mfma_f32_16x16x32_bf16 v[72:75], a[228:231], v[24:27], v[72:75]\n" \
"ds_read_b128 v[20:23], v12 offset:61440\n" \
"v_mfma_f32_16x16x32_bf16 v[76:79], a[228:231], v[28:31], v[76:79]\n" \
"s_waitcnt lgkmcnt(4)\n" \
"v_mfma_f32_16x16x32_bf16 v[64:67], a[104:107], v[32:35], v[64:67]\n" \
"buffer_load_dwordx4 v4, s[36:39], s61 offen offset:3072 sc1 lds\n" \
"v_mfma_f32_16x16x32_bf16 v[68:71], a[104:107], v[36:39], v[68:71]\n" \
"ds_read_b128 v[24:27], v12 offset:46080\n" \
"v_mfma_f32_16x16x32_bf16 v[72:75], a[232:235], v[32:35], v[72:75]\n" \
"ds_read_b128 v[28:31], v12 offset:62464\n" \
"v_mfma_f32_16x16x32_bf16 v[76:79], a[232:235], v[36:39], v[76:79]\n" \
"s_waitcnt lgkmcnt(4)\n" \
"v_mfma_f32_16x16x32_bf16 v[64:67], a[108:111], v[40:43], v[64:67]\n" \
"v_mfma_f32_16x16x32_bf16 v[68:71], a[108:111], v[44:47], v[68:71]\n" \
"ds_read_b128 v[32:35], v12 offset:47104\n" \
"v_mfma_f32_16x16x32_bf16 v[72:75], a[236:239], v[40:43], v[72:75]\n" \
"ds_read_b128 v[36:39], v12 offset:63488\n" \
"v_mfma_f32_16x16x32_bf16 v[76:79], a[236:239], v[44:47], v[76:79]\n" \
"s_waitcnt lgkmcnt(4)\n" \
"v_mfma_f32_16x16x32_bf16 v[64:67], a[112:115], v[16:19], v[64:67]\n" \
"v_mfma_f32_16x16x32_bf16 v[68:71], a[112:115], v[20:23], v[68:71]\n" \
"ds_read_b128 v[40:43], v12 offset:48128\n" \
"v_mfma_f32_16x16x32_bf16 v[72:75], a[240:243], v[16:19], v[72:75]\n" \
"ds_read_b128 v[44:47], v12 offset:64512\n" \
"v_mfma_f32_16x16x32_bf16 v[76:79], a[240:243], v[20:23], v[76:79]\n" \
"s_waitcnt lgkmcnt(4)\n" \
"v_mfma_f32_16x16x32_bf16 v[64:67], a[116:119], v[24:27], v[64:67]\n" \
"s_waitcnt vmcnt(8)\n" \
"s_barrier\n" \
"v_mfma_f32_16x16x32_bf16 v[68:71], a[116:119], v[28:31], v[68:71]\n" \
"ds_read_b128 v[16:19], v10 offset:0\n" \
"v_mfma_f32_16x16x32_bf16 v[72:75], a[244:247], v[24:27], v[72:75]\n" \
"ds_read_b128 v[20:23], v10 offset:16384\n" \
"v_mfma_f32_16x16x32_bf16 v[76:79], a[244:247], v[28:31], v[76:79]\n" \
"s_waitcnt lgkmcnt(4)\n" \
"buffer_store_dword v13, v6, s[36:39], 0 offen sc1\n" \
"v_mfma_f32_16x16x32_bf16 v[64:67], a[120:123], v[32:35], v[64:67]\n" \
"v_cmp_gt_u32_e64 vcc, 16, v15\n" \
"v_mfma_f32_16x16x32_bf16 v[68:71], a[120:123], v[36:39], v[68:71]\n" \
"ds_read_b128 v[24:27], v10 offset:1024\n" \
"v_mfma_f32_16x16x32_bf16 v[72:75], a[248:251], v[32:35], v[72:75]\n" \
"ds_read_b128 v[28:31], v10 offset:17408\n" \
"v_mfma_f32_16x16x32_bf16 v[76:79], a[248:251], v[36:39], v[76:79]\n" \
"s_waitcnt lgkmcnt(4)\n" \
"v_mfma_f32_16x16x32_bf16 v[64:67], a[124:127], v[40:43], v[64:67]\n" \
"s_cbranch_vccnz .Lp1qk_m1_%=\n" \
".Lp1qk_m1k_%=:\n" \
"v_mfma_f32_16x16x32_bf16 v[68:71], a[124:127], v[44:47], v[68:71]\n" \
"ds_read_b128 v[32:35], v10 offset:2048\n" \
"v_mfma_f32_16x16x32_bf16 v[72:75], a[252:255], v[40:43], v[72:75]\n" \
"ds_read_b128 v[36:39], v10 offset:18432\n" \
"v_mfma_f32_16x16x32_bf16 v[76:79], a[252:255], v[44:47], v[76:79]\n" \
"s_waitcnt lgkmcnt(4)\n" \
"s_add_u32 s57, s57, s49\n" \
"s_add_u32 s58, s58, s49\n" \
"s_add_u32 s59, s59, 1\n" \
"v_mov_b32_e64 v5, v7\n" \
"v_mov_b32_e64 v6, v11\n" \
"s_cmp_ge_u32 s59, s47\n" \
"s_nop 0\n" \
"s_cbranch_scc0 .Lp1qk_loop_%=\n" \
".Lp1qk_out1_%=:\n" \
"s_nop 7\n" \
"s_nop 7\n" \
"v_mul_f32 v64, s51, v64\n" \
"v_mul_f32 v65, s51, v65\n" \
"v_mul_f32 v66, s51, v66\n" \
"v_mul_f32 v67, s51, v67\n" \
"v_mul_f32 v72, s51, v72\n" \
"v_mul_f32 v73, s51, v73\n" \
"v_mul_f32 v74, s51, v74\n" \
"v_mul_f32 v75, s51, v75\n" \
"v_cvt_pk_bf16_f32 v80, v64, v65\n" \
"v_cvt_pk_bf16_f32 v81, v66, v67\n" \
"v_cvt_pk_bf16_f32 v82, v72, v73\n" \
"v_cvt_pk_bf16_f32 v83, v74, v75\n" \
"buffer_store_dwordx4 v[80:83], v5, s[36:39], s57 offen sc1\n" \
"v_mul_f32 v68, s51, v68\n" \
"v_mul_f32 v69, s51, v69\n" \
"v_mul_f32 v70, s51, v70\n" \
"v_mul_f32 v71, s51, v71\n" \
"v_mul_f32 v76, s51, v76\n" \
"v_mul_f32 v77, s51, v77\n" \
"v_mul_f32 v78, s51, v78\n" \
"v_mul_f32 v79, s51, v79\n" \
"v_cvt_pk_bf16_f32 v84, v68, v69\n" \
"v_cvt_pk_bf16_f32 v85, v70, v71\n" \
"v_cvt_pk_bf16_f32 v86, v76, v77\n" \
"v_cvt_pk_bf16_f32 v87, v78, v79\n" \
"buffer_store_dwordx4 v[84:87], v5, s[36:39], s58 offen sc1\n" \
"s_branch .Lp1qk_end_%=\n" \
".Lp1qk_out0_%=:\n" \
"s_nop 7\n" \
"s_nop 7\n" \
"v_mul_f32 v48, s51, v48\n" \
"v_mul_f32 v49, s51, v49\n" \
"v_mul_f32 v50, s51, v50\n" \
"v_mul_f32 v51, s51, v51\n" \
"v_mul_f32 v56, s51, v56\n" \
"v_mul_f32 v57, s51, v57\n" \
"v_mul_f32 v58, s51, v58\n" \
"v_mul_f32 v59, s51, v59\n" \
"v_cvt_pk_bf16_f32 v80, v48, v49\n" \
"v_cvt_pk_bf16_f32 v81, v50, v51\n" \
"v_cvt_pk_bf16_f32 v82, v56, v57\n" \
"v_cvt_pk_bf16_f32 v83, v58, v59\n" \
"buffer_store_dwordx4 v[80:83], v5, s[36:39], s57 offen sc1\n" \
"v_mul_f32 v52, s51, v52\n" \
"v_mul_f32 v53, s51, v53\n" \
"v_mul_f32 v54, s51, v54\n" \
"v_mul_f32 v55, s51, v55\n" \
"v_mul_f32 v60, s51, v60\n" \
"v_mul_f32 v61, s51, v61\n" \
"v_mul_f32 v62, s51, v62\n" \
"v_mul_f32 v63, s51, v63\n" \
"v_cvt_pk_bf16_f32 v84, v52, v53\n" \
"v_cvt_pk_bf16_f32 v85, v54, v55\n" \
"v_cvt_pk_bf16_f32 v86, v60, v61\n" \
"v_cvt_pk_bf16_f32 v87, v62, v63\n" \
"buffer_store_dwordx4 v[84:87], v5, s[36:39], s58 offen sc1\n" \
".Lp1qk_end_%=:\n" \
"s_waitcnt vmcnt(0) lgkmcnt(0)\n" \
"s_barrier\n" \
"v_mov_b32 v13, s59\n" \
"buffer_store_dword v13, v6, s[36:39], 0 offen sc1\n" \
"s_waitcnt vmcnt(0) lgkmcnt(0)\n" \
"s_branch .Lp1qk_x_%=\n" \
".Lp1qk_l0_%=:\n" \
"s_nop 7\n" \
"s_nop 7\n" \
"s_waitcnt vmcnt(0) lgkmcnt(0)\n" \
"s_mov_b32 s62, 0\n" \
".Lp1qk_l0a_%=:\n" \
"buffer_load_dword v15, v14, s[36:39], s63 offen sc1\n" \
"s_waitcnt vmcnt(0)\n" \
"v_cmp_gt_u32 vcc, 16, v15\n" \
"s_nop 4\n" \
"s_cbranch_vccz .Lp1qk_l0k_%=\n" \
"s_sleep 4\n" \
"s_add_u32 s62, s62, 1\n" \
"s_cmp_lt_u32 s62, 0x10000\n" \
"s_cbranch_scc1 .Lp1qk_l0a_%=\n" \
"v_mov_b32 v15, 256\n" \
"buffer_store_dword v15, v15, s[36:39], 0 offen sc1\n" \
"s_waitcnt vmcnt(0) lgkmcnt(0)\n" \
"s_branch .Lp1qk_l0k_%=\n" \
".Lp1qk_l1_%=:\n" \
"s_nop 7\n" \
"s_nop 7\n" \
"s_waitcnt vmcnt(0) lgkmcnt(0)\n" \
"s_mov_b32 s62, 0\n" \
".Lp1qk_l1a_%=:\n" \
"buffer_load_dword v15, v14, s[36:39], s63 offen sc1\n" \
"s_waitcnt vmcnt(0)\n" \
"v_cmp_gt_u32 vcc, 16, v15\n" \
"s_nop 4\n" \
"s_cbranch_vccz .Lp1qk_l1k_%=\n" \
"s_sleep 4\n" \
"s_add_u32 s62, s62, 1\n" \
"s_cmp_lt_u32 s62, 0x10000\n" \
"s_cbranch_scc1 .Lp1qk_l1a_%=\n" \
"v_mov_b32 v15, 256\n" \
"buffer_store_dword v15, v15, s[36:39], 0 offen sc1\n" \
"s_waitcnt vmcnt(0) lgkmcnt(0)\n" \
"s_branch .Lp1qk_l1k_%=\n" \
".Lp1qk_m0_%=:\n" \
"s_nop 7\n" \
"s_nop 7\n" \
"s_waitcnt vmcnt(0) lgkmcnt(0)\n" \
"s_mov_b32 s62, 0\n" \
".Lp1qk_m0a_%=:\n" \
"buffer_load_dword v15, v14, s[36:39], s63 offen sc1\n" \
"s_waitcnt vmcnt(0)\n" \
"v_cmp_gt_u32 vcc, 16, v15\n" \
"s_nop 4\n" \
"s_cbranch_vccz .Lp1qk_m0k_%=\n" \
"s_sleep 4\n" \
"s_add_u32 s62, s62, 1\n" \
"s_cmp_lt_u32 s62, 0x10000\n" \
"s_cbranch_scc1 .Lp1qk_m0a_%=\n" \
"v_mov_b32 v15, 256\n" \
"buffer_store_dword v15, v15, s[36:39], 0 offen sc1\n" \
"s_waitcnt vmcnt(0) lgkmcnt(0)\n" \
"s_branch .Lp1qk_m0k_%=\n" \
".Lp1qk_m1_%=:\n" \
"s_nop 7\n" \
"s_nop 7\n" \
"s_waitcnt vmcnt(0) lgkmcnt(0)\n" \
"s_mov_b32 s62, 0\n" \
".Lp1qk_m1a_%=:\n" \
"buffer_load_dword v15, v14, s[36:39], s63 offen sc1\n" \
"s_waitcnt vmcnt(0)\n" \
"v_cmp_gt_u32 vcc, 16, v15\n" \
"s_nop 4\n" \
"s_cbranch_vccz .Lp1qk_m1k_%=\n" \
"s_sleep 4\n" \
"s_add_u32 s62, s62, 1\n" \
"s_cmp_lt_u32 s62, 0x10000\n" \
"s_cbranch_scc1 .Lp1qk_m1a_%=\n" \
"v_mov_b32 v15, 256\n" \
"buffer_store_dword v15, v15, s[36:39], 0 offen sc1\n" \
"s_waitcnt vmcnt(0) lgkmcnt(0)\n" \
"s_branch .Lp1qk_m1k_%=\n" \
".Lp1qk_x_%=:\n"
#define ASM_P1_V \
"s_nop 7\n" \
"s_nop 7\n" \
"s_lshr_b32 s52, s48, 1\n" \
"s_lshl_b32 s52, s52, 13\n" \
"s_and_b32 s53, s48, 1\n" \
"s_lshl_b32 s53, s53, 10\n" \
"s_add_u32 s52, s52, s53\n" \
"s_mov_b32 s54, 0\n" \
"s_add_u32 m0, s52, 0\n" \
"s_nop 0\n" \
"buffer_load_dwordx4 v1, s[40:43], s54 offen nt lds\n" \
"s_add_u32 s54, s54, s44\n" \
"s_add_u32 m0, s52, 2048\n" \
"s_nop 0\n" \
"buffer_load_dwordx4 v1, s[40:43], s54 offen nt lds\n" \
"s_add_u32 s54, s54, s44\n" \
"s_add_u32 m0, s52, 4096\n" \
"s_nop 0\n" \
"buffer_load_dwordx4 v1, s[40:43], s54 offen nt lds\n" \
"s_add_u32 s54, s54, s44\n" \
"s_add_u32 m0, s52, 6144\n" \
"s_nop 0\n" \
"buffer_load_dwordx4 v1, s[40:43], s54 offen nt lds\n" \
"s_add_u32 s54, s54, s44\n" \
"s_add_u32 m0, s52, 16384\n" \
"s_nop 0\n" \
"buffer_load_dwordx4 v1, s[40:43], s54 offen nt lds\n" \
"s_add_u32 s54, s54, s44\n" \
"s_add_u32 m0, s52, 18432\n" \
"s_nop 0\n" \
"buffer_load_dwordx4 v1, s[40:43], s54 offen nt lds\n" \
"s_add_u32 s54, s54, s44\n" \
"s_add_u32 m0, s52, 20480\n" \
"s_nop 0\n" \
"buffer_load_dwordx4 v1, s[40:43], s54 offen nt lds\n" \
"s_add_u32 s54, s54, s44\n" \
"s_add_u32 m0, s52, 22528\n" \
"s_nop 0\n" \
"buffer_load_dwordx4 v1, s[40:43], s54 offen nt lds\n" \
"s_add_u32 s54, s54, s44\n" \
"s_add_u32 m0, s52, 32768\n" \
"s_nop 0\n" \
"buffer_load_dwordx4 v1, s[40:43], s54 offen nt lds\n" \
"s_add_u32 s54, s54, s44\n" \
"s_add_u32 m0, s52, 34816\n" \
"s_nop 0\n" \
"buffer_load_dwordx4 v1, s[40:43], s54 offen nt lds\n" \
"s_add_u32 s54, s54, s44\n" \
"s_add_u32 m0, s52, 36864\n" \
"s_nop 0\n" \
"buffer_load_dwordx4 v1, s[40:43], s54 offen nt lds\n" \
"s_add_u32 s54, s54, s44\n" \
"s_add_u32 m0, s52, 38912\n" \
"s_nop 0\n" \
"buffer_load_dwordx4 v1, s[40:43], s54 offen nt lds\n" \
"s_add_u32 s54, s54, s44\n" \
"s_add_u32 m0, s52, 49152\n" \
"s_nop 0\n" \
"buffer_load_dwordx4 v1, s[40:43], s54 offen nt lds\n" \
"s_add_u32 s54, s54, s44\n" \
"s_add_u32 m0, s52, 51200\n" \
"s_nop 0\n" \
"buffer_load_dwordx4 v1, s[40:43], s54 offen nt lds\n" \
"s_add_u32 s54, s54, s44\n" \
"s_add_u32 m0, s52, 53248\n" \
"s_nop 0\n" \
"buffer_load_dwordx4 v1, s[40:43], s54 offen nt lds\n" \
"s_add_u32 s54, s54, s44\n" \
"s_add_u32 m0, s52, 55296\n" \
"s_nop 0\n" \
"buffer_load_dwordx4 v1, s[40:43], s54 offen nt lds\n" \
"s_add_u32 s54, s54, s44\n" \
"s_add_u32 m0, s52, 65536\n" \
"s_nop 0\n" \
"buffer_load_dwordx4 v1, s[40:43], s54 offen nt lds\n" \
"s_add_u32 s54, s54, s44\n" \
"s_add_u32 m0, s52, 67584\n" \
"s_nop 0\n" \
"buffer_load_dwordx4 v1, s[40:43], s54 offen nt lds\n" \
"s_add_u32 s54, s54, s44\n" \
"s_add_u32 m0, s52, 69632\n" \
"s_nop 0\n" \
"buffer_load_dwordx4 v1, s[40:43], s54 offen nt lds\n" \
"s_add_u32 s54, s54, s44\n" \
"s_add_u32 m0, s52, 71680\n" \
"s_nop 0\n" \
"buffer_load_dwordx4 v1, s[40:43], s54 offen nt lds\n" \
"s_add_u32 s54, s54, s44\n" \
"s_add_u32 m0, s52, 81920\n" \
"s_nop 0\n" \
"buffer_load_dwordx4 v1, s[40:43], s54 offen nt lds\n" \
"s_add_u32 s54, s54, s44\n" \
"s_add_u32 m0, s52, 83968\n" \
"s_nop 0\n" \
"buffer_load_dwordx4 v1, s[40:43], s54 offen nt lds\n" \
"s_add_u32 s54, s54, s44\n" \
"s_add_u32 m0, s52, 86016\n" \
"s_nop 0\n" \
"buffer_load_dwordx4 v1, s[40:43], s54 offen nt lds\n" \
"s_add_u32 s54, s54, s44\n" \
"s_add_u32 m0, s52, 88064\n" \
"s_nop 0\n" \
"buffer_load_dwordx4 v1, s[40:43], s54 offen nt lds\n" \
"s_add_u32 s54, s54, s44\n" \
"s_add_u32 m0, s52, 98304\n" \
"s_nop 0\n" \
"buffer_load_dwordx4 v1, s[40:43], s54 offen nt lds\n" \
"s_add_u32 s54, s54, s44\n" \
"s_add_u32 m0, s52, 100352\n" \
"s_nop 0\n" \
"buffer_load_dwordx4 v1, s[40:43], s54 offen nt lds\n" \
"s_add_u32 s54, s54, s44\n" \
"s_add_u32 m0, s52, 102400\n" \
"s_nop 0\n" \
"buffer_load_dwordx4 v1, s[40:43], s54 offen nt lds\n" \
"s_add_u32 s54, s54, s44\n" \
"s_add_u32 m0, s52, 104448\n" \
"s_nop 0\n" \
"buffer_load_dwordx4 v1, s[40:43], s54 offen nt lds\n" \
"s_add_u32 s54, s54, s44\n" \
"s_add_u32 m0, s52, 114688\n" \
"s_nop 0\n" \
"buffer_load_dwordx4 v1, s[40:43], s54 offen nt lds\n" \
"s_add_u32 s54, s54, s44\n" \
"s_add_u32 m0, s52, 116736\n" \
"s_nop 0\n" \
"buffer_load_dwordx4 v1, s[40:43], s54 offen nt lds\n" \
"s_add_u32 s54, s54, s44\n" \
"s_add_u32 m0, s52, 118784\n" \
"s_nop 0\n" \
"buffer_load_dwordx4 v1, s[40:43], s54 offen nt lds\n" \
"s_add_u32 s54, s54, s44\n" \
"s_add_u32 m0, s52, 120832\n" \
"s_nop 0\n" \
"buffer_load_dwordx4 v1, s[40:43], s54 offen nt lds\n" \
"s_add_u32 s54, s54, s44\n" \
"v_add_u32 v8, 0x10000, v2\n" \
"v_add_u32 v9, 0x10000, v3\n" \
"s_waitcnt vmcnt(28)\n" \
"s_barrier\n" \
"ds_read_b64_tr_b16 a[0:1], v2 offset:0\n" \
"ds_read_b64_tr_b16 a[2:3], v2 offset:512\n" \
"ds_read_b64_tr_b16 a[128:129], v3 offset:0\n" \
"ds_read_b64_tr_b16 a[130:131], v3 offset:512\n" \
"ds_read_b64_tr_b16 a[4:5], v2 offset:4096\n" \
"ds_read_b64_tr_b16 a[6:7], v2 offset:4608\n" \
"ds_read_b64_tr_b16 a[132:133], v3 offset:4096\n" \
"ds_read_b64_tr_b16 a[134:135], v3 offset:4608\n" \
"s_waitcnt lgkmcnt(0)\n" \
"s_barrier\n" \
"s_add_u32 m0, s52, 0\n" \
"s_nop 0\n" \
"buffer_load_dwordx4 v1, s[40:43], s54 offen nt lds\n" \
"s_add_u32 s54, s54, s44\n" \
"s_add_u32 m0, s52, 2048\n" \
"s_nop 0\n" \
"buffer_load_dwordx4 v1, s[40:43], s54 offen nt lds\n" \
"s_add_u32 s54, s54, s44\n" \
"s_add_u32 m0, s52, 4096\n" \
"s_nop 0\n" \
"buffer_load_dwordx4 v1, s[40:43], s54 offen nt lds\n" \
"s_add_u32 s54, s54, s44\n" \
"s_add_u32 m0, s52, 6144\n" \
"s_nop 0\n" \
"buffer_load_dwordx4 v1, s[40:43], s54 offen nt lds\n" \
"s_add_u32 s54, s54, s44\n" \
"s_waitcnt vmcnt(28)\n" \
"s_barrier\n" \
"ds_read_b64_tr_b16 a[8:9], v2 offset:16384\n" \
"ds_read_b64_tr_b16 a[10:11], v2 offset:16896\n" \
"ds_read_b64_tr_b16 a[136:137], v3 offset:16384\n" \
"ds_read_b64_tr_b16 a[138:139], v3 offset:16896\n" \
"ds_read_b64_tr_b16 a[12:13], v2 offset:20480\n" \
"ds_read_b64_tr_b16 a[14:15], v2 offset:20992\n" \
"ds_read_b64_tr_b16 a[140:141], v3 offset:20480\n" \
"ds_read_b64_tr_b16 a[142:143], v3 offset:20992\n" \
"s_waitcnt lgkmcnt(0)\n" \
"s_barrier\n" \
"s_add_u32 m0, s52, 16384\n" \
"s_nop 0\n" \
"buffer_load_dwordx4 v1, s[40:43], s54 offen nt lds\n" \
"s_add_u32 s54, s54, s44\n" \
"s_add_u32 m0, s52, 18432\n" \
"s_nop 0\n" \
"buffer_load_dwordx4 v1, s[40:43], s54 offen nt lds\n" \
"s_add_u32 s54, s54, s44\n" \
"s_add_u32 m0, s52, 20480\n" \
"s_nop 0\n" \
"buffer_load_dwordx4 v1, s[40:43], s54 offen nt lds\n" \
"s_add_u32 s54, s54, s44\n" \
"s_add_u32 m0, s52, 22528\n" \
"s_nop 0\n" \
"buffer_load_dwordx4 v1, s[40:43], s54 offen nt lds\n" \
"s_add_u32 s54, s54, s44\n" \
"s_waitcnt vmcnt(28)\n" \
"s_barrier\n" \
"ds_read_b64_tr_b16 a[16:17], v2 offset:32768\n" \
"ds_read_b64_tr_b16 a[18:19], v2 offset:33280\n" \
"ds_read_b64_tr_b16 a[144:145], v3 offset:32768\n" \
"ds_read_b64_tr_b16 a[146:147], v3 offset:33280\n" \
"ds_read_b64_tr_b16 a[20:21], v2 offset:36864\n" \
"ds_read_b64_tr_b16 a[22:23], v2 offset:37376\n" \
"ds_read_b64_tr_b16 a[148:149], v3 offset:36864\n" \
"ds_read_b64_tr_b16 a[150:151], v3 offset:37376\n" \
"s_waitcnt lgkmcnt(0)\n" \
"s_barrier\n" \
"s_add_u32 m0, s52, 32768\n" \
"s_nop 0\n" \
"buffer_load_dwordx4 v1, s[40:43], s54 offen nt lds\n" \
"s_add_u32 s54, s54, s44\n" \
"s_add_u32 m0, s52, 34816\n" \
"s_nop 0\n" \
"buffer_load_dwordx4 v1, s[40:43], s54 offen nt lds\n" \
"s_add_u32 s54, s54, s44\n" \
"s_add_u32 m0, s52, 36864\n" \
"s_nop 0\n" \
"buffer_load_dwordx4 v1, s[40:43], s54 offen nt lds\n" \
"s_add_u32 s54, s54, s44\n" \
"s_add_u32 m0, s52, 38912\n" \
"s_nop 0\n" \
"buffer_load_dwordx4 v1, s[40:43], s54 offen nt lds\n" \
"s_add_u32 s54, s54, s44\n" \
"s_waitcnt vmcnt(28)\n" \
"s_barrier\n" \
"ds_read_b64_tr_b16 a[24:25], v2 offset:49152\n" \
"ds_read_b64_tr_b16 a[26:27], v2 offset:49664\n" \
"ds_read_b64_tr_b16 a[152:153], v3 offset:49152\n" \
"ds_read_b64_tr_b16 a[154:155], v3 offset:49664\n" \
"ds_read_b64_tr_b16 a[28:29], v2 offset:53248\n" \
"ds_read_b64_tr_b16 a[30:31], v2 offset:53760\n" \
"ds_read_b64_tr_b16 a[156:157], v3 offset:53248\n" \
"ds_read_b64_tr_b16 a[158:159], v3 offset:53760\n" \
"s_waitcnt lgkmcnt(0)\n" \
"s_barrier\n" \
"s_add_u32 m0, s52, 49152\n" \
"s_nop 0\n" \
"buffer_load_dwordx4 v1, s[40:43], s54 offen nt lds\n" \
"s_add_u32 s54, s54, s44\n" \
"s_add_u32 m0, s52, 51200\n" \
"s_nop 0\n" \
"buffer_load_dwordx4 v1, s[40:43], s54 offen nt lds\n" \
"s_add_u32 s54, s54, s44\n" \
"s_add_u32 m0, s52, 53248\n" \
"s_nop 0\n" \
"buffer_load_dwordx4 v1, s[40:43], s54 offen nt lds\n" \
"s_add_u32 s54, s54, s44\n" \
"s_add_u32 m0, s52, 55296\n" \
"s_nop 0\n" \
"buffer_load_dwordx4 v1, s[40:43], s54 offen nt lds\n" \
"s_add_u32 s54, s54, s44\n" \
"s_waitcnt vmcnt(28)\n" \
"s_barrier\n" \
"ds_read_b64_tr_b16 a[32:33], v8 offset:0\n" \
"ds_read_b64_tr_b16 a[34:35], v8 offset:512\n" \
"ds_read_b64_tr_b16 a[160:161], v9 offset:0\n" \
"ds_read_b64_tr_b16 a[162:163], v9 offset:512\n" \
"ds_read_b64_tr_b16 a[36:37], v8 offset:4096\n" \
"ds_read_b64_tr_b16 a[38:39], v8 offset:4608\n" \
"ds_read_b64_tr_b16 a[164:165], v9 offset:4096\n" \
"ds_read_b64_tr_b16 a[166:167], v9 offset:4608\n" \
"s_waitcnt lgkmcnt(0)\n" \
"s_barrier\n" \
"s_add_u32 m0, s52, 65536\n" \
"s_nop 0\n" \
"buffer_load_dwordx4 v1, s[40:43], s54 offen nt lds\n" \
"s_add_u32 s54, s54, s44\n" \
"s_add_u32 m0, s52, 67584\n" \
"s_nop 0\n" \
"buffer_load_dwordx4 v1, s[40:43], s54 offen nt lds\n" \
"s_add_u32 s54, s54, s44\n" \
"s_add_u32 m0, s52, 69632\n" \
"s_nop 0\n" \
"buffer_load_dwordx4 v1, s[40:43], s54 offen nt lds\n" \
"s_add_u32 s54, s54, s44\n" \
"s_add_u32 m0, s52, 71680\n" \
"s_nop 0\n" \
"buffer_load_dwordx4 v1, s[40:43], s54 offen nt lds\n" \
"s_add_u32 s54, s54, s44\n" \
"s_waitcnt vmcnt(28)\n" \
"s_barrier\n" \
"ds_read_b64_tr_b16 a[40:41], v8 offset:16384\n" \
"ds_read_b64_tr_b16 a[42:43], v8 offset:16896\n" \
"ds_read_b64_tr_b16 a[168:169], v9 offset:16384\n" \
"ds_read_b64_tr_b16 a[170:171], v9 offset:16896\n" \
"ds_read_b64_tr_b16 a[44:45], v8 offset:20480\n" \
"ds_read_b64_tr_b16 a[46:47], v8 offset:20992\n" \
"ds_read_b64_tr_b16 a[172:173], v9 offset:20480\n" \
"ds_read_b64_tr_b16 a[174:175], v9 offset:20992\n" \
"s_waitcnt lgkmcnt(0)\n" \
"s_barrier\n" \
"s_add_u32 m0, s52, 81920\n" \
"s_nop 0\n" \
"buffer_load_dwordx4 v1, s[40:43], s54 offen nt lds\n" \
"s_add_u32 s54, s54, s44\n" \
"s_add_u32 m0, s52, 83968\n" \
"s_nop 0\n" \
"buffer_load_dwordx4 v1, s[40:43], s54 offen nt lds\n" \
"s_add_u32 s54, s54, s44\n" \
"s_add_u32 m0, s52, 86016\n" \
"s_nop 0\n" \
"buffer_load_dwordx4 v1, s[40:43], s54 offen nt lds\n" \
"s_add_u32 s54, s54, s44\n" \
"s_add_u32 m0, s52, 88064\n" \
"s_nop 0\n" \
"buffer_load_dwordx4 v1, s[40:43], s54 offen nt lds\n" \
"s_add_u32 s54, s54, s44\n" \
"s_waitcnt vmcnt(28)\n" \
"s_barrier\n" \
"ds_read_b64_tr_b16 a[48:49], v8 offset:32768\n" \
"ds_read_b64_tr_b16 a[50:51], v8 offset:33280\n" \
"ds_read_b64_tr_b16 a[176:177], v9 offset:32768\n" \
"ds_read_b64_tr_b16 a[178:179], v9 offset:33280\n" \
"ds_read_b64_tr_b16 a[52:53], v8 offset:36864\n" \
"ds_read_b64_tr_b16 a[54:55], v8 offset:37376\n" \
"ds_read_b64_tr_b16 a[180:181], v9 offset:36864\n" \
"ds_read_b64_tr_b16 a[182:183], v9 offset:37376\n" \
"s_waitcnt lgkmcnt(0)\n" \
"s_barrier\n" \
"s_add_u32 m0, s52, 98304\n" \
"s_nop 0\n" \
"buffer_load_dwordx4 v1, s[40:43], s54 offen nt lds\n" \
"s_add_u32 s54, s54, s44\n" \
"s_add_u32 m0, s52, 100352\n" \
"s_nop 0\n" \
"buffer_load_dwordx4 v1, s[40:43], s54 offen nt lds\n" \
"s_add_u32 s54, s54, s44\n" \
"s_add_u32 m0, s52, 102400\n" \
"s_nop 0\n" \
"buffer_load_dwordx4 v1, s[40:43], s54 offen nt lds\n" \
"s_add_u32 s54, s54, s44\n" \
"s_add_u32 m0, s52, 104448\n" \
"s_nop 0\n" \
"buffer_load_dwordx4 v1, s[40:43], s54 offen nt lds\n" \
"s_add_u32 s54, s54, s44\n" \
"s_waitcnt vmcnt(28)\n" \
"s_barrier\n" \
"ds_read_b64_tr_b16 a[56:57], v8 offset:49152\n" \
"ds_read_b64_tr_b16 a[58:59], v8 offset:49664\n" \
"ds_read_b64_tr_b16 a[184:185], v9 offset:49152\n" \
"ds_read_b64_tr_b16 a[186:187], v9 offset:49664\n" \
"ds_read_b64_tr_b16 a[60:61], v8 offset:53248\n" \
"ds_read_b64_tr_b16 a[62:63], v8 offset:53760\n" \
"ds_read_b64_tr_b16 a[188:189], v9 offset:53248\n" \
"ds_read_b64_tr_b16 a[190:191], v9 offset:53760\n" \
"s_waitcnt lgkmcnt(0)\n" \
"s_barrier\n" \
"s_add_u32 m0, s52, 114688\n" \
"s_nop 0\n" \
"buffer_load_dwordx4 v1, s[40:43], s54 offen nt lds\n" \
"s_add_u32 s54, s54, s44\n" \
"s_add_u32 m0, s52, 116736\n" \
"s_nop 0\n" \
"buffer_load_dwordx4 v1, s[40:43], s54 offen nt lds\n" \
"s_add_u32 s54, s54, s44\n" \
"s_add_u32 m0, s52, 118784\n" \
"s_nop 0\n" \
"buffer_load_dwordx4 v1, s[40:43], s54 offen nt lds\n" \
"s_add_u32 s54, s54, s44\n" \
"s_add_u32 m0, s52, 120832\n" \
"s_nop 0\n" \
"buffer_load_dwordx4 v1, s[40:43], s54 offen nt lds\n" \
"s_add_u32 s54, s54, s44\n" \
"s_waitcnt vmcnt(28)\n" \
"s_barrier\n" \
"ds_read_b64_tr_b16 a[64:65], v2 offset:0\n" \
"ds_read_b64_tr_b16 a[66:67], v2 offset:512\n" \
"ds_read_b64_tr_b16 a[192:193], v3 offset:0\n" \
"ds_read_b64_tr_b16 a[194:195], v3 offset:512\n" \
"ds_read_b64_tr_b16 a[68:69], v2 offset:4096\n" \
"ds_read_b64_tr_b16 a[70:71], v2 offset:4608\n" \
"ds_read_b64_tr_b16 a[196:197], v3 offset:4096\n" \
"ds_read_b64_tr_b16 a[198:199], v3 offset:4608\n" \
"s_waitcnt vmcnt(24)\n" \
"s_barrier\n" \
"ds_read_b64_tr_b16 a[72:73], v2 offset:16384\n" \
"ds_read_b64_tr_b16 a[74:75], v2 offset:16896\n" \
"ds_read_b64_tr_b16 a[200:201], v3 offset:16384\n" \
"ds_read_b64_tr_b16 a[202:203], v3 offset:16896\n" \
"ds_read_b64_tr_b16 a[76:77], v2 offset:20480\n" \
"ds_read_b64_tr_b16 a[78:79], v2 offset:20992\n" \
"ds_read_b64_tr_b16 a[204:205], v3 offset:20480\n" \
"ds_read_b64_tr_b16 a[206:207], v3 offset:20992\n" \
"s_waitcnt vmcnt(20)\n" \
"s_barrier\n" \
"ds_read_b64_tr_b16 a[80:81], v2 offset:32768\n" \
"ds_read_b64_tr_b16 a[82:83], v2 offset:33280\n" \
"ds_read_b64_tr_b16 a[208:209], v3 offset:32768\n" \
"ds_read_b64_tr_b16 a[210:211], v3 offset:33280\n" \
"ds_read_b64_tr_b16 a[84:85], v2 offset:36864\n" \
"ds_read_b64_tr_b16 a[86:87], v2 offset:37376\n" \
"ds_read_b64_tr_b16 a[212:213], v3 offset:36864\n" \
"ds_read_b64_tr_b16 a[214:215], v3 offset:37376\n" \
"s_waitcnt vmcnt(16)\n" \
"s_barrier\n" \
"ds_read_b64_tr_b16 a[88:89], v2 offset:49152\n" \
"ds_read_b64_tr_b16 a[90:91], v2 offset:49664\n" \
"ds_read_b64_tr_b16 a[216:217], v3 offset:49152\n" \
"ds_read_b64_tr_b16 a[218:219], v3 offset:49664\n" \
"ds_read_b64_tr_b16 a[92:93], v2 offset:53248\n" \
"ds_read_b64_tr_b16 a[94:95], v2 offset:53760\n" \
"ds_read_b64_tr_b16 a[220:221], v3 offset:53248\n" \
"ds_read_b64_tr_b16 a[222:223], v3 offset:53760\n" \
"s_waitcnt vmcnt(12)\n" \
"s_barrier\n" \
"ds_read_b64_tr_b16 a[96:97], v8 offset:0\n" \
"ds_read_b64_tr_b16 a[98:99], v8 offset:512\n" \
"ds_read_b64_tr_b16 a[224:225], v9 offset:0\n" \
"ds_read_b64_tr_b16 a[226:227], v9 offset:512\n" \
"ds_read_b64_tr_b16 a[100:101], v8 offset:4096\n" \
"ds_read_b64_tr_b16 a[102:103], v8 offset:4608\n" \
"ds_read_b64_tr_b16 a[228:229], v9 offset:4096\n" \
"ds_read_b64_tr_b16 a[230:231], v9 offset:4608\n" \
"s_waitcnt vmcnt(8)\n" \
"s_barrier\n" \
"ds_read_b64_tr_b16 a[104:105], v8 offset:16384\n" \
"ds_read_b64_tr_b16 a[106:107], v8 offset:16896\n" \
"ds_read_b64_tr_b16 a[232:233], v9 offset:16384\n" \
"ds_read_b64_tr_b16 a[234:235], v9 offset:16896\n" \
"ds_read_b64_tr_b16 a[108:109], v8 offset:20480\n" \
"ds_read_b64_tr_b16 a[110:111], v8 offset:20992\n" \
"ds_read_b64_tr_b16 a[236:237], v9 offset:20480\n" \
"ds_read_b64_tr_b16 a[238:239], v9 offset:20992\n" \
"s_waitcnt vmcnt(4)\n" \
"s_barrier\n" \
"ds_read_b64_tr_b16 a[112:113], v8 offset:32768\n" \
"ds_read_b64_tr_b16 a[114:115], v8 offset:33280\n" \
"ds_read_b64_tr_b16 a[240:241], v9 offset:32768\n" \
"ds_read_b64_tr_b16 a[242:243], v9 offset:33280\n" \
"ds_read_b64_tr_b16 a[116:117], v8 offset:36864\n" \
"ds_read_b64_tr_b16 a[118:119], v8 offset:37376\n" \
"ds_read_b64_tr_b16 a[244:245], v9 offset:36864\n" \
"ds_read_b64_tr_b16 a[246:247], v9 offset:37376\n" \
"s_waitcnt vmcnt(0)\n" \
"s_barrier\n" \
"ds_read_b64_tr_b16 a[120:121], v8 offset:49152\n" \
"ds_read_b64_tr_b16 a[122:123], v8 offset:49664\n" \
"ds_read_b64_tr_b16 a[248:249], v9 offset:49152\n" \
"ds_read_b64_tr_b16 a[250:251], v9 offset:49664\n" \
"ds_read_b64_tr_b16 a[124:125], v8 offset:53248\n" \
"ds_read_b64_tr_b16 a[126:127], v8 offset:53760\n" \
"ds_read_b64_tr_b16 a[252:253], v9 offset:53248\n" \
"ds_read_b64_tr_b16 a[254:255], v9 offset:53760\n" \
"s_waitcnt lgkmcnt(0)\n" \
"s_barrier\n" \
"v_mov_b32 v14, 4096\n" \
"s_mov_b32 s59, s46\n" \
"s_add_u32 s63, s59, 0\n" \
"s_min_u32 s63, s63, 63\n" \
"s_lshr_b32 s63, s63, 3\n" \
"s_lshl_b32 s63, s63, 7\n" \
"s_branch .Lp1v_l0_%=\n" \
".Lp1v_l0k_%=:\n" \
"s_add_u32 s63, s59, 1\n" \
"s_min_u32 s63, s63, 63\n" \
"s_lshr_b32 s63, s63, 3\n" \
"s_lshl_b32 s63, s63, 7\n" \
"s_branch .Lp1v_l1_%=\n" \
".Lp1v_l1k_%=:\n" \
"s_lshl_b32 s55, s48, 13\n" \
"s_lshl_b32 s56, s46, 16\n" \
"s_lshl_b32 s60, s47, 16\n" \
"s_sub_u32 s60, s60, 0x10000\n" \
"s_mul_i32 s57, s46, s49\n" \
"s_sub_u32 s57, s57, s49\n" \
"s_add_u32 s58, s57, s50\n" \
"v_mov_b32 v7, v5\n" \
"v_mov_b32 v5, 0x7ffffff0\n" \
"v_mov_b32 v10, v0\n" \
"v_mov_b32 v11, v6\n" \
"v_mov_b32 v6, 0x7ffffff0\n" \
"s_mov_b32 s59, s46\n" \
"s_min_u32 s61, s56, s60\n" \
"s_add_u32 m0, s55, 0\n" \
"s_nop 0\n" \
"buffer_load_dwordx4 v4, s[36:39], s61 offen offset:0 sc1 lds\n" \
"buffer_load_dwordx4 v4, s[36:39], s61 offen offset:1024 sc1 lds\n" \
"buffer_load_dwordx4 v4, s[36:39], s61 offen offset:2048 sc1 lds\n" \
"buffer_load_dwordx4 v4, s[36:39], s61 offen offset:3072 sc1 lds\n" \
"s_add_u32 m0, s55, 4096\n" \
"s_add_u32 s61, s61, 4096\n" \
"buffer_load_dwordx4 v4, s[36:39], s61 offen offset:0 sc1 lds\n" \
"buffer_load_dwordx4 v4, s[36:39], s61 offen offset:1024 sc1 lds\n" \
"buffer_load_dwordx4 v4, s[36:39], s61 offen offset:2048 sc1 lds\n" \
"buffer_load_dwordx4 v4, s[36:39], s61 offen offset:3072 sc1 lds\n" \
"s_min_u32 s61, s56, s60\n" \
"s_add_u32 s61, s61, 16384\n" \
"s_add_u32 s56, s56, 0x10000\n" \
"s_add_u32 m0, s55, 32768\n" \
"s_nop 0\n" \
"buffer_load_dwordx4 v4, s[36:39], s61 offen offset:0 sc1 lds\n" \
"buffer_load_dwordx4 v4, s[36:39], s61 offen offset:1024 sc1 lds\n" \
"buffer_load_dwordx4 v4, s[36:39], s61 offen offset:2048 sc1 lds\n" \
"buffer_load_dwordx4 v4, s[36:39], s61 offen offset:3072 sc1 lds\n" \
"s_add_u32 m0, s55, 36864\n" \
"s_add_u32 s61, s61, 4096\n" \
"buffer_load_dwordx4 v4, s[36:39], s61 offen offset:0 sc1 lds\n" \
"buffer_load_dwordx4 v4, s[36:39], s61 offen offset:1024 sc1 lds\n" \
"buffer_load_dwordx4 v4, s[36:39], s61 offen offset:2048 sc1 lds\n" \
"buffer_load_dwordx4 v4, s[36:39], s61 offen offset:3072 sc1 lds\n" \
"s_mov_b32 s70, 0\n" \
"s_mov_b32 s72, 0\n" \
"s_waitcnt vmcnt(0) lgkmcnt(0)\n" \
"s_barrier\n" \
"v_add_u32 v12, 0x10000, v10\n" \
"ds_read_b128 v[16:19], v10 offset:0\n" \
"ds_read_b128 v[20:23], v10 offset:16384\n" \
"ds_read_b128 v[24:27], v10 offset:1024\n" \
"ds_read_b128 v[28:31], v10 offset:17408\n" \
"ds_read_b128 v[32:35], v10 offset:2048\n" \
"ds_read_b128 v[36:39], v10 offset:18432\n" \
"s_waitcnt lgkmcnt(4)\n" \
"s_nop 7\n" \
"s_nop 7\n" \
".p2align 6\n" \
".Lp1v_loop_%=:\n" \
"v_mfma_f32_16x16x32_bf16 v[48:51], v[16:19], a[0:3], 0\n" \
"s_min_u32 s61, s56, s60\n" \
"s_add_u32 m0, s55, 65536\n" \
"s_nop 0\n" \
"buffer_load_dwordx4 v4, s[36:39], s61 offen offset:0 sc1 lds\n" \
"v_mov_b32_e64 v13, s59\n" \
"v_mfma_f32_16x16x32_bf16 v[52:55], v[20:23], a[0:3], 0\n" \
"ds_read_b128 v[40:43], v10 offset:3072\n" \
"s_nop 0\n" \
"v_mfma_f32_16x16x32_bf16 v[56:59], v[16:19], a[128:131], 0\n" \
"ds_read_b128 v[44:47], v10 offset:19456\n" \
"s_nop 0\n" \
"v_mfma_f32_16x16x32_bf16 v[60:63], v[20:23], a[128:131], 0\n" \
"s_waitcnt lgkmcnt(4)\n" \
"v_cvt_pk_bf16_f32 v80, v64, v65\n" \
"v_cvt_pk_bf16_f32 v81, v66, v67\n" \
"v_mfma_f32_16x16x32_bf16 v[48:51], v[24:27], a[4:7], v[48:51]\n" \
"buffer_load_dwordx4 v4, s[36:39], s61 offen offset:1024 sc1 lds\n" \
"v_mfma_f32_16x16x32_bf16 v[52:55], v[28:31], a[4:7], v[52:55]\n" \
"ds_read_b128 v[16:19], v10 offset:4096\n" \
"v_cvt_pk_bf16_f32 v82, v68, v69\n" \
"v_mfma_f32_16x16x32_bf16 v[56:59], v[24:27], a[132:135], v[56:59]\n" \
"ds_read_b128 v[20:23], v10 offset:20480\n" \
"v_cvt_pk_bf16_f32 v83, v70, v71\n" \
"v_mfma_f32_16x16x32_bf16 v[60:63], v[28:31], a[132:135], v[60:63]\n" \
"s_waitcnt lgkmcnt(4)\n" \
"buffer_store_dwordx4 v[80:83], v5, s[36:39], s57 offen sc1\n" \
"v_cvt_pk_bf16_f32 v84, v72, v73\n" \
"v_mfma_f32_16x16x32_bf16 v[48:51], v[32:35], a[8:11], v[48:51]\n" \
"s_add_u32 s63, s59, 2\n" \
"s_min_u32 s63, s63, 63\n" \
"v_mfma_f32_16x16x32_bf16 v[52:55], v[36:39], a[8:11], v[52:55]\n" \
"ds_read_b128 v[24:27], v10 offset:5120\n" \
"s_lshr_b32 s63, s63, 3\n" \
"s_lshl_b32 s63, s63, 7\n" \
"v_cvt_pk_bf16_f32 v85, v74, v75\n" \
"v_mfma_f32_16x16x32_bf16 v[56:59], v[32:35], a[136:139], v[56:59]\n" \
"ds_read_b128 v[28:31], v10 offset:21504\n" \
"v_cvt_pk_bf16_f32 v86, v76, v77\n" \
"v_mfma_f32_16x16x32_bf16 v[60:63], v[36:39], a[136:139], v[60:63]\n" \
"s_waitcnt lgkmcnt(4)\n" \
"v_cvt_pk_bf16_f32 v87, v78, v79\n" \
"buffer_store_dwordx4 v[84:87], v5, s[36:39], s58 offen sc1\n" \
"v_mfma_f32_16x16x32_bf16 v[48:51], v[40:43], a[12:15], v[48:51]\n" \
"buffer_load_dwordx4 v4, s[36:39], s61 offen offset:2048 sc1 lds\n" \
"v_mfma_f32_16x16x32_bf16 v[52:55], v[44:47], a[12:15], v[52:55]\n" \
"ds_read_b128 v[32:35], v10 offset:6144\n" \
"v_mfma_f32_16x16x32_bf16 v[56:59], v[40:43], a[140:143], v[56:59]\n" \
"ds_read_b128 v[36:39], v10 offset:22528\n" \
"v_mfma_f32_16x16x32_bf16 v[60:63], v[44:47], a[140:143], v[60:63]\n" \
"s_waitcnt lgkmcnt(4)\n" \
"v_mfma_f32_16x16x32_bf16 v[48:51], v[16:19], a[16:19], v[48:51]\n" \
"buffer_load_dwordx4 v4, s[36:39], s61 offen offset:3072 sc1 lds\n" \
"v_mfma_f32_16x16x32_bf16 v[52:55], v[20:23], a[16:19], v[52:55]\n" \
"ds_read_b128 v[40:43], v10 offset:7168\n" \
"v_mfma_f32_16x16x32_bf16 v[56:59], v[16:19], a[144:147], v[56:59]\n" \
"ds_read_b128 v[44:47], v10 offset:23552\n" \
"v_mfma_f32_16x16x32_bf16 v[60:63], v[20:23], a[144:147], v[60:63]\n" \
"s_waitcnt lgkmcnt(4)\n" \
"v_mfma_f32_16x16x32_bf16 v[48:51], v[24:27], a[20:23], v[48:51]\n" \
"buffer_load_dword v15, v14, s[36:39], s63 offen sc1\n" \
"v_mfma_f32_16x16x32_bf16 v[52:55], v[28:31], a[20:23], v[52:55]\n" \
"ds_read_b128 v[16:19], v10 offset:8192\n" \
"v_mfma_f32_16x16x32_bf16 v[56:59], v[24:27], a[148:151], v[56:59]\n" \
"ds_read_b128 v[20:23], v10 offset:24576\n" \
"v_mfma_f32_16x16x32_bf16 v[60:63], v[28:31], a[148:151], v[60:63]\n" \
"s_waitcnt lgkmcnt(4)\n" \
"v_mfma_f32_16x16x32_bf16 v[48:51], v[32:35], a[24:27], v[48:51]\n" \
"s_add_u32 m0, s55, 69632\n" \
"s_add_u32 s61, s61, 4096\n" \
"buffer_load_dwordx4 v4, s[36:39], s61 offen offset:0 sc1 lds\n" \
"v_mfma_f32_16x16x32_bf16 v[52:55], v[36:39], a[24:27], v[52:55]\n" \
"ds_read_b128 v[24:27], v10 offset:9216\n" \
"v_mfma_f32_16x16x32_bf16 v[56:59], v[32:35], a[152:155], v[56:59]\n" \
"ds_read_b128 v[28:31], v10 offset:25600\n" \
"v_mfma_f32_16x16x32_bf16 v[60:63], v[36:39], a[152:155], v[60:63]\n" \
"s_waitcnt lgkmcnt(4)\n" \
"v_mfma_f32_16x16x32_bf16 v[48:51], v[40:43], a[28:31], v[48:51]\n" \
"buffer_load_dwordx4 v4, s[36:39], s61 offen offset:1024 sc1 lds\n" \
"v_mfma_f32_16x16x32_bf16 v[52:55], v[44:47], a[28:31], v[52:55]\n" \
"ds_read_b128 v[32:35], v10 offset:10240\n" \
"v_mfma_f32_16x16x32_bf16 v[56:59], v[40:43], a[156:159], v[56:59]\n" \
"ds_read_b128 v[36:39], v10 offset:26624\n" \
"v_mfma_f32_16x16x32_bf16 v[60:63], v[44:47], a[156:159], v[60:63]\n" \
"s_waitcnt lgkmcnt(4)\n" \
"v_mfma_f32_16x16x32_bf16 v[48:51], v[16:19], a[32:35], v[48:51]\n" \
"v_mfma_f32_16x16x32_bf16 v[52:55], v[20:23], a[32:35], v[52:55]\n" \
"ds_read_b128 v[40:43], v10 offset:11264\n" \
"v_mfma_f32_16x16x32_bf16 v[56:59], v[16:19], a[160:163], v[56:59]\n" \
"ds_read_b128 v[44:47], v10 offset:27648\n" \
"v_mfma_f32_16x16x32_bf16 v[60:63], v[20:23], a[160:163], v[60:63]\n" \
"s_waitcnt lgkmcnt(4)\n" \
"v_mfma_f32_16x16x32_bf16 v[48:51], v[24:27], a[36:39], v[48:51]\n" \
"buffer_load_dwordx4 v4, s[36:39], s61 offen offset:2048 sc1 lds\n" \
"v_mfma_f32_16x16x32_bf16 v[52:55], v[28:31], a[36:39], v[52:55]\n" \
"ds_read_b128 v[16:19], v10 offset:12288\n" \
"v_mfma_f32_16x16x32_bf16 v[56:59], v[24:27], a[164:167], v[56:59]\n" \
"ds_read_b128 v[20:23], v10 offset:28672\n" \
"v_mfma_f32_16x16x32_bf16 v[60:63], v[28:31], a[164:167], v[60:63]\n" \
"s_waitcnt lgkmcnt(4)\n" \
"v_mfma_f32_16x16x32_bf16 v[48:51], v[32:35], a[40:43], v[48:51]\n" \
"buffer_load_dwordx4 v4, s[36:39], s61 offen offset:3072 sc1 lds\n" \
"v_mfma_f32_16x16x32_bf16 v[52:55], v[36:39], a[40:43], v[52:55]\n" \
"ds_read_b128 v[24:27], v10 offset:13312\n" \
"v_mfma_f32_16x16x32_bf16 v[56:59], v[32:35], a[168:171], v[56:59]\n" \
"ds_read_b128 v[28:31], v10 offset:29696\n" \
"v_mfma_f32_16x16x32_bf16 v[60:63], v[36:39], a[168:171], v[60:63]\n" \
"s_waitcnt lgkmcnt(4)\n" \
"v_mfma_f32_16x16x32_bf16 v[48:51], v[40:43], a[44:47], v[48:51]\n" \
"v_mfma_f32_16x16x32_bf16 v[52:55], v[44:47], a[44:47], v[52:55]\n" \
"ds_read_b128 v[32:35], v10 offset:14336\n" \
"v_mfma_f32_16x16x32_bf16 v[56:59], v[40:43], a[172:175], v[56:59]\n" \
"ds_read_b128 v[36:39], v10 offset:30720\n" \
"v_mfma_f32_16x16x32_bf16 v[60:63], v[44:47], a[172:175], v[60:63]\n" \
"s_waitcnt lgkmcnt(4)\n" \
"v_mfma_f32_16x16x32_bf16 v[48:51], v[16:19], a[48:51], v[48:51]\n" \
"v_mfma_f32_16x16x32_bf16 v[52:55], v[20:23], a[48:51], v[52:55]\n" \
"ds_read_b128 v[40:43], v10 offset:15360\n" \
"v_mfma_f32_16x16x32_bf16 v[56:59], v[16:19], a[176:179], v[56:59]\n" \
"ds_read_b128 v[44:47], v10 offset:31744\n" \
"v_mfma_f32_16x16x32_bf16 v[60:63], v[20:23], a[176:179], v[60:63]\n" \
"s_waitcnt lgkmcnt(4)\n" \
"v_mfma_f32_16x16x32_bf16 v[48:51], v[24:27], a[52:55], v[48:51]\n" \
"s_waitcnt vmcnt(12)\n" \
"s_barrier\n" \
"v_mfma_f32_16x16x32_bf16 v[52:55], v[28:31], a[52:55], v[52:55]\n" \
"ds_read_b128 v[16:19], v10 offset:32768\n" \
"v_mfma_f32_16x16x32_bf16 v[56:59], v[24:27], a[180:183], v[56:59]\n" \
"ds_read_b128 v[20:23], v10 offset:49152\n" \
"v_mfma_f32_16x16x32_bf16 v[60:63], v[28:31], a[180:183], v[60:63]\n" \
"s_waitcnt lgkmcnt(4)\n" \
"v_mfma_f32_16x16x32_bf16 v[48:51], v[32:35], a[56:59], v[48:51]\n" \
"v_mfma_f32_16x16x32_bf16 v[52:55], v[36:39], a[56:59], v[52:55]\n" \
"ds_read_b128 v[24:27], v10 offset:33792\n" \
"v_mfma_f32_16x16x32_bf16 v[56:59], v[32:35], a[184:187], v[56:59]\n" \
"ds_read_b128 v[28:31], v10 offset:50176\n" \
"v_mfma_f32_16x16x32_bf16 v[60:63], v[36:39], a[184:187], v[60:63]\n" \
"s_waitcnt lgkmcnt(4)\n" \
"v_mfma_f32_16x16x32_bf16 v[48:51], v[40:43], a[60:63], v[48:51]\n" \
"v_mfma_f32_16x16x32_bf16 v[52:55], v[44:47], a[60:63], v[52:55]\n" \
"ds_read_b128 v[32:35], v10 offset:34816\n" \
"v_mfma_f32_16x16x32_bf16 v[56:59], v[40:43], a[188:191], v[56:59]\n" \
"ds_read_b128 v[36:39], v10 offset:51200\n" \
"v_mfma_f32_16x16x32_bf16 v[60:63], v[44:47], a[188:191], v[60:63]\n" \
"s_waitcnt lgkmcnt(4)\n" \
"v_mfma_f32_16x16x32_bf16 v[48:51], v[16:19], a[64:67], v[48:51]\n" \
"s_min_u32 s61, s56, s60\n" \
"s_add_u32 s61, s61, 16384\n" \
"s_add_u32 s56, s56, 0x10000\n" \
"s_add_u32 m0, s55, 98304\n" \
"s_nop 0\n" \
"buffer_load_dwordx4 v4, s[36:39], s61 offen offset:0 sc1 lds\n" \
"v_mfma_f32_16x16x32_bf16 v[52:55], v[20:23], a[64:67], v[52:55]\n" \
"ds_read_b128 v[40:43], v10 offset:35840\n" \
"v_mfma_f32_16x16x32_bf16 v[56:59], v[16:19], a[192:195], v[56:59]\n" \
"ds_read_b128 v[44:47], v10 offset:52224\n" \
"v_mfma_f32_16x16x32_bf16 v[60:63], v[20:23], a[192:195], v[60:63]\n" \
"s_waitcnt lgkmcnt(4)\n" \
"v_mfma_f32_16x16x32_bf16 v[48:51], v[24:27], a[68:71], v[48:51]\n" \
"buffer_load_dwordx4 v4, s[36:39], s61 offen offset:1024 sc1 lds\n" \
"v_mfma_f32_16x16x32_bf16 v[52:55], v[28:31], a[68:71], v[52:55]\n" \
"ds_read_b128 v[16:19], v10 offset:36864\n" \
"v_mfma_f32_16x16x32_bf16 v[56:59], v[24:27], a[196:199], v[56:59]\n" \
"ds_read_b128 v[20:23], v10 offset:53248\n" \
"v_mfma_f32_16x16x32_bf16 v[60:63], v[28:31], a[196:199], v[60:63]\n" \
"s_waitcnt lgkmcnt(4)\n" \
"v_mfma_f32_16x16x32_bf16 v[48:51], v[32:35], a[72:75], v[48:51]\n" \
"v_mfma_f32_16x16x32_bf16 v[52:55], v[36:39], a[72:75], v[52:55]\n" \
"ds_read_b128 v[24:27], v10 offset:37888\n" \
"v_mfma_f32_16x16x32_bf16 v[56:59], v[32:35], a[200:203], v[56:59]\n" \
"ds_read_b128 v[28:31], v10 offset:54272\n" \
"v_mfma_f32_16x16x32_bf16 v[60:63], v[36:39], a[200:203], v[60:63]\n" \
"s_waitcnt lgkmcnt(4)\n" \
"v_mfma_f32_16x16x32_bf16 v[48:51], v[40:43], a[76:79], v[48:51]\n" \
"buffer_load_dwordx4 v4, s[36:39], s61 offen offset:2048 sc1 lds\n" \
"v_mfma_f32_16x16x32_bf16 v[52:55], v[44:47], a[76:79], v[52:55]\n" \
"ds_read_b128 v[32:35], v10 offset:38912\n" \
"v_mfma_f32_16x16x32_bf16 v[56:59], v[40:43], a[204:207], v[56:59]\n" \
"ds_read_b128 v[36:39], v10 offset:55296\n" \
"v_mfma_f32_16x16x32_bf16 v[60:63], v[44:47], a[204:207], v[60:63]\n" \
"s_waitcnt lgkmcnt(4)\n" \
"v_mfma_f32_16x16x32_bf16 v[48:51], v[16:19], a[80:83], v[48:51]\n" \
"buffer_load_dwordx4 v4, s[36:39], s61 offen offset:3072 sc1 lds\n" \
"v_mfma_f32_16x16x32_bf16 v[52:55], v[20:23], a[80:83], v[52:55]\n" \
"ds_read_b128 v[40:43], v10 offset:39936\n" \
"v_mfma_f32_16x16x32_bf16 v[56:59], v[16:19], a[208:211], v[56:59]\n" \
"ds_read_b128 v[44:47], v10 offset:56320\n" \
"v_mfma_f32_16x16x32_bf16 v[60:63], v[20:23], a[208:211], v[60:63]\n" \
"s_waitcnt lgkmcnt(4)\n" \
"v_mfma_f32_16x16x32_bf16 v[48:51], v[24:27], a[84:87], v[48:51]\n" \
"v_mfma_f32_16x16x32_bf16 v[52:55], v[28:31], a[84:87], v[52:55]\n" \
"ds_read_b128 v[16:19], v10 offset:40960\n" \
"v_mfma_f32_16x16x32_bf16 v[56:59], v[24:27], a[212:215], v[56:59]\n" \
"ds_read_b128 v[20:23], v10 offset:57344\n" \
"v_mfma_f32_16x16x32_bf16 v[60:63], v[28:31], a[212:215], v[60:63]\n" \
"s_waitcnt lgkmcnt(4)\n" \
"v_mfma_f32_16x16x32_bf16 v[48:51], v[32:35], a[88:91], v[48:51]\n" \
"s_add_u32 m0, s55, 102400\n" \
"s_add_u32 s61, s61, 4096\n" \
"buffer_load_dwordx4 v4, s[36:39], s61 offen offset:0 sc1 lds\n" \
"v_mfma_f32_16x16x32_bf16 v[52:55], v[36:39], a[88:91], v[52:55]\n" \
"ds_read_b128 v[24:27], v10 offset:41984\n" \
"v_mfma_f32_16x16x32_bf16 v[56:59], v[32:35], a[216:219], v[56:59]\n" \
"ds_read_b128 v[28:31], v10 offset:58368\n" \
"v_mfma_f32_16x16x32_bf16 v[60:63], v[36:39], a[216:219], v[60:63]\n" \
"s_waitcnt lgkmcnt(4)\n" \
"v_mfma_f32_16x16x32_bf16 v[48:51], v[40:43], a[92:95], v[48:51]\n" \
"buffer_load_dwordx4 v4, s[36:39], s61 offen offset:1024 sc1 lds\n" \
"v_mfma_f32_16x16x32_bf16 v[52:55], v[44:47], a[92:95], v[52:55]\n" \
"ds_read_b128 v[32:35], v10 offset:43008\n" \
"v_mfma_f32_16x16x32_bf16 v[56:59], v[40:43], a[220:223], v[56:59]\n" \
"ds_read_b128 v[36:39], v10 offset:59392\n" \
"v_mfma_f32_16x16x32_bf16 v[60:63], v[44:47], a[220:223], v[60:63]\n" \
"s_waitcnt lgkmcnt(4)\n" \
"v_mfma_f32_16x16x32_bf16 v[48:51], v[16:19], a[96:99], v[48:51]\n" \
"v_mfma_f32_16x16x32_bf16 v[52:55], v[20:23], a[96:99], v[52:55]\n" \
"ds_read_b128 v[40:43], v10 offset:44032\n" \
"v_mfma_f32_16x16x32_bf16 v[56:59], v[16:19], a[224:227], v[56:59]\n" \
"ds_read_b128 v[44:47], v10 offset:60416\n" \
"v_mfma_f32_16x16x32_bf16 v[60:63], v[20:23], a[224:227], v[60:63]\n" \
"s_waitcnt lgkmcnt(4)\n" \
"v_mfma_f32_16x16x32_bf16 v[48:51], v[24:27], a[100:103], v[48:51]\n" \
"buffer_load_dwordx4 v4, s[36:39], s61 offen offset:2048 sc1 lds\n" \
"v_mfma_f32_16x16x32_bf16 v[52:55], v[28:31], a[100:103], v[52:55]\n" \
"ds_read_b128 v[16:19], v10 offset:45056\n" \
"v_mfma_f32_16x16x32_bf16 v[56:59], v[24:27], a[228:231], v[56:59]\n" \
"ds_read_b128 v[20:23], v10 offset:61440\n" \
"v_mfma_f32_16x16x32_bf16 v[60:63], v[28:31], a[228:231], v[60:63]\n" \
"s_waitcnt lgkmcnt(4)\n" \
"v_mfma_f32_16x16x32_bf16 v[48:51], v[32:35], a[104:107], v[48:51]\n" \
"buffer_load_dwordx4 v4, s[36:39], s61 offen offset:3072 sc1 lds\n" \
"v_mfma_f32_16x16x32_bf16 v[52:55], v[36:39], a[104:107], v[52:55]\n" \
"ds_read_b128 v[24:27], v10 offset:46080\n" \
"v_mfma_f32_16x16x32_bf16 v[56:59], v[32:35], a[232:235], v[56:59]\n" \
"ds_read_b128 v[28:31], v10 offset:62464\n" \
"v_mfma_f32_16x16x32_bf16 v[60:63], v[36:39], a[232:235], v[60:63]\n" \
"s_waitcnt lgkmcnt(4)\n" \
"v_mfma_f32_16x16x32_bf16 v[48:51], v[40:43], a[108:111], v[48:51]\n" \
"v_mfma_f32_16x16x32_bf16 v[52:55], v[44:47], a[108:111], v[52:55]\n" \
"ds_read_b128 v[32:35], v10 offset:47104\n" \
"v_mfma_f32_16x16x32_bf16 v[56:59], v[40:43], a[236:239], v[56:59]\n" \
"ds_read_b128 v[36:39], v10 offset:63488\n" \
"v_mfma_f32_16x16x32_bf16 v[60:63], v[44:47], a[236:239], v[60:63]\n" \
"s_waitcnt lgkmcnt(4)\n" \
"v_mfma_f32_16x16x32_bf16 v[48:51], v[16:19], a[112:115], v[48:51]\n" \
"v_mfma_f32_16x16x32_bf16 v[52:55], v[20:23], a[112:115], v[52:55]\n" \
"ds_read_b128 v[40:43], v10 offset:48128\n" \
"v_mfma_f32_16x16x32_bf16 v[56:59], v[16:19], a[240:243], v[56:59]\n" \
"ds_read_b128 v[44:47], v10 offset:64512\n" \
"v_mfma_f32_16x16x32_bf16 v[60:63], v[20:23], a[240:243], v[60:63]\n" \
"s_waitcnt lgkmcnt(4)\n" \
"v_mfma_f32_16x16x32_bf16 v[48:51], v[24:27], a[116:119], v[48:51]\n" \
"s_waitcnt vmcnt(8)\n" \
"s_barrier\n" \
"v_mfma_f32_16x16x32_bf16 v[52:55], v[28:31], a[116:119], v[52:55]\n" \
"ds_read_b128 v[16:19], v12 offset:0\n" \
"v_mfma_f32_16x16x32_bf16 v[56:59], v[24:27], a[244:247], v[56:59]\n" \
"ds_read_b128 v[20:23], v12 offset:16384\n" \
"v_mfma_f32_16x16x32_bf16 v[60:63], v[28:31], a[244:247], v[60:63]\n" \
"s_waitcnt lgkmcnt(4)\n" \
"buffer_store_dword v13, v6, s[36:39], 0 offen sc1\n" \
"v_mfma_f32_16x16x32_bf16 v[48:51], v[32:35], a[120:123], v[48:51]\n" \
"v_cmp_gt_u32_e64 vcc, 16, v15\n" \
"v_mfma_f32_16x16x32_bf16 v[52:55], v[36:39], a[120:123], v[52:55]\n" \
"ds_read_b128 v[24:27], v12 offset:1024\n" \
"v_mfma_f32_16x16x32_bf16 v[56:59], v[32:35], a[248:251], v[56:59]\n" \
"ds_read_b128 v[28:31], v12 offset:17408\n" \
"v_mfma_f32_16x16x32_bf16 v[60:63], v[36:39], a[248:251], v[60:63]\n" \
"s_waitcnt lgkmcnt(4)\n" \
"v_mfma_f32_16x16x32_bf16 v[48:51], v[40:43], a[124:127], v[48:51]\n" \
"s_nop 0\n" \
"s_cbranch_vccnz .Lp1v_m0_%=\n" \
".Lp1v_m0k_%=:\n" \
"v_mfma_f32_16x16x32_bf16 v[52:55], v[44:47], a[124:127], v[52:55]\n" \
"ds_read_b128 v[32:35], v12 offset:2048\n" \
"v_mfma_f32_16x16x32_bf16 v[56:59], v[40:43], a[252:255], v[56:59]\n" \
"ds_read_b128 v[36:39], v12 offset:18432\n" \
"v_mfma_f32_16x16x32_bf16 v[60:63], v[44:47], a[252:255], v[60:63]\n" \
"s_waitcnt lgkmcnt(4)\n" \
"s_add_u32 s57, s57, s49\n" \
"s_add_u32 s58, s58, s49\n" \
"s_add_u32 s59, s59, 1\n" \
"v_mov_b32 v5, v7\n" \
"v_mov_b32 v6, v11\n" \
"s_cmp_ge_u32 s59, s47\n" \
"s_cbranch_scc1 .Lp1v_out0_%=\n" \
"v_mfma_f32_16x16x32_bf16 v[64:67], v[16:19], a[0:3], 0\n" \
"s_min_u32 s61, s56, s60\n" \
"s_add_u32 m0, s55, 0\n" \
"s_nop 0\n" \
"buffer_load_dwordx4 v4, s[36:39], s61 offen offset:0 sc1 lds\n" \
"v_mov_b32_e64 v13, s59\n" \
"v_mfma_f32_16x16x32_bf16 v[68:71], v[20:23], a[0:3], 0\n" \
"ds_read_b128 v[40:43], v12 offset:3072\n" \
"s_nop 0\n" \
"v_mfma_f32_16x16x32_bf16 v[72:75], v[16:19], a[128:131], 0\n" \
"ds_read_b128 v[44:47], v12 offset:19456\n" \
"s_nop 0\n" \
"v_mfma_f32_16x16x32_bf16 v[76:79], v[20:23], a[128:131], 0\n" \
"s_waitcnt lgkmcnt(4)\n" \
"v_cvt_pk_bf16_f32 v80, v48, v49\n" \
"v_cvt_pk_bf16_f32 v81, v50, v51\n" \
"v_mfma_f32_16x16x32_bf16 v[64:67], v[24:27], a[4:7], v[64:67]\n" \
"buffer_load_dwordx4 v4, s[36:39], s61 offen offset:1024 sc1 lds\n" \
"v_mfma_f32_16x16x32_bf16 v[68:71], v[28:31], a[4:7], v[68:71]\n" \
"ds_read_b128 v[16:19], v12 offset:4096\n" \
"v_cvt_pk_bf16_f32 v82, v52, v53\n" \
"v_mfma_f32_16x16x32_bf16 v[72:75], v[24:27], a[132:135], v[72:75]\n" \
"ds_read_b128 v[20:23], v12 offset:20480\n" \
"v_cvt_pk_bf16_f32 v83, v54, v55\n" \
"v_mfma_f32_16x16x32_bf16 v[76:79], v[28:31], a[132:135], v[76:79]\n" \
"s_waitcnt lgkmcnt(4)\n" \
"buffer_store_dwordx4 v[80:83], v5, s[36:39], s57 offen sc1\n" \
"v_cvt_pk_bf16_f32 v84, v56, v57\n" \
"v_mfma_f32_16x16x32_bf16 v[64:67], v[32:35], a[8:11], v[64:67]\n" \
"s_add_u32 s63, s59, 2\n" \
"s_min_u32 s63, s63, 63\n" \
"v_mfma_f32_16x16x32_bf16 v[68:71], v[36:39], a[8:11], v[68:71]\n" \
"ds_read_b128 v[24:27], v12 offset:5120\n" \
"s_lshr_b32 s63, s63, 3\n" \
"s_lshl_b32 s63, s63, 7\n" \
"v_cvt_pk_bf16_f32 v85, v58, v59\n" \
"v_mfma_f32_16x16x32_bf16 v[72:75], v[32:35], a[136:139], v[72:75]\n" \
"ds_read_b128 v[28:31], v12 offset:21504\n" \
"v_cvt_pk_bf16_f32 v86, v60, v61\n" \
"v_mfma_f32_16x16x32_bf16 v[76:79], v[36:39], a[136:139], v[76:79]\n" \
"s_waitcnt lgkmcnt(4)\n" \
"v_cvt_pk_bf16_f32 v87, v62, v63\n" \
"buffer_store_dwordx4 v[84:87], v5, s[36:39], s58 offen sc1\n" \
"v_mfma_f32_16x16x32_bf16 v[64:67], v[40:43], a[12:15], v[64:67]\n" \
"buffer_load_dwordx4 v4, s[36:39], s61 offen offset:2048 sc1 lds\n" \
"v_mfma_f32_16x16x32_bf16 v[68:71], v[44:47], a[12:15], v[68:71]\n" \
"ds_read_b128 v[32:35], v12 offset:6144\n" \
"v_mfma_f32_16x16x32_bf16 v[72:75], v[40:43], a[140:143], v[72:75]\n" \
"ds_read_b128 v[36:39], v12 offset:22528\n" \
"v_mfma_f32_16x16x32_bf16 v[76:79], v[44:47], a[140:143], v[76:79]\n" \
"s_waitcnt lgkmcnt(4)\n" \
"v_mfma_f32_16x16x32_bf16 v[64:67], v[16:19], a[16:19], v[64:67]\n" \
"buffer_load_dwordx4 v4, s[36:39], s61 offen offset:3072 sc1 lds\n" \
"v_mfma_f32_16x16x32_bf16 v[68:71], v[20:23], a[16:19], v[68:71]\n" \
"ds_read_b128 v[40:43], v12 offset:7168\n" \
"v_mfma_f32_16x16x32_bf16 v[72:75], v[16:19], a[144:147], v[72:75]\n" \
"ds_read_b128 v[44:47], v12 offset:23552\n" \
"v_mfma_f32_16x16x32_bf16 v[76:79], v[20:23], a[144:147], v[76:79]\n" \
"s_waitcnt lgkmcnt(4)\n" \
"v_mfma_f32_16x16x32_bf16 v[64:67], v[24:27], a[20:23], v[64:67]\n" \
"buffer_load_dword v15, v14, s[36:39], s63 offen sc1\n" \
"v_mfma_f32_16x16x32_bf16 v[68:71], v[28:31], a[20:23], v[68:71]\n" \
"ds_read_b128 v[16:19], v12 offset:8192\n" \
"v_mfma_f32_16x16x32_bf16 v[72:75], v[24:27], a[148:151], v[72:75]\n" \
"ds_read_b128 v[20:23], v12 offset:24576\n" \
"v_mfma_f32_16x16x32_bf16 v[76:79], v[28:31], a[148:151], v[76:79]\n" \
"s_waitcnt lgkmcnt(4)\n" \
"v_mfma_f32_16x16x32_bf16 v[64:67], v[32:35], a[24:27], v[64:67]\n" \
"s_add_u32 m0, s55, 4096\n" \
"s_add_u32 s61, s61, 4096\n" \
"buffer_load_dwordx4 v4, s[36:39], s61 offen offset:0 sc1 lds\n" \
"v_mfma_f32_16x16x32_bf16 v[68:71], v[36:39], a[24:27], v[68:71]\n" \
"ds_read_b128 v[24:27], v12 offset:9216\n" \
"v_mfma_f32_16x16x32_bf16 v[72:75], v[32:35], a[152:155], v[72:75]\n" \
"ds_read_b128 v[28:31], v12 offset:25600\n" \
"v_mfma_f32_16x16x32_bf16 v[76:79], v[36:39], a[152:155], v[76:79]\n" \
"s_waitcnt lgkmcnt(4)\n" \
"v_mfma_f32_16x16x32_bf16 v[64:67], v[40:43], a[28:31], v[64:67]\n" \
"buffer_load_dwordx4 v4, s[36:39], s61 offen offset:1024 sc1 lds\n" \
"v_mfma_f32_16x16x32_bf16 v[68:71], v[44:47], a[28:31], v[68:71]\n" \
"ds_read_b128 v[32:35], v12 offset:10240\n" \
"v_mfma_f32_16x16x32_bf16 v[72:75], v[40:43], a[156:159], v[72:75]\n" \
"ds_read_b128 v[36:39], v12 offset:26624\n" \
"v_mfma_f32_16x16x32_bf16 v[76:79], v[44:47], a[156:159], v[76:79]\n" \
"s_waitcnt lgkmcnt(4)\n" \
"v_mfma_f32_16x16x32_bf16 v[64:67], v[16:19], a[32:35], v[64:67]\n" \
"v_mfma_f32_16x16x32_bf16 v[68:71], v[20:23], a[32:35], v[68:71]\n" \
"ds_read_b128 v[40:43], v12 offset:11264\n" \
"v_mfma_f32_16x16x32_bf16 v[72:75], v[16:19], a[160:163], v[72:75]\n" \
"ds_read_b128 v[44:47], v12 offset:27648\n" \
"v_mfma_f32_16x16x32_bf16 v[76:79], v[20:23], a[160:163], v[76:79]\n" \
"s_waitcnt lgkmcnt(4)\n" \
"v_mfma_f32_16x16x32_bf16 v[64:67], v[24:27], a[36:39], v[64:67]\n" \
"buffer_load_dwordx4 v4, s[36:39], s61 offen offset:2048 sc1 lds\n" \
"v_mfma_f32_16x16x32_bf16 v[68:71], v[28:31], a[36:39], v[68:71]\n" \
"ds_read_b128 v[16:19], v12 offset:12288\n" \
"v_mfma_f32_16x16x32_bf16 v[72:75], v[24:27], a[164:167], v[72:75]\n" \
"ds_read_b128 v[20:23], v12 offset:28672\n" \
"v_mfma_f32_16x16x32_bf16 v[76:79], v[28:31], a[164:167], v[76:79]\n" \
"s_waitcnt lgkmcnt(4)\n" \
"v_mfma_f32_16x16x32_bf16 v[64:67], v[32:35], a[40:43], v[64:67]\n" \
"buffer_load_dwordx4 v4, s[36:39], s61 offen offset:3072 sc1 lds\n" \
"v_mfma_f32_16x16x32_bf16 v[68:71], v[36:39], a[40:43], v[68:71]\n" \
"ds_read_b128 v[24:27], v12 offset:13312\n" \
"v_mfma_f32_16x16x32_bf16 v[72:75], v[32:35], a[168:171], v[72:75]\n" \
"ds_read_b128 v[28:31], v12 offset:29696\n" \
"v_mfma_f32_16x16x32_bf16 v[76:79], v[36:39], a[168:171], v[76:79]\n" \
"s_waitcnt lgkmcnt(4)\n" \
"v_mfma_f32_16x16x32_bf16 v[64:67], v[40:43], a[44:47], v[64:67]\n" \
"v_mfma_f32_16x16x32_bf16 v[68:71], v[44:47], a[44:47], v[68:71]\n" \
"ds_read_b128 v[32:35], v12 offset:14336\n" \
"v_mfma_f32_16x16x32_bf16 v[72:75], v[40:43], a[172:175], v[72:75]\n" \
"ds_read_b128 v[36:39], v12 offset:30720\n" \
"v_mfma_f32_16x16x32_bf16 v[76:79], v[44:47], a[172:175], v[76:79]\n" \
"s_waitcnt lgkmcnt(4)\n" \
"v_mfma_f32_16x16x32_bf16 v[64:67], v[16:19], a[48:51], v[64:67]\n" \
"v_mfma_f32_16x16x32_bf16 v[68:71], v[20:23], a[48:51], v[68:71]\n" \
"ds_read_b128 v[40:43], v12 offset:15360\n" \
"v_mfma_f32_16x16x32_bf16 v[72:75], v[16:19], a[176:179], v[72:75]\n" \
"ds_read_b128 v[44:47], v12 offset:31744\n" \
"v_mfma_f32_16x16x32_bf16 v[76:79], v[20:23], a[176:179], v[76:79]\n" \
"s_waitcnt lgkmcnt(4)\n" \
"v_mfma_f32_16x16x32_bf16 v[64:67], v[24:27], a[52:55], v[64:67]\n" \
"s_waitcnt vmcnt(12)\n" \
"s_barrier\n" \
"v_mfma_f32_16x16x32_bf16 v[68:71], v[28:31], a[52:55], v[68:71]\n" \
"ds_read_b128 v[16:19], v12 offset:32768\n" \
"v_mfma_f32_16x16x32_bf16 v[72:75], v[24:27], a[180:183], v[72:75]\n" \
"ds_read_b128 v[20:23], v12 offset:49152\n" \
"v_mfma_f32_16x16x32_bf16 v[76:79], v[28:31], a[180:183], v[76:79]\n" \
"s_waitcnt lgkmcnt(4)\n" \
"v_mfma_f32_16x16x32_bf16 v[64:67], v[32:35], a[56:59], v[64:67]\n" \
"v_mfma_f32_16x16x32_bf16 v[68:71], v[36:39], a[56:59], v[68:71]\n" \
"ds_read_b128 v[24:27], v12 offset:33792\n" \
"v_mfma_f32_16x16x32_bf16 v[72:75], v[32:35], a[184:187], v[72:75]\n" \
"ds_read_b128 v[28:31], v12 offset:50176\n" \
"v_mfma_f32_16x16x32_bf16 v[76:79], v[36:39], a[184:187], v[76:79]\n" \
"s_waitcnt lgkmcnt(4)\n" \
"v_mfma_f32_16x16x32_bf16 v[64:67], v[40:43], a[60:63], v[64:67]\n" \
"v_mfma_f32_16x16x32_bf16 v[68:71], v[44:47], a[60:63], v[68:71]\n" \
"ds_read_b128 v[32:35], v12 offset:34816\n" \
"v_mfma_f32_16x16x32_bf16 v[72:75], v[40:43], a[188:191], v[72:75]\n" \
"ds_read_b128 v[36:39], v12 offset:51200\n" \
"v_mfma_f32_16x16x32_bf16 v[76:79], v[44:47], a[188:191], v[76:79]\n" \
"s_waitcnt lgkmcnt(4)\n" \
"v_mfma_f32_16x16x32_bf16 v[64:67], v[16:19], a[64:67], v[64:67]\n" \
"s_min_u32 s61, s56, s60\n" \
"s_add_u32 s61, s61, 16384\n" \
"s_add_u32 s56, s56, 0x10000\n" \
"s_add_u32 m0, s55, 32768\n" \
"s_nop 0\n" \
"buffer_load_dwordx4 v4, s[36:39], s61 offen offset:0 sc1 lds\n" \
"v_mfma_f32_16x16x32_bf16 v[68:71], v[20:23], a[64:67], v[68:71]\n" \
"ds_read_b128 v[40:43], v12 offset:35840\n" \
"v_mfma_f32_16x16x32_bf16 v[72:75], v[16:19], a[192:195], v[72:75]\n" \
"ds_read_b128 v[44:47], v12 offset:52224\n" \
"v_mfma_f32_16x16x32_bf16 v[76:79], v[20:23], a[192:195], v[76:79]\n" \
"s_waitcnt lgkmcnt(4)\n" \
"v_mfma_f32_16x16x32_bf16 v[64:67], v[24:27], a[68:71], v[64:67]\n" \
"buffer_load_dwordx4 v4, s[36:39], s61 offen offset:1024 sc1 lds\n" \
"v_mfma_f32_16x16x32_bf16 v[68:71], v[28:31], a[68:71], v[68:71]\n" \
"ds_read_b128 v[16:19], v12 offset:36864\n" \
"v_mfma_f32_16x16x32_bf16 v[72:75], v[24:27], a[196:199], v[72:75]\n" \
"ds_read_b128 v[20:23], v12 offset:53248\n" \
"v_mfma_f32_16x16x32_bf16 v[76:79], v[28:31], a[196:199], v[76:79]\n" \
"s_waitcnt lgkmcnt(4)\n" \
"v_mfma_f32_16x16x32_bf16 v[64:67], v[32:35], a[72:75], v[64:67]\n" \
"v_mfma_f32_16x16x32_bf16 v[68:71], v[36:39], a[72:75], v[68:71]\n" \
"ds_read_b128 v[24:27], v12 offset:37888\n" \
"v_mfma_f32_16x16x32_bf16 v[72:75], v[32:35], a[200:203], v[72:75]\n" \
"ds_read_b128 v[28:31], v12 offset:54272\n" \
"v_mfma_f32_16x16x32_bf16 v[76:79], v[36:39], a[200:203], v[76:79]\n" \
"s_waitcnt lgkmcnt(4)\n" \
"v_mfma_f32_16x16x32_bf16 v[64:67], v[40:43], a[76:79], v[64:67]\n" \
"buffer_load_dwordx4 v4, s[36:39], s61 offen offset:2048 sc1 lds\n" \
"v_mfma_f32_16x16x32_bf16 v[68:71], v[44:47], a[76:79], v[68:71]\n" \
"ds_read_b128 v[32:35], v12 offset:38912\n" \
"v_mfma_f32_16x16x32_bf16 v[72:75], v[40:43], a[204:207], v[72:75]\n" \
"ds_read_b128 v[36:39], v12 offset:55296\n" \
"v_mfma_f32_16x16x32_bf16 v[76:79], v[44:47], a[204:207], v[76:79]\n" \
"s_waitcnt lgkmcnt(4)\n" \
"v_mfma_f32_16x16x32_bf16 v[64:67], v[16:19], a[80:83], v[64:67]\n" \
"buffer_load_dwordx4 v4, s[36:39], s61 offen offset:3072 sc1 lds\n" \
"v_mfma_f32_16x16x32_bf16 v[68:71], v[20:23], a[80:83], v[68:71]\n" \
"ds_read_b128 v[40:43], v12 offset:39936\n" \
"v_mfma_f32_16x16x32_bf16 v[72:75], v[16:19], a[208:211], v[72:75]\n" \
"ds_read_b128 v[44:47], v12 offset:56320\n" \
"v_mfma_f32_16x16x32_bf16 v[76:79], v[20:23], a[208:211], v[76:79]\n" \
"s_waitcnt lgkmcnt(4)\n" \
"v_mfma_f32_16x16x32_bf16 v[64:67], v[24:27], a[84:87], v[64:67]\n" \
"v_mfma_f32_16x16x32_bf16 v[68:71], v[28:31], a[84:87], v[68:71]\n" \
"ds_read_b128 v[16:19], v12 offset:40960\n" \
"v_mfma_f32_16x16x32_bf16 v[72:75], v[24:27], a[212:215], v[72:75]\n" \
"ds_read_b128 v[20:23], v12 offset:57344\n" \
"v_mfma_f32_16x16x32_bf16 v[76:79], v[28:31], a[212:215], v[76:79]\n" \
"s_waitcnt lgkmcnt(4)\n" \
"v_mfma_f32_16x16x32_bf16 v[64:67], v[32:35], a[88:91], v[64:67]\n" \
"s_add_u32 m0, s55, 36864\n" \
"s_add_u32 s61, s61, 4096\n" \
"buffer_load_dwordx4 v4, s[36:39], s61 offen offset:0 sc1 lds\n" \
"v_mfma_f32_16x16x32_bf16 v[68:71], v[36:39], a[88:91], v[68:71]\n" \
"ds_read_b128 v[24:27], v12 offset:41984\n" \
"v_mfma_f32_16x16x32_bf16 v[72:75], v[32:35], a[216:219], v[72:75]\n" \
"ds_read_b128 v[28:31], v12 offset:58368\n" \
"v_mfma_f32_16x16x32_bf16 v[76:79], v[36:39], a[216:219], v[76:79]\n" \
"s_waitcnt lgkmcnt(4)\n" \
"v_mfma_f32_16x16x32_bf16 v[64:67], v[40:43], a[92:95], v[64:67]\n" \
"buffer_load_dwordx4 v4, s[36:39], s61 offen offset:1024 sc1 lds\n" \
"v_mfma_f32_16x16x32_bf16 v[68:71], v[44:47], a[92:95], v[68:71]\n" \
"ds_read_b128 v[32:35], v12 offset:43008\n" \
"v_mfma_f32_16x16x32_bf16 v[72:75], v[40:43], a[220:223], v[72:75]\n" \
"ds_read_b128 v[36:39], v12 offset:59392\n" \
"v_mfma_f32_16x16x32_bf16 v[76:79], v[44:47], a[220:223], v[76:79]\n" \
"s_waitcnt lgkmcnt(4)\n" \
"v_mfma_f32_16x16x32_bf16 v[64:67], v[16:19], a[96:99], v[64:67]\n" \
"v_mfma_f32_16x16x32_bf16 v[68:71], v[20:23], a[96:99], v[68:71]\n" \
"ds_read_b128 v[40:43], v12 offset:44032\n" \
"v_mfma_f32_16x16x32_bf16 v[72:75], v[16:19], a[224:227], v[72:75]\n" \
"ds_read_b128 v[44:47], v12 offset:60416\n" \
"v_mfma_f32_16x16x32_bf16 v[76:79], v[20:23], a[224:227], v[76:79]\n" \
"s_waitcnt lgkmcnt(4)\n" \
"v_mfma_f32_16x16x32_bf16 v[64:67], v[24:27], a[100:103], v[64:67]\n" \
"buffer_load_dwordx4 v4, s[36:39], s61 offen offset:2048 sc1 lds\n" \
"v_mfma_f32_16x16x32_bf16 v[68:71], v[28:31], a[100:103], v[68:71]\n" \
"ds_read_b128 v[16:19], v12 offset:45056\n" \
"v_mfma_f32_16x16x32_bf16 v[72:75], v[24:27], a[228:231], v[72:75]\n" \
"ds_read_b128 v[20:23], v12 offset:61440\n" \
"v_mfma_f32_16x16x32_bf16 v[76:79], v[28:31], a[228:231], v[76:79]\n" \
"s_waitcnt lgkmcnt(4)\n" \
"v_mfma_f32_16x16x32_bf16 v[64:67], v[32:35], a[104:107], v[64:67]\n" \
"buffer_load_dwordx4 v4, s[36:39], s61 offen offset:3072 sc1 lds\n" \
"v_mfma_f32_16x16x32_bf16 v[68:71], v[36:39], a[104:107], v[68:71]\n" \
"ds_read_b128 v[24:27], v12 offset:46080\n" \
"v_mfma_f32_16x16x32_bf16 v[72:75], v[32:35], a[232:235], v[72:75]\n" \
"ds_read_b128 v[28:31], v12 offset:62464\n" \
"v_mfma_f32_16x16x32_bf16 v[76:79], v[36:39], a[232:235], v[76:79]\n" \
"s_waitcnt lgkmcnt(4)\n" \
"v_mfma_f32_16x16x32_bf16 v[64:67], v[40:43], a[108:111], v[64:67]\n" \
"v_mfma_f32_16x16x32_bf16 v[68:71], v[44:47], a[108:111], v[68:71]\n" \
"ds_read_b128 v[32:35], v12 offset:47104\n" \
"v_mfma_f32_16x16x32_bf16 v[72:75], v[40:43], a[236:239], v[72:75]\n" \
"ds_read_b128 v[36:39], v12 offset:63488\n" \
"v_mfma_f32_16x16x32_bf16 v[76:79], v[44:47], a[236:239], v[76:79]\n" \
"s_waitcnt lgkmcnt(4)\n" \
"v_mfma_f32_16x16x32_bf16 v[64:67], v[16:19], a[112:115], v[64:67]\n" \
"v_mfma_f32_16x16x32_bf16 v[68:71], v[20:23], a[112:115], v[68:71]\n" \
"ds_read_b128 v[40:43], v12 offset:48128\n" \
"v_mfma_f32_16x16x32_bf16 v[72:75], v[16:19], a[240:243], v[72:75]\n" \
"ds_read_b128 v[44:47], v12 offset:64512\n" \
"v_mfma_f32_16x16x32_bf16 v[76:79], v[20:23], a[240:243], v[76:79]\n" \
"s_waitcnt lgkmcnt(4)\n" \
"v_mfma_f32_16x16x32_bf16 v[64:67], v[24:27], a[116:119], v[64:67]\n" \
"s_waitcnt vmcnt(8)\n" \
"s_barrier\n" \
"v_mfma_f32_16x16x32_bf16 v[68:71], v[28:31], a[116:119], v[68:71]\n" \
"ds_read_b128 v[16:19], v10 offset:0\n" \
"v_mfma_f32_16x16x32_bf16 v[72:75], v[24:27], a[244:247], v[72:75]\n" \
"ds_read_b128 v[20:23], v10 offset:16384\n" \
"v_mfma_f32_16x16x32_bf16 v[76:79], v[28:31], a[244:247], v[76:79]\n" \
"s_waitcnt lgkmcnt(4)\n" \
"buffer_store_dword v13, v6, s[36:39], 0 offen sc1\n" \
"v_mfma_f32_16x16x32_bf16 v[64:67], v[32:35], a[120:123], v[64:67]\n" \
"v_cmp_gt_u32_e64 vcc, 16, v15\n" \
"v_mfma_f32_16x16x32_bf16 v[68:71], v[36:39], a[120:123], v[68:71]\n" \
"ds_read_b128 v[24:27], v10 offset:1024\n" \
"v_mfma_f32_16x16x32_bf16 v[72:75], v[32:35], a[248:251], v[72:75]\n" \
"ds_read_b128 v[28:31], v10 offset:17408\n" \
"v_mfma_f32_16x16x32_bf16 v[76:79], v[36:39], a[248:251], v[76:79]\n" \
"s_waitcnt lgkmcnt(4)\n" \
"v_mfma_f32_16x16x32_bf16 v[64:67], v[40:43], a[124:127], v[64:67]\n" \
"s_cbranch_vccnz .Lp1v_m1_%=\n" \
".Lp1v_m1k_%=:\n" \
"v_mfma_f32_16x16x32_bf16 v[68:71], v[44:47], a[124:127], v[68:71]\n" \
"ds_read_b128 v[32:35], v10 offset:2048\n" \
"v_mfma_f32_16x16x32_bf16 v[72:75], v[40:43], a[252:255], v[72:75]\n" \
"ds_read_b128 v[36:39], v10 offset:18432\n" \
"v_mfma_f32_16x16x32_bf16 v[76:79], v[44:47], a[252:255], v[76:79]\n" \
"s_waitcnt lgkmcnt(4)\n" \
"s_add_u32 s57, s57, s49\n" \
"s_add_u32 s58, s58, s49\n" \
"s_add_u32 s59, s59, 1\n" \
"v_mov_b32 v5, v7\n" \
"v_mov_b32 v6, v11\n" \
"s_cmp_ge_u32 s59, s47\n" \
"s_cbranch_scc0 .Lp1v_loop_%=\n" \
".Lp1v_out1_%=:\n" \
"s_nop 7\n" \
"s_nop 7\n" \
"v_cvt_pk_bf16_f32 v80, v64, v65\n" \
"v_cvt_pk_bf16_f32 v81, v66, v67\n" \
"v_cvt_pk_bf16_f32 v82, v68, v69\n" \
"v_cvt_pk_bf16_f32 v83, v70, v71\n" \
"buffer_store_dwordx4 v[80:83], v5, s[36:39], s57 offen sc1\n" \
"v_cvt_pk_bf16_f32 v84, v72, v73\n" \
"v_cvt_pk_bf16_f32 v85, v74, v75\n" \
"v_cvt_pk_bf16_f32 v86, v76, v77\n" \
"v_cvt_pk_bf16_f32 v87, v78, v79\n" \
"buffer_store_dwordx4 v[84:87], v5, s[36:39], s58 offen sc1\n" \
"s_branch .Lp1v_end_%=\n" \
".Lp1v_out0_%=:\n" \
"s_nop 7\n" \
"s_nop 7\n" \
"v_cvt_pk_bf16_f32 v80, v48, v49\n" \
"v_cvt_pk_bf16_f32 v81, v50, v51\n" \
"v_cvt_pk_bf16_f32 v82, v52, v53\n" \
"v_cvt_pk_bf16_f32 v83, v54, v55\n" \
"buffer_store_dwordx4 v[80:83], v5, s[36:39], s57 offen sc1\n" \
"v_cvt_pk_bf16_f32 v84, v56, v57\n" \
"v_cvt_pk_bf16_f32 v85, v58, v59\n" \
"v_cvt_pk_bf16_f32 v86, v60, v61\n" \
"v_cvt_pk_bf16_f32 v87, v62, v63\n" \
"buffer_store_dwordx4 v[84:87], v5, s[36:39], s58 offen sc1\n" \
".Lp1v_end_%=:\n" \
"s_waitcnt vmcnt(0) lgkmcnt(0)\n" \
"s_barrier\n" \
"v_mov_b32 v13, s59\n" \
"buffer_store_dword v13, v6, s[36:39], 0 offen sc1\n" \
"s_waitcnt vmcnt(0) lgkmcnt(0)\n" \
"s_branch .Lp1v_x_%=\n" \
".Lp1v_l0_%=:\n" \
"s_nop 7\n" \
"s_nop 7\n" \
"s_waitcnt vmcnt(0) lgkmcnt(0)\n" \
"s_mov_b32 s62, 0\n" \
".Lp1v_l0a_%=:\n" \
"buffer_load_dword v15, v14, s[36:39], s63 offen sc1\n" \
"s_waitcnt vmcnt(0)\n" \
"v_cmp_gt_u32 vcc, 16, v15\n" \
"s_nop 4\n" \
"s_cbranch_vccz .Lp1v_l0k_%=\n" \
"s_sleep 4\n" \
"s_add_u32 s62, s62, 1\n" \
"s_cmp_lt_u32 s62, 0x10000\n" \
"s_cbranch_scc1 .Lp1v_l0a_%=\n" \
"v_mov_b32 v15, 256\n" \
"buffer_store_dword v15, v15, s[36:39], 0 offen sc1\n" \
"s_waitcnt vmcnt(0) lgkmcnt(0)\n" \
"s_branch .Lp1v_l0k_%=\n" \
".Lp1v_l1_%=:\n" \
"s_nop 7\n" \
"s_nop 7\n" \
"s_waitcnt vmcnt(0) lgkmcnt(0)\n" \
"s_mov_b32 s62, 0\n" \
".Lp1v_l1a_%=:\n" \
"buffer_load_dword v15, v14, s[36:39], s63 offen sc1\n" \
"s_waitcnt vmcnt(0)\n" \
"v_cmp_gt_u32 vcc, 16, v15\n" \
"s_nop 4\n" \
"s_cbranch_vccz .Lp1v_l1k_%=\n" \
"s_sleep 4\n" \
"s_add_u32 s62, s62, 1\n" \
"s_cmp_lt_u32 s62, 0x10000\n" \
"s_cbranch_scc1 .Lp1v_l1a_%=\n" \
"v_mov_b32 v15, 256\n" \
"buffer_store_dword v15, v15, s[36:39], 0 offen sc1\n" \
"s_waitcnt vmcnt(0) lgkmcnt(0)\n" \
"s_branch .Lp1v_l1k_%=\n" \
".Lp1v_m0_%=:\n" \
"s_nop 7\n" \
"s_nop 7\n" \
"s_waitcnt vmcnt(0) lgkmcnt(0)\n" \
"s_mov_b32 s62, 0\n" \
".Lp1v_m0a_%=:\n" \
"buffer_load_dword v15, v14, s[36:39], s63 offen sc1\n" \
"s_waitcnt vmcnt(0)\n" \
"v_cmp_gt_u32 vcc, 16, v15\n" \
"s_nop 4\n" \
"s_cbranch_vccz .Lp1v_m0k_%=\n" \
"s_sleep 4\n" \
"s_add_u32 s62, s62, 1\n" \
"s_cmp_lt_u32 s62, 0x10000\n" \
"s_cbranch_scc1 .Lp1v_m0a_%=\n" \
"v_mov_b32 v15, 256\n" \
"buffer_store_dword v15, v15, s[36:39], 0 offen sc1\n" \
"s_waitcnt vmcnt(0) lgkmcnt(0)\n" \
"s_branch .Lp1v_m0k_%=\n" \
".Lp1v_m1_%=:\n" \
"s_nop 7\n" \
"s_nop 7\n" \
"s_waitcnt vmcnt(0) lgkmcnt(0)\n" \
"s_mov_b32 s62, 0\n" \
".Lp1v_m1a_%=:\n" \
"buffer_load_dword v15, v14, s[36:39], s63 offen sc1\n" \
"s_waitcnt vmcnt(0)\n" \
"v_cmp_gt_u32 vcc, 16, v15\n" \
"s_nop 4\n" \
"s_cbranch_vccz .Lp1v_m1k_%=\n" \
"s_sleep 4\n" \
"s_add_u32 s62, s62, 1\n" \
"s_cmp_lt_u32 s62, 0x10000\n" \
"s_cbranch_scc1 .Lp1v_m1a_%=\n" \
"v_mov_b32 v15, 256\n" \
"buffer_store_dword v15, v15, s[36:39], 0 offen sc1\n" \
"s_waitcnt vmcnt(0) lgkmcnt(0)\n" \
"s_branch .Lp1v_m1k_%=\n" \
".Lp1v_x_%=:\n"
#define ASM_P1_G1 \
"s_nop 7\n" \
"s_nop 7\n" \
"s_lshr_b32 s52, s48, 1\n" \
"s_lshl_b32 s52, s52, 13\n" \
"s_and_b32 s53, s48, 1\n" \
"s_lshl_b32 s53, s53, 10\n" \
"s_add_u32 s52, s52, s53\n" \
"s_mov_b32 s54, 0\n" \
"s_add_u32 m0, s52, 0\n" \
"s_nop 0\n" \
"buffer_load_dwordx4 v1, s[40:43], s54 offen nt lds\n" \
"s_add_u32 s54, s54, s44\n" \
"s_add_u32 m0, s52, 2048\n" \
"s_nop 0\n" \
"buffer_load_dwordx4 v1, s[40:43], s54 offen nt lds\n" \
"s_add_u32 s54, s54, s44\n" \
"s_add_u32 m0, s52, 4096\n" \
"s_nop 0\n" \
"buffer_load_dwordx4 v1, s[40:43], s54 offen nt lds\n" \
"s_add_u32 s54, s54, s44\n" \
"s_add_u32 m0, s52, 6144\n" \
"s_nop 0\n" \
"buffer_load_dwordx4 v1, s[40:43], s54 offen nt lds\n" \
"s_add_u32 s54, s54, s44\n" \
"s_add_u32 m0, s52, 16384\n" \
"s_nop 0\n" \
"buffer_load_dwordx4 v1, s[40:43], s54 offen nt lds\n" \
"s_add_u32 s54, s54, s44\n" \
"s_add_u32 m0, s52, 18432\n" \
"s_nop 0\n" \
"buffer_load_dwordx4 v1, s[40:43], s54 offen nt lds\n" \
"s_add_u32 s54, s54, s44\n" \
"s_add_u32 m0, s52, 20480\n" \
"s_nop 0\n" \
"buffer_load_dwordx4 v1, s[40:43], s54 offen nt lds\n" \
"s_add_u32 s54, s54, s44\n" \
"s_add_u32 m0, s52, 22528\n" \
"s_nop 0\n" \
"buffer_load_dwordx4 v1, s[40:43], s54 offen nt lds\n" \
"s_add_u32 s54, s54, s44\n" \
"s_add_u32 m0, s52, 32768\n" \
"s_nop 0\n" \
"buffer_load_dwordx4 v1, s[40:43], s54 offen nt lds\n" \
"s_add_u32 s54, s54, s44\n" \
"s_add_u32 m0, s52, 34816\n" \
"s_nop 0\n" \
"buffer_load_dwordx4 v1, s[40:43], s54 offen nt lds\n" \
"s_add_u32 s54, s54, s44\n" \
"s_add_u32 m0, s52, 36864\n" \
"s_nop 0\n" \
"buffer_load_dwordx4 v1, s[40:43], s54 offen nt lds\n" \
"s_add_u32 s54, s54, s44\n" \
"s_add_u32 m0, s52, 38912\n" \
"s_nop 0\n" \
"buffer_load_dwordx4 v1, s[40:43], s54 offen nt lds\n" \
"s_add_u32 s54, s54, s44\n" \
"s_add_u32 m0, s52, 49152\n" \
"s_nop 0\n" \
"buffer_load_dwordx4 v1, s[40:43], s54 offen nt lds\n" \
"s_add_u32 s54, s54, s44\n" \
"s_add_u32 m0, s52, 51200\n" \
"s_nop 0\n" \
"buffer_load_dwordx4 v1, s[40:43], s54 offen nt lds\n" \
"s_add_u32 s54, s54, s44\n" \
"s_add_u32 m0, s52, 53248\n" \
"s_nop 0\n" \
"buffer_load_dwordx4 v1, s[40:43], s54 offen nt lds\n" \
"s_add_u32 s54, s54, s44\n" \
"s_add_u32 m0, s52, 55296\n" \
"s_nop 0\n" \
"buffer_load_dwordx4 v1, s[40:43], s54 offen nt lds\n" \
"s_add_u32 s54, s54, s44\n" \
"s_add_u32 m0, s52, 65536\n" \
"s_nop 0\n" \
"buffer_load_dwordx4 v1, s[40:43], s54 offen nt lds\n" \
"s_add_u32 s54, s54, s44\n" \
"s_add_u32 m0, s52, 67584\n" \
"s_nop 0\n" \
"buffer_load_dwordx4 v1, s[40:43], s54 offen nt lds\n" \
"s_add_u32 s54, s54, s44\n" \
"s_add_u32 m0, s52, 69632\n" \
"s_nop 0\n" \
"buffer_load_dwordx4 v1, s[40:43], s54 offen nt lds\n" \
"s_add_u32 s54, s54, s44\n" \
"s_add_u32 m0, s52, 71680\n" \
"s_nop 0\n" \
"buffer_load_dwordx4 v1, s[40:43], s54 offen nt lds\n" \
"s_add_u32 s54, s54, s44\n" \
"s_add_u32 m0, s52, 81920\n" \
"s_nop 0\n" \
"buffer_load_dwordx4 v1, s[40:43], s54 offen nt lds\n" \
"s_add_u32 s54, s54, s44\n" \
"s_add_u32 m0, s52, 83968\n" \
"s_nop 0\n" \
"buffer_load_dwordx4 v1, s[40:43], s54 offen nt lds\n" \
"s_add_u32 s54, s54, s44\n" \
"s_add_u32 m0, s52, 86016\n" \
"s_nop 0\n" \
"buffer_load_dwordx4 v1, s[40:43], s54 offen nt lds\n" \
"s_add_u32 s54, s54, s44\n" \
"s_add_u32 m0, s52, 88064\n" \
"s_nop 0\n" \
"buffer_load_dwordx4 v1, s[40:43], s54 offen nt lds\n" \
"s_add_u32 s54, s54, s44\n" \
"s_add_u32 m0, s52, 98304\n" \
"s_nop 0\n" \
"buffer_load_dwordx4 v1, s[40:43], s54 offen nt lds\n" \
"s_add_u32 s54, s54, s44\n" \
"s_add_u32 m0, s52, 100352\n" \
"s_nop 0\n" \
"buffer_load_dwordx4 v1, s[40:43], s54 offen nt lds\n" \
"s_add_u32 s54, s54, s44\n" \
"s_add_u32 m0, s52, 102400\n" \
"s_nop 0\n" \
"buffer_load_dwordx4 v1, s[40:43], s54 offen nt lds\n" \
"s_add_u32 s54, s54, s44\n" \
"s_add_u32 m0, s52, 104448\n" \
"s_nop 0\n" \
"buffer_load_dwordx4 v1, s[40:43], s54 offen nt lds\n" \
"s_add_u32 s54, s54, s44\n" \
"s_add_u32 m0, s52, 114688\n" \
"s_nop 0\n" \
"buffer_load_dwordx4 v1, s[40:43], s54 offen nt lds\n" \
"s_add_u32 s54, s54, s44\n" \
"s_add_u32 m0, s52, 116736\n" \
"s_nop 0\n" \
"buffer_load_dwordx4 v1, s[40:43], s54 offen nt lds\n" \
"s_add_u32 s54, s54, s44\n" \
"s_add_u32 m0, s52, 118784\n" \
"s_nop 0\n" \
"buffer_load_dwordx4 v1, s[40:43], s54 offen nt lds\n" \
"s_add_u32 s54, s54, s44\n" \
"s_add_u32 m0, s52, 120832\n" \
"s_nop 0\n" \
"buffer_load_dwordx4 v1, s[40:43], s54 offen nt lds\n" \
"s_add_u32 s54, s54, s44\n" \
"v_add_u32 v8, 0x10000, v2\n" \
"v_add_u32 v9, 0x10000, v3\n" \
"s_waitcnt vmcnt(28)\n" \
"s_barrier\n" \
"ds_read_b64_tr_b16 a[0:1], v2 offset:0\n" \
"ds_read_b64_tr_b16 a[2:3], v2 offset:512\n" \
"ds_read_b64_tr_b16 a[128:129], v3 offset:0\n" \
"ds_read_b64_tr_b16 a[130:131], v3 offset:512\n" \
"ds_read_b64_tr_b16 a[4:5], v2 offset:4096\n" \
"ds_read_b64_tr_b16 a[6:7], v2 offset:4608\n" \
"ds_read_b64_tr_b16 a[132:133], v3 offset:4096\n" \
"ds_read_b64_tr_b16 a[134:135], v3 offset:4608\n" \
"s_waitcnt lgkmcnt(0)\n" \
"s_barrier\n" \
"s_add_u32 m0, s52, 0\n" \
"s_nop 0\n" \
"buffer_load_dwordx4 v1, s[40:43], s54 offen nt lds\n" \
"s_add_u32 s54, s54, s44\n" \
"s_add_u32 m0, s52, 2048\n" \
"s_nop 0\n" \
"buffer_load_dwordx4 v1, s[40:43], s54 offen nt lds\n" \
"s_add_u32 s54, s54, s44\n" \
"s_add_u32 m0, s52, 4096\n" \
"s_nop 0\n" \
"buffer_load_dwordx4 v1, s[40:43], s54 offen nt lds\n" \
"s_add_u32 s54, s54, s44\n" \
"s_add_u32 m0, s52, 6144\n" \
"s_nop 0\n" \
"buffer_load_dwordx4 v1, s[40:43], s54 offen nt lds\n" \
"s_add_u32 s54, s54, s44\n" \
"s_waitcnt vmcnt(28)\n" \
"s_barrier\n" \
"ds_read_b64_tr_b16 a[8:9], v2 offset:16384\n" \
"ds_read_b64_tr_b16 a[10:11], v2 offset:16896\n" \
"ds_read_b64_tr_b16 a[136:137], v3 offset:16384\n" \
"ds_read_b64_tr_b16 a[138:139], v3 offset:16896\n" \
"ds_read_b64_tr_b16 a[12:13], v2 offset:20480\n" \
"ds_read_b64_tr_b16 a[14:15], v2 offset:20992\n" \
"ds_read_b64_tr_b16 a[140:141], v3 offset:20480\n" \
"ds_read_b64_tr_b16 a[142:143], v3 offset:20992\n" \
"s_waitcnt lgkmcnt(0)\n" \
"s_barrier\n" \
"s_add_u32 m0, s52, 16384\n" \
"s_nop 0\n" \
"buffer_load_dwordx4 v1, s[40:43], s54 offen nt lds\n" \
"s_add_u32 s54, s54, s44\n" \
"s_add_u32 m0, s52, 18432\n" \
"s_nop 0\n" \
"buffer_load_dwordx4 v1, s[40:43], s54 offen nt lds\n" \
"s_add_u32 s54, s54, s44\n" \
"s_add_u32 m0, s52, 20480\n" \
"s_nop 0\n" \
"buffer_load_dwordx4 v1, s[40:43], s54 offen nt lds\n" \
"s_add_u32 s54, s54, s44\n" \
"s_add_u32 m0, s52, 22528\n" \
"s_nop 0\n" \
"buffer_load_dwordx4 v1, s[40:43], s54 offen nt lds\n" \
"s_add_u32 s54, s54, s44\n" \
"s_waitcnt vmcnt(28)\n" \
"s_barrier\n" \
"ds_read_b64_tr_b16 a[16:17], v2 offset:32768\n" \
"ds_read_b64_tr_b16 a[18:19], v2 offset:33280\n" \
"ds_read_b64_tr_b16 a[144:145], v3 offset:32768\n" \
"ds_read_b64_tr_b16 a[146:147], v3 offset:33280\n" \
"ds_read_b64_tr_b16 a[20:21], v2 offset:36864\n" \
"ds_read_b64_tr_b16 a[22:23], v2 offset:37376\n" \
"ds_read_b64_tr_b16 a[148:149], v3 offset:36864\n" \
"ds_read_b64_tr_b16 a[150:151], v3 offset:37376\n" \
"s_waitcnt lgkmcnt(0)\n" \
"s_barrier\n" \
"s_add_u32 m0, s52, 32768\n" \
"s_nop 0\n" \
"buffer_load_dwordx4 v1, s[40:43], s54 offen nt lds\n" \
"s_add_u32 s54, s54, s44\n" \
"s_add_u32 m0, s52, 34816\n" \
"s_nop 0\n" \
"buffer_load_dwordx4 v1, s[40:43], s54 offen nt lds\n" \
"s_add_u32 s54, s54, s44\n" \
"s_add_u32 m0, s52, 36864\n" \
"s_nop 0\n" \
"buffer_load_dwordx4 v1, s[40:43], s54 offen nt lds\n" \
"s_add_u32 s54, s54, s44\n" \
"s_add_u32 m0, s52, 38912\n" \
"s_nop 0\n" \
"buffer_load_dwordx4 v1, s[40:43], s54 offen nt lds\n" \
"s_add_u32 s54, s54, s44\n" \
"s_waitcnt vmcnt(28)\n" \
"s_barrier\n" \
"ds_read_b64_tr_b16 a[24:25], v2 offset:49152\n" \
"ds_read_b64_tr_b16 a[26:27], v2 offset:49664\n" \
"ds_read_b64_tr_b16 a[152:153], v3 offset:49152\n" \
"ds_read_b64_tr_b16 a[154:155], v3 offset:49664\n" \
"ds_read_b64_tr_b16 a[28:29], v2 offset:53248\n" \
"ds_read_b64_tr_b16 a[30:31], v2 offset:53760\n" \
"ds_read_b64_tr_b16 a[156:157], v3 offset:53248\n" \
"ds_read_b64_tr_b16 a[158:159], v3 offset:53760\n" \
"s_waitcnt lgkmcnt(0)\n" \
"s_barrier\n" \
"s_add_u32 m0, s52, 49152\n" \
"s_nop 0\n" \
"buffer_load_dwordx4 v1, s[40:43], s54 offen nt lds\n" \
"s_add_u32 s54, s54, s44\n" \
"s_add_u32 m0, s52, 51200\n" \
"s_nop 0\n" \
"buffer_load_dwordx4 v1, s[40:43], s54 offen nt lds\n" \
"s_add_u32 s54, s54, s44\n" \
"s_add_u32 m0, s52, 53248\n" \
"s_nop 0\n" \
"buffer_load_dwordx4 v1, s[40:43], s54 offen nt lds\n" \
"s_add_u32 s54, s54, s44\n" \
"s_add_u32 m0, s52, 55296\n" \
"s_nop 0\n" \
"buffer_load_dwordx4 v1, s[40:43], s54 offen nt lds\n" \
"s_add_u32 s54, s54, s44\n" \
"s_waitcnt vmcnt(28)\n" \
"s_barrier\n" \
"ds_read_b64_tr_b16 a[32:33], v8 offset:0\n" \
"ds_read_b64_tr_b16 a[34:35], v8 offset:512\n" \
"ds_read_b64_tr_b16 a[160:161], v9 offset:0\n" \
"ds_read_b64_tr_b16 a[162:163], v9 offset:512\n" \
"ds_read_b64_tr_b16 a[36:37], v8 offset:4096\n" \
"ds_read_b64_tr_b16 a[38:39], v8 offset:4608\n" \
"ds_read_b64_tr_b16 a[164:165], v9 offset:4096\n" \
"ds_read_b64_tr_b16 a[166:167], v9 offset:4608\n" \
"s_waitcnt lgkmcnt(0)\n" \
"s_barrier\n" \
"s_add_u32 m0, s52, 65536\n" \
"s_nop 0\n" \
"buffer_load_dwordx4 v1, s[40:43], s54 offen nt lds\n" \
"s_add_u32 s54, s54, s44\n" \
"s_add_u32 m0, s52, 67584\n" \
"s_nop 0\n" \
"buffer_load_dwordx4 v1, s[40:43], s54 offen nt lds\n" \
"s_add_u32 s54, s54, s44\n" \
"s_add_u32 m0, s52, 69632\n" \
"s_nop 0\n" \
"buffer_load_dwordx4 v1, s[40:43], s54 offen nt lds\n" \
"s_add_u32 s54, s54, s44\n" \
"s_add_u32 m0, s52, 71680\n" \
"s_nop 0\n" \
"buffer_load_dwordx4 v1, s[40:43], s54 offen nt lds\n" \
"s_add_u32 s54, s54, s44\n" \
"s_waitcnt vmcnt(28)\n" \
"s_barrier\n" \
"ds_read_b64_tr_b16 a[40:41], v8 offset:16384\n" \
"ds_read_b64_tr_b16 a[42:43], v8 offset:16896\n" \
"ds_read_b64_tr_b16 a[168:169], v9 offset:16384\n" \
"ds_read_b64_tr_b16 a[170:171], v9 offset:16896\n" \
"ds_read_b64_tr_b16 a[44:45], v8 offset:20480\n" \
"ds_read_b64_tr_b16 a[46:47], v8 offset:20992\n" \
"ds_read_b64_tr_b16 a[172:173], v9 offset:20480\n" \
"ds_read_b64_tr_b16 a[174:175], v9 offset:20992\n" \
"s_waitcnt lgkmcnt(0)\n" \
"s_barrier\n" \
"s_add_u32 m0, s52, 81920\n" \
"s_nop 0\n" \
"buffer_load_dwordx4 v1, s[40:43], s54 offen nt lds\n" \
"s_add_u32 s54, s54, s44\n" \
"s_add_u32 m0, s52, 83968\n" \
"s_nop 0\n" \
"buffer_load_dwordx4 v1, s[40:43], s54 offen nt lds\n" \
"s_add_u32 s54, s54, s44\n" \
"s_add_u32 m0, s52, 86016\n" \
"s_nop 0\n" \
"buffer_load_dwordx4 v1, s[40:43], s54 offen nt lds\n" \
"s_add_u32 s54, s54, s44\n" \
"s_add_u32 m0, s52, 88064\n" \
"s_nop 0\n" \
"buffer_load_dwordx4 v1, s[40:43], s54 offen nt lds\n" \
"s_add_u32 s54, s54, s44\n" \
"s_waitcnt vmcnt(28)\n" \
"s_barrier\n" \
"ds_read_b64_tr_b16 a[48:49], v8 offset:32768\n" \
"ds_read_b64_tr_b16 a[50:51], v8 offset:33280\n" \
"ds_read_b64_tr_b16 a[176:177], v9 offset:32768\n" \
"ds_read_b64_tr_b16 a[178:179], v9 offset:33280\n" \
"ds_read_b64_tr_b16 a[52:53], v8 offset:36864\n" \
"ds_read_b64_tr_b16 a[54:55], v8 offset:37376\n" \
"ds_read_b64_tr_b16 a[180:181], v9 offset:36864\n" \
"ds_read_b64_tr_b16 a[182:183], v9 offset:37376\n" \
"s_waitcnt lgkmcnt(0)\n" \
"s_barrier\n" \
"s_add_u32 m0, s52, 98304\n" \
"s_nop 0\n" \
"buffer_load_dwordx4 v1, s[40:43], s54 offen nt lds\n" \
"s_add_u32 s54, s54, s44\n" \
"s_add_u32 m0, s52, 100352\n" \
"s_nop 0\n" \
"buffer_load_dwordx4 v1, s[40:43], s54 offen nt lds\n" \
"s_add_u32 s54, s54, s44\n" \
"s_add_u32 m0, s52, 102400\n" \
"s_nop 0\n" \
"buffer_load_dwordx4 v1, s[40:43], s54 offen nt lds\n" \
"s_add_u32 s54, s54, s44\n" \
"s_add_u32 m0, s52, 104448\n" \
"s_nop 0\n" \
"buffer_load_dwordx4 v1, s[40:43], s54 offen nt lds\n" \
"s_add_u32 s54, s54, s44\n" \
"s_waitcnt vmcnt(28)\n" \
"s_barrier\n" \
"ds_read_b64_tr_b16 a[56:57], v8 offset:49152\n" \
"ds_read_b64_tr_b16 a[58:59], v8 offset:49664\n" \
"ds_read_b64_tr_b16 a[184:185], v9 offset:49152\n" \
"ds_read_b64_tr_b16 a[186:187], v9 offset:49664\n" \
"ds_read_b64_tr_b16 a[60:61], v8 offset:53248\n" \
"ds_read_b64_tr_b16 a[62:63], v8 offset:53760\n" \
"ds_read_b64_tr_b16 a[188:189], v9 offset:53248\n" \
"ds_read_b64_tr_b16 a[190:191], v9 offset:53760\n" \
"s_waitcnt lgkmcnt(0)\n" \
"s_barrier\n" \
"s_add_u32 m0, s52, 114688\n" \
"s_nop 0\n" \
"buffer_load_dwordx4 v1, s[40:43], s54 offen nt lds\n" \
"s_add_u32 s54, s54, s44\n" \
"s_add_u32 m0, s52, 116736\n" \
"s_nop 0\n" \
"buffer_load_dwordx4 v1, s[40:43], s54 offen nt lds\n" \
"s_add_u32 s54, s54, s44\n" \
"s_add_u32 m0, s52, 118784\n" \
"s_nop 0\n" \
"buffer_load_dwordx4 v1, s[40:43], s54 offen nt lds\n" \
"s_add_u32 s54, s54, s44\n" \
"s_add_u32 m0, s52, 120832\n" \
"s_nop 0\n" \
"buffer_load_dwordx4 v1, s[40:43], s54 offen nt lds\n" \
"s_add_u32 s54, s54, s44\n" \
"s_waitcnt vmcnt(28)\n" \
"s_barrier\n" \
"ds_read_b64_tr_b16 a[64:65], v2 offset:0\n" \
"ds_read_b64_tr_b16 a[66:67], v2 offset:512\n" \
"ds_read_b64_tr_b16 a[192:193], v3 offset:0\n" \
"ds_read_b64_tr_b16 a[194:195], v3 offset:512\n" \
"ds_read_b64_tr_b16 a[68:69], v2 offset:4096\n" \
"ds_read_b64_tr_b16 a[70:71], v2 offset:4608\n" \
"ds_read_b64_tr_b16 a[196:197], v3 offset:4096\n" \
"ds_read_b64_tr_b16 a[198:199], v3 offset:4608\n" \
"s_waitcnt vmcnt(24)\n" \
"s_barrier\n" \
"ds_read_b64_tr_b16 a[72:73], v2 offset:16384\n" \
"ds_read_b64_tr_b16 a[74:75], v2 offset:16896\n" \
"ds_read_b64_tr_b16 a[200:201], v3 offset:16384\n" \
"ds_read_b64_tr_b16 a[202:203], v3 offset:16896\n" \
"ds_read_b64_tr_b16 a[76:77], v2 offset:20480\n" \
"ds_read_b64_tr_b16 a[78:79], v2 offset:20992\n" \
"ds_read_b64_tr_b16 a[204:205], v3 offset:20480\n" \
"ds_read_b64_tr_b16 a[206:207], v3 offset:20992\n" \
"s_waitcnt vmcnt(20)\n" \
"s_barrier\n" \
"ds_read_b64_tr_b16 a[80:81], v2 offset:32768\n" \
"ds_read_b64_tr_b16 a[82:83], v2 offset:33280\n" \
"ds_read_b64_tr_b16 a[208:209], v3 offset:32768\n" \
"ds_read_b64_tr_b16 a[210:211], v3 offset:33280\n" \
"ds_read_b64_tr_b16 a[84:85], v2 offset:36864\n" \
"ds_read_b64_tr_b16 a[86:87], v2 offset:37376\n" \
"ds_read_b64_tr_b16 a[212:213], v3 offset:36864\n" \
"ds_read_b64_tr_b16 a[214:215], v3 offset:37376\n" \
"s_waitcnt vmcnt(16)\n" \
"s_barrier\n" \
"ds_read_b64_tr_b16 a[88:89], v2 offset:49152\n" \
"ds_read_b64_tr_b16 a[90:91], v2 offset:49664\n" \
"ds_read_b64_tr_b16 a[216:217], v3 offset:49152\n" \
"ds_read_b64_tr_b16 a[218:219], v3 offset:49664\n" \
"ds_read_b64_tr_b16 a[92:93], v2 offset:53248\n" \
"ds_read_b64_tr_b16 a[94:95], v2 offset:53760\n" \
"ds_read_b64_tr_b16 a[220:221], v3 offset:53248\n" \
"ds_read_b64_tr_b16 a[222:223], v3 offset:53760\n" \
"s_waitcnt vmcnt(12)\n" \
"s_barrier\n" \
"ds_read_b64_tr_b16 a[96:97], v8 offset:0\n" \
"ds_read_b64_tr_b16 a[98:99], v8 offset:512\n" \
"ds_read_b64_tr_b16 a[224:225], v9 offset:0\n" \
"ds_read_b64_tr_b16 a[226:227], v9 offset:512\n" \
"ds_read_b64_tr_b16 a[100:101], v8 offset:4096\n" \
"ds_read_b64_tr_b16 a[102:103], v8 offset:4608\n" \
"ds_read_b64_tr_b16 a[228:229], v9 offset:4096\n" \
"ds_read_b64_tr_b16 a[230:231], v9 offset:4608\n" \
"s_waitcnt vmcnt(8)\n" \
"s_barrier\n" \
"ds_read_b64_tr_b16 a[104:105], v8 offset:16384\n" \
"ds_read_b64_tr_b16 a[106:107], v8 offset:16896\n" \
"ds_read_b64_tr_b16 a[232:233], v9 offset:16384\n" \
"ds_read_b64_tr_b16 a[234:235], v9 offset:16896\n" \
"ds_read_b64_tr_b16 a[108:109], v8 offset:20480\n" \
"ds_read_b64_tr_b16 a[110:111], v8 offset:20992\n" \
"ds_read_b64_tr_b16 a[236:237], v9 offset:20480\n" \
"ds_read_b64_tr_b16 a[238:239], v9 offset:20992\n" \
"s_waitcnt vmcnt(4)\n" \
"s_barrier\n" \
"ds_read_b64_tr_b16 a[112:113], v8 offset:32768\n" \
"ds_read_b64_tr_b16 a[114:115], v8 offset:33280\n" \
"ds_read_b64_tr_b16 a[240:241], v9 offset:32768\n" \
"ds_read_b64_tr_b16 a[242:243], v9 offset:33280\n" \
"ds_read_b64_tr_b16 a[116:117], v8 offset:36864\n" \
"ds_read_b64_tr_b16 a[118:119], v8 offset:37376\n" \
"ds_read_b64_tr_b16 a[244:245], v9 offset:36864\n" \
"ds_read_b64_tr_b16 a[246:247], v9 offset:37376\n" \
"s_waitcnt vmcnt(0)\n" \
"s_barrier\n" \
"ds_read_b64_tr_b16 a[120:121], v8 offset:49152\n" \
"ds_read_b64_tr_b16 a[122:123], v8 offset:49664\n" \
"ds_read_b64_tr_b16 a[248:249], v9 offset:49152\n" \
"ds_read_b64_tr_b16 a[250:251], v9 offset:49664\n" \
"ds_read_b64_tr_b16 a[124:125], v8 offset:53248\n" \
"ds_read_b64_tr_b16 a[126:127], v8 offset:53760\n" \
"ds_read_b64_tr_b16 a[252:253], v9 offset:53248\n" \
"ds_read_b64_tr_b16 a[254:255], v9 offset:53760\n" \
"s_waitcnt lgkmcnt(0)\n" \
"s_barrier\n" \
"v_mov_b32 v14, 4096\n" \
"s_mov_b32 s59, s46\n" \
"s_add_u32 s63, s59, 0\n" \
"s_min_u32 s63, s63, 63\n" \
"s_lshr_b32 s63, s63, 3\n" \
"s_lshl_b32 s63, s63, 7\n" \
"s_branch .Lp1g1_l0_%=\n" \
".Lp1g1_l0k_%=:\n" \
"s_add_u32 s63, s59, 1\n" \
"s_min_u32 s63, s63, 63\n" \
"s_lshr_b32 s63, s63, 3\n" \
"s_lshl_b32 s63, s63, 7\n" \
"s_branch .Lp1g1_l1_%=\n" \
".Lp1g1_l1k_%=:\n" \
"s_lshl_b32 s55, s48, 13\n" \
"s_lshl_b32 s56, s46, 16\n" \
"s_lshl_b32 s60, s47, 16\n" \
"s_sub_u32 s60, s60, 0x10000\n" \
"s_mul_i32 s57, s46, s49\n" \
"s_sub_u32 s57, s57, s49\n" \
"s_add_u32 s58, s57, s50\n" \
"v_mov_b32 v7, v5\n" \
"v_mov_b32 v5, 0x7ffffff0\n" \
"v_mov_b32 v10, v0\n" \
"v_mov_b32 v11, v6\n" \
"v_mov_b32 v6, 0x7ffffff0\n" \
"s_mov_b32 s59, s46\n" \
"s_min_u32 s61, s56, s60\n" \
"s_add_u32 m0, s55, 0\n" \
"s_nop 0\n" \
"buffer_load_dwordx4 v4, s[36:39], s61 offen offset:0 sc1 lds\n" \
"buffer_load_dwordx4 v4, s[36:39], s61 offen offset:1024 sc1 lds\n" \
"buffer_load_dwordx4 v4, s[36:39], s61 offen offset:2048 sc1 lds\n" \
"buffer_load_dwordx4 v4, s[36:39], s61 offen offset:3072 sc1 lds\n" \
"s_add_u32 m0, s55, 4096\n" \
"s_add_u32 s61, s61, 4096\n" \
"buffer_load_dwordx4 v4, s[36:39], s61 offen offset:0 sc1 lds\n" \
"buffer_load_dwordx4 v4, s[36:39], s61 offen offset:1024 sc1 lds\n" \
"buffer_load_dwordx4 v4, s[36:39], s61 offen offset:2048 sc1 lds\n" \
"buffer_load_dwordx4 v4, s[36:39], s61 offen offset:3072 sc1 lds\n" \
"s_min_u32 s61, s56, s60\n" \
"s_add_u32 s61, s61, 16384\n" \
"s_add_u32 s56, s56, 0x10000\n" \
"s_add_u32 m0, s55, 32768\n" \
"s_nop 0\n" \
"buffer_load_dwordx4 v4, s[36:39], s61 offen offset:0 sc1 lds\n" \
"buffer_load_dwordx4 v4, s[36:39], s61 offen offset:1024 sc1 lds\n" \
"buffer_load_dwordx4 v4, s[36:39], s61 offen offset:2048 sc1 lds\n" \
"buffer_load_dwordx4 v4, s[36:39], s61 offen offset:3072 sc1 lds\n" \
"s_add_u32 m0, s55, 36864\n" \
"s_add_u32 s61, s61, 4096\n" \
"buffer_load_dwordx4 v4, s[36:39], s61 offen offset:0 sc1 lds\n" \
"buffer_load_dwordx4 v4, s[36:39], s61 offen offset:1024 sc1 lds\n" \
"buffer_load_dwordx4 v4, s[36:39], s61 offen offset:2048 sc1 lds\n" \
"buffer_load_dwordx4 v4, s[36:39], s61 offen offset:3072 sc1 lds\n" \
"s_mov_b32 s70, 0\n" \
"s_mov_b32 s72, 0\n" \
"s_waitcnt vmcnt(0) lgkmcnt(0)\n" \
"s_barrier\n" \
"v_add_u32 v12, 0x10000, v10\n" \
"ds_read_b128 v[16:19], v10 offset:0\n" \
"ds_read_b128 v[20:23], v10 offset:16384\n" \
"ds_read_b128 v[24:27], v10 offset:1024\n" \
"ds_read_b128 v[28:31], v10 offset:17408\n" \
"ds_read_b128 v[32:35], v10 offset:2048\n" \
"ds_read_b128 v[36:39], v10 offset:18432\n" \
"s_waitcnt lgkmcnt(4)\n" \
"s_nop 7\n" \
"s_nop 7\n" \
".p2align 6\n" \
".Lp1g1_loop_%=:\n" \
"v_mfma_f32_16x16x32_bf16 v[48:51], a[0:3], v[16:19], 0\n" \
"s_min_u32 s61, s56, s60\n" \
"s_add_u32 m0, s55, 65536\n" \
"s_nop 0\n" \
"buffer_load_dwordx4 v4, s[36:39], s61 offen offset:0 sc1 lds\n" \
"v_add_u32_e64 v13, -1, s59\n" \
"v_mfma_f32_16x16x32_bf16 v[52:55], a[0:3], v[20:23], 0\n" \
"ds_read_b128 v[40:43], v10 offset:3072\n" \
"s_nop 0\n" \
"v_mfma_f32_16x16x32_bf16 v[56:59], a[128:131], v[16:19], 0\n" \
"ds_read_b128 v[44:47], v10 offset:19456\n" \
"s_nop 0\n" \
"v_mfma_f32_16x16x32_bf16 v[60:63], a[128:131], v[20:23], 0\n" \
"s_waitcnt lgkmcnt(4)\n" \
"v_mul_f32 v64, 0xbfb8aa3b, v64\n" \
"v_mul_f32 v65, 0xbfb8aa3b, v65\n" \
"v_mfma_f32_16x16x32_bf16 v[48:51], a[4:7], v[24:27], v[48:51]\n" \
"buffer_load_dwordx4 v4, s[36:39], s61 offen offset:1024 sc1 lds\n" \
"v_mfma_f32_16x16x32_bf16 v[52:55], a[4:7], v[28:31], v[52:55]\n" \
"ds_read_b128 v[16:19], v10 offset:4096\n" \
"v_mul_f32 v66, 0xbfb8aa3b, v66\n" \
"v_mfma_f32_16x16x32_bf16 v[56:59], a[132:135], v[24:27], v[56:59]\n" \
"ds_read_b128 v[20:23], v10 offset:20480\n" \
"v_mul_f32 v67, 0xbfb8aa3b, v67\n" \
"v_mfma_f32_16x16x32_bf16 v[60:63], a[132:135], v[28:31], v[60:63]\n" \
"s_waitcnt lgkmcnt(4)\n" \
"v_exp_f32_e64 v64, v64\n" \
"v_mfma_f32_16x16x32_bf16 v[48:51], a[8:11], v[32:35], v[48:51]\n" \
"s_add_u32 s63, s59, 2\n" \
"s_min_u32 s63, s63, 63\n" \
"v_mfma_f32_16x16x32_bf16 v[52:55], a[8:11], v[36:39], v[52:55]\n" \
"ds_read_b128 v[24:27], v10 offset:5120\n" \
"s_lshr_b32 s63, s63, 3\n" \
"s_lshl_b32 s63, s63, 7\n" \
"v_mfma_f32_16x16x32_bf16 v[56:59], a[136:139], v[32:35], v[56:59]\n" \
"ds_read_b128 v[28:31], v10 offset:21504\n" \
"v_mfma_f32_16x16x32_bf16 v[60:63], a[136:139], v[36:39], v[60:63]\n" \
"s_waitcnt lgkmcnt(4)\n" \
"v_exp_f32 v65, v65\n" \
"v_mfma_f32_16x16x32_bf16 v[48:51], a[12:15], v[40:43], v[48:51]\n" \
"buffer_load_dwordx4 v4, s[36:39], s61 offen offset:2048 sc1 lds\n" \
"v_mfma_f32_16x16x32_bf16 v[52:55], a[12:15], v[44:47], v[52:55]\n" \
"ds_read_b128 v[32:35], v10 offset:6144\n" \
"v_mfma_f32_16x16x32_bf16 v[56:59], a[140:143], v[40:43], v[56:59]\n" \
"ds_read_b128 v[36:39], v10 offset:22528\n" \
"v_mfma_f32_16x16x32_bf16 v[60:63], a[140:143], v[44:47], v[60:63]\n" \
"s_waitcnt lgkmcnt(4)\n" \
"v_exp_f32 v66, v66\n" \
"v_mfma_f32_16x16x32_bf16 v[48:51], a[16:19], v[16:19], v[48:51]\n" \
"buffer_load_dwordx4 v4, s[36:39], s61 offen offset:3072 sc1 lds\n" \
"v_mfma_f32_16x16x32_bf16 v[52:55], a[16:19], v[20:23], v[52:55]\n" \
"ds_read_b128 v[40:43], v10 offset:7168\n" \
"v_mfma_f32_16x16x32_bf16 v[56:59], a[144:147], v[16:19], v[56:59]\n" \
"ds_read_b128 v[44:47], v10 offset:23552\n" \
"v_mfma_f32_16x16x32_bf16 v[60:63], a[144:147], v[20:23], v[60:63]\n" \
"s_waitcnt lgkmcnt(4)\n" \
"v_exp_f32 v67, v67\n" \
"v_mfma_f32_16x16x32_bf16 v[48:51], a[20:23], v[24:27], v[48:51]\n" \
"buffer_load_dword v15, v14, s[36:39], s63 offen sc1\n" \
"v_mfma_f32_16x16x32_bf16 v[52:55], a[20:23], v[28:31], v[52:55]\n" \
"ds_read_b128 v[16:19], v10 offset:8192\n" \
"v_add_f32_e64 v64, 1.0, v64\n" \
"v_mfma_f32_16x16x32_bf16 v[56:59], a[148:151], v[24:27], v[56:59]\n" \
"ds_read_b128 v[20:23], v10 offset:24576\n" \
"v_add_f32_e64 v65, 1.0, v65\n" \
"v_mfma_f32_16x16x32_bf16 v[60:63], a[148:151], v[28:31], v[60:63]\n" \
"s_waitcnt lgkmcnt(4)\n" \
"v_add_f32 v66, 1.0, v66\n" \
"v_add_f32_e64 v67, 1.0, v67\n" \
"v_mfma_f32_16x16x32_bf16 v[48:51], a[24:27], v[32:35], v[48:51]\n" \
"s_add_u32 m0, s55, 69632\n" \
"s_add_u32 s61, s61, 4096\n" \
"buffer_load_dwordx4 v4, s[36:39], s61 offen offset:0 sc1 lds\n" \
"v_mfma_f32_16x16x32_bf16 v[52:55], a[24:27], v[36:39], v[52:55]\n" \
"ds_read_b128 v[24:27], v10 offset:9216\n" \
"v_mfma_f32_16x16x32_bf16 v[56:59], a[152:155], v[32:35], v[56:59]\n" \
"ds_read_b128 v[28:31], v10 offset:25600\n" \
"v_mfma_f32_16x16x32_bf16 v[60:63], a[152:155], v[36:39], v[60:63]\n" \
"s_waitcnt lgkmcnt(4)\n" \
"v_rcp_f32 v64, v64\n" \
"v_mfma_f32_16x16x32_bf16 v[48:51], a[28:31], v[40:43], v[48:51]\n" \
"buffer_load_dwordx4 v4, s[36:39], s61 offen offset:1024 sc1 lds\n" \
"v_mfma_f32_16x16x32_bf16 v[52:55], a[28:31], v[44:47], v[52:55]\n" \
"ds_read_b128 v[32:35], v10 offset:10240\n" \
"v_mfma_f32_16x16x32_bf16 v[56:59], a[156:159], v[40:43], v[56:59]\n" \
"ds_read_b128 v[36:39], v10 offset:26624\n" \
"v_mfma_f32_16x16x32_bf16 v[60:63], a[156:159], v[44:47], v[60:63]\n" \
"s_waitcnt lgkmcnt(4)\n" \
"v_rcp_f32 v65, v65\n" \
"v_mfma_f32_16x16x32_bf16 v[48:51], a[32:35], v[16:19], v[48:51]\n" \
"v_rcp_f32_e64 v66, v66\n" \
"v_mfma_f32_16x16x32_bf16 v[52:55], a[32:35], v[20:23], v[52:55]\n" \
"ds_read_b128 v[40:43], v10 offset:11264\n" \
"v_mfma_f32_16x16x32_bf16 v[56:59], a[160:163], v[16:19], v[56:59]\n" \
"ds_read_b128 v[44:47], v10 offset:27648\n" \
"v_mfma_f32_16x16x32_bf16 v[60:63], a[160:163], v[20:23], v[60:63]\n" \
"s_waitcnt lgkmcnt(4)\n" \
"v_rcp_f32 v67, v67\n" \
"v_mfma_f32_16x16x32_bf16 v[48:51], a[36:39], v[24:27], v[48:51]\n" \
"buffer_load_dwordx4 v4, s[36:39], s61 offen offset:2048 sc1 lds\n" \
"v_mfma_f32_16x16x32_bf16 v[52:55], a[36:39], v[28:31], v[52:55]\n" \
"ds_read_b128 v[16:19], v10 offset:12288\n" \
"v_mul_f32_e64 v64, v64, v72\n" \
"v_mfma_f32_16x16x32_bf16 v[56:59], a[164:167], v[24:27], v[56:59]\n" \
"ds_read_b128 v[20:23], v10 offset:28672\n" \
"v_mul_f32_e64 v65, v65, v73\n" \
"v_mfma_f32_16x16x32_bf16 v[60:63], a[164:167], v[28:31], v[60:63]\n" \
"s_waitcnt lgkmcnt(4)\n" \
"v_mul_f32 v66, v66, v74\n" \
"v_mul_f32_e64 v67, v67, v75\n" \
"v_mfma_f32_16x16x32_bf16 v[48:51], a[40:43], v[32:35], v[48:51]\n" \
"buffer_load_dwordx4 v4, s[36:39], s61 offen offset:3072 sc1 lds\n" \
"v_mfma_f32_16x16x32_bf16 v[52:55], a[40:43], v[36:39], v[52:55]\n" \
"ds_read_b128 v[24:27], v10 offset:13312\n" \
"v_cvt_pk_bf16_f32 v80, v64, v65\n" \
"v_mfma_f32_16x16x32_bf16 v[56:59], a[168:171], v[32:35], v[56:59]\n" \
"ds_read_b128 v[28:31], v10 offset:29696\n" \
"v_cvt_pk_bf16_f32 v81, v66, v67\n" \
"v_mfma_f32_16x16x32_bf16 v[60:63], a[168:171], v[36:39], v[60:63]\n" \
"s_waitcnt lgkmcnt(4)\n" \
"v_mul_f32 v68, 0xbfb8aa3b, v68\n" \
"v_mul_f32 v69, 0xbfb8aa3b, v69\n" \
"v_mfma_f32_16x16x32_bf16 v[48:51], a[44:47], v[40:43], v[48:51]\n" \
"v_mul_f32 v70, 0xbfb8aa3b, v70\n" \
"v_mul_f32 v71, 0xbfb8aa3b, v71\n" \
"v_mfma_f32_16x16x32_bf16 v[52:55], a[44:47], v[44:47], v[52:55]\n" \
"ds_read_b128 v[32:35], v10 offset:14336\n" \
"v_mfma_f32_16x16x32_bf16 v[56:59], a[172:175], v[40:43], v[56:59]\n" \
"ds_read_b128 v[36:39], v10 offset:30720\n" \
"v_mfma_f32_16x16x32_bf16 v[60:63], a[172:175], v[44:47], v[60:63]\n" \
"s_waitcnt lgkmcnt(4)\n" \
"v_exp_f32_e64 v68, v68\n" \
"v_mfma_f32_16x16x32_bf16 v[48:51], a[48:51], v[16:19], v[48:51]\n" \
"v_exp_f32_e64 v69, v69\n" \
"v_mfma_f32_16x16x32_bf16 v[52:55], a[48:51], v[20:23], v[52:55]\n" \
"ds_read_b128 v[40:43], v10 offset:15360\n" \
"v_mfma_f32_16x16x32_bf16 v[56:59], a[176:179], v[16:19], v[56:59]\n" \
"ds_read_b128 v[44:47], v10 offset:31744\n" \
"v_mfma_f32_16x16x32_bf16 v[60:63], a[176:179], v[20:23], v[60:63]\n" \
"s_waitcnt lgkmcnt(4)\n" \
"v_exp_f32 v70, v70\n" \
"v_mfma_f32_16x16x32_bf16 v[48:51], a[52:55], v[24:27], v[48:51]\n" \
"s_waitcnt vmcnt(9)\n" \
"s_barrier\n" \
"v_mfma_f32_16x16x32_bf16 v[52:55], a[52:55], v[28:31], v[52:55]\n" \
"ds_read_b128 v[16:19], v10 offset:32768\n" \
"v_mfma_f32_16x16x32_bf16 v[56:59], a[180:183], v[24:27], v[56:59]\n" \
"ds_read_b128 v[20:23], v10 offset:49152\n" \
"v_mfma_f32_16x16x32_bf16 v[60:63], a[180:183], v[28:31], v[60:63]\n" \
"s_waitcnt lgkmcnt(4)\n" \
"buffer_store_dword v13, v6, s[36:39], 0 offen sc1\n" \
"v_exp_f32 v71, v71\n" \
"v_mfma_f32_16x16x32_bf16 v[48:51], a[56:59], v[32:35], v[48:51]\n" \
"v_add_f32 v68, 1.0, v68\n" \
"v_add_f32 v69, 1.0, v69\n" \
"v_mfma_f32_16x16x32_bf16 v[52:55], a[56:59], v[36:39], v[52:55]\n" \
"ds_read_b128 v[24:27], v10 offset:33792\n" \
"v_add_f32_e64 v70, 1.0, v70\n" \
"v_mfma_f32_16x16x32_bf16 v[56:59], a[184:187], v[32:35], v[56:59]\n" \
"ds_read_b128 v[28:31], v10 offset:50176\n" \
"v_add_f32_e64 v71, 1.0, v71\n" \
"v_mfma_f32_16x16x32_bf16 v[60:63], a[184:187], v[36:39], v[60:63]\n" \
"s_waitcnt lgkmcnt(4)\n" \
"v_rcp_f32 v68, v68\n" \
"v_mfma_f32_16x16x32_bf16 v[48:51], a[60:63], v[40:43], v[48:51]\n" \
"v_rcp_f32_e64 v69, v69\n" \
"v_mfma_f32_16x16x32_bf16 v[52:55], a[60:63], v[44:47], v[52:55]\n" \
"ds_read_b128 v[32:35], v10 offset:34816\n" \
"v_mfma_f32_16x16x32_bf16 v[56:59], a[188:191], v[40:43], v[56:59]\n" \
"ds_read_b128 v[36:39], v10 offset:51200\n" \
"v_mfma_f32_16x16x32_bf16 v[60:63], a[188:191], v[44:47], v[60:63]\n" \
"s_waitcnt lgkmcnt(4)\n" \
"v_rcp_f32 v70, v70\n" \
"v_mfma_f32_16x16x32_bf16 v[48:51], a[64:67], v[16:19], v[48:51]\n" \
"s_min_u32 s61, s56, s60\n" \
"s_add_u32 s61, s61, 16384\n" \
"s_add_u32 s56, s56, 0x10000\n" \
"s_add_u32 m0, s55, 98304\n" \
"s_nop 0\n" \
"buffer_load_dwordx4 v4, s[36:39], s61 offen offset:0 sc1 lds\n" \
"v_mfma_f32_16x16x32_bf16 v[52:55], a[64:67], v[20:23], v[52:55]\n" \
"ds_read_b128 v[40:43], v10 offset:35840\n" \
"v_mfma_f32_16x16x32_bf16 v[56:59], a[192:195], v[16:19], v[56:59]\n" \
"ds_read_b128 v[44:47], v10 offset:52224\n" \
"v_mfma_f32_16x16x32_bf16 v[60:63], a[192:195], v[20:23], v[60:63]\n" \
"s_waitcnt lgkmcnt(4)\n" \
"v_rcp_f32 v71, v71\n" \
"v_mfma_f32_16x16x32_bf16 v[48:51], a[68:71], v[24:27], v[48:51]\n" \
"buffer_load_dwordx4 v4, s[36:39], s61 offen offset:1024 sc1 lds\n" \
"v_mfma_f32_16x16x32_bf16 v[52:55], a[68:71], v[28:31], v[52:55]\n" \
"ds_read_b128 v[16:19], v10 offset:36864\n" \
"v_mul_f32_e64 v68, v68, v76\n" \
"v_mfma_f32_16x16x32_bf16 v[56:59], a[196:199], v[24:27], v[56:59]\n" \
"ds_read_b128 v[20:23], v10 offset:53248\n" \
"v_mul_f32_e64 v69, v69, v77\n" \
"v_mfma_f32_16x16x32_bf16 v[60:63], a[196:199], v[28:31], v[60:63]\n" \
"s_waitcnt lgkmcnt(4)\n" \
"v_mul_f32 v70, v70, v78\n" \
"v_mul_f32_e64 v71, v71, v79\n" \
"v_mfma_f32_16x16x32_bf16 v[48:51], a[72:75], v[32:35], v[48:51]\n" \
"v_cvt_pk_bf16_f32 v82, v68, v69\n" \
"v_cvt_pk_bf16_f32 v83, v70, v71\n" \
"v_mfma_f32_16x16x32_bf16 v[52:55], a[72:75], v[36:39], v[52:55]\n" \
"ds_read_b128 v[24:27], v10 offset:37888\n" \
"s_nop 0\n" \
"v_mfma_f32_16x16x32_bf16 v[56:59], a[200:203], v[32:35], v[56:59]\n" \
"ds_read_b128 v[28:31], v10 offset:54272\n" \
"s_nop 0\n" \
"v_mfma_f32_16x16x32_bf16 v[60:63], a[200:203], v[36:39], v[60:63]\n" \
"s_waitcnt lgkmcnt(4)\n" \
"v_permlane16_swap_b32 v80, v82\n" \
"v_permlane16_swap_b32 v81, v83\n" \
"v_mfma_f32_16x16x32_bf16 v[48:51], a[76:79], v[40:43], v[48:51]\n" \
"buffer_load_dwordx4 v4, s[36:39], s61 offen offset:2048 sc1 lds\n" \
"v_mfma_f32_16x16x32_bf16 v[52:55], a[76:79], v[44:47], v[52:55]\n" \
"ds_read_b128 v[32:35], v10 offset:38912\n" \
"buffer_store_dwordx4 v[80:83], v5, s[36:39], s57 offen sc1\n" \
"v_mfma_f32_16x16x32_bf16 v[56:59], a[204:207], v[40:43], v[56:59]\n" \
"ds_read_b128 v[36:39], v10 offset:55296\n" \
"v_mfma_f32_16x16x32_bf16 v[60:63], a[204:207], v[44:47], v[60:63]\n" \
"s_waitcnt lgkmcnt(4)\n" \
"v_mfma_f32_16x16x32_bf16 v[48:51], a[80:83], v[16:19], v[48:51]\n" \
"buffer_load_dwordx4 v4, s[36:39], s61 offen offset:3072 sc1 lds\n" \
"v_mfma_f32_16x16x32_bf16 v[52:55], a[80:83], v[20:23], v[52:55]\n" \
"ds_read_b128 v[40:43], v10 offset:39936\n" \
"v_mfma_f32_16x16x32_bf16 v[56:59], a[208:211], v[16:19], v[56:59]\n" \
"ds_read_b128 v[44:47], v10 offset:56320\n" \
"v_mfma_f32_16x16x32_bf16 v[60:63], a[208:211], v[20:23], v[60:63]\n" \
"s_waitcnt lgkmcnt(4)\n" \
"v_mfma_f32_16x16x32_bf16 v[48:51], a[84:87], v[24:27], v[48:51]\n" \
"v_mfma_f32_16x16x32_bf16 v[52:55], a[84:87], v[28:31], v[52:55]\n" \
"ds_read_b128 v[16:19], v10 offset:40960\n" \
"v_mfma_f32_16x16x32_bf16 v[56:59], a[212:215], v[24:27], v[56:59]\n" \
"ds_read_b128 v[20:23], v10 offset:57344\n" \
"v_mfma_f32_16x16x32_bf16 v[60:63], a[212:215], v[28:31], v[60:63]\n" \
"s_waitcnt lgkmcnt(4)\n" \
"v_mfma_f32_16x16x32_bf16 v[48:51], a[88:91], v[32:35], v[48:51]\n" \
"s_add_u32 m0, s55, 102400\n" \
"s_add_u32 s61, s61, 4096\n" \
"buffer_load_dwordx4 v4, s[36:39], s61 offen offset:0 sc1 lds\n" \
"v_mfma_f32_16x16x32_bf16 v[52:55], a[88:91], v[36:39], v[52:55]\n" \
"ds_read_b128 v[24:27], v10 offset:41984\n" \
"v_mfma_f32_16x16x32_bf16 v[56:59], a[216:219], v[32:35], v[56:59]\n" \
"ds_read_b128 v[28:31], v10 offset:58368\n" \
"v_mfma_f32_16x16x32_bf16 v[60:63], a[216:219], v[36:39], v[60:63]\n" \
"s_waitcnt lgkmcnt(4)\n" \
"v_mfma_f32_16x16x32_bf16 v[48:51], a[92:95], v[40:43], v[48:51]\n" \
"buffer_load_dwordx4 v4, s[36:39], s61 offen offset:1024 sc1 lds\n" \
"v_mfma_f32_16x16x32_bf16 v[52:55], a[92:95], v[44:47], v[52:55]\n" \
"ds_read_b128 v[32:35], v10 offset:43008\n" \
"v_mfma_f32_16x16x32_bf16 v[56:59], a[220:223], v[40:43], v[56:59]\n" \
"ds_read_b128 v[36:39], v10 offset:59392\n" \
"v_mfma_f32_16x16x32_bf16 v[60:63], a[220:223], v[44:47], v[60:63]\n" \
"s_waitcnt lgkmcnt(4)\n" \
"v_mfma_f32_16x16x32_bf16 v[48:51], a[96:99], v[16:19], v[48:51]\n" \
"v_mfma_f32_16x16x32_bf16 v[52:55], a[96:99], v[20:23], v[52:55]\n" \
"ds_read_b128 v[40:43], v10 offset:44032\n" \
"v_mfma_f32_16x16x32_bf16 v[56:59], a[224:227], v[16:19], v[56:59]\n" \
"ds_read_b128 v[44:47], v10 offset:60416\n" \
"v_mfma_f32_16x16x32_bf16 v[60:63], a[224:227], v[20:23], v[60:63]\n" \
"s_waitcnt lgkmcnt(4)\n" \
"v_mfma_f32_16x16x32_bf16 v[48:51], a[100:103], v[24:27], v[48:51]\n" \
"buffer_load_dwordx4 v4, s[36:39], s61 offen offset:2048 sc1 lds\n" \
"v_mfma_f32_16x16x32_bf16 v[52:55], a[100:103], v[28:31], v[52:55]\n" \
"ds_read_b128 v[16:19], v10 offset:45056\n" \
"v_mfma_f32_16x16x32_bf16 v[56:59], a[228:231], v[24:27], v[56:59]\n" \
"ds_read_b128 v[20:23], v10 offset:61440\n" \
"v_mfma_f32_16x16x32_bf16 v[60:63], a[228:231], v[28:31], v[60:63]\n" \
"s_waitcnt lgkmcnt(4)\n" \
"v_mfma_f32_16x16x32_bf16 v[48:51], a[104:107], v[32:35], v[48:51]\n" \
"buffer_load_dwordx4 v4, s[36:39], s61 offen offset:3072 sc1 lds\n" \
"v_mfma_f32_16x16x32_bf16 v[52:55], a[104:107], v[36:39], v[52:55]\n" \
"ds_read_b128 v[24:27], v10 offset:46080\n" \
"v_mfma_f32_16x16x32_bf16 v[56:59], a[232:235], v[32:35], v[56:59]\n" \
"ds_read_b128 v[28:31], v10 offset:62464\n" \
"v_mfma_f32_16x16x32_bf16 v[60:63], a[232:235], v[36:39], v[60:63]\n" \
"s_waitcnt lgkmcnt(4)\n" \
"v_mfma_f32_16x16x32_bf16 v[48:51], a[108:111], v[40:43], v[48:51]\n" \
"v_mfma_f32_16x16x32_bf16 v[52:55], a[108:111], v[44:47], v[52:55]\n" \
"ds_read_b128 v[32:35], v10 offset:47104\n" \
"v_mfma_f32_16x16x32_bf16 v[56:59], a[236:239], v[40:43], v[56:59]\n" \
"ds_read_b128 v[36:39], v10 offset:63488\n" \
"v_mfma_f32_16x16x32_bf16 v[60:63], a[236:239], v[44:47], v[60:63]\n" \
"s_waitcnt lgkmcnt(4)\n" \
"v_mfma_f32_16x16x32_bf16 v[48:51], a[112:115], v[16:19], v[48:51]\n" \
"v_mfma_f32_16x16x32_bf16 v[52:55], a[112:115], v[20:23], v[52:55]\n" \
"ds_read_b128 v[40:43], v10 offset:48128\n" \
"v_mfma_f32_16x16x32_bf16 v[56:59], a[240:243], v[16:19], v[56:59]\n" \
"ds_read_b128 v[44:47], v10 offset:64512\n" \
"v_mfma_f32_16x16x32_bf16 v[60:63], a[240:243], v[20:23], v[60:63]\n" \
"s_waitcnt lgkmcnt(4)\n" \
"v_mfma_f32_16x16x32_bf16 v[48:51], a[116:119], v[24:27], v[48:51]\n" \
"s_waitcnt vmcnt(10)\n" \
"s_barrier\n" \
"v_mfma_f32_16x16x32_bf16 v[52:55], a[116:119], v[28:31], v[52:55]\n" \
"ds_read_b128 v[16:19], v12 offset:0\n" \
"v_mfma_f32_16x16x32_bf16 v[56:59], a[244:247], v[24:27], v[56:59]\n" \
"ds_read_b128 v[20:23], v12 offset:16384\n" \
"v_mfma_f32_16x16x32_bf16 v[60:63], a[244:247], v[28:31], v[60:63]\n" \
"s_waitcnt lgkmcnt(4)\n" \
"v_mfma_f32_16x16x32_bf16 v[48:51], a[120:123], v[32:35], v[48:51]\n" \
"v_cmp_gt_u32_e64 vcc, 16, v15\n" \
"v_mfma_f32_16x16x32_bf16 v[52:55], a[120:123], v[36:39], v[52:55]\n" \
"ds_read_b128 v[24:27], v12 offset:1024\n" \
"v_mfma_f32_16x16x32_bf16 v[56:59], a[248:251], v[32:35], v[56:59]\n" \
"ds_read_b128 v[28:31], v12 offset:17408\n" \
"v_mfma_f32_16x16x32_bf16 v[60:63], a[248:251], v[36:39], v[60:63]\n" \
"s_waitcnt lgkmcnt(4)\n" \
"v_mfma_f32_16x16x32_bf16 v[48:51], a[124:127], v[40:43], v[48:51]\n" \
"s_cbranch_vccnz .Lp1g1_m0_%=\n" \
".Lp1g1_m0k_%=:\n" \
"v_mfma_f32_16x16x32_bf16 v[52:55], a[124:127], v[44:47], v[52:55]\n" \
"ds_read_b128 v[32:35], v12 offset:2048\n" \
"v_mfma_f32_16x16x32_bf16 v[56:59], a[252:255], v[40:43], v[56:59]\n" \
"ds_read_b128 v[36:39], v12 offset:18432\n" \
"v_mfma_f32_16x16x32_bf16 v[60:63], a[252:255], v[44:47], v[60:63]\n" \
"s_waitcnt lgkmcnt(4)\n" \
"s_add_u32 s57, s57, s49\n" \
"s_add_u32 s58, s58, s49\n" \
"s_add_u32 s59, s59, 1\n" \
"v_mov_b32 v5, v7\n" \
"v_mov_b32 v6, v11\n" \
"s_cmp_ge_u32 s59, s47\n" \
"s_cbranch_scc1 .Lp1g1_out0_%=\n" \
"v_mfma_f32_16x16x32_bf16 v[64:67], a[0:3], v[16:19], 0\n" \
"s_min_u32 s61, s56, s60\n" \
"s_add_u32 m0, s55, 0\n" \
"s_nop 0\n" \
"buffer_load_dwordx4 v4, s[36:39], s61 offen offset:0 sc1 lds\n" \
"v_add_u32_e64 v13, -1, s59\n" \
"v_mfma_f32_16x16x32_bf16 v[68:71], a[0:3], v[20:23], 0\n" \
"ds_read_b128 v[40:43], v12 offset:3072\n" \
"s_nop 0\n" \
"v_mfma_f32_16x16x32_bf16 v[72:75], a[128:131], v[16:19], 0\n" \
"ds_read_b128 v[44:47], v12 offset:19456\n" \
"s_nop 0\n" \
"v_mfma_f32_16x16x32_bf16 v[76:79], a[128:131], v[20:23], 0\n" \
"s_waitcnt lgkmcnt(4)\n" \
"v_mul_f32 v48, 0xbfb8aa3b, v48\n" \
"v_mul_f32 v49, 0xbfb8aa3b, v49\n" \
"v_mfma_f32_16x16x32_bf16 v[64:67], a[4:7], v[24:27], v[64:67]\n" \
"buffer_load_dwordx4 v4, s[36:39], s61 offen offset:1024 sc1 lds\n" \
"v_mfma_f32_16x16x32_bf16 v[68:71], a[4:7], v[28:31], v[68:71]\n" \
"ds_read_b128 v[16:19], v12 offset:4096\n" \
"v_mul_f32 v50, 0xbfb8aa3b, v50\n" \
"v_mfma_f32_16x16x32_bf16 v[72:75], a[132:135], v[24:27], v[72:75]\n" \
"ds_read_b128 v[20:23], v12 offset:20480\n" \
"v_mul_f32 v51, 0xbfb8aa3b, v51\n" \
"v_mfma_f32_16x16x32_bf16 v[76:79], a[132:135], v[28:31], v[76:79]\n" \
"s_waitcnt lgkmcnt(4)\n" \
"v_exp_f32 v48, v48\n" \
"v_mfma_f32_16x16x32_bf16 v[64:67], a[8:11], v[32:35], v[64:67]\n" \
"s_add_u32 s63, s59, 2\n" \
"s_min_u32 s63, s63, 63\n" \
"v_mfma_f32_16x16x32_bf16 v[68:71], a[8:11], v[36:39], v[68:71]\n" \
"ds_read_b128 v[24:27], v12 offset:5120\n" \
"s_lshr_b32 s63, s63, 3\n" \
"s_lshl_b32 s63, s63, 7\n" \
"v_mfma_f32_16x16x32_bf16 v[72:75], a[136:139], v[32:35], v[72:75]\n" \
"ds_read_b128 v[28:31], v12 offset:21504\n" \
"v_mfma_f32_16x16x32_bf16 v[76:79], a[136:139], v[36:39], v[76:79]\n" \
"s_waitcnt lgkmcnt(4)\n" \
"v_exp_f32 v49, v49\n" \
"v_mfma_f32_16x16x32_bf16 v[64:67], a[12:15], v[40:43], v[64:67]\n" \
"buffer_load_dwordx4 v4, s[36:39], s61 offen offset:2048 sc1 lds\n" \
"v_mfma_f32_16x16x32_bf16 v[68:71], a[12:15], v[44:47], v[68:71]\n" \
"ds_read_b128 v[32:35], v12 offset:6144\n" \
"v_mfma_f32_16x16x32_bf16 v[72:75], a[140:143], v[40:43], v[72:75]\n" \
"ds_read_b128 v[36:39], v12 offset:22528\n" \
"v_mfma_f32_16x16x32_bf16 v[76:79], a[140:143], v[44:47], v[76:79]\n" \
"s_waitcnt lgkmcnt(4)\n" \
"v_exp_f32 v50, v50\n" \
"v_mfma_f32_16x16x32_bf16 v[64:67], a[16:19], v[16:19], v[64:67]\n" \
"buffer_load_dwordx4 v4, s[36:39], s61 offen offset:3072 sc1 lds\n" \
"v_mfma_f32_16x16x32_bf16 v[68:71], a[16:19], v[20:23], v[68:71]\n" \
"ds_read_b128 v[40:43], v12 offset:7168\n" \
"v_mfma_f32_16x16x32_bf16 v[72:75], a[144:147], v[16:19], v[72:75]\n" \
"ds_read_b128 v[44:47], v12 offset:23552\n" \
"v_mfma_f32_16x16x32_bf16 v[76:79], a[144:147], v[20:23], v[76:79]\n" \
"s_waitcnt lgkmcnt(4)\n" \
"v_exp_f32 v51, v51\n" \
"v_mfma_f32_16x16x32_bf16 v[64:67], a[20:23], v[24:27], v[64:67]\n" \
"buffer_load_dword v15, v14, s[36:39], s63 offen sc1\n" \
"v_mfma_f32_16x16x32_bf16 v[68:71], a[20:23], v[28:31], v[68:71]\n" \
"ds_read_b128 v[16:19], v12 offset:8192\n" \
"v_add_f32_e64 v48, 1.0, v48\n" \
"v_mfma_f32_16x16x32_bf16 v[72:75], a[148:151], v[24:27], v[72:75]\n" \
"ds_read_b128 v[20:23], v12 offset:24576\n" \
"v_add_f32_e64 v49, 1.0, v49\n" \
"v_mfma_f32_16x16x32_bf16 v[76:79], a[148:151], v[28:31], v[76:79]\n" \
"s_waitcnt lgkmcnt(4)\n" \
"v_add_f32 v50, 1.0, v50\n" \
"v_add_f32_e64 v51, 1.0, v51\n" \
"v_mfma_f32_16x16x32_bf16 v[64:67], a[24:27], v[32:35], v[64:67]\n" \
"s_add_u32 m0, s55, 4096\n" \
"s_add_u32 s61, s61, 4096\n" \
"buffer_load_dwordx4 v4, s[36:39], s61 offen offset:0 sc1 lds\n" \
"v_mfma_f32_16x16x32_bf16 v[68:71], a[24:27], v[36:39], v[68:71]\n" \
"ds_read_b128 v[24:27], v12 offset:9216\n" \
"v_mfma_f32_16x16x32_bf16 v[72:75], a[152:155], v[32:35], v[72:75]\n" \
"ds_read_b128 v[28:31], v12 offset:25600\n" \
"v_mfma_f32_16x16x32_bf16 v[76:79], a[152:155], v[36:39], v[76:79]\n" \
"s_waitcnt lgkmcnt(4)\n" \
"v_rcp_f32 v48, v48\n" \
"v_mfma_f32_16x16x32_bf16 v[64:67], a[28:31], v[40:43], v[64:67]\n" \
"buffer_load_dwordx4 v4, s[36:39], s61 offen offset:1024 sc1 lds\n" \
"v_mfma_f32_16x16x32_bf16 v[68:71], a[28:31], v[44:47], v[68:71]\n" \
"ds_read_b128 v[32:35], v12 offset:10240\n" \
"v_mfma_f32_16x16x32_bf16 v[72:75], a[156:159], v[40:43], v[72:75]\n" \
"ds_read_b128 v[36:39], v12 offset:26624\n" \
"v_mfma_f32_16x16x32_bf16 v[76:79], a[156:159], v[44:47], v[76:79]\n" \
"s_waitcnt lgkmcnt(4)\n" \
"v_rcp_f32 v49, v49\n" \
"v_mfma_f32_16x16x32_bf16 v[64:67], a[32:35], v[16:19], v[64:67]\n" \
"v_rcp_f32_e64 v50, v50\n" \
"v_mfma_f32_16x16x32_bf16 v[68:71], a[32:35], v[20:23], v[68:71]\n" \
"ds_read_b128 v[40:43], v12 offset:11264\n" \
"v_mfma_f32_16x16x32_bf16 v[72:75], a[160:163], v[16:19], v[72:75]\n" \
"ds_read_b128 v[44:47], v12 offset:27648\n" \
"v_mfma_f32_16x16x32_bf16 v[76:79], a[160:163], v[20:23], v[76:79]\n" \
"s_waitcnt lgkmcnt(4)\n" \
"v_rcp_f32 v51, v51\n" \
"v_mfma_f32_16x16x32_bf16 v[64:67], a[36:39], v[24:27], v[64:67]\n" \
"buffer_load_dwordx4 v4, s[36:39], s61 offen offset:2048 sc1 lds\n" \
"v_mfma_f32_16x16x32_bf16 v[68:71], a[36:39], v[28:31], v[68:71]\n" \
"ds_read_b128 v[16:19], v12 offset:12288\n" \
"v_mul_f32_e64 v48, v48, v56\n" \
"v_mfma_f32_16x16x32_bf16 v[72:75], a[164:167], v[24:27], v[72:75]\n" \
"ds_read_b128 v[20:23], v12 offset:28672\n" \
"v_mul_f32_e64 v49, v49, v57\n" \
"v_mfma_f32_16x16x32_bf16 v[76:79], a[164:167], v[28:31], v[76:79]\n" \
"s_waitcnt lgkmcnt(4)\n" \
"v_mul_f32 v50, v50, v58\n" \
"v_mul_f32_e64 v51, v51, v59\n" \
"v_mfma_f32_16x16x32_bf16 v[64:67], a[40:43], v[32:35], v[64:67]\n" \
"buffer_load_dwordx4 v4, s[36:39], s61 offen offset:3072 sc1 lds\n" \
"v_mfma_f32_16x16x32_bf16 v[68:71], a[40:43], v[36:39], v[68:71]\n" \
"ds_read_b128 v[24:27], v12 offset:13312\n" \
"v_cvt_pk_bf16_f32 v80, v48, v49\n" \
"v_mfma_f32_16x16x32_bf16 v[72:75], a[168:171], v[32:35], v[72:75]\n" \
"ds_read_b128 v[28:31], v12 offset:29696\n" \
"v_cvt_pk_bf16_f32 v81, v50, v51\n" \
"v_mfma_f32_16x16x32_bf16 v[76:79], a[168:171], v[36:39], v[76:79]\n" \
"s_waitcnt lgkmcnt(4)\n" \
"v_mul_f32 v52, 0xbfb8aa3b, v52\n" \
"v_mul_f32 v53, 0xbfb8aa3b, v53\n" \
"v_mfma_f32_16x16x32_bf16 v[64:67], a[44:47], v[40:43], v[64:67]\n" \
"v_mul_f32 v54, 0xbfb8aa3b, v54\n" \
"v_mul_f32 v55, 0xbfb8aa3b, v55\n" \
"v_mfma_f32_16x16x32_bf16 v[68:71], a[44:47], v[44:47], v[68:71]\n" \
"ds_read_b128 v[32:35], v12 offset:14336\n" \
"v_mfma_f32_16x16x32_bf16 v[72:75], a[172:175], v[40:43], v[72:75]\n" \
"ds_read_b128 v[36:39], v12 offset:30720\n" \
"v_mfma_f32_16x16x32_bf16 v[76:79], a[172:175], v[44:47], v[76:79]\n" \
"s_waitcnt lgkmcnt(4)\n" \
"v_exp_f32_e64 v52, v52\n" \
"v_mfma_f32_16x16x32_bf16 v[64:67], a[48:51], v[16:19], v[64:67]\n" \
"v_exp_f32_e64 v53, v53\n" \
"v_mfma_f32_16x16x32_bf16 v[68:71], a[48:51], v[20:23], v[68:71]\n" \
"ds_read_b128 v[40:43], v12 offset:15360\n" \
"v_mfma_f32_16x16x32_bf16 v[72:75], a[176:179], v[16:19], v[72:75]\n" \
"ds_read_b128 v[44:47], v12 offset:31744\n" \
"v_mfma_f32_16x16x32_bf16 v[76:79], a[176:179], v[20:23], v[76:79]\n" \
"s_waitcnt lgkmcnt(4)\n" \
"v_exp_f32 v54, v54\n" \
"v_mfma_f32_16x16x32_bf16 v[64:67], a[52:55], v[24:27], v[64:67]\n" \
"s_waitcnt vmcnt(9)\n" \
"s_barrier\n" \
"v_mfma_f32_16x16x32_bf16 v[68:71], a[52:55], v[28:31], v[68:71]\n" \
"ds_read_b128 v[16:19], v12 offset:32768\n" \
"v_mfma_f32_16x16x32_bf16 v[72:75], a[180:183], v[24:27], v[72:75]\n" \
"ds_read_b128 v[20:23], v12 offset:49152\n" \
"v_mfma_f32_16x16x32_bf16 v[76:79], a[180:183], v[28:31], v[76:79]\n" \
"s_waitcnt lgkmcnt(4)\n" \
"buffer_store_dword v13, v6, s[36:39], 0 offen sc1\n" \
"v_exp_f32 v55, v55\n" \
"v_mfma_f32_16x16x32_bf16 v[64:67], a[56:59], v[32:35], v[64:67]\n" \
"v_add_f32 v52, 1.0, v52\n" \
"v_add_f32 v53, 1.0, v53\n" \
"v_mfma_f32_16x16x32_bf16 v[68:71], a[56:59], v[36:39], v[68:71]\n" \
"ds_read_b128 v[24:27], v12 offset:33792\n" \
"v_add_f32_e64 v54, 1.0, v54\n" \
"v_mfma_f32_16x16x32_bf16 v[72:75], a[184:187], v[32:35], v[72:75]\n" \
"ds_read_b128 v[28:31], v12 offset:50176\n" \
"v_add_f32_e64 v55, 1.0, v55\n" \
"v_mfma_f32_16x16x32_bf16 v[76:79], a[184:187], v[36:39], v[76:79]\n" \
"s_waitcnt lgkmcnt(4)\n" \
"v_rcp_f32 v52, v52\n" \
"v_mfma_f32_16x16x32_bf16 v[64:67], a[60:63], v[40:43], v[64:67]\n" \
"v_rcp_f32_e64 v53, v53\n" \
"v_mfma_f32_16x16x32_bf16 v[68:71], a[60:63], v[44:47], v[68:71]\n" \
"ds_read_b128 v[32:35], v12 offset:34816\n" \
"v_mfma_f32_16x16x32_bf16 v[72:75], a[188:191], v[40:43], v[72:75]\n" \
"ds_read_b128 v[36:39], v12 offset:51200\n" \
"v_mfma_f32_16x16x32_bf16 v[76:79], a[188:191], v[44:47], v[76:79]\n" \
"s_waitcnt lgkmcnt(4)\n" \
"v_rcp_f32 v54, v54\n" \
"v_mfma_f32_16x16x32_bf16 v[64:67], a[64:67], v[16:19], v[64:67]\n" \
"s_min_u32 s61, s56, s60\n" \
"s_add_u32 s61, s61, 16384\n" \
"s_add_u32 s56, s56, 0x10000\n" \
"s_add_u32 m0, s55, 32768\n" \
"s_nop 0\n" \
"buffer_load_dwordx4 v4, s[36:39], s61 offen offset:0 sc1 lds\n" \
"v_mfma_f32_16x16x32_bf16 v[68:71], a[64:67], v[20:23], v[68:71]\n" \
"ds_read_b128 v[40:43], v12 offset:35840\n" \
"v_mfma_f32_16x16x32_bf16 v[72:75], a[192:195], v[16:19], v[72:75]\n" \
"ds_read_b128 v[44:47], v12 offset:52224\n" \
"v_mfma_f32_16x16x32_bf16 v[76:79], a[192:195], v[20:23], v[76:79]\n" \
"s_waitcnt lgkmcnt(4)\n" \
"v_rcp_f32 v55, v55\n" \
"v_mfma_f32_16x16x32_bf16 v[64:67], a[68:71], v[24:27], v[64:67]\n" \
"buffer_load_dwordx4 v4, s[36:39], s61 offen offset:1024 sc1 lds\n" \
"v_mfma_f32_16x16x32_bf16 v[68:71], a[68:71], v[28:31], v[68:71]\n" \
"ds_read_b128 v[16:19], v12 offset:36864\n" \
"v_mul_f32_e64 v52, v52, v60\n" \
"v_mfma_f32_16x16x32_bf16 v[72:75], a[196:199], v[24:27], v[72:75]\n" \
"ds_read_b128 v[20:23], v12 offset:53248\n" \
"v_mul_f32_e64 v53, v53, v61\n" \
"v_mfma_f32_16x16x32_bf16 v[76:79], a[196:199], v[28:31], v[76:79]\n" \
"s_waitcnt lgkmcnt(4)\n" \
"v_mul_f32 v54, v54, v62\n" \
"v_mul_f32_e64 v55, v55, v63\n" \
"v_mfma_f32_16x16x32_bf16 v[64:67], a[72:75], v[32:35], v[64:67]\n" \
"v_cvt_pk_bf16_f32 v82, v52, v53\n" \
"v_cvt_pk_bf16_f32 v83, v54, v55\n" \
"v_mfma_f32_16x16x32_bf16 v[68:71], a[72:75], v[36:39], v[68:71]\n" \
"ds_read_b128 v[24:27], v12 offset:37888\n" \
"s_nop 0\n" \
"v_mfma_f32_16x16x32_bf16 v[72:75], a[200:203], v[32:35], v[72:75]\n" \
"ds_read_b128 v[28:31], v12 offset:54272\n" \
"s_nop 0\n" \
"v_mfma_f32_16x16x32_bf16 v[76:79], a[200:203], v[36:39], v[76:79]\n" \
"s_waitcnt lgkmcnt(4)\n" \
"v_permlane16_swap_b32 v80, v82\n" \
"v_permlane16_swap_b32 v81, v83\n" \
"v_mfma_f32_16x16x32_bf16 v[64:67], a[76:79], v[40:43], v[64:67]\n" \
"buffer_load_dwordx4 v4, s[36:39], s61 offen offset:2048 sc1 lds\n" \
"v_mfma_f32_16x16x32_bf16 v[68:71], a[76:79], v[44:47], v[68:71]\n" \
"ds_read_b128 v[32:35], v12 offset:38912\n" \
"buffer_store_dwordx4 v[80:83], v5, s[36:39], s57 offen sc1\n" \
"v_mfma_f32_16x16x32_bf16 v[72:75], a[204:207], v[40:43], v[72:75]\n" \
"ds_read_b128 v[36:39], v12 offset:55296\n" \
"v_mfma_f32_16x16x32_bf16 v[76:79], a[204:207], v[44:47], v[76:79]\n" \
"s_waitcnt lgkmcnt(4)\n" \
"v_mfma_f32_16x16x32_bf16 v[64:67], a[80:83], v[16:19], v[64:67]\n" \
"buffer_load_dwordx4 v4, s[36:39], s61 offen offset:3072 sc1 lds\n" \
"v_mfma_f32_16x16x32_bf16 v[68:71], a[80:83], v[20:23], v[68:71]\n" \
"ds_read_b128 v[40:43], v12 offset:39936\n" \
"v_mfma_f32_16x16x32_bf16 v[72:75], a[208:211], v[16:19], v[72:75]\n" \
"ds_read_b128 v[44:47], v12 offset:56320\n" \
"v_mfma_f32_16x16x32_bf16 v[76:79], a[208:211], v[20:23], v[76:79]\n" \
"s_waitcnt lgkmcnt(4)\n" \
"v_mfma_f32_16x16x32_bf16 v[64:67], a[84:87], v[24:27], v[64:67]\n" \
"v_mfma_f32_16x16x32_bf16 v[68:71], a[84:87], v[28:31], v[68:71]\n" \
"ds_read_b128 v[16:19], v12 offset:40960\n" \
"v_mfma_f32_16x16x32_bf16 v[72:75], a[212:215], v[24:27], v[72:75]\n" \
"ds_read_b128 v[20:23], v12 offset:57344\n" \
"v_mfma_f32_16x16x32_bf16 v[76:79], a[212:215], v[28:31], v[76:79]\n" \
"s_waitcnt lgkmcnt(4)\n" \
"v_mfma_f32_16x16x32_bf16 v[64:67], a[88:91], v[32:35], v[64:67]\n" \
"s_add_u32 m0, s55, 36864\n" \
"s_add_u32 s61, s61, 4096\n" \
"buffer_load_dwordx4 v4, s[36:39], s61 offen offset:0 sc1 lds\n" \
"v_mfma_f32_16x16x32_bf16 v[68:71], a[88:91], v[36:39], v[68:71]\n" \
"ds_read_b128 v[24:27], v12 offset:41984\n" \
"v_mfma_f32_16x16x32_bf16 v[72:75], a[216:219], v[32:35], v[72:75]\n" \
"ds_read_b128 v[28:31], v12 offset:58368\n" \
"v_mfma_f32_16x16x32_bf16 v[76:79], a[216:219], v[36:39], v[76:79]\n" \
"s_waitcnt lgkmcnt(4)\n" \
"v_mfma_f32_16x16x32_bf16 v[64:67], a[92:95], v[40:43], v[64:67]\n" \
"buffer_load_dwordx4 v4, s[36:39], s61 offen offset:1024 sc1 lds\n" \
"v_mfma_f32_16x16x32_bf16 v[68:71], a[92:95], v[44:47], v[68:71]\n" \
"ds_read_b128 v[32:35], v12 offset:43008\n" \
"v_mfma_f32_16x16x32_bf16 v[72:75], a[220:223], v[40:43], v[72:75]\n" \
"ds_read_b128 v[36:39], v12 offset:59392\n" \
"v_mfma_f32_16x16x32_bf16 v[76:79], a[220:223], v[44:47], v[76:79]\n" \
"s_waitcnt lgkmcnt(4)\n" \
"v_mfma_f32_16x16x32_bf16 v[64:67], a[96:99], v[16:19], v[64:67]\n" \
"v_mfma_f32_16x16x32_bf16 v[68:71], a[96:99], v[20:23], v[68:71]\n" \
"ds_read_b128 v[40:43], v12 offset:44032\n" \
"v_mfma_f32_16x16x32_bf16 v[72:75], a[224:227], v[16:19], v[72:75]\n" \
"ds_read_b128 v[44:47], v12 offset:60416\n" \
"v_mfma_f32_16x16x32_bf16 v[76:79], a[224:227], v[20:23], v[76:79]\n" \
"s_waitcnt lgkmcnt(4)\n" \
"v_mfma_f32_16x16x32_bf16 v[64:67], a[100:103], v[24:27], v[64:67]\n" \
"buffer_load_dwordx4 v4, s[36:39], s61 offen offset:2048 sc1 lds\n" \
"v_mfma_f32_16x16x32_bf16 v[68:71], a[100:103], v[28:31], v[68:71]\n" \
"ds_read_b128 v[16:19], v12 offset:45056\n" \
"v_mfma_f32_16x16x32_bf16 v[72:75], a[228:231], v[24:27], v[72:75]\n" \
"ds_read_b128 v[20:23], v12 offset:61440\n" \
"v_mfma_f32_16x16x32_bf16 v[76:79], a[228:231], v[28:31], v[76:79]\n" \
"s_waitcnt lgkmcnt(4)\n" \
"v_mfma_f32_16x16x32_bf16 v[64:67], a[104:107], v[32:35], v[64:67]\n" \
"buffer_load_dwordx4 v4, s[36:39], s61 offen offset:3072 sc1 lds\n" \
"v_mfma_f32_16x16x32_bf16 v[68:71], a[104:107], v[36:39], v[68:71]\n" \
"ds_read_b128 v[24:27], v12 offset:46080\n" \
"v_mfma_f32_16x16x32_bf16 v[72:75], a[232:235], v[32:35], v[72:75]\n" \
"ds_read_b128 v[28:31], v12 offset:62464\n" \
"v_mfma_f32_16x16x32_bf16 v[76:79], a[232:235], v[36:39], v[76:79]\n" \
"s_waitcnt lgkmcnt(4)\n" \
"v_mfma_f32_16x16x32_bf16 v[64:67], a[108:111], v[40:43], v[64:67]\n" \
"v_mfma_f32_16x16x32_bf16 v[68:71], a[108:111], v[44:47], v[68:71]\n" \
"ds_read_b128 v[32:35], v12 offset:47104\n" \
"v_mfma_f32_16x16x32_bf16 v[72:75], a[236:239], v[40:43], v[72:75]\n" \
"ds_read_b128 v[36:39], v12 offset:63488\n" \
"v_mfma_f32_16x16x32_bf16 v[76:79], a[236:239], v[44:47], v[76:79]\n" \
"s_waitcnt lgkmcnt(4)\n" \
"v_mfma_f32_16x16x32_bf16 v[64:67], a[112:115], v[16:19], v[64:67]\n" \
"v_mfma_f32_16x16x32_bf16 v[68:71], a[112:115], v[20:23], v[68:71]\n" \
"ds_read_b128 v[40:43], v12 offset:48128\n" \
"v_mfma_f32_16x16x32_bf16 v[72:75], a[240:243], v[16:19], v[72:75]\n" \
"ds_read_b128 v[44:47], v12 offset:64512\n" \
"v_mfma_f32_16x16x32_bf16 v[76:79], a[240:243], v[20:23], v[76:79]\n" \
"s_waitcnt lgkmcnt(4)\n" \
"v_mfma_f32_16x16x32_bf16 v[64:67], a[116:119], v[24:27], v[64:67]\n" \
"s_waitcnt vmcnt(10)\n" \
"s_barrier\n" \
"v_mfma_f32_16x16x32_bf16 v[68:71], a[116:119], v[28:31], v[68:71]\n" \
"ds_read_b128 v[16:19], v10 offset:0\n" \
"v_mfma_f32_16x16x32_bf16 v[72:75], a[244:247], v[24:27], v[72:75]\n" \
"ds_read_b128 v[20:23], v10 offset:16384\n" \
"v_mfma_f32_16x16x32_bf16 v[76:79], a[244:247], v[28:31], v[76:79]\n" \
"s_waitcnt lgkmcnt(4)\n" \
"v_mfma_f32_16x16x32_bf16 v[64:67], a[120:123], v[32:35], v[64:67]\n" \
"v_cmp_gt_u32_e64 vcc, 16, v15\n" \
"v_mfma_f32_16x16x32_bf16 v[68:71], a[120:123], v[36:39], v[68:71]\n" \
"ds_read_b128 v[24:27], v10 offset:1024\n" \
"v_mfma_f32_16x16x32_bf16 v[72:75], a[248:251], v[32:35], v[72:75]\n" \
"ds_read_b128 v[28:31], v10 offset:17408\n" \
"v_mfma_f32_16x16x32_bf16 v[76:79], a[248:251], v[36:39], v[76:79]\n" \
"s_waitcnt lgkmcnt(4)\n" \
"v_mfma_f32_16x16x32_bf16 v[64:67], a[124:127], v[40:43], v[64:67]\n" \
"s_cbranch_vccnz .Lp1g1_m1_%=\n" \
".Lp1g1_m1k_%=:\n" \
"v_mfma_f32_16x16x32_bf16 v[68:71], a[124:127], v[44:47], v[68:71]\n" \
"ds_read_b128 v[32:35], v10 offset:2048\n" \
"v_mfma_f32_16x16x32_bf16 v[72:75], a[252:255], v[40:43], v[72:75]\n" \
"ds_read_b128 v[36:39], v10 offset:18432\n" \
"v_mfma_f32_16x16x32_bf16 v[76:79], a[252:255], v[44:47], v[76:79]\n" \
"s_waitcnt lgkmcnt(4)\n" \
"s_add_u32 s57, s57, s49\n" \
"s_add_u32 s58, s58, s49\n" \
"s_add_u32 s59, s59, 1\n" \
"v_mov_b32_e64 v5, v7\n" \
"v_mov_b32_e64 v6, v11\n" \
"s_cmp_ge_u32 s59, s47\n" \
"s_nop 0\n" \
"s_cbranch_scc0 .Lp1g1_loop_%=\n" \
".Lp1g1_out1_%=:\n" \
"s_nop 7\n" \
"s_nop 7\n" \
"v_mul_f32 v64, 0xbfb8aa3b, v64\n" \
"v_mul_f32 v65, 0xbfb8aa3b, v65\n" \
"v_mul_f32 v66, 0xbfb8aa3b, v66\n" \
"v_mul_f32 v67, 0xbfb8aa3b, v67\n" \
"v_exp_f32 v64, v64\n" \
"v_exp_f32 v65, v65\n" \
"v_exp_f32 v66, v66\n" \
"v_exp_f32 v67, v67\n" \
"v_add_f32 v64, 1.0, v64\n" \
"v_add_f32 v65, 1.0, v65\n" \
"v_add_f32 v66, 1.0, v66\n" \
"v_add_f32 v67, 1.0, v67\n" \
"v_rcp_f32 v64, v64\n" \
"v_rcp_f32 v65, v65\n" \
"v_rcp_f32 v66, v66\n" \
"v_rcp_f32 v67, v67\n" \
"v_mul_f32 v64, v64, v72\n" \
"v_mul_f32 v65, v65, v73\n" \
"v_mul_f32 v66, v66, v74\n" \
"v_mul_f32 v67, v67, v75\n" \
"v_cvt_pk_bf16_f32 v80, v64, v65\n" \
"v_cvt_pk_bf16_f32 v81, v66, v67\n" \
"v_mul_f32 v68, 0xbfb8aa3b, v68\n" \
"v_mul_f32 v69, 0xbfb8aa3b, v69\n" \
"v_mul_f32 v70, 0xbfb8aa3b, v70\n" \
"v_mul_f32 v71, 0xbfb8aa3b, v71\n" \
"v_exp_f32 v68, v68\n" \
"v_exp_f32 v69, v69\n" \
"v_exp_f32 v70, v70\n" \
"v_exp_f32 v71, v71\n" \
"v_add_f32 v68, 1.0, v68\n" \
"v_add_f32 v69, 1.0, v69\n" \
"v_add_f32 v70, 1.0, v70\n" \
"v_add_f32 v71, 1.0, v71\n" \
"v_rcp_f32 v68, v68\n" \
"v_rcp_f32 v69, v69\n" \
"v_rcp_f32 v70, v70\n" \
"v_rcp_f32 v71, v71\n" \
"v_mul_f32 v68, v68, v76\n" \
"v_mul_f32 v69, v69, v77\n" \
"v_mul_f32 v70, v70, v78\n" \
"v_mul_f32 v71, v71, v79\n" \
"v_cvt_pk_bf16_f32 v82, v68, v69\n" \
"v_cvt_pk_bf16_f32 v83, v70, v71\n" \
"s_nop 0\n" \
"s_nop 0\n" \
"v_permlane16_swap_b32 v80, v82\n" \
"v_permlane16_swap_b32 v81, v83\n" \
"buffer_store_dwordx4 v[80:83], v5, s[36:39], s57 offen sc1\n" \
"s_branch .Lp1g1_end_%=\n" \
".Lp1g1_out0_%=:\n" \
"s_nop 7\n" \
"s_nop 7\n" \
"v_mul_f32 v48, 0xbfb8aa3b, v48\n" \
"v_mul_f32 v49, 0xbfb8aa3b, v49\n" \
"v_mul_f32 v50, 0xbfb8aa3b, v50\n" \
"v_mul_f32 v51, 0xbfb8aa3b, v51\n" \
"v_exp_f32 v48, v48\n" \
"v_exp_f32 v49, v49\n" \
"v_exp_f32 v50, v50\n" \
"v_exp_f32 v51, v51\n" \
"v_add_f32 v48, 1.0, v48\n" \
"v_add_f32 v49, 1.0, v49\n" \
"v_add_f32 v50, 1.0, v50\n" \
"v_add_f32 v51, 1.0, v51\n" \
"v_rcp_f32 v48, v48\n" \
"v_rcp_f32 v49, v49\n" \
"v_rcp_f32 v50, v50\n" \
"v_rcp_f32 v51, v51\n" \
"v_mul_f32 v48, v48, v56\n" \
"v_mul_f32 v49, v49, v57\n" \
"v_mul_f32 v50, v50, v58\n" \
"v_mul_f32 v51, v51, v59\n" \
"v_cvt_pk_bf16_f32 v80, v48, v49\n" \
"v_cvt_pk_bf16_f32 v81, v50, v51\n" \
"v_mul_f32 v52, 0xbfb8aa3b, v52\n" \
"v_mul_f32 v53, 0xbfb8aa3b, v53\n" \
"v_mul_f32 v54, 0xbfb8aa3b, v54\n" \
"v_mul_f32 v55, 0xbfb8aa3b, v55\n" \
"v_exp_f32 v52, v52\n" \
"v_exp_f32 v53, v53\n" \
"v_exp_f32 v54, v54\n" \
"v_exp_f32 v55, v55\n" \
"v_add_f32 v52, 1.0, v52\n" \
"v_add_f32 v53, 1.0, v53\n" \
"v_add_f32 v54, 1.0, v54\n" \
"v_add_f32 v55, 1.0, v55\n" \
"v_rcp_f32 v52, v52\n" \
"v_rcp_f32 v53, v53\n" \
"v_rcp_f32 v54, v54\n" \
"v_rcp_f32 v55, v55\n" \
"v_mul_f32 v52, v52, v60\n" \
"v_mul_f32 v53, v53, v61\n" \
"v_mul_f32 v54, v54, v62\n" \
"v_mul_f32 v55, v55, v63\n" \
"v_cvt_pk_bf16_f32 v82, v52, v53\n" \
"v_cvt_pk_bf16_f32 v83, v54, v55\n" \
"s_nop 0\n" \
"s_nop 0\n" \
"v_permlane16_swap_b32 v80, v82\n" \
"v_permlane16_swap_b32 v81, v83\n" \
"buffer_store_dwordx4 v[80:83], v5, s[36:39], s57 offen sc1\n" \
".Lp1g1_end_%=:\n" \
"s_waitcnt vmcnt(0) lgkmcnt(0)\n" \
"s_barrier\n" \
"v_mov_b32 v13, s59\n" \
"buffer_store_dword v13, v6, s[36:39], 0 offen sc1\n" \
"s_waitcnt vmcnt(0) lgkmcnt(0)\n" \
"s_branch .Lp1g1_x_%=\n" \
".Lp1g1_l0_%=:\n" \
"s_nop 7\n" \
"s_nop 7\n" \
"s_waitcnt vmcnt(0) lgkmcnt(0)\n" \
"s_mov_b32 s62, 0\n" \
".Lp1g1_l0a_%=:\n" \
"buffer_load_dword v15, v14, s[36:39], s63 offen sc1\n" \
"s_waitcnt vmcnt(0)\n" \
"v_cmp_gt_u32 vcc, 16, v15\n" \
"s_nop 4\n" \
"s_cbranch_vccz .Lp1g1_l0k_%=\n" \
"s_sleep 4\n" \
"s_add_u32 s62, s62, 1\n" \
"s_cmp_lt_u32 s62, 0x10000\n" \
"s_cbranch_scc1 .Lp1g1_l0a_%=\n" \
"v_mov_b32 v15, 256\n" \
"buffer_store_dword v15, v15, s[36:39], 0 offen sc1\n" \
"s_waitcnt vmcnt(0) lgkmcnt(0)\n" \
"s_branch .Lp1g1_l0k_%=\n" \
".Lp1g1_l1_%=:\n" \
"s_nop 7\n" \
"s_nop 7\n" \
"s_waitcnt vmcnt(0) lgkmcnt(0)\n" \
"s_mov_b32 s62, 0\n" \
".Lp1g1_l1a_%=:\n" \
"buffer_load_dword v15, v14, s[36:39], s63 offen sc1\n" \
"s_waitcnt vmcnt(0)\n" \
"v_cmp_gt_u32 vcc, 16, v15\n" \
"s_nop 4\n" \
"s_cbranch_vccz .Lp1g1_l1k_%=\n" \
"s_sleep 4\n" \
"s_add_u32 s62, s62, 1\n" \
"s_cmp_lt_u32 s62, 0x10000\n" \
"s_cbranch_scc1 .Lp1g1_l1a_%=\n" \
"v_mov_b32 v15, 256\n" \
"buffer_store_dword v15, v15, s[36:39], 0 offen sc1\n" \
"s_waitcnt vmcnt(0) lgkmcnt(0)\n" \
"s_branch .Lp1g1_l1k_%=\n" \
".Lp1g1_m0_%=:\n" \
"s_nop 7\n" \
"s_nop 7\n" \
"s_waitcnt vmcnt(0) lgkmcnt(0)\n" \
"s_mov_b32 s62, 0\n" \
".Lp1g1_m0a_%=:\n" \
"buffer_load_dword v15, v14, s[36:39], s63 offen sc1\n" \
"s_waitcnt vmcnt(0)\n" \
"v_cmp_gt_u32 vcc, 16, v15\n" \
"s_nop 4\n" \
"s_cbranch_vccz .Lp1g1_m0k_%=\n" \
"s_sleep 4\n" \
"s_add_u32 s62, s62, 1\n" \
"s_cmp_lt_u32 s62, 0x10000\n" \
"s_cbranch_scc1 .Lp1g1_m0a_%=\n" \
"v_mov_b32 v15, 256\n" \
"buffer_store_dword v15, v15, s[36:39], 0 offen sc1\n" \
"s_waitcnt vmcnt(0) lgkmcnt(0)\n" \
"s_branch .Lp1g1_m0k_%=\n" \
".Lp1g1_m1_%=:\n" \
"s_nop 7\n" \
"s_nop 7\n" \
"s_waitcnt vmcnt(0) lgkmcnt(0)\n" \
"s_mov_b32 s62, 0\n" \
".Lp1g1_m1a_%=:\n" \
"buffer_load_dword v15, v14, s[36:39], s63 offen sc1\n" \
"s_waitcnt vmcnt(0)\n" \
"v_cmp_gt_u32 vcc, 16, v15\n" \
"s_nop 4\n" \
"s_cbranch_vccz .Lp1g1_m1k_%=\n" \
"s_sleep 4\n" \
"s_add_u32 s62, s62, 1\n" \
"s_cmp_lt_u32 s62, 0x10000\n" \
"s_cbranch_scc1 .Lp1g1_m1a_%=\n" \
"v_mov_b32 v15, 256\n" \
"buffer_store_dword v15, v15, s[36:39], 0 offen sc1\n" \
"s_waitcnt vmcnt(0) lgkmcnt(0)\n" \
"s_branch .Lp1g1_m1k_%=\n" \
".Lp1g1_x_%=:\n"
#define CLOB_P1 \
"a0", "a1", "a2", "a3", "a4", "a5", "a6", "a7", "a8", "a9", "a10", "a11", "a12", "a13", "a14", "a15", "a16", "a17", "a18", "a19", "a20", "a21", "a22", "a23", "a24", "a25", "a26", "a27", "a28", "a29", "a30", "a31", "a32", "a33", "a34", "a35", "a36", "a37", "a38", "a39", "a40", "a41", "a42", "a43", "a44", "a45", "a46", "a47", "a48", "a49", "a50", "a51", "a52", "a53", "a54", "a55", "a56", "a57", "a58", "a59", "a60", "a61", "a62", "a63", "a64", "a65", "a66", "a67", "a68", "a69", "a70", "a71", "a72", "a73", "a74", "a75", "a76", "a77", "a78", "a79", "a80", "a81", "a82", "a83", "a84", "a85", "a86", "a87", "a88", "a89", "a90", "a91", "a92", "a93", "a94", "a95", "a96", "a97", "a98", "a99", "a100", "a101", "a102", "a103", "a104", "a105", "a106", "a107", "a108", "a109", "a110", "a111", "a112", "a113", "a114", "a115", "a116", "a117", "a118", "a119", "a120", "a121", "a122", "a123", "a124", "a125", "a126", "a127", "a128", "a129", "a130", "a131", "a132", "a133", "a134", "a135", "a136", "a137", "a138", "a139", "a140", "a141", "a142", "a143", "a144", "a145", "a146", "a147", "a148", "a149", "a150", "a151", "a152", "a153", "a154", "a155", "a156", "a157", "a158", "a159", "a160", "a161", "a162", "a163", "a164", "a165", "a166", "a167", "a168", "a169", "a170", "a171", "a172", "a173", "a174", "a175", "a176", "a177", "a178", "a179", "a180", "a181", "a182", "a183", "a184", "a185", "a186", "a187", "a188", "a189", "a190", "a191", "a192", "a193", "a194", "a195", "a196", "a197", "a198", "a199", "a200", "a201", "a202", "a203", "a204", "a205", "a206", "a207", "a208", "a209", "a210", "a211", "a212", "a213", "a214", "a215", "a216", "a217", "a218", "a219", "a220", "a221", "a222", "a223", "a224", "a225", "a226", "a227", "a228", "a229", "a230", "a231", "a232", "a233", "a234", "a235", "a236", "a237", "a238", "a239", "a240", "a241", "a242", "a243", "a244", "a245", "a246", "a247", "a248", "a249", "a250", "a251", "a252", "a253", "a254", "a255", "s52", "s53", "s54", "s55", "s56", "s57", "s58", "s59", "s60", "s61", "s62", "s63", "v7", "v8", "v9", "v10", "v11", "v12", "v13", "v14", "v15", "v16", "v17", "v18", "v19", "v20", "v21", "v22", "v23", "v24", "v25", "v26", "v27", "v28", "v29", "v30", "v31", "v32", "v33", "v34", "v35", "v36", "v37", "v38", "v39", "v40", "v41", "v42", "v43", "v44", "v45", "v46", "v47", "v48", "v49", "v50", "v51", "v52", "v53", "v54", "v55", "v56", "v57", "v58", "v59", "v60", "v61", "v62", "v63", "v64", "v65", "v66", "v67", "v68", "v69", "v70", "v71", "v72", "v73", "v74", "v75", "v76", "v77", "v78", "v79", "v80", "v81", "v82", "v83", "v84", "v85", "v86", "v87", "vcc", "scc", "memory"
#define ASM_QK \
"s_nop 7\n" \
"s_nop 7\n" \
"v_add_u32 v36, 0x10000, v34\n" \
"v_add_u32 v38, 0x20000, v34\n" \
"v_add_u32 v37, 0x10000, v35\n" \
"v_add_u32 v39, 0x20000, v35\n" \
"s_lshl_b32 s62, s48, 12\n" \
"s_add_u32 m0, s62, 147456\n" \
"s_xor_b32 s54, s15, 524288\n" \
"buffer_load_dwordx4 v32, s[40:43], s54 offen nt lds\n" \
"s_add_u32 m0, s62, 148480\n" \
"s_xor_b32 s54, s15, 524288\n" \
"buffer_load_dwordx4 v33, s[40:43], s54 offen nt lds\n" \
"s_add_u32 m0, s62, 149504\n" \
"s_xor_b32 s54, s15, 655360\n" \
"buffer_load_dwordx4 v32, s[40:43], s54 offen nt lds\n" \
"s_add_u32 m0, s62, 150528\n" \
"s_xor_b32 s54, s15, 655360\n" \
"buffer_load_dwordx4 v33, s[40:43], s54 offen nt lds\n" \
"s_add_u32 m0, s62, 131072\n" \
"s_xor_b32 s54, s15, 786432\n" \
"buffer_load_dwordx4 v32, s[40:43], s54 offen nt lds\n" \
"s_add_u32 m0, s62, 132096\n" \
"s_xor_b32 s54, s15, 786432\n" \
"buffer_load_dwordx4 v33, s[40:43], s54 offen nt lds\n" \
"s_add_u32 m0, s62, 133120\n" \
"s_xor_b32 s54, s15, 917504\n" \
"buffer_load_dwordx4 v32, s[40:43], s54 offen nt lds\n" \
"s_add_u32 m0, s62, 134144\n" \
"s_xor_b32 s54, s15, 917504\n" \
"buffer_load_dwordx4 v33, s[40:43], s54 offen nt lds\n" \
"s_add_u32 m0, s62, 114688\n" \
"s_xor_b32 s54, s15, 1572864\n" \
"buffer_load_dwordx4 v32, s[40:43], s54 offen nt lds\n" \
"s_add_u32 m0, s62, 115712\n" \
"s_xor_b32 s54, s15, 1572864\n" \
"buffer_load_dwordx4 v33, s[40:43], s54 offen nt lds\n" \
"s_add_u32 m0, s62, 116736\n" \
"s_xor_b32 s54, s15, 1703936\n" \
"buffer_load_dwordx4 v32, s[40:43], s54 offen nt lds\n" \
"s_add_u32 m0, s62, 117760\n" \
"s_xor_b32 s54, s15, 1703936\n" \
"buffer_load_dwordx4 v33, s[40:43], s54 offen nt lds\n" \
"s_add_u32 m0, s62, 98304\n" \
"s_xor_b32 s54, s15, 1835008\n" \
"buffer_load_dwordx4 v32, s[40:43], s54 offen nt lds\n" \
"s_add_u32 m0, s62, 99328\n" \
"s_xor_b32 s54, s15, 1835008\n" \
"buffer_load_dwordx4 v33, s[40:43], s54 offen nt lds\n" \
"s_add_u32 m0, s62, 100352\n" \
"s_xor_b32 s54, s15, 1966080\n" \
"buffer_load_dwordx4 v32, s[40:43], s54 offen nt lds\n" \
"s_add_u32 m0, s62, 101376\n" \
"s_xor_b32 s54, s15, 1966080\n" \
"buffer_load_dwordx4 v33, s[40:43], s54 offen nt lds\n" \
"s_add_u32 m0, s62, 81920\n" \
"s_xor_b32 s54, s15, 2621440\n" \
"buffer_load_dwordx4 v32, s[40:43], s54 offen nt lds\n" \
"s_add_u32 m0, s62, 82944\n" \
"s_xor_b32 s54, s15, 2621440\n" \
"buffer_load_dwordx4 v33, s[40:43], s54 offen nt lds\n" \
"s_add_u32 m0, s62, 83968\n" \
"s_xor_b32 s54, s15, 2752512\n" \
"buffer_load_dwordx4 v32, s[40:43], s54 offen nt lds\n" \
"s_add_u32 m0, s62, 84992\n" \
"s_xor_b32 s54, s15, 2752512\n" \
"buffer_load_dwordx4 v33, s[40:43], s54 offen nt lds\n" \
"s_add_u32 m0, s62, 65536\n" \
"s_xor_b32 s54, s15, 2883584\n" \
"buffer_load_dwordx4 v32, s[40:43], s54 offen nt lds\n" \
"s_add_u32 m0, s62, 66560\n" \
"s_xor_b32 s54, s15, 2883584\n" \
"buffer_load_dwordx4 v33, s[40:43], s54 offen nt lds\n" \
"s_add_u32 m0, s62, 67584\n" \
"s_xor_b32 s54, s15, 3014656\n" \
"buffer_load_dwordx4 v32, s[40:43], s54 offen nt lds\n" \
"s_add_u32 m0, s62, 68608\n" \
"s_xor_b32 s54, s15, 3014656\n" \
"buffer_load_dwordx4 v33, s[40:43], s54 offen nt lds\n" \
"s_add_u32 m0, s62, 49152\n" \
"s_xor_b32 s54, s15, 3670016\n" \
"buffer_load_dwordx4 v32, s[40:43], s54 offen nt lds\n" \
"s_add_u32 m0, s62, 50176\n" \
"s_xor_b32 s54, s15, 3670016\n" \
"buffer_load_dwordx4 v33, s[40:43], s54 offen nt lds\n" \
"s_add_u32 m0, s62, 51200\n" \
"s_xor_b32 s54, s15, 3801088\n" \
"buffer_load_dwordx4 v32, s[40:43], s54 offen nt lds\n" \
"s_add_u32 m0, s62, 52224\n" \
"s_xor_b32 s54, s15, 3801088\n" \
"buffer_load_dwordx4 v33, s[40:43], s54 offen nt lds\n" \
"s_add_u32 m0, s62, 32768\n" \
"s_xor_b32 s54, s15, 3932160\n" \
"buffer_load_dwordx4 v32, s[40:43], s54 offen nt lds\n" \
"s_add_u32 m0, s62, 33792\n" \
"s_xor_b32 s54, s15, 3932160\n" \
"buffer_load_dwordx4 v33, s[40:43], s54 offen nt lds\n" \
"s_add_u32 m0, s62, 34816\n" \
"s_xor_b32 s54, s15, 4063232\n" \
"buffer_load_dwordx4 v32, s[40:43], s54 offen nt lds\n" \
"s_add_u32 m0, s62, 35840\n" \
"s_xor_b32 s54, s15, 4063232\n" \
"buffer_load_dwordx4 v33, s[40:43], s54 offen nt lds\n" \
"s_add_u32 m0, s62, 16384\n" \
"s_xor_b32 s54, s15, 4718592\n" \
"buffer_load_dwordx4 v32, s[40:43], s54 offen nt lds\n" \
"s_add_u32 m0, s62, 17408\n" \
"s_xor_b32 s54, s15, 4718592\n" \
"buffer_load_dwordx4 v33, s[40:43], s54 offen nt lds\n" \
"s_add_u32 m0, s62, 18432\n" \
"s_xor_b32 s54, s15, 4849664\n" \
"buffer_load_dwordx4 v32, s[40:43], s54 offen nt lds\n" \
"s_add_u32 m0, s62, 19456\n" \
"s_xor_b32 s54, s15, 4849664\n" \
"buffer_load_dwordx4 v33, s[40:43], s54 offen nt lds\n" \
"s_add_u32 m0, s62, 0\n" \
"s_xor_b32 s54, s15, 4980736\n" \
"buffer_load_dwordx4 v32, s[40:43], s54 offen nt lds\n" \
"s_add_u32 m0, s62, 1024\n" \
"s_xor_b32 s54, s15, 4980736\n" \
"buffer_load_dwordx4 v33, s[40:43], s54 offen nt lds\n" \
"s_add_u32 m0, s62, 2048\n" \
"s_xor_b32 s54, s15, 5111808\n" \
"buffer_load_dwordx4 v32, s[40:43], s54 offen nt lds\n" \
"s_add_u32 m0, s62, 3072\n" \
"s_xor_b32 s54, s15, 5111808\n" \
"buffer_load_dwordx4 v33, s[40:43], s54 offen nt lds\n" \
"s_waitcnt vmcnt(32)\n" \
"s_barrier\n" \
"ds_read_b64_tr_b16 a[16:17], v38 offset:16384\n" \
"ds_read_b64_tr_b16 a[18:19], v38 offset:16896\n" \
"ds_read_b64_tr_b16 a[24:25], v39 offset:16384\n" \
"ds_read_b64_tr_b16 a[26:27], v39 offset:16896\n" \
"ds_read_b64_tr_b16 v[112:113], v38 offset:24576\n" \
"ds_read_b64_tr_b16 v[114:115], v38 offset:25088\n" \
"ds_read_b64_tr_b16 v[120:121], v39 offset:24576\n" \
"ds_read_b64_tr_b16 v[122:123], v39 offset:25088\n" \
"ds_read_b64_tr_b16 a[20:21], v38 offset:0\n" \
"ds_read_b64_tr_b16 a[22:23], v38 offset:512\n" \
"ds_read_b64_tr_b16 a[28:29], v39 offset:0\n" \
"ds_read_b64_tr_b16 a[30:31], v39 offset:512\n" \
"ds_read_b64_tr_b16 v[116:117], v38 offset:8192\n" \
"ds_read_b64_tr_b16 v[118:119], v38 offset:8704\n" \
"ds_read_b64_tr_b16 v[124:125], v39 offset:8192\n" \
"ds_read_b64_tr_b16 v[126:127], v39 offset:8704\n" \
"s_waitcnt lgkmcnt(0)\n" \
"s_barrier\n" \
"s_add_u32 m0, s62, 147456\n" \
"s_xor_b32 s54, s15, 5767168\n" \
"buffer_load_dwordx4 v32, s[40:43], s54 offen nt lds\n" \
"s_add_u32 m0, s62, 148480\n" \
"s_xor_b32 s54, s15, 5767168\n" \
"buffer_load_dwordx4 v33, s[40:43], s54 offen nt lds\n" \
"s_add_u32 m0, s62, 149504\n" \
"s_xor_b32 s54, s15, 5898240\n" \
"buffer_load_dwordx4 v32, s[40:43], s54 offen nt lds\n" \
"s_add_u32 m0, s62, 150528\n" \
"s_xor_b32 s54, s15, 5898240\n" \
"buffer_load_dwordx4 v33, s[40:43], s54 offen nt lds\n" \
"s_add_u32 m0, s62, 131072\n" \
"s_xor_b32 s54, s15, 6029312\n" \
"buffer_load_dwordx4 v32, s[40:43], s54 offen nt lds\n" \
"s_add_u32 m0, s62, 132096\n" \
"s_xor_b32 s54, s15, 6029312\n" \
"buffer_load_dwordx4 v33, s[40:43], s54 offen nt lds\n" \
"s_add_u32 m0, s62, 133120\n" \
"s_xor_b32 s54, s15, 6160384\n" \
"buffer_load_dwordx4 v32, s[40:43], s54 offen nt lds\n" \
"s_add_u32 m0, s62, 134144\n" \
"s_xor_b32 s54, s15, 6160384\n" \
"buffer_load_dwordx4 v33, s[40:43], s54 offen nt lds\n" \
"s_waitcnt vmcnt(32)\n" \
"s_barrier\n" \
"ds_read_b64_tr_b16 a[48:49], v36 offset:49152\n" \
"ds_read_b64_tr_b16 a[50:51], v36 offset:49664\n" \
"ds_read_b64_tr_b16 a[56:57], v37 offset:49152\n" \
"ds_read_b64_tr_b16 a[58:59], v37 offset:49664\n" \
"ds_read_b64_tr_b16 v[128:129], v36 offset:57344\n" \
"ds_read_b64_tr_b16 v[130:131], v36 offset:57856\n" \
"ds_read_b64_tr_b16 v[136:137], v37 offset:57344\n" \
"ds_read_b64_tr_b16 v[138:139], v37 offset:57856\n" \
"ds_read_b64_tr_b16 a[52:53], v36 offset:32768\n" \
"ds_read_b64_tr_b16 a[54:55], v36 offset:33280\n" \
"ds_read_b64_tr_b16 a[60:61], v37 offset:32768\n" \
"ds_read_b64_tr_b16 a[62:63], v37 offset:33280\n" \
"ds_read_b64_tr_b16 v[132:133], v36 offset:40960\n" \
"ds_read_b64_tr_b16 v[134:135], v36 offset:41472\n" \
"ds_read_b64_tr_b16 v[140:141], v37 offset:40960\n" \
"ds_read_b64_tr_b16 v[142:143], v37 offset:41472\n" \
"s_waitcnt lgkmcnt(0)\n" \
"s_barrier\n" \
"s_add_u32 m0, s62, 114688\n" \
"s_xor_b32 s54, s15, 6815744\n" \
"buffer_load_dwordx4 v32, s[40:43], s54 offen nt lds\n" \
"s_add_u32 m0, s62, 115712\n" \
"s_xor_b32 s54, s15, 6815744\n" \
"buffer_load_dwordx4 v33, s[40:43], s54 offen nt lds\n" \
"s_add_u32 m0, s62, 116736\n" \
"s_xor_b32 s54, s15, 6946816\n" \
"buffer_load_dwordx4 v32, s[40:43], s54 offen nt lds\n" \
"s_add_u32 m0, s62, 117760\n" \
"s_xor_b32 s54, s15, 6946816\n" \
"buffer_load_dwordx4 v33, s[40:43], s54 offen nt lds\n" \
"s_add_u32 m0, s62, 98304\n" \
"s_xor_b32 s54, s15, 7077888\n" \
"buffer_load_dwordx4 v32, s[40:43], s54 offen nt lds\n" \
"s_add_u32 m0, s62, 99328\n" \
"s_xor_b32 s54, s15, 7077888\n" \
"buffer_load_dwordx4 v33, s[40:43], s54 offen nt lds\n" \
"s_add_u32 m0, s62, 100352\n" \
"s_xor_b32 s54, s15, 7208960\n" \
"buffer_load_dwordx4 v32, s[40:43], s54 offen nt lds\n" \
"s_add_u32 m0, s62, 101376\n" \
"s_xor_b32 s54, s15, 7208960\n" \
"buffer_load_dwordx4 v33, s[40:43], s54 offen nt lds\n" \
"s_waitcnt vmcnt(32)\n" \
"s_barrier\n" \
"ds_read_b64_tr_b16 a[80:81], v36 offset:16384\n" \
"ds_read_b64_tr_b16 a[82:83], v36 offset:16896\n" \
"ds_read_b64_tr_b16 a[88:89], v37 offset:16384\n" \
"ds_read_b64_tr_b16 a[90:91], v37 offset:16896\n" \
"ds_read_b64_tr_b16 v[144:145], v36 offset:24576\n" \
"ds_read_b64_tr_b16 v[146:147], v36 offset:25088\n" \
"ds_read_b64_tr_b16 v[152:153], v37 offset:24576\n" \
"ds_read_b64_tr_b16 v[154:155], v37 offset:25088\n" \
"ds_read_b64_tr_b16 a[84:85], v36 offset:0\n" \
"ds_read_b64_tr_b16 a[86:87], v36 offset:512\n" \
"ds_read_b64_tr_b16 a[92:93], v37 offset:0\n" \
"ds_read_b64_tr_b16 a[94:95], v37 offset:512\n" \
"ds_read_b64_tr_b16 v[148:149], v36 offset:8192\n" \
"ds_read_b64_tr_b16 v[150:151], v36 offset:8704\n" \
"ds_read_b64_tr_b16 v[156:157], v37 offset:8192\n" \
"ds_read_b64_tr_b16 v[158:159], v37 offset:8704\n" \
"s_waitcnt lgkmcnt(0)\n" \
"s_barrier\n" \
"s_add_u32 m0, s62, 81920\n" \
"s_xor_b32 s54, s15, 7340032\n" \
"buffer_load_dwordx4 v32, s[40:43], s54 offen nt lds\n" \
"s_add_u32 m0, s62, 82944\n" \
"s_xor_b32 s54, s15, 7340032\n" \
"buffer_load_dwordx4 v33, s[40:43], s54 offen nt lds\n" \
"s_add_u32 m0, s62, 83968\n" \
"s_xor_b32 s54, s15, 7471104\n" \
"buffer_load_dwordx4 v32, s[40:43], s54 offen nt lds\n" \
"s_add_u32 m0, s62, 84992\n" \
"s_xor_b32 s54, s15, 7471104\n" \
"buffer_load_dwordx4 v33, s[40:43], s54 offen nt lds\n" \
"s_add_u32 m0, s62, 65536\n" \
"s_xor_b32 s54, s15, 7602176\n" \
"buffer_load_dwordx4 v32, s[40:43], s54 offen nt lds\n" \
"s_add_u32 m0, s62, 66560\n" \
"s_xor_b32 s54, s15, 7602176\n" \
"buffer_load_dwordx4 v33, s[40:43], s54 offen nt lds\n" \
"s_add_u32 m0, s62, 67584\n" \
"s_xor_b32 s54, s15, 7733248\n" \
"buffer_load_dwordx4 v32, s[40:43], s54 offen nt lds\n" \
"s_add_u32 m0, s62, 68608\n" \
"s_xor_b32 s54, s15, 7733248\n" \
"buffer_load_dwordx4 v33, s[40:43], s54 offen nt lds\n" \
"s_waitcnt vmcnt(32)\n" \
"s_barrier\n" \
"ds_read_b64_tr_b16 a[112:113], v34 offset:49152\n" \
"ds_read_b64_tr_b16 a[114:115], v34 offset:49664\n" \
"ds_read_b64_tr_b16 a[120:121], v35 offset:49152\n" \
"ds_read_b64_tr_b16 a[122:123], v35 offset:49664\n" \
"ds_read_b64_tr_b16 v[160:161], v34 offset:57344\n" \
"ds_read_b64_tr_b16 v[162:163], v34 offset:57856\n" \
"ds_read_b64_tr_b16 v[168:169], v35 offset:57344\n" \
"ds_read_b64_tr_b16 v[170:171], v35 offset:57856\n" \
"ds_read_b64_tr_b16 a[116:117], v34 offset:32768\n" \
"ds_read_b64_tr_b16 a[118:119], v34 offset:33280\n" \
"ds_read_b64_tr_b16 a[124:125], v35 offset:32768\n" \
"ds_read_b64_tr_b16 a[126:127], v35 offset:33280\n" \
"ds_read_b64_tr_b16 v[164:165], v34 offset:40960\n" \
"ds_read_b64_tr_b16 v[166:167], v34 offset:41472\n" \
"ds_read_b64_tr_b16 v[172:173], v35 offset:40960\n" \
"ds_read_b64_tr_b16 v[174:175], v35 offset:41472\n" \
"s_waitcnt lgkmcnt(0)\n" \
"s_barrier\n" \
"s_add_u32 m0, s62, 49152\n" \
"s_xor_b32 s54, s15, 7864320\n" \
"buffer_load_dwordx4 v32, s[40:43], s54 offen nt lds\n" \
"s_add_u32 m0, s62, 50176\n" \
"s_xor_b32 s54, s15, 7864320\n" \
"buffer_load_dwordx4 v33, s[40:43], s54 offen nt lds\n" \
"s_add_u32 m0, s62, 51200\n" \
"s_xor_b32 s54, s15, 7995392\n" \
"buffer_load_dwordx4 v32, s[40:43], s54 offen nt lds\n" \
"s_add_u32 m0, s62, 52224\n" \
"s_xor_b32 s54, s15, 7995392\n" \
"buffer_load_dwordx4 v33, s[40:43], s54 offen nt lds\n" \
"s_add_u32 m0, s62, 32768\n" \
"s_xor_b32 s54, s15, 8126464\n" \
"buffer_load_dwordx4 v32, s[40:43], s54 offen nt lds\n" \
"s_add_u32 m0, s62, 33792\n" \
"s_xor_b32 s54, s15, 8126464\n" \
"buffer_load_dwordx4 v33, s[40:43], s54 offen nt lds\n" \
"s_add_u32 m0, s62, 34816\n" \
"s_xor_b32 s54, s15, 8257536\n" \
"buffer_load_dwordx4 v32, s[40:43], s54 offen nt lds\n" \
"s_add_u32 m0, s62, 35840\n" \
"s_xor_b32 s54, s15, 8257536\n" \
"buffer_load_dwordx4 v33, s[40:43], s54 offen nt lds\n" \
"s_waitcnt vmcnt(32)\n" \
"s_barrier\n" \
"ds_read_b64_tr_b16 a[144:145], v34 offset:16384\n" \
"ds_read_b64_tr_b16 a[146:147], v34 offset:16896\n" \
"ds_read_b64_tr_b16 a[152:153], v35 offset:16384\n" \
"ds_read_b64_tr_b16 a[154:155], v35 offset:16896\n" \
"ds_read_b64_tr_b16 v[176:177], v34 offset:24576\n" \
"ds_read_b64_tr_b16 v[178:179], v34 offset:25088\n" \
"ds_read_b64_tr_b16 v[184:185], v35 offset:24576\n" \
"ds_read_b64_tr_b16 v[186:187], v35 offset:25088\n" \
"ds_read_b64_tr_b16 a[148:149], v34 offset:0\n" \
"ds_read_b64_tr_b16 a[150:151], v34 offset:512\n" \
"ds_read_b64_tr_b16 a[156:157], v35 offset:0\n" \
"ds_read_b64_tr_b16 a[158:159], v35 offset:512\n" \
"ds_read_b64_tr_b16 v[180:181], v34 offset:8192\n" \
"ds_read_b64_tr_b16 v[182:183], v34 offset:8704\n" \
"ds_read_b64_tr_b16 v[188:189], v35 offset:8192\n" \
"ds_read_b64_tr_b16 v[190:191], v35 offset:8704\n" \
"s_waitcnt lgkmcnt(0)\n" \
"s_barrier\n" \
"s_add_u32 m0, s62, 0\n" \
"s_xor_b32 s54, s15, 0\n" \
"buffer_load_dwordx4 v32, s[40:43], s54 offen nt lds\n" \
"s_add_u32 m0, s62, 1024\n" \
"s_xor_b32 s54, s15, 0\n" \
"buffer_load_dwordx4 v33, s[40:43], s54 offen nt lds\n" \
"s_add_u32 m0, s62, 2048\n" \
"s_xor_b32 s54, s15, 131072\n" \
"buffer_load_dwordx4 v32, s[40:43], s54 offen nt lds\n" \
"s_add_u32 m0, s62, 3072\n" \
"s_xor_b32 s54, s15, 131072\n" \
"buffer_load_dwordx4 v33, s[40:43], s54 offen nt lds\n" \
"s_add_u32 m0, s62, 16384\n" \
"s_xor_b32 s54, s15, 262144\n" \
"buffer_load_dwordx4 v32, s[40:43], s54 offen nt lds\n" \
"s_add_u32 m0, s62, 17408\n" \
"s_xor_b32 s54, s15, 262144\n" \
"buffer_load_dwordx4 v33, s[40:43], s54 offen nt lds\n" \
"s_add_u32 m0, s62, 18432\n" \
"s_xor_b32 s54, s15, 393216\n" \
"buffer_load_dwordx4 v32, s[40:43], s54 offen nt lds\n" \
"s_add_u32 m0, s62, 19456\n" \
"s_xor_b32 s54, s15, 393216\n" \
"buffer_load_dwordx4 v33, s[40:43], s54 offen nt lds\n" \
"s_waitcnt vmcnt(32)\n" \
"s_barrier\n" \
"ds_read_b64_tr_b16 a[176:177], v38 offset:16384\n" \
"ds_read_b64_tr_b16 a[178:179], v38 offset:16896\n" \
"ds_read_b64_tr_b16 a[184:185], v39 offset:16384\n" \
"ds_read_b64_tr_b16 a[186:187], v39 offset:16896\n" \
"ds_read_b64_tr_b16 v[192:193], v38 offset:24576\n" \
"ds_read_b64_tr_b16 v[194:195], v38 offset:25088\n" \
"ds_read_b64_tr_b16 v[200:201], v39 offset:24576\n" \
"ds_read_b64_tr_b16 v[202:203], v39 offset:25088\n" \
"ds_read_b64_tr_b16 a[180:181], v38 offset:0\n" \
"ds_read_b64_tr_b16 a[182:183], v38 offset:512\n" \
"ds_read_b64_tr_b16 a[188:189], v39 offset:0\n" \
"ds_read_b64_tr_b16 a[190:191], v39 offset:512\n" \
"ds_read_b64_tr_b16 v[196:197], v38 offset:8192\n" \
"ds_read_b64_tr_b16 v[198:199], v38 offset:8704\n" \
"ds_read_b64_tr_b16 v[204:205], v39 offset:8192\n" \
"ds_read_b64_tr_b16 v[206:207], v39 offset:8704\n" \
"s_waitcnt lgkmcnt(0)\n" \
"s_barrier\n" \
"s_waitcnt vmcnt(24)\n" \
"s_barrier\n" \
"ds_read_b64_tr_b16 a[208:209], v36 offset:49152\n" \
"ds_read_b64_tr_b16 a[210:211], v36 offset:49664\n" \
"ds_read_b64_tr_b16 a[216:217], v37 offset:49152\n" \
"ds_read_b64_tr_b16 a[218:219], v37 offset:49664\n" \
"ds_read_b64_tr_b16 v[208:209], v36 offset:57344\n" \
"ds_read_b64_tr_b16 v[210:211], v36 offset:57856\n" \
"ds_read_b64_tr_b16 v[216:217], v37 offset:57344\n" \
"ds_read_b64_tr_b16 v[218:219], v37 offset:57856\n" \
"ds_read_b64_tr_b16 a[212:213], v36 offset:32768\n" \
"ds_read_b64_tr_b16 a[214:215], v36 offset:33280\n" \
"ds_read_b64_tr_b16 a[220:221], v37 offset:32768\n" \
"ds_read_b64_tr_b16 a[222:223], v37 offset:33280\n" \
"ds_read_b64_tr_b16 v[212:213], v36 offset:40960\n" \
"ds_read_b64_tr_b16 v[214:215], v36 offset:41472\n" \
"ds_read_b64_tr_b16 v[220:221], v37 offset:40960\n" \
"ds_read_b64_tr_b16 v[222:223], v37 offset:41472\n" \
"s_waitcnt lgkmcnt(0)\n" \
"s_barrier\n" \
"s_add_u32 m0, s62, 98304\n" \
"s_xor_b32 s54, s15, 1048576\n" \
"buffer_load_dwordx4 v32, s[40:43], s54 offen nt lds\n" \
"s_add_u32 m0, s62, 99328\n" \
"s_xor_b32 s54, s15, 1048576\n" \
"buffer_load_dwordx4 v33, s[40:43], s54 offen nt lds\n" \
"s_add_u32 m0, s62, 100352\n" \
"s_xor_b32 s54, s15, 1179648\n" \
"buffer_load_dwordx4 v32, s[40:43], s54 offen nt lds\n" \
"s_add_u32 m0, s62, 101376\n" \
"s_xor_b32 s54, s15, 1179648\n" \
"buffer_load_dwordx4 v33, s[40:43], s54 offen nt lds\n" \
"s_add_u32 m0, s62, 147456\n" \
"s_xor_b32 s54, s15, 1310720\n" \
"buffer_load_dwordx4 v32, s[40:43], s54 offen nt lds\n" \
"s_add_u32 m0, s62, 148480\n" \
"s_xor_b32 s54, s15, 1310720\n" \
"buffer_load_dwordx4 v33, s[40:43], s54 offen nt lds\n" \
"s_add_u32 m0, s62, 149504\n" \
"s_xor_b32 s54, s15, 1441792\n" \
"buffer_load_dwordx4 v32, s[40:43], s54 offen nt lds\n" \
"s_add_u32 m0, s62, 150528\n" \
"s_xor_b32 s54, s15, 1441792\n" \
"buffer_load_dwordx4 v33, s[40:43], s54 offen nt lds\n" \
"s_waitcnt vmcnt(24)\n" \
"s_barrier\n" \
"ds_read_b64_tr_b16 a[224:225], v36 offset:16384\n" \
"ds_read_b64_tr_b16 a[226:227], v36 offset:16896\n" \
"ds_read_b64_tr_b16 a[232:233], v37 offset:16384\n" \
"ds_read_b64_tr_b16 a[234:235], v37 offset:16896\n" \
"ds_read_b64_tr_b16 v[224:225], v36 offset:24576\n" \
"ds_read_b64_tr_b16 v[226:227], v36 offset:25088\n" \
"ds_read_b64_tr_b16 v[232:233], v37 offset:24576\n" \
"ds_read_b64_tr_b16 v[234:235], v37 offset:25088\n" \
"ds_read_b64_tr_b16 a[228:229], v36 offset:0\n" \
"ds_read_b64_tr_b16 a[230:231], v36 offset:512\n" \
"ds_read_b64_tr_b16 a[236:237], v37 offset:0\n" \
"ds_read_b64_tr_b16 a[238:239], v37 offset:512\n" \
"ds_read_b64_tr_b16 v[228:229], v36 offset:8192\n" \
"ds_read_b64_tr_b16 v[230:231], v36 offset:8704\n" \
"ds_read_b64_tr_b16 v[236:237], v37 offset:8192\n" \
"ds_read_b64_tr_b16 v[238:239], v37 offset:8704\n" \
"s_waitcnt lgkmcnt(0)\n" \
"s_barrier\n" \
"s_add_u32 m0, s62, 65536\n" \
"s_xor_b32 s54, s15, 2097152\n" \
"buffer_load_dwordx4 v32, s[40:43], s54 offen nt lds\n" \
"s_add_u32 m0, s62, 66560\n" \
"s_xor_b32 s54, s15, 2097152\n" \
"buffer_load_dwordx4 v33, s[40:43], s54 offen nt lds\n" \
"s_add_u32 m0, s62, 67584\n" \
"s_xor_b32 s54, s15, 2228224\n" \
"buffer_load_dwordx4 v32, s[40:43], s54 offen nt lds\n" \
"s_add_u32 m0, s62, 68608\n" \
"s_xor_b32 s54, s15, 2228224\n" \
"buffer_load_dwordx4 v33, s[40:43], s54 offen nt lds\n" \
"s_add_u32 m0, s62, 131072\n" \
"s_xor_b32 s54, s15, 2359296\n" \
"buffer_load_dwordx4 v32, s[40:43], s54 offen nt lds\n" \
"s_add_u32 m0, s62, 132096\n" \
"s_xor_b32 s54, s15, 2359296\n" \
"buffer_load_dwordx4 v33, s[40:43], s54 offen nt lds\n" \
"s_add_u32 m0, s62, 133120\n" \
"s_xor_b32 s54, s15, 2490368\n" \
"buffer_load_dwordx4 v32, s[40:43], s54 offen nt lds\n" \
"s_add_u32 m0, s62, 134144\n" \
"s_xor_b32 s54, s15, 2490368\n" \
"buffer_load_dwordx4 v33, s[40:43], s54 offen nt lds\n" \
"s_add_u32 m0, s62, 81920\n" \
"s_xor_b32 s54, s15, 3145728\n" \
"buffer_load_dwordx4 v32, s[40:43], s54 offen nt lds\n" \
"s_add_u32 m0, s62, 82944\n" \
"s_xor_b32 s54, s15, 3145728\n" \
"buffer_load_dwordx4 v33, s[40:43], s54 offen nt lds\n" \
"s_add_u32 m0, s62, 83968\n" \
"s_xor_b32 s54, s15, 3276800\n" \
"buffer_load_dwordx4 v32, s[40:43], s54 offen nt lds\n" \
"s_add_u32 m0, s62, 84992\n" \
"s_xor_b32 s54, s15, 3276800\n" \
"buffer_load_dwordx4 v33, s[40:43], s54 offen nt lds\n" \
"s_add_u32 m0, s62, 114688\n" \
"s_xor_b32 s54, s15, 3407872\n" \
"buffer_load_dwordx4 v32, s[40:43], s54 offen nt lds\n" \
"s_add_u32 m0, s62, 115712\n" \
"s_xor_b32 s54, s15, 3407872\n" \
"buffer_load_dwordx4 v33, s[40:43], s54 offen nt lds\n" \
"s_add_u32 m0, s62, 116736\n" \
"s_xor_b32 s54, s15, 3538944\n" \
"buffer_load_dwordx4 v32, s[40:43], s54 offen nt lds\n" \
"s_add_u32 m0, s62, 117760\n" \
"s_xor_b32 s54, s15, 3538944\n" \
"buffer_load_dwordx4 v33, s[40:43], s54 offen nt lds\n" \
"s_waitcnt vmcnt(32)\n" \
"s_barrier\n" \
"ds_read_b64_tr_b16 a[240:241], v34 offset:49152\n" \
"ds_read_b64_tr_b16 a[242:243], v34 offset:49664\n" \
"ds_read_b64_tr_b16 a[248:249], v35 offset:49152\n" \
"ds_read_b64_tr_b16 a[250:251], v35 offset:49664\n" \
"ds_read_b64_tr_b16 v[240:241], v34 offset:57344\n" \
"ds_read_b64_tr_b16 v[242:243], v34 offset:57856\n" \
"ds_read_b64_tr_b16 v[248:249], v35 offset:57344\n" \
"ds_read_b64_tr_b16 v[250:251], v35 offset:57856\n" \
"ds_read_b64_tr_b16 a[244:245], v34 offset:32768\n" \
"ds_read_b64_tr_b16 a[246:247], v34 offset:33280\n" \
"ds_read_b64_tr_b16 a[252:253], v35 offset:32768\n" \
"ds_read_b64_tr_b16 a[254:255], v35 offset:33280\n" \
"ds_read_b64_tr_b16 v[244:245], v34 offset:40960\n" \
"ds_read_b64_tr_b16 v[246:247], v34 offset:41472\n" \
"ds_read_b64_tr_b16 v[252:253], v35 offset:40960\n" \
"ds_read_b64_tr_b16 v[254:255], v35 offset:41472\n" \
"s_waitcnt lgkmcnt(0)\n" \
"s_barrier\n" \
"s_add_u32 m0, s62, 32768\n" \
"s_xor_b32 s54, s15, 4194304\n" \
"buffer_load_dwordx4 v32, s[40:43], s54 offen nt lds\n" \
"s_add_u32 m0, s62, 33792\n" \
"s_xor_b32 s54, s15, 4194304\n" \
"buffer_load_dwordx4 v33, s[40:43], s54 offen nt lds\n" \
"s_add_u32 m0, s62, 34816\n" \
"s_xor_b32 s54, s15, 4325376\n" \
"buffer_load_dwordx4 v32, s[40:43], s54 offen nt lds\n" \
"s_add_u32 m0, s62, 35840\n" \
"s_xor_b32 s54, s15, 4325376\n" \
"buffer_load_dwordx4 v33, s[40:43], s54 offen nt lds\n" \
"s_add_u32 m0, s62, 49152\n" \
"s_xor_b32 s54, s15, 4456448\n" \
"buffer_load_dwordx4 v32, s[40:43], s54 offen nt lds\n" \
"s_add_u32 m0, s62, 50176\n" \
"s_xor_b32 s54, s15, 4456448\n" \
"buffer_load_dwordx4 v33, s[40:43], s54 offen nt lds\n" \
"s_add_u32 m0, s62, 51200\n" \
"s_xor_b32 s54, s15, 4587520\n" \
"buffer_load_dwordx4 v32, s[40:43], s54 offen nt lds\n" \
"s_add_u32 m0, s62, 52224\n" \
"s_xor_b32 s54, s15, 4587520\n" \
"buffer_load_dwordx4 v33, s[40:43], s54 offen nt lds\n" \
"s_waitcnt vmcnt(32)\n" \
"s_barrier\n" \
"ds_read_b64_tr_b16 a[0:1], v34 offset:0\n" \
"ds_read_b64_tr_b16 a[2:3], v34 offset:512\n" \
"ds_read_b64_tr_b16 a[8:9], v35 offset:0\n" \
"ds_read_b64_tr_b16 a[10:11], v35 offset:512\n" \
"ds_read_b64_tr_b16 v[48:49], v34 offset:8192\n" \
"ds_read_b64_tr_b16 v[50:51], v34 offset:8704\n" \
"ds_read_b64_tr_b16 v[56:57], v35 offset:8192\n" \
"ds_read_b64_tr_b16 v[58:59], v35 offset:8704\n" \
"ds_read_b64_tr_b16 a[4:5], v34 offset:16384\n" \
"ds_read_b64_tr_b16 a[6:7], v34 offset:16896\n" \
"ds_read_b64_tr_b16 a[12:13], v35 offset:16384\n" \
"ds_read_b64_tr_b16 a[14:15], v35 offset:16896\n" \
"ds_read_b64_tr_b16 v[52:53], v34 offset:24576\n" \
"ds_read_b64_tr_b16 v[54:55], v34 offset:25088\n" \
"ds_read_b64_tr_b16 v[60:61], v35 offset:24576\n" \
"ds_read_b64_tr_b16 v[62:63], v35 offset:25088\n" \
"s_waitcnt lgkmcnt(0)\n" \
"s_barrier\n" \
"ds_write_b128 v1, v[48:51] offset:0\n" \
"ds_write_b128 v1, v[52:55] offset:1024\n" \
"ds_write_b128 v1, v[56:59] offset:2048\n" \
"ds_write_b128 v1, v[60:63] offset:3072\n" \
"s_add_u32 m0, s62, 16384\n" \
"s_xor_b32 s54, s15, 5242880\n" \
"buffer_load_dwordx4 v32, s[40:43], s54 offen nt lds\n" \
"s_add_u32 m0, s62, 17408\n" \
"s_xor_b32 s54, s15, 5242880\n" \
"buffer_load_dwordx4 v33, s[40:43], s54 offen nt lds\n" \
"s_add_u32 m0, s62, 18432\n" \
"s_xor_b32 s54, s15, 5373952\n" \
"buffer_load_dwordx4 v32, s[40:43], s54 offen nt lds\n" \
"s_add_u32 m0, s62, 19456\n" \
"s_xor_b32 s54, s15, 5373952\n" \
"buffer_load_dwordx4 v33, s[40:43], s54 offen nt lds\n" \
"s_waitcnt vmcnt(28)\n" \
"s_barrier\n" \
"ds_read_b64_tr_b16 a[32:33], v36 offset:32768\n" \
"ds_read_b64_tr_b16 a[34:35], v36 offset:33280\n" \
"ds_read_b64_tr_b16 a[40:41], v37 offset:32768\n" \
"ds_read_b64_tr_b16 a[42:43], v37 offset:33280\n" \
"ds_read_b64_tr_b16 v[48:49], v36 offset:40960\n" \
"ds_read_b64_tr_b16 v[50:51], v36 offset:41472\n" \
"ds_read_b64_tr_b16 v[56:57], v37 offset:40960\n" \
"ds_read_b64_tr_b16 v[58:59], v37 offset:41472\n" \
"ds_read_b64_tr_b16 a[36:37], v38 offset:16384\n" \
"ds_read_b64_tr_b16 a[38:39], v38 offset:16896\n" \
"ds_read_b64_tr_b16 a[44:45], v39 offset:16384\n" \
"ds_read_b64_tr_b16 a[46:47], v39 offset:16896\n" \
"ds_read_b64_tr_b16 v[52:53], v38 offset:24576\n" \
"ds_read_b64_tr_b16 v[54:55], v38 offset:25088\n" \
"ds_read_b64_tr_b16 v[60:61], v39 offset:24576\n" \
"ds_read_b64_tr_b16 v[62:63], v39 offset:25088\n" \
"s_waitcnt lgkmcnt(0)\n" \
"s_barrier\n" \
"ds_write_b128 v2, v[48:51] offset:32768\n" \
"ds_write_b128 v2, v[52:55] offset:33792\n" \
"ds_write_b128 v2, v[56:59] offset:34816\n" \
"ds_write_b128 v2, v[60:63] offset:35840\n" \
"s_add_u32 m0, s62, 147456\n" \
"s_xor_b32 s54, s15, 5505024\n" \
"buffer_load_dwordx4 v32, s[40:43], s54 offen nt lds\n" \
"s_add_u32 m0, s62, 148480\n" \
"s_xor_b32 s54, s15, 5505024\n" \
"buffer_load_dwordx4 v33, s[40:43], s54 offen nt lds\n" \
"s_add_u32 m0, s62, 149504\n" \
"s_xor_b32 s54, s15, 5636096\n" \
"buffer_load_dwordx4 v32, s[40:43], s54 offen nt lds\n" \
"s_add_u32 m0, s62, 150528\n" \
"s_xor_b32 s54, s15, 5636096\n" \
"buffer_load_dwordx4 v33, s[40:43], s54 offen nt lds\n" \
"s_waitcnt vmcnt(24)\n" \
"s_barrier\n" \
"ds_read_b64_tr_b16 a[64:65], v36 offset:0\n" \
"ds_read_b64_tr_b16 a[66:67], v36 offset:512\n" \
"ds_read_b64_tr_b16 a[72:73], v37 offset:0\n" \
"ds_read_b64_tr_b16 a[74:75], v37 offset:512\n" \
"ds_read_b64_tr_b16 v[48:49], v36 offset:8192\n" \
"ds_read_b64_tr_b16 v[50:51], v36 offset:8704\n" \
"ds_read_b64_tr_b16 v[56:57], v37 offset:8192\n" \
"ds_read_b64_tr_b16 v[58:59], v37 offset:8704\n" \
"ds_read_b64_tr_b16 a[68:69], v38 offset:0\n" \
"ds_read_b64_tr_b16 a[70:71], v38 offset:512\n" \
"ds_read_b64_tr_b16 a[76:77], v39 offset:0\n" \
"ds_read_b64_tr_b16 a[78:79], v39 offset:512\n" \
"ds_read_b64_tr_b16 v[52:53], v38 offset:8192\n" \
"ds_read_b64_tr_b16 v[54:55], v38 offset:8704\n" \
"ds_read_b64_tr_b16 v[60:61], v39 offset:8192\n" \
"ds_read_b64_tr_b16 v[62:63], v39 offset:8704\n" \
"s_waitcnt lgkmcnt(0)\n" \
"s_barrier\n" \
"ds_write_b128 v2, v[48:51] offset:0\n" \
"ds_write_b128 v2, v[52:55] offset:1024\n" \
"ds_write_b128 v2, v[56:59] offset:2048\n" \
"ds_write_b128 v2, v[60:63] offset:3072\n" \
"s_waitcnt vmcnt(16)\n" \
"s_barrier\n" \
"ds_read_b64_tr_b16 a[96:97], v36 offset:16384\n" \
"ds_read_b64_tr_b16 a[98:99], v36 offset:16896\n" \
"ds_read_b64_tr_b16 a[104:105], v37 offset:16384\n" \
"ds_read_b64_tr_b16 a[106:107], v37 offset:16896\n" \
"ds_read_b64_tr_b16 v[48:49], v36 offset:24576\n" \
"ds_read_b64_tr_b16 v[50:51], v36 offset:25088\n" \
"ds_read_b64_tr_b16 v[56:57], v37 offset:24576\n" \
"ds_read_b64_tr_b16 v[58:59], v37 offset:25088\n" \
"ds_read_b64_tr_b16 a[100:101], v36 offset:49152\n" \
"ds_read_b64_tr_b16 a[102:103], v36 offset:49664\n" \
"ds_read_b64_tr_b16 a[108:109], v37 offset:49152\n" \
"ds_read_b64_tr_b16 a[110:111], v37 offset:49664\n" \
"ds_read_b64_tr_b16 v[52:53], v36 offset:57344\n" \
"ds_read_b64_tr_b16 v[54:55], v36 offset:57856\n" \
"ds_read_b64_tr_b16 v[60:61], v37 offset:57344\n" \
"ds_read_b64_tr_b16 v[62:63], v37 offset:57856\n" \
"s_waitcnt lgkmcnt(0)\n" \
"s_barrier\n" \
"ds_write_b128 v2, v[48:51] offset:16384\n" \
"ds_write_b128 v2, v[52:55] offset:17408\n" \
"ds_write_b128 v2, v[56:59] offset:18432\n" \
"ds_write_b128 v2, v[60:63] offset:19456\n" \
"s_waitcnt vmcnt(8)\n" \
"s_barrier\n" \
"ds_read_b64_tr_b16 a[128:129], v34 offset:32768\n" \
"ds_read_b64_tr_b16 a[130:131], v34 offset:33280\n" \
"ds_read_b64_tr_b16 a[136:137], v35 offset:32768\n" \
"ds_read_b64_tr_b16 a[138:139], v35 offset:33280\n" \
"ds_read_b64_tr_b16 v[48:49], v34 offset:40960\n" \
"ds_read_b64_tr_b16 v[50:51], v34 offset:41472\n" \
"ds_read_b64_tr_b16 v[56:57], v35 offset:40960\n" \
"ds_read_b64_tr_b16 v[58:59], v35 offset:41472\n" \
"ds_read_b64_tr_b16 a[132:133], v34 offset:49152\n" \
"ds_read_b64_tr_b16 a[134:135], v34 offset:49664\n" \
"ds_read_b64_tr_b16 a[140:141], v35 offset:49152\n" \
"ds_read_b64_tr_b16 a[142:143], v35 offset:49664\n" \
"ds_read_b64_tr_b16 v[52:53], v34 offset:57344\n" \
"ds_read_b64_tr_b16 v[54:55], v34 offset:57856\n" \
"ds_read_b64_tr_b16 v[60:61], v35 offset:57344\n" \
"ds_read_b64_tr_b16 v[62:63], v35 offset:57856\n" \
"s_waitcnt lgkmcnt(0)\n" \
"s_barrier\n" \
"ds_write_b128 v1, v[48:51] offset:32768\n" \
"ds_write_b128 v1, v[52:55] offset:33792\n" \
"ds_write_b128 v1, v[56:59] offset:34816\n" \
"ds_write_b128 v1, v[60:63] offset:35840\n" \
"s_add_u32 m0, s62, 49152\n" \
"s_xor_b32 s54, s15, 6291456\n" \
"buffer_load_dwordx4 v32, s[40:43], s54 offen nt lds\n" \
"s_add_u32 m0, s62, 50176\n" \
"s_xor_b32 s54, s15, 6291456\n" \
"buffer_load_dwordx4 v33, s[40:43], s54 offen nt lds\n" \
"s_add_u32 m0, s62, 51200\n" \
"s_xor_b32 s54, s15, 6422528\n" \
"buffer_load_dwordx4 v32, s[40:43], s54 offen nt lds\n" \
"s_add_u32 m0, s62, 52224\n" \
"s_xor_b32 s54, s15, 6422528\n" \
"buffer_load_dwordx4 v33, s[40:43], s54 offen nt lds\n" \
"s_add_u32 m0, s62, 131072\n" \
"s_xor_b32 s54, s15, 6553600\n" \
"buffer_load_dwordx4 v32, s[40:43], s54 offen nt lds\n" \
"s_add_u32 m0, s62, 132096\n" \
"s_xor_b32 s54, s15, 6553600\n" \
"buffer_load_dwordx4 v33, s[40:43], s54 offen nt lds\n" \
"s_add_u32 m0, s62, 133120\n" \
"s_xor_b32 s54, s15, 6684672\n" \
"buffer_load_dwordx4 v32, s[40:43], s54 offen nt lds\n" \
"s_add_u32 m0, s62, 134144\n" \
"s_xor_b32 s54, s15, 6684672\n" \
"buffer_load_dwordx4 v33, s[40:43], s54 offen nt lds\n" \
"s_waitcnt vmcnt(8)\n" \
"s_barrier\n" \
"ds_read_b64_tr_b16 a[160:161], v34 offset:16384\n" \
"ds_read_b64_tr_b16 a[162:163], v34 offset:16896\n" \
"ds_read_b64_tr_b16 a[168:169], v35 offset:16384\n" \
"ds_read_b64_tr_b16 a[170:171], v35 offset:16896\n" \
"ds_read_b64_tr_b16 v[48:49], v34 offset:24576\n" \
"ds_read_b64_tr_b16 v[50:51], v34 offset:25088\n" \
"ds_read_b64_tr_b16 v[56:57], v35 offset:24576\n" \
"ds_read_b64_tr_b16 v[58:59], v35 offset:25088\n" \
"ds_read_b64_tr_b16 a[164:165], v38 offset:16384\n" \
"ds_read_b64_tr_b16 a[166:167], v38 offset:16896\n" \
"ds_read_b64_tr_b16 a[172:173], v39 offset:16384\n" \
"ds_read_b64_tr_b16 a[174:175], v39 offset:16896\n" \
"ds_read_b64_tr_b16 v[52:53], v38 offset:24576\n" \
"ds_read_b64_tr_b16 v[54:55], v38 offset:25088\n" \
"ds_read_b64_tr_b16 v[60:61], v39 offset:24576\n" \
"ds_read_b64_tr_b16 v[62:63], v39 offset:25088\n" \
"s_waitcnt lgkmcnt(0)\n" \
"s_barrier\n" \
"ds_write_b128 v1, v[48:51] offset:16384\n" \
"ds_write_b128 v1, v[52:55] offset:17408\n" \
"ds_write_b128 v1, v[56:59] offset:18432\n" \
"ds_write_b128 v1, v[60:63] offset:19456\n" \
"s_waitcnt vmcnt(0)\n" \
"s_barrier\n" \
"ds_read_b64_tr_b16 a[192:193], v34 offset:49152\n" \
"ds_read_b64_tr_b16 a[194:195], v34 offset:49664\n" \
"ds_read_b64_tr_b16 a[200:201], v35 offset:49152\n" \
"ds_read_b64_tr_b16 a[202:203], v35 offset:49664\n" \
"ds_read_b64_tr_b16 v[48:49], v34 offset:57344\n" \
"ds_read_b64_tr_b16 v[50:51], v34 offset:57856\n" \
"ds_read_b64_tr_b16 v[56:57], v35 offset:57344\n" \
"ds_read_b64_tr_b16 v[58:59], v35 offset:57856\n" \
"ds_read_b64_tr_b16 a[196:197], v38 offset:0\n" \
"ds_read_b64_tr_b16 a[198:199], v38 offset:512\n" \
"ds_read_b64_tr_b16 a[204:205], v39 offset:0\n" \
"ds_read_b64_tr_b16 a[206:207], v39 offset:512\n" \
"ds_read_b64_tr_b16 v[52:53], v38 offset:8192\n" \
"ds_read_b64_tr_b16 v[54:55], v38 offset:8704\n" \
"ds_read_b64_tr_b16 v[60:61], v39 offset:8192\n" \
"ds_read_b64_tr_b16 v[62:63], v39 offset:8704\n" \
"s_waitcnt lgkmcnt(0)\n" \
"s_barrier\n" \
"ds_write_b128 v1, v[48:51] offset:49152\n" \
"ds_write_b128 v1, v[52:55] offset:50176\n" \
"ds_write_b128 v1, v[56:59] offset:51200\n" \
"ds_write_b128 v1, v[60:63] offset:52224\n" \
"s_waitcnt vmcnt(0) lgkmcnt(0)\n" \
"s_barrier\n" \
"s_lshl_b32 s62, s48, 11\n" \
"s_add_u32 s63, s63, s62\n" \
"s_mov_b32 s49, 0\n" \
"s_mov_b32 s52, 0\n" \
"s_mov_b32 s81, 1\n" \
"s_mov_b32 s88, 0\n" \
"s_mov_b32 s95, 0x7ffffff0\n" \
"s_mov_b32 s98, 0x3e0293ee\n" \
"s_mov_b32 s99, 0x3e0293ee\n" \
"v_mov_b32 v20, 0x3f803f80\n" \
"v_mov_b32 v21, 0x3f803f80\n" \
"v_mov_b32 v22, 0x3f803f80\n" \
"v_mov_b32 v23, 0x3f803f80\n" \
"v_mov_b32 v24, 0\n" \
"v_mov_b32 v25, 0\n" \
"v_mov_b32 v26, 0\n" \
"v_mov_b32 v27, 0\n" \
"v_mov_b32 v28, 0\n" \
"v_mov_b32 v29, 0\n" \
"v_mov_b32 v104, 0\n" \
"v_mov_b32 v105, 0\n" \
"v_mov_b32 v106, 0\n" \
"v_mov_b32 v107, 0\n" \
"v_mov_b32 v108, 0\n" \
"v_mov_b32 v109, 0\n" \
"v_mov_b32 v110, 0\n" \
"v_mov_b32 v111, 0\n" \
"v_cmp_le_i32_e64 s[64:65], 0, v14\n" \
"v_cmp_le_i32_e64 s[66:67], 1, v14\n" \
"v_cmp_le_i32_e64 s[68:69], 2, v14\n" \
"v_cmp_le_i32_e64 s[70:71], 3, v14\n" \
"v_cmp_le_i32_e64 s[72:73], 16, v14\n" \
"v_cmp_le_i32_e64 s[74:75], 17, v14\n" \
"v_cmp_le_i32_e64 s[76:77], 18, v14\n" \
"v_cmp_le_i32_e64 s[78:79], 19, v14\n" \
"s_mov_b32 s82, -1\n" \
"s_mov_b32 s83, 0\n" \
"s_mov_b32 s6, 0x33333333\n" \
"s_mov_b32 s7, 0x33333333\n" \
"s_mov_b32 s8, 0x53800000\n" \
"s_mov_b32 s22, 0xfffffc00\n" \
"s_mov_b32 s23, -1\n" \
"s_mul_i32 s32, s46, 1\n" \
"s_mov_b32 s33, s32\n" \
"s_mov_b32 s34, s46\n" \
"s_mov_b32 s35, s17\n" \
"s_mov_b32 s13, -1\n" \
"s_mov_b32 s41, s10\n" \
"s_lshl_b32 s42, s48, 10\n" \
"s_add_u32 s42, s42, 114688\n" \
"s_and_b32 s43, s48, 1\n" \
"s_lshl_b32 s43, s43, 12\n" \
"s_mov_b32 s54, 0\n" \
"buffer_store_dwordx4 v[24:27], v7, s[36:39], s54 offen sc1\n" \
"s_mov_b32 s54, 131072\n" \
"buffer_store_dwordx4 v[24:27], v7, s[36:39], s54 offen sc1\n" \
"s_add_u32 s54, s94, 65536\n" \
"buffer_store_dword v24, v10, s[36:39], s54 offen sc1\n" \
"s_mov_b32 s54, 262144\n" \
"buffer_store_dwordx4 v[24:27], v7, s[36:39], s54 offen sc1\n" \
"s_add_u32 s54, s94, 131072\n" \
"buffer_store_dword v24, v10, s[36:39], s54 offen sc1\n" \
"s_mov_b32 s54, 393216\n" \
"buffer_store_dwordx4 v[24:27], v7, s[36:39], s54 offen sc1\n" \
"s_add_u32 s54, s94, 196608\n" \
"buffer_store_dword v24, v10, s[36:39], s54 offen sc1\n" \
"s_cmp_eq_u32 s48, 0\n" \
"s_cselect_b32 s9, 7, 0\n" \
"buffer_load_dword v30, v15, s[36:39], s80 offen sc1\n" \
"s_waitcnt vmcnt(0)\n" \
"v_cmp_ge_u32_e64 s[96:97], s81, v30\n" \
"s_nop 4\n" \
"s_and_b32 s96, s96, s9\n" \
"s_cbranch_scc1 .Lq_f0_cold_%=\n" \
".Lq_f0_ok_%=:\n" \
"s_barrier\n" \
"s_add_u32 m0, s62, 131072\n" \
"s_add_u32 s15, s63, 10371072\n" \
"buffer_load_dwordx4 v3, s[36:39], s15 offen offset:0 sc1 lds\n" \
"buffer_load_dwordx4 v3, s[36:39], s15 offen offset:1024 sc1 lds\n" \
"s_memrealtime s[56:57]\n" \
"s_waitcnt lgkmcnt(0)\n" \
"s_sub_u32 s50, s56, 20\n" \
"s_waitcnt vmcnt(0) lgkmcnt(0)\n" \
"s_add_u32 s54, s49, 0\n" \
"s_and_b32 s54, s54, 3\n" \
"s_mul_i32 s58, s54, 131072\n" \
"s_add_u32 s54, s49, 1\n" \
"s_and_b32 s54, s54, 3\n" \
"s_mul_i32 s59, s54, 131072\n" \
"s_add_u32 s54, s49, 2\n" \
"s_and_b32 s54, s54, 3\n" \
"s_mul_i32 s60, s54, 131072\n" \
"s_add_u32 s54, s49, 3\n" \
"s_and_b32 s54, s54, 3\n" \
"s_mul_i32 s61, s54, 131072\n" \
"s_lshr_b32 s53, s58, 1\n" \
"s_add_u32 s92, s53, s94\n" \
"s_add_u32 s55, s53, s41\n" \
"s_xor_b32 s89, s55, 0x4800\n" \
"s_xor_b32 s90, s55, 0x800\n" \
"s_xor_b32 s91, s55, 0x4000\n" \
"s_lshr_b32 s93, s59, 1\n" \
"s_add_u32 s93, s93, s94\n" \
"s_add_u32 s81, s49, 2\n" \
"s_min_u32 s81, s81, 63\n" \
"ds_read_b128 v[32:35], v1 offset:0\n" \
"ds_read_b128 v[36:39], v1 offset:1024\n" \
"ds_read_b128 v[40:43], v1 offset:2048\n" \
"ds_read_b128 v[44:47], v1 offset:3072\n" \
"ds_read_b128 v[48:51], v2 offset:32768\n" \
"ds_read_b128 v[52:55], v2 offset:33792\n" \
"ds_read_b128 v[56:59], v2 offset:34816\n" \
"ds_read_b128 v[60:63], v2 offset:35840\n" \
"v_mov_b64 v[80:81], 0\n" \
"v_mov_b64 v[82:83], 0\n" \
"v_mov_b64 v[84:85], 0\n" \
"v_mov_b64 v[86:87], 0\n" \
"v_mov_b64 v[88:89], 0\n" \
"v_mov_b64 v[90:91], 0\n" \
"v_mov_b64 v[92:93], 0\n" \
"v_mov_b64 v[94:95], 0\n" \
"v_mov_b32 v13, -1\n" \
"v_add_u32 v14, s62, v3\n" \
"v_xor_b32 v31, 0x400, v11\n" \
"s_max_u32 s53, s49, 1\n" \
"s_sub_u32 s53, s53, 1\n" \
"v_mov_b32 v30, s53\n" \
"s_mov_b32 m0, s42\n" \
"s_waitcnt vmcnt(0) lgkmcnt(0)\n" \
"s_nop 7\n" \
"s_nop 7\n" \
".p2align 6\n" \
".Lq_loop_%=:\n" \
"s_getpc_b64 s[18:19]\n" \
".Lq_wp_%=:\n" \
"s_add_u32 s18, s18, .Lq_ws_%=-.Lq_wp_%=\n" \
"s_addc_u32 s19, s19, 0\n" \
"s_sub_u32 s21, s50, 5\n" \
"s_nop 0\n" \
".Lq_w_%=:\n" \
"s_memrealtime s[56:57]\n" \
"s_waitcnt lgkmcnt(0)\n" \
"s_sub_u32 s54, s56, s21\n" \
"s_cmp_gt_u32 s54, s22\n" \
"s_cbranch_scc1 .Lq_w_%=\n" \
"s_min_u32 s21, s54, 3\n" \
"s_lshl2_add_u32 s18, s21, s18\n" \
"s_setpc_b64 s[18:19]\n" \
"s_nop 0\n" \
".Lq_ws_%=:\n" \
"s_nop 4\n" \
"s_nop 5\n" \
"s_nop 4\n" \
".Lq_w4_%=:\n" \
"s_barrier\n" \
".Lq_go_%=:\n" \
"buffer_load_dwordx4 v4, s[36:39], s55 offen sc1 lds\n" \
"s_memtime s[0:1]\n" \
"s_cmp_lt_u32 s54, 8\n" \
"s_cselect_b32 s51, s50, s56\n" \
"s_cselect_b32 s23, s23, s49\n" \
"s_cselect_b32 s53, 0, s14\n" \
"s_add_u32 s53, s53, s34\n" \
"v_mov_b32 v17, s35\n" \
"s_nop 7\n" \
"s_nop 7\n" \
"s_add_u32 m0, s42, 4096\n" \
"s_nop 0\n" \
"buffer_load_dwordx4 v4, s[36:39], s89 offen sc1 lds\n" \
"s_nop 7\n" \
"s_nop 7\n" \
"s_nop 7\n" \
"s_add_u32 m0, s42, 8192\n" \
"s_nop 0\n" \
"buffer_load_dwordx4 v4, s[36:39], s90 offen sc1 lds\n" \
"s_add_u32 m0, s42, 12288\n" \
"s_nop 0\n" \
"buffer_load_dwordx4 v4, s[36:39], s91 offen sc1 lds\n" \
"s_add_u32 m0, s62, 147456\n" \
"s_add_u32 s15, s63, 10387456\n" \
"buffer_load_dwordx4 v3, s[36:39], s15 offen offset:0 sc1 lds\n" \
"buffer_load_dwordx4 v3, s[36:39], s15 offen offset:1024 sc1 lds\n" \
"s_add_u32 m0, s62, 139264\n" \
"s_add_u32 s15, s63, 10379264\n" \
"buffer_load_dwordx4 v3, s[36:39], s15 offen offset:0 sc1 lds\n" \
"buffer_load_dwordx4 v3, s[36:39], s15 offen offset:1024 sc1 lds\n" \
"s_waitcnt lgkmcnt(0)\n" \
"s_waitcnt vmcnt(7)\n" \
"s_barrier\n" \
"s_memtime s[2:3]\n" \
"ds_read_b128 v[64:67], v3 offset:0\n" \
"ds_read_b128 v[68:71], v3 offset:1024\n" \
"ds_read_b128 v[72:75], v3 offset:2048\n" \
"ds_read_b128 v[76:79], v3 offset:3072\n" \
"s_waitcnt lgkmcnt(3)\n" \
"v_smfmac_f32_16x16x64_bf16 v[80:83], v[64:67], a[0:7], v12\n" \
"v_min3_u32 v13, v13, v64, v66\n" \
"v_smfmac_f32_16x16x64_bf16 v[84:87], v[64:67], a[8:15], v12\n" \
"s_waitcnt lgkmcnt(2)\n" \
"v_min3_u32 v13, v13, v68, v70\n" \
"v_smfmac_f32_16x16x64_bf16 v[88:91], v[64:67], v[32:39], v12\n" \
"s_waitcnt lgkmcnt(1)\n" \
"v_min3_u32 v13, v13, v72, v74\n" \
"v_smfmac_f32_16x16x64_bf16 v[92:95], v[64:67], v[40:47], v12\n" \
"s_waitcnt lgkmcnt(0)\n" \
"v_min3_u32 v13, v13, v76, v78\n" \
"v_smfmac_f32_16x16x64_bf16 v[80:83], v[68:71], a[16:23], v12\n" \
"s_sub_u32 s2, s2, s0\n" \
"s_mul_i32 s2, s2, 23\n" \
"v_cmp_eq_u32 vcc, 0, v13\n" \
"v_smfmac_f32_16x16x64_bf16 v[84:87], v[68:71], a[24:31], v12\n" \
"ds_read_b128 v[32:35], v2 offset:0\n" \
"v_smfmac_f32_16x16x64_bf16 v[88:91], v[68:71], v[112:119], v12\n" \
"ds_read_b128 v[36:39], v2 offset:1024\n" \
"v_smfmac_f32_16x16x64_bf16 v[92:95], v[68:71], v[120:127], v12\n" \
"s_cbranch_vccnz .Lq_retry_%=\n" \
"s_waitcnt vmcnt(6)\n" \
"s_barrier\n" \
"ds_read_b128 v[64:67], v3 offset:4096\n" \
"v_smfmac_f32_16x16x64_bf16 v[80:83], v[72:75], a[32:39], v12\n" \
"ds_read_b128 v[68:71], v3 offset:5120\n" \
"s_lshr_b32 s2, s2, 9\n" \
"v_smfmac_f32_16x16x64_bf16 v[84:87], v[72:75], a[40:47], v12\n" \
"ds_read_b128 v[40:43], v2 offset:2048\n" \
"s_add_u32 s3, s51, s2\n" \
"v_smfmac_f32_16x16x64_bf16 v[88:91], v[72:75], v[48:55], v12\n" \
"ds_read_b128 v[44:47], v2 offset:3072\n" \
"s_add_u32 s53, s53, s3\n" \
"v_smfmac_f32_16x16x64_bf16 v[92:95], v[72:75], v[56:63], v12\n" \
"ds_read_b128 v[72:75], v3 offset:6144\n" \
"s_max_u32 s53, s53, 1\n" \
"v_mov_b32 v16, s53\n" \
"v_smfmac_f32_16x16x64_bf16 v[80:83], v[76:79], a[48:55], v12\n" \
"buffer_store_dwordx2 v[16:17], v10, s[36:39], s92 offen sc1\n" \
"v_smfmac_f32_16x16x64_bf16 v[84:87], v[76:79], a[56:63], v12\n" \
"ds_read_b128 v[48:51], v2 offset:16384\n" \
"v_smfmac_f32_16x16x64_bf16 v[88:91], v[76:79], v[128:135], v12\n" \
"ds_read_b128 v[52:55], v2 offset:17408\n" \
"v_smfmac_f32_16x16x64_bf16 v[92:95], v[76:79], v[136:143], v12\n" \
"ds_read_b128 v[76:79], v3 offset:7168\n" \
"s_waitcnt lgkmcnt(7)\n" \
"v_smfmac_f32_16x16x64_bf16 v[80:83], v[64:67], a[64:71], v12\n" \
"v_min3_u32 v13, v13, v64, v66\n" \
"v_smfmac_f32_16x16x64_bf16 v[84:87], v[64:67], a[72:79], v12\n" \
"ds_read_b128 v[56:59], v2 offset:18432\n" \
"v_smfmac_f32_16x16x64_bf16 v[88:91], v[64:67], v[32:39], v12\n" \
"ds_read_b128 v[60:63], v2 offset:19456\n" \
"s_waitcnt lgkmcnt(6)\n" \
"v_smfmac_f32_16x16x64_bf16 v[92:95], v[64:67], v[40:47], v12\n" \
"v_smfmac_f32_16x16x64_bf16 v[80:83], v[68:71], a[80:87], v12\n" \
"v_min3_u32 v13, v13, v68, v70\n" \
"v_smfmac_f32_16x16x64_bf16 v[84:87], v[68:71], a[88:95], v12\n" \
"ds_read_b128 v[32:35], v1 offset:32768\n" \
"v_smfmac_f32_16x16x64_bf16 v[88:91], v[68:71], v[144:151], v12\n" \
"ds_read_b128 v[36:39], v1 offset:33792\n" \
"v_smfmac_f32_16x16x64_bf16 v[92:95], v[68:71], v[152:159], v12\n" \
"s_waitcnt lgkmcnt(7)\n" \
"v_smfmac_f32_16x16x64_bf16 v[80:83], v[72:75], a[96:103], v12\n" \
"v_min3_u32 v13, v13, v72, v74\n" \
"v_smfmac_f32_16x16x64_bf16 v[84:87], v[72:75], a[104:111], v12\n" \
"ds_read_b128 v[40:43], v1 offset:34816\n" \
"s_waitcnt lgkmcnt(6)\n" \
"v_smfmac_f32_16x16x64_bf16 v[88:91], v[72:75], v[48:55], v12\n" \
"ds_read_b128 v[44:47], v1 offset:35840\n" \
"s_waitcnt lgkmcnt(4)\n" \
"v_smfmac_f32_16x16x64_bf16 v[92:95], v[72:75], v[56:63], v12\n" \
"s_waitcnt vmcnt(5)\n" \
"s_barrier\n" \
"ds_read_b128 v[64:67], v3 offset:8192\n" \
"v_smfmac_f32_16x16x64_bf16 v[80:83], v[76:79], a[112:119], v12\n" \
"ds_read_b128 v[68:71], v3 offset:9216\n" \
"v_min3_u32 v13, v13, v76, v78\n" \
"v_smfmac_f32_16x16x64_bf16 v[84:87], v[76:79], a[120:127], v12\n" \
"ds_read_b128 v[48:51], v1 offset:16384\n" \
"v_smfmac_f32_16x16x64_bf16 v[88:91], v[76:79], v[160:167], v12\n" \
"ds_read_b128 v[52:55], v1 offset:17408\n" \
"v_smfmac_f32_16x16x64_bf16 v[92:95], v[76:79], v[168:175], v12\n" \
"ds_read_b128 v[72:75], v3 offset:10240\n" \
"ds_read_b128 v[76:79], v3 offset:11264\n" \
"s_waitcnt lgkmcnt(5)\n" \
"v_smfmac_f32_16x16x64_bf16 v[80:83], v[64:67], a[128:135], v12\n" \
"buffer_store_dwordx4 v[96:99], v11, s[36:39], s95 offen sc1\n" \
"v_min3_u32 v13, v13, v64, v66\n" \
"v_smfmac_f32_16x16x64_bf16 v[84:87], v[64:67], a[136:143], v12\n" \
"ds_read_b128 v[56:59], v1 offset:18432\n" \
"v_smfmac_f32_16x16x64_bf16 v[88:91], v[64:67], v[32:39], v12\n" \
"ds_read_b128 v[60:63], v1 offset:19456\n" \
"v_smfmac_f32_16x16x64_bf16 v[92:95], v[64:67], v[40:47], v12\n" \
"ds_read_b128 v[64:67], v3 offset:12288\n" \
"s_waitcnt lgkmcnt(7)\n" \
"v_smfmac_f32_16x16x64_bf16 v[80:83], v[68:71], a[144:151], v12\n" \
"v_min3_u32 v13, v13, v68, v70\n" \
"v_smfmac_f32_16x16x64_bf16 v[84:87], v[68:71], a[152:159], v12\n" \
"ds_read_b128 v[32:35], v1 offset:49152\n" \
"v_smfmac_f32_16x16x64_bf16 v[88:91], v[68:71], v[176:183], v12\n" \
"ds_read_b128 v[36:39], v1 offset:50176\n" \
"v_smfmac_f32_16x16x64_bf16 v[92:95], v[68:71], v[184:191], v12\n" \
"ds_read_b128 v[68:71], v3 offset:13312\n" \
"s_waitcnt lgkmcnt(7)\n" \
"v_smfmac_f32_16x16x64_bf16 v[80:83], v[72:75], a[160:167], v12\n" \
"v_min3_u32 v13, v13, v72, v74\n" \
"v_smfmac_f32_16x16x64_bf16 v[84:87], v[72:75], a[168:175], v12\n" \
"ds_read_b128 v[40:43], v1 offset:51200\n" \
"v_smfmac_f32_16x16x64_bf16 v[88:91], v[72:75], v[48:55], v12\n" \
"ds_read_b128 v[44:47], v1 offset:52224\n" \
"s_waitcnt lgkmcnt(6)\n" \
"v_smfmac_f32_16x16x64_bf16 v[92:95], v[72:75], v[56:63], v12\n" \
"ds_read_b128 v[72:75], v3 offset:14336\n" \
"v_smfmac_f32_16x16x64_bf16 v[80:83], v[76:79], a[176:183], v12\n" \
"buffer_store_dwordx4 v[100:103], v31, s[36:39], s95 offen sc1\n" \
"v_min3_u32 v13, v13, v76, v78\n" \
"v_smfmac_f32_16x16x64_bf16 v[84:87], v[76:79], a[184:191], v12\n" \
"ds_read_b128 v[48:51], v3 offset:20480\n" \
"v_smfmac_f32_16x16x64_bf16 v[88:91], v[76:79], v[192:199], v12\n" \
"ds_read_b128 v[52:55], v3 offset:21504\n" \
"v_smfmac_f32_16x16x64_bf16 v[92:95], v[76:79], v[200:207], v12\n" \
"ds_read_b128 v[76:79], v3 offset:15360\n" \
"s_waitcnt lgkmcnt(9)\n" \
"v_smfmac_f32_16x16x64_bf16 v[80:83], v[64:67], a[192:199], v12\n" \
"s_mov_b32 s95, 0x7ffffff0\n" \
"v_min3_u32 v13, v13, v64, v66\n" \
"v_smfmac_f32_16x16x64_bf16 v[84:87], v[64:67], a[200:207], v12\n" \
"ds_read_b128 v[56:59], v3 offset:22528\n" \
"s_waitcnt lgkmcnt(8)\n" \
"v_smfmac_f32_16x16x64_bf16 v[88:91], v[64:67], v[32:39], v12\n" \
"ds_read_b128 v[60:63], v3 offset:23552\n" \
"s_waitcnt lgkmcnt(6)\n" \
"v_smfmac_f32_16x16x64_bf16 v[92:95], v[64:67], v[40:47], v12\n" \
"v_smfmac_f32_16x16x64_bf16 v[80:83], v[68:71], a[208:215], v12\n" \
"v_min3_u32 v13, v13, v68, v70\n" \
"v_smfmac_f32_16x16x64_bf16 v[84:87], v[68:71], a[216:223], v12\n" \
"ds_read_b128 v[32:35], v3 offset:16384\n" \
"v_smfmac_f32_16x16x64_bf16 v[88:91], v[68:71], v[208:215], v12\n" \
"ds_read_b128 v[36:39], v3 offset:17408\n" \
"v_smfmac_f32_16x16x64_bf16 v[92:95], v[68:71], v[216:223], v12\n" \
"s_waitcnt lgkmcnt(7)\n" \
"v_smfmac_f32_16x16x64_bf16 v[80:83], v[72:75], a[224:231], v12\n" \
"v_min3_u32 v13, v13, v72, v74\n" \
"v_smfmac_f32_16x16x64_bf16 v[84:87], v[72:75], a[232:239], v12\n" \
"ds_read_b128 v[40:43], v3 offset:18432\n" \
"s_waitcnt lgkmcnt(5)\n" \
"v_smfmac_f32_16x16x64_bf16 v[80:83], v[76:79], a[240:247], v12\n" \
"ds_read_b128 v[44:47], v3 offset:19456\n" \
"v_min3_u32 v13, v13, v76, v78\n" \
"v_smfmac_f32_16x16x64_bf16 v[84:87], v[76:79], a[248:255], v12\n" \
"v_cmp_eq_u32_e64 vcc, 0, v13\n" \
"v_smfmac_f32_16x16x64_bf16 v[88:91], v[72:75], v[224:231], v12\n" \
"v_add_f32_e64 v80, v80, v82\n" \
"v_smfmac_f32_16x16x64_bf16 v[92:95], v[72:75], v[232:239], v12\n" \
"v_add_f32_e64 v81, v81, v83\n" \
"v_smfmac_f32_16x16x64_bf16 v[88:91], v[76:79], v[240:247], v12\n" \
"s_cbranch_vccnz .Lq_retry_%=\n" \
"v_add_f32 v84, v84, v86\n" \
"v_smfmac_f32_16x16x64_bf16 v[92:95], v[76:79], v[248:255], v12\n" \
"v_add_f32_e64 v85, v85, v87\n" \
"v_pk_mul_f32 v[80:81], v[80:81], s[98:99]\n" \
"v_pk_mul_f32 v[84:85], v[84:85], s[98:99]\n" \
"v_cvt_pk_bf16_f32 v80, v80, v81\n" \
"v_cvt_pk_bf16_f32 v84, v84, v85\n" \
"v_pk_add_f32 v[88:89], v[88:89], v[90:91]\n" \
"v_cvt_pk_bf16_f32 v81, v88, v89\n" \
"s_nop 0\n" \
"v_pk_add_f32 v[92:93], v[92:93], v[94:95]\n" \
"v_cvt_pk_bf16_f32 v85, v92, v93\n" \
"ds_write2_b64 v5, v[80:81], v[84:85] offset1:64\n" \
"buffer_load_dword v104, v8, s[36:39], s58 offen offset:0 sc1\n" \
"buffer_load_dword v106, v8, s[36:39], s58 offen offset:256 sc1\n" \
"s_cmp_eq_u32 s49, 63\n" \
"s_waitcnt vmcnt(5)\n" \
"s_waitcnt lgkmcnt(0)\n" \
"s_barrier\n" \
"ds_read_b64_tr_b16 v[64:65], v6 offset:0\n" \
"ds_read_b64_tr_b16 v[66:67], v6 offset:512\n" \
"ds_read_b64_tr_b16 v[68:69], v6 offset:1024\n" \
"ds_read_b64_tr_b16 v[70:71], v6 offset:1536\n" \
"ds_read_b64_tr_b16 v[72:73], v6 offset:2048\n" \
"ds_read_b64_tr_b16 v[74:75], v6 offset:2560\n" \
"s_nop 0\n" \
"s_cbranch_scc1 .Lq_last_%=\n" \
"s_waitcnt lgkmcnt(4)\n" \
"v_mfma_f32_16x16x32_bf16 v[88:91], v[64:67], v[64:67], 0\n" \
"ds_read_b64_tr_b16 v[76:77], v6 offset:3072\n" \
"v_mfma_f32_16x16x32_bf16 v[80:83], v[32:35], v[64:67], 0\n" \
"ds_read_b64_tr_b16 v[78:79], v6 offset:3584\n" \
"s_waitcnt lgkmcnt(4)\n" \
"v_mfma_f32_16x16x32_bf16 v[88:91], v[68:71], v[68:71], v[88:91]\n" \
"ds_read_b128 v[96:99], v14 offset:32768\n" \
"s_waitcnt lgkmcnt(3)\n" \
"v_mfma_f32_16x16x32_bf16 v[88:91], v[72:75], v[72:75], v[88:91]\n" \
"ds_read_b128 v[100:103], v14 offset:33792\n" \
"v_mfma_f32_16x16x32_bf16 v[80:83], v[36:39], v[68:71], v[80:83]\n" \
"buffer_store_dword v30, v10, s[36:39], s87 offen sc1\n" \
"s_waitcnt lgkmcnt(2)\n" \
"v_mfma_f32_16x16x32_bf16 v[88:91], v[76:79], v[76:79], v[88:91]\n" \
"buffer_load_dword v30, v15, s[36:39], s80 offen sc1\n" \
"v_mfma_f32_16x16x32_bf16 v[80:83], v[40:43], v[72:75], v[80:83]\n" \
"v_mfma_f32_16x16x32_bf16 v[80:83], v[44:47], v[76:79], v[80:83]\n" \
"v_max_f32 v13, v90, v91\n" \
"v_max_f32_e64 v14, v90, v91\n" \
"v_mfma_f32_16x16x32_bf16 v[84:87], v[48:51], v[64:67], 0\n" \
"s_nop 0\n" \
"v_permlane16_swap_b32 v13, v14\n" \
"v_mfma_f32_16x16x32_bf16 v[84:87], v[52:55], v[68:71], v[84:87]\n" \
"v_max_f32 v13, v13, v14\n" \
"v_max_f32 v14, v13, v14\n" \
"v_mfma_f32_16x16x32_bf16 v[84:87], v[56:59], v[72:75], v[84:87]\n" \
"s_nop 0\n" \
"v_permlane32_swap_b32 v13, v14\n" \
"v_mfma_f32_16x16x32_bf16 v[84:87], v[60:63], v[76:79], v[84:87]\n" \
"v_max_f32 v14, v13, v14\n" \
"v_sub_f32 v80, v80, v14\n" \
"v_sub_f32 v81, v81, v14\n" \
"v_exp_f32 v80, v80\n" \
"v_pk_add_f32 v[82:83], v[82:83], v[14:15] op_sel_hi:[1,0] neg_lo:[0,1] neg_hi:[0,1]\n" \
"v_exp_f32 v81, v81\n" \
"v_exp_f32 v82, v82\n" \
"v_exp_f32_e64 v83, v83\n" \
"v_pk_add_f32 v[84:85], v[84:85], v[14:15] op_sel_hi:[1,0] neg_lo:[0,1] neg_hi:[0,1]\n" \
"v_pk_add_f32 v[86:87], v[86:87], v[14:15] op_sel_hi:[1,0] neg_lo:[0,1] neg_hi:[0,1]\n" \
"v_pk_add_f32 v[90:91], v[90:91], v[14:15] op_sel_hi:[1,0] neg_lo:[0,1] neg_hi:[0,1]\n" \
"v_exp_f32 v84, v84\n" \
"v_exp_f32 v85, v85\n" \
"v_exp_f32 v86, v86\n" \
"v_exp_f32 v87, v87\n" \
"v_cvt_pk_bf16_f32 v80, v80, v81\n" \
"v_cvt_pk_bf16_f32 v81, v82, v83\n" \
"v_cvt_pk_bf16_f32 v82, v84, v85\n" \
"v_cvt_pk_bf16_f32 v83, v86, v87\n" \
"v_exp_f32 v90, v90\n" \
"s_waitcnt lgkmcnt(1)\n" \
"v_mfma_f32_16x16x32_bf16 v[84:87], v[96:99], v[80:83], 0\n" \
"v_exp_f32 v91, v91\n" \
"s_waitcnt lgkmcnt(0)\n" \
"v_mfma_f32_16x16x32_bf16 v[92:95], v[100:103], v[80:83], 0\n" \
"v_cvt_pk_bf16_f32 v26, v90, v91\n" \
"v_mfma_f32_16x16x32_bf16 v[16:19], v[20:23], v[80:83], 0\n" \
"s_nop 0\n" \
"v_mfma_f32_16x16x32_bf16 v[16:19], v[20:23], v[26:29], v[16:19]\n" \
"s_waitcnt vmcnt(2)\n" \
"v_mfma_f32_16x16x32_bf16 v[84:87], v[104:107], v[26:29], v[84:87]\n" \
"v_mfma_f32_16x16x32_bf16 v[92:95], v[104:107], v[24:27], v[92:95]\n" \
"v_min_u32 v13, v104, v106\n" \
"v_cmp_eq_u32 vcc, 0, v13\n" \
"v_rcp_f32_e64 v14, v16\n" \
"v_cmp_nlt_f32_e64 s[26:27], v16, s8\n" \
".Lq_vr2_%=:\n" \
"v_pk_mul_f32 v[84:85], v[84:85], v[14:15] op_sel_hi:[1,0]\n" \
"v_pk_mul_f32 v[86:87], v[86:87], v[14:15] op_sel_hi:[1,0]\n" \
"v_cvt_pk_bf16_f32 v88, v84, v85\n" \
"v_cvt_pk_bf16_f32 v89, v86, v87\n" \
"v_max_u32 v88, 1, v88\n" \
"s_and_b64 s[26:27], s[26:27], s[6:7]\n" \
"s_or_b64 vcc, vcc, s[26:27]\n" \
"s_cbranch_vccnz .Lq_vr_%=\n" \
"v_pk_mul_f32 v[92:93], v[92:93], v[14:15] op_sel_hi:[1,0]\n" \
"v_pk_mul_f32 v[94:95], v[94:95], v[14:15] op_sel_hi:[1,0]\n" \
"v_cvt_pk_bf16_f32 v90, v92, v93\n" \
"v_cvt_pk_bf16_f32 v91, v94, v95\n" \
"v_max_u32_e64 v90, 1, v90\n" \
"buffer_store_dwordx4 v[88:91], v7, s[36:39], s58 offen sc1\n" \
".Lq_tok_%=:\n" \
"s_memrealtime s[24:25]\n" \
"v_add_u32_e64 v14, s43, v3\n" \
"ds_read_b128 v[96:99], v14 offset:24576\n" \
"ds_read_b128 v[100:103], v14 offset:25600\n" \
"ds_read_b128 v[92:95], v14 offset:26624\n" \
"s_memrealtime s[26:27]\n" \
"ds_read_b128 v[16:19], v14 offset:27648\n" \
"v_xor_b32 v0, 512, v8\n" \
"s_nop 2\n" \
"s_memrealtime s[28:29]\n" \
"s_nop 4\n" \
"s_memrealtime s[30:31]\n" \
"s_waitcnt lgkmcnt(0)\n" \
"v_mfma_f32_16x16x32_bf16 v[88:91], v[64:67], v[96:99], 0\n" \
"s_cmp_lt_u32 s49, 63\n" \
"s_cselect_b32 s54, 24576, 0\n" \
"s_add_u32 s63, s63, s54\n" \
"v_mfma_f32_16x16x32_bf16 v[80:83], v[32:35], v[96:99], 0\n" \
"buffer_load_dword v108, v0, s[36:39], s58 offen offset:0 sc1\n" \
"v_mfma_f32_16x16x32_bf16 v[88:91], v[68:71], v[100:103], v[88:91]\n" \
"buffer_load_dword v110, v0, s[36:39], s58 offen offset:256 sc1\n" \
"v_mfma_f32_16x16x32_bf16 v[88:91], v[72:75], v[92:95], v[88:91]\n" \
"s_add_u32 m0, s62, 131072\n" \
"s_add_u32 s15, s63, 10371072\n" \
"buffer_load_dwordx4 v3, s[36:39], s15 offen offset:0 sc1 lds\n" \
"v_mfma_f32_16x16x32_bf16 v[80:83], v[36:39], v[100:103], v[80:83]\n" \
"buffer_load_dwordx4 v3, s[36:39], s15 offen offset:1024 sc1 lds\n" \
"v_mfma_f32_16x16x32_bf16 v[88:91], v[76:79], v[16:19], v[88:91]\n" \
"s_add_u32 s25, s24, 3\n" \
"s_cmp_eq_u32 s26, s24\n" \
"s_subb_u32 s25, s25, 0\n" \
"v_mfma_f32_16x16x32_bf16 v[80:83], v[40:43], v[92:95], v[80:83]\n" \
"s_cmp_eq_u32 s28, s24\n" \
"s_subb_u32 s25, s25, 0\n" \
"v_mfma_f32_16x16x32_bf16 v[80:83], v[44:47], v[16:19], v[80:83]\n" \
"s_cmp_eq_u32 s30, s24\n" \
"s_subb_u32 s25, s25, 0\n" \
"s_max_u32 s16, s25, 1\n" \
"v_add_u32 v14, s62, v3\n" \
"v_mfma_f32_16x16x32_bf16 v[84:87], v[48:51], v[96:99], 0\n" \
"s_sub_u32 s24, s25, s3\n" \
"s_min_u32 s24, s24, 255\n" \
"v_xor_b32 v0, 0x800, v14\n" \
"v_mfma_f32_16x16x32_bf16 v[84:87], v[52:55], v[100:103], v[84:87]\n" \
"ds_read_b128 v[32:35], v14 offset:32768\n" \
"ds_read_b128 v[36:39], v14 offset:33792\n" \
"s_cmp_eq_u32 s23, s49\n" \
"s_cselect_b32 s24, s34, s24\n" \
"v_mov_b32 v13, 0xceabdc6e\n" \
"v_mfma_f32_16x16x32_bf16 v[84:87], v[56:59], v[92:95], v[84:87]\n" \
"ds_read_b128 v[40:43], v0 offset:32768\n" \
"ds_read_b128 v[44:47], v0 offset:33792\n" \
"s_min_u32 s34, s24, s32\n" \
"s_max_u32 s25, s24, s32\n" \
"v_mfma_f32_16x16x32_bf16 v[84:87], v[60:63], v[16:19], v[84:87]\n" \
"s_min_u32 s25, s25, s33\n" \
"s_max_u32 s34, s34, s25\n" \
"s_mov_b32 s33, s32\n" \
"s_mov_b32 s32, s24\n" \
"v_cndmask_b32_e64 v80, v13, v80, s[64:65]\n" \
"v_cndmask_b32_e64 v81, v13, v81, s[66:67]\n" \
"v_cndmask_b32_e64 v82, v13, v82, s[68:69]\n" \
"v_cndmask_b32_e64 v83, v13, v83, s[70:71]\n" \
"s_waitcnt vmcnt(5)\n" \
"v_cmp_ge_u32_e64 s[96:97], s81, v30\n" \
"v_readlane_b32 s35, v30, 3\n" \
"v_readlane_b32 s53, v30, 4\n" \
"v_cndmask_b32_e64 v84, v13, v84, s[72:73]\n" \
"v_cndmask_b32_e64 v85, v13, v85, s[74:75]\n" \
"v_cndmask_b32_e64 v86, v13, v86, s[76:77]\n" \
"v_cndmask_b32_e64 v87, v13, v87, s[78:79]\n" \
"s_max_u32 s35, s35, s53\n" \
"s_cmp_eq_u32 s35, 0\n" \
"s_cselect_b32 s35, s17, s35\n" \
"s_and_b32 s96, s96, s9\n" \
"s_cbranch_scc1 .Lq_f_cold_%=\n" \
".Lq_f_ok_%=:\n" \
"s_lshr_b32 s11, s58, 1\n" \
"s_mov_b32 s54, s58\n" \
"s_mov_b32 s58, s59\n" \
"s_mov_b32 s59, s60\n" \
"s_mov_b32 s60, s61\n" \
"s_mov_b32 s61, s54\n" \
"buffer_load_dwordx2 v[64:65], v9, s[36:39], s11 offen sc1\n" \
"v_max3_f32 v13, v90, v91, v80\n" \
"v_max3_f32 v14, v81, v82, v83\n" \
"v_max3_f32 v14, v14, v84, v85\n" \
"v_max3_f32 v13, v13, v86, v87\n" \
"v_max_f32 v13, v13, v14\n" \
"v_max_f32 v14, v13, v14\n" \
"s_nop 1\n" \
"v_permlane16_swap_b32 v13, v14\n" \
"v_max_f32 v13, v13, v14\n" \
"v_max_f32 v14, v13, v14\n" \
"s_nop 1\n" \
"v_permlane32_swap_b32 v13, v14\n" \
"v_max_f32 v14, v13, v14\n" \
"v_pk_add_f32 v[80:81], v[80:81], v[14:15] op_sel_hi:[1,0] neg_lo:[0,1] neg_hi:[0,1]\n" \
"v_pk_add_f32 v[82:83], v[82:83], v[14:15] op_sel_hi:[1,0] neg_lo:[0,1] neg_hi:[0,1]\n" \
"v_pk_add_f32 v[84:85], v[84:85], v[14:15] op_sel_hi:[1,0] neg_lo:[0,1] neg_hi:[0,1]\n" \
"v_pk_add_f32 v[86:87], v[86:87], v[14:15] op_sel_hi:[1,0] neg_lo:[0,1] neg_hi:[0,1]\n" \
"v_pk_add_f32 v[90:91], v[90:91], v[14:15] op_sel_hi:[1,0] neg_lo:[0,1] neg_hi:[0,1]\n" \
"v_exp_f32 v80, v80\n" \
"v_exp_f32 v81, v81\n" \
"v_exp_f32 v82, v82\n" \
"v_exp_f32 v83, v83\n" \
"v_exp_f32 v84, v84\n" \
"v_exp_f32 v85, v85\n" \
"v_exp_f32 v86, v86\n" \
"v_exp_f32 v87, v87\n" \
"v_cvt_pk_bf16_f32 v80, v80, v81\n" \
"v_cvt_pk_bf16_f32 v81, v82, v83\n" \
"v_cvt_pk_bf16_f32 v82, v84, v85\n" \
"v_cvt_pk_bf16_f32 v83, v86, v87\n" \
"v_exp_f32 v90, v90\n" \
"s_waitcnt lgkmcnt(3)\n" \
"v_mfma_f32_16x16x32_bf16 v[48:51], v[32:35], v[80:83], 0\n" \
"v_exp_f32 v91, v91\n" \
"s_waitcnt lgkmcnt(2)\n" \
"v_mfma_f32_16x16x32_bf16 v[52:55], v[36:39], v[80:83], 0\n" \
"v_cvt_pk_bf16_f32 v26, v90, v91\n" \
"s_waitcnt lgkmcnt(1)\n" \
"v_mfma_f32_16x16x32_bf16 v[56:59], v[40:43], v[80:83], 0\n" \
"s_waitcnt lgkmcnt(0)\n" \
"v_mfma_f32_16x16x32_bf16 v[60:63], v[44:47], v[80:83], 0\n" \
"v_mfma_f32_16x16x32_bf16 v[92:95], v[20:23], v[80:83], 0\n" \
"s_waitcnt vmcnt(4)\n" \
"v_min3_u32 v13, v104, v106, v108\n" \
"s_waitcnt vmcnt(3)\n" \
"v_min3_u32 v13, v13, v110, v110\n" \
"v_cmp_eq_u32 vcc, 0, v13\n" \
"s_nop 0\n" \
"s_cbranch_vccnz .Lq_v2_cold_%=\n" \
".Lq_v2_ok_%=:\n" \
"v_mfma_f32_16x16x32_bf16 v[92:95], v[20:23], v[26:29], v[92:95]\n" \
"v_mfma_f32_16x16x32_bf16 v[48:51], v[104:107], v[26:29], v[48:51]\n" \
"ds_read_b128 v[32:35], v1 offset:0\n" \
"s_lshl_b32 s95, s49, 13\n" \
"s_add_u32 s49, s49, 1\n" \
"v_mfma_f32_16x16x32_bf16 v[52:55], v[104:107], v[24:27], v[52:55]\n" \
"ds_read_b128 v[36:39], v1 offset:1024\n" \
"s_lshr_b32 s53, s58, 1\n" \
"s_add_u32 s92, s53, s94\n" \
"v_mfma_f32_16x16x32_bf16 v[56:59], v[108:111], v[26:29], v[56:59]\n" \
"ds_read_b128 v[40:43], v1 offset:2048\n" \
"s_add_u32 s55, s53, s41\n" \
"s_xor_b32 s89, s55, 0x4800\n" \
"v_mfma_f32_16x16x32_bf16 v[60:63], v[108:111], v[24:27], v[60:63]\n" \
"ds_read_b128 v[44:47], v1 offset:3072\n" \
"s_xor_b32 s90, s55, 0x800\n" \
"s_xor_b32 s91, s55, 0x4000\n" \
"s_lshr_b32 s93, s59, 1\n" \
"s_add_u32 s93, s93, s94\n" \
"s_add_u32 s81, s49, 2\n" \
"s_min_u32 s81, s81, 63\n" \
"v_rcp_f32 v14, v92\n" \
"s_nop 0\n" \
"v_pk_mul_f32 v[48:49], v[48:49], v[14:15] op_sel_hi:[1,0]\n" \
"v_pk_mul_f32 v[50:51], v[50:51], v[14:15] op_sel_hi:[1,0]\n" \
"v_pk_mul_f32 v[52:53], v[52:53], v[14:15] op_sel_hi:[1,0]\n" \
"v_pk_mul_f32 v[54:55], v[54:55], v[14:15] op_sel_hi:[1,0]\n" \
"v_cvt_pk_bf16_f32 v96, v48, v49\n" \
"v_cvt_pk_bf16_f32 v97, v50, v51\n" \
"v_cvt_pk_bf16_f32 v98, v52, v53\n" \
"v_cvt_pk_bf16_f32 v99, v54, v55\n" \
"ds_read_b128 v[48:51], v2 offset:32768\n" \
"ds_read_b128 v[52:55], v2 offset:33792\n" \
"v_pk_mul_f32 v[56:57], v[56:57], v[14:15] op_sel_hi:[1,0]\n" \
"v_pk_mul_f32 v[58:59], v[58:59], v[14:15] op_sel_hi:[1,0]\n" \
"v_pk_mul_f32 v[60:61], v[60:61], v[14:15] op_sel_hi:[1,0]\n" \
"v_pk_mul_f32 v[62:63], v[62:63], v[14:15] op_sel_hi:[1,0]\n" \
"v_cvt_pk_bf16_f32 v100, v56, v57\n" \
"v_cvt_pk_bf16_f32 v101, v58, v59\n" \
"v_cvt_pk_bf16_f32 v102, v60, v61\n" \
"v_cvt_pk_bf16_f32 v103, v62, v63\n" \
"ds_read_b128 v[56:59], v2 offset:34816\n" \
"ds_read_b128 v[60:63], v2 offset:35840\n" \
"s_cmp_eq_u32 s49, 64\n" \
"s_nop 0\n" \
"s_nop 0\n" \
"s_nop 0\n" \
"s_cbranch_scc1 .Lq_end_%=\n" \
"v_mov_b64 v[80:81], 0\n" \
"v_mov_b64 v[82:83], 0\n" \
"v_mov_b64 v[84:85], 0\n" \
"v_mov_b64 v[86:87], 0\n" \
"v_mov_b64 v[88:89], 0\n" \
"v_mov_b64 v[90:91], 0\n" \
"v_mov_b64 v[92:93], 0\n" \
"v_mov_b64 v[94:95], 0\n" \
"v_mov_b32 v13, -1\n" \
"v_add_u32_e64 v14, s62, v3\n" \
"v_xor_b32 v31, 0x400, v11\n" \
"s_max_u32 s53, s49, 1\n" \
"s_sub_u32 s53, s53, 1\n" \
"v_mov_b32 v30, s53\n" \
"s_mov_b32 m0, s42\n" \
"buffer_store_dwordx4 v[80:83], v7, s[36:39], s59 offen sc1\n" \
"buffer_store_dword v24, v10, s[36:39], s93 offen sc1\n" \
"s_waitcnt lgkmcnt(0)\n" \
"s_mov_b32 s54, 0\n" \
"s_cmp_lg_u32 s48, 0\n" \
"s_cbranch_scc1 .Lq_w4_%=\n" \
"s_waitcnt vmcnt(2)\n" \
"v_cmp_eq_u32_e64 vcc, 0, v64\n" \
"s_nop 0\n" \
"s_and_b64 vcc, vcc, s[82:83]\n" \
"s_nop 0\n" \
"s_cbranch_vccnz .Lq_h_cold_%=\n" \
".Lq_h_ok_%=:\n" \
"v_subrev_u32 v64, s51, v64\n" \
"v_max_i32_dpp v65, v65, v65 row_ror:8 row_mask:0xf bank_mask:0xf\n" \
"s_nop 0\n" \
"v_max_i32_dpp v64, v64, v64 row_ror:8 row_mask:0xf bank_mask:0xf\n" \
"v_max_i32_dpp v65, v65, v65 row_ror:4 row_mask:0xf bank_mask:0xf\n" \
"s_nop 0\n" \
"v_max_i32_dpp v64, v64, v64 row_ror:4 row_mask:0xf bank_mask:0xf\n" \
"v_max_i32_dpp v65, v65, v65 row_ror:2 row_mask:0xf bank_mask:0xf\n" \
"s_nop 0\n" \
"v_max_i32_dpp v64, v64, v64 row_ror:2 row_mask:0xf bank_mask:0xf\n" \
"v_max_i32_dpp v65, v65, v65 row_ror:1 row_mask:0xf bank_mask:0xf\n" \
"s_nop 0\n" \
"v_max_i32_dpp v64, v64, v64 row_ror:1 row_mask:0xf bank_mask:0xf\n" \
"v_readlane_b32 s53, v65, 0\n" \
"v_readlane_b32 s50, v65, 16\n" \
"v_readlane_b32 s54, v64, 0\n" \
"v_readlane_b32 s40, v64, 16\n" \
"s_max_i32 s50, s50, s53\n" \
"s_max_i32 s40, s40, s54\n" \
"s_add_u32 s40, s40, s51\n" \
"s_add_u32 s40, s40, s44\n" \
"s_add_u32 s50, s50, s40\n" \
"s_add_u32 s50, s50, s45\n" \
"s_branch .Lq_loop_%=\n" \
".Lq_f0_cold_%=:\n" \
"s_nop 7\n" \
"s_nop 7\n" \
"s_waitcnt vmcnt(0) lgkmcnt(0)\n" \
"s_mov_b32 s53, 0\n" \
".Lq_f0_again_%=:\n" \
"s_cmp_lg_u32 s52, 0\n" \
"s_cbranch_scc1 .Lq_f0_ok_%=\n" \
"s_sleep 4\n" \
"buffer_load_dword v30, v15, s[36:39], s80 offen sc1\n" \
"s_waitcnt vmcnt(0)\n" \
"v_cmp_ge_u32_e64 s[96:97], s81, v30\n" \
"s_nop 4\n" \
"s_and_b32 s96, s96, s9\n" \
"s_cbranch_scc0 .Lq_f0_ok_%=\n" \
"s_add_u32 s53, s53, 1\n" \
"s_cmp_lt_u32 s53, 32768\n" \
"s_cbranch_scc1 .Lq_f0_again_%=\n" \
"s_mov_b32 s52, 1\n" \
"v_mov_b32 v30, 256\n" \
"v_mov_b32 v31, 1\n" \
"buffer_store_dword v31, v30, s[36:39], 0 offen sc1\n" \
"s_waitcnt vmcnt(0) lgkmcnt(0)\n" \
"s_branch .Lq_f0_ok_%=\n" \
".Lq_last_%=:\n" \
"ds_read_b64_tr_b16 v[76:77], v6 offset:3072\n" \
"ds_read_b64_tr_b16 v[78:79], v6 offset:3584\n" \
"ds_read_b128 v[96:99], v14 offset:32768\n" \
"ds_read_b128 v[100:103], v14 offset:33792\n" \
"buffer_store_dword v30, v10, s[36:39], s87 offen sc1\n" \
"buffer_load_dword v30, v15, s[36:39], s80 offen sc1\n" \
"s_waitcnt vmcnt(0) lgkmcnt(0)\n" \
"s_nop 7\n" \
"s_nop 7\n" \
"s_branch .Lq_tok_%=\n" \
".Lq_vr_%=:\n" \
"s_nop 7\n" \
"s_nop 7\n" \
"s_waitcnt vmcnt(0) lgkmcnt(0)\n" \
"v_min3_u32 v13, v104, v106, v106\n" \
"v_cmp_eq_u32 vcc, 0, v13\n" \
"s_nop 0\n" \
"s_cbranch_vccnz .Lq_v_cold_%=\n" \
".Lq_v_ok_%=:\n" \
"s_mov_b32 s23, s49\n" \
"s_cmp_eq_u64 s[26:27], 0\n" \
"s_cbranch_scc1 .Lq_vr3_%=\n" \
"v_mfma_f32_16x16x32_bf16 v[88:91], v[64:67], v[64:67], 0\n" \
"v_mfma_f32_16x16x32_bf16 v[80:83], v[32:35], v[64:67], 0\n" \
"v_mfma_f32_16x16x32_bf16 v[88:91], v[68:71], v[68:71], v[88:91]\n" \
"v_mfma_f32_16x16x32_bf16 v[88:91], v[72:75], v[72:75], v[88:91]\n" \
"v_mfma_f32_16x16x32_bf16 v[80:83], v[36:39], v[68:71], v[80:83]\n" \
"v_mfma_f32_16x16x32_bf16 v[88:91], v[76:79], v[76:79], v[88:91]\n" \
"v_mfma_f32_16x16x32_bf16 v[80:83], v[40:43], v[72:75], v[80:83]\n" \
"v_mfma_f32_16x16x32_bf16 v[80:83], v[44:47], v[76:79], v[80:83]\n" \
"v_mfma_f32_16x16x32_bf16 v[84:87], v[48:51], v[64:67], 0\n" \
"v_mfma_f32_16x16x32_bf16 v[84:87], v[52:55], v[68:71], v[84:87]\n" \
"v_mfma_f32_16x16x32_bf16 v[84:87], v[56:59], v[72:75], v[84:87]\n" \
"v_max3_f32 v13, v90, v91, v80\n" \
"v_mfma_f32_16x16x32_bf16 v[84:87], v[60:63], v[76:79], v[84:87]\n" \
"v_max3_f32 v14, v81, v82, v83\n" \
"s_nop 6\n" \
"v_max3_f32 v14, v14, v84, v85\n" \
"v_max3_f32 v13, v13, v86, v87\n" \
"v_max_f32 v13, v13, v14\n" \
"v_max_f32 v14, v13, v14\n" \
"s_nop 1\n" \
"v_permlane16_swap_b32 v13, v14\n" \
"v_max_f32 v13, v13, v14\n" \
"v_max_f32 v14, v13, v14\n" \
"s_nop 1\n" \
"v_permlane32_swap_b32 v13, v14\n" \
"v_max_f32 v14, v13, v14\n" \
"v_pk_add_f32 v[80:81], v[80:81], v[14:15] op_sel_hi:[1,0] neg_lo:[0,1] neg_hi:[0,1]\n" \
"v_pk_add_f32 v[82:83], v[82:83], v[14:15] op_sel_hi:[1,0] neg_lo:[0,1] neg_hi:[0,1]\n" \
"v_pk_add_f32 v[84:85], v[84:85], v[14:15] op_sel_hi:[1,0] neg_lo:[0,1] neg_hi:[0,1]\n" \
"v_pk_add_f32 v[86:87], v[86:87], v[14:15] op_sel_hi:[1,0] neg_lo:[0,1] neg_hi:[0,1]\n" \
"v_pk_add_f32 v[90:91], v[90:91], v[14:15] op_sel_hi:[1,0] neg_lo:[0,1] neg_hi:[0,1]\n" \
"v_exp_f32 v80, v80\n" \
"v_exp_f32 v81, v81\n" \
"v_exp_f32 v82, v82\n" \
"v_exp_f32 v83, v83\n" \
"v_exp_f32 v84, v84\n" \
"v_exp_f32 v85, v85\n" \
"v_exp_f32 v86, v86\n" \
"v_exp_f32 v87, v87\n" \
"v_cvt_pk_bf16_f32 v80, v80, v81\n" \
"v_cvt_pk_bf16_f32 v81, v82, v83\n" \
"v_cvt_pk_bf16_f32 v82, v84, v85\n" \
"v_cvt_pk_bf16_f32 v83, v86, v87\n" \
"v_exp_f32 v90, v90\n" \
"s_nop 0\n" \
"v_mfma_f32_16x16x32_bf16 v[84:87], v[96:99], v[80:83], 0\n" \
"v_exp_f32 v91, v91\n" \
"v_mfma_f32_16x16x32_bf16 v[92:95], v[100:103], v[80:83], 0\n" \
"v_cvt_pk_bf16_f32 v26, v90, v91\n" \
"v_mfma_f32_16x16x32_bf16 v[16:19], v[20:23], v[80:83], 0\n" \
".Lq_vr3_%=:\n" \
"s_nop 7\n" \
"s_nop 7\n" \
"v_mfma_f32_16x16x32_bf16 v[84:87], v[96:99], v[80:83], 0\n" \
"v_mfma_f32_16x16x32_bf16 v[92:95], v[100:103], v[80:83], 0\n" \
"v_mfma_f32_16x16x32_bf16 v[16:19], v[20:23], v[80:83], 0\n" \
"v_mfma_f32_16x16x32_bf16 v[16:19], v[20:23], v[26:29], v[16:19]\n" \
"v_mfma_f32_16x16x32_bf16 v[84:87], v[104:107], v[26:29], v[84:87]\n" \
"v_mfma_f32_16x16x32_bf16 v[92:95], v[104:107], v[24:27], v[92:95]\n" \
"v_rcp_f32 v14, v16\n" \
"s_mov_b64 vcc, 0\n" \
"s_mov_b64 s[26:27], 0\n" \
"s_nop 7\n" \
"s_nop 7\n" \
"s_branch .Lq_vr2_%=\n" \
".Lq_v_cold_%=:\n" \
"s_nop 7\n" \
"s_nop 7\n" \
"s_waitcnt vmcnt(0) lgkmcnt(0)\n" \
"s_mov_b32 s53, 0\n" \
".Lq_v_again_%=:\n" \
"s_cmp_lg_u32 s52, 0\n" \
"s_cbranch_scc1 .Lq_v_ok_%=\n" \
"buffer_load_dword v104, v8, s[36:39], s58 offen offset:0 sc1\n" \
"buffer_load_dword v106, v8, s[36:39], s58 offen offset:256 sc1\n" \
"s_waitcnt vmcnt(0)\n" \
"v_min3_u32 v13, v104, v106, v106\n" \
"v_cmp_eq_u32 vcc, 0, v13\n" \
"s_nop 4\n" \
"s_cbranch_vccz .Lq_v_ok_%=\n" \
"s_add_u32 s53, s53, 1\n" \
"s_cmp_lt_u32 s53, 32768\n" \
"s_cbranch_scc1 .Lq_v_again_%=\n" \
"s_mov_b32 s52, 1\n" \
"v_mov_b32 v13, 256\n" \
"v_mov_b32 v14, 1\n" \
"buffer_store_dword v14, v13, s[36:39], 0 offen sc1\n" \
"s_waitcnt vmcnt(0) lgkmcnt(0)\n" \
"s_branch .Lq_v_ok_%=\n" \
".Lq_f_cold_%=:\n" \
"s_nop 7\n" \
"s_nop 7\n" \
"s_waitcnt vmcnt(0) lgkmcnt(0)\n" \
"s_mov_b32 s53, 0\n" \
".Lq_f_again_%=:\n" \
"s_cmp_lg_u32 s52, 0\n" \
"s_cbranch_scc1 .Lq_f_ok_%=\n" \
"s_sleep 4\n" \
"buffer_load_dword v30, v15, s[36:39], s80 offen sc1\n" \
"s_waitcnt vmcnt(0)\n" \
"v_cmp_ge_u32_e64 s[96:97], s81, v30\n" \
"s_nop 4\n" \
"s_and_b32 s96, s96, s9\n" \
"s_cbranch_scc0 .Lq_f_ok_%=\n" \
"s_add_u32 s53, s53, 1\n" \
"s_cmp_lt_u32 s53, 32768\n" \
"s_cbranch_scc1 .Lq_f_again_%=\n" \
"s_mov_b32 s52, 1\n" \
"v_mov_b32 v30, 256\n" \
"v_mov_b32 v31, 1\n" \
"buffer_store_dword v31, v30, s[36:39], 0 offen sc1\n" \
"s_waitcnt vmcnt(0) lgkmcnt(0)\n" \
"s_branch .Lq_f_ok_%=\n" \
".Lq_v2_cold_%=:\n" \
"s_nop 7\n" \
"s_nop 7\n" \
"s_waitcnt vmcnt(0) lgkmcnt(0)\n" \
"s_mov_b32 s53, 0\n" \
".Lq_v2_again_%=:\n" \
"s_cmp_lg_u32 s52, 0\n" \
"s_cbranch_scc1 .Lq_v2_ok_%=\n" \
"s_sleep 1\n" \
"v_xor_b32 v31, 512, v8\n" \
"buffer_load_dword v104, v8, s[36:39], s61 offen offset:0 sc1\n" \
"buffer_load_dword v106, v8, s[36:39], s61 offen offset:256 sc1\n" \
"buffer_load_dword v108, v31, s[36:39], s61 offen offset:0 sc1\n" \
"buffer_load_dword v110, v31, s[36:39], s61 offen offset:256 sc1\n" \
"s_waitcnt vmcnt(1)\n" \
"v_min3_u32 v13, v104, v106, v108\n" \
"s_waitcnt vmcnt(0)\n" \
"v_min3_u32 v13, v13, v110, v110\n" \
"v_cmp_eq_u32 vcc, 0, v13\n" \
"s_nop 4\n" \
"s_cbranch_vccz .Lq_v2_ok_%=\n" \
"s_add_u32 s53, s53, 1\n" \
"s_cmp_lt_u32 s53, 32768\n" \
"s_cbranch_scc1 .Lq_v2_again_%=\n" \
"s_mov_b32 s52, 1\n" \
"v_mov_b32 v13, 256\n" \
"v_mov_b32 v14, 1\n" \
"buffer_store_dword v14, v13, s[36:39], 0 offen sc1\n" \
"s_waitcnt vmcnt(0) lgkmcnt(0)\n" \
"s_branch .Lq_v2_ok_%=\n" \
".Lq_h_cold_%=:\n" \
"s_nop 7\n" \
"s_nop 7\n" \
"s_waitcnt vmcnt(0) lgkmcnt(0)\n" \
"s_mov_b32 s53, 0\n" \
".Lq_h_again_%=:\n" \
"s_cmp_lg_u32 s52, 0\n" \
"s_cbranch_scc1 .Lq_h_ok_%=\n" \
"s_sleep 1\n" \
"buffer_load_dwordx2 v[64:65], v9, s[36:39], s11 offen sc1\n" \
"s_waitcnt vmcnt(0)\n" \
"v_cmp_eq_u32 vcc, 0, v64\n" \
"s_nop 0\n" \
"s_and_b64 vcc, vcc, s[82:83]\n" \
"s_nop 4\n" \
"s_cbranch_vccz .Lq_h_ok_%=\n" \
"s_add_u32 s53, s53, 1\n" \
"s_cmp_lt_u32 s53, 32768\n" \
"s_cbranch_scc1 .Lq_h_again_%=\n" \
"s_mov_b32 s52, 1\n" \
"v_mov_b32 v18, 256\n" \
"v_mov_b32 v19, 1\n" \
"buffer_store_dword v19, v18, s[36:39], 0 offen sc1\n" \
"s_waitcnt vmcnt(0) lgkmcnt(0)\n" \
"s_branch .Lq_h_ok_%=\n" \
".Lq_soon_%=:\n" \
"s_memrealtime s[56:57]\n" \
"s_mov_b32 s54, 100\n" \
"s_mov_b32 m0, s42\n" \
"s_cmp_lg_u32 s13, s49\n" \
"s_cselect_b32 s12, 0, s12\n" \
"s_mov_b32 s13, s49\n" \
"s_add_u32 s12, s12, 1\n" \
"s_cmp_gt_u32 s12, 3\n" \
"s_cbranch_scc1 .Lq_retry_%=\n" \
"ds_read_b128 v[32:35], v1 offset:0\n" \
"ds_read_b128 v[36:39], v1 offset:1024\n" \
"s_nop 3\n" \
"v_mov_b64 v[80:81], 0\n" \
"v_mov_b64 v[82:83], 0\n" \
"v_mov_b64 v[84:85], 0\n" \
"v_mov_b64 v[86:87], 0\n" \
"v_mov_b64 v[88:89], 0\n" \
"v_mov_b64 v[90:91], 0\n" \
"v_mov_b64 v[92:93], 0\n" \
"v_mov_b64 v[94:95], 0\n" \
"v_mov_b32 v13, -1\n" \
"s_waitcnt lgkmcnt(0)\n" \
"s_barrier\n" \
"s_branch .Lq_go_%=\n" \
".Lq_retry_%=:\n" \
"s_nop 7\n" \
"s_nop 7\n" \
"s_waitcnt vmcnt(0) lgkmcnt(0)\n" \
"ds_read_b128 v[32:35], v1 offset:0\n" \
"ds_read_b128 v[36:39], v1 offset:1024\n" \
"ds_read_b128 v[40:43], v1 offset:2048\n" \
"ds_read_b128 v[44:47], v1 offset:3072\n" \
"ds_read_b128 v[48:51], v2 offset:32768\n" \
"ds_read_b128 v[52:55], v2 offset:33792\n" \
"ds_read_b128 v[56:59], v2 offset:34816\n" \
"ds_read_b128 v[60:63], v2 offset:35840\n" \
"v_mov_b64 v[80:81], 0\n" \
"v_mov_b64 v[82:83], 0\n" \
"v_mov_b64 v[84:85], 0\n" \
"v_mov_b64 v[86:87], 0\n" \
"v_mov_b64 v[88:89], 0\n" \
"v_mov_b64 v[90:91], 0\n" \
"v_mov_b64 v[92:93], 0\n" \
"v_mov_b64 v[94:95], 0\n" \
"v_mov_b32 v13, -1\n" \
"v_add_u32 v14, s62, v3\n" \
"v_xor_b32 v31, 0x400, v11\n" \
"s_max_u32 s53, s49, 1\n" \
"s_sub_u32 s53, s53, 1\n" \
"v_mov_b32 v30, s53\n" \
"s_mov_b32 m0, s42\n" \
"s_memrealtime s[56:57]\n" \
"s_mov_b32 s54, 100\n" \
"s_waitcnt vmcnt(0) lgkmcnt(0)\n" \
"s_barrier\n" \
"s_add_u32 s88, s88, 1\n" \
"s_cmp_lt_u32 s88, 32768\n" \
"s_cbranch_scc1 .Lq_r1_%=\n" \
"s_mov_b32 s52, 1\n" \
"v_mov_b32 v13, 256\n" \
"v_mov_b32 v14, 1\n" \
"buffer_store_dword v14, v13, s[36:39], 0 offen sc1\n" \
"s_waitcnt vmcnt(0) lgkmcnt(0)\n" \
"s_endpgm\n" \
".Lq_r1_%=:\n" \
"s_cmp_lg_u32 s49, 0\n" \
"s_cbranch_scc1 .Lq_r2_%=\n" \
"s_sleep 8\n" \
".Lq_r2_%=:\n" \
"s_branch .Lq_go_%=\n" \
".Lq_end_%=:\n" \
"s_nop 7\n" \
"s_nop 7\n" \
"v_xor_b32 v31, 0x400, v11\n" \
"buffer_store_dwordx4 v[96:99], v11, s[36:39], s95 offen sc1\n" \
"buffer_store_dwordx4 v[100:103], v31, s[36:39], s95 offen sc1\n" \
"s_waitcnt vmcnt(0) lgkmcnt(0)\n" \
"s_barrier\n" \
"v_mov_b32 v31, 64\n" \
"buffer_store_dword v31, v10, s[36:39], s87 offen sc1\n" \
"s_waitcnt vmcnt(0) lgkmcnt(0)\n" \
"s_endpgm\n"
#define ASM_VO \
"s_nop 7\n" \
"s_nop 7\n" \
"buffer_load_dword v20, v13, s[68:71], 0 offen offset:0\n" \
"buffer_load_dword v21, v13, s[68:71], 0 offen offset:8\n" \
"buffer_load_dword v22, v13, s[68:71], 0 offen offset:16\n" \
"buffer_load_dword v23, v13, s[68:71], 0 offen offset:24\n" \
"v_mov_b32 v16, 0\n" \
"v_mov_b32 v17, 0\n" \
"v_mov_b32 v18, 0\n" \
"v_mov_b32 v19, 0\n" \
"s_mov_b32 s54, 0\n" \
"buffer_store_dword v16, v7, s[36:39], s54 offen sc1\n" \
"s_mov_b32 s54, 131072\n" \
"buffer_store_dword v16, v7, s[36:39], s54 offen sc1\n" \
"s_mov_b32 s54, 65536\n" \
"buffer_store_dwordx4 v[16:19], v8, s[36:39], s54 offen sc1\n" \
"s_mov_b32 s54, 262144\n" \
"buffer_store_dword v16, v7, s[36:39], s54 offen sc1\n" \
"s_mov_b32 s54, 131072\n" \
"buffer_store_dwordx4 v[16:19], v8, s[36:39], s54 offen sc1\n" \
"s_mov_b32 s54, 393216\n" \
"buffer_store_dword v16, v7, s[36:39], s54 offen sc1\n" \
"s_mov_b32 s54, 196608\n" \
"buffer_store_dwordx4 v[16:19], v8, s[36:39], s54 offen sc1\n" \
"s_mul_i32 s62, s48, 40960\n" \
"v_add_u32 v32, 40960, v3\n" \
"v_add_u32 v33, 81920, v3\n" \
"v_add_u32 v34, 122880, v3\n" \
"s_add_u32 m0, s62, 0\n" \
"s_mov_b32 s54, 0\n" \
"buffer_load_dwordx4 v1, s[40:43], s54 offen nt lds\n" \
"s_add_u32 m0, s62, 1024\n" \
"s_mov_b32 s54, 65536\n" \
"buffer_load_dwordx4 v1, s[40:43], s54 offen nt lds\n" \
"s_add_u32 m0, s62, 2048\n" \
"s_mov_b32 s54, 131072\n" \
"buffer_load_dwordx4 v1, s[40:43], s54 offen nt lds\n" \
"s_add_u32 m0, s62, 3072\n" \
"s_mov_b32 s54, 196608\n" \
"buffer_load_dwordx4 v1, s[40:43], s54 offen nt lds\n" \
"s_add_u32 m0, s62, 4096\n" \
"s_mov_b32 s54, 262144\n" \
"buffer_load_dwordx4 v1, s[40:43], s54 offen nt lds\n" \
"s_add_u32 m0, s62, 5120\n" \
"s_mov_b32 s54, 327680\n" \
"buffer_load_dwordx4 v1, s[40:43], s54 offen nt lds\n" \
"s_add_u32 m0, s62, 6144\n" \
"s_mov_b32 s54, 393216\n" \
"buffer_load_dwordx4 v1, s[40:43], s54 offen nt lds\n" \
"s_add_u32 m0, s62, 7168\n" \
"s_mov_b32 s54, 458752\n" \
"buffer_load_dwordx4 v1, s[40:43], s54 offen nt lds\n" \
"s_add_u32 m0, s62, 8192\n" \
"s_mov_b32 s54, 524288\n" \
"buffer_load_dwordx4 v1, s[40:43], s54 offen nt lds\n" \
"s_add_u32 m0, s62, 9216\n" \
"s_mov_b32 s54, 589824\n" \
"buffer_load_dwordx4 v1, s[40:43], s54 offen nt lds\n" \
"s_add_u32 m0, s62, 10240\n" \
"s_mov_b32 s54, 655360\n" \
"buffer_load_dwordx4 v1, s[40:43], s54 offen nt lds\n" \
"s_add_u32 m0, s62, 11264\n" \
"s_mov_b32 s54, 720896\n" \
"buffer_load_dwordx4 v1, s[40:43], s54 offen nt lds\n" \
"s_add_u32 m0, s62, 12288\n" \
"s_mov_b32 s54, 786432\n" \
"buffer_load_dwordx4 v1, s[40:43], s54 offen nt lds\n" \
"s_add_u32 m0, s62, 13312\n" \
"s_mov_b32 s54, 851968\n" \
"buffer_load_dwordx4 v1, s[40:43], s54 offen nt lds\n" \
"s_add_u32 m0, s62, 14336\n" \
"s_mov_b32 s54, 917504\n" \
"buffer_load_dwordx4 v1, s[40:43], s54 offen nt lds\n" \
"s_add_u32 m0, s62, 15360\n" \
"s_mov_b32 s54, 983040\n" \
"buffer_load_dwordx4 v1, s[40:43], s54 offen nt lds\n" \
"s_add_u32 m0, s62, 16384\n" \
"s_mov_b32 s54, 1048576\n" \
"buffer_load_dwordx4 v1, s[40:43], s54 offen nt lds\n" \
"s_add_u32 m0, s62, 17408\n" \
"s_mov_b32 s54, 1114112\n" \
"buffer_load_dwordx4 v1, s[40:43], s54 offen nt lds\n" \
"s_add_u32 m0, s62, 18432\n" \
"s_mov_b32 s54, 1179648\n" \
"buffer_load_dwordx4 v1, s[40:43], s54 offen nt lds\n" \
"s_add_u32 m0, s62, 19456\n" \
"s_mov_b32 s54, 1245184\n" \
"buffer_load_dwordx4 v1, s[40:43], s54 offen nt lds\n" \
"s_add_u32 m0, s62, 20480\n" \
"s_mov_b32 s54, 1310720\n" \
"buffer_load_dwordx4 v1, s[40:43], s54 offen nt lds\n" \
"s_add_u32 m0, s62, 21504\n" \
"s_mov_b32 s54, 1376256\n" \
"buffer_load_dwordx4 v1, s[40:43], s54 offen nt lds\n" \
"s_add_u32 m0, s62, 22528\n" \
"s_mov_b32 s54, 1441792\n" \
"buffer_load_dwordx4 v1, s[40:43], s54 offen nt lds\n" \
"s_add_u32 m0, s62, 23552\n" \
"s_mov_b32 s54, 1507328\n" \
"buffer_load_dwordx4 v1, s[40:43], s54 offen nt lds\n" \
"s_add_u32 m0, s62, 24576\n" \
"s_mov_b32 s54, 1572864\n" \
"buffer_load_dwordx4 v1, s[40:43], s54 offen nt lds\n" \
"s_add_u32 m0, s62, 25600\n" \
"s_mov_b32 s54, 1638400\n" \
"buffer_load_dwordx4 v1, s[40:43], s54 offen nt lds\n" \
"s_add_u32 m0, s62, 26624\n" \
"s_mov_b32 s54, 1703936\n" \
"buffer_load_dwordx4 v1, s[40:43], s54 offen nt lds\n" \
"s_add_u32 m0, s62, 27648\n" \
"s_mov_b32 s54, 1769472\n" \
"buffer_load_dwordx4 v1, s[40:43], s54 offen nt lds\n" \
"s_add_u32 m0, s62, 28672\n" \
"s_mov_b32 s54, 1835008\n" \
"buffer_load_dwordx4 v1, s[40:43], s54 offen nt lds\n" \
"s_add_u32 m0, s62, 29696\n" \
"s_mov_b32 s54, 1900544\n" \
"buffer_load_dwordx4 v1, s[40:43], s54 offen nt lds\n" \
"s_add_u32 m0, s62, 30720\n" \
"s_mov_b32 s54, 1966080\n" \
"buffer_load_dwordx4 v1, s[40:43], s54 offen nt lds\n" \
"s_add_u32 m0, s62, 31744\n" \
"s_mov_b32 s54, 2031616\n" \
"buffer_load_dwordx4 v1, s[40:43], s54 offen nt lds\n" \
"s_add_u32 m0, s62, 32768\n" \
"s_mov_b32 s54, 0\n" \
"buffer_load_dwordx4 v2, s[64:67], s54 offen lds\n" \
"s_add_u32 m0, s62, 33792\n" \
"s_mov_b32 s54, 32768\n" \
"buffer_load_dwordx4 v2, s[64:67], s54 offen lds\n" \
"s_add_u32 m0, s62, 34816\n" \
"s_mov_b32 s54, 65536\n" \
"buffer_load_dwordx4 v2, s[64:67], s54 offen lds\n" \
"s_add_u32 m0, s62, 35840\n" \
"s_mov_b32 s54, 98304\n" \
"buffer_load_dwordx4 v2, s[64:67], s54 offen lds\n" \
"s_add_u32 m0, s62, 36864\n" \
"s_mov_b32 s54, 131072\n" \
"buffer_load_dwordx4 v2, s[64:67], s54 offen lds\n" \
"s_add_u32 m0, s62, 37888\n" \
"s_mov_b32 s54, 163840\n" \
"buffer_load_dwordx4 v2, s[64:67], s54 offen lds\n" \
"s_add_u32 m0, s62, 38912\n" \
"s_mov_b32 s54, 196608\n" \
"buffer_load_dwordx4 v2, s[64:67], s54 offen lds\n" \
"s_add_u32 m0, s62, 39936\n" \
"s_mov_b32 s54, 229376\n" \
"buffer_load_dwordx4 v2, s[64:67], s54 offen lds\n" \
"s_waitcnt vmcnt(32)\n" \
"s_barrier\n" \
"ds_read_b64_tr_b16 v[128:129], v3 offset:0\n" \
"ds_read_b64_tr_b16 v[130:131], v3 offset:512\n" \
"ds_read_b64_tr_b16 v[132:133], v3 offset:4096\n" \
"ds_read_b64_tr_b16 v[134:135], v3 offset:4608\n" \
"ds_read_b64_tr_b16 v[160:161], v32 offset:0\n" \
"ds_read_b64_tr_b16 v[162:163], v32 offset:512\n" \
"ds_read_b64_tr_b16 v[164:165], v32 offset:4096\n" \
"ds_read_b64_tr_b16 v[166:167], v32 offset:4608\n" \
"ds_read_b64_tr_b16 v[192:193], v33 offset:0\n" \
"ds_read_b64_tr_b16 v[194:195], v33 offset:512\n" \
"ds_read_b64_tr_b16 v[196:197], v33 offset:4096\n" \
"ds_read_b64_tr_b16 v[198:199], v33 offset:4608\n" \
"ds_read_b64_tr_b16 v[224:225], v34 offset:0\n" \
"ds_read_b64_tr_b16 v[226:227], v34 offset:512\n" \
"ds_read_b64_tr_b16 v[228:229], v34 offset:4096\n" \
"ds_read_b64_tr_b16 v[230:231], v34 offset:4608\n" \
"s_waitcnt lgkmcnt(0)\n" \
"s_barrier\n" \
"s_add_u32 m0, s62, 0\n" \
"s_mov_b32 s54, 262144\n" \
"buffer_load_dwordx4 v2, s[64:67], s54 offen lds\n" \
"s_add_u32 m0, s62, 1024\n" \
"s_mov_b32 s54, 294912\n" \
"buffer_load_dwordx4 v2, s[64:67], s54 offen lds\n" \
"s_add_u32 m0, s62, 2048\n" \
"s_mov_b32 s54, 327680\n" \
"buffer_load_dwordx4 v2, s[64:67], s54 offen lds\n" \
"s_add_u32 m0, s62, 3072\n" \
"s_mov_b32 s54, 360448\n" \
"buffer_load_dwordx4 v2, s[64:67], s54 offen lds\n" \
"s_add_u32 m0, s62, 4096\n" \
"s_mov_b32 s54, 393216\n" \
"buffer_load_dwordx4 v2, s[64:67], s54 offen lds\n" \
"s_add_u32 m0, s62, 5120\n" \
"s_mov_b32 s54, 425984\n" \
"buffer_load_dwordx4 v2, s[64:67], s54 offen lds\n" \
"s_add_u32 m0, s62, 6144\n" \
"s_mov_b32 s54, 458752\n" \
"buffer_load_dwordx4 v2, s[64:67], s54 offen lds\n" \
"s_add_u32 m0, s62, 7168\n" \
"s_mov_b32 s54, 491520\n" \
"buffer_load_dwordx4 v2, s[64:67], s54 offen lds\n" \
"s_waitcnt vmcnt(32)\n" \
"s_barrier\n" \
"ds_read_b64_tr_b16 v[136:137], v3 offset:8192\n" \
"ds_read_b64_tr_b16 v[138:139], v3 offset:8704\n" \
"ds_read_b64_tr_b16 v[140:141], v3 offset:12288\n" \
"ds_read_b64_tr_b16 v[142:143], v3 offset:12800\n" \
"ds_read_b64_tr_b16 v[168:169], v32 offset:8192\n" \
"ds_read_b64_tr_b16 v[170:171], v32 offset:8704\n" \
"ds_read_b64_tr_b16 v[172:173], v32 offset:12288\n" \
"ds_read_b64_tr_b16 v[174:175], v32 offset:12800\n" \
"ds_read_b64_tr_b16 v[200:201], v33 offset:8192\n" \
"ds_read_b64_tr_b16 v[202:203], v33 offset:8704\n" \
"ds_read_b64_tr_b16 v[204:205], v33 offset:12288\n" \
"ds_read_b64_tr_b16 v[206:207], v33 offset:12800\n" \
"ds_read_b64_tr_b16 v[232:233], v34 offset:8192\n" \
"ds_read_b64_tr_b16 v[234:235], v34 offset:8704\n" \
"ds_read_b64_tr_b16 v[236:237], v34 offset:12288\n" \
"ds_read_b64_tr_b16 v[238:239], v34 offset:12800\n" \
"s_waitcnt lgkmcnt(0)\n" \
"s_barrier\n" \
"s_add_u32 m0, s62, 8192\n" \
"s_mov_b32 s54, 524288\n" \
"buffer_load_dwordx4 v2, s[64:67], s54 offen lds\n" \
"s_add_u32 m0, s62, 9216\n" \
"s_mov_b32 s54, 557056\n" \
"buffer_load_dwordx4 v2, s[64:67], s54 offen lds\n" \
"s_add_u32 m0, s62, 10240\n" \
"s_mov_b32 s54, 589824\n" \
"buffer_load_dwordx4 v2, s[64:67], s54 offen lds\n" \
"s_add_u32 m0, s62, 11264\n" \
"s_mov_b32 s54, 622592\n" \
"buffer_load_dwordx4 v2, s[64:67], s54 offen lds\n" \
"s_add_u32 m0, s62, 12288\n" \
"s_mov_b32 s54, 655360\n" \
"buffer_load_dwordx4 v2, s[64:67], s54 offen lds\n" \
"s_add_u32 m0, s62, 13312\n" \
"s_mov_b32 s54, 688128\n" \
"buffer_load_dwordx4 v2, s[64:67], s54 offen lds\n" \
"s_add_u32 m0, s62, 14336\n" \
"s_mov_b32 s54, 720896\n" \
"buffer_load_dwordx4 v2, s[64:67], s54 offen lds\n" \
"s_add_u32 m0, s62, 15360\n" \
"s_mov_b32 s54, 753664\n" \
"buffer_load_dwordx4 v2, s[64:67], s54 offen lds\n" \
"s_waitcnt vmcnt(32)\n" \
"s_barrier\n" \
"ds_read_b64_tr_b16 v[144:145], v3 offset:16384\n" \
"ds_read_b64_tr_b16 v[146:147], v3 offset:16896\n" \
"ds_read_b64_tr_b16 v[148:149], v3 offset:20480\n" \
"ds_read_b64_tr_b16 v[150:151], v3 offset:20992\n" \
"ds_read_b64_tr_b16 v[176:177], v32 offset:16384\n" \
"ds_read_b64_tr_b16 v[178:179], v32 offset:16896\n" \
"ds_read_b64_tr_b16 v[180:181], v32 offset:20480\n" \
"ds_read_b64_tr_b16 v[182:183], v32 offset:20992\n" \
"ds_read_b64_tr_b16 v[208:209], v33 offset:16384\n" \
"ds_read_b64_tr_b16 v[210:211], v33 offset:16896\n" \
"ds_read_b64_tr_b16 v[212:213], v33 offset:20480\n" \
"ds_read_b64_tr_b16 v[214:215], v33 offset:20992\n" \
"ds_read_b64_tr_b16 v[240:241], v34 offset:16384\n" \
"ds_read_b64_tr_b16 v[242:243], v34 offset:16896\n" \
"ds_read_b64_tr_b16 v[244:245], v34 offset:20480\n" \
"ds_read_b64_tr_b16 v[246:247], v34 offset:20992\n" \
"s_waitcnt lgkmcnt(0)\n" \
"s_barrier\n" \
"s_add_u32 m0, s62, 16384\n" \
"s_mov_b32 s54, 786432\n" \
"buffer_load_dwordx4 v2, s[64:67], s54 offen lds\n" \
"s_add_u32 m0, s62, 17408\n" \
"s_mov_b32 s54, 819200\n" \
"buffer_load_dwordx4 v2, s[64:67], s54 offen lds\n" \
"s_add_u32 m0, s62, 18432\n" \
"s_mov_b32 s54, 851968\n" \
"buffer_load_dwordx4 v2, s[64:67], s54 offen lds\n" \
"s_add_u32 m0, s62, 19456\n" \
"s_mov_b32 s54, 884736\n" \
"buffer_load_dwordx4 v2, s[64:67], s54 offen lds\n" \
"s_add_u32 m0, s62, 20480\n" \
"s_mov_b32 s54, 917504\n" \
"buffer_load_dwordx4 v2, s[64:67], s54 offen lds\n" \
"s_add_u32 m0, s62, 21504\n" \
"s_mov_b32 s54, 950272\n" \
"buffer_load_dwordx4 v2, s[64:67], s54 offen lds\n" \
"s_add_u32 m0, s62, 22528\n" \
"s_mov_b32 s54, 983040\n" \
"buffer_load_dwordx4 v2, s[64:67], s54 offen lds\n" \
"s_add_u32 m0, s62, 23552\n" \
"s_mov_b32 s54, 1015808\n" \
"buffer_load_dwordx4 v2, s[64:67], s54 offen lds\n" \
"s_waitcnt vmcnt(32)\n" \
"s_barrier\n" \
"ds_read_b64_tr_b16 v[152:153], v3 offset:24576\n" \
"ds_read_b64_tr_b16 v[154:155], v3 offset:25088\n" \
"ds_read_b64_tr_b16 v[156:157], v3 offset:28672\n" \
"ds_read_b64_tr_b16 v[158:159], v3 offset:29184\n" \
"ds_read_b64_tr_b16 v[184:185], v32 offset:24576\n" \
"ds_read_b64_tr_b16 v[186:187], v32 offset:25088\n" \
"ds_read_b64_tr_b16 v[188:189], v32 offset:28672\n" \
"ds_read_b64_tr_b16 v[190:191], v32 offset:29184\n" \
"ds_read_b64_tr_b16 v[216:217], v33 offset:24576\n" \
"ds_read_b64_tr_b16 v[218:219], v33 offset:25088\n" \
"ds_read_b64_tr_b16 v[220:221], v33 offset:28672\n" \
"ds_read_b64_tr_b16 v[222:223], v33 offset:29184\n" \
"ds_read_b64_tr_b16 v[248:249], v34 offset:24576\n" \
"ds_read_b64_tr_b16 v[250:251], v34 offset:25088\n" \
"ds_read_b64_tr_b16 v[252:253], v34 offset:28672\n" \
"ds_read_b64_tr_b16 v[254:255], v34 offset:29184\n" \
"s_waitcnt lgkmcnt(0)\n" \
"s_barrier\n" \
"s_add_u32 m0, s62, 24576\n" \
"s_mov_b32 s54, 1048576\n" \
"buffer_load_dwordx4 v2, s[64:67], s54 offen lds\n" \
"s_add_u32 m0, s62, 25600\n" \
"s_mov_b32 s54, 1081344\n" \
"buffer_load_dwordx4 v2, s[64:67], s54 offen lds\n" \
"s_add_u32 m0, s62, 26624\n" \
"s_mov_b32 s54, 1114112\n" \
"buffer_load_dwordx4 v2, s[64:67], s54 offen lds\n" \
"s_add_u32 m0, s62, 27648\n" \
"s_mov_b32 s54, 1146880\n" \
"buffer_load_dwordx4 v2, s[64:67], s54 offen lds\n" \
"s_add_u32 m0, s62, 28672\n" \
"s_mov_b32 s54, 1179648\n" \
"buffer_load_dwordx4 v2, s[64:67], s54 offen lds\n" \
"s_add_u32 m0, s62, 29696\n" \
"s_mov_b32 s54, 1212416\n" \
"buffer_load_dwordx4 v2, s[64:67], s54 offen lds\n" \
"s_add_u32 m0, s62, 30720\n" \
"s_mov_b32 s54, 1245184\n" \
"buffer_load_dwordx4 v2, s[64:67], s54 offen lds\n" \
"s_add_u32 m0, s62, 31744\n" \
"s_mov_b32 s54, 1277952\n" \
"buffer_load_dwordx4 v2, s[64:67], s54 offen lds\n" \
"s_waitcnt vmcnt(36)\n" \
"ds_read_b64_tr_b16 a[0:1], v4 offset:32768\n" \
"ds_read_b64_tr_b16 a[2:3], v4 offset:32896\n" \
"ds_read_b64_tr_b16 a[4:5], v4 offset:34816\n" \
"ds_read_b64_tr_b16 a[6:7], v4 offset:34944\n" \
"ds_read_b64_tr_b16 a[8:9], v4 offset:32800\n" \
"ds_read_b64_tr_b16 a[10:11], v4 offset:32928\n" \
"ds_read_b64_tr_b16 a[12:13], v4 offset:34848\n" \
"ds_read_b64_tr_b16 a[14:15], v4 offset:34976\n" \
"s_waitcnt lgkmcnt(0)\n" \
"s_add_u32 m0, s62, 32768\n" \
"s_mov_b32 s54, 1310720\n" \
"buffer_load_dwordx4 v2, s[64:67], s54 offen lds\n" \
"s_add_u32 m0, s62, 33792\n" \
"s_mov_b32 s54, 1343488\n" \
"buffer_load_dwordx4 v2, s[64:67], s54 offen lds\n" \
"s_add_u32 m0, s62, 34816\n" \
"s_mov_b32 s54, 1376256\n" \
"buffer_load_dwordx4 v2, s[64:67], s54 offen lds\n" \
"s_add_u32 m0, s62, 35840\n" \
"s_mov_b32 s54, 1409024\n" \
"buffer_load_dwordx4 v2, s[64:67], s54 offen lds\n" \
"s_waitcnt vmcnt(36)\n" \
"ds_read_b64_tr_b16 a[16:17], v4 offset:36864\n" \
"ds_read_b64_tr_b16 a[18:19], v4 offset:36992\n" \
"ds_read_b64_tr_b16 a[20:21], v4 offset:38912\n" \
"ds_read_b64_tr_b16 a[22:23], v4 offset:39040\n" \
"ds_read_b64_tr_b16 a[24:25], v4 offset:36896\n" \
"ds_read_b64_tr_b16 a[26:27], v4 offset:37024\n" \
"ds_read_b64_tr_b16 a[28:29], v4 offset:38944\n" \
"ds_read_b64_tr_b16 a[30:31], v4 offset:39072\n" \
"s_waitcnt lgkmcnt(0)\n" \
"s_add_u32 m0, s62, 36864\n" \
"s_mov_b32 s54, 1441792\n" \
"buffer_load_dwordx4 v2, s[64:67], s54 offen lds\n" \
"s_add_u32 m0, s62, 37888\n" \
"s_mov_b32 s54, 1474560\n" \
"buffer_load_dwordx4 v2, s[64:67], s54 offen lds\n" \
"s_add_u32 m0, s62, 38912\n" \
"s_mov_b32 s54, 1507328\n" \
"buffer_load_dwordx4 v2, s[64:67], s54 offen lds\n" \
"s_add_u32 m0, s62, 39936\n" \
"s_mov_b32 s54, 1540096\n" \
"buffer_load_dwordx4 v2, s[64:67], s54 offen lds\n" \
"s_waitcnt vmcnt(36)\n" \
"ds_read_b64_tr_b16 a[32:33], v4 offset:0\n" \
"ds_read_b64_tr_b16 a[34:35], v4 offset:128\n" \
"ds_read_b64_tr_b16 a[36:37], v4 offset:2048\n" \
"ds_read_b64_tr_b16 a[38:39], v4 offset:2176\n" \
"ds_read_b64_tr_b16 a[40:41], v4 offset:32\n" \
"ds_read_b64_tr_b16 a[42:43], v4 offset:160\n" \
"ds_read_b64_tr_b16 a[44:45], v4 offset:2080\n" \
"ds_read_b64_tr_b16 a[46:47], v4 offset:2208\n" \
"s_waitcnt lgkmcnt(0)\n" \
"s_add_u32 m0, s62, 0\n" \
"s_mov_b32 s54, 1572864\n" \
"buffer_load_dwordx4 v2, s[64:67], s54 offen lds\n" \
"s_add_u32 m0, s62, 1024\n" \
"s_mov_b32 s54, 1605632\n" \
"buffer_load_dwordx4 v2, s[64:67], s54 offen lds\n" \
"s_add_u32 m0, s62, 2048\n" \
"s_mov_b32 s54, 1638400\n" \
"buffer_load_dwordx4 v2, s[64:67], s54 offen lds\n" \
"s_add_u32 m0, s62, 3072\n" \
"s_mov_b32 s54, 1671168\n" \
"buffer_load_dwordx4 v2, s[64:67], s54 offen lds\n" \
"s_waitcnt vmcnt(36)\n" \
"ds_read_b64_tr_b16 a[48:49], v4 offset:4096\n" \
"ds_read_b64_tr_b16 a[50:51], v4 offset:4224\n" \
"ds_read_b64_tr_b16 a[52:53], v4 offset:6144\n" \
"ds_read_b64_tr_b16 a[54:55], v4 offset:6272\n" \
"ds_read_b64_tr_b16 a[56:57], v4 offset:4128\n" \
"ds_read_b64_tr_b16 a[58:59], v4 offset:4256\n" \
"ds_read_b64_tr_b16 a[60:61], v4 offset:6176\n" \
"ds_read_b64_tr_b16 a[62:63], v4 offset:6304\n" \
"s_waitcnt lgkmcnt(0)\n" \
"s_add_u32 m0, s62, 4096\n" \
"s_mov_b32 s54, 1703936\n" \
"buffer_load_dwordx4 v2, s[64:67], s54 offen lds\n" \
"s_add_u32 m0, s62, 5120\n" \
"s_mov_b32 s54, 1736704\n" \
"buffer_load_dwordx4 v2, s[64:67], s54 offen lds\n" \
"s_add_u32 m0, s62, 6144\n" \
"s_mov_b32 s54, 1769472\n" \
"buffer_load_dwordx4 v2, s[64:67], s54 offen lds\n" \
"s_add_u32 m0, s62, 7168\n" \
"s_mov_b32 s54, 1802240\n" \
"buffer_load_dwordx4 v2, s[64:67], s54 offen lds\n" \
"s_waitcnt vmcnt(36)\n" \
"ds_read_b64_tr_b16 a[64:65], v4 offset:8192\n" \
"ds_read_b64_tr_b16 a[66:67], v4 offset:8320\n" \
"ds_read_b64_tr_b16 a[68:69], v4 offset:10240\n" \
"ds_read_b64_tr_b16 a[70:71], v4 offset:10368\n" \
"ds_read_b64_tr_b16 a[72:73], v4 offset:8224\n" \
"ds_read_b64_tr_b16 a[74:75], v4 offset:8352\n" \
"ds_read_b64_tr_b16 a[76:77], v4 offset:10272\n" \
"ds_read_b64_tr_b16 a[78:79], v4 offset:10400\n" \
"s_waitcnt lgkmcnt(0)\n" \
"s_add_u32 m0, s62, 8192\n" \
"s_mov_b32 s54, 1835008\n" \
"buffer_load_dwordx4 v2, s[64:67], s54 offen lds\n" \
"s_add_u32 m0, s62, 9216\n" \
"s_mov_b32 s54, 1867776\n" \
"buffer_load_dwordx4 v2, s[64:67], s54 offen lds\n" \
"s_add_u32 m0, s62, 10240\n" \
"s_mov_b32 s54, 1900544\n" \
"buffer_load_dwordx4 v2, s[64:67], s54 offen lds\n" \
"s_add_u32 m0, s62, 11264\n" \
"s_mov_b32 s54, 1933312\n" \
"buffer_load_dwordx4 v2, s[64:67], s54 offen lds\n" \
"s_waitcnt vmcnt(36)\n" \
"ds_read_b64_tr_b16 a[80:81], v4 offset:12288\n" \
"ds_read_b64_tr_b16 a[82:83], v4 offset:12416\n" \
"ds_read_b64_tr_b16 a[84:85], v4 offset:14336\n" \
"ds_read_b64_tr_b16 a[86:87], v4 offset:14464\n" \
"ds_read_b64_tr_b16 a[88:89], v4 offset:12320\n" \
"ds_read_b64_tr_b16 a[90:91], v4 offset:12448\n" \
"ds_read_b64_tr_b16 a[92:93], v4 offset:14368\n" \
"ds_read_b64_tr_b16 a[94:95], v4 offset:14496\n" \
"s_waitcnt lgkmcnt(0)\n" \
"s_add_u32 m0, s62, 12288\n" \
"s_mov_b32 s54, 1966080\n" \
"buffer_load_dwordx4 v2, s[64:67], s54 offen lds\n" \
"s_add_u32 m0, s62, 13312\n" \
"s_mov_b32 s54, 1998848\n" \
"buffer_load_dwordx4 v2, s[64:67], s54 offen lds\n" \
"s_add_u32 m0, s62, 14336\n" \
"s_mov_b32 s54, 2031616\n" \
"buffer_load_dwordx4 v2, s[64:67], s54 offen lds\n" \
"s_add_u32 m0, s62, 15360\n" \
"s_mov_b32 s54, 2064384\n" \
"buffer_load_dwordx4 v2, s[64:67], s54 offen lds\n" \
"s_waitcnt vmcnt(36)\n" \
"ds_read_b64_tr_b16 a[96:97], v4 offset:16384\n" \
"ds_read_b64_tr_b16 a[98:99], v4 offset:16512\n" \
"ds_read_b64_tr_b16 a[100:101], v4 offset:18432\n" \
"ds_read_b64_tr_b16 a[102:103], v4 offset:18560\n" \
"ds_read_b64_tr_b16 a[104:105], v4 offset:16416\n" \
"ds_read_b64_tr_b16 a[106:107], v4 offset:16544\n" \
"ds_read_b64_tr_b16 a[108:109], v4 offset:18464\n" \
"ds_read_b64_tr_b16 a[110:111], v4 offset:18592\n" \
"s_waitcnt vmcnt(32)\n" \
"ds_read_b64_tr_b16 a[112:113], v4 offset:20480\n" \
"ds_read_b64_tr_b16 a[114:115], v4 offset:20608\n" \
"ds_read_b64_tr_b16 a[116:117], v4 offset:22528\n" \
"ds_read_b64_tr_b16 a[118:119], v4 offset:22656\n" \
"ds_read_b64_tr_b16 a[120:121], v4 offset:20512\n" \
"ds_read_b64_tr_b16 a[122:123], v4 offset:20640\n" \
"ds_read_b64_tr_b16 a[124:125], v4 offset:22560\n" \
"ds_read_b64_tr_b16 a[126:127], v4 offset:22688\n" \
"s_waitcnt vmcnt(28)\n" \
"ds_read_b64_tr_b16 a[128:129], v4 offset:24576\n" \
"ds_read_b64_tr_b16 a[130:131], v4 offset:24704\n" \
"ds_read_b64_tr_b16 a[132:133], v4 offset:26624\n" \
"ds_read_b64_tr_b16 a[134:135], v4 offset:26752\n" \
"ds_read_b64_tr_b16 a[136:137], v4 offset:24608\n" \
"ds_read_b64_tr_b16 a[138:139], v4 offset:24736\n" \
"ds_read_b64_tr_b16 a[140:141], v4 offset:26656\n" \
"ds_read_b64_tr_b16 a[142:143], v4 offset:26784\n" \
"s_waitcnt vmcnt(24)\n" \
"ds_read_b64_tr_b16 a[144:145], v4 offset:28672\n" \
"ds_read_b64_tr_b16 a[146:147], v4 offset:28800\n" \
"ds_read_b64_tr_b16 a[148:149], v4 offset:30720\n" \
"ds_read_b64_tr_b16 a[150:151], v4 offset:30848\n" \
"ds_read_b64_tr_b16 a[152:153], v4 offset:28704\n" \
"ds_read_b64_tr_b16 a[154:155], v4 offset:28832\n" \
"ds_read_b64_tr_b16 a[156:157], v4 offset:30752\n" \
"ds_read_b64_tr_b16 a[158:159], v4 offset:30880\n" \
"s_waitcnt vmcnt(20)\n" \
"ds_read_b64_tr_b16 a[160:161], v4 offset:32768\n" \
"ds_read_b64_tr_b16 a[162:163], v4 offset:32896\n" \
"ds_read_b64_tr_b16 a[164:165], v4 offset:34816\n" \
"ds_read_b64_tr_b16 a[166:167], v4 offset:34944\n" \
"ds_read_b64_tr_b16 a[168:169], v4 offset:32800\n" \
"ds_read_b64_tr_b16 a[170:171], v4 offset:32928\n" \
"ds_read_b64_tr_b16 a[172:173], v4 offset:34848\n" \
"ds_read_b64_tr_b16 a[174:175], v4 offset:34976\n" \
"s_waitcnt vmcnt(16)\n" \
"ds_read_b64_tr_b16 a[176:177], v4 offset:36864\n" \
"ds_read_b64_tr_b16 a[178:179], v4 offset:36992\n" \
"ds_read_b64_tr_b16 a[180:181], v4 offset:38912\n" \
"ds_read_b64_tr_b16 a[182:183], v4 offset:39040\n" \
"ds_read_b64_tr_b16 a[184:185], v4 offset:36896\n" \
"ds_read_b64_tr_b16 a[186:187], v4 offset:37024\n" \
"ds_read_b64_tr_b16 a[188:189], v4 offset:38944\n" \
"ds_read_b64_tr_b16 a[190:191], v4 offset:39072\n" \
"s_waitcnt vmcnt(12)\n" \
"ds_read_b64_tr_b16 a[192:193], v4 offset:0\n" \
"ds_read_b64_tr_b16 a[194:195], v4 offset:128\n" \
"ds_read_b64_tr_b16 a[196:197], v4 offset:2048\n" \
"ds_read_b64_tr_b16 a[198:199], v4 offset:2176\n" \
"ds_read_b64_tr_b16 a[200:201], v4 offset:32\n" \
"ds_read_b64_tr_b16 a[202:203], v4 offset:160\n" \
"ds_read_b64_tr_b16 a[204:205], v4 offset:2080\n" \
"ds_read_b64_tr_b16 a[206:207], v4 offset:2208\n" \
"s_waitcnt vmcnt(8)\n" \
"ds_read_b64_tr_b16 a[208:209], v4 offset:4096\n" \
"ds_read_b64_tr_b16 a[210:211], v4 offset:4224\n" \
"ds_read_b64_tr_b16 a[212:213], v4 offset:6144\n" \
"ds_read_b64_tr_b16 a[214:215], v4 offset:6272\n" \
"ds_read_b64_tr_b16 a[216:217], v4 offset:4128\n" \
"ds_read_b64_tr_b16 a[218:219], v4 offset:4256\n" \
"ds_read_b64_tr_b16 a[220:221], v4 offset:6176\n" \
"ds_read_b64_tr_b16 a[222:223], v4 offset:6304\n" \
"s_waitcnt vmcnt(4)\n" \
"ds_read_b64_tr_b16 a[224:225], v4 offset:8192\n" \
"ds_read_b64_tr_b16 a[226:227], v4 offset:8320\n" \
"ds_read_b64_tr_b16 a[228:229], v4 offset:10240\n" \
"ds_read_b64_tr_b16 a[230:231], v4 offset:10368\n" \
"ds_read_b64_tr_b16 a[232:233], v4 offset:8224\n" \
"ds_read_b64_tr_b16 a[234:235], v4 offset:8352\n" \
"ds_read_b64_tr_b16 a[236:237], v4 offset:10272\n" \
"ds_read_b64_tr_b16 a[238:239], v4 offset:10400\n" \
"s_waitcnt vmcnt(0)\n" \
"ds_read_b64_tr_b16 a[240:241], v4 offset:12288\n" \
"ds_read_b64_tr_b16 a[242:243], v4 offset:12416\n" \
"ds_read_b64_tr_b16 a[244:245], v4 offset:14336\n" \
"ds_read_b64_tr_b16 a[246:247], v4 offset:14464\n" \
"ds_read_b64_tr_b16 a[248:249], v4 offset:12320\n" \
"ds_read_b64_tr_b16 a[250:251], v4 offset:12448\n" \
"ds_read_b64_tr_b16 a[252:253], v4 offset:14368\n" \
"ds_read_b64_tr_b16 a[254:255], v4 offset:14496\n" \
"s_waitcnt vmcnt(0) lgkmcnt(0)\n" \
"s_barrier\n" \
"s_mov_b32 s49, 0\n" \
"s_mov_b32 s52, 0\n" \
"s_mov_b32 s88, 0\n" \
"s_mov_b32 s76, 0\n" \
"s_mov_b32 s22, 0xfffffc00\n" \
"s_mov_b32 s23, -1\n" \
"s_mul_i32 s32, s17, 1\n" \
"s_mov_b32 s33, s32\n" \
"s_mov_b32 s34, s17\n" \
"v_max_u32 v20, 1, v20\n" \
"v_max_u32 v21, 1, v21\n" \
"v_max_u32 v22, 1, v22\n" \
"v_max_u32 v23, 1, v23\n" \
"buffer_store_dwordx4 v[20:23], v8, s[36:39], 0 offen sc1\n" \
"s_mov_b32 s72, 0\n" \
"s_mov_b32 s73, -1\n" \
"v_lshlrev_b32 v31, 1, v0\n" \
"v_add_u32 v31, 16384, v31\n" \
"s_lshl_b32 s79, s48, 10\n" \
"s_add_u32 s79, s79, 0\n" \
"v_xor_b32 v30, 0x1000, v6\n" \
"s_memrealtime s[56:57]\n" \
"s_waitcnt lgkmcnt(0)\n" \
"s_mov_b32 s50, s56\n" \
"s_waitcnt vmcnt(0) lgkmcnt(0)\n" \
"s_add_u32 s54, s49, 0\n" \
"s_and_b32 s54, s54, 3\n" \
"s_mul_i32 s58, s54, 131072\n" \
"s_add_u32 s54, s49, 1\n" \
"s_and_b32 s54, s54, 3\n" \
"s_mul_i32 s59, s54, 131072\n" \
"s_add_u32 s54, s49, 2\n" \
"s_and_b32 s54, s54, 3\n" \
"s_mul_i32 s60, s54, 131072\n" \
"s_add_u32 s54, s49, 3\n" \
"s_and_b32 s54, s54, 3\n" \
"s_mul_i32 s61, s54, 131072\n" \
"s_mov_b32 s77, 0\n" \
"s_add_u32 s78, s77, s63\n" \
"s_xor_b32 s89, s78, 0x4800\n" \
"s_xor_b32 s90, s78, 0x800\n" \
"s_xor_b32 s91, s78, 0x4000\n" \
"s_mov_b32 m0, s79\n" \
"s_add_u32 s51, s50, s46\n" \
"v_mov_b64 v[80:81], 0\n" \
"v_mov_b64 v[82:83], 0\n" \
"v_mov_b32 v13, -1\n" \
"s_mov_b32 s23, -1\n" \
"s_cmp_eq_u32 s48, 0\n" \
"s_cselect_b32 s80, -1, 0\n" \
"s_mov_b32 s81, 0\n" \
"s_nop 7\n" \
"s_nop 7\n" \
".p2align 6\n" \
".Lv_loop_%=:\n" \
"s_getpc_b64 s[18:19]\n" \
".Lv_wp_%=:\n" \
"s_add_u32 s18, s18, .Lv_ws_%=-.Lv_wp_%=\n" \
"s_addc_u32 s19, s19, 0\n" \
"s_sub_u32 s21, s51, 5\n" \
"s_nop 0\n" \
".Lv_w_%=:\n" \
"s_memrealtime s[56:57]\n" \
"s_waitcnt lgkmcnt(0)\n" \
"s_sub_u32 s54, s56, s21\n" \
"s_cmp_gt_u32 s54, s22\n" \
"s_cbranch_scc1 .Lv_w_%=\n" \
"s_min_u32 s21, s54, 3\n" \
"s_lshl2_add_u32 s18, s21, s18\n" \
"s_setpc_b64 s[18:19]\n" \
"s_nop 0\n" \
".Lv_ws_%=:\n" \
"s_nop 4\n" \
"s_nop 5\n" \
"s_nop 4\n" \
".Lv_w4_%=:\n" \
"s_barrier\n" \
".Lv_go_%=:\n" \
"buffer_load_dwordx4 v5, s[36:39], s78 offen sc1 lds\n" \
"s_nop 7\n" \
"s_add_u32 m0, s79, 4096\n" \
"s_nop 0\n" \
"buffer_load_dwordx4 v5, s[36:39], s89 offen sc1 lds\n" \
"s_nop 7\n" \
"s_add_u32 m0, s79, 8192\n" \
"s_nop 0\n" \
"buffer_load_dwordx4 v5, s[36:39], s90 offen sc1 lds\n" \
"s_nop 7\n" \
"s_add_u32 m0, s79, 12288\n" \
"s_nop 0\n" \
"buffer_load_dwordx4 v5, s[36:39], s91 offen sc1 lds\n" \
".Lv_x_%=:\n" \
"s_waitcnt vmcnt(2)\n" \
"s_barrier\n" \
"ds_read_b128 v[32:35], v0 offset:0\n" \
"ds_read_b128 v[36:39], v0 offset:1024\n" \
"ds_read_b128 v[40:43], v0 offset:2048\n" \
"ds_read_b128 v[44:47], v0 offset:3072\n" \
"ds_read_b128 v[48:51], v0 offset:4096\n" \
"s_waitcnt lgkmcnt(4)\n" \
"v_smfmac_f32_16x16x64_bf16 v[80:83], v[32:35], v[128:135], v12\n" \
"ds_read_b128 v[52:55], v0 offset:5120\n" \
"s_waitcnt lgkmcnt(4)\n" \
"v_min3_u32 v13, v13, v32, v34\n" \
"v_smfmac_f32_16x16x64_bf16 v[80:83], v[36:39], v[136:143], v12\n" \
"ds_read_b128 v[56:59], v0 offset:6144\n" \
"s_waitcnt lgkmcnt(4)\n" \
"v_min3_u32 v13, v13, v36, v38\n" \
"v_smfmac_f32_16x16x64_bf16 v[80:83], v[40:43], v[144:151], v12\n" \
"ds_read_b128 v[60:63], v0 offset:7168\n" \
"s_waitcnt lgkmcnt(4)\n" \
"v_min3_u32 v13, v13, v40, v42\n" \
"v_smfmac_f32_16x16x64_bf16 v[80:83], v[44:47], v[152:159], v12\n" \
"s_waitcnt vmcnt(0)\n" \
"s_barrier\n" \
"ds_read_b128 v[32:35], v0 offset:8192\n" \
"s_waitcnt lgkmcnt(4)\n" \
"v_min3_u32 v13, v13, v44, v46\n" \
"v_smfmac_f32_16x16x64_bf16 v[80:83], v[48:51], v[160:167], v12\n" \
"ds_read_b128 v[36:39], v0 offset:9216\n" \
"s_waitcnt lgkmcnt(4)\n" \
"v_min3_u32 v13, v13, v48, v50\n" \
"v_cmp_eq_u32_e64 vcc, 0, v13\n" \
"v_smfmac_f32_16x16x64_bf16 v[80:83], v[52:55], v[168:175], v12\n" \
"ds_read_b128 v[40:43], v0 offset:10240\n" \
"s_waitcnt lgkmcnt(4)\n" \
"v_min3_u32 v13, v13, v52, v54\n" \
"v_smfmac_f32_16x16x64_bf16 v[80:83], v[56:59], v[176:183], v12\n" \
"ds_read_b128 v[44:47], v0 offset:11264\n" \
"s_waitcnt lgkmcnt(4)\n" \
"v_min3_u32 v13, v13, v56, v58\n" \
"v_smfmac_f32_16x16x64_bf16 v[80:83], v[60:63], v[184:191], v12\n" \
"ds_read_b128 v[48:51], v0 offset:12288\n" \
"s_waitcnt lgkmcnt(4)\n" \
"s_nop 0\n" \
"s_cbranch_vccnz .Lv_mretry_%=\n" \
"v_min3_u32 v13, v13, v60, v62\n" \
"v_smfmac_f32_16x16x64_bf16 v[80:83], v[32:35], v[192:199], v12\n" \
"ds_read_b128 v[52:55], v0 offset:13312\n" \
"s_waitcnt lgkmcnt(4)\n" \
"v_min3_u32 v13, v13, v32, v34\n" \
"v_smfmac_f32_16x16x64_bf16 v[80:83], v[36:39], v[200:207], v12\n" \
"ds_read_b128 v[56:59], v0 offset:14336\n" \
"s_waitcnt lgkmcnt(4)\n" \
"v_min3_u32 v13, v13, v36, v38\n" \
"v_smfmac_f32_16x16x64_bf16 v[80:83], v[40:43], v[208:215], v12\n" \
"ds_read_b128 v[60:63], v0 offset:15360\n" \
"s_waitcnt lgkmcnt(4)\n" \
"v_min3_u32 v13, v13, v40, v42\n" \
"v_smfmac_f32_16x16x64_bf16 v[80:83], v[44:47], v[216:223], v12\n" \
"s_waitcnt lgkmcnt(3)\n" \
"v_min3_u32 v13, v13, v44, v46\n" \
"v_smfmac_f32_16x16x64_bf16 v[80:83], v[48:51], v[224:231], v12\n" \
"s_waitcnt lgkmcnt(2)\n" \
"v_min3_u32 v13, v13, v48, v50\n" \
"v_smfmac_f32_16x16x64_bf16 v[80:83], v[52:55], v[232:239], v12\n" \
"s_waitcnt lgkmcnt(1)\n" \
"v_min3_u32 v13, v13, v52, v54\n" \
"v_smfmac_f32_16x16x64_bf16 v[80:83], v[56:59], v[240:247], v12\n" \
"s_waitcnt lgkmcnt(0)\n" \
"v_min3_u32 v13, v13, v56, v58\n" \
"v_smfmac_f32_16x16x64_bf16 v[80:83], v[60:63], v[248:255], v12\n" \
"v_min3_u32 v13, v13, v60, v62\n" \
"v_cmp_eq_u32 vcc, 0, v13\n" \
"s_nop 5\n" \
"v_pk_add_f32 v[80:81], v[80:81], v[82:83]\n" \
"s_cbranch_vccnz .Lv_mretry_%=\n" \
"v_cvt_pk_bf16_f32 v84, v80, v81\n" \
"v_max_u32 v84, 1, v84\n" \
"buffer_store_dword v84, v7, s[36:39], s58 offen sc1\n" \
"s_mov_b32 s75, s77\n" \
"s_lshr_b32 s77, s59, 1\n" \
"buffer_load_dwordx2 v[64:65], v9, s[36:39], s75 offen sc1\n" \
"s_sub_u32 s53, s49, 1\n" \
"s_add_u32 s25, s24, 3\n" \
"s_cmp_eq_u32 s26, s24\n" \
"s_subb_u32 s25, s25, 0\n" \
"s_cmp_eq_u32 s28, s24\n" \
"s_subb_u32 s25, s25, 0\n" \
"s_cmp_eq_u32 s30, s24\n" \
"s_subb_u32 s25, s25, 0\n" \
"s_max_u32 s16, s25, 1\n" \
"s_sub_u32 s24, s25, s55\n" \
"s_min_u32 s24, s24, 255\n" \
"s_cmp_eq_u32 s23, s53\n" \
"s_cselect_b32 s24, s34, s24\n" \
"s_min_u32 s34, s24, s32\n" \
"s_max_u32 s25, s24, s32\n" \
"s_min_u32 s25, s25, s33\n" \
"s_max_u32 s34, s34, s25\n" \
"s_mov_b32 s33, s32\n" \
"s_mov_b32 s32, s24\n" \
"s_cmp_eq_u32 s49, 63\n" \
"s_cbranch_scc1 .Lv_end_%=\n" \
"v_mov_b64 v[80:81], 0\n" \
"v_mov_b64 v[82:83], 0\n" \
"v_mov_b64 v[84:85], 0\n" \
"v_mov_b64 v[86:87], 0\n" \
"v_mov_b64 v[88:89], 0\n" \
"v_mov_b64 v[90:91], 0\n" \
"v_mov_b64 v[92:93], 0\n" \
"v_mov_b64 v[94:95], 0\n" \
"buffer_store_dword v16, v7, s[36:39], s60 offen sc1\n" \
"s_lshr_b32 s54, s61, 1\n" \
"buffer_store_dwordx4 v[16:19], v8, s[36:39], s54 offen sc1\n" \
"s_sub_u32 s54, s34, s74\n" \
"v_mov_b32_e64 v13, s54\n" \
"buffer_store_dword v13, v28, s[36:39], 0 offen sc1\n" \
"v_mov_b32_e64 v13, -1\n" \
"s_mov_b32 s54, 0\n" \
"s_cmp_lg_u32 s48, 0\n" \
"s_cbranch_scc1 .Lv_wo4_%=\n" \
"s_waitcnt vmcnt(3)\n" \
"v_cmp_eq_u32_e64 vcc, 0, v64\n" \
"s_nop 0\n" \
"s_and_b64 vcc, vcc, s[80:81]\n" \
"s_cbranch_vccnz .Lv_h_cold_%=\n" \
".Lv_h_ok_%=:\n" \
"v_subrev_u32 v64, s51, v64\n" \
"v_max_i32_dpp v65, v65, v65 row_ror:8 row_mask:0xf bank_mask:0xf\n" \
"s_nop 0\n" \
"v_max_i32_dpp v64, v64, v64 row_ror:8 row_mask:0xf bank_mask:0xf\n" \
"v_max_i32_dpp v65, v65, v65 row_ror:4 row_mask:0xf bank_mask:0xf\n" \
"s_nop 0\n" \
"v_max_i32_dpp v64, v64, v64 row_ror:4 row_mask:0xf bank_mask:0xf\n" \
"v_max_i32_dpp v65, v65, v65 row_ror:2 row_mask:0xf bank_mask:0xf\n" \
"s_nop 0\n" \
"v_max_i32_dpp v64, v64, v64 row_ror:2 row_mask:0xf bank_mask:0xf\n" \
"v_max_i32_dpp v65, v65, v65 row_ror:1 row_mask:0xf bank_mask:0xf\n" \
"s_nop 0\n" \
"v_max_i32_dpp v64, v64, v64 row_ror:1 row_mask:0xf bank_mask:0xf\n" \
"v_readlane_b32 s53, v65, 0\n" \
"v_readlane_b32 s50, v65, 16\n" \
"v_readlane_b32 s54, v64, 0\n" \
"v_readlane_b32 s55, v64, 16\n" \
"s_max_i32 s50, s50, s53\n" \
"s_max_i32 s55, s55, s54\n" \
"s_add_u32 s55, s55, s51\n" \
"s_add_u32 s55, s55, s44\n" \
"s_add_u32 s50, s50, s55\n" \
"s_add_u32 s50, s50, s45\n" \
"s_getpc_b64 s[18:19]\n" \
".Lv_wop_%=:\n" \
"s_add_u32 s18, s18, .Lv_wos_%=-.Lv_wop_%=\n" \
"s_addc_u32 s19, s19, 0\n" \
"s_sub_u32 s21, s55, 5\n" \
"s_nop 0\n" \
"s_nop 0\n" \
"s_nop 0\n" \
"s_nop 0\n" \
"s_nop 0\n" \
".Lv_wo_%=:\n" \
"s_memrealtime s[56:57]\n" \
"s_waitcnt lgkmcnt(0)\n" \
"s_sub_u32 s54, s56, s21\n" \
"s_cmp_gt_u32 s54, s22\n" \
"s_cbranch_scc1 .Lv_wo_%=\n" \
"s_min_u32 s21, s54, 3\n" \
"s_lshl2_add_u32 s18, s21, s18\n" \
"s_setpc_b64 s[18:19]\n" \
"s_nop 0\n" \
".Lv_wos_%=:\n" \
"s_nop 4\n" \
"s_nop 5\n" \
"s_nop 4\n" \
".Lv_wo4_%=:\n" \
"s_barrier\n" \
".Lv_o_%=:\n" \
"buffer_load_dwordx4 v[32:35], v6, s[36:39], s58 offen offset:0 sc1\n" \
"s_cmp_lt_u32 s54, 8\n" \
"s_cselect_b32 s23, s23, s49\n" \
"s_nop 8\n" \
"buffer_load_dwordx4 v[36:39], v6, s[36:39], s58 offen offset:1024 sc1\n" \
"s_nop 10\n" \
"buffer_load_dwordx4 v[40:43], v6, s[36:39], s58 offen offset:2048 sc1\n" \
"s_nop 10\n" \
"buffer_load_dwordx4 v[44:47], v6, s[36:39], s58 offen offset:3072 sc1\n" \
"s_nop 10\n" \
"buffer_load_dwordx4 v[48:51], v30, s[36:39], s58 offen offset:0 sc1\n" \
"s_nop 10\n" \
"buffer_load_dwordx4 v[52:55], v30, s[36:39], s58 offen offset:1024 sc1\n" \
"s_nop 10\n" \
"buffer_load_dwordx4 v[56:59], v30, s[36:39], s58 offen offset:2048 sc1\n" \
"s_nop 10\n" \
"buffer_load_dwordx4 v[60:63], v30, s[36:39], s58 offen offset:3072 sc1\n" \
".Lv_y_%=:\n" \
"s_waitcnt vmcnt(7)\n" \
"v_smfmac_f32_16x16x64_bf16 v[80:83], v[32:35], a[0:7], v12\n" \
"v_min3_u32 v13, v13, v32, v34\n" \
"v_smfmac_f32_16x16x64_bf16 v[84:87], v[32:35], a[8:15], v12\n" \
"v_smfmac_f32_16x16x64_bf16 v[88:91], v[32:35], a[16:23], v12\n" \
"v_smfmac_f32_16x16x64_bf16 v[92:95], v[32:35], a[24:31], v12\n" \
"s_waitcnt vmcnt(6)\n" \
"v_smfmac_f32_16x16x64_bf16 v[80:83], v[36:39], a[32:39], v12\n" \
"v_min3_u32 v13, v13, v36, v38\n" \
"v_smfmac_f32_16x16x64_bf16 v[84:87], v[36:39], a[40:47], v12\n" \
"v_cmp_eq_u32 vcc, 0, v13\n" \
"v_smfmac_f32_16x16x64_bf16 v[88:91], v[36:39], a[48:55], v12\n" \
"v_smfmac_f32_16x16x64_bf16 v[92:95], v[36:39], a[56:63], v12\n" \
"s_cbranch_vccnz .Lv_retry_%=\n" \
"s_waitcnt vmcnt(5)\n" \
"v_smfmac_f32_16x16x64_bf16 v[80:83], v[40:43], a[64:71], v12\n" \
"v_min3_u32 v13, v13, v40, v42\n" \
"v_smfmac_f32_16x16x64_bf16 v[84:87], v[40:43], a[72:79], v12\n" \
"v_smfmac_f32_16x16x64_bf16 v[88:91], v[40:43], a[80:87], v12\n" \
"v_smfmac_f32_16x16x64_bf16 v[92:95], v[40:43], a[88:95], v12\n" \
"s_waitcnt vmcnt(4)\n" \
"v_smfmac_f32_16x16x64_bf16 v[80:83], v[44:47], a[96:103], v12\n" \
"v_min3_u32 v13, v13, v44, v46\n" \
"v_smfmac_f32_16x16x64_bf16 v[84:87], v[44:47], a[104:111], v12\n" \
"v_smfmac_f32_16x16x64_bf16 v[88:91], v[44:47], a[112:119], v12\n" \
"v_smfmac_f32_16x16x64_bf16 v[92:95], v[44:47], a[120:127], v12\n" \
"s_waitcnt vmcnt(3)\n" \
"v_smfmac_f32_16x16x64_bf16 v[80:83], v[48:51], a[128:135], v12\n" \
"v_min3_u32 v13, v13, v48, v50\n" \
"v_smfmac_f32_16x16x64_bf16 v[84:87], v[48:51], a[136:143], v12\n" \
"v_smfmac_f32_16x16x64_bf16 v[88:91], v[48:51], a[144:151], v12\n" \
"v_smfmac_f32_16x16x64_bf16 v[92:95], v[48:51], a[152:159], v12\n" \
"s_waitcnt vmcnt(2)\n" \
"v_smfmac_f32_16x16x64_bf16 v[80:83], v[52:55], a[160:167], v12\n" \
"v_min3_u32 v13, v13, v52, v54\n" \
"v_smfmac_f32_16x16x64_bf16 v[84:87], v[52:55], a[168:175], v12\n" \
"v_smfmac_f32_16x16x64_bf16 v[88:91], v[52:55], a[176:183], v12\n" \
"v_smfmac_f32_16x16x64_bf16 v[92:95], v[52:55], a[184:191], v12\n" \
"s_waitcnt vmcnt(1)\n" \
"v_smfmac_f32_16x16x64_bf16 v[80:83], v[56:59], a[192:199], v12\n" \
"v_min3_u32 v13, v13, v56, v58\n" \
"v_smfmac_f32_16x16x64_bf16 v[84:87], v[56:59], a[200:207], v12\n" \
"v_smfmac_f32_16x16x64_bf16 v[88:91], v[56:59], a[208:215], v12\n" \
"v_smfmac_f32_16x16x64_bf16 v[92:95], v[56:59], a[216:223], v12\n" \
"s_waitcnt vmcnt(0)\n" \
"v_smfmac_f32_16x16x64_bf16 v[80:83], v[60:63], a[224:231], v12\n" \
"v_min3_u32 v13, v13, v60, v62\n" \
"v_smfmac_f32_16x16x64_bf16 v[84:87], v[60:63], a[232:239], v12\n" \
"v_cmp_eq_u32 vcc, 0, v13\n" \
"v_smfmac_f32_16x16x64_bf16 v[88:91], v[60:63], a[240:247], v12\n" \
"v_smfmac_f32_16x16x64_bf16 v[92:95], v[60:63], a[248:255], v12\n" \
"s_cbranch_vccnz .Lv_retry_%=\n" \
".Lv_y2_%=:\n" \
"v_pk_add_f32 v[80:81], v[80:81], v[82:83]\n" \
"v_pk_add_f32 v[84:85], v[84:85], v[86:87]\n" \
"s_nop 0\n" \
"v_pk_add_f32 v[88:89], v[88:89], v[90:91]\n" \
"s_nop 2\n" \
"v_pk_add_f32 v[92:93], v[92:93], v[94:95]\n" \
"v_cndmask_b32_e64 v80, v80, v88, s[72:73]\n" \
"v_cndmask_b32_e64 v81, v81, v89, s[72:73]\n" \
"v_cndmask_b32_e64 v84, v84, v92, s[72:73]\n" \
"v_cndmask_b32_e64 v85, v85, v93, s[72:73]\n" \
"s_nop 0\n" \
"v_permlane32_swap_b32 v80, v84\n" \
"v_permlane32_swap_b32 v81, v85\n" \
"v_pk_add_f32 v[80:81], v[80:81], v[84:85]\n" \
"ds_write2_b32 v15, v80, v81 offset1:32\n" \
"s_cmp_lg_u32 s48, 0\n" \
"s_waitcnt lgkmcnt(0)\n" \
"s_barrier\n" \
"s_nop 0\n" \
"s_nop 0\n" \
"s_nop 0\n" \
"s_cbranch_scc1 .Lv_skip_%=\n" \
"ds_read_b128 v[64:67], v31\n" \
"ds_read_b128 v[68:71], v31 offset:16\n" \
"s_waitcnt lgkmcnt(1)\n" \
"v_pk_add_f32 v[64:65], v[64:65], v[66:67]\n" \
"s_waitcnt lgkmcnt(0)\n" \
"v_pk_add_f32 v[68:69], v[68:69], v[70:71]\n" \
"v_pk_add_f32 v[64:65], v[64:65], v[68:69]\n" \
"v_cvt_pk_bf16_f32 v72, v64, v65\n" \
"v_max_u32 v72, 1, v72\n" \
"s_nop 1\n" \
"v_mov_b32_dpp v73, v72 quad_perm:[1,1,1,1] row_mask:0xf bank_mask:0xf\n" \
"v_mov_b32_dpp v74, v72 quad_perm:[2,2,2,2] row_mask:0xf bank_mask:0xf\n" \
"v_mov_b32_dpp v75, v72 quad_perm:[3,3,3,3] row_mask:0xf bank_mask:0xf\n" \
".Lv_skip_%=:\n" \
"buffer_store_dwordx4 v[72:75], v8, s[36:39], s77 offen sc1\n" \
"s_memrealtime s[24:25]\n" \
"s_mov_b32 s54, s58\n" \
"s_mov_b32 s58, s59\n" \
"s_mov_b32 s59, s60\n" \
"s_mov_b32 s60, s61\n" \
"s_mov_b32 s61, s54\n" \
"s_memrealtime s[26:27]\n" \
"s_add_u32 s78, s77, s63\n" \
"s_xor_b32 s89, s78, 0x4800\n" \
"s_xor_b32 s90, s78, 0x800\n" \
"s_xor_b32 s91, s78, 0x4000\n" \
"s_mov_b32 m0, s79\n" \
"s_memrealtime s[28:29]\n" \
"s_add_u32 s51, s50, s46\n" \
"v_mov_b64 v[80:81], 0\n" \
"v_mov_b64 v[82:83], 0\n" \
"v_mov_b32 v13, -1\n" \
"s_memrealtime s[30:31]\n" \
"s_add_u32 s49, s49, 1\n" \
"s_waitcnt lgkmcnt(0)\n" \
"s_cmp_lg_u32 s48, 0\n" \
"s_nop 0\n" \
"s_nop 0\n" \
"s_nop 0\n" \
"s_cbranch_scc1 .Lv_w4_%=\n" \
"s_branch .Lv_loop_%=\n" \
".Lv_h_cold_%=:\n" \
"s_nop 7\n" \
"s_nop 7\n" \
"s_waitcnt vmcnt(0) lgkmcnt(0)\n" \
"s_mov_b32 s53, 0\n" \
".Lv_h_again_%=:\n" \
"s_cmp_lg_u32 s52, 0\n" \
"s_cbranch_scc1 .Lv_h_ok_%=\n" \
"s_sleep 1\n" \
"buffer_load_dwordx2 v[64:65], v9, s[36:39], s75 offen sc1\n" \
"s_waitcnt vmcnt(0)\n" \
"v_cmp_eq_u32 vcc, 0, v64\n" \
"s_nop 0\n" \
"s_and_b64 vcc, vcc, s[80:81]\n" \
"s_nop 4\n" \
"s_cbranch_vccz .Lv_h_ok_%=\n" \
"s_add_u32 s53, s53, 1\n" \
"s_cmp_lt_u32 s53, 32768\n" \
"s_cbranch_scc1 .Lv_h_again_%=\n" \
"s_mov_b32 s52, 1\n" \
"v_mov_b32 v10, 256\n" \
"v_mov_b32 v11, 1\n" \
"buffer_store_dword v11, v10, s[36:39], 0 offen sc1\n" \
"s_waitcnt vmcnt(0) lgkmcnt(0)\n" \
"s_branch .Lv_h_ok_%=\n" \
".Lv_retry_%=:\n" \
"s_nop 7\n" \
"s_nop 7\n" \
"s_cmp_lg_u32 s52, 0\n" \
"s_cbranch_scc1 .Lv_dead_%=\n" \
"v_mov_b64 v[80:81], 0\n" \
"v_mov_b64 v[82:83], 0\n" \
"v_mov_b64 v[84:85], 0\n" \
"v_mov_b64 v[86:87], 0\n" \
"v_mov_b64 v[88:89], 0\n" \
"v_mov_b64 v[90:91], 0\n" \
"v_mov_b64 v[92:93], 0\n" \
"v_mov_b64 v[94:95], 0\n" \
"v_mov_b32 v13, -1\n" \
"s_mov_b32 s54, 100\n" \
"s_add_u32 s76, s76, 1\n" \
"s_cmp_lt_u32 s76, 32768\n" \
"s_cbranch_scc1 .Lv_o_%=\n" \
"s_mov_b32 s52, 1\n" \
"v_mov_b32 v13, 256\n" \
"v_mov_b32 v14, 1\n" \
"buffer_store_dword v14, v13, s[36:39], 0 offen sc1\n" \
".Lv_dead_%=:\n" \
"s_waitcnt vmcnt(0) lgkmcnt(0)\n" \
"s_branch .Lv_y2_%=\n" \
".Lv_mretry_%=:\n" \
"s_nop 7\n" \
"s_nop 7\n" \
"v_mov_b64 v[80:81], 0\n" \
"v_mov_b64 v[82:83], 0\n" \
"v_mov_b32 v13, -1\n" \
"s_mov_b32 m0, s79\n" \
"s_waitcnt vmcnt(0) lgkmcnt(0)\n" \
"s_barrier\n" \
"s_add_u32 s88, s88, 1\n" \
"s_cmp_lt_u32 s88, 32768\n" \
"s_cbranch_scc1 .Lv_r1_%=\n" \
"s_mov_b32 s52, 1\n" \
"v_mov_b32 v13, 256\n" \
"v_mov_b32 v14, 1\n" \
"buffer_store_dword v14, v13, s[36:39], 0 offen sc1\n" \
"s_waitcnt vmcnt(0) lgkmcnt(0)\n" \
"s_endpgm\n" \
".Lv_r1_%=:\n" \
"s_cmp_lg_u32 s49, 0\n" \
"s_cbranch_scc1 .Lv_r2_%=\n" \
"s_sleep 8\n" \
".Lv_r2_%=:\n" \
"s_branch .Lv_go_%=\n" \
".Lv_end_%=:\n" \
"s_nop 7\n" \
"s_nop 7\n" \
"s_waitcnt vmcnt(0) lgkmcnt(0)\n" \
"s_waitcnt vmcnt(0) lgkmcnt(0)\n" \
"s_endpgm\n"
#define ASM_S64_WO \
"s_nop 7\n" \
"s_nop 7\n" \
"s_lshl_b32 s52, s48, 15\n" \
"s_lshl_b32 s53, s48, 13\n" \
"s_add_u32 s53, s53, 98304\n" \
"s_sub_u32 s54, s53, s52\n" \
"v_add_u32 v9, s54, v1\n" \
"s_mov_b32 s54, s85\n" \
"s_mul_i32 s86, s54, 262144\n" \
"s_add_u32 s54, s54, 1\n" \
"s_cmp_eq_u32 s54, 8\n" \
"s_cselect_b32 s54, 0, s54\n" \
"s_mul_i32 s87, s54, 262144\n" \
"s_add_u32 s54, s54, 1\n" \
"s_cmp_eq_u32 s54, 8\n" \
"s_cselect_b32 s54, 0, s54\n" \
"s_mul_i32 s88, s54, 262144\n" \
"s_add_u32 s54, s54, 1\n" \
"s_cmp_eq_u32 s54, 8\n" \
"s_cselect_b32 s54, 0, s54\n" \
"s_mul_i32 s89, s54, 262144\n" \
"s_add_u32 s54, s54, 1\n" \
"s_cmp_eq_u32 s54, 8\n" \
"s_cselect_b32 s54, 0, s54\n" \
"s_mul_i32 s90, s54, 262144\n" \
"s_add_u32 s54, s54, 1\n" \
"s_cmp_eq_u32 s54, 8\n" \
"s_cselect_b32 s54, 0, s54\n" \
"s_mul_i32 s91, s54, 262144\n" \
"s_add_u32 s54, s54, 1\n" \
"s_cmp_eq_u32 s54, 8\n" \
"s_cselect_b32 s54, 0, s54\n" \
"s_mul_i32 s92, s54, 262144\n" \
"s_add_u32 s54, s54, 1\n" \
"s_cmp_eq_u32 s54, 8\n" \
"s_cselect_b32 s54, 0, s54\n" \
"s_mul_i32 s93, s54, 262144\n" \
"s_add_u32 s54, s54, 1\n" \
"s_cmp_eq_u32 s54, 8\n" \
"s_cselect_b32 s54, 0, s54\n" \
"s_add_u32 m0, s52, 0\n" \
"s_add_u32 s54, s86, 0\n" \
"buffer_load_dwordx4 v0, s[40:43], s54 offen sc1 lds\n" \
"s_add_u32 m0, s52, 1024\n" \
"s_add_u32 s54, s86, 16384\n" \
"buffer_load_dwordx4 v0, s[40:43], s54 offen sc1 lds\n" \
"s_add_u32 m0, s52, 2048\n" \
"s_add_u32 s54, s86, 32768\n" \
"buffer_load_dwordx4 v0, s[40:43], s54 offen sc1 lds\n" \
"s_add_u32 m0, s52, 3072\n" \
"s_add_u32 s54, s86, 49152\n" \
"buffer_load_dwordx4 v0, s[40:43], s54 offen sc1 lds\n" \
"s_mov_b32 s54, s99\n" \
".Lswo_z0_%=:\n" \
"s_sleep 1\n" \
"s_sub_u32 s54, s54, 1\n" \
"s_cbranch_scc0 .Lswo_z0_%=\n" \
"s_add_u32 m0, s52, 4096\n" \
"s_add_u32 s54, s86, 65536\n" \
"buffer_load_dwordx4 v0, s[40:43], s54 offen sc1 lds\n" \
"s_add_u32 m0, s52, 5120\n" \
"s_add_u32 s54, s86, 81920\n" \
"buffer_load_dwordx4 v0, s[40:43], s54 offen sc1 lds\n" \
"s_add_u32 m0, s52, 6144\n" \
"s_add_u32 s54, s86, 98304\n" \
"buffer_load_dwordx4 v0, s[40:43], s54 offen sc1 lds\n" \
"s_add_u32 m0, s52, 7168\n" \
"s_add_u32 s54, s86, 114688\n" \
"buffer_load_dwordx4 v0, s[40:43], s54 offen sc1 lds\n" \
"s_mov_b32 s54, s99\n" \
".Lswo_z1_%=:\n" \
"s_sleep 1\n" \
"s_sub_u32 s54, s54, 1\n" \
"s_cbranch_scc0 .Lswo_z1_%=\n" \
"s_add_u32 m0, s52, 8192\n" \
"s_add_u32 s54, s86, 131072\n" \
"buffer_load_dwordx4 v0, s[40:43], s54 offen sc1 lds\n" \
"s_add_u32 m0, s52, 9216\n" \
"s_add_u32 s54, s86, 147456\n" \
"buffer_load_dwordx4 v0, s[40:43], s54 offen sc1 lds\n" \
"s_add_u32 m0, s52, 10240\n" \
"s_add_u32 s54, s86, 163840\n" \
"buffer_load_dwordx4 v0, s[40:43], s54 offen sc1 lds\n" \
"s_add_u32 m0, s52, 11264\n" \
"s_add_u32 s54, s86, 180224\n" \
"buffer_load_dwordx4 v0, s[40:43], s54 offen sc1 lds\n" \
"s_mov_b32 s54, s99\n" \
".Lswo_z2_%=:\n" \
"s_sleep 1\n" \
"s_sub_u32 s54, s54, 1\n" \
"s_cbranch_scc0 .Lswo_z2_%=\n" \
"s_add_u32 m0, s52, 12288\n" \
"s_add_u32 s54, s86, 196608\n" \
"buffer_load_dwordx4 v0, s[40:43], s54 offen sc1 lds\n" \
"s_add_u32 m0, s52, 13312\n" \
"s_add_u32 s54, s86, 212992\n" \
"buffer_load_dwordx4 v0, s[40:43], s54 offen sc1 lds\n" \
"s_add_u32 m0, s52, 14336\n" \
"s_add_u32 s54, s86, 229376\n" \
"buffer_load_dwordx4 v0, s[40:43], s54 offen sc1 lds\n" \
"s_add_u32 m0, s52, 15360\n" \
"s_add_u32 s54, s86, 245760\n" \
"buffer_load_dwordx4 v0, s[40:43], s54 offen sc1 lds\n" \
"s_mov_b32 s54, s99\n" \
".Lswo_z3_%=:\n" \
"s_sleep 1\n" \
"s_sub_u32 s54, s54, 1\n" \
"s_cbranch_scc0 .Lswo_z3_%=\n" \
"s_add_u32 m0, s52, 16384\n" \
"s_add_u32 s54, s87, 0\n" \
"buffer_load_dwordx4 v0, s[40:43], s54 offen sc1 lds\n" \
"s_add_u32 m0, s52, 17408\n" \
"s_add_u32 s54, s87, 16384\n" \
"buffer_load_dwordx4 v0, s[40:43], s54 offen sc1 lds\n" \
"s_add_u32 m0, s52, 18432\n" \
"s_add_u32 s54, s87, 32768\n" \
"buffer_load_dwordx4 v0, s[40:43], s54 offen sc1 lds\n" \
"s_add_u32 m0, s52, 19456\n" \
"s_add_u32 s54, s87, 49152\n" \
"buffer_load_dwordx4 v0, s[40:43], s54 offen sc1 lds\n" \
"s_mov_b32 s54, s99\n" \
".Lswo_z4_%=:\n" \
"s_sleep 1\n" \
"s_sub_u32 s54, s54, 1\n" \
"s_cbranch_scc0 .Lswo_z4_%=\n" \
"s_add_u32 m0, s52, 20480\n" \
"s_add_u32 s54, s87, 65536\n" \
"buffer_load_dwordx4 v0, s[40:43], s54 offen sc1 lds\n" \
"s_add_u32 m0, s52, 21504\n" \
"s_add_u32 s54, s87, 81920\n" \
"buffer_load_dwordx4 v0, s[40:43], s54 offen sc1 lds\n" \
"s_add_u32 m0, s52, 22528\n" \
"s_add_u32 s54, s87, 98304\n" \
"buffer_load_dwordx4 v0, s[40:43], s54 offen sc1 lds\n" \
"s_add_u32 m0, s52, 23552\n" \
"s_add_u32 s54, s87, 114688\n" \
"buffer_load_dwordx4 v0, s[40:43], s54 offen sc1 lds\n" \
"s_mov_b32 s54, s99\n" \
".Lswo_z5_%=:\n" \
"s_sleep 1\n" \
"s_sub_u32 s54, s54, 1\n" \
"s_cbranch_scc0 .Lswo_z5_%=\n" \
"s_add_u32 m0, s52, 24576\n" \
"s_add_u32 s54, s87, 131072\n" \
"buffer_load_dwordx4 v0, s[40:43], s54 offen sc1 lds\n" \
"s_add_u32 m0, s52, 25600\n" \
"s_add_u32 s54, s87, 147456\n" \
"buffer_load_dwordx4 v0, s[40:43], s54 offen sc1 lds\n" \
"s_add_u32 m0, s52, 26624\n" \
"s_add_u32 s54, s87, 163840\n" \
"buffer_load_dwordx4 v0, s[40:43], s54 offen sc1 lds\n" \
"s_add_u32 m0, s52, 27648\n" \
"s_add_u32 s54, s87, 180224\n" \
"buffer_load_dwordx4 v0, s[40:43], s54 offen sc1 lds\n" \
"s_mov_b32 s54, s99\n" \
".Lswo_z6_%=:\n" \
"s_sleep 1\n" \
"s_sub_u32 s54, s54, 1\n" \
"s_cbranch_scc0 .Lswo_z6_%=\n" \
"s_add_u32 m0, s52, 28672\n" \
"s_add_u32 s54, s87, 196608\n" \
"buffer_load_dwordx4 v0, s[40:43], s54 offen sc1 lds\n" \
"s_add_u32 m0, s52, 29696\n" \
"s_add_u32 s54, s87, 212992\n" \
"buffer_load_dwordx4 v0, s[40:43], s54 offen sc1 lds\n" \
"s_add_u32 m0, s52, 30720\n" \
"s_add_u32 s54, s87, 229376\n" \
"buffer_load_dwordx4 v0, s[40:43], s54 offen sc1 lds\n" \
"s_add_u32 m0, s52, 31744\n" \
"s_add_u32 s54, s87, 245760\n" \
"buffer_load_dwordx4 v0, s[40:43], s54 offen sc1 lds\n" \
"s_mov_b32 s54, s99\n" \
".Lswo_z7_%=:\n" \
"s_sleep 1\n" \
"s_sub_u32 s54, s54, 1\n" \
"s_cbranch_scc0 .Lswo_z7_%=\n" \
"s_add_u32 m0, s53, 32768\n" \
"s_add_u32 s54, s88, 0\n" \
"buffer_load_dwordx4 v0, s[40:43], s54 offen sc1 lds\n" \
"s_add_u32 m0, s53, 33792\n" \
"s_add_u32 s54, s88, 16384\n" \
"buffer_load_dwordx4 v0, s[40:43], s54 offen sc1 lds\n" \
"s_add_u32 m0, s53, 34816\n" \
"s_add_u32 s54, s88, 32768\n" \
"buffer_load_dwordx4 v0, s[40:43], s54 offen sc1 lds\n" \
"s_add_u32 m0, s53, 35840\n" \
"s_add_u32 s54, s88, 49152\n" \
"buffer_load_dwordx4 v0, s[40:43], s54 offen sc1 lds\n" \
"s_mov_b32 s54, s99\n" \
".Lswo_z8_%=:\n" \
"s_sleep 1\n" \
"s_sub_u32 s54, s54, 1\n" \
"s_cbranch_scc0 .Lswo_z8_%=\n" \
"s_add_u32 m0, s53, 36864\n" \
"s_add_u32 s54, s88, 65536\n" \
"buffer_load_dwordx4 v0, s[40:43], s54 offen sc1 lds\n" \
"s_add_u32 m0, s53, 37888\n" \
"s_add_u32 s54, s88, 81920\n" \
"buffer_load_dwordx4 v0, s[40:43], s54 offen sc1 lds\n" \
"s_add_u32 m0, s53, 38912\n" \
"s_add_u32 s54, s88, 98304\n" \
"buffer_load_dwordx4 v0, s[40:43], s54 offen sc1 lds\n" \
"s_add_u32 m0, s53, 39936\n" \
"s_add_u32 s54, s88, 114688\n" \
"buffer_load_dwordx4 v0, s[40:43], s54 offen sc1 lds\n" \
"s_mov_b32 s54, s99\n" \
".Lswo_z9_%=:\n" \
"s_sleep 1\n" \
"s_sub_u32 s54, s54, 1\n" \
"s_cbranch_scc0 .Lswo_z9_%=\n" \
"s_waitcnt vmcnt(36)\n" \
"ds_read_b64_tr_b16 a[0:1], v1 offset:0\n" \
"ds_read_b64_tr_b16 a[2:3], v1 offset:2048\n" \
"ds_read_b64_tr_b16 a[128:129], v1 offset:32\n" \
"ds_read_b64_tr_b16 a[130:131], v1 offset:2080\n" \
"ds_read_b64_tr_b16 v[128:129], v1 offset:64\n" \
"ds_read_b64_tr_b16 v[130:131], v1 offset:2112\n" \
"ds_read_b64_tr_b16 v[16:17], v1 offset:96\n" \
"ds_read_b64_tr_b16 v[18:19], v1 offset:2144\n" \
"s_waitcnt lgkmcnt(0)\n" \
"s_add_u32 m0, s52, 0\n" \
"s_add_u32 s54, s88, 131072\n" \
"buffer_load_dwordx4 v0, s[40:43], s54 offen sc1 lds\n" \
"s_add_u32 m0, s52, 1024\n" \
"s_add_u32 s54, s88, 147456\n" \
"buffer_load_dwordx4 v0, s[40:43], s54 offen sc1 lds\n" \
"s_add_u32 m0, s52, 2048\n" \
"s_add_u32 s54, s88, 163840\n" \
"buffer_load_dwordx4 v0, s[40:43], s54 offen sc1 lds\n" \
"s_add_u32 m0, s52, 3072\n" \
"s_add_u32 s54, s88, 180224\n" \
"buffer_load_dwordx4 v0, s[40:43], s54 offen sc1 lds\n" \
"s_mov_b32 s54, s99\n" \
".Lswo_z10_%=:\n" \
"s_sleep 1\n" \
"s_sub_u32 s54, s54, 1\n" \
"s_cbranch_scc0 .Lswo_z10_%=\n" \
"s_waitcnt vmcnt(36)\n" \
"ds_read_b64_tr_b16 a[4:5], v1 offset:4096\n" \
"ds_read_b64_tr_b16 a[6:7], v1 offset:6144\n" \
"ds_read_b64_tr_b16 a[132:133], v1 offset:4128\n" \
"ds_read_b64_tr_b16 a[134:135], v1 offset:6176\n" \
"ds_read_b64_tr_b16 v[132:133], v1 offset:4160\n" \
"ds_read_b64_tr_b16 v[134:135], v1 offset:6208\n" \
"ds_read_b64_tr_b16 v[20:21], v1 offset:4192\n" \
"ds_read_b64_tr_b16 v[22:23], v1 offset:6240\n" \
"s_waitcnt lgkmcnt(0)\n" \
"s_add_u32 m0, s52, 4096\n" \
"s_add_u32 s54, s88, 196608\n" \
"buffer_load_dwordx4 v0, s[40:43], s54 offen sc1 lds\n" \
"s_add_u32 m0, s52, 5120\n" \
"s_add_u32 s54, s88, 212992\n" \
"buffer_load_dwordx4 v0, s[40:43], s54 offen sc1 lds\n" \
"s_add_u32 m0, s52, 6144\n" \
"s_add_u32 s54, s88, 229376\n" \
"buffer_load_dwordx4 v0, s[40:43], s54 offen sc1 lds\n" \
"s_add_u32 m0, s52, 7168\n" \
"s_add_u32 s54, s88, 245760\n" \
"buffer_load_dwordx4 v0, s[40:43], s54 offen sc1 lds\n" \
"s_mov_b32 s54, s99\n" \
".Lswo_z11_%=:\n" \
"s_sleep 1\n" \
"s_sub_u32 s54, s54, 1\n" \
"s_cbranch_scc0 .Lswo_z11_%=\n" \
"s_waitcnt vmcnt(36)\n" \
"ds_read_b64_tr_b16 a[8:9], v1 offset:8192\n" \
"ds_read_b64_tr_b16 a[10:11], v1 offset:10240\n" \
"ds_read_b64_tr_b16 a[136:137], v1 offset:8224\n" \
"ds_read_b64_tr_b16 a[138:139], v1 offset:10272\n" \
"ds_read_b64_tr_b16 v[136:137], v1 offset:8256\n" \
"ds_read_b64_tr_b16 v[138:139], v1 offset:10304\n" \
"ds_read_b64_tr_b16 v[24:25], v1 offset:8288\n" \
"ds_read_b64_tr_b16 v[26:27], v1 offset:10336\n" \
"s_waitcnt lgkmcnt(0)\n" \
"s_add_u32 m0, s52, 8192\n" \
"s_add_u32 s54, s89, 0\n" \
"buffer_load_dwordx4 v0, s[40:43], s54 offen sc1 lds\n" \
"s_add_u32 m0, s52, 9216\n" \
"s_add_u32 s54, s89, 16384\n" \
"buffer_load_dwordx4 v0, s[40:43], s54 offen sc1 lds\n" \
"s_add_u32 m0, s52, 10240\n" \
"s_add_u32 s54, s89, 32768\n" \
"buffer_load_dwordx4 v0, s[40:43], s54 offen sc1 lds\n" \
"s_add_u32 m0, s52, 11264\n" \
"s_add_u32 s54, s89, 49152\n" \
"buffer_load_dwordx4 v0, s[40:43], s54 offen sc1 lds\n" \
"s_mov_b32 s54, s99\n" \
".Lswo_z12_%=:\n" \
"s_sleep 1\n" \
"s_sub_u32 s54, s54, 1\n" \
"s_cbranch_scc0 .Lswo_z12_%=\n" \
"s_waitcnt vmcnt(36)\n" \
"ds_read_b64_tr_b16 a[12:13], v1 offset:12288\n" \
"ds_read_b64_tr_b16 a[14:15], v1 offset:14336\n" \
"ds_read_b64_tr_b16 a[140:141], v1 offset:12320\n" \
"ds_read_b64_tr_b16 a[142:143], v1 offset:14368\n" \
"ds_read_b64_tr_b16 v[140:141], v1 offset:12352\n" \
"ds_read_b64_tr_b16 v[142:143], v1 offset:14400\n" \
"ds_read_b64_tr_b16 v[28:29], v1 offset:12384\n" \
"ds_read_b64_tr_b16 v[30:31], v1 offset:14432\n" \
"s_waitcnt lgkmcnt(0)\n" \
"s_add_u32 m0, s52, 12288\n" \
"s_add_u32 s54, s89, 65536\n" \
"buffer_load_dwordx4 v0, s[40:43], s54 offen sc1 lds\n" \
"s_add_u32 m0, s52, 13312\n" \
"s_add_u32 s54, s89, 81920\n" \
"buffer_load_dwordx4 v0, s[40:43], s54 offen sc1 lds\n" \
"s_add_u32 m0, s52, 14336\n" \
"s_add_u32 s54, s89, 98304\n" \
"buffer_load_dwordx4 v0, s[40:43], s54 offen sc1 lds\n" \
"s_add_u32 m0, s52, 15360\n" \
"s_add_u32 s54, s89, 114688\n" \
"buffer_load_dwordx4 v0, s[40:43], s54 offen sc1 lds\n" \
"s_mov_b32 s54, s99\n" \
".Lswo_z13_%=:\n" \
"s_sleep 1\n" \
"s_sub_u32 s54, s54, 1\n" \
"s_cbranch_scc0 .Lswo_z13_%=\n" \
"s_waitcnt vmcnt(36)\n" \
"ds_read_b64_tr_b16 a[16:17], v1 offset:16384\n" \
"ds_read_b64_tr_b16 a[18:19], v1 offset:18432\n" \
"ds_read_b64_tr_b16 a[144:145], v1 offset:16416\n" \
"ds_read_b64_tr_b16 a[146:147], v1 offset:18464\n" \
"ds_read_b64_tr_b16 v[144:145], v1 offset:16448\n" \
"ds_read_b64_tr_b16 v[146:147], v1 offset:18496\n" \
"ds_read_b64_tr_b16 v[32:33], v1 offset:16480\n" \
"ds_read_b64_tr_b16 v[34:35], v1 offset:18528\n" \
"s_waitcnt lgkmcnt(0)\n" \
"s_add_u32 m0, s52, 16384\n" \
"s_add_u32 s54, s89, 131072\n" \
"buffer_load_dwordx4 v0, s[40:43], s54 offen sc1 lds\n" \
"s_add_u32 m0, s52, 17408\n" \
"s_add_u32 s54, s89, 147456\n" \
"buffer_load_dwordx4 v0, s[40:43], s54 offen sc1 lds\n" \
"s_add_u32 m0, s52, 18432\n" \
"s_add_u32 s54, s89, 163840\n" \
"buffer_load_dwordx4 v0, s[40:43], s54 offen sc1 lds\n" \
"s_add_u32 m0, s52, 19456\n" \
"s_add_u32 s54, s89, 180224\n" \
"buffer_load_dwordx4 v0, s[40:43], s54 offen sc1 lds\n" \
"s_mov_b32 s54, s99\n" \
".Lswo_z14_%=:\n" \
"s_sleep 1\n" \
"s_sub_u32 s54, s54, 1\n" \
"s_cbranch_scc0 .Lswo_z14_%=\n" \
"s_waitcnt vmcnt(36)\n" \
"ds_read_b64_tr_b16 a[20:21], v1 offset:20480\n" \
"ds_read_b64_tr_b16 a[22:23], v1 offset:22528\n" \
"ds_read_b64_tr_b16 a[148:149], v1 offset:20512\n" \
"ds_read_b64_tr_b16 a[150:151], v1 offset:22560\n" \
"ds_read_b64_tr_b16 v[148:149], v1 offset:20544\n" \
"ds_read_b64_tr_b16 v[150:151], v1 offset:22592\n" \
"ds_read_b64_tr_b16 v[36:37], v1 offset:20576\n" \
"ds_read_b64_tr_b16 v[38:39], v1 offset:22624\n" \
"s_waitcnt lgkmcnt(0)\n" \
"s_add_u32 m0, s52, 20480\n" \
"s_add_u32 s54, s89, 196608\n" \
"buffer_load_dwordx4 v0, s[40:43], s54 offen sc1 lds\n" \
"s_add_u32 m0, s52, 21504\n" \
"s_add_u32 s54, s89, 212992\n" \
"buffer_load_dwordx4 v0, s[40:43], s54 offen sc1 lds\n" \
"s_add_u32 m0, s52, 22528\n" \
"s_add_u32 s54, s89, 229376\n" \
"buffer_load_dwordx4 v0, s[40:43], s54 offen sc1 lds\n" \
"s_add_u32 m0, s52, 23552\n" \
"s_add_u32 s54, s89, 245760\n" \
"buffer_load_dwordx4 v0, s[40:43], s54 offen sc1 lds\n" \
"s_mov_b32 s54, s99\n" \
".Lswo_z15_%=:\n" \
"s_sleep 1\n" \
"s_sub_u32 s54, s54, 1\n" \
"s_cbranch_scc0 .Lswo_z15_%=\n" \
"s_waitcnt vmcnt(36)\n" \
"ds_read_b64_tr_b16 a[24:25], v1 offset:24576\n" \
"ds_read_b64_tr_b16 a[26:27], v1 offset:26624\n" \
"ds_read_b64_tr_b16 a[152:153], v1 offset:24608\n" \
"ds_read_b64_tr_b16 a[154:155], v1 offset:26656\n" \
"ds_read_b64_tr_b16 v[152:153], v1 offset:24640\n" \
"ds_read_b64_tr_b16 v[154:155], v1 offset:26688\n" \
"ds_read_b64_tr_b16 v[40:41], v1 offset:24672\n" \
"ds_read_b64_tr_b16 v[42:43], v1 offset:26720\n" \
"s_waitcnt lgkmcnt(0)\n" \
"s_add_u32 m0, s52, 24576\n" \
"s_add_u32 s54, s90, 0\n" \
"buffer_load_dwordx4 v0, s[40:43], s54 offen sc1 lds\n" \
"s_add_u32 m0, s52, 25600\n" \
"s_add_u32 s54, s90, 16384\n" \
"buffer_load_dwordx4 v0, s[40:43], s54 offen sc1 lds\n" \
"s_add_u32 m0, s52, 26624\n" \
"s_add_u32 s54, s90, 32768\n" \
"buffer_load_dwordx4 v0, s[40:43], s54 offen sc1 lds\n" \
"s_add_u32 m0, s52, 27648\n" \
"s_add_u32 s54, s90, 49152\n" \
"buffer_load_dwordx4 v0, s[40:43], s54 offen sc1 lds\n" \
"s_mov_b32 s54, s99\n" \
".Lswo_z16_%=:\n" \
"s_sleep 1\n" \
"s_sub_u32 s54, s54, 1\n" \
"s_cbranch_scc0 .Lswo_z16_%=\n" \
"s_waitcnt vmcnt(36)\n" \
"ds_read_b64_tr_b16 a[28:29], v1 offset:28672\n" \
"ds_read_b64_tr_b16 a[30:31], v1 offset:30720\n" \
"ds_read_b64_tr_b16 a[156:157], v1 offset:28704\n" \
"ds_read_b64_tr_b16 a[158:159], v1 offset:30752\n" \
"ds_read_b64_tr_b16 v[156:157], v1 offset:28736\n" \
"ds_read_b64_tr_b16 v[158:159], v1 offset:30784\n" \
"ds_read_b64_tr_b16 v[44:45], v1 offset:28768\n" \
"ds_read_b64_tr_b16 v[46:47], v1 offset:30816\n" \
"s_waitcnt lgkmcnt(0)\n" \
"s_add_u32 m0, s52, 28672\n" \
"s_add_u32 s54, s90, 65536\n" \
"buffer_load_dwordx4 v0, s[40:43], s54 offen sc1 lds\n" \
"s_add_u32 m0, s52, 29696\n" \
"s_add_u32 s54, s90, 81920\n" \
"buffer_load_dwordx4 v0, s[40:43], s54 offen sc1 lds\n" \
"s_add_u32 m0, s52, 30720\n" \
"s_add_u32 s54, s90, 98304\n" \
"buffer_load_dwordx4 v0, s[40:43], s54 offen sc1 lds\n" \
"s_add_u32 m0, s52, 31744\n" \
"s_add_u32 s54, s90, 114688\n" \
"buffer_load_dwordx4 v0, s[40:43], s54 offen sc1 lds\n" \
"s_mov_b32 s54, s99\n" \
".Lswo_z17_%=:\n" \
"s_sleep 1\n" \
"s_sub_u32 s54, s54, 1\n" \
"s_cbranch_scc0 .Lswo_z17_%=\n" \
"s_waitcnt vmcnt(36)\n" \
"ds_read_b64_tr_b16 a[32:33], v9 offset:32768\n" \
"ds_read_b64_tr_b16 a[34:35], v9 offset:34816\n" \
"ds_read_b64_tr_b16 a[160:161], v9 offset:32800\n" \
"ds_read_b64_tr_b16 a[162:163], v9 offset:34848\n" \
"ds_read_b64_tr_b16 v[160:161], v9 offset:32832\n" \
"ds_read_b64_tr_b16 v[162:163], v9 offset:34880\n" \
"ds_read_b64_tr_b16 v[48:49], v9 offset:32864\n" \
"ds_read_b64_tr_b16 v[50:51], v9 offset:34912\n" \
"s_waitcnt lgkmcnt(0)\n" \
"s_add_u32 m0, s53, 32768\n" \
"s_add_u32 s54, s90, 131072\n" \
"buffer_load_dwordx4 v0, s[40:43], s54 offen sc1 lds\n" \
"s_add_u32 m0, s53, 33792\n" \
"s_add_u32 s54, s90, 147456\n" \
"buffer_load_dwordx4 v0, s[40:43], s54 offen sc1 lds\n" \
"s_add_u32 m0, s53, 34816\n" \
"s_add_u32 s54, s90, 163840\n" \
"buffer_load_dwordx4 v0, s[40:43], s54 offen sc1 lds\n" \
"s_add_u32 m0, s53, 35840\n" \
"s_add_u32 s54, s90, 180224\n" \
"buffer_load_dwordx4 v0, s[40:43], s54 offen sc1 lds\n" \
"s_mov_b32 s54, s99\n" \
".Lswo_z18_%=:\n" \
"s_sleep 1\n" \
"s_sub_u32 s54, s54, 1\n" \
"s_cbranch_scc0 .Lswo_z18_%=\n" \
"s_waitcnt vmcnt(36)\n" \
"ds_read_b64_tr_b16 a[36:37], v9 offset:36864\n" \
"ds_read_b64_tr_b16 a[38:39], v9 offset:38912\n" \
"ds_read_b64_tr_b16 a[164:165], v9 offset:36896\n" \
"ds_read_b64_tr_b16 a[166:167], v9 offset:38944\n" \
"ds_read_b64_tr_b16 v[164:165], v9 offset:36928\n" \
"ds_read_b64_tr_b16 v[166:167], v9 offset:38976\n" \
"ds_read_b64_tr_b16 v[52:53], v9 offset:36960\n" \
"ds_read_b64_tr_b16 v[54:55], v9 offset:39008\n" \
"s_waitcnt lgkmcnt(0)\n" \
"s_add_u32 m0, s53, 36864\n" \
"s_add_u32 s54, s90, 196608\n" \
"buffer_load_dwordx4 v0, s[40:43], s54 offen sc1 lds\n" \
"s_add_u32 m0, s53, 37888\n" \
"s_add_u32 s54, s90, 212992\n" \
"buffer_load_dwordx4 v0, s[40:43], s54 offen sc1 lds\n" \
"s_add_u32 m0, s53, 38912\n" \
"s_add_u32 s54, s90, 229376\n" \
"buffer_load_dwordx4 v0, s[40:43], s54 offen sc1 lds\n" \
"s_add_u32 m0, s53, 39936\n" \
"s_add_u32 s54, s90, 245760\n" \
"buffer_load_dwordx4 v0, s[40:43], s54 offen sc1 lds\n" \
"s_mov_b32 s54, s99\n" \
".Lswo_z19_%=:\n" \
"s_sleep 1\n" \
"s_sub_u32 s54, s54, 1\n" \
"s_cbranch_scc0 .Lswo_z19_%=\n" \
"s_waitcnt vmcnt(36)\n" \
"ds_read_b64_tr_b16 a[40:41], v1 offset:0\n" \
"ds_read_b64_tr_b16 a[42:43], v1 offset:2048\n" \
"ds_read_b64_tr_b16 a[168:169], v1 offset:32\n" \
"ds_read_b64_tr_b16 a[170:171], v1 offset:2080\n" \
"ds_read_b64_tr_b16 v[168:169], v1 offset:64\n" \
"ds_read_b64_tr_b16 v[170:171], v1 offset:2112\n" \
"ds_read_b64_tr_b16 v[56:57], v1 offset:96\n" \
"ds_read_b64_tr_b16 v[58:59], v1 offset:2144\n" \
"s_waitcnt lgkmcnt(0)\n" \
"s_add_u32 m0, s52, 0\n" \
"s_add_u32 s54, s91, 0\n" \
"buffer_load_dwordx4 v0, s[40:43], s54 offen sc1 lds\n" \
"s_add_u32 m0, s52, 1024\n" \
"s_add_u32 s54, s91, 16384\n" \
"buffer_load_dwordx4 v0, s[40:43], s54 offen sc1 lds\n" \
"s_add_u32 m0, s52, 2048\n" \
"s_add_u32 s54, s91, 32768\n" \
"buffer_load_dwordx4 v0, s[40:43], s54 offen sc1 lds\n" \
"s_add_u32 m0, s52, 3072\n" \
"s_add_u32 s54, s91, 49152\n" \
"buffer_load_dwordx4 v0, s[40:43], s54 offen sc1 lds\n" \
"s_mov_b32 s54, s99\n" \
".Lswo_z20_%=:\n" \
"s_sleep 1\n" \
"s_sub_u32 s54, s54, 1\n" \
"s_cbranch_scc0 .Lswo_z20_%=\n" \
"s_waitcnt vmcnt(36)\n" \
"ds_read_b64_tr_b16 a[44:45], v1 offset:4096\n" \
"ds_read_b64_tr_b16 a[46:47], v1 offset:6144\n" \
"ds_read_b64_tr_b16 a[172:173], v1 offset:4128\n" \
"ds_read_b64_tr_b16 a[174:175], v1 offset:6176\n" \
"ds_read_b64_tr_b16 v[172:173], v1 offset:4160\n" \
"ds_read_b64_tr_b16 v[174:175], v1 offset:6208\n" \
"ds_read_b64_tr_b16 v[60:61], v1 offset:4192\n" \
"ds_read_b64_tr_b16 v[62:63], v1 offset:6240\n" \
"s_waitcnt lgkmcnt(0)\n" \
"s_add_u32 m0, s52, 4096\n" \
"s_add_u32 s54, s91, 65536\n" \
"buffer_load_dwordx4 v0, s[40:43], s54 offen sc1 lds\n" \
"s_add_u32 m0, s52, 5120\n" \
"s_add_u32 s54, s91, 81920\n" \
"buffer_load_dwordx4 v0, s[40:43], s54 offen sc1 lds\n" \
"s_add_u32 m0, s52, 6144\n" \
"s_add_u32 s54, s91, 98304\n" \
"buffer_load_dwordx4 v0, s[40:43], s54 offen sc1 lds\n" \
"s_add_u32 m0, s52, 7168\n" \
"s_add_u32 s54, s91, 114688\n" \
"buffer_load_dwordx4 v0, s[40:43], s54 offen sc1 lds\n" \
"s_mov_b32 s54, s99\n" \
".Lswo_z21_%=:\n" \
"s_sleep 1\n" \
"s_sub_u32 s54, s54, 1\n" \
"s_cbranch_scc0 .Lswo_z21_%=\n" \
"s_waitcnt vmcnt(36)\n" \
"ds_read_b64_tr_b16 a[48:49], v1 offset:8192\n" \
"ds_read_b64_tr_b16 a[50:51], v1 offset:10240\n" \
"ds_read_b64_tr_b16 a[176:177], v1 offset:8224\n" \
"ds_read_b64_tr_b16 a[178:179], v1 offset:10272\n" \
"ds_read_b64_tr_b16 v[176:177], v1 offset:8256\n" \
"ds_read_b64_tr_b16 v[178:179], v1 offset:10304\n" \
"ds_read_b64_tr_b16 v[64:65], v1 offset:8288\n" \
"ds_read_b64_tr_b16 v[66:67], v1 offset:10336\n" \
"s_waitcnt lgkmcnt(0)\n" \
"s_add_u32 m0, s52, 8192\n" \
"s_add_u32 s54, s91, 131072\n" \
"buffer_load_dwordx4 v0, s[40:43], s54 offen sc1 lds\n" \
"s_add_u32 m0, s52, 9216\n" \
"s_add_u32 s54, s91, 147456\n" \
"buffer_load_dwordx4 v0, s[40:43], s54 offen sc1 lds\n" \
"s_add_u32 m0, s52, 10240\n" \
"s_add_u32 s54, s91, 163840\n" \
"buffer_load_dwordx4 v0, s[40:43], s54 offen sc1 lds\n" \
"s_add_u32 m0, s52, 11264\n" \
"s_add_u32 s54, s91, 180224\n" \
"buffer_load_dwordx4 v0, s[40:43], s54 offen sc1 lds\n" \
"s_mov_b32 s54, s99\n" \
".Lswo_z22_%=:\n" \
"s_sleep 1\n" \
"s_sub_u32 s54, s54, 1\n" \
"s_cbranch_scc0 .Lswo_z22_%=\n" \
"s_waitcnt vmcnt(36)\n" \
"ds_read_b64_tr_b16 a[52:53], v1 offset:12288\n" \
"ds_read_b64_tr_b16 a[54:55], v1 offset:14336\n" \
"ds_read_b64_tr_b16 a[180:181], v1 offset:12320\n" \
"ds_read_b64_tr_b16 a[182:183], v1 offset:14368\n" \
"ds_read_b64_tr_b16 v[180:181], v1 offset:12352\n" \
"ds_read_b64_tr_b16 v[182:183], v1 offset:14400\n" \
"ds_read_b64_tr_b16 v[68:69], v1 offset:12384\n" \
"ds_read_b64_tr_b16 v[70:71], v1 offset:14432\n" \
"s_waitcnt lgkmcnt(0)\n" \
"s_add_u32 m0, s52, 12288\n" \
"s_add_u32 s54, s91, 196608\n" \
"buffer_load_dwordx4 v0, s[40:43], s54 offen sc1 lds\n" \
"s_add_u32 m0, s52, 13312\n" \
"s_add_u32 s54, s91, 212992\n" \
"buffer_load_dwordx4 v0, s[40:43], s54 offen sc1 lds\n" \
"s_add_u32 m0, s52, 14336\n" \
"s_add_u32 s54, s91, 229376\n" \
"buffer_load_dwordx4 v0, s[40:43], s54 offen sc1 lds\n" \
"s_add_u32 m0, s52, 15360\n" \
"s_add_u32 s54, s91, 245760\n" \
"buffer_load_dwordx4 v0, s[40:43], s54 offen sc1 lds\n" \
"s_mov_b32 s54, s99\n" \
".Lswo_z23_%=:\n" \
"s_sleep 1\n" \
"s_sub_u32 s54, s54, 1\n" \
"s_cbranch_scc0 .Lswo_z23_%=\n" \
"s_waitcnt vmcnt(36)\n" \
"ds_read_b64_tr_b16 a[56:57], v1 offset:16384\n" \
"ds_read_b64_tr_b16 a[58:59], v1 offset:18432\n" \
"ds_read_b64_tr_b16 a[184:185], v1 offset:16416\n" \
"ds_read_b64_tr_b16 a[186:187], v1 offset:18464\n" \
"ds_read_b64_tr_b16 v[184:185], v1 offset:16448\n" \
"ds_read_b64_tr_b16 v[186:187], v1 offset:18496\n" \
"ds_read_b64_tr_b16 v[72:73], v1 offset:16480\n" \
"ds_read_b64_tr_b16 v[74:75], v1 offset:18528\n" \
"s_waitcnt lgkmcnt(0)\n" \
"s_add_u32 m0, s52, 16384\n" \
"s_add_u32 s54, s92, 0\n" \
"buffer_load_dwordx4 v0, s[40:43], s54 offen sc1 lds\n" \
"s_add_u32 m0, s52, 17408\n" \
"s_add_u32 s54, s92, 16384\n" \
"buffer_load_dwordx4 v0, s[40:43], s54 offen sc1 lds\n" \
"s_add_u32 m0, s52, 18432\n" \
"s_add_u32 s54, s92, 32768\n" \
"buffer_load_dwordx4 v0, s[40:43], s54 offen sc1 lds\n" \
"s_add_u32 m0, s52, 19456\n" \
"s_add_u32 s54, s92, 49152\n" \
"buffer_load_dwordx4 v0, s[40:43], s54 offen sc1 lds\n" \
"s_mov_b32 s54, s99\n" \
".Lswo_z24_%=:\n" \
"s_sleep 1\n" \
"s_sub_u32 s54, s54, 1\n" \
"s_cbranch_scc0 .Lswo_z24_%=\n" \
"s_waitcnt vmcnt(36)\n" \
"ds_read_b64_tr_b16 a[60:61], v1 offset:20480\n" \
"ds_read_b64_tr_b16 a[62:63], v1 offset:22528\n" \
"ds_read_b64_tr_b16 a[188:189], v1 offset:20512\n" \
"ds_read_b64_tr_b16 a[190:191], v1 offset:22560\n" \
"ds_read_b64_tr_b16 v[188:189], v1 offset:20544\n" \
"ds_read_b64_tr_b16 v[190:191], v1 offset:22592\n" \
"ds_read_b64_tr_b16 v[76:77], v1 offset:20576\n" \
"ds_read_b64_tr_b16 v[78:79], v1 offset:22624\n" \
"s_waitcnt lgkmcnt(0)\n" \
"s_add_u32 m0, s52, 20480\n" \
"s_add_u32 s54, s92, 65536\n" \
"buffer_load_dwordx4 v0, s[40:43], s54 offen sc1 lds\n" \
"s_add_u32 m0, s52, 21504\n" \
"s_add_u32 s54, s92, 81920\n" \
"buffer_load_dwordx4 v0, s[40:43], s54 offen sc1 lds\n" \
"s_add_u32 m0, s52, 22528\n" \
"s_add_u32 s54, s92, 98304\n" \
"buffer_load_dwordx4 v0, s[40:43], s54 offen sc1 lds\n" \
"s_add_u32 m0, s52, 23552\n" \
"s_add_u32 s54, s92, 114688\n" \
"buffer_load_dwordx4 v0, s[40:43], s54 offen sc1 lds\n" \
"s_mov_b32 s54, s99\n" \
".Lswo_z25_%=:\n" \
"s_sleep 1\n" \
"s_sub_u32 s54, s54, 1\n" \
"s_cbranch_scc0 .Lswo_z25_%=\n" \
"s_waitcnt vmcnt(36)\n" \
"ds_read_b64_tr_b16 a[64:65], v1 offset:24576\n" \
"ds_read_b64_tr_b16 a[66:67], v1 offset:26624\n" \
"ds_read_b64_tr_b16 a[192:193], v1 offset:24608\n" \
"ds_read_b64_tr_b16 a[194:195], v1 offset:26656\n" \
"ds_read_b64_tr_b16 v[192:193], v1 offset:24640\n" \
"ds_read_b64_tr_b16 v[194:195], v1 offset:26688\n" \
"ds_read_b64_tr_b16 v[80:81], v1 offset:24672\n" \
"ds_read_b64_tr_b16 v[82:83], v1 offset:26720\n" \
"s_waitcnt lgkmcnt(0)\n" \
"s_add_u32 m0, s52, 24576\n" \
"s_add_u32 s54, s92, 131072\n" \
"buffer_load_dwordx4 v0, s[40:43], s54 offen sc1 lds\n" \
"s_add_u32 m0, s52, 25600\n" \
"s_add_u32 s54, s92, 147456\n" \
"buffer_load_dwordx4 v0, s[40:43], s54 offen sc1 lds\n" \
"s_add_u32 m0, s52, 26624\n" \
"s_add_u32 s54, s92, 163840\n" \
"buffer_load_dwordx4 v0, s[40:43], s54 offen sc1 lds\n" \
"s_add_u32 m0, s52, 27648\n" \
"s_add_u32 s54, s92, 180224\n" \
"buffer_load_dwordx4 v0, s[40:43], s54 offen sc1 lds\n" \
"s_mov_b32 s54, s99\n" \
".Lswo_z26_%=:\n" \
"s_sleep 1\n" \
"s_sub_u32 s54, s54, 1\n" \
"s_cbranch_scc0 .Lswo_z26_%=\n" \
"s_waitcnt vmcnt(36)\n" \
"ds_read_b64_tr_b16 a[68:69], v1 offset:28672\n" \
"ds_read_b64_tr_b16 a[70:71], v1 offset:30720\n" \
"ds_read_b64_tr_b16 a[196:197], v1 offset:28704\n" \
"ds_read_b64_tr_b16 a[198:199], v1 offset:30752\n" \
"ds_read_b64_tr_b16 v[196:197], v1 offset:28736\n" \
"ds_read_b64_tr_b16 v[198:199], v1 offset:30784\n" \
"ds_read_b64_tr_b16 v[84:85], v1 offset:28768\n" \
"ds_read_b64_tr_b16 v[86:87], v1 offset:30816\n" \
"s_waitcnt lgkmcnt(0)\n" \
"s_add_u32 m0, s52, 28672\n" \
"s_add_u32 s54, s92, 196608\n" \
"buffer_load_dwordx4 v0, s[40:43], s54 offen sc1 lds\n" \
"s_add_u32 m0, s52, 29696\n" \
"s_add_u32 s54, s92, 212992\n" \
"buffer_load_dwordx4 v0, s[40:43], s54 offen sc1 lds\n" \
"s_add_u32 m0, s52, 30720\n" \
"s_add_u32 s54, s92, 229376\n" \
"buffer_load_dwordx4 v0, s[40:43], s54 offen sc1 lds\n" \
"s_add_u32 m0, s52, 31744\n" \
"s_add_u32 s54, s92, 245760\n" \
"buffer_load_dwordx4 v0, s[40:43], s54 offen sc1 lds\n" \
"s_mov_b32 s54, s99\n" \
".Lswo_z27_%=:\n" \
"s_sleep 1\n" \
"s_sub_u32 s54, s54, 1\n" \
"s_cbranch_scc0 .Lswo_z27_%=\n" \
"s_waitcnt vmcnt(36)\n" \
"ds_read_b64_tr_b16 a[72:73], v9 offset:32768\n" \
"ds_read_b64_tr_b16 a[74:75], v9 offset:34816\n" \
"ds_read_b64_tr_b16 a[200:201], v9 offset:32800\n" \
"ds_read_b64_tr_b16 a[202:203], v9 offset:34848\n" \
"ds_read_b64_tr_b16 v[200:201], v9 offset:32832\n" \
"ds_read_b64_tr_b16 v[202:203], v9 offset:34880\n" \
"ds_read_b64_tr_b16 v[88:89], v9 offset:32864\n" \
"ds_read_b64_tr_b16 v[90:91], v9 offset:34912\n" \
"s_waitcnt lgkmcnt(0)\n" \
"s_add_u32 m0, s53, 32768\n" \
"s_add_u32 s54, s93, 0\n" \
"buffer_load_dwordx4 v0, s[40:43], s54 offen sc1 lds\n" \
"s_add_u32 m0, s53, 33792\n" \
"s_add_u32 s54, s93, 16384\n" \
"buffer_load_dwordx4 v0, s[40:43], s54 offen sc1 lds\n" \
"s_add_u32 m0, s53, 34816\n" \
"s_add_u32 s54, s93, 32768\n" \
"buffer_load_dwordx4 v0, s[40:43], s54 offen sc1 lds\n" \
"s_add_u32 m0, s53, 35840\n" \
"s_add_u32 s54, s93, 49152\n" \
"buffer_load_dwordx4 v0, s[40:43], s54 offen sc1 lds\n" \
"s_mov_b32 s54, s99\n" \
".Lswo_z28_%=:\n" \
"s_sleep 1\n" \
"s_sub_u32 s54, s54, 1\n" \
"s_cbranch_scc0 .Lswo_z28_%=\n" \
"s_waitcnt vmcnt(36)\n" \
"ds_read_b64_tr_b16 a[76:77], v9 offset:36864\n" \
"ds_read_b64_tr_b16 a[78:79], v9 offset:38912\n" \
"ds_read_b64_tr_b16 a[204:205], v9 offset:36896\n" \
"ds_read_b64_tr_b16 a[206:207], v9 offset:38944\n" \
"ds_read_b64_tr_b16 v[204:205], v9 offset:36928\n" \
"ds_read_b64_tr_b16 v[206:207], v9 offset:38976\n" \
"ds_read_b64_tr_b16 v[92:93], v9 offset:36960\n" \
"ds_read_b64_tr_b16 v[94:95], v9 offset:39008\n" \
"s_waitcnt lgkmcnt(0)\n" \
"s_add_u32 m0, s53, 36864\n" \
"s_add_u32 s54, s93, 65536\n" \
"buffer_load_dwordx4 v0, s[40:43], s54 offen sc1 lds\n" \
"s_add_u32 m0, s53, 37888\n" \
"s_add_u32 s54, s93, 81920\n" \
"buffer_load_dwordx4 v0, s[40:43], s54 offen sc1 lds\n" \
"s_add_u32 m0, s53, 38912\n" \
"s_add_u32 s54, s93, 98304\n" \
"buffer_load_dwordx4 v0, s[40:43], s54 offen sc1 lds\n" \
"s_add_u32 m0, s53, 39936\n" \
"s_add_u32 s54, s93, 114688\n" \
"buffer_load_dwordx4 v0, s[40:43], s54 offen sc1 lds\n" \
"s_mov_b32 s54, s99\n" \
".Lswo_z29_%=:\n" \
"s_sleep 1\n" \
"s_sub_u32 s54, s54, 1\n" \
"s_cbranch_scc0 .Lswo_z29_%=\n" \
"s_waitcnt vmcnt(36)\n" \
"ds_read_b64_tr_b16 a[80:81], v1 offset:0\n" \
"ds_read_b64_tr_b16 a[82:83], v1 offset:2048\n" \
"ds_read_b64_tr_b16 a[208:209], v1 offset:32\n" \
"ds_read_b64_tr_b16 a[210:211], v1 offset:2080\n" \
"ds_read_b64_tr_b16 v[208:209], v1 offset:64\n" \
"ds_read_b64_tr_b16 v[210:211], v1 offset:2112\n" \
"ds_read_b64_tr_b16 v[96:97], v1 offset:96\n" \
"ds_read_b64_tr_b16 v[98:99], v1 offset:2144\n" \
"s_waitcnt lgkmcnt(0)\n" \
"s_add_u32 m0, s52, 0\n" \
"s_add_u32 s54, s93, 131072\n" \
"buffer_load_dwordx4 v0, s[40:43], s54 offen sc1 lds\n" \
"s_add_u32 m0, s52, 1024\n" \
"s_add_u32 s54, s93, 147456\n" \
"buffer_load_dwordx4 v0, s[40:43], s54 offen sc1 lds\n" \
"s_add_u32 m0, s52, 2048\n" \
"s_add_u32 s54, s93, 163840\n" \
"buffer_load_dwordx4 v0, s[40:43], s54 offen sc1 lds\n" \
"s_add_u32 m0, s52, 3072\n" \
"s_add_u32 s54, s93, 180224\n" \
"buffer_load_dwordx4 v0, s[40:43], s54 offen sc1 lds\n" \
"s_mov_b32 s54, s99\n" \
".Lswo_z30_%=:\n" \
"s_sleep 1\n" \
"s_sub_u32 s54, s54, 1\n" \
"s_cbranch_scc0 .Lswo_z30_%=\n" \
"s_waitcnt vmcnt(36)\n" \
"ds_read_b64_tr_b16 a[84:85], v1 offset:4096\n" \
"ds_read_b64_tr_b16 a[86:87], v1 offset:6144\n" \
"ds_read_b64_tr_b16 a[212:213], v1 offset:4128\n" \
"ds_read_b64_tr_b16 a[214:215], v1 offset:6176\n" \
"ds_read_b64_tr_b16 v[212:213], v1 offset:4160\n" \
"ds_read_b64_tr_b16 v[214:215], v1 offset:6208\n" \
"ds_read_b64_tr_b16 v[100:101], v1 offset:4192\n" \
"ds_read_b64_tr_b16 v[102:103], v1 offset:6240\n" \
"s_waitcnt lgkmcnt(0)\n" \
"s_add_u32 m0, s52, 4096\n" \
"s_add_u32 s54, s93, 196608\n" \
"buffer_load_dwordx4 v0, s[40:43], s54 offen sc1 lds\n" \
"s_add_u32 m0, s52, 5120\n" \
"s_add_u32 s54, s93, 212992\n" \
"buffer_load_dwordx4 v0, s[40:43], s54 offen sc1 lds\n" \
"s_add_u32 m0, s52, 6144\n" \
"s_add_u32 s54, s93, 229376\n" \
"buffer_load_dwordx4 v0, s[40:43], s54 offen sc1 lds\n" \
"s_add_u32 m0, s52, 7168\n" \
"s_add_u32 s54, s93, 245760\n" \
"buffer_load_dwordx4 v0, s[40:43], s54 offen sc1 lds\n" \
"s_mov_b32 s54, s99\n" \
".Lswo_z31_%=:\n" \
"s_sleep 1\n" \
"s_sub_u32 s54, s54, 1\n" \
"s_cbranch_scc0 .Lswo_z31_%=\n" \
"s_waitcnt vmcnt(36)\n" \
"ds_read_b64_tr_b16 a[88:89], v1 offset:8192\n" \
"ds_read_b64_tr_b16 a[90:91], v1 offset:10240\n" \
"ds_read_b64_tr_b16 a[216:217], v1 offset:8224\n" \
"ds_read_b64_tr_b16 a[218:219], v1 offset:10272\n" \
"ds_read_b64_tr_b16 v[216:217], v1 offset:8256\n" \
"ds_read_b64_tr_b16 v[218:219], v1 offset:10304\n" \
"ds_read_b64_tr_b16 v[104:105], v1 offset:8288\n" \
"ds_read_b64_tr_b16 v[106:107], v1 offset:10336\n" \
"s_waitcnt lgkmcnt(0)\n" \
"ds_write_b128 v2, v[16:19] offset:8192\n" \
"ds_write_b128 v2, v[20:23] offset:9216\n" \
"ds_write_b128 v2, v[24:27] offset:10240\n" \
"ds_write_b128 v2, v[28:31] offset:11264\n" \
"s_waitcnt vmcnt(32)\n" \
"ds_read_b64_tr_b16 a[92:93], v1 offset:12288\n" \
"ds_read_b64_tr_b16 a[94:95], v1 offset:14336\n" \
"ds_read_b64_tr_b16 a[220:221], v1 offset:12320\n" \
"ds_read_b64_tr_b16 a[222:223], v1 offset:14368\n" \
"ds_read_b64_tr_b16 v[220:221], v1 offset:12352\n" \
"ds_read_b64_tr_b16 v[222:223], v1 offset:14400\n" \
"ds_read_b64_tr_b16 v[108:109], v1 offset:12384\n" \
"ds_read_b64_tr_b16 v[110:111], v1 offset:14432\n" \
"s_waitcnt lgkmcnt(0)\n" \
"ds_write_b128 v2, v[32:35] offset:12288\n" \
"ds_write_b128 v2, v[36:39] offset:13312\n" \
"ds_write_b128 v2, v[40:43] offset:14336\n" \
"ds_write_b128 v2, v[44:47] offset:15360\n" \
"s_waitcnt vmcnt(28)\n" \
"ds_read_b64_tr_b16 a[96:97], v1 offset:16384\n" \
"ds_read_b64_tr_b16 a[98:99], v1 offset:18432\n" \
"ds_read_b64_tr_b16 a[224:225], v1 offset:16416\n" \
"ds_read_b64_tr_b16 a[226:227], v1 offset:18464\n" \
"ds_read_b64_tr_b16 v[224:225], v1 offset:16448\n" \
"ds_read_b64_tr_b16 v[226:227], v1 offset:18496\n" \
"ds_read_b64_tr_b16 v[16:17], v1 offset:16480\n" \
"ds_read_b64_tr_b16 v[18:19], v1 offset:18528\n" \
"s_waitcnt lgkmcnt(0)\n" \
"ds_write_b128 v2, v[48:51] offset:16384\n" \
"ds_write_b128 v2, v[52:55] offset:17408\n" \
"ds_write_b128 v2, v[56:59] offset:18432\n" \
"ds_write_b128 v2, v[60:63] offset:19456\n" \
"s_waitcnt vmcnt(24)\n" \
"ds_read_b64_tr_b16 a[100:101], v1 offset:20480\n" \
"ds_read_b64_tr_b16 a[102:103], v1 offset:22528\n" \
"ds_read_b64_tr_b16 a[228:229], v1 offset:20512\n" \
"ds_read_b64_tr_b16 a[230:231], v1 offset:22560\n" \
"ds_read_b64_tr_b16 v[228:229], v1 offset:20544\n" \
"ds_read_b64_tr_b16 v[230:231], v1 offset:22592\n" \
"ds_read_b64_tr_b16 v[20:21], v1 offset:20576\n" \
"ds_read_b64_tr_b16 v[22:23], v1 offset:22624\n" \
"s_waitcnt lgkmcnt(0)\n" \
"ds_write_b128 v2, v[64:67] offset:20480\n" \
"ds_write_b128 v2, v[68:71] offset:21504\n" \
"ds_write_b128 v2, v[72:75] offset:22528\n" \
"ds_write_b128 v2, v[76:79] offset:23552\n" \
"s_waitcnt vmcnt(20)\n" \
"ds_read_b64_tr_b16 a[104:105], v1 offset:24576\n" \
"ds_read_b64_tr_b16 a[106:107], v1 offset:26624\n" \
"ds_read_b64_tr_b16 a[232:233], v1 offset:24608\n" \
"ds_read_b64_tr_b16 a[234:235], v1 offset:26656\n" \
"ds_read_b64_tr_b16 v[232:233], v1 offset:24640\n" \
"ds_read_b64_tr_b16 v[234:235], v1 offset:26688\n" \
"ds_read_b64_tr_b16 v[24:25], v1 offset:24672\n" \
"ds_read_b64_tr_b16 v[26:27], v1 offset:26720\n" \
"s_waitcnt lgkmcnt(0)\n" \
"ds_write_b128 v2, v[80:83] offset:24576\n" \
"ds_write_b128 v2, v[84:87] offset:25600\n" \
"ds_write_b128 v2, v[88:91] offset:26624\n" \
"ds_write_b128 v2, v[92:95] offset:27648\n" \
"s_waitcnt vmcnt(16)\n" \
"ds_read_b64_tr_b16 a[108:109], v1 offset:28672\n" \
"ds_read_b64_tr_b16 a[110:111], v1 offset:30720\n" \
"ds_read_b64_tr_b16 a[236:237], v1 offset:28704\n" \
"ds_read_b64_tr_b16 a[238:239], v1 offset:30752\n" \
"ds_read_b64_tr_b16 v[236:237], v1 offset:28736\n" \
"ds_read_b64_tr_b16 v[238:239], v1 offset:30784\n" \
"ds_read_b64_tr_b16 v[28:29], v1 offset:28768\n" \
"ds_read_b64_tr_b16 v[30:31], v1 offset:30816\n" \
"s_waitcnt lgkmcnt(0)\n" \
"ds_write_b128 v2, v[96:99] offset:28672\n" \
"ds_write_b128 v2, v[100:103] offset:29696\n" \
"ds_write_b128 v2, v[104:107] offset:30720\n" \
"ds_write_b128 v2, v[108:111] offset:31744\n" \
"s_waitcnt vmcnt(12)\n" \
"ds_read_b64_tr_b16 a[112:113], v9 offset:32768\n" \
"ds_read_b64_tr_b16 a[114:115], v9 offset:34816\n" \
"ds_read_b64_tr_b16 a[240:241], v9 offset:32800\n" \
"ds_read_b64_tr_b16 a[242:243], v9 offset:34848\n" \
"ds_read_b64_tr_b16 v[240:241], v9 offset:32832\n" \
"ds_read_b64_tr_b16 v[242:243], v9 offset:34880\n" \
"ds_read_b64_tr_b16 v[32:33], v9 offset:32864\n" \
"ds_read_b64_tr_b16 v[34:35], v9 offset:34912\n" \
"s_waitcnt lgkmcnt(0)\n" \
"s_waitcnt vmcnt(8)\n" \
"ds_read_b64_tr_b16 a[116:117], v9 offset:36864\n" \
"ds_read_b64_tr_b16 a[118:119], v9 offset:38912\n" \
"ds_read_b64_tr_b16 a[244:245], v9 offset:36896\n" \
"ds_read_b64_tr_b16 a[246:247], v9 offset:38944\n" \
"ds_read_b64_tr_b16 v[244:245], v9 offset:36928\n" \
"ds_read_b64_tr_b16 v[246:247], v9 offset:38976\n" \
"ds_read_b64_tr_b16 v[36:37], v9 offset:36960\n" \
"ds_read_b64_tr_b16 v[38:39], v9 offset:39008\n" \
"s_waitcnt lgkmcnt(0)\n" \
"s_waitcnt vmcnt(4)\n" \
"ds_read_b64_tr_b16 a[120:121], v1 offset:0\n" \
"ds_read_b64_tr_b16 a[122:123], v1 offset:2048\n" \
"ds_read_b64_tr_b16 a[248:249], v1 offset:32\n" \
"ds_read_b64_tr_b16 a[250:251], v1 offset:2080\n" \
"ds_read_b64_tr_b16 v[248:249], v1 offset:64\n" \
"ds_read_b64_tr_b16 v[250:251], v1 offset:2112\n" \
"ds_read_b64_tr_b16 v[40:41], v1 offset:96\n" \
"ds_read_b64_tr_b16 v[42:43], v1 offset:2144\n" \
"s_waitcnt lgkmcnt(0)\n" \
"ds_write_b128 v2, v[16:19] offset:0\n" \
"ds_write_b128 v2, v[20:23] offset:1024\n" \
"ds_write_b128 v2, v[24:27] offset:2048\n" \
"ds_write_b128 v2, v[28:31] offset:3072\n" \
"s_waitcnt vmcnt(0)\n" \
"ds_read_b64_tr_b16 a[124:125], v1 offset:4096\n" \
"ds_read_b64_tr_b16 a[126:127], v1 offset:6144\n" \
"ds_read_b64_tr_b16 a[252:253], v1 offset:4128\n" \
"ds_read_b64_tr_b16 a[254:255], v1 offset:6176\n" \
"ds_read_b64_tr_b16 v[252:253], v1 offset:4160\n" \
"ds_read_b64_tr_b16 v[254:255], v1 offset:6208\n" \
"ds_read_b64_tr_b16 v[44:45], v1 offset:4192\n" \
"ds_read_b64_tr_b16 v[46:47], v1 offset:6240\n" \
"s_waitcnt lgkmcnt(0)\n" \
"ds_write_b128 v2, v[32:35] offset:4096\n" \
"ds_write_b128 v2, v[36:39] offset:5120\n" \
"ds_write_b128 v2, v[40:43] offset:6144\n" \
"ds_write_b128 v2, v[44:47] offset:7168\n" \
"s_waitcnt vmcnt(0) lgkmcnt(0)\n" \
"s_barrier\n" \
"s_mov_b32 s54, s85\n" \
"s_mul_i32 s86, s54, 532480\n" \
"s_add_u32 s54, s54, 1\n" \
"s_cmp_eq_u32 s54, 8\n" \
"s_cselect_b32 s54, 0, s54\n" \
"s_mul_i32 s87, s54, 532480\n" \
"s_add_u32 s54, s54, 1\n" \
"s_cmp_eq_u32 s54, 8\n" \
"s_cselect_b32 s54, 0, s54\n" \
"s_mul_i32 s88, s54, 532480\n" \
"s_add_u32 s54, s54, 1\n" \
"s_cmp_eq_u32 s54, 8\n" \
"s_cselect_b32 s54, 0, s54\n" \
"s_mul_i32 s89, s54, 532480\n" \
"s_add_u32 s54, s54, 1\n" \
"s_cmp_eq_u32 s54, 8\n" \
"s_cselect_b32 s54, 0, s54\n" \
"s_mul_i32 s90, s54, 532480\n" \
"s_add_u32 s54, s54, 1\n" \
"s_cmp_eq_u32 s54, 8\n" \
"s_cselect_b32 s54, 0, s54\n" \
"s_mul_i32 s91, s54, 532480\n" \
"s_add_u32 s54, s54, 1\n" \
"s_cmp_eq_u32 s54, 8\n" \
"s_cselect_b32 s54, 0, s54\n" \
"s_mul_i32 s92, s54, 532480\n" \
"s_add_u32 s54, s54, 1\n" \
"s_cmp_eq_u32 s54, 8\n" \
"s_cselect_b32 s54, 0, s54\n" \
"s_mul_i32 s93, s54, 532480\n" \
"s_add_u32 s54, s54, 1\n" \
"s_cmp_eq_u32 s54, 8\n" \
"s_cselect_b32 s54, 0, s54\n" \
"s_mov_b32 s55, 0\n" \
"s_mov_b32 s60, 0\n" \
".Lswo_prime_%=:\n" \
"s_lshr_b32 s59, s44, 1\n" \
"s_add_u32 s59, s59, 1\n" \
"s_mul_i32 s56, s44, 4096\n" \
"s_lshl_b32 s78, s44, 15\n" \
"s_lshl_b32 s97, s44, 2\n" \
"s_add_u32 s97, s97, s49\n" \
"s_mov_b32 s79, s97\n" \
"buffer_load_dword v9, off, s[36:39], s97 sc1\n" \
"buffer_load_dword v11, v4, s[36:39], 0 offen sc1\n" \
"s_waitcnt vmcnt(0)\n" \
"v_cmp_gt_u32_e64 s[62:63], s59, v11\n" \
"s_nop 4\n" \
"s_cmp_lg_u64 s[62:63], 0\n" \
"s_cbranch_scc1 .Lswo_w_%=\n" \
".Lswo_wk_%=:\n" \
"s_mov_b32 s98, 0x70000000\n" \
"s_mov_b32 s81, 1\n" \
"s_mov_b32 s84, 0\n" \
"v_mov_b32 v10, 0\n" \
"s_waitcnt vmcnt(0) lgkmcnt(0)\n" \
"s_add_u32 s57, s56, s86\n" \
"buffer_load_dwordx4 v[16:19], v3, s[36:39], s57 offen offset:0 sc1\n" \
"buffer_load_dwordx4 v[20:23], v3, s[36:39], s57 offen offset:1024 sc1\n" \
"buffer_load_dwordx4 v[24:27], v3, s[36:39], s57 offen offset:2048 sc1\n" \
"buffer_load_dwordx4 v[28:31], v3, s[36:39], s57 offen offset:3072 sc1\n" \
"s_add_u32 s57, s56, s87\n" \
"buffer_load_dwordx4 v[32:35], v3, s[36:39], s57 offen offset:0 sc1\n" \
"buffer_load_dwordx4 v[36:39], v3, s[36:39], s57 offen offset:1024 sc1\n" \
"buffer_load_dwordx4 v[40:43], v3, s[36:39], s57 offen offset:2048 sc1\n" \
"buffer_load_dwordx4 v[44:47], v3, s[36:39], s57 offen offset:3072 sc1\n" \
"s_add_u32 s57, s56, s88\n" \
"buffer_load_dwordx4 v[48:51], v3, s[36:39], s57 offen offset:0 sc1\n" \
"buffer_load_dwordx4 v[52:55], v3, s[36:39], s57 offen offset:1024 sc1\n" \
"buffer_load_dwordx4 v[56:59], v3, s[36:39], s57 offen offset:2048 sc1\n" \
"buffer_load_dwordx4 v[60:63], v3, s[36:39], s57 offen offset:3072 sc1\n" \
"s_add_u32 s57, s56, s89\n" \
"buffer_load_dwordx4 v[64:67], v3, s[36:39], s57 offen offset:0 sc1\n" \
"buffer_load_dwordx4 v[68:71], v3, s[36:39], s57 offen offset:1024 sc1\n" \
"buffer_load_dwordx4 v[72:75], v3, s[36:39], s57 offen offset:2048 sc1\n" \
"buffer_load_dwordx4 v[76:79], v3, s[36:39], s57 offen offset:3072 sc1\n" \
"ds_read_b128 v[80:83], v2 offset:8192\n" \
"ds_read_b128 v[84:87], v2 offset:9216\n" \
"ds_read_b128 v[88:91], v2 offset:10240\n" \
"s_waitcnt lgkmcnt(0)\n" \
"v_mov_b32 v13, 0x7ffffff0\n" \
"buffer_store_dword v13, v13, s[36:39], 0 offen\n" \
"buffer_store_dword v13, v13, s[36:39], 0 offen\n" \
"buffer_store_dword v13, v13, s[36:39], 0 offen\n" \
"s_waitcnt vmcnt(18)\n" \
"s_nop 7\n" \
"s_nop 7\n" \
".p2align 6\n" \
".Lswo_loop_%=:\n" \
"v_mfma_f32_16x16x32_bf16 v[96:99], a[0:3], v[16:19], 0\n" \
"ds_write_b128 v7, v[104:107] offset:2048\n" \
"v_xor_b32_e64 v7, 64, v7\n" \
"v_mfma_f32_16x16x32_bf16 v[100:103], a[128:131], v[16:19], 0\n" \
"ds_write_b128 v7, v[108:111] offset:3072\n" \
"ds_read_b128 v[92:95], v2 offset:11264\n" \
"v_xor_b32 v7, 192, v7\n" \
"v_mfma_f32_16x16x32_bf16 v[104:107], v[128:131], v[16:19], 0\n" \
"s_waitcnt vmcnt(17) lgkmcnt(7)\n" \
"v_mfma_f32_16x16x32_bf16 v[108:111], v[80:83], v[16:19], 0\n" \
"s_add_u32 s57, s56, s90\n" \
"buffer_load_dwordx4 v[16:19], v3, s[36:39], s57 offen offset:0 sc1\n" \
"v_mfma_f32_16x16x32_bf16 v[96:99], a[4:7], v[20:23], v[96:99]\n" \
"v_readfirstlane_b32 s96, v9\n" \
"v_mfma_f32_16x16x32_bf16 v[100:103], a[132:135], v[20:23], v[100:103]\n" \
"ds_read_b128 v[80:83], v2 offset:12288\n" \
"v_mfma_f32_16x16x32_bf16 v[104:107], v[132:135], v[20:23], v[104:107]\n" \
"s_waitcnt vmcnt(17) lgkmcnt(7)\n" \
"v_mfma_f32_16x16x32_bf16 v[108:111], v[84:87], v[20:23], v[108:111]\n" \
"buffer_load_dwordx4 v[20:23], v3, s[36:39], s57 offen offset:1024 sc1\n" \
"v_mfma_f32_16x16x32_bf16 v[96:99], a[8:11], v[24:27], v[96:99]\n" \
"s_waitcnt lgkmcnt(2)\n" \
"s_cmp_lg_u32 s81, 0\n" \
"s_cbranch_scc1 .Lswo_nb_%=\n" \
"s_barrier\n" \
".Lswo_nb_%=:\n" \
"v_mfma_f32_16x16x32_bf16 v[100:103], a[136:139], v[24:27], v[100:103]\n" \
"ds_read_b128 v[84:87], v2 offset:13312\n" \
"v_xor_b32_e64 v7, s84, v7\n" \
"v_mfma_f32_16x16x32_bf16 v[104:107], v[136:139], v[24:27], v[104:107]\n" \
"s_waitcnt vmcnt(17)\n" \
"v_mfma_f32_16x16x32_bf16 v[108:111], v[88:91], v[24:27], v[108:111]\n" \
"buffer_load_dwordx4 v[24:27], v3, s[36:39], s57 offen offset:2048 sc1\n" \
"v_mfma_f32_16x16x32_bf16 v[96:99], a[12:15], v[28:31], v[96:99]\n" \
"s_add_u32 s76, s78, 0\n" \
"s_add_u32 s80, s78, 4194304\n" \
"s_and_b32 s96, s96, 2\n" \
"v_mfma_f32_16x16x32_bf16 v[100:103], a[140:143], v[28:31], v[100:103]\n" \
"ds_read_b128 v[88:91], v2 offset:14336\n" \
"s_cselect_b32 s76, 0x7ffffff0, s76\n" \
"s_cselect_b32 s77, s78, 0x7ffffff0\n" \
"v_mfma_f32_16x16x32_bf16 v[104:107], v[140:143], v[28:31], v[104:107]\n" \
"s_waitcnt vmcnt(17) lgkmcnt(3)\n" \
"s_cselect_b32 s80, s80, 0x7ffffff0\n" \
"s_cselect_b32 s94, 0x7ffffff0, s79\n" \
"v_mfma_f32_16x16x32_bf16 v[108:111], v[92:95], v[28:31], v[108:111]\n" \
"buffer_load_dwordx4 v[28:31], v3, s[36:39], s57 offen offset:3072 sc1\n" \
"v_mfma_f32_16x16x32_bf16 v[96:99], a[16:19], v[32:35], v[96:99]\n" \
"s_cmp_lt_i32 s44, s50\n" \
"s_cselect_b32 s82, s45, s47\n" \
"v_mfma_f32_16x16x32_bf16 v[100:103], a[144:147], v[32:35], v[100:103]\n" \
"ds_read_b128 v[92:95], v2 offset:15360\n" \
"s_add_u32 s82, s82, s44\n" \
"v_mfma_f32_16x16x32_bf16 v[104:107], v[144:147], v[32:35], v[104:107]\n" \
"s_waitcnt vmcnt(17) lgkmcnt(3)\n" \
"s_lshr_b32 s59, s82, 1\n" \
"s_add_u32 s59, s59, 1\n" \
"v_mfma_f32_16x16x32_bf16 v[108:111], v[80:83], v[32:35], v[108:111]\n" \
"s_add_u32 s57, s56, s91\n" \
"buffer_load_dwordx4 v[32:35], v3, s[36:39], s57 offen offset:0 sc1\n" \
"v_mfma_f32_16x16x32_bf16 v[96:99], a[20:23], v[36:39], v[96:99]\n" \
"s_or_b32 s85, s77, s98\n" \
"s_waitcnt vmcnt(7)\n" \
"v_readfirstlane_b32 s54, v10\n" \
"v_mfma_f32_16x16x32_bf16 v[100:103], a[148:151], v[36:39], v[100:103]\n" \
"ds_read_b128 v[80:83], v2 offset:16384\n" \
"s_or_b32 s76, s76, s98\n" \
"s_or_b32 s94, s94, s98\n" \
"v_mfma_f32_16x16x32_bf16 v[104:107], v[148:151], v[36:39], v[104:107]\n" \
"s_waitcnt lgkmcnt(3)\n" \
"s_and_b32 s54, s54, 2\n" \
"s_cbranch_scc1 .Lswo_p_%=\n" \
".Lswo_pk_%=:\n" \
"v_mfma_f32_16x16x32_bf16 v[108:111], v[84:87], v[36:39], v[108:111]\n" \
"buffer_load_dwordx4 v[36:39], v3, s[36:39], s57 offen offset:1024 sc1\n" \
"v_mfma_f32_16x16x32_bf16 v[96:99], a[24:27], v[40:43], v[96:99]\n" \
"buffer_load_dwordx2 v[14:15], v6, s[36:39], s80 offen sc1\n" \
"ds_read_b128 v[112:115], v8 offset:0\n" \
"v_mfma_f32_16x16x32_bf16 v[100:103], a[152:155], v[40:43], v[100:103]\n" \
"ds_read_b128 v[84:87], v2 offset:17408\n" \
"v_mfma_f32_16x16x32_bf16 v[104:107], v[152:155], v[40:43], v[104:107]\n" \
"s_waitcnt lgkmcnt(4)\n" \
"buffer_load_dwordx2 v[12:13], v5, s[64:67], s77 offen\n" \
"ds_read_b128 v[116:119], v8 offset:4096\n" \
"v_mfma_f32_16x16x32_bf16 v[108:111], v[88:91], v[40:43], v[108:111]\n" \
"buffer_load_dwordx4 v[40:43], v3, s[36:39], s57 offen offset:2048 sc1\n" \
"v_mfma_f32_16x16x32_bf16 v[96:99], a[28:31], v[44:47], v[96:99]\n" \
"ds_read_b128 v[120:123], v8 offset:8192\n" \
"v_mfma_f32_16x16x32_bf16 v[100:103], a[156:159], v[44:47], v[100:103]\n" \
"ds_read_b128 v[88:91], v2 offset:18432\n" \
"v_mfma_f32_16x16x32_bf16 v[104:107], v[156:159], v[44:47], v[104:107]\n" \
"s_waitcnt lgkmcnt(6)\n" \
"ds_read_b128 v[124:127], v8 offset:12288\n" \
"v_mfma_f32_16x16x32_bf16 v[108:111], v[92:95], v[44:47], v[108:111]\n" \
"buffer_load_dwordx4 v[44:47], v3, s[36:39], s57 offen offset:3072 sc1\n" \
"v_mfma_f32_16x16x32_bf16 v[96:99], a[32:35], v[48:51], v[96:99]\n" \
"v_mfma_f32_16x16x32_bf16 v[100:103], a[160:163], v[48:51], v[100:103]\n" \
"ds_read_b128 v[92:95], v2 offset:19456\n" \
"v_mfma_f32_16x16x32_bf16 v[104:107], v[160:163], v[48:51], v[104:107]\n" \
"s_waitcnt lgkmcnt(7)\n" \
"v_mfma_f32_16x16x32_bf16 v[108:111], v[80:83], v[48:51], v[108:111]\n" \
"s_add_u32 s57, s56, s92\n" \
"buffer_load_dwordx4 v[48:51], v3, s[36:39], s57 offen offset:0 sc1\n" \
"v_mfma_f32_16x16x32_bf16 v[96:99], a[36:39], v[52:55], v[96:99]\n" \
"s_waitcnt lgkmcnt(4)\n" \
"v_add_f32 v112, v112, v116\n" \
"v_add_f32 v113, v113, v117\n" \
"v_mfma_f32_16x16x32_bf16 v[100:103], a[164:167], v[52:55], v[100:103]\n" \
"ds_read_b128 v[80:83], v2 offset:20480\n" \
"v_add_f32_e64 v114, v114, v118\n" \
"v_mfma_f32_16x16x32_bf16 v[104:107], v[164:167], v[52:55], v[104:107]\n" \
"v_add_f32_e64 v115, v115, v119\n" \
"v_mfma_f32_16x16x32_bf16 v[108:111], v[84:87], v[52:55], v[108:111]\n" \
"buffer_load_dwordx4 v[52:55], v3, s[36:39], s57 offen offset:1024 sc1\n" \
"s_waitcnt lgkmcnt(2)\n" \
"v_add_f32 v120, v120, v124\n" \
"v_mfma_f32_16x16x32_bf16 v[96:99], a[40:43], v[56:59], v[96:99]\n" \
"v_add_f32 v121, v121, v125\n" \
"v_add_f32 v122, v122, v126\n" \
"v_mfma_f32_16x16x32_bf16 v[100:103], a[168:171], v[56:59], v[100:103]\n" \
"ds_read_b128 v[84:87], v2 offset:21504\n" \
"v_add_f32_e64 v123, v123, v127\n" \
"v_mfma_f32_16x16x32_bf16 v[104:107], v[168:171], v[56:59], v[104:107]\n" \
"v_add_f32_e64 v112, v112, v120\n" \
"v_mfma_f32_16x16x32_bf16 v[108:111], v[88:91], v[56:59], v[108:111]\n" \
"buffer_load_dwordx4 v[56:59], v3, s[36:39], s57 offen offset:2048 sc1\n" \
"v_add_f32_e64 v113, v113, v121\n" \
"v_mfma_f32_16x16x32_bf16 v[96:99], a[44:47], v[60:63], v[96:99]\n" \
"v_add_f32 v114, v114, v122\n" \
"v_add_f32 v115, v115, v123\n" \
"v_mfma_f32_16x16x32_bf16 v[100:103], a[172:175], v[60:63], v[100:103]\n" \
"ds_read_b128 v[88:91], v2 offset:22528\n" \
"v_xor_b32_e64 v8, s84, v8\n" \
"v_mfma_f32_16x16x32_bf16 v[104:107], v[172:175], v[60:63], v[104:107]\n" \
"s_waitcnt lgkmcnt(3)\n" \
"v_cvt_pk_bf16_f32 v0, v112, v113\n" \
"v_mfma_f32_16x16x32_bf16 v[108:111], v[92:95], v[60:63], v[108:111]\n" \
"buffer_load_dwordx4 v[60:63], v3, s[36:39], s57 offen offset:3072 sc1\n" \
"v_cvt_pk_bf16_f32 v1, v114, v115\n" \
"v_mfma_f32_16x16x32_bf16 v[96:99], a[48:51], v[64:67], v[96:99]\n" \
"s_lshl_b32 s97, s44, 2\n" \
"s_add_u32 s97, s97, s49\n" \
"v_mfma_f32_16x16x32_bf16 v[100:103], a[176:179], v[64:67], v[100:103]\n" \
"ds_read_b128 v[92:95], v2 offset:23552\n" \
"buffer_store_dwordx2 v[0:1], v6, s[36:39], s76 offen sc1\n" \
"v_lshlrev_b32 v112, 16, v0\n" \
"v_mfma_f32_16x16x32_bf16 v[104:107], v[176:179], v[64:67], v[104:107]\n" \
"s_waitcnt lgkmcnt(3)\n" \
"v_and_b32 v113, 0xffff0000, v0\n" \
"v_mfma_f32_16x16x32_bf16 v[108:111], v[80:83], v[64:67], v[108:111]\n" \
"s_add_u32 s57, s56, s93\n" \
"buffer_load_dwordx4 v[64:67], v3, s[36:39], s57 offen offset:0 sc1\n" \
"v_lshlrev_b32_e64 v114, 16, v1\n" \
"v_mfma_f32_16x16x32_bf16 v[96:99], a[52:55], v[68:71], v[96:99]\n" \
"v_cmp_gt_u32_e64 s[62:63], s59, v11\n" \
"v_and_b32 v115, 0xffff0000, v1\n" \
"v_mfma_f32_16x16x32_bf16 v[100:103], a[180:183], v[68:71], v[100:103]\n" \
"ds_read_b128 v[80:83], v2 offset:24576\n" \
"v_mfma_f32_16x16x32_bf16 v[104:107], v[180:183], v[68:71], v[104:107]\n" \
"s_waitcnt vmcnt(17) lgkmcnt(3)\n" \
"v_mfma_f32_16x16x32_bf16 v[108:111], v[84:87], v[68:71], v[108:111]\n" \
"buffer_load_dwordx4 v[68:71], v3, s[36:39], s57 offen offset:1024 sc1\n" \
"v_mfma_f32_16x16x32_bf16 v[96:99], a[56:59], v[72:75], v[96:99]\n" \
"s_add_u32 s54, s55, 1\n" \
"s_cmp_lt_u32 s54, s46\n" \
"v_mfma_f32_16x16x32_bf16 v[100:103], a[184:187], v[72:75], v[100:103]\n" \
"ds_read_b128 v[84:87], v2 offset:25600\n" \
"s_cselect_b64 s[62:63], s[62:63], -1\n" \
"v_mfma_f32_16x16x32_bf16 v[104:107], v[184:187], v[72:75], v[104:107]\n" \
"s_waitcnt vmcnt(17) lgkmcnt(3)\n" \
"s_mul_i32 s83, s82, 4096\n" \
"v_mfma_f32_16x16x32_bf16 v[108:111], v[88:91], v[72:75], v[108:111]\n" \
"buffer_load_dwordx4 v[72:75], v3, s[36:39], s57 offen offset:2048 sc1\n" \
"v_mfma_f32_16x16x32_bf16 v[96:99], a[60:63], v[76:79], v[96:99]\n" \
"s_cmp_lg_u64 s[62:63], 0\n" \
"s_cselect_b32 s81, 1, 0\n" \
"v_mfma_f32_16x16x32_bf16 v[100:103], a[188:191], v[76:79], v[100:103]\n" \
"ds_read_b128 v[88:91], v2 offset:26624\n" \
"s_cselect_b32 s83, 0x70000000, s83\n" \
"v_mfma_f32_16x16x32_bf16 v[104:107], v[188:191], v[76:79], v[104:107]\n" \
"s_waitcnt vmcnt(17) lgkmcnt(3)\n" \
"v_mfma_f32_16x16x32_bf16 v[108:111], v[92:95], v[76:79], v[108:111]\n" \
"buffer_load_dwordx4 v[76:79], v3, s[36:39], s57 offen offset:3072 sc1\n" \
"v_mfma_f32_16x16x32_bf16 v[96:99], a[64:67], v[16:19], v[96:99]\n" \
"v_mfma_f32_16x16x32_bf16 v[100:103], a[192:195], v[16:19], v[100:103]\n" \
"ds_read_b128 v[92:95], v2 offset:27648\n" \
"v_mfma_f32_16x16x32_bf16 v[104:107], v[192:195], v[16:19], v[104:107]\n" \
"s_waitcnt vmcnt(17) lgkmcnt(3)\n" \
"s_mov_b32 s56, s83\n" \
"v_mfma_f32_16x16x32_bf16 v[108:111], v[80:83], v[16:19], v[108:111]\n" \
"s_add_u32 s57, s56, s86\n" \
"buffer_load_dwordx4 v[16:19], v3, s[36:39], s57 offen offset:0 sc1\n" \
"v_mfma_f32_16x16x32_bf16 v[96:99], a[68:71], v[20:23], v[96:99]\n" \
"buffer_load_dword v9, off, s[36:39], s97 sc1\n" \
"v_mfma_f32_16x16x32_bf16 v[100:103], a[196:199], v[20:23], v[100:103]\n" \
"ds_read_b128 v[80:83], v2 offset:28672\n" \
"v_mfma_f32_16x16x32_bf16 v[104:107], v[196:199], v[20:23], v[104:107]\n" \
"s_waitcnt vmcnt(18) lgkmcnt(3)\n" \
"v_mfma_f32_16x16x32_bf16 v[108:111], v[84:87], v[20:23], v[108:111]\n" \
"buffer_load_dwordx4 v[20:23], v3, s[36:39], s57 offen offset:1024 sc1\n" \
"v_mfma_f32_16x16x32_bf16 v[96:99], a[72:75], v[24:27], v[96:99]\n" \
"v_mfma_f32_16x16x32_bf16 v[100:103], a[200:203], v[24:27], v[100:103]\n" \
"ds_read_b128 v[84:87], v2 offset:29696\n" \
"v_mfma_f32_16x16x32_bf16 v[104:107], v[200:203], v[24:27], v[104:107]\n" \
"s_waitcnt vmcnt(18) lgkmcnt(3)\n" \
"v_mfma_f32_16x16x32_bf16 v[108:111], v[88:91], v[24:27], v[108:111]\n" \
"buffer_load_dwordx4 v[24:27], v3, s[36:39], s57 offen offset:2048 sc1\n" \
"v_mfma_f32_16x16x32_bf16 v[96:99], a[76:79], v[28:31], v[96:99]\n" \
"v_mfma_f32_16x16x32_bf16 v[100:103], a[204:207], v[28:31], v[100:103]\n" \
"ds_read_b128 v[88:91], v2 offset:30720\n" \
"v_mfma_f32_16x16x32_bf16 v[104:107], v[204:207], v[28:31], v[104:107]\n" \
"s_waitcnt vmcnt(18) lgkmcnt(3)\n" \
"v_mfma_f32_16x16x32_bf16 v[108:111], v[92:95], v[28:31], v[108:111]\n" \
"buffer_load_dwordx4 v[28:31], v3, s[36:39], s57 offen offset:3072 sc1\n" \
"v_mfma_f32_16x16x32_bf16 v[96:99], a[80:83], v[32:35], v[96:99]\n" \
"s_waitcnt vmcnt(17)\n" \
"v_lshlrev_b32 v120, 16, v14\n" \
"v_and_b32 v121, 0xffff0000, v14\n" \
"v_mfma_f32_16x16x32_bf16 v[100:103], a[208:211], v[32:35], v[100:103]\n" \
"ds_read_b128 v[92:95], v2 offset:31744\n" \
"s_waitcnt vmcnt(16)\n" \
"v_lshlrev_b32 v124, 16, v12\n" \
"v_mfma_f32_16x16x32_bf16 v[104:107], v[208:211], v[32:35], v[104:107]\n" \
"s_waitcnt lgkmcnt(3)\n" \
"v_and_b32 v125, 0xffff0000, v12\n" \
"v_mfma_f32_16x16x32_bf16 v[108:111], v[80:83], v[32:35], v[108:111]\n" \
"s_add_u32 s57, s56, s87\n" \
"buffer_load_dwordx4 v[32:35], v3, s[36:39], s57 offen offset:0 sc1\n" \
"v_lshlrev_b32_e64 v122, 16, v15\n" \
"v_mfma_f32_16x16x32_bf16 v[96:99], a[84:87], v[36:39], v[96:99]\n" \
"v_and_b32 v123, 0xffff0000, v15\n" \
"v_lshlrev_b32_e64 v126, 16, v13\n" \
"v_mfma_f32_16x16x32_bf16 v[100:103], a[212:215], v[36:39], v[100:103]\n" \
"ds_read_b128 v[80:83], v2 offset:0\n" \
"v_and_b32 v127, 0xffff0000, v13\n" \
"v_mfma_f32_16x16x32_bf16 v[104:107], v[212:215], v[36:39], v[104:107]\n" \
"s_waitcnt vmcnt(16) lgkmcnt(3)\n" \
"v_fmac_f32 v124, v112, v120\n" \
"v_mfma_f32_16x16x32_bf16 v[108:111], v[84:87], v[36:39], v[108:111]\n" \
"buffer_load_dwordx4 v[36:39], v3, s[36:39], s57 offen offset:1024 sc1\n" \
"v_fmac_f32_e64 v125, v113, v121\n" \
"v_mfma_f32_16x16x32_bf16 v[96:99], a[88:91], v[40:43], v[96:99]\n" \
"v_fmac_f32 v126, v114, v122\n" \
"v_fmac_f32 v127, v115, v123\n" \
"v_mfma_f32_16x16x32_bf16 v[100:103], a[216:219], v[40:43], v[100:103]\n" \
"ds_read_b128 v[84:87], v2 offset:1024\n" \
"v_cvt_pk_bf16_f32 v118, v124, v125\n" \
"v_mfma_f32_16x16x32_bf16 v[104:107], v[216:219], v[40:43], v[104:107]\n" \
"s_waitcnt vmcnt(16) lgkmcnt(3)\n" \
"v_cvt_pk_bf16_f32 v119, v126, v127\n" \
"v_mfma_f32_16x16x32_bf16 v[108:111], v[88:91], v[40:43], v[108:111]\n" \
"buffer_load_dwordx4 v[40:43], v3, s[36:39], s57 offen offset:2048 sc1\n" \
"v_mfma_f32_16x16x32_bf16 v[96:99], a[92:95], v[44:47], v[96:99]\n" \
"buffer_store_dwordx2 v[118:119], v5, s[68:71], s85 offen\n" \
"v_mov_b32 v10, 1\n" \
"v_mfma_f32_16x16x32_bf16 v[100:103], a[220:223], v[44:47], v[100:103]\n" \
"ds_read_b128 v[88:91], v2 offset:2048\n" \
"v_mfma_f32_16x16x32_bf16 v[104:107], v[220:223], v[44:47], v[104:107]\n" \
"s_waitcnt vmcnt(17) lgkmcnt(3)\n" \
"v_mfma_f32_16x16x32_bf16 v[108:111], v[92:95], v[44:47], v[108:111]\n" \
"buffer_load_dwordx4 v[44:47], v3, s[36:39], s57 offen offset:3072 sc1\n" \
"v_mfma_f32_16x16x32_bf16 v[96:99], a[96:99], v[48:51], v[96:99]\n" \
"v_mfma_f32_16x16x32_bf16 v[100:103], a[224:227], v[48:51], v[100:103]\n" \
"ds_read_b128 v[92:95], v2 offset:3072\n" \
"v_mfma_f32_16x16x32_bf16 v[104:107], v[224:227], v[48:51], v[104:107]\n" \
"s_waitcnt vmcnt(17) lgkmcnt(3)\n" \
"v_mfma_f32_16x16x32_bf16 v[108:111], v[80:83], v[48:51], v[108:111]\n" \
"s_add_u32 s57, s56, s88\n" \
"buffer_load_dwordx4 v[48:51], v3, s[36:39], s57 offen offset:0 sc1\n" \
"v_mfma_f32_16x16x32_bf16 v[96:99], a[100:103], v[52:55], v[96:99]\n" \
"v_mfma_f32_16x16x32_bf16 v[100:103], a[228:231], v[52:55], v[100:103]\n" \
"ds_read_b128 v[80:83], v2 offset:4096\n" \
"v_mfma_f32_16x16x32_bf16 v[104:107], v[228:231], v[52:55], v[104:107]\n" \
"s_waitcnt vmcnt(17) lgkmcnt(3)\n" \
"v_mfma_f32_16x16x32_bf16 v[108:111], v[84:87], v[52:55], v[108:111]\n" \
"buffer_load_dwordx4 v[52:55], v3, s[36:39], s57 offen offset:1024 sc1\n" \
"v_mfma_f32_16x16x32_bf16 v[96:99], a[104:107], v[56:59], v[96:99]\n" \
"v_mfma_f32_16x16x32_bf16 v[100:103], a[232:235], v[56:59], v[100:103]\n" \
"ds_read_b128 v[84:87], v2 offset:5120\n" \
"v_mfma_f32_16x16x32_bf16 v[104:107], v[232:235], v[56:59], v[104:107]\n" \
"s_waitcnt vmcnt(17) lgkmcnt(3)\n" \
"v_mfma_f32_16x16x32_bf16 v[108:111], v[88:91], v[56:59], v[108:111]\n" \
"buffer_load_dwordx4 v[56:59], v3, s[36:39], s57 offen offset:2048 sc1\n" \
"v_mfma_f32_16x16x32_bf16 v[96:99], a[108:111], v[60:63], v[96:99]\n" \
"v_mfma_f32_16x16x32_bf16 v[100:103], a[236:239], v[60:63], v[100:103]\n" \
"ds_read_b128 v[88:91], v2 offset:6144\n" \
"v_mfma_f32_16x16x32_bf16 v[104:107], v[236:239], v[60:63], v[104:107]\n" \
"s_waitcnt vmcnt(16) lgkmcnt(3)\n" \
"v_mfma_f32_16x16x32_bf16 v[108:111], v[92:95], v[60:63], v[108:111]\n" \
"buffer_load_dwordx4 v[60:63], v3, s[36:39], s57 offen offset:3072 sc1\n" \
"v_mfma_f32_16x16x32_bf16 v[96:99], a[112:115], v[64:67], v[96:99]\n" \
"v_mfma_f32_16x16x32_bf16 v[100:103], a[240:243], v[64:67], v[100:103]\n" \
"ds_read_b128 v[92:95], v2 offset:7168\n" \
"v_mfma_f32_16x16x32_bf16 v[104:107], v[240:243], v[64:67], v[104:107]\n" \
"s_waitcnt vmcnt(16) lgkmcnt(3)\n" \
"s_mov_b32 s84, 0x4000\n" \
"v_mfma_f32_16x16x32_bf16 v[108:111], v[80:83], v[64:67], v[108:111]\n" \
"s_add_u32 s57, s56, s89\n" \
"buffer_load_dwordx4 v[64:67], v3, s[36:39], s57 offen offset:0 sc1\n" \
"v_mfma_f32_16x16x32_bf16 v[96:99], a[116:119], v[68:71], v[96:99]\n" \
"buffer_load_dword v11, v4, s[36:39], 0 offen sc1\n" \
"v_mfma_f32_16x16x32_bf16 v[100:103], a[244:247], v[68:71], v[100:103]\n" \
"ds_read_b128 v[80:83], v2 offset:8192\n" \
"v_mfma_f32_16x16x32_bf16 v[104:107], v[244:247], v[68:71], v[104:107]\n" \
"s_waitcnt vmcnt(17) lgkmcnt(3)\n" \
"v_mfma_f32_16x16x32_bf16 v[108:111], v[84:87], v[68:71], v[108:111]\n" \
"buffer_load_dwordx4 v[68:71], v3, s[36:39], s57 offen offset:1024 sc1\n" \
"v_mfma_f32_16x16x32_bf16 v[96:99], a[120:123], v[72:75], v[96:99]\n" \
"s_mov_b32 s99, s98\n" \
"s_mov_b32 s98, 0\n" \
"v_mfma_f32_16x16x32_bf16 v[100:103], a[248:251], v[72:75], v[100:103]\n" \
"ds_read_b128 v[84:87], v2 offset:9216\n" \
"v_mfma_f32_16x16x32_bf16 v[104:107], v[248:251], v[72:75], v[104:107]\n" \
"s_waitcnt vmcnt(17) lgkmcnt(3)\n" \
"s_mov_b64 exec, 1\n" \
"buffer_atomic_or v10, off, s[36:39], s94 sc0\n" \
"s_mov_b64 exec, -1\n" \
"s_mov_b32 s95, s78\n" \
"v_mfma_f32_16x16x32_bf16 v[108:111], v[88:91], v[72:75], v[108:111]\n" \
"buffer_load_dwordx4 v[72:75], v3, s[36:39], s57 offen offset:2048 sc1\n" \
"v_mfma_f32_16x16x32_bf16 v[96:99], a[124:127], v[76:79], v[96:99]\n" \
"s_lshl_b32 s78, s44, 15\n" \
"s_mov_b32 s79, s97\n" \
"v_mfma_f32_16x16x32_bf16 v[100:103], a[252:255], v[76:79], v[100:103]\n" \
"ds_read_b128 v[88:91], v2 offset:10240\n" \
"s_mov_b32 s44, s82\n" \
"s_add_u32 s55, s55, 1\n" \
"v_mfma_f32_16x16x32_bf16 v[104:107], v[252:255], v[76:79], v[104:107]\n" \
"s_waitcnt vmcnt(18) lgkmcnt(3)\n" \
"ds_write_b128 v7, v[96:99] offset:0\n" \
"v_xor_b32_e64 v7, 64, v7\n" \
"v_mfma_f32_16x16x32_bf16 v[108:111], v[92:95], v[76:79], v[108:111]\n" \
"buffer_load_dwordx4 v[76:79], v3, s[36:39], s57 offen offset:3072 sc1\n" \
"ds_write_b128 v7, v[100:103] offset:1024\n" \
"v_xor_b32 v7, 192, v7\n" \
"s_cmp_eq_u32 s81, 0\n" \
"s_nop 0\n" \
"s_cbranch_scc1 .Lswo_loop_%=\n" \
"s_nop 7\n" \
"s_nop 7\n" \
"s_waitcnt vmcnt(0) lgkmcnt(0)\n" \
"ds_write_b128 v7, v[104:107] offset:2048\n" \
"v_xor_b32 v7, 64, v7\n" \
"ds_write_b128 v7, v[108:111] offset:3072\n" \
"v_xor_b32 v7, 64, v7\n" \
"s_waitcnt lgkmcnt(0)\n" \
"s_barrier\n" \
"s_cmp_eq_u32 s99, 0\n" \
"s_cselect_b32 s54, 0, s96\n" \
"s_mov_b32 s85, s77\n" \
"s_cmp_lg_u32 s54, 0\n" \
"s_cbranch_scc1 .Lswo_f_%=\n" \
"buffer_load_dword v9, off, s[36:39], s97 sc1\n" \
"v_readfirstlane_b32 s54, v10\n" \
"s_nop 0\n" \
"s_and_b32 s54, s54, 2\n" \
"s_cbranch_scc1 .Lswo_q_%=\n" \
".Lswo_qk_%=:\n" \
"s_waitcnt vmcnt(0)\n" \
"v_readfirstlane_b32 s96, v9\n" \
"s_add_u32 s76, s78, 0\n" \
"s_add_u32 s80, s78, 4194304\n" \
"s_and_b32 s96, s96, 2\n" \
"s_cselect_b32 s76, 0x7ffffff0, s76\n" \
"s_cselect_b32 s77, s78, 0x7ffffff0\n" \
"s_cselect_b32 s80, s80, 0x7ffffff0\n" \
"s_cselect_b32 s94, 0x7ffffff0, s79\n" \
"s_or_b32 s76, s76, s98\n" \
"s_or_b32 s94, s94, s98\n" \
"s_or_b32 s85, s77, s98\n" \
"buffer_load_dwordx2 v[14:15], v6, s[36:39], s80 offen sc1\n" \
"buffer_load_dwordx2 v[12:13], v5, s[64:67], s77 offen\n" \
".Lswo_f_%=:\n" \
"ds_read_b128 v[112:115], v8 offset:0\n" \
"ds_read_b128 v[116:119], v8 offset:4096\n" \
"ds_read_b128 v[120:123], v8 offset:8192\n" \
"ds_read_b128 v[124:127], v8 offset:12288\n" \
"s_waitcnt lgkmcnt(2)\n" \
"v_add_f32 v112, v112, v116\n" \
"v_add_f32 v113, v113, v117\n" \
"v_add_f32 v114, v114, v118\n" \
"v_add_f32 v115, v115, v119\n" \
"s_waitcnt lgkmcnt(0)\n" \
"v_add_f32 v120, v120, v124\n" \
"v_add_f32 v121, v121, v125\n" \
"v_add_f32 v122, v122, v126\n" \
"v_add_f32 v123, v123, v127\n" \
"v_add_f32 v112, v112, v120\n" \
"v_add_f32 v113, v113, v121\n" \
"v_add_f32 v114, v114, v122\n" \
"v_add_f32 v115, v115, v123\n" \
"v_xor_b32 v8, s84, v8\n" \
"v_cvt_pk_bf16_f32 v0, v112, v113\n" \
"v_cvt_pk_bf16_f32 v1, v114, v115\n" \
"buffer_store_dwordx2 v[0:1], v6, s[36:39], s76 offen sc1\n" \
"v_lshlrev_b32 v112, 16, v0\n" \
"v_and_b32 v113, 0xffff0000, v0\n" \
"v_lshlrev_b32 v114, 16, v1\n" \
"v_and_b32 v115, 0xffff0000, v1\n" \
"s_waitcnt vmcnt(2)\n" \
"v_lshlrev_b32 v120, 16, v14\n" \
"v_and_b32 v121, 0xffff0000, v14\n" \
"s_waitcnt vmcnt(1)\n" \
"v_lshlrev_b32 v124, 16, v12\n" \
"v_and_b32 v125, 0xffff0000, v12\n" \
"v_lshlrev_b32 v122, 16, v15\n" \
"v_and_b32 v123, 0xffff0000, v15\n" \
"v_lshlrev_b32 v126, 16, v13\n" \
"v_and_b32 v127, 0xffff0000, v13\n" \
"v_fmac_f32 v124, v112, v120\n" \
"v_fmac_f32 v125, v113, v121\n" \
"v_fmac_f32 v126, v114, v122\n" \
"v_fmac_f32 v127, v115, v123\n" \
"v_cvt_pk_bf16_f32 v118, v124, v125\n" \
"v_cvt_pk_bf16_f32 v119, v126, v127\n" \
"buffer_store_dwordx2 v[118:119], v5, s[68:71], s85 offen\n" \
"v_mov_b32 v10, 1\n" \
"v_xor_b32 v7, 0x4000, v7\n" \
"s_cmp_lg_u32 s96, 0\n" \
"s_cbranch_scc1 .Lswo_g_%=\n" \
"s_waitcnt vmcnt(1)\n" \
"s_mov_b64 exec, 1\n" \
"buffer_atomic_or v10, off, s[36:39], s94 sc0\n" \
"s_mov_b64 exec, -1\n" \
"s_mov_b32 s95, s78\n" \
"s_waitcnt vmcnt(0)\n" \
"v_readfirstlane_b32 s54, v10\n" \
"s_nop 0\n" \
"s_and_b32 s54, s54, 2\n" \
"s_cbranch_scc1 .Lswo_r_%=\n" \
".Lswo_rk_%=:\n" \
".Lswo_g_%=:\n" \
"s_cmp_lt_u32 s55, s46\n" \
"s_cbranch_scc1 .Lswo_prime_%=\n" \
"s_branch .Lswo_end_%=\n" \
".Lswo_w_%=:\n" \
"s_nop 7\n" \
"s_nop 7\n" \
"s_waitcnt vmcnt(0) lgkmcnt(0)\n" \
"s_mov_b32 s61, 0\n" \
".Lswo_wa_%=:\n" \
"s_cmp_lg_u32 s60, 0\n" \
"s_cbranch_scc1 .Lswo_wk_%=\n" \
"buffer_load_dword v9, off, s[36:39], s97 sc1\n" \
"buffer_load_dword v11, v4, s[36:39], 0 offen sc1\n" \
"s_waitcnt vmcnt(0)\n" \
"v_cmp_gt_u32_e64 s[62:63], s59, v11\n" \
"s_nop 4\n" \
"s_cmp_eq_u64 s[62:63], 0\n" \
"s_cbranch_scc1 .Lswo_wk_%=\n" \
"s_sleep 1\n" \
"s_add_u32 s61, s61, 1\n" \
"s_cmp_lt_u32 s61, 65536\n" \
"s_cbranch_scc1 .Lswo_wa_%=\n" \
"s_mov_b32 s60, 1\n" \
"v_mov_b32 v11, 256\n" \
"v_mov_b32 v12, 1\n" \
"buffer_store_dword v12, v11, s[36:39], 0 offen sc1\n" \
"s_waitcnt vmcnt(0) lgkmcnt(0)\n" \
"s_branch .Lswo_wk_%=\n" \
".Lswo_p_%=:\n" \
"s_nop 7\n" \
"s_nop 7\n" \
"s_waitcnt vmcnt(0) lgkmcnt(0)\n" \
"s_add_u32 s54, s95, 4194304\n" \
"buffer_load_dwordx2 v[14:15], v6, s[36:39], s54 offen sc1\n" \
"buffer_load_dwordx2 v[12:13], v5, s[64:67], s95 offen\n" \
"v_lshlrev_b32 v112, 16, v0\n" \
"v_and_b32 v113, 0xffff0000, v0\n" \
"v_lshlrev_b32 v114, 16, v1\n" \
"v_and_b32 v115, 0xffff0000, v1\n" \
"s_waitcnt vmcnt(1)\n" \
"v_lshlrev_b32 v120, 16, v14\n" \
"v_and_b32 v121, 0xffff0000, v14\n" \
"s_waitcnt vmcnt(0)\n" \
"v_lshlrev_b32 v124, 16, v12\n" \
"v_and_b32 v125, 0xffff0000, v12\n" \
"v_lshlrev_b32 v122, 16, v15\n" \
"v_and_b32 v123, 0xffff0000, v15\n" \
"v_lshlrev_b32 v126, 16, v13\n" \
"v_and_b32 v127, 0xffff0000, v13\n" \
"v_fmac_f32 v124, v112, v120\n" \
"v_fmac_f32 v125, v113, v121\n" \
"v_fmac_f32 v126, v114, v122\n" \
"v_fmac_f32 v127, v115, v123\n" \
"v_cvt_pk_bf16_f32 v118, v124, v125\n" \
"v_cvt_pk_bf16_f32 v119, v126, v127\n" \
"buffer_store_dwordx2 v[118:119], v5, s[68:71], s95 offen\n" \
"s_waitcnt vmcnt(0) lgkmcnt(0)\n" \
"s_branch .Lswo_pk_%=\n" \
".Lswo_q_%=:\n" \
"s_nop 7\n" \
"s_nop 7\n" \
"s_waitcnt vmcnt(0) lgkmcnt(0)\n" \
"s_add_u32 s54, s95, 4194304\n" \
"buffer_load_dwordx2 v[14:15], v6, s[36:39], s54 offen sc1\n" \
"buffer_load_dwordx2 v[12:13], v5, s[64:67], s95 offen\n" \
"v_lshlrev_b32 v112, 16, v0\n" \
"v_and_b32 v113, 0xffff0000, v0\n" \
"v_lshlrev_b32 v114, 16, v1\n" \
"v_and_b32 v115, 0xffff0000, v1\n" \
"s_waitcnt vmcnt(1)\n" \
"v_lshlrev_b32 v120, 16, v14\n" \
"v_and_b32 v121, 0xffff0000, v14\n" \
"s_waitcnt vmcnt(0)\n" \
"v_lshlrev_b32 v124, 16, v12\n" \
"v_and_b32 v125, 0xffff0000, v12\n" \
"v_lshlrev_b32 v122, 16, v15\n" \
"v_and_b32 v123, 0xffff0000, v15\n" \
"v_lshlrev_b32 v126, 16, v13\n" \
"v_and_b32 v127, 0xffff0000, v13\n" \
"v_fmac_f32 v124, v112, v120\n" \
"v_fmac_f32 v125, v113, v121\n" \
"v_fmac_f32 v126, v114, v122\n" \
"v_fmac_f32 v127, v115, v123\n" \
"v_cvt_pk_bf16_f32 v118, v124, v125\n" \
"v_cvt_pk_bf16_f32 v119, v126, v127\n" \
"buffer_store_dwordx2 v[118:119], v5, s[68:71], s95 offen\n" \
"s_waitcnt vmcnt(0) lgkmcnt(0)\n" \
"s_branch .Lswo_qk_%=\n" \
".Lswo_r_%=:\n" \
"s_nop 7\n" \
"s_nop 7\n" \
"s_waitcnt vmcnt(0) lgkmcnt(0)\n" \
"s_add_u32 s54, s95, 4194304\n" \
"buffer_load_dwordx2 v[14:15], v6, s[36:39], s54 offen sc1\n" \
"buffer_load_dwordx2 v[12:13], v5, s[64:67], s95 offen\n" \
"v_lshlrev_b32 v112, 16, v0\n" \
"v_and_b32 v113, 0xffff0000, v0\n" \
"v_lshlrev_b32 v114, 16, v1\n" \
"v_and_b32 v115, 0xffff0000, v1\n" \
"s_waitcnt vmcnt(1)\n" \
"v_lshlrev_b32 v120, 16, v14\n" \
"v_and_b32 v121, 0xffff0000, v14\n" \
"s_waitcnt vmcnt(0)\n" \
"v_lshlrev_b32 v124, 16, v12\n" \
"v_and_b32 v125, 0xffff0000, v12\n" \
"v_lshlrev_b32 v122, 16, v15\n" \
"v_and_b32 v123, 0xffff0000, v15\n" \
"v_lshlrev_b32 v126, 16, v13\n" \
"v_and_b32 v127, 0xffff0000, v13\n" \
"v_fmac_f32 v124, v112, v120\n" \
"v_fmac_f32 v125, v113, v121\n" \
"v_fmac_f32 v126, v114, v122\n" \
"v_fmac_f32 v127, v115, v123\n" \
"v_cvt_pk_bf16_f32 v118, v124, v125\n" \
"v_cvt_pk_bf16_f32 v119, v126, v127\n" \
"buffer_store_dwordx2 v[118:119], v5, s[68:71], s95 offen\n" \
"s_waitcnt vmcnt(0) lgkmcnt(0)\n" \
"s_branch .Lswo_rk_%=\n" \
".Lswo_end_%=:\n" \
"s_endpgm\n"
#define ASM_S64_G2 \
"s_nop 7\n" \
"s_nop 7\n" \
"s_lshl_b32 s52, s48, 15\n" \
"s_lshl_b32 s53, s48, 13\n" \
"s_add_u32 s53, s53, 98304\n" \
"s_sub_u32 s54, s53, s52\n" \
"v_add_u32 v9, s54, v1\n" \
"s_mov_b32 s54, s85\n" \
"s_mul_i32 s86, s54, 278528\n" \
"s_add_u32 s54, s54, 1\n" \
"s_cmp_eq_u32 s54, 7\n" \
"s_cselect_b32 s54, 0, s54\n" \
"s_mul_i32 s87, s54, 278528\n" \
"s_add_u32 s54, s54, 1\n" \
"s_cmp_eq_u32 s54, 7\n" \
"s_cselect_b32 s54, 0, s54\n" \
"s_mul_i32 s88, s54, 278528\n" \
"s_add_u32 s54, s54, 1\n" \
"s_cmp_eq_u32 s54, 7\n" \
"s_cselect_b32 s54, 0, s54\n" \
"s_mul_i32 s89, s54, 278528\n" \
"s_add_u32 s54, s54, 1\n" \
"s_cmp_eq_u32 s54, 7\n" \
"s_cselect_b32 s54, 0, s54\n" \
"s_mul_i32 s90, s54, 278528\n" \
"s_add_u32 s54, s54, 1\n" \
"s_cmp_eq_u32 s54, 7\n" \
"s_cselect_b32 s54, 0, s54\n" \
"s_mul_i32 s91, s54, 278528\n" \
"s_add_u32 s54, s54, 1\n" \
"s_cmp_eq_u32 s54, 7\n" \
"s_cselect_b32 s54, 0, s54\n" \
"s_mul_i32 s92, s54, 278528\n" \
"s_add_u32 s54, s54, 1\n" \
"s_cmp_eq_u32 s54, 7\n" \
"s_cselect_b32 s54, 0, s54\n" \
"s_add_u32 m0, s52, 0\n" \
"s_add_u32 s54, s86, 0\n" \
"buffer_load_dwordx4 v0, s[40:43], s54 offen sc1 lds\n" \
"s_add_u32 m0, s52, 1024\n" \
"s_add_u32 s54, s86, 17408\n" \
"buffer_load_dwordx4 v0, s[40:43], s54 offen sc1 lds\n" \
"s_add_u32 m0, s52, 2048\n" \
"s_add_u32 s54, s86, 34816\n" \
"buffer_load_dwordx4 v0, s[40:43], s54 offen sc1 lds\n" \
"s_add_u32 m0, s52, 3072\n" \
"s_add_u32 s54, s86, 52224\n" \
"buffer_load_dwordx4 v0, s[40:43], s54 offen sc1 lds\n" \
"s_mov_b32 s54, s99\n" \
".Lsg2_z0_%=:\n" \
"s_sleep 1\n" \
"s_sub_u32 s54, s54, 1\n" \
"s_cbranch_scc0 .Lsg2_z0_%=\n" \
"s_add_u32 m0, s52, 4096\n" \
"s_add_u32 s54, s86, 69632\n" \
"buffer_load_dwordx4 v0, s[40:43], s54 offen sc1 lds\n" \
"s_add_u32 m0, s52, 5120\n" \
"s_add_u32 s54, s86, 87040\n" \
"buffer_load_dwordx4 v0, s[40:43], s54 offen sc1 lds\n" \
"s_add_u32 m0, s52, 6144\n" \
"s_add_u32 s54, s86, 104448\n" \
"buffer_load_dwordx4 v0, s[40:43], s54 offen sc1 lds\n" \
"s_add_u32 m0, s52, 7168\n" \
"s_add_u32 s54, s86, 121856\n" \
"buffer_load_dwordx4 v0, s[40:43], s54 offen sc1 lds\n" \
"s_mov_b32 s54, s99\n" \
".Lsg2_z1_%=:\n" \
"s_sleep 1\n" \
"s_sub_u32 s54, s54, 1\n" \
"s_cbranch_scc0 .Lsg2_z1_%=\n" \
"s_add_u32 m0, s52, 8192\n" \
"s_add_u32 s54, s86, 139264\n" \
"buffer_load_dwordx4 v0, s[40:43], s54 offen sc1 lds\n" \
"s_add_u32 m0, s52, 9216\n" \
"s_add_u32 s54, s86, 156672\n" \
"buffer_load_dwordx4 v0, s[40:43], s54 offen sc1 lds\n" \
"s_add_u32 m0, s52, 10240\n" \
"s_add_u32 s54, s86, 174080\n" \
"buffer_load_dwordx4 v0, s[40:43], s54 offen sc1 lds\n" \
"s_add_u32 m0, s52, 11264\n" \
"s_add_u32 s54, s86, 191488\n" \
"buffer_load_dwordx4 v0, s[40:43], s54 offen sc1 lds\n" \
"s_mov_b32 s54, s99\n" \
".Lsg2_z2_%=:\n" \
"s_sleep 1\n" \
"s_sub_u32 s54, s54, 1\n" \
"s_cbranch_scc0 .Lsg2_z2_%=\n" \
"s_add_u32 m0, s52, 12288\n" \
"s_add_u32 s54, s86, 208896\n" \
"buffer_load_dwordx4 v0, s[40:43], s54 offen sc1 lds\n" \
"s_add_u32 m0, s52, 13312\n" \
"s_add_u32 s54, s86, 226304\n" \
"buffer_load_dwordx4 v0, s[40:43], s54 offen sc1 lds\n" \
"s_add_u32 m0, s52, 14336\n" \
"s_add_u32 s54, s86, 243712\n" \
"buffer_load_dwordx4 v0, s[40:43], s54 offen sc1 lds\n" \
"s_add_u32 m0, s52, 15360\n" \
"s_add_u32 s54, s86, 261120\n" \
"buffer_load_dwordx4 v0, s[40:43], s54 offen sc1 lds\n" \
"s_mov_b32 s54, s99\n" \
".Lsg2_z3_%=:\n" \
"s_sleep 1\n" \
"s_sub_u32 s54, s54, 1\n" \
"s_cbranch_scc0 .Lsg2_z3_%=\n" \
"s_add_u32 m0, s52, 16384\n" \
"s_add_u32 s54, s87, 0\n" \
"buffer_load_dwordx4 v0, s[40:43], s54 offen sc1 lds\n" \
"s_add_u32 m0, s52, 17408\n" \
"s_add_u32 s54, s87, 17408\n" \
"buffer_load_dwordx4 v0, s[40:43], s54 offen sc1 lds\n" \
"s_add_u32 m0, s52, 18432\n" \
"s_add_u32 s54, s87, 34816\n" \
"buffer_load_dwordx4 v0, s[40:43], s54 offen sc1 lds\n" \
"s_add_u32 m0, s52, 19456\n" \
"s_add_u32 s54, s87, 52224\n" \
"buffer_load_dwordx4 v0, s[40:43], s54 offen sc1 lds\n" \
"s_mov_b32 s54, s99\n" \
".Lsg2_z4_%=:\n" \
"s_sleep 1\n" \
"s_sub_u32 s54, s54, 1\n" \
"s_cbranch_scc0 .Lsg2_z4_%=\n" \
"s_add_u32 m0, s52, 20480\n" \
"s_add_u32 s54, s87, 69632\n" \
"buffer_load_dwordx4 v0, s[40:43], s54 offen sc1 lds\n" \
"s_add_u32 m0, s52, 21504\n" \
"s_add_u32 s54, s87, 87040\n" \
"buffer_load_dwordx4 v0, s[40:43], s54 offen sc1 lds\n" \
"s_add_u32 m0, s52, 22528\n" \
"s_add_u32 s54, s87, 104448\n" \
"buffer_load_dwordx4 v0, s[40:43], s54 offen sc1 lds\n" \
"s_add_u32 m0, s52, 23552\n" \
"s_add_u32 s54, s87, 121856\n" \
"buffer_load_dwordx4 v0, s[40:43], s54 offen sc1 lds\n" \
"s_mov_b32 s54, s99\n" \
".Lsg2_z5_%=:\n" \
"s_sleep 1\n" \
"s_sub_u32 s54, s54, 1\n" \
"s_cbranch_scc0 .Lsg2_z5_%=\n" \
"s_add_u32 m0, s52, 24576\n" \
"s_add_u32 s54, s87, 139264\n" \
"buffer_load_dwordx4 v0, s[40:43], s54 offen sc1 lds\n" \
"s_add_u32 m0, s52, 25600\n" \
"s_add_u32 s54, s87, 156672\n" \
"buffer_load_dwordx4 v0, s[40:43], s54 offen sc1 lds\n" \
"s_add_u32 m0, s52, 26624\n" \
"s_add_u32 s54, s87, 174080\n" \
"buffer_load_dwordx4 v0, s[40:43], s54 offen sc1 lds\n" \
"s_add_u32 m0, s52, 27648\n" \
"s_add_u32 s54, s87, 191488\n" \
"buffer_load_dwordx4 v0, s[40:43], s54 offen sc1 lds\n" \
"s_mov_b32 s54, s99\n" \
".Lsg2_z6_%=:\n" \
"s_sleep 1\n" \
"s_sub_u32 s54, s54, 1\n" \
"s_cbranch_scc0 .Lsg2_z6_%=\n" \
"s_add_u32 m0, s52, 28672\n" \
"s_add_u32 s54, s87, 208896\n" \
"buffer_load_dwordx4 v0, s[40:43], s54 offen sc1 lds\n" \
"s_add_u32 m0, s52, 29696\n" \
"s_add_u32 s54, s87, 226304\n" \
"buffer_load_dwordx4 v0, s[40:43], s54 offen sc1 lds\n" \
"s_add_u32 m0, s52, 30720\n" \
"s_add_u32 s54, s87, 243712\n" \
"buffer_load_dwordx4 v0, s[40:43], s54 offen sc1 lds\n" \
"s_add_u32 m0, s52, 31744\n" \
"s_add_u32 s54, s87, 261120\n" \
"buffer_load_dwordx4 v0, s[40:43], s54 offen sc1 lds\n" \
"s_mov_b32 s54, s99\n" \
".Lsg2_z7_%=:\n" \
"s_sleep 1\n" \
"s_sub_u32 s54, s54, 1\n" \
"s_cbranch_scc0 .Lsg2_z7_%=\n" \
"s_add_u32 m0, s53, 32768\n" \
"s_add_u32 s54, s88, 0\n" \
"buffer_load_dwordx4 v0, s[40:43], s54 offen sc1 lds\n" \
"s_add_u32 m0, s53, 33792\n" \
"s_add_u32 s54, s88, 17408\n" \
"buffer_load_dwordx4 v0, s[40:43], s54 offen sc1 lds\n" \
"s_add_u32 m0, s53, 34816\n" \
"s_add_u32 s54, s88, 34816\n" \
"buffer_load_dwordx4 v0, s[40:43], s54 offen sc1 lds\n" \
"s_add_u32 m0, s53, 35840\n" \
"s_add_u32 s54, s88, 52224\n" \
"buffer_load_dwordx4 v0, s[40:43], s54 offen sc1 lds\n" \
"s_mov_b32 s54, s99\n" \
".Lsg2_z8_%=:\n" \
"s_sleep 1\n" \
"s_sub_u32 s54, s54, 1\n" \
"s_cbranch_scc0 .Lsg2_z8_%=\n" \
"s_add_u32 m0, s53, 36864\n" \
"s_add_u32 s54, s88, 69632\n" \
"buffer_load_dwordx4 v0, s[40:43], s54 offen sc1 lds\n" \
"s_add_u32 m0, s53, 37888\n" \
"s_add_u32 s54, s88, 87040\n" \
"buffer_load_dwordx4 v0, s[40:43], s54 offen sc1 lds\n" \
"s_add_u32 m0, s53, 38912\n" \
"s_add_u32 s54, s88, 104448\n" \
"buffer_load_dwordx4 v0, s[40:43], s54 offen sc1 lds\n" \
"s_add_u32 m0, s53, 39936\n" \
"s_add_u32 s54, s88, 121856\n" \
"buffer_load_dwordx4 v0, s[40:43], s54 offen sc1 lds\n" \
"s_mov_b32 s54, s99\n" \
".Lsg2_z9_%=:\n" \
"s_sleep 1\n" \
"s_sub_u32 s54, s54, 1\n" \
"s_cbranch_scc0 .Lsg2_z9_%=\n" \
"s_waitcnt vmcnt(36)\n" \
"ds_read_b64_tr_b16 a[0:1], v1 offset:0\n" \
"ds_read_b64_tr_b16 a[2:3], v1 offset:512\n" \
"ds_read_b64_tr_b16 a[128:129], v1 offset:32\n" \
"ds_read_b64_tr_b16 a[130:131], v1 offset:544\n" \
"ds_read_b64_tr_b16 v[128:129], v1 offset:64\n" \
"ds_read_b64_tr_b16 v[130:131], v1 offset:576\n" \
"ds_read_b64_tr_b16 v[16:17], v1 offset:96\n" \
"ds_read_b64_tr_b16 v[18:19], v1 offset:608\n" \
"s_waitcnt lgkmcnt(0)\n" \
"s_add_u32 m0, s52, 0\n" \
"s_add_u32 s54, s88, 139264\n" \
"buffer_load_dwordx4 v0, s[40:43], s54 offen sc1 lds\n" \
"s_add_u32 m0, s52, 1024\n" \
"s_add_u32 s54, s88, 156672\n" \
"buffer_load_dwordx4 v0, s[40:43], s54 offen sc1 lds\n" \
"s_add_u32 m0, s52, 2048\n" \
"s_add_u32 s54, s88, 174080\n" \
"buffer_load_dwordx4 v0, s[40:43], s54 offen sc1 lds\n" \
"s_add_u32 m0, s52, 3072\n" \
"s_add_u32 s54, s88, 191488\n" \
"buffer_load_dwordx4 v0, s[40:43], s54 offen sc1 lds\n" \
"s_mov_b32 s54, s99\n" \
".Lsg2_z10_%=:\n" \
"s_sleep 1\n" \
"s_sub_u32 s54, s54, 1\n" \
"s_cbranch_scc0 .Lsg2_z10_%=\n" \
"s_waitcnt vmcnt(36)\n" \
"ds_read_b64_tr_b16 a[4:5], v1 offset:4096\n" \
"ds_read_b64_tr_b16 a[6:7], v1 offset:4608\n" \
"ds_read_b64_tr_b16 a[132:133], v1 offset:4128\n" \
"ds_read_b64_tr_b16 a[134:135], v1 offset:4640\n" \
"ds_read_b64_tr_b16 v[132:133], v1 offset:4160\n" \
"ds_read_b64_tr_b16 v[134:135], v1 offset:4672\n" \
"ds_read_b64_tr_b16 v[20:21], v1 offset:4192\n" \
"ds_read_b64_tr_b16 v[22:23], v1 offset:4704\n" \
"s_waitcnt lgkmcnt(0)\n" \
"s_add_u32 m0, s52, 4096\n" \
"s_add_u32 s54, s88, 208896\n" \
"buffer_load_dwordx4 v0, s[40:43], s54 offen sc1 lds\n" \
"s_add_u32 m0, s52, 5120\n" \
"s_add_u32 s54, s88, 226304\n" \
"buffer_load_dwordx4 v0, s[40:43], s54 offen sc1 lds\n" \
"s_add_u32 m0, s52, 6144\n" \
"s_add_u32 s54, s88, 243712\n" \
"buffer_load_dwordx4 v0, s[40:43], s54 offen sc1 lds\n" \
"s_add_u32 m0, s52, 7168\n" \
"s_add_u32 s54, s88, 261120\n" \
"buffer_load_dwordx4 v0, s[40:43], s54 offen sc1 lds\n" \
"s_mov_b32 s54, s99\n" \
".Lsg2_z11_%=:\n" \
"s_sleep 1\n" \
"s_sub_u32 s54, s54, 1\n" \
"s_cbranch_scc0 .Lsg2_z11_%=\n" \
"s_waitcnt vmcnt(36)\n" \
"ds_read_b64_tr_b16 a[8:9], v1 offset:8192\n" \
"ds_read_b64_tr_b16 a[10:11], v1 offset:8704\n" \
"ds_read_b64_tr_b16 a[136:137], v1 offset:8224\n" \
"ds_read_b64_tr_b16 a[138:139], v1 offset:8736\n" \
"ds_read_b64_tr_b16 v[136:137], v1 offset:8256\n" \
"ds_read_b64_tr_b16 v[138:139], v1 offset:8768\n" \
"ds_read_b64_tr_b16 v[24:25], v1 offset:8288\n" \
"ds_read_b64_tr_b16 v[26:27], v1 offset:8800\n" \
"s_waitcnt lgkmcnt(0)\n" \
"s_add_u32 m0, s52, 8192\n" \
"s_add_u32 s54, s89, 0\n" \
"buffer_load_dwordx4 v0, s[40:43], s54 offen sc1 lds\n" \
"s_add_u32 m0, s52, 9216\n" \
"s_add_u32 s54, s89, 17408\n" \
"buffer_load_dwordx4 v0, s[40:43], s54 offen sc1 lds\n" \
"s_add_u32 m0, s52, 10240\n" \
"s_add_u32 s54, s89, 34816\n" \
"buffer_load_dwordx4 v0, s[40:43], s54 offen sc1 lds\n" \
"s_add_u32 m0, s52, 11264\n" \
"s_add_u32 s54, s89, 52224\n" \
"buffer_load_dwordx4 v0, s[40:43], s54 offen sc1 lds\n" \
"s_mov_b32 s54, s99\n" \
".Lsg2_z12_%=:\n" \
"s_sleep 1\n" \
"s_sub_u32 s54, s54, 1\n" \
"s_cbranch_scc0 .Lsg2_z12_%=\n" \
"s_waitcnt vmcnt(36)\n" \
"ds_read_b64_tr_b16 a[12:13], v1 offset:12288\n" \
"ds_read_b64_tr_b16 a[14:15], v1 offset:12800\n" \
"ds_read_b64_tr_b16 a[140:141], v1 offset:12320\n" \
"ds_read_b64_tr_b16 a[142:143], v1 offset:12832\n" \
"ds_read_b64_tr_b16 v[140:141], v1 offset:12352\n" \
"ds_read_b64_tr_b16 v[142:143], v1 offset:12864\n" \
"ds_read_b64_tr_b16 v[28:29], v1 offset:12384\n" \
"ds_read_b64_tr_b16 v[30:31], v1 offset:12896\n" \
"s_waitcnt lgkmcnt(0)\n" \
"s_add_u32 m0, s52, 12288\n" \
"s_add_u32 s54, s89, 69632\n" \
"buffer_load_dwordx4 v0, s[40:43], s54 offen sc1 lds\n" \
"s_add_u32 m0, s52, 13312\n" \
"s_add_u32 s54, s89, 87040\n" \
"buffer_load_dwordx4 v0, s[40:43], s54 offen sc1 lds\n" \
"s_add_u32 m0, s52, 14336\n" \
"s_add_u32 s54, s89, 104448\n" \
"buffer_load_dwordx4 v0, s[40:43], s54 offen sc1 lds\n" \
"s_add_u32 m0, s52, 15360\n" \
"s_add_u32 s54, s89, 121856\n" \
"buffer_load_dwordx4 v0, s[40:43], s54 offen sc1 lds\n" \
"s_mov_b32 s54, s99\n" \
".Lsg2_z13_%=:\n" \
"s_sleep 1\n" \
"s_sub_u32 s54, s54, 1\n" \
"s_cbranch_scc0 .Lsg2_z13_%=\n" \
"s_waitcnt vmcnt(36)\n" \
"ds_read_b64_tr_b16 a[16:17], v1 offset:16384\n" \
"ds_read_b64_tr_b16 a[18:19], v1 offset:16896\n" \
"ds_read_b64_tr_b16 a[144:145], v1 offset:16416\n" \
"ds_read_b64_tr_b16 a[146:147], v1 offset:16928\n" \
"ds_read_b64_tr_b16 v[144:145], v1 offset:16448\n" \
"ds_read_b64_tr_b16 v[146:147], v1 offset:16960\n" \
"ds_read_b64_tr_b16 v[32:33], v1 offset:16480\n" \
"ds_read_b64_tr_b16 v[34:35], v1 offset:16992\n" \
"s_waitcnt lgkmcnt(0)\n" \
"s_add_u32 m0, s52, 16384\n" \
"s_add_u32 s54, s89, 139264\n" \
"buffer_load_dwordx4 v0, s[40:43], s54 offen sc1 lds\n" \
"s_add_u32 m0, s52, 17408\n" \
"s_add_u32 s54, s89, 156672\n" \
"buffer_load_dwordx4 v0, s[40:43], s54 offen sc1 lds\n" \
"s_add_u32 m0, s52, 18432\n" \
"s_add_u32 s54, s89, 174080\n" \
"buffer_load_dwordx4 v0, s[40:43], s54 offen sc1 lds\n" \
"s_add_u32 m0, s52, 19456\n" \
"s_add_u32 s54, s89, 191488\n" \
"buffer_load_dwordx4 v0, s[40:43], s54 offen sc1 lds\n" \
"s_mov_b32 s54, s99\n" \
".Lsg2_z14_%=:\n" \
"s_sleep 1\n" \
"s_sub_u32 s54, s54, 1\n" \
"s_cbranch_scc0 .Lsg2_z14_%=\n" \
"s_waitcnt vmcnt(36)\n" \
"ds_read_b64_tr_b16 a[20:21], v1 offset:20480\n" \
"ds_read_b64_tr_b16 a[22:23], v1 offset:20992\n" \
"ds_read_b64_tr_b16 a[148:149], v1 offset:20512\n" \
"ds_read_b64_tr_b16 a[150:151], v1 offset:21024\n" \
"ds_read_b64_tr_b16 v[148:149], v1 offset:20544\n" \
"ds_read_b64_tr_b16 v[150:151], v1 offset:21056\n" \
"ds_read_b64_tr_b16 v[36:37], v1 offset:20576\n" \
"ds_read_b64_tr_b16 v[38:39], v1 offset:21088\n" \
"s_waitcnt lgkmcnt(0)\n" \
"s_add_u32 m0, s52, 20480\n" \
"s_add_u32 s54, s89, 208896\n" \
"buffer_load_dwordx4 v0, s[40:43], s54 offen sc1 lds\n" \
"s_add_u32 m0, s52, 21504\n" \
"s_add_u32 s54, s89, 226304\n" \
"buffer_load_dwordx4 v0, s[40:43], s54 offen sc1 lds\n" \
"s_add_u32 m0, s52, 22528\n" \
"s_add_u32 s54, s89, 243712\n" \
"buffer_load_dwordx4 v0, s[40:43], s54 offen sc1 lds\n" \
"s_add_u32 m0, s52, 23552\n" \
"s_add_u32 s54, s89, 261120\n" \
"buffer_load_dwordx4 v0, s[40:43], s54 offen sc1 lds\n" \
"s_mov_b32 s54, s99\n" \
".Lsg2_z15_%=:\n" \
"s_sleep 1\n" \
"s_sub_u32 s54, s54, 1\n" \
"s_cbranch_scc0 .Lsg2_z15_%=\n" \
"s_waitcnt vmcnt(36)\n" \
"ds_read_b64_tr_b16 a[24:25], v1 offset:24576\n" \
"ds_read_b64_tr_b16 a[26:27], v1 offset:25088\n" \
"ds_read_b64_tr_b16 a[152:153], v1 offset:24608\n" \
"ds_read_b64_tr_b16 a[154:155], v1 offset:25120\n" \
"ds_read_b64_tr_b16 v[152:153], v1 offset:24640\n" \
"ds_read_b64_tr_b16 v[154:155], v1 offset:25152\n" \
"ds_read_b64_tr_b16 v[40:41], v1 offset:24672\n" \
"ds_read_b64_tr_b16 v[42:43], v1 offset:25184\n" \
"s_waitcnt lgkmcnt(0)\n" \
"s_add_u32 m0, s52, 24576\n" \
"s_add_u32 s54, s90, 0\n" \
"buffer_load_dwordx4 v0, s[40:43], s54 offen sc1 lds\n" \
"s_add_u32 m0, s52, 25600\n" \
"s_add_u32 s54, s90, 17408\n" \
"buffer_load_dwordx4 v0, s[40:43], s54 offen sc1 lds\n" \
"s_add_u32 m0, s52, 26624\n" \
"s_add_u32 s54, s90, 34816\n" \
"buffer_load_dwordx4 v0, s[40:43], s54 offen sc1 lds\n" \
"s_add_u32 m0, s52, 27648\n" \
"s_add_u32 s54, s90, 52224\n" \
"buffer_load_dwordx4 v0, s[40:43], s54 offen sc1 lds\n" \
"s_mov_b32 s54, s99\n" \
".Lsg2_z16_%=:\n" \
"s_sleep 1\n" \
"s_sub_u32 s54, s54, 1\n" \
"s_cbranch_scc0 .Lsg2_z16_%=\n" \
"s_waitcnt vmcnt(36)\n" \
"ds_read_b64_tr_b16 a[28:29], v1 offset:28672\n" \
"ds_read_b64_tr_b16 a[30:31], v1 offset:29184\n" \
"ds_read_b64_tr_b16 a[156:157], v1 offset:28704\n" \
"ds_read_b64_tr_b16 a[158:159], v1 offset:29216\n" \
"ds_read_b64_tr_b16 v[156:157], v1 offset:28736\n" \
"ds_read_b64_tr_b16 v[158:159], v1 offset:29248\n" \
"ds_read_b64_tr_b16 v[44:45], v1 offset:28768\n" \
"ds_read_b64_tr_b16 v[46:47], v1 offset:29280\n" \
"s_waitcnt lgkmcnt(0)\n" \
"s_add_u32 m0, s52, 28672\n" \
"s_add_u32 s54, s90, 69632\n" \
"buffer_load_dwordx4 v0, s[40:43], s54 offen sc1 lds\n" \
"s_add_u32 m0, s52, 29696\n" \
"s_add_u32 s54, s90, 87040\n" \
"buffer_load_dwordx4 v0, s[40:43], s54 offen sc1 lds\n" \
"s_add_u32 m0, s52, 30720\n" \
"s_add_u32 s54, s90, 104448\n" \
"buffer_load_dwordx4 v0, s[40:43], s54 offen sc1 lds\n" \
"s_add_u32 m0, s52, 31744\n" \
"s_add_u32 s54, s90, 121856\n" \
"buffer_load_dwordx4 v0, s[40:43], s54 offen sc1 lds\n" \
"s_mov_b32 s54, s99\n" \
".Lsg2_z17_%=:\n" \
"s_sleep 1\n" \
"s_sub_u32 s54, s54, 1\n" \
"s_cbranch_scc0 .Lsg2_z17_%=\n" \
"s_waitcnt vmcnt(36)\n" \
"ds_read_b64_tr_b16 a[32:33], v9 offset:32768\n" \
"ds_read_b64_tr_b16 a[34:35], v9 offset:33280\n" \
"ds_read_b64_tr_b16 a[160:161], v9 offset:32800\n" \
"ds_read_b64_tr_b16 a[162:163], v9 offset:33312\n" \
"ds_read_b64_tr_b16 v[160:161], v9 offset:32832\n" \
"ds_read_b64_tr_b16 v[162:163], v9 offset:33344\n" \
"ds_read_b64_tr_b16 v[48:49], v9 offset:32864\n" \
"ds_read_b64_tr_b16 v[50:51], v9 offset:33376\n" \
"s_waitcnt lgkmcnt(0)\n" \
"s_add_u32 m0, s53, 32768\n" \
"s_add_u32 s54, s90, 139264\n" \
"buffer_load_dwordx4 v0, s[40:43], s54 offen sc1 lds\n" \
"s_add_u32 m0, s53, 33792\n" \
"s_add_u32 s54, s90, 156672\n" \
"buffer_load_dwordx4 v0, s[40:43], s54 offen sc1 lds\n" \
"s_add_u32 m0, s53, 34816\n" \
"s_add_u32 s54, s90, 174080\n" \
"buffer_load_dwordx4 v0, s[40:43], s54 offen sc1 lds\n" \
"s_add_u32 m0, s53, 35840\n" \
"s_add_u32 s54, s90, 191488\n" \
"buffer_load_dwordx4 v0, s[40:43], s54 offen sc1 lds\n" \
"s_mov_b32 s54, s99\n" \
".Lsg2_z18_%=:\n" \
"s_sleep 1\n" \
"s_sub_u32 s54, s54, 1\n" \
"s_cbranch_scc0 .Lsg2_z18_%=\n" \
"s_waitcnt vmcnt(36)\n" \
"ds_read_b64_tr_b16 a[36:37], v9 offset:36864\n" \
"ds_read_b64_tr_b16 a[38:39], v9 offset:37376\n" \
"ds_read_b64_tr_b16 a[164:165], v9 offset:36896\n" \
"ds_read_b64_tr_b16 a[166:167], v9 offset:37408\n" \
"ds_read_b64_tr_b16 v[164:165], v9 offset:36928\n" \
"ds_read_b64_tr_b16 v[166:167], v9 offset:37440\n" \
"ds_read_b64_tr_b16 v[52:53], v9 offset:36960\n" \
"ds_read_b64_tr_b16 v[54:55], v9 offset:37472\n" \
"s_waitcnt lgkmcnt(0)\n" \
"s_add_u32 m0, s53, 36864\n" \
"s_add_u32 s54, s90, 208896\n" \
"buffer_load_dwordx4 v0, s[40:43], s54 offen sc1 lds\n" \
"s_add_u32 m0, s53, 37888\n" \
"s_add_u32 s54, s90, 226304\n" \
"buffer_load_dwordx4 v0, s[40:43], s54 offen sc1 lds\n" \
"s_add_u32 m0, s53, 38912\n" \
"s_add_u32 s54, s90, 243712\n" \
"buffer_load_dwordx4 v0, s[40:43], s54 offen sc1 lds\n" \
"s_add_u32 m0, s53, 39936\n" \
"s_add_u32 s54, s90, 261120\n" \
"buffer_load_dwordx4 v0, s[40:43], s54 offen sc1 lds\n" \
"s_mov_b32 s54, s99\n" \
".Lsg2_z19_%=:\n" \
"s_sleep 1\n" \
"s_sub_u32 s54, s54, 1\n" \
"s_cbranch_scc0 .Lsg2_z19_%=\n" \
"s_waitcnt vmcnt(36)\n" \
"ds_read_b64_tr_b16 a[40:41], v1 offset:0\n" \
"ds_read_b64_tr_b16 a[42:43], v1 offset:512\n" \
"ds_read_b64_tr_b16 a[168:169], v1 offset:32\n" \
"ds_read_b64_tr_b16 a[170:171], v1 offset:544\n" \
"ds_read_b64_tr_b16 v[168:169], v1 offset:64\n" \
"ds_read_b64_tr_b16 v[170:171], v1 offset:576\n" \
"ds_read_b64_tr_b16 v[56:57], v1 offset:96\n" \
"ds_read_b64_tr_b16 v[58:59], v1 offset:608\n" \
"s_waitcnt lgkmcnt(0)\n" \
"s_add_u32 m0, s52, 0\n" \
"s_add_u32 s54, s91, 0\n" \
"buffer_load_dwordx4 v0, s[40:43], s54 offen sc1 lds\n" \
"s_add_u32 m0, s52, 1024\n" \
"s_add_u32 s54, s91, 17408\n" \
"buffer_load_dwordx4 v0, s[40:43], s54 offen sc1 lds\n" \
"s_add_u32 m0, s52, 2048\n" \
"s_add_u32 s54, s91, 34816\n" \
"buffer_load_dwordx4 v0, s[40:43], s54 offen sc1 lds\n" \
"s_add_u32 m0, s52, 3072\n" \
"s_add_u32 s54, s91, 52224\n" \
"buffer_load_dwordx4 v0, s[40:43], s54 offen sc1 lds\n" \
"s_mov_b32 s54, s99\n" \
".Lsg2_z20_%=:\n" \
"s_sleep 1\n" \
"s_sub_u32 s54, s54, 1\n" \
"s_cbranch_scc0 .Lsg2_z20_%=\n" \
"s_waitcnt vmcnt(36)\n" \
"ds_read_b64_tr_b16 a[44:45], v1 offset:4096\n" \
"ds_read_b64_tr_b16 a[46:47], v1 offset:4608\n" \
"ds_read_b64_tr_b16 a[172:173], v1 offset:4128\n" \
"ds_read_b64_tr_b16 a[174:175], v1 offset:4640\n" \
"ds_read_b64_tr_b16 v[172:173], v1 offset:4160\n" \
"ds_read_b64_tr_b16 v[174:175], v1 offset:4672\n" \
"ds_read_b64_tr_b16 v[60:61], v1 offset:4192\n" \
"ds_read_b64_tr_b16 v[62:63], v1 offset:4704\n" \
"s_waitcnt lgkmcnt(0)\n" \
"s_add_u32 m0, s52, 4096\n" \
"s_add_u32 s54, s91, 69632\n" \
"buffer_load_dwordx4 v0, s[40:43], s54 offen sc1 lds\n" \
"s_add_u32 m0, s52, 5120\n" \
"s_add_u32 s54, s91, 87040\n" \
"buffer_load_dwordx4 v0, s[40:43], s54 offen sc1 lds\n" \
"s_add_u32 m0, s52, 6144\n" \
"s_add_u32 s54, s91, 104448\n" \
"buffer_load_dwordx4 v0, s[40:43], s54 offen sc1 lds\n" \
"s_add_u32 m0, s52, 7168\n" \
"s_add_u32 s54, s91, 121856\n" \
"buffer_load_dwordx4 v0, s[40:43], s54 offen sc1 lds\n" \
"s_mov_b32 s54, s99\n" \
".Lsg2_z21_%=:\n" \
"s_sleep 1\n" \
"s_sub_u32 s54, s54, 1\n" \
"s_cbranch_scc0 .Lsg2_z21_%=\n" \
"s_waitcnt vmcnt(36)\n" \
"ds_read_b64_tr_b16 a[48:49], v1 offset:8192\n" \
"ds_read_b64_tr_b16 a[50:51], v1 offset:8704\n" \
"ds_read_b64_tr_b16 a[176:177], v1 offset:8224\n" \
"ds_read_b64_tr_b16 a[178:179], v1 offset:8736\n" \
"ds_read_b64_tr_b16 v[176:177], v1 offset:8256\n" \
"ds_read_b64_tr_b16 v[178:179], v1 offset:8768\n" \
"ds_read_b64_tr_b16 v[64:65], v1 offset:8288\n" \
"ds_read_b64_tr_b16 v[66:67], v1 offset:8800\n" \
"s_waitcnt lgkmcnt(0)\n" \
"s_add_u32 m0, s52, 8192\n" \
"s_add_u32 s54, s91, 139264\n" \
"buffer_load_dwordx4 v0, s[40:43], s54 offen sc1 lds\n" \
"s_add_u32 m0, s52, 9216\n" \
"s_add_u32 s54, s91, 156672\n" \
"buffer_load_dwordx4 v0, s[40:43], s54 offen sc1 lds\n" \
"s_add_u32 m0, s52, 10240\n" \
"s_add_u32 s54, s91, 174080\n" \
"buffer_load_dwordx4 v0, s[40:43], s54 offen sc1 lds\n" \
"s_add_u32 m0, s52, 11264\n" \
"s_add_u32 s54, s91, 191488\n" \
"buffer_load_dwordx4 v0, s[40:43], s54 offen sc1 lds\n" \
"s_mov_b32 s54, s99\n" \
".Lsg2_z22_%=:\n" \
"s_sleep 1\n" \
"s_sub_u32 s54, s54, 1\n" \
"s_cbranch_scc0 .Lsg2_z22_%=\n" \
"s_waitcnt vmcnt(36)\n" \
"ds_read_b64_tr_b16 a[52:53], v1 offset:12288\n" \
"ds_read_b64_tr_b16 a[54:55], v1 offset:12800\n" \
"ds_read_b64_tr_b16 a[180:181], v1 offset:12320\n" \
"ds_read_b64_tr_b16 a[182:183], v1 offset:12832\n" \
"ds_read_b64_tr_b16 v[180:181], v1 offset:12352\n" \
"ds_read_b64_tr_b16 v[182:183], v1 offset:12864\n" \
"ds_read_b64_tr_b16 v[68:69], v1 offset:12384\n" \
"ds_read_b64_tr_b16 v[70:71], v1 offset:12896\n" \
"s_waitcnt lgkmcnt(0)\n" \
"s_add_u32 m0, s52, 12288\n" \
"s_add_u32 s54, s91, 208896\n" \
"buffer_load_dwordx4 v0, s[40:43], s54 offen sc1 lds\n" \
"s_add_u32 m0, s52, 13312\n" \
"s_add_u32 s54, s91, 226304\n" \
"buffer_load_dwordx4 v0, s[40:43], s54 offen sc1 lds\n" \
"s_add_u32 m0, s52, 14336\n" \
"s_add_u32 s54, s91, 243712\n" \
"buffer_load_dwordx4 v0, s[40:43], s54 offen sc1 lds\n" \
"s_add_u32 m0, s52, 15360\n" \
"s_add_u32 s54, s91, 261120\n" \
"buffer_load_dwordx4 v0, s[40:43], s54 offen sc1 lds\n" \
"s_mov_b32 s54, s99\n" \
".Lsg2_z23_%=:\n" \
"s_sleep 1\n" \
"s_sub_u32 s54, s54, 1\n" \
"s_cbranch_scc0 .Lsg2_z23_%=\n" \
"s_waitcnt vmcnt(36)\n" \
"ds_read_b64_tr_b16 a[56:57], v1 offset:16384\n" \
"ds_read_b64_tr_b16 a[58:59], v1 offset:16896\n" \
"ds_read_b64_tr_b16 a[184:185], v1 offset:16416\n" \
"ds_read_b64_tr_b16 a[186:187], v1 offset:16928\n" \
"ds_read_b64_tr_b16 v[184:185], v1 offset:16448\n" \
"ds_read_b64_tr_b16 v[186:187], v1 offset:16960\n" \
"ds_read_b64_tr_b16 v[72:73], v1 offset:16480\n" \
"ds_read_b64_tr_b16 v[74:75], v1 offset:16992\n" \
"s_waitcnt lgkmcnt(0)\n" \
"s_add_u32 m0, s52, 16384\n" \
"s_add_u32 s54, s92, 0\n" \
"buffer_load_dwordx4 v0, s[40:43], s54 offen sc1 lds\n" \
"s_add_u32 m0, s52, 17408\n" \
"s_add_u32 s54, s92, 17408\n" \
"buffer_load_dwordx4 v0, s[40:43], s54 offen sc1 lds\n" \
"s_add_u32 m0, s52, 18432\n" \
"s_add_u32 s54, s92, 34816\n" \
"buffer_load_dwordx4 v0, s[40:43], s54 offen sc1 lds\n" \
"s_add_u32 m0, s52, 19456\n" \
"s_add_u32 s54, s92, 52224\n" \
"buffer_load_dwordx4 v0, s[40:43], s54 offen sc1 lds\n" \
"s_mov_b32 s54, s99\n" \
".Lsg2_z24_%=:\n" \
"s_sleep 1\n" \
"s_sub_u32 s54, s54, 1\n" \
"s_cbranch_scc0 .Lsg2_z24_%=\n" \
"s_waitcnt vmcnt(36)\n" \
"ds_read_b64_tr_b16 a[60:61], v1 offset:20480\n" \
"ds_read_b64_tr_b16 a[62:63], v1 offset:20992\n" \
"ds_read_b64_tr_b16 a[188:189], v1 offset:20512\n" \
"ds_read_b64_tr_b16 a[190:191], v1 offset:21024\n" \
"ds_read_b64_tr_b16 v[188:189], v1 offset:20544\n" \
"ds_read_b64_tr_b16 v[190:191], v1 offset:21056\n" \
"ds_read_b64_tr_b16 v[76:77], v1 offset:20576\n" \
"ds_read_b64_tr_b16 v[78:79], v1 offset:21088\n" \
"s_waitcnt lgkmcnt(0)\n" \
"s_add_u32 m0, s52, 20480\n" \
"s_add_u32 s54, s92, 69632\n" \
"buffer_load_dwordx4 v0, s[40:43], s54 offen sc1 lds\n" \
"s_add_u32 m0, s52, 21504\n" \
"s_add_u32 s54, s92, 87040\n" \
"buffer_load_dwordx4 v0, s[40:43], s54 offen sc1 lds\n" \
"s_add_u32 m0, s52, 22528\n" \
"s_add_u32 s54, s92, 104448\n" \
"buffer_load_dwordx4 v0, s[40:43], s54 offen sc1 lds\n" \
"s_add_u32 m0, s52, 23552\n" \
"s_add_u32 s54, s92, 121856\n" \
"buffer_load_dwordx4 v0, s[40:43], s54 offen sc1 lds\n" \
"s_mov_b32 s54, s99\n" \
".Lsg2_z25_%=:\n" \
"s_sleep 1\n" \
"s_sub_u32 s54, s54, 1\n" \
"s_cbranch_scc0 .Lsg2_z25_%=\n" \
"s_waitcnt vmcnt(36)\n" \
"ds_read_b64_tr_b16 a[64:65], v1 offset:24576\n" \
"ds_read_b64_tr_b16 a[66:67], v1 offset:25088\n" \
"ds_read_b64_tr_b16 a[192:193], v1 offset:24608\n" \
"ds_read_b64_tr_b16 a[194:195], v1 offset:25120\n" \
"ds_read_b64_tr_b16 v[192:193], v1 offset:24640\n" \
"ds_read_b64_tr_b16 v[194:195], v1 offset:25152\n" \
"ds_read_b64_tr_b16 v[80:81], v1 offset:24672\n" \
"ds_read_b64_tr_b16 v[82:83], v1 offset:25184\n" \
"s_waitcnt lgkmcnt(0)\n" \
"s_add_u32 m0, s52, 24576\n" \
"s_add_u32 s54, s92, 139264\n" \
"buffer_load_dwordx4 v0, s[40:43], s54 offen sc1 lds\n" \
"s_add_u32 m0, s52, 25600\n" \
"s_add_u32 s54, s92, 156672\n" \
"buffer_load_dwordx4 v0, s[40:43], s54 offen sc1 lds\n" \
"s_add_u32 m0, s52, 26624\n" \
"s_add_u32 s54, s92, 174080\n" \
"buffer_load_dwordx4 v0, s[40:43], s54 offen sc1 lds\n" \
"s_add_u32 m0, s52, 27648\n" \
"s_add_u32 s54, s92, 191488\n" \
"buffer_load_dwordx4 v0, s[40:43], s54 offen sc1 lds\n" \
"s_mov_b32 s54, s99\n" \
".Lsg2_z26_%=:\n" \
"s_sleep 1\n" \
"s_sub_u32 s54, s54, 1\n" \
"s_cbranch_scc0 .Lsg2_z26_%=\n" \
"s_waitcnt vmcnt(36)\n" \
"ds_read_b64_tr_b16 a[68:69], v1 offset:28672\n" \
"ds_read_b64_tr_b16 a[70:71], v1 offset:29184\n" \
"ds_read_b64_tr_b16 a[196:197], v1 offset:28704\n" \
"ds_read_b64_tr_b16 a[198:199], v1 offset:29216\n" \
"ds_read_b64_tr_b16 v[196:197], v1 offset:28736\n" \
"ds_read_b64_tr_b16 v[198:199], v1 offset:29248\n" \
"ds_read_b64_tr_b16 v[84:85], v1 offset:28768\n" \
"ds_read_b64_tr_b16 v[86:87], v1 offset:29280\n" \
"s_waitcnt lgkmcnt(0)\n" \
"s_add_u32 m0, s52, 28672\n" \
"s_add_u32 s54, s92, 208896\n" \
"buffer_load_dwordx4 v0, s[40:43], s54 offen sc1 lds\n" \
"s_add_u32 m0, s52, 29696\n" \
"s_add_u32 s54, s92, 226304\n" \
"buffer_load_dwordx4 v0, s[40:43], s54 offen sc1 lds\n" \
"s_add_u32 m0, s52, 30720\n" \
"s_add_u32 s54, s92, 243712\n" \
"buffer_load_dwordx4 v0, s[40:43], s54 offen sc1 lds\n" \
"s_add_u32 m0, s52, 31744\n" \
"s_add_u32 s54, s92, 261120\n" \
"buffer_load_dwordx4 v0, s[40:43], s54 offen sc1 lds\n" \
"s_mov_b32 s54, s99\n" \
".Lsg2_z27_%=:\n" \
"s_sleep 1\n" \
"s_sub_u32 s54, s54, 1\n" \
"s_cbranch_scc0 .Lsg2_z27_%=\n" \
"s_waitcnt vmcnt(36)\n" \
"ds_read_b64_tr_b16 a[72:73], v9 offset:32768\n" \
"ds_read_b64_tr_b16 a[74:75], v9 offset:33280\n" \
"ds_read_b64_tr_b16 a[200:201], v9 offset:32800\n" \
"ds_read_b64_tr_b16 a[202:203], v9 offset:33312\n" \
"ds_read_b64_tr_b16 v[200:201], v9 offset:32832\n" \
"ds_read_b64_tr_b16 v[202:203], v9 offset:33344\n" \
"ds_read_b64_tr_b16 v[88:89], v9 offset:32864\n" \
"ds_read_b64_tr_b16 v[90:91], v9 offset:33376\n" \
"s_waitcnt lgkmcnt(0)\n" \
"s_waitcnt vmcnt(32)\n" \
"ds_read_b64_tr_b16 a[76:77], v9 offset:36864\n" \
"ds_read_b64_tr_b16 a[78:79], v9 offset:37376\n" \
"ds_read_b64_tr_b16 a[204:205], v9 offset:36896\n" \
"ds_read_b64_tr_b16 a[206:207], v9 offset:37408\n" \
"ds_read_b64_tr_b16 v[204:205], v9 offset:36928\n" \
"ds_read_b64_tr_b16 v[206:207], v9 offset:37440\n" \
"ds_read_b64_tr_b16 v[92:93], v9 offset:36960\n" \
"ds_read_b64_tr_b16 v[94:95], v9 offset:37472\n" \
"s_waitcnt lgkmcnt(0)\n" \
"s_waitcnt vmcnt(28)\n" \
"ds_read_b64_tr_b16 a[80:81], v1 offset:0\n" \
"ds_read_b64_tr_b16 a[82:83], v1 offset:512\n" \
"ds_read_b64_tr_b16 a[208:209], v1 offset:32\n" \
"ds_read_b64_tr_b16 a[210:211], v1 offset:544\n" \
"ds_read_b64_tr_b16 v[208:209], v1 offset:64\n" \
"ds_read_b64_tr_b16 v[210:211], v1 offset:576\n" \
"ds_read_b64_tr_b16 v[96:97], v1 offset:96\n" \
"ds_read_b64_tr_b16 v[98:99], v1 offset:608\n" \
"s_waitcnt lgkmcnt(0)\n" \
"ds_write_b128 v2, v[16:19] offset:0\n" \
"ds_write_b128 v2, v[20:23] offset:1024\n" \
"ds_write_b128 v2, v[24:27] offset:2048\n" \
"ds_write_b128 v2, v[28:31] offset:3072\n" \
"s_waitcnt vmcnt(24)\n" \
"ds_read_b64_tr_b16 a[84:85], v1 offset:4096\n" \
"ds_read_b64_tr_b16 a[86:87], v1 offset:4608\n" \
"ds_read_b64_tr_b16 a[212:213], v1 offset:4128\n" \
"ds_read_b64_tr_b16 a[214:215], v1 offset:4640\n" \
"ds_read_b64_tr_b16 v[212:213], v1 offset:4160\n" \
"ds_read_b64_tr_b16 v[214:215], v1 offset:4672\n" \
"ds_read_b64_tr_b16 v[100:101], v1 offset:4192\n" \
"ds_read_b64_tr_b16 v[102:103], v1 offset:4704\n" \
"s_waitcnt lgkmcnt(0)\n" \
"ds_write_b128 v2, v[32:35] offset:4096\n" \
"ds_write_b128 v2, v[36:39] offset:5120\n" \
"ds_write_b128 v2, v[40:43] offset:6144\n" \
"ds_write_b128 v2, v[44:47] offset:7168\n" \
"s_waitcnt vmcnt(20)\n" \
"ds_read_b64_tr_b16 a[88:89], v1 offset:8192\n" \
"ds_read_b64_tr_b16 a[90:91], v1 offset:8704\n" \
"ds_read_b64_tr_b16 a[216:217], v1 offset:8224\n" \
"ds_read_b64_tr_b16 a[218:219], v1 offset:8736\n" \
"ds_read_b64_tr_b16 v[216:217], v1 offset:8256\n" \
"ds_read_b64_tr_b16 v[218:219], v1 offset:8768\n" \
"ds_read_b64_tr_b16 v[104:105], v1 offset:8288\n" \
"ds_read_b64_tr_b16 v[106:107], v1 offset:8800\n" \
"s_waitcnt lgkmcnt(0)\n" \
"ds_write_b128 v2, v[48:51] offset:8192\n" \
"ds_write_b128 v2, v[52:55] offset:9216\n" \
"ds_write_b128 v2, v[56:59] offset:10240\n" \
"ds_write_b128 v2, v[60:63] offset:11264\n" \
"s_waitcnt vmcnt(16)\n" \
"ds_read_b64_tr_b16 a[92:93], v1 offset:12288\n" \
"ds_read_b64_tr_b16 a[94:95], v1 offset:12800\n" \
"ds_read_b64_tr_b16 a[220:221], v1 offset:12320\n" \
"ds_read_b64_tr_b16 a[222:223], v1 offset:12832\n" \
"ds_read_b64_tr_b16 v[220:221], v1 offset:12352\n" \
"ds_read_b64_tr_b16 v[222:223], v1 offset:12864\n" \
"ds_read_b64_tr_b16 v[108:109], v1 offset:12384\n" \
"ds_read_b64_tr_b16 v[110:111], v1 offset:12896\n" \
"s_waitcnt lgkmcnt(0)\n" \
"ds_write_b128 v2, v[64:67] offset:12288\n" \
"ds_write_b128 v2, v[68:71] offset:13312\n" \
"ds_write_b128 v2, v[72:75] offset:14336\n" \
"ds_write_b128 v2, v[76:79] offset:15360\n" \
"s_waitcnt vmcnt(12)\n" \
"ds_read_b64_tr_b16 a[96:97], v1 offset:16384\n" \
"ds_read_b64_tr_b16 a[98:99], v1 offset:16896\n" \
"ds_read_b64_tr_b16 a[224:225], v1 offset:16416\n" \
"ds_read_b64_tr_b16 a[226:227], v1 offset:16928\n" \
"ds_read_b64_tr_b16 v[224:225], v1 offset:16448\n" \
"ds_read_b64_tr_b16 v[226:227], v1 offset:16960\n" \
"ds_read_b64_tr_b16 v[16:17], v1 offset:16480\n" \
"ds_read_b64_tr_b16 v[18:19], v1 offset:16992\n" \
"s_waitcnt lgkmcnt(0)\n" \
"ds_write_b128 v2, v[80:83] offset:16384\n" \
"ds_write_b128 v2, v[84:87] offset:17408\n" \
"ds_write_b128 v2, v[88:91] offset:18432\n" \
"ds_write_b128 v2, v[92:95] offset:19456\n" \
"s_waitcnt vmcnt(8)\n" \
"ds_read_b64_tr_b16 a[100:101], v1 offset:20480\n" \
"ds_read_b64_tr_b16 a[102:103], v1 offset:20992\n" \
"ds_read_b64_tr_b16 a[228:229], v1 offset:20512\n" \
"ds_read_b64_tr_b16 a[230:231], v1 offset:21024\n" \
"ds_read_b64_tr_b16 v[228:229], v1 offset:20544\n" \
"ds_read_b64_tr_b16 v[230:231], v1 offset:21056\n" \
"ds_read_b64_tr_b16 v[20:21], v1 offset:20576\n" \
"ds_read_b64_tr_b16 v[22:23], v1 offset:21088\n" \
"s_waitcnt lgkmcnt(0)\n" \
"ds_write_b128 v2, v[96:99] offset:20480\n" \
"ds_write_b128 v2, v[100:103] offset:21504\n" \
"ds_write_b128 v2, v[104:107] offset:22528\n" \
"ds_write_b128 v2, v[108:111] offset:23552\n" \
"s_waitcnt vmcnt(4)\n" \
"ds_read_b64_tr_b16 a[104:105], v1 offset:24576\n" \
"ds_read_b64_tr_b16 a[106:107], v1 offset:25088\n" \
"ds_read_b64_tr_b16 a[232:233], v1 offset:24608\n" \
"ds_read_b64_tr_b16 a[234:235], v1 offset:25120\n" \
"ds_read_b64_tr_b16 v[232:233], v1 offset:24640\n" \
"ds_read_b64_tr_b16 v[234:235], v1 offset:25152\n" \
"ds_read_b64_tr_b16 v[24:25], v1 offset:24672\n" \
"ds_read_b64_tr_b16 v[26:27], v1 offset:25184\n" \
"s_waitcnt lgkmcnt(0)\n" \
"ds_write_b128 v2, v[16:19] offset:24576\n" \
"ds_write_b128 v2, v[20:23] offset:25600\n" \
"ds_write_b128 v2, v[24:27] offset:26624\n" \
"s_waitcnt vmcnt(0)\n" \
"ds_read_b64_tr_b16 a[108:109], v1 offset:28672\n" \
"ds_read_b64_tr_b16 a[110:111], v1 offset:29184\n" \
"ds_read_b64_tr_b16 a[236:237], v1 offset:28704\n" \
"ds_read_b64_tr_b16 a[238:239], v1 offset:29216\n" \
"ds_read_b64_tr_b16 v[236:237], v1 offset:28736\n" \
"ds_read_b64_tr_b16 v[238:239], v1 offset:29248\n" \
"ds_read_b64_tr_b16 v[28:29], v1 offset:28768\n" \
"ds_read_b64_tr_b16 v[30:31], v1 offset:29280\n" \
"s_waitcnt lgkmcnt(0)\n" \
"ds_write_b128 v2, v[28:31] offset:28672\n" \
"s_waitcnt vmcnt(0) lgkmcnt(0)\n" \
"s_barrier\n" \
"s_mov_b32 s54, s85\n" \
"s_mul_i32 s86, s54, 4096\n" \
"s_add_u32 s54, s54, 1\n" \
"s_cmp_eq_u32 s54, 7\n" \
"s_cselect_b32 s54, 0, s54\n" \
"s_mul_i32 s87, s54, 4096\n" \
"s_add_u32 s54, s54, 1\n" \
"s_cmp_eq_u32 s54, 7\n" \
"s_cselect_b32 s54, 0, s54\n" \
"s_mul_i32 s88, s54, 4096\n" \
"s_add_u32 s54, s54, 1\n" \
"s_cmp_eq_u32 s54, 7\n" \
"s_cselect_b32 s54, 0, s54\n" \
"s_mul_i32 s89, s54, 4096\n" \
"s_add_u32 s54, s54, 1\n" \
"s_cmp_eq_u32 s54, 7\n" \
"s_cselect_b32 s54, 0, s54\n" \
"s_mul_i32 s90, s54, 4096\n" \
"s_add_u32 s54, s54, 1\n" \
"s_cmp_eq_u32 s54, 7\n" \
"s_cselect_b32 s54, 0, s54\n" \
"s_mul_i32 s91, s54, 4096\n" \
"s_add_u32 s54, s54, 1\n" \
"s_cmp_eq_u32 s54, 7\n" \
"s_cselect_b32 s54, 0, s54\n" \
"s_mul_i32 s92, s54, 4096\n" \
"s_add_u32 s54, s54, 1\n" \
"s_cmp_eq_u32 s54, 7\n" \
"s_cselect_b32 s54, 0, s54\n" \
"s_mov_b32 s55, 0\n" \
"s_mov_b32 s60, 0\n" \
".Lsg2_prime_%=:\n" \
"s_lshr_b32 s59, s44, 1\n" \
"s_add_u32 s59, s59, 1\n" \
"s_mul_i32 s56, s44, 114688\n" \
"s_lshl_b32 s78, s44, 15\n" \
"s_lshl_b32 s97, s44, 2\n" \
"s_add_u32 s97, s97, s49\n" \
"s_mov_b32 s79, s97\n" \
"buffer_load_dword v9, off, s[36:39], s97 sc1\n" \
"buffer_load_dword v11, v4, s[36:39], 0 offen sc1\n" \
"s_waitcnt vmcnt(0)\n" \
"v_cmp_gt_u32_e64 s[62:63], s59, v11\n" \
"s_nop 4\n" \
"s_cmp_lg_u64 s[62:63], 0\n" \
"s_cbranch_scc1 .Lsg2_w_%=\n" \
".Lsg2_wk_%=:\n" \
"s_mov_b32 s98, 0x70000000\n" \
"s_mov_b32 s81, 1\n" \
"s_mov_b32 s84, 0\n" \
"v_mov_b32 v10, 0\n" \
"s_waitcnt vmcnt(0) lgkmcnt(0)\n" \
"s_add_u32 s57, s56, s86\n" \
"buffer_load_dwordx4 v[16:19], v3, s[36:39], s57 offen offset:0 sc1\n" \
"buffer_load_dwordx4 v[20:23], v3, s[36:39], s57 offen offset:1024 sc1\n" \
"buffer_load_dwordx4 v[24:27], v3, s[36:39], s57 offen offset:2048 sc1\n" \
"buffer_load_dwordx4 v[28:31], v3, s[36:39], s57 offen offset:3072 sc1\n" \
"s_add_u32 s57, s56, s87\n" \
"buffer_load_dwordx4 v[32:35], v3, s[36:39], s57 offen offset:0 sc1\n" \
"buffer_load_dwordx4 v[36:39], v3, s[36:39], s57 offen offset:1024 sc1\n" \
"buffer_load_dwordx4 v[40:43], v3, s[36:39], s57 offen offset:2048 sc1\n" \
"buffer_load_dwordx4 v[44:47], v3, s[36:39], s57 offen offset:3072 sc1\n" \
"s_add_u32 s57, s56, s88\n" \
"buffer_load_dwordx4 v[48:51], v3, s[36:39], s57 offen offset:0 sc1\n" \
"buffer_load_dwordx4 v[52:55], v3, s[36:39], s57 offen offset:1024 sc1\n" \
"buffer_load_dwordx4 v[56:59], v3, s[36:39], s57 offen offset:2048 sc1\n" \
"buffer_load_dwordx4 v[60:63], v3, s[36:39], s57 offen offset:3072 sc1\n" \
"s_add_u32 s57, s56, s89\n" \
"buffer_load_dwordx4 v[64:67], v3, s[36:39], s57 offen offset:0 sc1\n" \
"buffer_load_dwordx4 v[68:71], v3, s[36:39], s57 offen offset:1024 sc1\n" \
"ds_read_b128 v[80:83], v2 offset:0\n" \
"ds_read_b128 v[84:87], v2 offset:1024\n" \
"ds_read_b128 v[88:91], v2 offset:2048\n" \
"s_waitcnt lgkmcnt(0)\n" \
"v_mov_b32 v13, 0x7ffffff0\n" \
"buffer_store_dword v13, v13, s[36:39], 0 offen\n" \
"buffer_store_dword v13, v13, s[36:39], 0 offen\n" \
"buffer_store_dword v13, v13, s[36:39], 0 offen\n" \
"s_waitcnt vmcnt(14)\n" \
"s_nop 7\n" \
"s_nop 7\n" \
".p2align 6\n" \
".Lsg2_loop_%=:\n" \
"v_mfma_f32_16x16x32_bf16 v[96:99], a[0:3], v[16:19], 0\n" \
"ds_write_b128 v7, v[104:107] offset:2048\n" \
"v_xor_b32_e64 v7, 64, v7\n" \
"v_mfma_f32_16x16x32_bf16 v[100:103], a[128:131], v[16:19], 0\n" \
"ds_write_b128 v7, v[108:111] offset:3072\n" \
"ds_read_b128 v[92:95], v2 offset:3072\n" \
"v_xor_b32 v7, 192, v7\n" \
"v_mfma_f32_16x16x32_bf16 v[104:107], v[128:131], v[16:19], 0\n" \
"s_waitcnt lgkmcnt(7)\n" \
"v_mfma_f32_16x16x32_bf16 v[108:111], v[80:83], v[16:19], 0\n" \
"buffer_load_dwordx4 v[16:19], v3, s[36:39], s57 offen offset:2048 sc1\n" \
"v_mfma_f32_16x16x32_bf16 v[96:99], a[4:7], v[20:23], v[96:99]\n" \
"v_readfirstlane_b32 s96, v9\n" \
"v_mfma_f32_16x16x32_bf16 v[100:103], a[132:135], v[20:23], v[100:103]\n" \
"ds_read_b128 v[80:83], v2 offset:4096\n" \
"v_mfma_f32_16x16x32_bf16 v[104:107], v[132:135], v[20:23], v[104:107]\n" \
"s_waitcnt lgkmcnt(7)\n" \
"v_mfma_f32_16x16x32_bf16 v[108:111], v[84:87], v[20:23], v[108:111]\n" \
"buffer_load_dwordx4 v[20:23], v3, s[36:39], s57 offen offset:3072 sc1\n" \
"v_mfma_f32_16x16x32_bf16 v[96:99], a[8:11], v[24:27], v[96:99]\n" \
"s_waitcnt lgkmcnt(2)\n" \
"s_cmp_lg_u32 s81, 0\n" \
"s_nop 0\n" \
"s_cbranch_scc1 .Lsg2_nb_%=\n" \
"s_barrier\n" \
".Lsg2_nb_%=:\n" \
"v_mfma_f32_16x16x32_bf16 v[100:103], a[136:139], v[24:27], v[100:103]\n" \
"ds_read_b128 v[84:87], v2 offset:5120\n" \
"v_xor_b32_e64 v7, s84, v7\n" \
"v_mfma_f32_16x16x32_bf16 v[104:107], v[136:139], v[24:27], v[104:107]\n" \
"s_waitcnt vmcnt(15)\n" \
"v_mfma_f32_16x16x32_bf16 v[108:111], v[88:91], v[24:27], v[108:111]\n" \
"s_add_u32 s57, s56, s90\n" \
"buffer_load_dwordx4 v[24:27], v3, s[36:39], s57 offen offset:0 sc1\n" \
"v_mfma_f32_16x16x32_bf16 v[96:99], a[12:15], v[28:31], v[96:99]\n" \
"s_add_u32 s76, s78, 4194304\n" \
"s_add_u32 s80, s78, 0\n" \
"s_and_b32 s96, s96, 1\n" \
"v_mfma_f32_16x16x32_bf16 v[100:103], a[140:143], v[28:31], v[100:103]\n" \
"ds_read_b128 v[88:91], v2 offset:6144\n" \
"s_cselect_b32 s76, 0x7ffffff0, s76\n" \
"s_cselect_b32 s77, s78, 0x7ffffff0\n" \
"v_mfma_f32_16x16x32_bf16 v[104:107], v[140:143], v[28:31], v[104:107]\n" \
"s_waitcnt vmcnt(15) lgkmcnt(3)\n" \
"s_cselect_b32 s80, s80, 0x7ffffff0\n" \
"s_cselect_b32 s94, 0x7ffffff0, s79\n" \
"v_mfma_f32_16x16x32_bf16 v[108:111], v[92:95], v[28:31], v[108:111]\n" \
"buffer_load_dwordx4 v[28:31], v3, s[36:39], s57 offen offset:1024 sc1\n" \
"v_mfma_f32_16x16x32_bf16 v[96:99], a[16:19], v[32:35], v[96:99]\n" \
"s_cmp_lt_i32 s44, s50\n" \
"s_cselect_b32 s82, s45, s47\n" \
"v_mfma_f32_16x16x32_bf16 v[100:103], a[144:147], v[32:35], v[100:103]\n" \
"ds_read_b128 v[92:95], v2 offset:7168\n" \
"s_add_u32 s82, s82, s44\n" \
"v_mfma_f32_16x16x32_bf16 v[104:107], v[144:147], v[32:35], v[104:107]\n" \
"s_waitcnt vmcnt(15) lgkmcnt(3)\n" \
"s_lshr_b32 s59, s82, 1\n" \
"s_add_u32 s59, s59, 1\n" \
"v_mfma_f32_16x16x32_bf16 v[108:111], v[80:83], v[32:35], v[108:111]\n" \
"buffer_load_dwordx4 v[32:35], v3, s[36:39], s57 offen offset:2048 sc1\n" \
"v_mfma_f32_16x16x32_bf16 v[96:99], a[20:23], v[36:39], v[96:99]\n" \
"s_or_b32 s85, s77, s98\n" \
"s_waitcnt vmcnt(7)\n" \
"v_readfirstlane_b32 s54, v10\n" \
"v_mfma_f32_16x16x32_bf16 v[100:103], a[148:151], v[36:39], v[100:103]\n" \
"ds_read_b128 v[80:83], v2 offset:8192\n" \
"s_or_b32 s76, s76, s98\n" \
"s_or_b32 s94, s94, s98\n" \
"v_mfma_f32_16x16x32_bf16 v[104:107], v[148:151], v[36:39], v[104:107]\n" \
"s_waitcnt lgkmcnt(3)\n" \
"s_and_b32 s54, s54, 1\n" \
"s_cbranch_scc1 .Lsg2_p_%=\n" \
".Lsg2_pk_%=:\n" \
"v_mfma_f32_16x16x32_bf16 v[108:111], v[84:87], v[36:39], v[108:111]\n" \
"buffer_load_dwordx4 v[36:39], v3, s[36:39], s57 offen offset:3072 sc1\n" \
"v_mfma_f32_16x16x32_bf16 v[96:99], a[24:27], v[40:43], v[96:99]\n" \
"buffer_load_dwordx2 v[14:15], v6, s[36:39], s80 offen sc1\n" \
"ds_read_b128 v[112:115], v8 offset:0\n" \
"v_mfma_f32_16x16x32_bf16 v[100:103], a[152:155], v[40:43], v[100:103]\n" \
"ds_read_b128 v[84:87], v2 offset:9216\n" \
"v_mfma_f32_16x16x32_bf16 v[104:107], v[152:155], v[40:43], v[104:107]\n" \
"s_waitcnt lgkmcnt(4)\n" \
"buffer_load_dwordx2 v[12:13], v5, s[64:67], s77 offen\n" \
"ds_read_b128 v[116:119], v8 offset:4096\n" \
"v_mfma_f32_16x16x32_bf16 v[108:111], v[88:91], v[40:43], v[108:111]\n" \
"s_add_u32 s57, s56, s91\n" \
"buffer_load_dwordx4 v[40:43], v3, s[36:39], s57 offen offset:0 sc1\n" \
"v_mfma_f32_16x16x32_bf16 v[96:99], a[28:31], v[44:47], v[96:99]\n" \
"ds_read_b128 v[120:123], v8 offset:8192\n" \
"v_mfma_f32_16x16x32_bf16 v[100:103], a[156:159], v[44:47], v[100:103]\n" \
"ds_read_b128 v[88:91], v2 offset:10240\n" \
"v_mfma_f32_16x16x32_bf16 v[104:107], v[156:159], v[44:47], v[104:107]\n" \
"s_waitcnt lgkmcnt(6)\n" \
"ds_read_b128 v[124:127], v8 offset:12288\n" \
"v_mfma_f32_16x16x32_bf16 v[108:111], v[92:95], v[44:47], v[108:111]\n" \
"buffer_load_dwordx4 v[44:47], v3, s[36:39], s57 offen offset:1024 sc1\n" \
"v_mfma_f32_16x16x32_bf16 v[96:99], a[32:35], v[48:51], v[96:99]\n" \
"v_mfma_f32_16x16x32_bf16 v[100:103], a[160:163], v[48:51], v[100:103]\n" \
"ds_read_b128 v[92:95], v2 offset:11264\n" \
"v_mfma_f32_16x16x32_bf16 v[104:107], v[160:163], v[48:51], v[104:107]\n" \
"s_waitcnt lgkmcnt(7)\n" \
"v_mfma_f32_16x16x32_bf16 v[108:111], v[80:83], v[48:51], v[108:111]\n" \
"buffer_load_dwordx4 v[48:51], v3, s[36:39], s57 offen offset:2048 sc1\n" \
"v_mfma_f32_16x16x32_bf16 v[96:99], a[36:39], v[52:55], v[96:99]\n" \
"s_waitcnt lgkmcnt(4)\n" \
"v_add_f32 v112, v112, v116\n" \
"v_add_f32 v113, v113, v117\n" \
"v_mfma_f32_16x16x32_bf16 v[100:103], a[164:167], v[52:55], v[100:103]\n" \
"ds_read_b128 v[80:83], v2 offset:12288\n" \
"v_add_f32_e64 v114, v114, v118\n" \
"v_mfma_f32_16x16x32_bf16 v[104:107], v[164:167], v[52:55], v[104:107]\n" \
"v_add_f32_e64 v115, v115, v119\n" \
"v_mfma_f32_16x16x32_bf16 v[108:111], v[84:87], v[52:55], v[108:111]\n" \
"buffer_load_dwordx4 v[52:55], v3, s[36:39], s57 offen offset:3072 sc1\n" \
"s_waitcnt lgkmcnt(2)\n" \
"v_add_f32 v120, v120, v124\n" \
"v_mfma_f32_16x16x32_bf16 v[96:99], a[40:43], v[56:59], v[96:99]\n" \
"s_lshl_b32 s97, s44, 2\n" \
"s_add_u32 s97, s97, s49\n" \
"v_add_f32 v121, v121, v125\n" \
"v_add_f32 v122, v122, v126\n" \
"v_mfma_f32_16x16x32_bf16 v[100:103], a[168:171], v[56:59], v[100:103]\n" \
"ds_read_b128 v[84:87], v2 offset:13312\n" \
"v_add_f32_e64 v123, v123, v127\n" \
"v_mfma_f32_16x16x32_bf16 v[104:107], v[168:171], v[56:59], v[104:107]\n" \
"v_add_f32_e64 v112, v112, v120\n" \
"v_mfma_f32_16x16x32_bf16 v[108:111], v[88:91], v[56:59], v[108:111]\n" \
"s_add_u32 s57, s56, s92\n" \
"buffer_load_dwordx4 v[56:59], v3, s[36:39], s57 offen offset:0 sc1\n" \
"v_add_f32 v113, v113, v121\n" \
"v_mfma_f32_16x16x32_bf16 v[96:99], a[44:47], v[60:63], v[96:99]\n" \
"v_cmp_gt_u32_e64 s[62:63], s59, v11\n" \
"v_add_f32 v114, v114, v122\n" \
"v_add_f32 v115, v115, v123\n" \
"v_mfma_f32_16x16x32_bf16 v[100:103], a[172:175], v[60:63], v[100:103]\n" \
"ds_read_b128 v[88:91], v2 offset:14336\n" \
"v_xor_b32_e64 v8, s84, v8\n" \
"v_mfma_f32_16x16x32_bf16 v[104:107], v[172:175], v[60:63], v[104:107]\n" \
"s_waitcnt vmcnt(14) lgkmcnt(3)\n" \
"v_mul_f32 v116, 0xbfb8aa3b, v112\n" \
"v_mfma_f32_16x16x32_bf16 v[108:111], v[92:95], v[60:63], v[108:111]\n" \
"buffer_load_dwordx4 v[60:63], v3, s[36:39], s57 offen offset:1024 sc1\n" \
"v_mul_f32 v117, 0xbfb8aa3b, v113\n" \
"v_mfma_f32_16x16x32_bf16 v[96:99], a[48:51], v[64:67], v[96:99]\n" \
"s_add_u32 s54, s55, 1\n" \
"s_cmp_lt_u32 s54, s46\n" \
"v_mul_f32 v118, 0xbfb8aa3b, v114\n" \
"v_mul_f32 v119, 0xbfb8aa3b, v115\n" \
"v_mfma_f32_16x16x32_bf16 v[100:103], a[176:179], v[64:67], v[100:103]\n" \
"ds_read_b128 v[92:95], v2 offset:15360\n" \
"s_cselect_b64 s[62:63], s[62:63], -1\n" \
"v_exp_f32_e64 v116, v116\n" \
"v_mfma_f32_16x16x32_bf16 v[104:107], v[176:179], v[64:67], v[104:107]\n" \
"s_waitcnt vmcnt(14) lgkmcnt(3)\n" \
"s_mul_i32 s83, s82, 114688\n" \
"v_exp_f32 v117, v117\n" \
"v_mfma_f32_16x16x32_bf16 v[108:111], v[80:83], v[64:67], v[108:111]\n" \
"buffer_load_dwordx4 v[64:67], v3, s[36:39], s57 offen offset:2048 sc1\n" \
"v_exp_f32_e64 v118, v118\n" \
"v_mfma_f32_16x16x32_bf16 v[96:99], a[52:55], v[68:71], v[96:99]\n" \
"s_cmp_lg_u64 s[62:63], 0\n" \
"s_cselect_b32 s81, 1, 0\n" \
"v_exp_f32_e64 v119, v119\n" \
"v_mfma_f32_16x16x32_bf16 v[100:103], a[180:183], v[68:71], v[100:103]\n" \
"ds_read_b128 v[80:83], v2 offset:16384\n" \
"s_cselect_b32 s83, 0x70000000, s83\n" \
"v_add_f32_e64 v116, 1.0, v116\n" \
"v_mfma_f32_16x16x32_bf16 v[104:107], v[180:183], v[68:71], v[104:107]\n" \
"s_waitcnt vmcnt(14) lgkmcnt(3)\n" \
"v_add_f32 v117, 1.0, v117\n" \
"v_mfma_f32_16x16x32_bf16 v[108:111], v[84:87], v[68:71], v[108:111]\n" \
"buffer_load_dwordx4 v[68:71], v3, s[36:39], s57 offen offset:3072 sc1\n" \
"v_add_f32_e64 v118, 1.0, v118\n" \
"v_mfma_f32_16x16x32_bf16 v[96:99], a[56:59], v[16:19], v[96:99]\n" \
"v_add_f32_e64 v119, 1.0, v119\n" \
"v_mfma_f32_16x16x32_bf16 v[100:103], a[184:187], v[16:19], v[100:103]\n" \
"ds_read_b128 v[84:87], v2 offset:17408\n" \
"v_rcp_f32_e64 v116, v116\n" \
"v_mfma_f32_16x16x32_bf16 v[104:107], v[184:187], v[16:19], v[104:107]\n" \
"s_waitcnt vmcnt(14) lgkmcnt(3)\n" \
"s_mov_b32 s56, s83\n" \
"v_rcp_f32_e64 v117, v117\n" \
"v_mfma_f32_16x16x32_bf16 v[108:111], v[88:91], v[16:19], v[108:111]\n" \
"s_add_u32 s57, s56, s86\n" \
"buffer_load_dwordx4 v[16:19], v3, s[36:39], s57 offen offset:0 sc1\n" \
"v_rcp_f32 v118, v118\n" \
"v_mfma_f32_16x16x32_bf16 v[96:99], a[60:63], v[20:23], v[96:99]\n" \
"buffer_load_dword v9, off, s[36:39], s97 sc1\n" \
"v_rcp_f32_e64 v119, v119\n" \
"v_mfma_f32_16x16x32_bf16 v[100:103], a[188:191], v[20:23], v[100:103]\n" \
"ds_read_b128 v[88:91], v2 offset:18432\n" \
"v_mul_f32_e64 v112, v112, v116\n" \
"v_mfma_f32_16x16x32_bf16 v[104:107], v[188:191], v[20:23], v[104:107]\n" \
"s_waitcnt vmcnt(15) lgkmcnt(3)\n" \
"v_mul_f32 v113, v113, v117\n" \
"v_mfma_f32_16x16x32_bf16 v[108:111], v[92:95], v[20:23], v[108:111]\n" \
"buffer_load_dwordx4 v[20:23], v3, s[36:39], s57 offen offset:1024 sc1\n" \
"v_mul_f32_e64 v114, v114, v118\n" \
"v_mfma_f32_16x16x32_bf16 v[96:99], a[64:67], v[24:27], v[96:99]\n" \
"v_mul_f32_e64 v115, v115, v119\n" \
"v_cvt_pk_bf16_f32 v0, v112, v113\n" \
"v_mfma_f32_16x16x32_bf16 v[100:103], a[192:195], v[24:27], v[100:103]\n" \
"ds_read_b128 v[92:95], v2 offset:19456\n" \
"v_cvt_pk_bf16_f32 v1, v114, v115\n" \
"v_mfma_f32_16x16x32_bf16 v[104:107], v[192:195], v[24:27], v[104:107]\n" \
"s_waitcnt vmcnt(15) lgkmcnt(3)\n" \
"v_mfma_f32_16x16x32_bf16 v[108:111], v[80:83], v[24:27], v[108:111]\n" \
"buffer_load_dwordx4 v[24:27], v3, s[36:39], s57 offen offset:2048 sc1\n" \
"buffer_store_dwordx2 v[0:1], v6, s[36:39], s76 offen sc1\n" \
"v_lshlrev_b32 v112, 16, v0\n" \
"v_mfma_f32_16x16x32_bf16 v[96:99], a[68:71], v[28:31], v[96:99]\n" \
"v_and_b32 v113, 0xffff0000, v0\n" \
"v_lshlrev_b32_e64 v114, 16, v1\n" \
"v_mfma_f32_16x16x32_bf16 v[100:103], a[196:199], v[28:31], v[100:103]\n" \
"ds_read_b128 v[80:83], v2 offset:20480\n" \
"v_and_b32 v115, 0xffff0000, v1\n" \
"v_mfma_f32_16x16x32_bf16 v[104:107], v[196:199], v[28:31], v[104:107]\n" \
"s_waitcnt vmcnt(16) lgkmcnt(3)\n" \
"v_mfma_f32_16x16x32_bf16 v[108:111], v[84:87], v[28:31], v[108:111]\n" \
"buffer_load_dwordx4 v[28:31], v3, s[36:39], s57 offen offset:3072 sc1\n" \
"v_mfma_f32_16x16x32_bf16 v[96:99], a[72:75], v[32:35], v[96:99]\n" \
"s_waitcnt vmcnt(15)\n" \
"v_lshlrev_b32_e64 v120, 16, v14\n" \
"v_and_b32 v121, 0xffff0000, v14\n" \
"v_mfma_f32_16x16x32_bf16 v[100:103], a[200:203], v[32:35], v[100:103]\n" \
"ds_read_b128 v[84:87], v2 offset:21504\n" \
"s_waitcnt vmcnt(14)\n" \
"v_lshlrev_b32 v124, 16, v12\n" \
"v_mfma_f32_16x16x32_bf16 v[104:107], v[200:203], v[32:35], v[104:107]\n" \
"s_waitcnt lgkmcnt(3)\n" \
"v_and_b32 v125, 0xffff0000, v12\n" \
"v_mfma_f32_16x16x32_bf16 v[108:111], v[88:91], v[32:35], v[108:111]\n" \
"s_add_u32 s57, s56, s87\n" \
"buffer_load_dwordx4 v[32:35], v3, s[36:39], s57 offen offset:0 sc1\n" \
"v_lshlrev_b32_e64 v122, 16, v15\n" \
"v_mfma_f32_16x16x32_bf16 v[96:99], a[76:79], v[36:39], v[96:99]\n" \
"v_and_b32 v123, 0xffff0000, v15\n" \
"v_lshlrev_b32_e64 v126, 16, v13\n" \
"v_mfma_f32_16x16x32_bf16 v[100:103], a[204:207], v[36:39], v[100:103]\n" \
"ds_read_b128 v[88:91], v2 offset:22528\n" \
"v_and_b32 v127, 0xffff0000, v13\n" \
"v_mfma_f32_16x16x32_bf16 v[104:107], v[204:207], v[36:39], v[104:107]\n" \
"s_waitcnt vmcnt(14) lgkmcnt(3)\n" \
"v_fmac_f32 v124, v112, v120\n" \
"v_mfma_f32_16x16x32_bf16 v[108:111], v[92:95], v[36:39], v[108:111]\n" \
"buffer_load_dwordx4 v[36:39], v3, s[36:39], s57 offen offset:1024 sc1\n" \
"v_fmac_f32_e64 v125, v113, v121\n" \
"v_mfma_f32_16x16x32_bf16 v[96:99], a[80:83], v[40:43], v[96:99]\n" \
"v_fmac_f32 v126, v114, v122\n" \
"v_fmac_f32 v127, v115, v123\n" \
"v_mfma_f32_16x16x32_bf16 v[100:103], a[208:211], v[40:43], v[100:103]\n" \
"ds_read_b128 v[92:95], v2 offset:23552\n" \
"v_cvt_pk_bf16_f32 v118, v124, v125\n" \
"v_mfma_f32_16x16x32_bf16 v[104:107], v[208:211], v[40:43], v[104:107]\n" \
"s_waitcnt vmcnt(14) lgkmcnt(3)\n" \
"v_cvt_pk_bf16_f32 v119, v126, v127\n" \
"v_mfma_f32_16x16x32_bf16 v[108:111], v[80:83], v[40:43], v[108:111]\n" \
"buffer_load_dwordx4 v[40:43], v3, s[36:39], s57 offen offset:2048 sc1\n" \
"v_mfma_f32_16x16x32_bf16 v[96:99], a[84:87], v[44:47], v[96:99]\n" \
"buffer_store_dwordx2 v[118:119], v5, s[68:71], s85 offen\n" \
"v_mov_b32 v10, 2\n" \
"v_mfma_f32_16x16x32_bf16 v[100:103], a[212:215], v[44:47], v[100:103]\n" \
"ds_read_b128 v[80:83], v2 offset:24576\n" \
"v_mfma_f32_16x16x32_bf16 v[104:107], v[212:215], v[44:47], v[104:107]\n" \
"s_waitcnt vmcnt(15) lgkmcnt(3)\n" \
"v_mfma_f32_16x16x32_bf16 v[108:111], v[84:87], v[44:47], v[108:111]\n" \
"buffer_load_dwordx4 v[44:47], v3, s[36:39], s57 offen offset:3072 sc1\n" \
"v_mfma_f32_16x16x32_bf16 v[96:99], a[88:91], v[48:51], v[96:99]\n" \
"v_mfma_f32_16x16x32_bf16 v[100:103], a[216:219], v[48:51], v[100:103]\n" \
"ds_read_b128 v[84:87], v2 offset:25600\n" \
"v_mfma_f32_16x16x32_bf16 v[104:107], v[216:219], v[48:51], v[104:107]\n" \
"s_waitcnt vmcnt(15) lgkmcnt(3)\n" \
"v_mfma_f32_16x16x32_bf16 v[108:111], v[88:91], v[48:51], v[108:111]\n" \
"s_add_u32 s57, s56, s88\n" \
"buffer_load_dwordx4 v[48:51], v3, s[36:39], s57 offen offset:0 sc1\n" \
"v_mfma_f32_16x16x32_bf16 v[96:99], a[92:95], v[52:55], v[96:99]\n" \
"v_mfma_f32_16x16x32_bf16 v[100:103], a[220:223], v[52:55], v[100:103]\n" \
"ds_read_b128 v[88:91], v2 offset:26624\n" \
"v_mfma_f32_16x16x32_bf16 v[104:107], v[220:223], v[52:55], v[104:107]\n" \
"s_waitcnt vmcnt(15) lgkmcnt(3)\n" \
"v_mfma_f32_16x16x32_bf16 v[108:111], v[92:95], v[52:55], v[108:111]\n" \
"buffer_load_dwordx4 v[52:55], v3, s[36:39], s57 offen offset:1024 sc1\n" \
"v_mfma_f32_16x16x32_bf16 v[96:99], a[96:99], v[56:59], v[96:99]\n" \
"v_mfma_f32_16x16x32_bf16 v[100:103], a[224:227], v[56:59], v[100:103]\n" \
"ds_read_b128 v[92:95], v2 offset:28672\n" \
"v_mfma_f32_16x16x32_bf16 v[104:107], v[224:227], v[56:59], v[104:107]\n" \
"s_waitcnt vmcnt(15) lgkmcnt(3)\n" \
"s_mov_b32 s84, 0x4000\n" \
"v_mfma_f32_16x16x32_bf16 v[108:111], v[80:83], v[56:59], v[108:111]\n" \
"buffer_load_dwordx4 v[56:59], v3, s[36:39], s57 offen offset:2048 sc1\n" \
"v_mfma_f32_16x16x32_bf16 v[96:99], a[100:103], v[60:63], v[96:99]\n" \
"buffer_load_dword v11, v4, s[36:39], 0 offen sc1\n" \
"v_mfma_f32_16x16x32_bf16 v[100:103], a[228:231], v[60:63], v[100:103]\n" \
"ds_read_b128 v[80:83], v2 offset:0\n" \
"v_mfma_f32_16x16x32_bf16 v[104:107], v[228:231], v[60:63], v[104:107]\n" \
"s_waitcnt vmcnt(16) lgkmcnt(3)\n" \
"v_mfma_f32_16x16x32_bf16 v[108:111], v[84:87], v[60:63], v[108:111]\n" \
"buffer_load_dwordx4 v[60:63], v3, s[36:39], s57 offen offset:3072 sc1\n" \
"v_mfma_f32_16x16x32_bf16 v[96:99], a[104:107], v[64:67], v[96:99]\n" \
"s_mov_b32 s99, s98\n" \
"s_mov_b32 s98, 0\n" \
"v_mfma_f32_16x16x32_bf16 v[100:103], a[232:235], v[64:67], v[100:103]\n" \
"ds_read_b128 v[84:87], v2 offset:1024\n" \
"v_mfma_f32_16x16x32_bf16 v[104:107], v[232:235], v[64:67], v[104:107]\n" \
"s_waitcnt vmcnt(16) lgkmcnt(3)\n" \
"s_waitcnt vmcnt(11)\n" \
"s_mov_b64 exec, 1\n" \
"buffer_atomic_or v10, off, s[36:39], s94 sc0\n" \
"s_mov_b64 exec, -1\n" \
"s_mov_b32 s95, s78\n" \
"v_mfma_f32_16x16x32_bf16 v[108:111], v[88:91], v[64:67], v[108:111]\n" \
"s_add_u32 s57, s56, s89\n" \
"buffer_load_dwordx4 v[64:67], v3, s[36:39], s57 offen offset:0 sc1\n" \
"v_mfma_f32_16x16x32_bf16 v[96:99], a[108:111], v[68:71], v[96:99]\n" \
"s_lshl_b32 s78, s44, 15\n" \
"s_mov_b32 s79, s97\n" \
"v_mfma_f32_16x16x32_bf16 v[100:103], a[236:239], v[68:71], v[100:103]\n" \
"ds_read_b128 v[88:91], v2 offset:2048\n" \
"s_mov_b32 s44, s82\n" \
"s_add_u32 s55, s55, 1\n" \
"v_mfma_f32_16x16x32_bf16 v[104:107], v[236:239], v[68:71], v[104:107]\n" \
"s_waitcnt lgkmcnt(3)\n" \
"ds_write_b128 v7, v[96:99] offset:0\n" \
"v_xor_b32 v7, 64, v7\n" \
"v_mfma_f32_16x16x32_bf16 v[108:111], v[92:95], v[68:71], v[108:111]\n" \
"buffer_load_dwordx4 v[68:71], v3, s[36:39], s57 offen offset:1024 sc1\n" \
"ds_write_b128 v7, v[100:103] offset:1024\n" \
"v_xor_b32 v7, 192, v7\n" \
"s_cmp_eq_u32 s81, 0\n" \
"s_nop 0\n" \
"s_cbranch_scc1 .Lsg2_loop_%=\n" \
"s_nop 7\n" \
"s_nop 7\n" \
"s_waitcnt vmcnt(0) lgkmcnt(0)\n" \
"ds_write_b128 v7, v[104:107] offset:2048\n" \
"v_xor_b32 v7, 64, v7\n" \
"ds_write_b128 v7, v[108:111] offset:3072\n" \
"v_xor_b32 v7, 64, v7\n" \
"s_waitcnt lgkmcnt(0)\n" \
"s_barrier\n" \
"s_cmp_eq_u32 s99, 0\n" \
"s_cselect_b32 s54, 0, s96\n" \
"s_mov_b32 s85, s77\n" \
"s_cmp_lg_u32 s54, 0\n" \
"s_cbranch_scc1 .Lsg2_f_%=\n" \
"buffer_load_dword v9, off, s[36:39], s97 sc1\n" \
"v_readfirstlane_b32 s54, v10\n" \
"s_nop 0\n" \
"s_and_b32 s54, s54, 1\n" \
"s_cbranch_scc1 .Lsg2_q_%=\n" \
".Lsg2_qk_%=:\n" \
"s_waitcnt vmcnt(0)\n" \
"v_readfirstlane_b32 s96, v9\n" \
"s_add_u32 s76, s78, 4194304\n" \
"s_add_u32 s80, s78, 0\n" \
"s_and_b32 s96, s96, 1\n" \
"s_cselect_b32 s76, 0x7ffffff0, s76\n" \
"s_cselect_b32 s77, s78, 0x7ffffff0\n" \
"s_cselect_b32 s80, s80, 0x7ffffff0\n" \
"s_cselect_b32 s94, 0x7ffffff0, s79\n" \
"s_or_b32 s76, s76, s98\n" \
"s_or_b32 s94, s94, s98\n" \
"s_or_b32 s85, s77, s98\n" \
"buffer_load_dwordx2 v[14:15], v6, s[36:39], s80 offen sc1\n" \
"buffer_load_dwordx2 v[12:13], v5, s[64:67], s77 offen\n" \
".Lsg2_f_%=:\n" \
"ds_read_b128 v[112:115], v8 offset:0\n" \
"ds_read_b128 v[116:119], v8 offset:4096\n" \
"ds_read_b128 v[120:123], v8 offset:8192\n" \
"ds_read_b128 v[124:127], v8 offset:12288\n" \
"s_waitcnt lgkmcnt(2)\n" \
"v_add_f32 v112, v112, v116\n" \
"v_add_f32 v113, v113, v117\n" \
"v_add_f32 v114, v114, v118\n" \
"v_add_f32 v115, v115, v119\n" \
"s_waitcnt lgkmcnt(0)\n" \
"v_add_f32 v120, v120, v124\n" \
"v_add_f32 v121, v121, v125\n" \
"v_add_f32 v122, v122, v126\n" \
"v_add_f32 v123, v123, v127\n" \
"v_add_f32 v112, v112, v120\n" \
"v_add_f32 v113, v113, v121\n" \
"v_add_f32 v114, v114, v122\n" \
"v_add_f32 v115, v115, v123\n" \
"v_xor_b32 v8, s84, v8\n" \
"v_mul_f32 v116, 0xbfb8aa3b, v112\n" \
"v_mul_f32 v117, 0xbfb8aa3b, v113\n" \
"v_mul_f32 v118, 0xbfb8aa3b, v114\n" \
"v_mul_f32 v119, 0xbfb8aa3b, v115\n" \
"v_exp_f32 v116, v116\n" \
"v_exp_f32 v117, v117\n" \
"v_exp_f32 v118, v118\n" \
"v_exp_f32 v119, v119\n" \
"v_add_f32 v116, 1.0, v116\n" \
"v_add_f32 v117, 1.0, v117\n" \
"v_add_f32 v118, 1.0, v118\n" \
"v_add_f32 v119, 1.0, v119\n" \
"v_rcp_f32 v116, v116\n" \
"v_rcp_f32 v117, v117\n" \
"v_rcp_f32 v118, v118\n" \
"v_rcp_f32 v119, v119\n" \
"v_mul_f32 v112, v112, v116\n" \
"v_mul_f32 v113, v113, v117\n" \
"v_mul_f32 v114, v114, v118\n" \
"v_mul_f32 v115, v115, v119\n" \
"v_cvt_pk_bf16_f32 v0, v112, v113\n" \
"v_cvt_pk_bf16_f32 v1, v114, v115\n" \
"buffer_store_dwordx2 v[0:1], v6, s[36:39], s76 offen sc1\n" \
"v_lshlrev_b32 v112, 16, v0\n" \
"v_and_b32 v113, 0xffff0000, v0\n" \
"v_lshlrev_b32 v114, 16, v1\n" \
"v_and_b32 v115, 0xffff0000, v1\n" \
"s_waitcnt vmcnt(2)\n" \
"v_lshlrev_b32 v120, 16, v14\n" \
"v_and_b32 v121, 0xffff0000, v14\n" \
"s_waitcnt vmcnt(1)\n" \
"v_lshlrev_b32 v124, 16, v12\n" \
"v_and_b32 v125, 0xffff0000, v12\n" \
"v_lshlrev_b32 v122, 16, v15\n" \
"v_and_b32 v123, 0xffff0000, v15\n" \
"v_lshlrev_b32 v126, 16, v13\n" \
"v_and_b32 v127, 0xffff0000, v13\n" \
"v_fmac_f32 v124, v112, v120\n" \
"v_fmac_f32 v125, v113, v121\n" \
"v_fmac_f32 v126, v114, v122\n" \
"v_fmac_f32 v127, v115, v123\n" \
"v_cvt_pk_bf16_f32 v118, v124, v125\n" \
"v_cvt_pk_bf16_f32 v119, v126, v127\n" \
"buffer_store_dwordx2 v[118:119], v5, s[68:71], s85 offen\n" \
"v_mov_b32 v10, 2\n" \
"v_xor_b32 v7, 0x4000, v7\n" \
"s_cmp_lg_u32 s96, 0\n" \
"s_cbranch_scc1 .Lsg2_g_%=\n" \
"s_waitcnt vmcnt(1)\n" \
"s_mov_b64 exec, 1\n" \
"buffer_atomic_or v10, off, s[36:39], s94 sc0\n" \
"s_mov_b64 exec, -1\n" \
"s_mov_b32 s95, s78\n" \
"s_waitcnt vmcnt(0)\n" \
"v_readfirstlane_b32 s54, v10\n" \
"s_nop 0\n" \
"s_and_b32 s54, s54, 1\n" \
"s_cbranch_scc1 .Lsg2_r_%=\n" \
".Lsg2_rk_%=:\n" \
".Lsg2_g_%=:\n" \
"s_cmp_lt_u32 s55, s46\n" \
"s_cbranch_scc1 .Lsg2_prime_%=\n" \
"s_branch .Lsg2_end_%=\n" \
".Lsg2_w_%=:\n" \
"s_nop 7\n" \
"s_nop 7\n" \
"s_waitcnt vmcnt(0) lgkmcnt(0)\n" \
"s_mov_b32 s61, 0\n" \
".Lsg2_wa_%=:\n" \
"s_cmp_lg_u32 s60, 0\n" \
"s_cbranch_scc1 .Lsg2_wk_%=\n" \
"buffer_load_dword v9, off, s[36:39], s97 sc1\n" \
"buffer_load_dword v11, v4, s[36:39], 0 offen sc1\n" \
"s_waitcnt vmcnt(0)\n" \
"v_cmp_gt_u32_e64 s[62:63], s59, v11\n" \
"s_nop 4\n" \
"s_cmp_eq_u64 s[62:63], 0\n" \
"s_cbranch_scc1 .Lsg2_wk_%=\n" \
"s_sleep 1\n" \
"s_add_u32 s61, s61, 1\n" \
"s_cmp_lt_u32 s61, 65536\n" \
"s_cbranch_scc1 .Lsg2_wa_%=\n" \
"s_mov_b32 s60, 1\n" \
"v_mov_b32 v11, 256\n" \
"v_mov_b32 v12, 1\n" \
"buffer_store_dword v12, v11, s[36:39], 0 offen sc1\n" \
"s_waitcnt vmcnt(0) lgkmcnt(0)\n" \
"s_branch .Lsg2_wk_%=\n" \
".Lsg2_p_%=:\n" \
"s_nop 7\n" \
"s_nop 7\n" \
"s_waitcnt vmcnt(0) lgkmcnt(0)\n" \
"s_add_u32 s54, s95, 0\n" \
"buffer_load_dwordx2 v[14:15], v6, s[36:39], s54 offen sc1\n" \
"buffer_load_dwordx2 v[12:13], v5, s[64:67], s95 offen\n" \
"v_lshlrev_b32 v112, 16, v0\n" \
"v_and_b32 v113, 0xffff0000, v0\n" \
"v_lshlrev_b32 v114, 16, v1\n" \
"v_and_b32 v115, 0xffff0000, v1\n" \
"s_waitcnt vmcnt(1)\n" \
"v_lshlrev_b32 v120, 16, v14\n" \
"v_and_b32 v121, 0xffff0000, v14\n" \
"s_waitcnt vmcnt(0)\n" \
"v_lshlrev_b32 v124, 16, v12\n" \
"v_and_b32 v125, 0xffff0000, v12\n" \
"v_lshlrev_b32 v122, 16, v15\n" \
"v_and_b32 v123, 0xffff0000, v15\n" \
"v_lshlrev_b32 v126, 16, v13\n" \
"v_and_b32 v127, 0xffff0000, v13\n" \
"v_fmac_f32 v124, v112, v120\n" \
"v_fmac_f32 v125, v113, v121\n" \
"v_fmac_f32 v126, v114, v122\n" \
"v_fmac_f32 v127, v115, v123\n" \
"v_cvt_pk_bf16_f32 v118, v124, v125\n" \
"v_cvt_pk_bf16_f32 v119, v126, v127\n" \
"buffer_store_dwordx2 v[118:119], v5, s[68:71], s95 offen\n" \
"s_waitcnt vmcnt(0) lgkmcnt(0)\n" \
"s_branch .Lsg2_pk_%=\n" \
".Lsg2_q_%=:\n" \
"s_nop 7\n" \
"s_nop 7\n" \
"s_waitcnt vmcnt(0) lgkmcnt(0)\n" \
"s_add_u32 s54, s95, 0\n" \
"buffer_load_dwordx2 v[14:15], v6, s[36:39], s54 offen sc1\n" \
"buffer_load_dwordx2 v[12:13], v5, s[64:67], s95 offen\n" \
"v_lshlrev_b32 v112, 16, v0\n" \
"v_and_b32 v113, 0xffff0000, v0\n" \
"v_lshlrev_b32 v114, 16, v1\n" \
"v_and_b32 v115, 0xffff0000, v1\n" \
"s_waitcnt vmcnt(1)\n" \
"v_lshlrev_b32 v120, 16, v14\n" \
"v_and_b32 v121, 0xffff0000, v14\n" \
"s_waitcnt vmcnt(0)\n" \
"v_lshlrev_b32 v124, 16, v12\n" \
"v_and_b32 v125, 0xffff0000, v12\n" \
"v_lshlrev_b32 v122, 16, v15\n" \
"v_and_b32 v123, 0xffff0000, v15\n" \
"v_lshlrev_b32 v126, 16, v13\n" \
"v_and_b32 v127, 0xffff0000, v13\n" \
"v_fmac_f32 v124, v112, v120\n" \
"v_fmac_f32 v125, v113, v121\n" \
"v_fmac_f32 v126, v114, v122\n" \
"v_fmac_f32 v127, v115, v123\n" \
"v_cvt_pk_bf16_f32 v118, v124, v125\n" \
"v_cvt_pk_bf16_f32 v119, v126, v127\n" \
"buffer_store_dwordx2 v[118:119], v5, s[68:71], s95 offen\n" \
"s_waitcnt vmcnt(0) lgkmcnt(0)\n" \
"s_branch .Lsg2_qk_%=\n" \
".Lsg2_r_%=:\n" \
"s_nop 7\n" \
"s_nop 7\n" \
"s_waitcnt vmcnt(0) lgkmcnt(0)\n" \
"s_add_u32 s54, s95, 0\n" \
"buffer_load_dwordx2 v[14:15], v6, s[36:39], s54 offen sc1\n" \
"buffer_load_dwordx2 v[12:13], v5, s[64:67], s95 offen\n" \
"v_lshlrev_b32 v112, 16, v0\n" \
"v_and_b32 v113, 0xffff0000, v0\n" \
"v_lshlrev_b32 v114, 16, v1\n" \
"v_and_b32 v115, 0xffff0000, v1\n" \
"s_waitcnt vmcnt(1)\n" \
"v_lshlrev_b32 v120, 16, v14\n" \
"v_and_b32 v121, 0xffff0000, v14\n" \
"s_waitcnt vmcnt(0)\n" \
"v_lshlrev_b32 v124, 16, v12\n" \
"v_and_b32 v125, 0xffff0000, v12\n" \
"v_lshlrev_b32 v122, 16, v15\n" \
"v_and_b32 v123, 0xffff0000, v15\n" \
"v_lshlrev_b32 v126, 16, v13\n" \
"v_and_b32 v127, 0xffff0000, v13\n" \
"v_fmac_f32 v124, v112, v120\n" \
"v_fmac_f32 v125, v113, v121\n" \
"v_fmac_f32 v126, v114, v122\n" \
"v_fmac_f32 v127, v115, v123\n" \
"v_cvt_pk_bf16_f32 v118, v124, v125\n" \
"v_cvt_pk_bf16_f32 v119, v126, v127\n" \
"buffer_store_dwordx2 v[118:119], v5, s[68:71], s95 offen\n" \
"s_waitcnt vmcnt(0) lgkmcnt(0)\n" \
"s_branch .Lsg2_rk_%=\n" \
".Lsg2_end_%=:\n" \
"s_endpgm\n"

typedef u32 v32u __attribute__((ext_vector_type(32)));
typedef float f32x2 __attribute__((ext_vector_type(2)));
__device__ __forceinline__ float xsum(float s) {
    auto a = __builtin_amdgcn_permlane16_swap(__builtin_bit_cast(u32, s), __builtin_bit_cast(u32, s), false, false);
    s = __builtin_bit_cast(float, (u32)a[0]) + __builtin_bit_cast(float, (u32)a[1]);
    auto b = __builtin_amdgcn_permlane32_swap(__builtin_bit_cast(u32, s), __builtin_bit_cast(u32, s), false, false);
    return __builtin_bit_cast(float, (u32)b[0]) + __builtin_bit_cast(float, (u32)b[1]);
}
__device__ __forceinline__ void ln_tile(const Params& p, const v4u (&xv)[8], int rt, int w, int lane) {
    const int n = lane & 15, g = lane >> 4;
    const rsrc_t rs = __builtin_amdgcn_make_buffer_rsrc(p.ws, 0, WS_END, 0x00020000);
    LAS float* red = (LAS float*)(uintptr_t)(136 * 1024);
    f32x2 xf[32], s2 = {0.f, 0.f};
#pragma unroll
    for (int i = 0; i < 32; ++i) { xf[i] = f32x2{bflo(xv[i >> 2][i & 3]), bfhi(xv[i >> 2][i & 3])}; s2 += xf[i]; }
    const float s = xsum(s2.x + s2.y);
    if (g == 0) red[n * 4 + w] = s;
    BAR();
    f32x4 t = *(LAS f32x4*)(red + n * 4);
    const float mean = ((t.x + t.y) + (t.z + t.w)) * (1.0f / DM);
    f32x2 q2 = {0.f, 0.f};
#pragma unroll
    for (int i = 0; i < 32; ++i) { const f32x2 a = xf[i] - mean; q2 += a * a; }
    const float q = xsum(q2.x + q2.y);
    if (g == 0) red[64 + n * 4 + w] = q;
    BAR();
    t = *(LAS f32x4*)(red + 64 + n * 4);
    const float var = ((t.x + t.y) + (t.z + t.w)) * (1.0f / DM);
    const float mb = rbf(mean);
    const float rd = 1.0f / rbf(sqrtf(rbf(rbf(var) + 1e-5f)));
#pragma unroll
    for (int i = 0; i < 8; ++i) {
        v4u o;
#pragma unroll
        for (int e = 0; e < 4; ++e) { const f32x2 a = xf[4 * i + e] - mb; const u32 d = pack2(a.x, a.y); const f32x2 y = f32x2{bflo(d), bfhi(d)} * rd; o[e] = pack2(y.x, y.y); }
        st128s(o, rs, WS_XF + rt * 32768 + (w * 8 + i) * 1024 + lane * 16);
    }
    WAIT_V0();
    __builtin_amdgcn_s_barrier(); CFENCE();
    if (w == 0 && lane == 0) __hip_atomic_fetch_add((GAS u32*)(p.ws + WS_CT) + CT_LN + (rt >> 4) * 32, 1u, RLXA);
}
__device__ __forceinline__ void ln_role(const Params& p, int c, int w) {
    const int lane = threadIdx.x & 63;
    const u16* xr = p.x + (size_t)(c * 16 + (lane & 15)) * DM + w * 256 + (lane >> 4) * 8;
    v4u xa[8], xb[8];
#pragma unroll
    for (int i = 0; i < 8; ++i) { xa[i] = *(const v4u*)(xr + i * 32); xb[i] = *(const v4u*)(xr + 128 * DM + i * 32); }
#pragma unroll 1
    for (int k = 0; k < 16; ++k) {
        v4u xc[8];
#pragma unroll
        for (int i = 0; i < 8; ++i) xc[i] = *(const v4u*)(xr + (size_t)min(k + 2, 15) * 128 * DM + i * 32);
        ln_tile(p, xa, k * 8 + c, w, lane);
#pragma unroll
        for (int i = 0; i < 8; ++i) { xa[i] = xb[i]; xb[i] = xc[i]; }
    }
}

__device__ __forceinline__ void p1_role(const Params& p, int b, int s0, int s1, int id, int w) {
    const int lane = __builtin_amdgcn_mbcnt_hi(~0u, __builtin_amdgcn_mbcnt_lo(~0u, 0u)), n = lane & 15, g = lane >> 4;
    const bool g1 = b >= 96;
    const int h = b & 31, kind = b >> 5;
    const int stride = g1 ? FF * 2 : 8192;
    const u16* src = g1 ? ((w >> 1) ? p.wu : p.wg) + (b - 96) * 64 : colsrc(p, kind == 0 ? 1 : (kind == 1 ? 0 : 2), 2 * h + (w >> 1));
    const v4u rs = mksrd(p.ws, WS_END), rw = mksrd(src, 1023 * stride + 128);
    const int v0 = lane * 16;
    const int v1 = (8 * (w & 1) + (lane >> 3)) * stride + ((lane & 7) ^ ((((lane >> 4) & 1) | ((w & 1) << 1)) << 1)) * 16;
    const int swz = ((n >> 3) | ((g & 1) << 1)), rowo = (8 * g + (n >> 2)) * 128 + (n & 3) * 8;
    const int v2 = (g1 ? 0 : (w >> 1) * 8192) + rowo + (((g1 ? w : 2 * (w & 1)) ^ swz) * 32);
    const int v3 = (g1 ? 8192 : (w >> 1) * 8192) + rowo + (((g1 ? w : 2 * (w & 1) + 1) ^ swz) * 32);
    const int v4 = WS_XF + (w >> 1) * 32768 + (w & 1) * 8192 + lane * 16;
    int v5, ostr, od;
    if (g1) { v5 = WS_HF + (g & 1) * (112 * 1024) + ((b - 96) * 2 + (w >> 1)) * 1024 + (2 * (w & 1) + (g >> 1)) * 256 + n * 16; ostr = 2 * 112 * 1024; od = 0; }
    else if (kind == 2) { v5 = WS_TK + h * TK_STR + 16384 + w * 2048 + lane * 16; ostr = 24576; od = 1024; }
    else { v5 = WS_TK + h * TK_STR + (kind == 0 ? 0 : 8192) + w * 1024 + lane * 16; ostr = 24576; od = 4096; }
    const int v6 = (w == 0 && lane == 0) ? WS_CT + (g1 ? CT_G1 + (b - 96) * 32 : CT_P1 + h * 32 + kind * 4) * 4 : OOB;
#define P1_IN "{v0}"(v0), "{v1}"(v1), "{v2}"(v2), "{v3}"(v3), "{v4}"(v4), "{s[36:39]}"(rs), "{s[40:43]}"(rw), "{s44}"(16 * stride), "{s46}"(s0), "{s47}"(s1), "{s48}"(w), "{s49}"(ostr), "{s50}"(od), "{s51}"(rfl(kind == 1 ? 0x3e0293ee : 0x3f800000))
    int a5 = v5, a6 = v6; u32 tc, tr;
    if (g1) asm volatile(ASM_P1_G1 : "+{v5}"(a5), "+{v6}"(a6), "={s70}"(tc), "={s72}"(tr) : P1_IN : CLOB_P1);
    else if (kind == 2) asm volatile(ASM_P1_V : "+{v5}"(a5), "+{v6}"(a6), "={s70}"(tc), "={s72}"(tr) : P1_IN : CLOB_P1);
    else asm volatile(ASM_P1_QK : "+{v5}"(a5), "+{v6}"(a6), "={s70}"(tc), "={s72}"(tr) : P1_IN : CLOB_P1);
}

#define TUNEV(i, c) (c)
#define C_X 108
#define C_Y 80
#define C_DS 30
#define C_VD 12
#define C_YL 9
#define C_LT -50
#define C_LA 0
#define C_PW3 2
#define C_GD 0
#define C_OM 0
__device__ __forceinline__ int mlane(int n, int g) { return (g >> 1) * 512 + (n >> 3) * 256 + (g & 1) * 128 + (n & 7) * 16; }
__device__ __forceinline__ int mq(int j) { return ((j & 1) ? 0x4800 : 0) ^ ((j & 2) ? 0x800 : 0); }
__device__ __forceinline__ int hword(int i, int cls) { return WS_M + (cls ^ 0x1000 ^ ((i & 32) ? 0x4800 : 0)) + ((i >> 4) & 1) * 32768 + (i & 15) * 128; }
__device__ __forceinline__ int oblk(const Params& p, int rg, int q, int cls) { const int m = TUNEV(8, C_OM); return m == 0 ? rg * 32768 + q * 8192 : (q * 2 + (rg ^ ((cls >> 14) & 1) ^ (m & 1))) * 8192; }
__device__ __forceinline__ void qk_role(const Params& p, int h, int par, int rot) {
    const int lane = __builtin_amdgcn_mbcnt_hi(~0u, __builtin_amdgcn_mbcnt_lo(~0u, 0u)), w = rfl(threadIdx.x >> 6), n = lane & 15, g = lane >> 4;
    const v4u rs = mksrd(p.ws, WS_END), rw = mksrd(colsrc(p, w >> 1, 2 * h + (w & 1)), 1023 * 8192 + 128);
    const int l16 = lane * 16, v1 = l16 + w * 4096, v2 = v1 + 65536, v3 = l16 + 112 * 1024;
    const int v4 = WS_M + (w >> 1) * 32768 + (w & 1) * 1024 + mlane(n, g);
    const int v5 = 152 * 1024 + (32 * w + n) * 32 + (g ^ (n >> 2)) * 8;
    const int v6 = 152 * 1024 + (4 * g + (n >> 2)) * 32 + ((n & 3) ^ g) * 8;
    const int v15 = w == 0 ? min(l16, 64) : OOB;
    const int v7 = (n & 2) == 0 ? WS_O + amap(oblk(p, n >> 3, h >> 3, par) + (h & 7) * 1024 + w * 256 + (g >> 1) * 128 + ((n >> 2) & 1) * 64 + (g & 1) * 32 + (n & 1) * 16, par) : OOB;
    const int v8 = WS_V + amap(h * 2048 + w * 512 + lane * 4, par);
    const int v9 = (w == 0 && lane < 32) ? hword(lane, par) : OOB;
    const int v10 = (w == 0 && lane == 0) ? 0 : OOB;
    const int v11 = WS_OF + h * OF_STR + (w & 1) * 4096 + w * 1024 + l16;
    const int v12 = (n & 2) ? 0xEEEE : 0x4444;
    const int v14 = 16 * (w & 1) + n - 4 * g;
    const int r8 = lane >> 3;
    const int v32 = r8 * 8192 + ((lane & 7) ^ (((r8 >> 1) & 1) << 1)) * 16, v33 = (8 + r8) * 8192 + ((lane & 7) ^ ((((r8 >> 1) & 1) | 2) << 1)) * 16;
    const int swz = (n >> 3) | ((g & 1) << 1), rowo = (w >> 1) * 4096 + (8 * g + (n >> 2)) * 128 + (n & 3) * 8;
    const int v34 = rowo + ((2 * (w & 1)) ^ swz) * 32, v35 = rowo + ((2 * (w & 1) + 1) ^ swz) * 32;
    asm volatile(ASM_QK :: "{v1}"(v1), "{v2}"(v2), "{v3}"(v3), "{v4}"(v4), "{v5}"(v5), "{v6}"(v6), "{v7}"(v7), "{v8}"(v8), "{v9}"(v9), "{v10}"(v10), "{v11}"(v11), "{v12}"(v12),
        "{v14}"(v14), "{v15}"(v15), "{v[32:35]}"(v4u{(u32)v32, (u32)v33, (u32)v34, (u32)v35}), "{s[36:39]}"(rs), "{s[40:43]}"(rw), "{s[44:45]}"(v2u{(u32)TUNEV(0, C_MO), (u32)TUNEV(1, C_MM)}), "{s46}"(TUNEV(2, C_X)), "{s17}"(TUNEV(3, C_Y)), "{s[12:15]}"(v4u{(u32)TUNEV(10, C_LT), (u32)TUNEV(11, C_LA), (u32)TUNEV(4, C_DS), (u32)rot << 21}), "{s10}"(par ^ mq(rot)), "{s47}"(WS_DBG + 131072 + (h * 4 + w) * 2048), "{s48}"(w),
        "{s63}"(h * TK_STR), "{s80}"(WS_CT + (CT_P1 + h * 32) * 4), "{s87}"(WS_CT + (CT_AT + h * 32) * 4), "{s94}"(hword(h, par)) : "v255", "a255", "s99", "memory");
    __builtin_unreachable();
}
__device__ __forceinline__ void vo_role(const Params& p, int c, int par) {
    const int lane = __builtin_amdgcn_mbcnt_hi(~0u, __builtin_amdgcn_mbcnt_lo(~0u, 0u)), w = rfl(threadIdx.x >> 6), n = lane & 15, g = lane >> 4;
    const int rg = RGX ? c >> 5 : (c >> 4) & 1, cs = RGX ? c & 31 : (c & 15) + 16 * (c >> 5);
    const v4u rs = mksrd(p.ws, WS_END), rw = mksrd(colsrc(p, 2, c) + (size_t)(256 * w) * 4096, 255 * 8192 + 128), ro = mksrd(colsrc(p, 3, cs >> 1) + (size_t)(1024 * w) * 1024 + (cs & 1) * 32, 1023 * 2048 + 64), rm = mksrd(p.mem0, 16384);
    const int v0 = lane * 16, v1 = (lane >> 3) * 8192 + (lane & 7) * 16, v2 = (lane >> 2) * 2048 + (lane & 3) * 16;
    const int v3 = (8 * g + (n >> 2)) * 128 + w * 32 + (n & 3) * 8;
    const int v4 = w * 40960 + (16 * (g & 1) + 8 * (g >> 1) + 4 * (n >> 3) + ((n >> 2) & 1)) * 64 + (n & 3) * 8;
    const int v5 = WS_M + (w >> 1) * 32768 + (w & 1) * 1024 + mlane(n, g);
    const int v6 = WS_O + amap(oblk(p, rg, w, par) + (2 * (n >> 3) + (g >> 1)) * 256 + (g & 1) * 128 + (n & 7) * 16, par);
    const int v7 = WS_V + amap(c * 1024 + w * 256 + lane * 4, par);
    const int v8 = (w == 0 && (lane & 3) == 0) ? WS_M + (par ^ mq(cs >> 3)) + ((cs >> 2) & 1) * 32768 + ((cs & 3) * 2 + rg) * 256 + lane * 4 : OOB;
    const int v9 = (w == 0 && lane < 32) ? hword(lane, par) : OOB;
    const int v12 = (n & 2) ? 0xEEEE : 0x4444;
    const int v13 = (4 * rg + 2 * ((lane >> 4) & 1) + ((lane >> 2) & 1)) * 2048 + (32 * cs + 16 * (lane >> 5) + 2 * ((lane >> 3) & 1)) * 2;
    const int v15 = 16384 + (g * 16 + ((n >> 1) & 1) * 8 + (n >> 2)) * 32 + w * 8 + (n & 1) * 4;
    asm volatile(ASM_VO :: "{v0}"(v0), "{v1}"(v1), "{v2}"(v2), "{v3}"(v3), "{v4}"(v4), "{v5}"(v5), "{v6}"(v6), "{v7}"(v7), "{v8}"(v8), "{v9}"(v9), "{v12}"(v12),
        "{v13}"(v13), "{v15}"(v15), "{v28}"((w == 0 && lane == 0) ? WS_CT + (CT_P1 + (c >> 1) * 32 + 12 + (c & 1) * 4) * 4 : OOB), "{s[36:39]}"(rs), "{s[40:43]}"(rw), "{s[64:67]}"(ro), "{s[68:71]}"(rm), "{s[44:45]}"(v2u{(u32)TUNEV(0, C_MO), (u32)TUNEV(1, C_MM)}), "{s46}"(TUNEV(12, C_VD)), "{s17}"(TUNEV(3, C_Y)),
        "{s63}"(par), "{s74}"(cs >= 16 ? TUNEV(15, C_YL) : 0), "{s47}"(WS_DBG + 131072 + ((32 + c) * 4 + w) * 2048), "{s48}"(w) : "v255", "a255", "s99", "memory");
    __builtin_unreachable();
}
__device__ __forceinline__ void copy_wd(const Params& p, int u) {
    const int lane = threadIdx.x & 63;
    const rsrc_t rs = __builtin_amdgcn_make_buffer_rsrc(p.ws + WS_WD, 0, FF * WD_STR, 0x00020000);
#pragma unroll 1
    for (int k = 0; k < 14; ++k) {
        const int row = (k * 32 + u) * 8;
        v4u t[16];
#pragma unroll
        for (int i = 0; i < 16; ++i) t[i] = __builtin_nontemporal_load((const v4u*)(p.wd + (size_t)row * 1024 + i * 512 + lane * 8));
#pragma unroll
        for (int i = 0; i < 16; ++i) st128s(t[i], rs, (row + (i >> 1)) * WD_STR + (i & 1) * 1024 + lane * 16);
        for (int i = TUNEV(7, C_GD); i > 0; --i) __builtin_amdgcn_s_sleep(8);
    }
    WAIT_V0();
    if (lane == 0) __hip_atomic_fetch_add((GAS u32*)(p.ws + WS_CT) + CT_WD, 1u, RLXA);
}
__device__ __forceinline__ void s64_role(const Params& p, int kind, int cb, int r, int id, int w) {
    const int lane = __builtin_amdgcn_mbcnt_hi(~0u, __builtin_amdgcn_mbcnt_lo(~0u, 0u)), n = lane & 15, g = lane >> 4;
    const int kr = kind ? 896 : 1024, wst = kind ? WD_STR : 2048;
    if (kind) {
        GAS u32* ct = (GAS u32*)(p.ws + WS_CT);
        for (u32 spins = 0; (u32)rfl(__hip_atomic_load(ct + CT_WD, RLXA)) < 32u; ++spins) {
            if (spins > (1u << 18)) { __hip_atomic_store(ct + CT_TMO, 1u, RLXA); break; }
            __builtin_amdgcn_s_sleep(8);
        }
        CFENCE();
    }
    const v4u rs = mksrd(p.ws, WS_END), rw = mksrd(kind ? p.ws + WS_WD + cb * 128 + (size_t)(kr * w) * WD_STR : (const u8*)(colsrc(p, 3, cb) + (size_t)(kr * w) * 1024), (kr - 1) * wst + 128), rx = mksrd(p.x, 2048 * 2048), ro = mksrd(p.out, 2048 * 2048);
    const int v2 = lane * 16 + w * 32768, v0 = (lane >> 3) * wst + (lane & 7) * 16;
    const int v1 = w * 32768 + ((kind ? 8 : 4) * g + (n >> 2)) * 128 + (n & 3) * 8;
    const int v3 = (kind ? WS_HF + w * 28 * 1024 : WS_OF + 8 * w * OF_STR) + lane * 16;
    const int v4 = WS_CT + (kind ? CT_G1 + (14 * w + min(n, 13)) * 32 : CT_AT + (8 * w + (n & 7)) * 32) * 4;
    const int v5 = (4 * w + g) * 2048 + cb * 128 + n * 8, v6 = WS_GT + cb * 2048 + w * 512 + lane * 8;
    const int v7 = 131072 + w * 4096 + g * 256 + (n ^ g ^ 8) * 16, v8 = 131072 + n * 256 + ((4 * w + g) ^ n) * 16;
    const int ha = TUNEV(6, S64_HEAD);
    const int nt = kind ? (r < 2 ? 22 : 21) : (128 - ha) / 4 + (r < 2 ? ha / 2 : 0), rt = kind ? r : (r < 2 ? r : ha + r);
#define S64_IO "{v7}"(v7), "{v8}"(v8), "{s44}"(rt), "{v0}"(v0), "{v1}"(v1), "{v2}"(v2), "{v3}"(v3), "{v4}"(v4), "{v5}"(v5), "{v6}"(v6), "{s[36:39]}"(rs), "{s[40:43]}"(rw), "{s[64:67]}"(rx), "{s[68:71]}"(ro), \
        "{s45}"(2), "{s47}"(kind ? 6 : 4), "{s50}"(kind ? -128 : ha), "{s46}"(nt), "{s48}"(w), "{s49}"(WS_CT + CT_S * 4 + (cb * 4 + w) * 512), "{s51}"(w ? OOB : WS_DBG + id * 128), "{s99}"(kind == 0 ? TUNEV(9, C_PW3) : TUNEV(5, C_PW)), "{s85}"(kind ? cb % 7 : cb & 7)
    if (kind) asm volatile(ASM_S64_G2 :: S64_IO : "v255", "a255", "memory");
    else asm volatile(ASM_S64_WO :: S64_IO : "v255", "a255", "memory");
    __builtin_unreachable();
}
#define MAIN_BODY \
int gx = (id >> 5) - 4, gr = id & 31; \
    if (id < 128) { \
        const int x = id >> 5, t = id & 31; \
        const int nq = PLACE == 0 ? (x < 2 ? 16 : 0) : (PLACE == 1 ? 8 : (x < 2 ? 16 : 0)), nv = PLACE == 2 ? (x < 2 ? 16 : (x == 2 ? 32 : 0)) : 16; \
        if (t < nq) qk_role(p, nq * x + t, par, x & TUNEV(14, 1)); \
        else if (t < nq + nv) vo_role(p, 16 * x + t - nq, par); \
        gx = -1; gr = PLACE == 0 ? (x - 2) * 16 + t - 16 : (PLACE == 1 ? x * 8 + t - 24 : t); \
        id = 96 + gr; \
        STAMP(0); \
    } \
    if (gx < 0 || gr < 30) p1_role(p, gx < 0 ? 64 + gr : (gr < 16 ? gx * 8 + (gr & 7) + (gr >> 3) * 32 : 96 + gx * 14 + gr - 16), 0, 64, id, wv_); \
    else { ln_role(p, gx * 2 + gr - 30, wv_); copy_wd(p, (gx * 2 + gr - 30) * 4 + wv_); } \
    STAMP(2); \
    s64_role(p, gx < 1 ? 0 : 1, gr & 15, (gx < 1 ? 2 * gx + 2 : 2 * gx - 2) + (gr >> 4), id, wv_);
constexpr int LDS_BYTES = 160 * 1024;
__device__ __forceinline__ int take_role(const Params& p, LAS u32* L) {
    if (threadIdx.x == 0) {
        GAS u32* tk = (GAS u32*)(p.ws + WS_CT) + CT_TICK;
        const u32 x = xcc_id() & 7u;
        const u32 t = __hip_atomic_fetch_add(tk + 64 * x, 1u, RLXA);
        u32 id = 32 * x + t;
        if (t >= 32u) {
            u32 o = __hip_atomic_fetch_add(tk + 64 * 8, 1u, RLXA);
            for (u32 spins = 0; spins < (1u << 20); ++spins) {
                u32 tot = 0;
                for (int k = 0; k < 8; ++k) tot += __hip_atomic_load(tk + 64 * k, RLXA);
                if (tot >= 256u) break;
                __builtin_amdgcn_s_sleep(8);
            }
            id = 255u;
            for (int k = 0; k < 8; ++k) {
                const u32 ck = __hip_atomic_load(tk + 64 * k, RLXA);
                const u32 fr = ck < 32u ? 32u - ck : 0u;
                if (o < fr) { id = 32 * k + ck + o; break; }
                o -= fr;
            }
        }
        *L = id;
    }
    BAR();
    const int id = rfl(*L);
    BAR();
    return id;
}
#define STAMP(i) do {} while (0)
__global__ void __launch_bounds__(256, 1) k_main(Params p) {
    extern __shared__ __attribute__((aligned(16))) u8 smem[];
    int par = rfl(*(const GAS u32*)(p.ws + WS_CT + CT_PAR * 4)) & 1;
    par = (par << 13) | ((TUNEV(13, 0) & 3) << 11) | ((TUNEV(13, 0) & 4) << 12);
    int id = take_role(p, (LAS u32*)smem);
    const int wv_ = rfl(threadIdx.x >> 6);
    STAMP(0);
    MAIN_BODY
    STAMP(7);
}
__global__ void __launch_bounds__(256) k_zero(Params p) {
    __shared__ u32 vote[4];
    u8* ws = p.ws;
    v4u z = {0u, 0u, 0u, 0u};
    if (WARM && blockIdx.x != 0) {
        const int i = (blockIdx.x - 1) * 32 + (threadIdx.x >> 3), m2 = min(i & 7, 2);
        v4u t[5];
#pragma unroll
        for (int m = 0; m < 4; ++m) t[m] = __builtin_nontemporal_load((const v4u*)(wmat(p, m) + (size_t)i * 512 + slowpos(wmat(p, m)) * 64 + (threadIdx.x & 7) * 8));
        if ((i & 7) < 3) t[4] = __builtin_nontemporal_load((const v4u*)(wmat(p, m2) + (size_t)(i >> 3) * 4096 + slowpos2(wmat(p, m2)) * 64 + (threadIdx.x & 7) * 8));
#pragma unroll
        for (int m = 0; m < 4; ++m) *(v4u*)(ws + WS_WC + (size_t)i * 1024 + (m == 3 ? 4 : m) * 128 + (threadIdx.x & 7) * 16) = t[m];
        if ((i & 7) < 3) *(v4u*)(ws + WS_WC + (size_t)(i >> 3) * 8192 + (5 + m2) * 128 + (threadIdx.x & 7) * 16) = t[4];
    }
    if (blockIdx.x == 0) {
        const rsrc_t rs = __builtin_amdgcn_make_buffer_rsrc(ws, 0, 2 * MB, 0x00020000);
        const int lane = threadIdx.x & 63, w = threadIdx.x >> 6, base = 2 * MB - 32768 + (w & 1) * 16384 + (w >> 1) * 2048 + (lane & 31) * 4;
        st32s(0u, rs, base + 1024); st32s(0u, rs, base + 8192 + 1024);
        u32 a = 65535u, b = 65535u;
#pragma unroll 1
        for (int k = 0; k < 8; ++k) {
            u64 t0, t1, t2;
            asm volatile("s_waitcnt vmcnt(0)\n\ts_memtime %0\n\ts_waitcnt lgkmcnt(0)" : "=s"(t0) :: "memory");
            st32s(0u, rs, base);
            asm volatile("s_waitcnt vmcnt(0)\n\ts_memtime %0\n\ts_waitcnt lgkmcnt(0)" : "=s"(t1) :: "memory");
            st32s(0u, rs, base + 8192);
            asm volatile("s_waitcnt vmcnt(0)\n\ts_memtime %0\n\ts_waitcnt lgkmcnt(0)" : "=s"(t2) :: "memory");
            a = min(a, (u32)(t1 - t0)); b = min(b, (u32)(t2 - t1));
            if (4 * max(a, b) > 5 * min(a, b) && min(a, b) < 600u && max(a, b) < 1000u) break;
        }
        vote[w] = (a << 16) | b;
        __syncthreads();
        if (threadIdx.x == 0) {
            for (int k = 0; k < 4; ++k) { a = min(a, vote[k] >> 16); b = min(b, vote[k] & 65535u); }
            z.x = (b < a ? 1u : 0u) ^ (xcc_id() >= 4 ? 1u : 0u);
        }
    }
    if (blockIdx.x < 32) ((v4u*)(ws + (blockIdx.x >> 4) * WS_M))[(blockIdx.x & 15) * 256 + threadIdx.x] = z;
}

extern "C" void kernel_launch(void* const* d_in, const int* in_sizes, int n_in, void* d_out, int out_size, void* d_ws, size_t ws_size, hipStream_t stream) {
    Params p;
    p.x = (const u16*)d_in[0]; p.mem0 = (const u16*)d_in[1]; p.wq = (const u16*)d_in[2]; p.wk = (const u16*)d_in[3]; p.wv = (const u16*)d_in[4]; p.wo = (const u16*)d_in[5];
    p.wg = (const u16*)d_in[8]; p.wu = (const u16*)d_in[9]; p.wd = (const u16*)d_in[10];
    p.out = (u16*)d_out;
    p.ws = (u8*)(((uintptr_t)d_ws + (2u * MB - 1)) & ~(uintptr_t)(2u * MB - 1));
    (void)hipFuncSetAttribute((const void*)k_main, hipFuncAttributeMaxDynamicSharedMemorySize, LDS_BYTES);
    hipLaunchKernelGGL(k_zero, dim3(257), dim3(256), 0, stream, p);
    hipLaunchKernelGGL(k_main, dim3(256), dim3(256), LDS_BYTES, stream, p);
}
```

```cpp
#pragma clang diagnostic ignored "-Wint-to-pointer-cast"
#pragma clang diagnostic ignored "-Winline-asm"
#include <hip/hip_runtime.h>
#include <hip/hip_bf16.h>
#include <stdint.h>

#define LAS __attribute__((address_space(3)))
#define GAS __attribute__((address_space(1)))
typedef unsigned u32;
typedef unsigned char u8;
typedef unsigned short u16;
typedef unsigned long long u64;
typedef u32 v2u __attribute__((ext_vector_type(2)));
typedef u32 v4u __attribute__((ext_vector_type(4)));
typedef float f32x4 __attribute__((ext_vector_type(4)));
typedef __bf16 bf16x2 __attribute__((ext_vector_type(2)));
typedef __amdgpu_buffer_rsrc_t rsrc_t;

constexpr int NSEG = 64, DM = 1024, NH = 32, FF = 3584;
constexpr int MB = 1 << 20;
constexpr int WS_CT = 0;
constexpr int WS_M = 262144;
constexpr int WS_O = 524288, WS_V = 1048576;
constexpr int WS_DBG = 4 * MB;
constexpr int WS_XF = 8 * MB;
constexpr int WS_HF = 32 * MB;
constexpr int WS_GT = 16 * MB;
constexpr int WS_TK = 64 * MB;
constexpr int WS_END = 192 * MB;
__device__ __forceinline__ int farx(int off) { return off ^ ((off >> 8) & 0x2000); }
constexpr int CT_PAR = 0, CT_TMO = 64, CT_TICK = 256;
constexpr int CT_LN = 1024;
constexpr int CT_WD = 1536;
constexpr int CT_P1 = 2048;
constexpr int CT_G1 = 4096;
constexpr int CT_AT = 3072;
constexpr int CT_S = 8192;
#define PLACE 2
#define RGX 0
#define ZAUX 16
#define ZR 8
#define WARM 1
#define S64_HEAD 24
#define S64_R0 108
constexpr int OOB = 0x7ffffff0;
constexpr int AUX_SC1 = 16;

struct Params {
    const u16 *x, *mem0, *wq, *wk, *wv, *wo, *wg, *wu, *wd;
    u16* out;
    u8* ws;
};
__device__ __forceinline__ u32 pack2(float a, float b) { bf16x2 v = {(__bf16)a, (__bf16)b}; return __builtin_bit_cast(u32, v); }
__device__ __forceinline__ float bflo(u32 d) { return __builtin_bit_cast(float, d << 16); }
__device__ __forceinline__ float bfhi(u32 d) { return __builtin_bit_cast(float, d & 0xFFFF0000u); }
__device__ __forceinline__ float rbf(float a) { return (float)(__bf16)a; }
__device__ __forceinline__ void st128s(v4u v, rsrc_t rs, int voff) { __builtin_amdgcn_raw_buffer_store_b128(v, rs, voff, 0, AUX_SC1); }
__device__ __forceinline__ void st32s(u32 v, rsrc_t rs, int voff) { __builtin_amdgcn_raw_buffer_store_b32(v, rs, voff, 0, AUX_SC1); }
__device__ __forceinline__ u32 ld32s(rsrc_t rs, int voff) { return __builtin_amdgcn_raw_buffer_load_b32(rs, voff, 0, AUX_SC1); }
__device__ __forceinline__ u32 xcc_id() { return (u32)__builtin_amdgcn_s_getreg(20 | (3 << 11)) & 0xfu; }
__device__ __forceinline__ int rfl(int x) { return __builtin_amdgcn_readfirstlane(x); }
__device__ __forceinline__ v4u mksrd(const void* p, u32 bytes) {
    const u64 a = (u64)p;
    return v4u{(u32)rfl((u32)a), (u32)rfl((u32)(a >> 32) & 0xFFFFu), (u32)rfl(bytes), 0x00020000u};
}
#define CFENCE() asm volatile("" ::: "memory")
#define WAIT_V0() asm volatile("s_waitcnt vmcnt(0)" ::: "memory")
#define BAR() do { asm volatile("s_waitcnt lgkmcnt(0)" ::: "memory"); __builtin_amdgcn_s_barrier(); asm volatile("" ::: "memory"); } while (0)
#define RLXA __ATOMIC_RELAXED, __HIP_MEMORY_SCOPE_AGENT
__device__ __forceinline__ int amap(int off, int cls) { return ((off >> 13) << 14) + (off & 8191) + (cls & 8192); }

constexpr int WS_WC = 208 * MB;
constexpr int WS_WG = 216 * MB;
constexpr int WS_WD = 192 * MB;
__device__ __forceinline__ const u16* wmat(const Params& p, int m) { return m == 0 ? p.wq : (m == 1 ? p.wk : (m == 2 ? p.wv : p.wo)); }
__device__ __forceinline__ int slowpos(const void* b) { return (3 - (int)((u64)b >> 7)) & 7; }
__device__ __forceinline__ int slowpos2(const void* b) { return (63 - (int)((u64)b >> 7)) & 63; }
__device__ __forceinline__ const u16* colsrc(const Params& p, int m, int j) {
    const u16* b = wmat(p, m);
    if (WARM && m < 3 && j == slowpos2(b)) return (const u16*)(p.ws + WS_WC) + (5 + m) * 64;
    return (WARM && (j & 7) == slowpos(b)) ? (const u16*)(p.ws + WS_WC) + (j >> 3) * 512 + (m == 3 ? 4 : m) * 64 : b + j * 64;
}
__device__ __forceinline__ const u16* colsrc2(const Params& p, int m, int j) {
    const u16* b = m ? p.wu : p.wg;
    return (WARM > 1 && (j & 7) == slowpos(b)) ? (const u16*)(p.ws + WS_WG) + (j >> 3) * 512 + m * 64 : b + j * 64;
}
#define ASM_P1_QK \
"s_nop 7\n" \
"s_nop 7\n" \
"s_lshr_b32 s52, s48, 1\n" \
"s_lshl_b32 s52, s52, 13\n" \
"s_and_b32 s53, s48, 1\n" \
"s_lshl_b32 s53, s53, 10\n" \
"s_add_u32 s52, s52, s53\n" \
"s_mov_b32 s54, 0\n" \
"s_add_u32 m0, s52, 0\n" \
"s_nop 0\n" \
"buffer_load_dwordx4 v1, s[40:43], s54 offen nt lds\n" \
"s_add_u32 s54, s54, s44\n" \
"s_add_u32 m0, s52, 2048\n" \
"s_nop 0\n" \
"buffer_load_dwordx4 v1, s[40:43], s54 offen nt lds\n" \
"s_add_u32 s54, s54, s44\n" \
"s_add_u32 m0, s52, 4096\n" \
"s_nop 0\n" \
"buffer_load_dwordx4 v1, s[40:43], s54 offen nt lds\n" \
"s_add_u32 s54, s54, s44\n" \
"s_add_u32 m0, s52, 6144\n" \
"s_nop 0\n" \
"buffer_load_dwordx4 v1, s[40:43], s54 offen nt lds\n" \
"s_add_u32 s54, s54, s44\n" \
"s_add_u32 m0, s52, 16384\n" \
"s_nop 0\n" \
"buffer_load_dwordx4 v1, s[40:43], s54 offen nt lds\n" \
"s_add_u32 s54, s54, s44\n" \
"s_add_u32 m0, s52, 18432\n" \
"s_nop 0\n" \
"buffer_load_dwordx4 v1, s[40:43], s54 offen nt lds\n" \
"s_add_u32 s54, s54, s44\n" \
"s_add_u32 m0, s52, 20480\n" \
"s_nop 0\n" \
"buffer_load_dwordx4 v1, s[40:43], s54 offen nt lds\n" \
"s_add_u32 s54, s54, s44\n" \
"s_add_u32 m0, s52, 22528\n" \
"s_nop 0\n" \
"buffer_load_dwordx4 v1, s[40:43], s54 offen nt lds\n" \
"s_add_u32 s54, s54, s44\n" \
"s_add_u32 m0, s52, 32768\n" \
"s_nop 0\n" \
"buffer_load_dwordx4 v1, s[40:43], s54 offen nt lds\n" \
"s_add_u32 s54, s54, s44\n" \
"s_add_u32 m0, s52, 34816\n" \
"s_nop 0\n" \
"buffer_load_dwordx4 v1, s[40:43], s54 offen nt lds\n" \
"s_add_u32 s54, s54, s44\n" \
"s_add_u32 m0, s52, 36864\n" \
"s_nop 0\n" \
"buffer_load_dwordx4 v1, s[40:43], s54 offen nt lds\n" \
"s_add_u32 s54, s54, s44\n" \
"s_add_u32 m0, s52, 38912\n" \
"s_nop 0\n" \
"buffer_load_dwordx4 v1, s[40:43], s54 offen nt lds\n" \
"s_add_u32 s54, s54, s44\n" \
"s_add_u32 m0, s52, 49152\n" \
"s_nop 0\n" \
"buffer_load_dwordx4 v1, s[40:43], s54 offen nt lds\n" \
"s_add_u32 s54, s54, s44\n" \
"s_add_u32 m0, s52, 51200\n" \
"s_nop 0\n" \
"buffer_load_dwordx4 v1, s[40:43], s54 offen nt lds\n" \
"s_add_u32 s54, s54, s44\n" \
"s_add_u32 m0, s52, 53248\n" \
"s_nop 0\n" \
"buffer_load_dwordx4 v1, s[40:43], s54 offen nt lds\n" \
"s_add_u32 s54, s54, s44\n" \
"s_add_u32 m0, s52, 55296\n" \
"s_nop 0\n" \
"buffer_load_dwordx4 v1, s[40:43], s54 offen nt lds\n" \
"s_add_u32 s54, s54, s44\n" \
"s_add_u32 m0, s52, 65536\n" \
"s_nop 0\n" \
"buffer_load_dwordx4 v1, s[40:43], s54 offen nt lds\n" \
"s_add_u32 s54, s54, s44\n" \
"s_add_u32 m0, s52, 67584\n" \
"s_nop 0\n" \
"buffer_load_dwordx4 v1, s[40:43], s54 offen nt lds\n" \
"s_add_u32 s54, s54, s44\n" \
"s_add_u32 m0, s52, 69632\n" \
"s_nop 0\n" \
"buffer_load_dwordx4 v1, s[40:43], s54 offen nt lds\n" \
"s_add_u32 s54, s54, s44\n" \
"s_add_u32 m0, s52, 71680\n" \
"s_nop 0\n" \
"buffer_load_dwordx4 v1, s[40:43], s54 offen nt lds\n" \
"s_add_u32 s54, s54, s44\n" \
"s_add_u32 m0, s52, 81920\n" \
"s_nop 0\n" \
"buffer_load_dwordx4 v1, s[40:43], s54 offen nt lds\n" \
"s_add_u32 s54, s54, s44\n" \
"s_add_u32 m0, s52, 83968\n" \
"s_nop 0\n" \
"buffer_load_dwordx4 v1, s[40:43], s54 offen nt lds\n" \
"s_add_u32 s54, s54, s44\n" \
"s_add_u32 m0, s52, 86016\n" \
"s_nop 0\n" \
"buffer_load_dwordx4 v1, s[40:43], s54 offen nt lds\n" \
"s_add_u32 s54, s54, s44\n" \
"s_add_u32 m0, s52, 88064\n" \
"s_nop 0\n" \
"buffer_load_dwordx4 v1, s[40:43], s54 offen nt lds\n" \
"s_add_u32 s54, s54, s44\n" \
"s_add_u32 m0, s52, 98304\n" \
"s_nop 0\n" \
"buffer_load_dwordx4 v1, s[40:43], s54 offen nt lds\n" \
"s_add_u32 s54, s54, s44\n" \
"s_add_u32 m0, s52, 100352\n" \
"s_nop 0\n" \
"buffer_load_dwordx4 v1, s[40:43], s54 offen nt lds\n" \
"s_add_u32 s54, s54, s44\n" \
"s_add_u32 m0, s52, 102400\n" \
"s_nop 0\n" \
"buffer_load_dwordx4 v1, s[40:43], s54 offen nt lds\n" \
"s_add_u32 s54, s54, s44\n" \
"s_add_u32 m0, s52, 104448\n" \
"s_nop 0\n" \
"buffer_load_dwordx4 v1, s[40:43], s54 offen nt lds\n" \
"s_add_u32 s54, s54, s44\n" \
"s_add_u32 m0, s52, 114688\n" \
"s_nop 0\n" \
"buffer_load_dwordx4 v1, s[40:43], s54 offen nt lds\n" \
"s_add_u32 s54, s54, s44\n" \
"s_add_u32 m0, s52, 116736\n" \
"s_nop 0\n" \
"buffer_load_dwordx4 v1, s[40:43], s54 offen nt lds\n" \
"s_add_u32 s54, s54, s44\n" \
"s_add_u32 m0, s52, 118784\n" \
"s_nop 0\n" \
"buffer_load_dwordx4 v1, s[40:43], s54 offen nt lds\n" \
"s_add_u32 s54, s54, s44\n" \
"s_add_u32 m0, s52, 120832\n" \
"s_nop 0\n" \
"buffer_load_dwordx4 v1, s[40:43], s54 offen nt lds\n" \
"s_add_u32 s54, s54, s44\n" \
"v_add_u32 v8, 0x10000, v2\n" \
"v_add_u32 v9, 0x10000, v3\n" \
"s_waitcnt vmcnt(28)\n" \
"s_barrier\n" \
"ds_read_b64_tr_b16 a[0:1], v2 offset:0\n" \
"ds_read_b64_tr_b16 a[2:3], v2 offset:512\n" \
"ds_read_b64_tr_b16 a[128:129], v3 offset:0\n" \
"ds_read_b64_tr_b16 a[130:131], v3 offset:512\n" \
"ds_read_b64_tr_b16 a[4:5], v2 offset:4096\n" \
"ds_read_b64_tr_b16 a[6:7], v2 offset:4608\n" \
"ds_read_b64_tr_b16 a[132:133], v3 offset:4096\n" \
"ds_read_b64_tr_b16 a[134:135], v3 offset:4608\n" \
"s_waitcnt lgkmcnt(0)\n" \
"s_barrier\n" \
"s_add_u32 m0, s52, 0\n" \
"s_nop 0\n" \
"buffer_load_dwordx4 v1, s[40:43], s54 offen nt lds\n" \
"s_add_u32 s54, s54, s44\n" \
"s_add_u32 m0, s52, 2048\n" \
"s_nop 0\n" \
"buffer_load_dwordx4 v1, s[40:43], s54 offen nt lds\n" \
"s_add_u32 s54, s54, s44\n" \
"s_add_u32 m0, s52, 4096\n" \
"s_nop 0\n" \
"buffer_load_dwordx4 v1, s[40:43], s54 offen nt lds\n" \
"s_add_u32 s54, s54, s44\n" \
"s_add_u32 m0, s52, 6144\n" \
"s_nop 0\n" \
"buffer_load_dwordx4 v1, s[40:43], s54 offen nt lds\n" \
"s_add_u32 s54, s54, s44\n" \
"s_waitcnt vmcnt(28)\n" \
"s_barrier\n" \
"ds_read_b64_tr_b16 a[8:9], v2 offset:16384\n" \
"ds_read_b64_tr_b16 a[10:11], v2 offset:16896\n" \
"ds_read_b64_tr_b16 a[136:137], v3 offset:16384\n" \
"ds_read_b64_tr_b16 a[138:139], v3 offset:16896\n" \
"ds_read_b64_tr_b16 a[12:13], v2 offset:20480\n" \
"ds_read_b64_tr_b16 a[14:15], v2 offset:20992\n" \
"ds_read_b64_tr_b16 a[140:141], v3 offset:20480\n" \
"ds_read_b64_tr_b16 a[142:143], v3 offset:20992\n" \
"s_waitcnt lgkmcnt(0)\n" \
"s_barrier\n" \
"s_add_u32 m0, s52, 16384\n" \
"s_nop 0\n" \
"buffer_load_dwordx4 v1, s[40:43], s54 offen nt lds\n" \
"s_add_u32 s54, s54, s44\n" \
"s_add_u32 m0, s52, 18432\n" \
"s_nop 0\n" \
"buffer_load_dwordx4 v1, s[40:43], s54 offen nt lds\n" \
"s_add_u32 s54, s54, s44\n" \
"s_add_u32 m0, s52, 20480\n" \
"s_nop 0\n" \
"buffer_load_dwordx4 v1, s[40:43], s54 offen nt lds\n" \
"s_add_u32 s54, s54, s44\n" \
"s_add_u32 m0, s52, 22528\n" \
"s_nop 0\n" \
"buffer_load_dwordx4 v1, s[40:43], s54 offen nt lds\n" \
"s_add_u32 s54, s54, s44\n" \
"s_waitcnt vmcnt(28)\n" \
"s_barrier\n" \
"ds_read_b64_tr_b16 a[16:17], v2 offset:32768\n" \
"ds_read_b64_tr_b16 a[18:19], v2 offset:33280\n" \
"ds_read_b64_tr_b16 a[144:145], v3 offset:32768\n" \
"ds_read_b64_tr_b16 a[146:147], v3 offset:33280\n" \
"ds_read_b64_tr_b16 a[20:21], v2 offset:36864\n" \
"ds_read_b64_tr_b16 a[22:23], v2 offset:37376\n" \
"ds_read_b64_tr_b16 a[148:149], v3 offset:36864\n" \
"ds_read_b64_tr_b16 a[150:151], v3 offset:37376\n" \
"s_waitcnt lgkmcnt(0)\n" \
"s_barrier\n" \
"s_add_u32 m0, s52, 32768\n" \
"s_nop 0\n" \
"buffer_load_dwordx4 v1, s[40:43], s54 offen nt lds\n" \
"s_add_u32 s54, s54, s44\n" \
"s_add_u32 m0, s52, 34816\n" \
"s_nop 0\n" \
"buffer_load_dwordx4 v1, s[40:43], s54 offen nt lds\n" \
"s_add_u32 s54, s54, s44\n" \
"s_add_u32 m0, s52, 36864\n" \
"s_nop 0\n" \
"buffer_load_dwordx4 v1, s[40:43], s54 offen nt lds\n" \
"s_add_u32 s54, s54, s44\n" \
"s_add_u32 m0, s52, 38912\n" \
"s_nop 0\n" \
"buffer_load_dwordx4 v1, s[40:43], s54 offen nt lds\n" \
"s_add_u32 s54, s54, s44\n" \
"s_waitcnt vmcnt(28)\n" \
"s_barrier\n" \
"ds_read_b64_tr_b16 a[24:25], v2 offset:49152\n" \
"ds_read_b64_tr_b16 a[26:27], v2 offset:49664\n" \
"ds_read_b64_tr_b16 a[152:153], v3 offset:49152\n" \
"ds_read_b64_tr_b16 a[154:155], v3 offset:49664\n" \
"ds_read_b64_tr_b16 a[28:29], v2 offset:53248\n" \
"ds_read_b64_tr_b16 a[30:31], v2 offset:53760\n" \
"ds_read_b64_tr_b16 a[156:157], v3 offset:53248\n" \
"ds_read_b64_tr_b16 a[158:159], v3 offset:53760\n" \
"s_waitcnt lgkmcnt(0)\n" \
"s_barrier\n" \
"s_add_u32 m0, s52, 49152\n" \
"s_nop 0\n" \
"buffer_load_dwordx4 v1, s[40:43], s54 offen nt lds\n" \
"s_add_u32 s54, s54, s44\n" \
"s_add_u32 m0, s52, 51200\n" \
"s_nop 0\n" \
"buffer_load_dwordx4 v1, s[40:43], s54 offen nt lds\n" \
"s_add_u32 s54, s54, s44\n" \
"s_add_u32 m0, s52, 53248\n" \
"s_nop 0\n" \
"buffer_load_dwordx4 v1, s[40:43], s54 offen nt lds\n" \
"s_add_u32 s54, s54, s44\n" \
"s_add_u32 m0, s52, 55296\n" \
"s_nop 0\n" \
"buffer_load_dwordx4 v1, s[40:43], s54 offen nt lds\n" \
"s_add_u32 s54, s54, s44\n" \
"s_waitcnt vmcnt(28)\n" \
"s_barrier\n" \
"ds_read_b64_tr_b16 a[32:33], v8 offset:0\n" \
"ds_read_b64_tr_b16 a[34:35], v8 offset:512\n" \
"ds_read_b64_tr_b16 a[160:161], v9 offset:0\n" \
"ds_read_b64_tr_b16 a[162:163], v9 offset:512\n" \
"ds_read_b64_tr_b16 a[36:37], v8 offset:4096\n" \
"ds_read_b64_tr_b16 a[38:39], v8 offset:4608\n" \
"ds_read_b64_tr_b16 a[164:165], v9 offset:4096\n" \
"ds_read_b64_tr_b16 a[166:167], v9 offset:4608\n" \
"s_waitcnt lgkmcnt(0)\n" \
"s_barrier\n" \
"s_add_u32 m0, s52, 65536\n" \
"s_nop 0\n" \
"buffer_load_dwordx4 v1, s[40:43], s54 offen nt lds\n" \
"s_add_u32 s54, s54, s44\n" \
"s_add_u32 m0, s52, 67584\n" \
"s_nop 0\n" \
"buffer_load_dwordx4 v1, s[40:43], s54 offen nt lds\n" \
"s_add_u32 s54, s54, s44\n" \
"s_add_u32 m0, s52, 69632\n" \
"s_nop 0\n" \
"buffer_load_dwordx4 v1, s[40:43], s54 offen nt lds\n" \
"s_add_u32 s54, s54, s44\n" \
"s_add_u32 m0, s52, 71680\n" \
"s_nop 0\n" \
"buffer_load_dwordx4 v1, s[40:43], s54 offen nt lds\n" \
"s_add_u32 s54, s54, s44\n" \
"s_waitcnt vmcnt(28)\n" \
"s_barrier\n" \
"ds_read_b64_tr_b16 a[40:41], v8 offset:16384\n" \
"ds_read_b64_tr_b16 a[42:43], v8 offset:16896\n" \
"ds_read_b64_tr_b16 a[168:169], v9 offset:16384\n" \
"ds_read_b64_tr_b16 a[170:171], v9 offset:16896\n" \
"ds_read_b64_tr_b16 a[44:45], v8 offset:20480\n" \
"ds_read_b64_tr_b16 a[46:47], v8 offset:20992\n" \
"ds_read_b64_tr_b16 a[172:173], v9 offset:20480\n" \
"ds_read_b64_tr_b16 a[174:175], v9 offset:20992\n" \
"s_waitcnt lgkmcnt(0)\n" \
"s_barrier\n" \
"s_add_u32 m0, s52, 81920\n" \
"s_nop 0\n" \
"buffer_load_dwordx4 v1, s[40:43], s54 offen nt lds\n" \
"s_add_u32 s54, s54, s44\n" \
"s_add_u32 m0, s52, 83968\n" \
"s_nop 0\n" \
"buffer_load_dwordx4 v1, s[40:43], s54 offen nt lds\n" \
"s_add_u32 s54, s54, s44\n" \
"s_add_u32 m0, s52, 86016\n" \
"s_nop 0\n" \
"buffer_load_dwordx4 v1, s[40:43], s54 offen nt lds\n" \
"s_add_u32 s54, s54, s44\n" \
"s_add_u32 m0, s52, 88064\n" \
"s_nop 0\n" \
"buffer_load_dwordx4 v1, s[40:43], s54 offen nt lds\n" \
"s_add_u32 s54, s54, s44\n" \
"s_waitcnt vmcnt(28)\n" \
"s_barrier\n" \
"ds_read_b64_tr_b16 a[48:49], v8 offset:32768\n" \
"ds_read_b64_tr_b16 a[50:51], v8 offset:33280\n" \
"ds_read_b64_tr_b16 a[176:177], v9 offset:32768\n" \
"ds_read_b64_tr_b16 a[178:179], v9 offset:33280\n" \
"ds_read_b64_tr_b16 a[52:53], v8 offset:36864\n" \
"ds_read_b64_tr_b16 a[54:55], v8 offset:37376\n" \
"ds_read_b64_tr_b16 a[180:181], v9 offset:36864\n" \
"ds_read_b64_tr_b16 a[182:183], v9 offset:37376\n" \
"s_waitcnt lgkmcnt(0)\n" \
"s_barrier\n" \
"s_add_u32 m0, s52, 98304\n" \
"s_nop 0\n" \
"buffer_load_dwordx4 v1, s[40:43], s54 offen nt lds\n" \
"s_add_u32 s54, s54, s44\n" \
"s_add_u32 m0, s52, 100352\n" \
"s_nop 0\n" \
"buffer_load_dwordx4 v1, s[40:43], s54 offen nt lds\n" \
"s_add_u32 s54, s54, s44\n" \
"s_add_u32 m0, s52, 102400\n" \
"s_nop 0\n" \
"buffer_load_dwordx4 v1, s[40:43], s54 offen nt lds\n" \
"s_add_u32 s54, s54, s44\n" \
"s_add_u32 m0, s52, 104448\n" \
"s_nop 0\n" \
"buffer_load_dwordx4 v1, s[40:43], s54 offen nt lds\n" \
"s_add_u32 s54, s54, s44\n" \
"s_waitcnt vmcnt(28)\n" \
"s_barrier\n" \
"ds_read_b64_tr_b16 a[56:57], v8 offset:49152\n" \
"ds_read_b64_tr_b16 a[58:59], v8 offset:49664\n" \
"ds_read_b64_tr_b16 a[184:185], v9 offset:49152\n" \
"ds_read_b64_tr_b16 a[186:187], v9 offset:49664\n" \
"ds_read_b64_tr_b16 a[60:61], v8 offset:53248\n" \
"ds_read_b64_tr_b16 a[62:63], v8 offset:53760\n" \
"ds_read_b64_tr_b16 a[188:189], v9 offset:53248\n" \
"ds_read_b64_tr_b16 a[190:191], v9 offset:53760\n" \
"s_waitcnt lgkmcnt(0)\n" \
"s_barrier\n" \
"s_add_u32 m0, s52, 114688\n" \
"s_nop 0\n" \
"buffer_load_dwordx4 v1, s[40:43], s54 offen nt lds\n" \
"s_add_u32 s54, s54, s44\n" \
"s_add_u32 m0, s52, 116736\n" \
"s_nop 0\n" \
"buffer_load_dwordx4 v1, s[40:43], s54 offen nt lds\n" \
"s_add_u32 s54, s54, s44\n" \
"s_add_u32 m0, s52, 118784\n" \
"s_nop 0\n" \
"buffer_load_dwordx4 v1, s[40:43], s54 offen nt lds\n" \
"s_add_u32 s54, s54, s44\n" \
"s_add_u32 m0, s52, 120832\n" \
"s_nop 0\n" \
"buffer_load_dwordx4 v1, s[40:43], s54 offen nt lds\n" \
"s_add_u32 s54, s54, s44\n" \
"s_waitcnt vmcnt(28)\n" \
"s_barrier\n" \
"ds_read_b64_tr_b16 a[64:65], v2 offset:0\n" \
"ds_read_b64_tr_b16 a[66:67], v2 offset:512\n" \
"ds_read_b64_tr_b16 a[192:193], v3 offset:0\n" \
"ds_read_b64_tr_b16 a[194:195], v3 offset:512\n" \
"ds_read_b64_tr_b16 a[68:69], v2 offset:4096\n" \
"ds_read_b64_tr_b16 a[70:71], v2 offset:4608\n" \
"ds_read_b64_tr_b16 a[196:197], v3 offset:4096\n" \
"ds_read_b64_tr_b16 a[198:199], v3 offset:4608\n" \
"s_waitcnt vmcnt(24)\n" \
"s_barrier\n" \
"ds_read_b64_tr_b16 a[72:73], v2 offset:16384\n" \
"ds_read_b64_tr_b16 a[74:75], v2 offset:16896\n" \
"ds_read_b64_tr_b16 a[200:201], v3 offset:16384\n" \
"ds_read_b64_tr_b16 a[202:203], v3 offset:16896\n" \
"ds_read_b64_tr_b16 a[76:77], v2 offset:20480\n" \
"ds_read_b64_tr_b16 a[78:79], v2 offset:20992\n" \
"ds_read_b64_tr_b16 a[204:205], v3 offset:20480\n" \
"ds_read_b64_tr_b16 a[206:207], v3 offset:20992\n" \
"s_waitcnt vmcnt(20)\n" \
"s_barrier\n" \
"ds_read_b64_tr_b16 a[80:81], v2 offset:32768\n" \
"ds_read_b64_tr_b16 a[82:83], v2 offset:33280\n" \
"ds_read_b64_tr_b16 a[208:209], v3 offset:32768\n" \
"ds_read_b64_tr_b16 a[210:211], v3 offset:33280\n" \
"ds_read_b64_tr_b16 a[84:85], v2 offset:36864\n" \
"ds_read_b64_tr_b16 a[86:87], v2 offset:37376\n" \
"ds_read_b64_tr_b16 a[212:213], v3 offset:36864\n" \
"ds_read_b64_tr_b16 a[214:215], v3 offset:37376\n" \
"s_waitcnt vmcnt(16)\n" \
"s_barrier\n" \
"ds_read_b64_tr_b16 a[88:89], v2 offset:49152\n" \
"ds_read_b64_tr_b16 a[90:91], v2 offset:49664\n" \
"ds_read_b64_tr_b16 a[216:217], v3 offset:49152\n" \
"ds_read_b64_tr_b16 a[218:219], v3 offset:49664\n" \
"ds_read_b64_tr_b16 a[92:93], v2 offset:53248\n" \
"ds_read_b64_tr_b16 a[94:95], v2 offset:53760\n" \
"ds_read_b64_tr_b16 a[220:221], v3 offset:53248\n" \
"ds_read_b64_tr_b16 a[222:223], v3 offset:53760\n" \
"s_waitcnt vmcnt(12)\n" \
"s_barrier\n" \
"ds_read_b64_tr_b16 a[96:97], v8 offset:0\n" \
"ds_read_b64_tr_b16 a[98:99], v8 offset:512\n" \
"ds_read_b64_tr_b16 a[224:225], v9 offset:0\n" \
"ds_read_b64_tr_b16 a[226:227], v9 offset:512\n" \
"ds_read_b64_tr_b16 a[100:101], v8 offset:4096\n" \
"ds_read_b64_tr_b16 a[102:103], v8 offset:4608\n" \
"ds_read_b64_tr_b16 a[228:229], v9 offset:4096\n" \
"ds_read_b64_tr_b16 a[230:231], v9 offset:4608\n" \
"s_waitcnt vmcnt(8)\n" \
"s_barrier\n" \
"ds_read_b64_tr_b16 a[104:105], v8 offset:16384\n" \
"ds_read_b64_tr_b16 a[106:107], v8 offset:16896\n" \
"ds_read_b64_tr_b16 a[232:233], v9 offset:16384\n" \
"ds_read_b64_tr_b16 a[234:235], v9 offset:16896\n" \
"ds_read_b64_tr_b16 a[108:109], v8 offset:20480\n" \
"ds_read_b64_tr_b16 a[110:111], v8 offset:20992\n" \
"ds_read_b64_tr_b16 a[236:237], v9 offset:20480\n" \
"ds_read_b64_tr_b16 a[238:239], v9 offset:20992\n" \
"s_waitcnt vmcnt(4)\n" \
"s_barrier\n" \
"ds_read_b64_tr_b16 a[112:113], v8 offset:32768\n" \
"ds_read_b64_tr_b16 a[114:115], v8 offset:33280\n" \
"ds_read_b64_tr_b16 a[240:241], v9 offset:32768\n" \
"ds_read_b64_tr_b16 a[242:243], v9 offset:33280\n" \
"ds_read_b64_tr_b16 a[116:117], v8 offset:36864\n" \
"ds_read_b64_tr_b16 a[118:119], v8 offset:37376\n" \
"ds_read_b64_tr_b16 a[244:245], v9 offset:36864\n" \
"ds_read_b64_tr_b16 a[246:247], v9 offset:37376\n" \
"s_waitcnt vmcnt(0)\n" \
"s_barrier\n" \
"ds_read_b64_tr_b16 a[120:121], v8 offset:49152\n" \
"ds_read_b64_tr_b16 a[122:123], v8 offset:49664\n" \
"ds_read_b64_tr_b16 a[248:249], v9 offset:49152\n" \
"ds_read_b64_tr_b16 a[250:251], v9 offset:49664\n" \
"ds_read_b64_tr_b16 a[124:125], v8 offset:53248\n" \
"ds_read_b64_tr_b16 a[126:127], v8 offset:53760\n" \
"ds_read_b64_tr_b16 a[252:253], v9 offset:53248\n" \
"ds_read_b64_tr_b16 a[254:255], v9 offset:53760\n" \
"s_waitcnt lgkmcnt(0)\n" \
"s_barrier\n" \
"v_mov_b32 v14, 4096\n" \
"s_mov_b32 s59, s46\n" \
"s_add_u32 s63, s59, 0\n" \
"s_min_u32 s63, s63, 63\n" \
"s_lshr_b32 s63, s63, 3\n" \
"s_lshl_b32 s63, s63, 7\n" \
"s_branch .Lp1qk_l0_%=\n" \
".Lp1qk_l0k_%=:\n" \
"s_add_u32 s63, s59, 1\n" \
"s_min_u32 s63, s63, 63\n" \
"s_lshr_b32 s63, s63, 3\n" \
"s_lshl_b32 s63, s63, 7\n" \
"s_branch .Lp1qk_l1_%=\n" \
".Lp1qk_l1k_%=:\n" \
"s_lshl_b32 s55, s48, 13\n" \
"s_lshl_b32 s56, s46, 17\n" \
"s_add_u32 s56, s56, s45\n" \
"s_lshl_b32 s60, s47, 17\n" \
"s_sub_u32 s60, s60, 0x20000\n" \
"s_add_u32 s60, s60, s45\n" \
"s_mov_b32 s57, 0\n" \
"s_mov_b32 s58, 0\n" \
"v_mov_b32 v7, v5\n" \
"v_mov_b32 v5, 0x7ffffff0\n" \
"v_mov_b32 v10, v0\n" \
"v_mov_b32 v11, v6\n" \
"v_mov_b32 v6, 0x7ffffff0\n" \
"s_mov_b32 s59, s46\n" \
"s_min_u32 s61, s56, s60\n" \
"s_lshr_b32 s65, s61, 8\n" \
"s_and_b32 s65, s65, 0x2000\n" \
"s_xor_b32 s61, s61, s65\n" \
"s_add_u32 m0, s55, 0\n" \
"s_nop 0\n" \
"buffer_load_dwordx4 v4, s[36:39], s61 offen offset:0 sc1 lds\n" \
"buffer_load_dwordx4 v4, s[36:39], s61 offen offset:1024 sc1 lds\n" \
"buffer_load_dwordx4 v4, s[36:39], s61 offen offset:2048 sc1 lds\n" \
"buffer_load_dwordx4 v4, s[36:39], s61 offen offset:3072 sc1 lds\n" \
"s_add_u32 m0, s55, 4096\n" \
"s_add_u32 s61, s61, 4096\n" \
"buffer_load_dwordx4 v4, s[36:39], s61 offen offset:0 sc1 lds\n" \
"buffer_load_dwordx4 v4, s[36:39], s61 offen offset:1024 sc1 lds\n" \
"buffer_load_dwordx4 v4, s[36:39], s61 offen offset:2048 sc1 lds\n" \
"buffer_load_dwordx4 v4, s[36:39], s61 offen offset:3072 sc1 lds\n" \
"s_add_u32 s61, s61, 28672\n" \
"s_add_u32 s56, s56, 0x20000\n" \
"s_add_u32 m0, s55, 32768\n" \
"s_nop 0\n" \
"buffer_load_dwordx4 v4, s[36:39], s61 offen offset:0 sc1 lds\n" \
"buffer_load_dwordx4 v4, s[36:39], s61 offen offset:1024 sc1 lds\n" \
"buffer_load_dwordx4 v4, s[36:39], s61 offen offset:2048 sc1 lds\n" \
"buffer_load_dwordx4 v4, s[36:39], s61 offen offset:3072 sc1 lds\n" \
"s_add_u32 m0, s55, 36864\n" \
"s_add_u32 s61, s61, 4096\n" \
"buffer_load_dwordx4 v4, s[36:39], s61 offen offset:0 sc1 lds\n" \
"buffer_load_dwordx4 v4, s[36:39], s61 offen offset:1024 sc1 lds\n" \
"buffer_load_dwordx4 v4, s[36:39], s61 offen offset:2048 sc1 lds\n" \
"buffer_load_dwordx4 v4, s[36:39], s61 offen offset:3072 sc1 lds\n" \
"s_mov_b32 s70, 0\n" \
"s_mov_b32 s72, 0\n" \
"s_waitcnt vmcnt(0) lgkmcnt(0)\n" \
"s_barrier\n" \
"v_add_u32 v12, 0x10000, v10\n" \
"ds_read_b128 v[16:19], v10 offset:0\n" \
"ds_read_b128 v[20:23], v10 offset:16384\n" \
"ds_read_b128 v[24:27], v10 offset:1024\n" \
"ds_read_b128 v[28:31], v10 offset:17408\n" \
"ds_read_b128 v[32:35], v10 offset:2048\n" \
"ds_read_b128 v[36:39], v10 offset:18432\n" \
"s_waitcnt lgkmcnt(4)\n" \
"s_nop 7\n" \
"s_nop 7\n" \
".p2align 6\n" \
".Lp1qk_loop_%=:\n" \
"v_mfma_f32_16x16x32_bf16 v[48:51], a[0:3], v[16:19], 0\n" \
"s_min_u32 s61, s56, s60\n" \
"s_lshr_b32 s65, s61, 8\n" \
"s_and_b32 s65, s65, 0x2000\n" \
"s_xor_b32 s61, s61, s65\n" \
"s_add_u32 m0, s55, 65536\n" \
"s_nop 0\n" \
"buffer_load_dwordx4 v4, s[36:39], s61 offen offset:0 sc1 lds\n" \
"v_mov_b32_e64 v13, s59\n" \
"v_mfma_f32_16x16x32_bf16 v[52:55], a[0:3], v[20:23], 0\n" \
"ds_read_b128 v[40:43], v10 offset:3072\n" \
"s_nop 0\n" \
"v_mfma_f32_16x16x32_bf16 v[56:59], a[128:131], v[16:19], 0\n" \
"ds_read_b128 v[44:47], v10 offset:19456\n" \
"s_nop 0\n" \
"v_mfma_f32_16x16x32_bf16 v[60:63], a[128:131], v[20:23], 0\n" \
"s_waitcnt lgkmcnt(4)\n" \
"v_mul_f32 v64, s51, v64\n" \
"v_mul_f32_e64 v65, s51, v65\n" \
"v_mfma_f32_16x16x32_bf16 v[48:51], a[4:7], v[24:27], v[48:51]\n" \
"buffer_load_dwordx4 v4, s[36:39], s61 offen offset:1024 sc1 lds\n" \
"v_mfma_f32_16x16x32_bf16 v[52:55], a[4:7], v[28:31], v[52:55]\n" \
"ds_read_b128 v[16:19], v10 offset:4096\n" \
"v_mul_f32_e64 v66, s51, v66\n" \
"v_mfma_f32_16x16x32_bf16 v[56:59], a[132:135], v[24:27], v[56:59]\n" \
"ds_read_b128 v[20:23], v10 offset:20480\n" \
"v_mul_f32_e64 v67, s51, v67\n" \
"v_mfma_f32_16x16x32_bf16 v[60:63], a[132:135], v[28:31], v[60:63]\n" \
"s_waitcnt lgkmcnt(4)\n" \
"v_mul_f32 v72, s51, v72\n" \
"v_mul_f32_e64 v73, s51, v73\n" \
"v_mfma_f32_16x16x32_bf16 v[48:51], a[8:11], v[32:35], v[48:51]\n" \
"s_add_u32 s63, s59, 2\n" \
"s_min_u32 s63, s63, 63\n" \
"v_mfma_f32_16x16x32_bf16 v[52:55], a[8:11], v[36:39], v[52:55]\n" \
"ds_read_b128 v[24:27], v10 offset:5120\n" \
"s_lshr_b32 s63, s63, 3\n" \
"s_lshl_b32 s63, s63, 7\n" \
"v_mul_f32_e64 v74, s51, v74\n" \
"v_mfma_f32_16x16x32_bf16 v[56:59], a[136:139], v[32:35], v[56:59]\n" \
"ds_read_b128 v[28:31], v10 offset:21504\n" \
"v_mul_f32_e64 v75, s51, v75\n" \
"v_mfma_f32_16x16x32_bf16 v[60:63], a[136:139], v[36:39], v[60:63]\n" \
"s_waitcnt lgkmcnt(4)\n" \
"v_cvt_pk_bf16_f32 v80, v64, v65\n" \
"v_cvt_pk_bf16_f32 v81, v66, v67\n" \
"v_mfma_f32_16x16x32_bf16 v[48:51], a[12:15], v[40:43], v[48:51]\n" \
"buffer_load_dwordx4 v4, s[36:39], s61 offen offset:2048 sc1 lds\n" \
"v_mfma_f32_16x16x32_bf16 v[52:55], a[12:15], v[44:47], v[52:55]\n" \
"ds_read_b128 v[32:35], v10 offset:6144\n" \
"v_cvt_pk_bf16_f32 v82, v72, v73\n" \
"v_mfma_f32_16x16x32_bf16 v[56:59], a[140:143], v[40:43], v[56:59]\n" \
"ds_read_b128 v[36:39], v10 offset:22528\n" \
"v_cvt_pk_bf16_f32 v83, v74, v75\n" \
"v_mfma_f32_16x16x32_bf16 v[60:63], a[140:143], v[44:47], v[60:63]\n" \
"s_waitcnt lgkmcnt(4)\n" \
"buffer_store_dwordx4 v[80:83], v5, s[36:39], s57 offen sc1\n" \
"v_mul_f32_e64 v68, s51, v68\n" \
"v_mfma_f32_16x16x32_bf16 v[48:51], a[16:19], v[16:19], v[48:51]\n" \
"buffer_load_dwordx4 v4, s[36:39], s61 offen offset:3072 sc1 lds\n" \
"v_mfma_f32_16x16x32_bf16 v[52:55], a[16:19], v[20:23], v[52:55]\n" \
"ds_read_b128 v[40:43], v10 offset:7168\n" \
"v_mul_f32_e64 v69, s51, v69\n" \
"v_mfma_f32_16x16x32_bf16 v[56:59], a[144:147], v[16:19], v[56:59]\n" \
"ds_read_b128 v[44:47], v10 offset:23552\n" \
"v_mul_f32_e64 v70, s51, v70\n" \
"v_mfma_f32_16x16x32_bf16 v[60:63], a[144:147], v[20:23], v[60:63]\n" \
"s_waitcnt lgkmcnt(4)\n" \
"v_mul_f32 v71, s51, v71\n" \
"v_mul_f32_e64 v76, s51, v76\n" \
"v_mfma_f32_16x16x32_bf16 v[48:51], a[20:23], v[24:27], v[48:51]\n" \
"buffer_load_dword v15, v14, s[36:39], s63 offen sc1\n" \
"v_mfma_f32_16x16x32_bf16 v[52:55], a[20:23], v[28:31], v[52:55]\n" \
"ds_read_b128 v[16:19], v10 offset:8192\n" \
"v_mul_f32_e64 v77, s51, v77\n" \
"v_mfma_f32_16x16x32_bf16 v[56:59], a[148:151], v[24:27], v[56:59]\n" \
"ds_read_b128 v[20:23], v10 offset:24576\n" \
"v_mul_f32_e64 v78, s51, v78\n" \
"v_mfma_f32_16x16x32_bf16 v[60:63], a[148:151], v[28:31], v[60:63]\n" \
"s_waitcnt lgkmcnt(4)\n" \
"v_mul_f32 v79, s51, v79\n" \
"v_cvt_pk_bf16_f32 v84, v68, v69\n" \
"v_mfma_f32_16x16x32_bf16 v[48:51], a[24:27], v[32:35], v[48:51]\n" \
"s_add_u32 m0, s55, 69632\n" \
"s_add_u32 s61, s61, 4096\n" \
"buffer_load_dwordx4 v4, s[36:39], s61 offen offset:0 sc1 lds\n" \
"v_mfma_f32_16x16x32_bf16 v[52:55], a[24:27], v[36:39], v[52:55]\n" \
"ds_read_b128 v[24:27], v10 offset:9216\n" \
"v_cvt_pk_bf16_f32 v85, v70, v71\n" \
"v_mfma_f32_16x16x32_bf16 v[56:59], a[152:155], v[32:35], v[56:59]\n" \
"ds_read_b128 v[28:31], v10 offset:25600\n" \
"v_cvt_pk_bf16_f32 v86, v76, v77\n" \
"v_mfma_f32_16x16x32_bf16 v[60:63], a[152:155], v[36:39], v[60:63]\n" \
"s_waitcnt lgkmcnt(4)\n" \
"v_cvt_pk_bf16_f32 v87, v78, v79\n" \
"buffer_store_dwordx4 v[84:87], v5, s[36:39], s58 offen sc1\n" \
"v_mfma_f32_16x16x32_bf16 v[48:51], a[28:31], v[40:43], v[48:51]\n" \
"buffer_load_dwordx4 v4, s[36:39], s61 offen offset:1024 sc1 lds\n" \
"v_mfma_f32_16x16x32_bf16 v[52:55], a[28:31], v[44:47], v[52:55]\n" \
"ds_read_b128 v[32:35], v10 offset:10240\n" \
"v_mfma_f32_16x16x32_bf16 v[56:59], a[156:159], v[40:43], v[56:59]\n" \
"ds_read_b128 v[36:39], v10 offset:26624\n" \
"v_mfma_f32_16x16x32_bf16 v[60:63], a[156:159], v[44:47], v[60:63]\n" \
"s_waitcnt lgkmcnt(4)\n" \
"v_mfma_f32_16x16x32_bf16 v[48:51], a[32:35], v[16:19], v[48:51]\n" \
"v_mfma_f32_16x16x32_bf16 v[52:55], a[32:35], v[20:23], v[52:55]\n" \
"ds_read_b128 v[40:43], v10 offset:11264\n" \
"v_mfma_f32_16x16x32_bf16 v[56:59], a[160:163], v[16:19], v[56:59]\n" \
"ds_read_b128 v[44:47], v10 offset:27648\n" \
"v_mfma_f32_16x16x32_bf16 v[60:63], a[160:163], v[20:23], v[60:63]\n" \
"s_waitcnt lgkmcnt(4)\n" \
"v_mfma_f32_16x16x32_bf16 v[48:51], a[36:39], v[24:27], v[48:51]\n" \
"buffer_load_dwordx4 v4, s[36:39], s61 offen offset:2048 sc1 lds\n" \
"v_mfma_f32_16x16x32_bf16 v[52:55], a[36:39], v[28:31], v[52:55]\n" \
"ds_read_b128 v[16:19], v10 offset:12288\n" \
"v_mfma_f32_16x16x32_bf16 v[56:59], a[164:167], v[24:27], v[56:59]\n" \
"ds_read_b128 v[20:23], v10 offset:28672\n" \
"v_mfma_f32_16x16x32_bf16 v[60:63], a[164:167], v[28:31], v[60:63]\n" \
"s_waitcnt lgkmcnt(4)\n" \
"v_mfma_f32_16x16x32_bf16 v[48:51], a[40:43], v[32:35], v[48:51]\n" \
"buffer_load_dwordx4 v4, s[36:39], s61 offen offset:3072 sc1 lds\n" \
"v_mfma_f32_16x16x32_bf16 v[52:55], a[40:43], v[36:39], v[52:55]\n" \
"ds_read_b128 v[24:27], v10 offset:13312\n" \
"v_mfma_f32_16x16x32_bf16 v[56:59], a[168:171], v[32:35], v[56:59]\n" \
"ds_read_b128 v[28:31], v10 offset:29696\n" \
"v_mfma_f32_16x16x32_bf16 v[60:63], a[168:171], v[36:39], v[60:63]\n" \
"s_waitcnt lgkmcnt(4)\n" \
"v_mfma_f32_16x16x32_bf16 v[48:51], a[44:47], v[40:43], v[48:51]\n" \
"v_mfma_f32_16x16x32_bf16 v[52:55], a[44:47], v[44:47], v[52:55]\n" \
"ds_read_b128 v[32:35], v10 offset:14336\n" \
"v_mfma_f32_16x16x32_bf16 v[56:59], a[172:175], v[40:43], v[56:59]\n" \
"ds_read_b128 v[36:39], v10 offset:30720\n" \
"v_mfma_f32_16x16x32_bf16 v[60:63], a[172:175], v[44:47], v[60:63]\n" \
"s_waitcnt lgkmcnt(4)\n" \
"v_mfma_f32_16x16x32_bf16 v[48:51], a[48:51], v[16:19], v[48:51]\n" \
"v_mfma_f32_16x16x32_bf16 v[52:55], a[48:51], v[20:23], v[52:55]\n" \
"ds_read_b128 v[40:43], v10 offset:15360\n" \
"v_mfma_f32_16x16x32_bf16 v[56:59], a[176:179], v[16:19], v[56:59]\n" \
"ds_read_b128 v[44:47], v10 offset:31744\n" \
"v_mfma_f32_16x16x32_bf16 v[60:63], a[176:179], v[20:23], v[60:63]\n" \
"s_waitcnt lgkmcnt(4)\n" \
"v_mfma_f32_16x16x32_bf16 v[48:51], a[52:55], v[24:27], v[48:51]\n" \
"s_waitcnt vmcnt(12)\n" \
"s_barrier\n" \
"v_mfma_f32_16x16x32_bf16 v[52:55], a[52:55], v[28:31], v[52:55]\n" \
"ds_read_b128 v[16:19], v10 offset:32768\n" \
"v_mfma_f32_16x16x32_bf16 v[56:59], a[180:183], v[24:27], v[56:59]\n" \
"ds_read_b128 v[20:23], v10 offset:49152\n" \
"v_mfma_f32_16x16x32_bf16 v[60:63], a[180:183], v[28:31], v[60:63]\n" \
"s_waitcnt lgkmcnt(4)\n" \
"v_mfma_f32_16x16x32_bf16 v[48:51], a[56:59], v[32:35], v[48:51]\n" \
"v_mfma_f32_16x16x32_bf16 v[52:55], a[56:59], v[36:39], v[52:55]\n" \
"ds_read_b128 v[24:27], v10 offset:33792\n" \
"v_mfma_f32_16x16x32_bf16 v[56:59], a[184:187], v[32:35], v[56:59]\n" \
"ds_read_b128 v[28:31], v10 offset:50176\n" \
"v_mfma_f32_16x16x32_bf16 v[60:63], a[184:187], v[36:39], v[60:63]\n" \
"s_waitcnt lgkmcnt(4)\n" \
"v_mfma_f32_16x16x32_bf16 v[48:51], a[60:63], v[40:43], v[48:51]\n" \
"v_mfma_f32_16x16x32_bf16 v[52:55], a[60:63], v[44:47], v[52:55]\n" \
"ds_read_b128 v[32:35], v10 offset:34816\n" \
"v_mfma_f32_16x16x32_bf16 v[56:59], a[188:191], v[40:43], v[56:59]\n" \
"ds_read_b128 v[36:39], v10 offset:51200\n" \
"v_mfma_f32_16x16x32_bf16 v[60:63], a[188:191], v[44:47], v[60:63]\n" \
"s_waitcnt lgkmcnt(4)\n" \
"v_mfma_f32_16x16x32_bf16 v[48:51], a[64:67], v[16:19], v[48:51]\n" \
"s_add_u32 s61, s61, 28672\n" \
"s_add_u32 s56, s56, 0x20000\n" \
"s_add_u32 m0, s55, 98304\n" \
"s_nop 0\n" \
"buffer_load_dwordx4 v4, s[36:39], s61 offen offset:0 sc1 lds\n" \
"v_mfma_f32_16x16x32_bf16 v[52:55], a[64:67], v[20:23], v[52:55]\n" \
"ds_read_b128 v[40:43], v10 offset:35840\n" \
"v_mfma_f32_16x16x32_bf16 v[56:59], a[192:195], v[16:19], v[56:59]\n" \
"ds_read_b128 v[44:47], v10 offset:52224\n" \
"v_mfma_f32_16x16x32_bf16 v[60:63], a[192:195], v[20:23], v[60:63]\n" \
"s_waitcnt lgkmcnt(4)\n" \
"v_mfma_f32_16x16x32_bf16 v[48:51], a[68:71], v[24:27], v[48:51]\n" \
"buffer_load_dwordx4 v4, s[36:39], s61 offen offset:1024 sc1 lds\n" \
"v_mfma_f32_16x16x32_bf16 v[52:55], a[68:71], v[28:31], v[52:55]\n" \
"ds_read_b128 v[16:19], v10 offset:36864\n" \
"v_mfma_f32_16x16x32_bf16 v[56:59], a[196:199], v[24:27], v[56:59]\n" \
"ds_read_b128 v[20:23], v10 offset:53248\n" \
"v_mfma_f32_16x16x32_bf16 v[60:63], a[196:199], v[28:31], v[60:63]\n" \
"s_waitcnt lgkmcnt(4)\n" \
"v_mfma_f32_16x16x32_bf16 v[48:51], a[72:75], v[32:35], v[48:51]\n" \
"v_mfma_f32_16x16x32_bf16 v[52:55], a[72:75], v[36:39], v[52:55]\n" \
"ds_read_b128 v[24:27], v10 offset:37888\n" \
"v_mfma_f32_16x16x32_bf16 v[56:59], a[200:203], v[32:35], v[56:59]\n" \
"ds_read_b128 v[28:31], v10 offset:54272\n" \
"v_mfma_f32_16x16x32_bf16 v[60:63], a[200:203], v[36:39], v[60:63]\n" \
"s_waitcnt lgkmcnt(4)\n" \
"v_mfma_f32_16x16x32_bf16 v[48:51], a[76:79], v[40:43], v[48:51]\n" \
"buffer_load_dwordx4 v4, s[36:39], s61 offen offset:2048 sc1 lds\n" \
"v_mfma_f32_16x16x32_bf16 v[52:55], a[76:79], v[44:47], v[52:55]\n" \
"ds_read_b128 v[32:35], v10 offset:38912\n" \
"v_mfma_f32_16x16x32_bf16 v[56:59], a[204:207], v[40:43], v[56:59]\n" \
"ds_read_b128 v[36:39], v10 offset:55296\n" \
"v_mfma_f32_16x16x32_bf16 v[60:63], a[204:207], v[44:47], v[60:63]\n" \
"s_waitcnt lgkmcnt(4)\n" \
"v_mfma_f32_16x16x32_bf16 v[48:51], a[80:83], v[16:19], v[48:51]\n" \
"buffer_load_dwordx4 v4, s[36:39], s61 offen offset:3072 sc1 lds\n" \
"v_mfma_f32_16x16x32_bf16 v[52:55], a[80:83], v[20:23], v[52:55]\n" \
"ds_read_b128 v[40:43], v10 offset:39936\n" \
"v_mfma_f32_16x16x32_bf16 v[56:59], a[208:211], v[16:19], v[56:59]\n" \
"ds_read_b128 v[44:47], v10 offset:56320\n" \
"v_mfma_f32_16x16x32_bf16 v[60:63], a[208:211], v[20:23], v[60:63]\n" \
"s_waitcnt lgkmcnt(4)\n" \
"v_mfma_f32_16x16x32_bf16 v[48:51], a[84:87], v[24:27], v[48:51]\n" \
"v_mfma_f32_16x16x32_bf16 v[52:55], a[84:87], v[28:31], v[52:55]\n" \
"ds_read_b128 v[16:19], v10 offset:40960\n" \
"v_mfma_f32_16x16x32_bf16 v[56:59], a[212:215], v[24:27], v[56:59]\n" \
"ds_read_b128 v[20:23], v10 offset:57344\n" \
"v_mfma_f32_16x16x32_bf16 v[60:63], a[212:215], v[28:31], v[60:63]\n" \
"s_waitcnt lgkmcnt(4)\n" \
"v_mfma_f32_16x16x32_bf16 v[48:51], a[88:91], v[32:35], v[48:51]\n" \
"s_add_u32 m0, s55, 102400\n" \
"s_add_u32 s61, s61, 4096\n" \
"buffer_load_dwordx4 v4, s[36:39], s61 offen offset:0 sc1 lds\n" \
"v_mfma_f32_16x16x32_bf16 v[52:55], a[88:91], v[36:39], v[52:55]\n" \
"ds_read_b128 v[24:27], v10 offset:41984\n" \
"v_mfma_f32_16x16x32_bf16 v[56:59], a[216:219], v[32:35], v[56:59]\n" \
"ds_read_b128 v[28:31], v10 offset:58368\n" \
"v_mfma_f32_16x16x32_bf16 v[60:63], a[216:219], v[36:39], v[60:63]\n" \
"s_waitcnt lgkmcnt(4)\n" \
"v_mfma_f32_16x16x32_bf16 v[48:51], a[92:95], v[40:43], v[48:51]\n" \
"buffer_load_dwordx4 v4, s[36:39], s61 offen offset:1024 sc1 lds\n" \
"v_mfma_f32_16x16x32_bf16 v[52:55], a[92:95], v[44:47], v[52:55]\n" \
"ds_read_b128 v[32:35], v10 offset:43008\n" \
"v_mfma_f32_16x16x32_bf16 v[56:59], a[220:223], v[40:43], v[56:59]\n" \
"ds_read_b128 v[36:39], v10 offset:59392\n" \
"v_mfma_f32_16x16x32_bf16 v[60:63], a[220:223], v[44:47], v[60:63]\n" \
"s_waitcnt lgkmcnt(4)\n" \
"v_mfma_f32_16x16x32_bf16 v[48:51], a[96:99], v[16:19], v[48:51]\n" \
"v_mfma_f32_16x16x32_bf16 v[52:55], a[96:99], v[20:23], v[52:55]\n" \
"ds_read_b128 v[40:43], v10 offset:44032\n" \
"v_mfma_f32_16x16x32_bf16 v[56:59], a[224:227], v[16:19], v[56:59]\n" \
"ds_read_b128 v[44:47], v10 offset:60416\n" \
"v_mfma_f32_16x16x32_bf16 v[60:63], a[224:227], v[20:23], v[60:63]\n" \
"s_waitcnt lgkmcnt(4)\n" \
"v_mfma_f32_16x16x32_bf16 v[48:51], a[100:103], v[24:27], v[48:51]\n" \
"buffer_load_dwordx4 v4, s[36:39], s61 offen offset:2048 sc1 lds\n" \
"v_mfma_f32_16x16x32_bf16 v[52:55], a[100:103], v[28:31], v[52:55]\n" \
"ds_read_b128 v[16:19], v10 offset:45056\n" \
"v_mfma_f32_16x16x32_bf16 v[56:59], a[228:231], v[24:27], v[56:59]\n" \
"ds_read_b128 v[20:23], v10 offset:61440\n" \
"v_mfma_f32_16x16x32_bf16 v[60:63], a[228:231], v[28:31], v[60:63]\n" \
"s_waitcnt lgkmcnt(4)\n" \
"v_mfma_f32_16x16x32_bf16 v[48:51], a[104:107], v[32:35], v[48:51]\n" \
"buffer_load_dwordx4 v4, s[36:39], s61 offen offset:3072 sc1 lds\n" \
"v_mfma_f32_16x16x32_bf16 v[52:55], a[104:107], v[36:39], v[52:55]\n" \
"ds_read_b128 v[24:27], v10 offset:46080\n" \
"v_mfma_f32_16x16x32_bf16 v[56:59], a[232:235], v[32:35], v[56:59]\n" \
"ds_read_b128 v[28:31], v10 offset:62464\n" \
"v_mfma_f32_16x16x32_bf16 v[60:63], a[232:235], v[36:39], v[60:63]\n" \
"s_waitcnt lgkmcnt(4)\n" \
"v_mfma_f32_16x16x32_bf16 v[48:51], a[108:111], v[40:43], v[48:51]\n" \
"s_lshl_b32 s66, s59, s49\n" \
"s_lshr_b32 s65, s66, 8\n" \
"v_mfma_f32_16x16x32_bf16 v[52:55], a[108:111], v[44:47], v[52:55]\n" \
"ds_read_b128 v[32:35], v10 offset:47104\n" \
"v_mfma_f32_16x16x32_bf16 v[56:59], a[236:239], v[40:43], v[56:59]\n" \
"ds_read_b128 v[36:39], v10 offset:63488\n" \
"v_mfma_f32_16x16x32_bf16 v[60:63], a[236:239], v[44:47], v[60:63]\n" \
"s_waitcnt lgkmcnt(4)\n" \
"s_and_b32 s65, s65, 0x2000\n" \
"s_xor_b32 s66, s66, s65\n" \
"v_mfma_f32_16x16x32_bf16 v[48:51], a[112:115], v[16:19], v[48:51]\n" \
"v_mfma_f32_16x16x32_bf16 v[52:55], a[112:115], v[20:23], v[52:55]\n" \
"ds_read_b128 v[40:43], v10 offset:48128\n" \
"v_mfma_f32_16x16x32_bf16 v[56:59], a[240:243], v[16:19], v[56:59]\n" \
"ds_read_b128 v[44:47], v10 offset:64512\n" \
"v_mfma_f32_16x16x32_bf16 v[60:63], a[240:243], v[20:23], v[60:63]\n" \
"s_waitcnt lgkmcnt(4)\n" \
"v_mfma_f32_16x16x32_bf16 v[48:51], a[116:119], v[24:27], v[48:51]\n" \
"s_waitcnt vmcnt(8)\n" \
"s_barrier\n" \
"v_mfma_f32_16x16x32_bf16 v[52:55], a[116:119], v[28:31], v[52:55]\n" \
"ds_read_b128 v[16:19], v12 offset:0\n" \
"v_mfma_f32_16x16x32_bf16 v[56:59], a[244:247], v[24:27], v[56:59]\n" \
"ds_read_b128 v[20:23], v12 offset:16384\n" \
"v_mfma_f32_16x16x32_bf16 v[60:63], a[244:247], v[28:31], v[60:63]\n" \
"s_waitcnt lgkmcnt(4)\n" \
"buffer_store_dword v13, v6, s[36:39], 0 offen sc1\n" \
"v_mfma_f32_16x16x32_bf16 v[48:51], a[120:123], v[32:35], v[48:51]\n" \
"v_cmp_gt_u32_e64 vcc, 16, v15\n" \
"v_mfma_f32_16x16x32_bf16 v[52:55], a[120:123], v[36:39], v[52:55]\n" \
"ds_read_b128 v[24:27], v12 offset:1024\n" \
"v_mfma_f32_16x16x32_bf16 v[56:59], a[248:251], v[32:35], v[56:59]\n" \
"ds_read_b128 v[28:31], v12 offset:17408\n" \
"v_mfma_f32_16x16x32_bf16 v[60:63], a[248:251], v[36:39], v[60:63]\n" \
"s_waitcnt lgkmcnt(4)\n" \
"v_mfma_f32_16x16x32_bf16 v[48:51], a[124:127], v[40:43], v[48:51]\n" \
"s_cbranch_vccnz .Lp1qk_m0_%=\n" \
".Lp1qk_m0k_%=:\n" \
"v_mfma_f32_16x16x32_bf16 v[52:55], a[124:127], v[44:47], v[52:55]\n" \
"ds_read_b128 v[32:35], v12 offset:2048\n" \
"v_mfma_f32_16x16x32_bf16 v[56:59], a[252:255], v[40:43], v[56:59]\n" \
"ds_read_b128 v[36:39], v12 offset:18432\n" \
"v_mfma_f32_16x16x32_bf16 v[60:63], a[252:255], v[44:47], v[60:63]\n" \
"s_waitcnt lgkmcnt(4)\n" \
"s_xor_b32 s57, s66, s64\n" \
"s_add_u32 s58, s57, s50\n" \
"s_add_u32 s59, s59, 1\n" \
"v_mov_b32_e64 v5, v7\n" \
"v_mov_b32_e64 v6, v11\n" \
"s_cmp_ge_u32 s59, s47\n" \
"s_nop 0\n" \
"s_cbranch_scc1 .Lp1qk_out0_%=\n" \
"v_mfma_f32_16x16x32_bf16 v[64:67], a[0:3], v[16:19], 0\n" \
"s_min_u32 s61, s56, s60\n" \
"s_lshr_b32 s65, s61, 8\n" \
"s_and_b32 s65, s65, 0x2000\n" \
"s_xor_b32 s61, s61, s65\n" \
"s_add_u32 m0, s55, 0\n" \
"s_nop 0\n" \
"buffer_load_dwordx4 v4, s[36:39], s61 offen offset:0 sc1 lds\n" \
"v_mov_b32_e64 v13, s59\n" \
"v_mfma_f32_16x16x32_bf16 v[68:71], a[0:3], v[20:23], 0\n" \
"ds_read_b128 v[40:43], v12 offset:3072\n" \
"s_nop 0\n" \
"v_mfma_f32_16x16x32_bf16 v[72:75], a[128:131], v[16:19], 0\n" \
"ds_read_b128 v[44:47], v12 offset:19456\n" \
"s_nop 0\n" \
"v_mfma_f32_16x16x32_bf16 v[76:79], a[128:131], v[20:23], 0\n" \
"s_waitcnt lgkmcnt(4)\n" \
"v_mul_f32 v48, s51, v48\n" \
"v_mul_f32_e64 v49, s51, v49\n" \
"v_mfma_f32_16x16x32_bf16 v[64:67], a[4:7], v[24:27], v[64:67]\n" \
"buffer_load_dwordx4 v4, s[36:39], s61 offen offset:1024 sc1 lds\n" \
"v_mfma_f32_16x16x32_bf16 v[68:71], a[4:7], v[28:31], v[68:71]\n" \
"ds_read_b128 v[16:19], v12 offset:4096\n" \
"v_mul_f32_e64 v50, s51, v50\n" \
"v_mfma_f32_16x16x32_bf16 v[72:75], a[132:135], v[24:27], v[72:75]\n" \
"ds_read_b128 v[20:23], v12 offset:20480\n" \
"v_mul_f32_e64 v51, s51, v51\n" \
"v_mfma_f32_16x16x32_bf16 v[76:79], a[132:135], v[28:31], v[76:79]\n" \
"s_waitcnt lgkmcnt(4)\n" \
"v_mul_f32 v56, s51, v56\n" \
"v_mul_f32_e64 v57, s51, v57\n" \
"v_mfma_f32_16x16x32_bf16 v[64:67], a[8:11], v[32:35], v[64:67]\n" \
"s_add_u32 s63, s59, 2\n" \
"s_min_u32 s63, s63, 63\n" \
"v_mfma_f32_16x16x32_bf16 v[68:71], a[8:11], v[36:39], v[68:71]\n" \
"ds_read_b128 v[24:27], v12 offset:5120\n" \
"s_lshr_b32 s63, s63, 3\n" \
"s_lshl_b32 s63, s63, 7\n" \
"v_mul_f32_e64 v58, s51, v58\n" \
"v_mfma_f32_16x16x32_bf16 v[72:75], a[136:139], v[32:35], v[72:75]\n" \
"ds_read_b128 v[28:31], v12 offset:21504\n" \
"v_mul_f32_e64 v59, s51, v59\n" \
"v_mfma_f32_16x16x32_bf16 v[76:79], a[136:139], v[36:39], v[76:79]\n" \
"s_waitcnt lgkmcnt(4)\n" \
"v_cvt_pk_bf16_f32 v80, v48, v49\n" \
"v_cvt_pk_bf16_f32 v81, v50, v51\n" \
"v_mfma_f32_16x16x32_bf16 v[64:67], a[12:15], v[40:43], v[64:67]\n" \
"buffer_load_dwordx4 v4, s[36:39], s61 offen offset:2048 sc1 lds\n" \
"v_mfma_f32_16x16x32_bf16 v[68:71], a[12:15], v[44:47], v[68:71]\n" \
"ds_read_b128 v[32:35], v12 offset:6144\n" \
"v_cvt_pk_bf16_f32 v82, v56, v57\n" \
"v_mfma_f32_16x16x32_bf16 v[72:75], a[140:143], v[40:43], v[72:75]\n" \
"ds_read_b128 v[36:39], v12 offset:22528\n" \
"v_cvt_pk_bf16_f32 v83, v58, v59\n" \
"v_mfma_f32_16x16x32_bf16 v[76:79], a[140:143], v[44:47], v[76:79]\n" \
"s_waitcnt lgkmcnt(4)\n" \
"buffer_store_dwordx4 v[80:83], v5, s[36:39], s57 offen sc1\n" \
"v_mul_f32_e64 v52, s51, v52\n" \
"v_mfma_f32_16x16x32_bf16 v[64:67], a[16:19], v[16:19], v[64:67]\n" \
"buffer_load_dwordx4 v4, s[36:39], s61 offen offset:3072 sc1 lds\n" \
"v_mfma_f32_16x16x32_bf16 v[68:71], a[16:19], v[20:23], v[68:71]\n" \
"ds_read_b128 v[40:43], v12 offset:7168\n" \
"v_mul_f32_e64 v53, s51, v53\n" \
"v_mfma_f32_16x16x32_bf16 v[72:75], a[144:147], v[16:19], v[72:75]\n" \
"ds_read_b128 v[44:47], v12 offset:23552\n" \
"v_mul_f32_e64 v54, s51, v54\n" \
"v_mfma_f32_16x16x32_bf16 v[76:79], a[144:147], v[20:23], v[76:79]\n" \
"s_waitcnt lgkmcnt(4)\n" \
"v_mul_f32 v55, s51, v55\n" \
"v_mul_f32_e64 v60, s51, v60\n" \
"v_mfma_f32_16x16x32_bf16 v[64:67], a[20:23], v[24:27], v[64:67]\n" \
"buffer_load_dword v15, v14, s[36:39], s63 offen sc1\n" \
"v_mfma_f32_16x16x32_bf16 v[68:71], a[20:23], v[28:31], v[68:71]\n" \
"ds_read_b128 v[16:19], v12 offset:8192\n" \
"v_mul_f32_e64 v61, s51, v61\n" \
"v_mfma_f32_16x16x32_bf16 v[72:75], a[148:151], v[24:27], v[72:75]\n" \
"ds_read_b128 v[20:23], v12 offset:24576\n" \
"v_mul_f32_e64 v62, s51, v62\n" \
"v_mfma_f32_16x16x32_bf16 v[76:79], a[148:151], v[28:31], v[76:79]\n" \
"s_waitcnt lgkmcnt(4)\n" \
"v_mul_f32 v63, s51, v63\n" \
"v_cvt_pk_bf16_f32 v84, v52, v53\n" \
"v_mfma_f32_16x16x32_bf16 v[64:67], a[24:27], v[32:35], v[64:67]\n" \
"s_add_u32 m0, s55, 4096\n" \
"s_add_u32 s61, s61, 4096\n" \
"buffer_load_dwordx4 v4, s[36:39], s61 offen offset:0 sc1 lds\n" \
"v_mfma_f32_16x16x32_bf16 v[68:71], a[24:27], v[36:39], v[68:71]\n" \
"ds_read_b128 v[24:27], v12 offset:9216\n" \
"v_cvt_pk_bf16_f32 v85, v54, v55\n" \
"v_mfma_f32_16x16x32_bf16 v[72:75], a[152:155], v[32:35], v[72:75]\n" \
"ds_read_b128 v[28:31], v12 offset:25600\n" \
"v_cvt_pk_bf16_f32 v86, v60, v61\n" \
"v_mfma_f32_16x16x32_bf16 v[76:79], a[152:155], v[36:39], v[76:79]\n" \
"s_waitcnt lgkmcnt(4)\n" \
"v_cvt_pk_bf16_f32 v87, v62, v63\n" \
"buffer_store_dwordx4 v[84:87], v5, s[36:39], s58 offen sc1\n" \
"v_mfma_f32_16x16x32_bf16 v[64:67], a[28:31], v[40:43], v[64:67]\n" \
"buffer_load_dwordx4 v4, s[36:39], s61 offen offset:1024 sc1 lds\n" \
"v_mfma_f32_16x16x32_bf16 v[68:71], a[28:31], v[44:47], v[68:71]\n" \
"ds_read_b128 v[32:35], v12 offset:10240\n" \
"v_mfma_f32_16x16x32_bf16 v[72:75], a[156:159], v[40:43], v[72:75]\n" \
"ds_read_b128 v[36:39], v12 offset:26624\n" \
"v_mfma_f32_16x16x32_bf16 v[76:79], a[156:159], v[44:47], v[76:79]\n" \
"s_waitcnt lgkmcnt(4)\n" \
"v_mfma_f32_16x16x32_bf16 v[64:67], a[32:35], v[16:19], v[64:67]\n" \
"v_mfma_f32_16x16x32_bf16 v[68:71], a[32:35], v[20:23], v[68:71]\n" \
"ds_read_b128 v[40:43], v12 offset:11264\n" \
"v_mfma_f32_16x16x32_bf16 v[72:75], a[160:163], v[16:19], v[72:75]\n" \
"ds_read_b128 v[44:47], v12 offset:27648\n" \
"v_mfma_f32_16x16x32_bf16 v[76:79], a[160:163], v[20:23], v[76:79]\n" \
"s_waitcnt lgkmcnt(4)\n" \
"v_mfma_f32_16x16x32_bf16 v[64:67], a[36:39], v[24:27], v[64:67]\n" \
"buffer_load_dwordx4 v4, s[36:39], s61 offen offset:2048 sc1 lds\n" \
"v_mfma_f32_16x16x32_bf16 v[68:71], a[36:39], v[28:31], v[68:71]\n" \
"ds_read_b128 v[16:19], v12 offset:12288\n" \
"v_mfma_f32_16x16x32_bf16 v[72:75], a[164:167], v[24:27], v[72:75]\n" \
"ds_read_b128 v[20:23], v12 offset:28672\n" \
"v_mfma_f32_16x16x32_bf16 v[76:79], a[164:167], v[28:31], v[76:79]\n" \
"s_waitcnt lgkmcnt(4)\n" \
"v_mfma_f32_16x16x32_bf16 v[64:67], a[40:43], v[32:35], v[64:67]\n" \
"buffer_load_dwordx4 v4, s[36:39], s61 offen offset:3072 sc1 lds\n" \
"v_mfma_f32_16x16x32_bf16 v[68:71], a[40:43], v[36:39], v[68:71]\n" \
"ds_read_b128 v[24:27], v12 offset:13312\n" \
"v_mfma_f32_16x16x32_bf16 v[72:75], a[168:171], v[32:35], v[72:75]\n" \
"ds_read_b128 v[28:31], v12 offset:29696\n" \
"v_mfma_f32_16x16x32_bf16 v[76:79], a[168:171], v[36:39], v[76:79]\n" \
"s_waitcnt lgkmcnt(4)\n" \
"v_mfma_f32_16x16x32_bf16 v[64:67], a[44:47], v[40:43], v[64:67]\n" \
"v_mfma_f32_16x16x32_bf16 v[68:71], a[44:47], v[44:47], v[68:71]\n" \
"ds_read_b128 v[32:35], v12 offset:14336\n" \
"v_mfma_f32_16x16x32_bf16 v[72:75], a[172:175], v[40:43], v[72:75]\n" \
"ds_read_b128 v[36:39], v12 offset:30720\n" \
"v_mfma_f32_16x16x32_bf16 v[76:79], a[172:175], v[44:47], v[76:79]\n" \
"s_waitcnt lgkmcnt(4)\n" \
"v_mfma_f32_16x16x32_bf16 v[64:67], a[48:51], v[16:19], v[64:67]\n" \
"v_mfma_f32_16x16x32_bf16 v[68:71], a[48:51], v[20:23], v[68:71]\n" \
"ds_read_b128 v[40:43], v12 offset:15360\n" \
"v_mfma_f32_16x16x32_bf16 v[72:75], a[176:179], v[16:19], v[72:75]\n" \
"ds_read_b128 v[44:47], v12 offset:31744\n" \
"v_mfma_f32_16x16x32_bf16 v[76:79], a[176:179], v[20:23], v[76:79]\n" \
"s_waitcnt lgkmcnt(4)\n" \
"v_mfma_f32_16x16x32_bf16 v[64:67], a[52:55], v[24:27], v[64:67]\n" \
"s_waitcnt vmcnt(12)\n" \
"s_barrier\n" \
"v_mfma_f32_16x16x32_bf16 v[68:71], a[52:55], v[28:31], v[68:71]\n" \
"ds_read_b128 v[16:19], v12 offset:32768\n" \
"v_mfma_f32_16x16x32_bf16 v[72:75], a[180:183], v[24:27], v[72:75]\n" \
"ds_read_b128 v[20:23], v12 offset:49152\n" \
"v_mfma_f32_16x16x32_bf16 v[76:79], a[180:183], v[28:31], v[76:79]\n" \
"s_waitcnt lgkmcnt(4)\n" \
"v_mfma_f32_16x16x32_bf16 v[64:67], a[56:59], v[32:35], v[64:67]\n" \
"v_mfma_f32_16x16x32_bf16 v[68:71], a[56:59], v[36:39], v[68:71]\n" \
"ds_read_b128 v[24:27], v12 offset:33792\n" \
"v_mfma_f32_16x16x32_bf16 v[72:75], a[184:187], v[32:35], v[72:75]\n" \
"ds_read_b128 v[28:31], v12 offset:50176\n" \
"v_mfma_f32_16x16x32_bf16 v[76:79], a[184:187], v[36:39], v[76:79]\n" \
"s_waitcnt lgkmcnt(4)\n" \
"v_mfma_f32_16x16x32_bf16 v[64:67], a[60:63], v[40:43], v[64:67]\n" \
"v_mfma_f32_16x16x32_bf16 v[68:71], a[60:63], v[44:47], v[68:71]\n" \
"ds_read_b128 v[32:35], v12 offset:34816\n" \
"v_mfma_f32_16x16x32_bf16 v[72:75], a[188:191], v[40:43], v[72:75]\n" \
"ds_read_b128 v[36:39], v12 offset:51200\n" \
"v_mfma_f32_16x16x32_bf16 v[76:79], a[188:191], v[44:47], v[76:79]\n" \
"s_waitcnt lgkmcnt(4)\n" \
"v_mfma_f32_16x16x32_bf16 v[64:67], a[64:67], v[16:19], v[64:67]\n" \
"s_add_u32 s61, s61, 28672\n" \
"s_add_u32 s56, s56, 0x20000\n" \
"s_add_u32 m0, s55, 32768\n" \
"s_nop 0\n" \
"buffer_load_dwordx4 v4, s[36:39], s61 offen offset:0 sc1 lds\n" \
"v_mfma_f32_16x16x32_bf16 v[68:71], a[64:67], v[20:23], v[68:71]\n" \
"ds_read_b128 v[40:43], v12 offset:35840\n" \
"v_mfma_f32_16x16x32_bf16 v[72:75], a[192:195], v[16:19], v[72:75]\n" \
"ds_read_b128 v[44:47], v12 offset:52224\n" \
"v_mfma_f32_16x16x32_bf16 v[76:79], a[192:195], v[20:23], v[76:79]\n" \
"s_waitcnt lgkmcnt(4)\n" \
"v_mfma_f32_16x16x32_bf16 v[64:67], a[68:71], v[24:27], v[64:67]\n" \
"buffer_load_dwordx4 v4, s[36:39], s61 offen offset:1024 sc1 lds\n" \
"v_mfma_f32_16x16x32_bf16 v[68:71], a[68:71], v[28:31], v[68:71]\n" \
"ds_read_b128 v[16:19], v12 offset:36864\n" \
"v_mfma_f32_16x16x32_bf16 v[72:75], a[196:199], v[24:27], v[72:75]\n" \
"ds_read_b128 v[20:23], v12 offset:53248\n" \
"v_mfma_f32_16x16x32_bf16 v[76:79], a[196:199], v[28:31], v[76:79]\n" \
"s_waitcnt lgkmcnt(4)\n" \
"v_mfma_f32_16x16x32_bf16 v[64:67], a[72:75], v[32:35], v[64:67]\n" \
"v_mfma_f32_16x16x32_bf16 v[68:71], a[72:75], v[36:39], v[68:71]\n" \
"ds_read_b128 v[24:27], v12 offset:37888\n" \
"v_mfma_f32_16x16x32_bf16 v[72:75], a[200:203], v[32:35], v[72:75]\n" \
"ds_read_b128 v[28:31], v12 offset:54272\n" \
"v_mfma_f32_16x16x32_bf16 v[76:79], a[200:203], v[36:39], v[76:79]\n" \
"s_waitcnt lgkmcnt(4)\n" \
"v_mfma_f32_16x16x32_bf16 v[64:67], a[76:79], v[40:43], v[64:67]\n" \
"buffer_load_dwordx4 v4, s[36:39], s61 offen offset:2048 sc1 lds\n" \
"v_mfma_f32_16x16x32_bf16 v[68:71], a[76:79], v[44:47], v[68:71]\n" \
"ds_read_b128 v[32:35], v12 offset:38912\n" \
"v_mfma_f32_16x16x32_bf16 v[72:75], a[204:207], v[40:43], v[72:75]\n" \
"ds_read_b128 v[36:39], v12 offset:55296\n" \
"v_mfma_f32_16x16x32_bf16 v[76:79], a[204:207], v[44:47], v[76:79]\n" \
"s_waitcnt lgkmcnt(4)\n" \
"v_mfma_f32_16x16x32_bf16 v[64:67], a[80:83], v[16:19], v[64:67]\n" \
"buffer_load_dwordx4 v4, s[36:39], s61 offen offset:3072 sc1 lds\n" \
"v_mfma_f32_16x16x32_bf16 v[68:71], a[80:83], v[20:23], v[68:71]\n" \
"ds_read_b128 v[40:43], v12 offset:39936\n" \
"v_mfma_f32_16x16x32_bf16 v[72:75], a[208:211], v[16:19], v[72:75]\n" \
"ds_read_b128 v[44:47], v12 offset:56320\n" \
"v_mfma_f32_16x16x32_bf16 v[76:79], a[208:211], v[20:23], v[76:79]\n" \
"s_waitcnt lgkmcnt(4)\n" \
"v_mfma_f32_16x16x32_bf16 v[64:67], a[84:87], v[24:27], v[64:67]\n" \
"v_mfma_f32_16x16x32_bf16 v[68:71], a[84:87], v[28:31], v[68:71]\n" \
"ds_read_b128 v[16:19], v12 offset:40960\n" \
"v_mfma_f32_16x16x32_bf16 v[72:75], a[212:215], v[24:27], v[72:75]\n" \
"ds_read_b128 v[20:23], v12 offset:57344\n" \
"v_mfma_f32_16x16x32_bf16 v[76:79], a[212:215], v[28:31], v[76:79]\n" \
"s_waitcnt lgkmcnt(4)\n" \
"v_mfma_f32_16x16x32_bf16 v[64:67], a[88:91], v[32:35], v[64:67]\n" \
"s_add_u32 m0, s55, 36864\n" \
"s_add_u32 s61, s61, 4096\n" \
"buffer_load_dwordx4 v4, s[36:39], s61 offen offset:0 sc1 lds\n" \
"v_mfma_f32_16x16x32_bf16 v[68:71], a[88:91], v[36:39], v[68:71]\n" \
"ds_read_b128 v[24:27], v12 offset:41984\n" \
"v_mfma_f32_16x16x32_bf16 v[72:75], a[216:219], v[32:35], v[72:75]\n" \
"ds_read_b128 v[28:31], v12 offset:58368\n" \
"v_mfma_f32_16x16x32_bf16 v[76:79], a[216:219], v[36:39], v[76:79]\n" \
"s_waitcnt lgkmcnt(4)\n" \
"v_mfma_f32_16x16x32_bf16 v[64:67], a[92:95], v[40:43], v[64:67]\n" \
"buffer_load_dwordx4 v4, s[36:39], s61 offen offset:1024 sc1 lds\n" \
"v_mfma_f32_16x16x32_bf16 v[68:71], a[92:95], v[44:47], v[68:71]\n" \
"ds_read_b128 v[32:35], v12 offset:43008\n" \
"v_mfma_f32_16x16x32_bf16 v[72:75], a[220:223], v[40:43], v[72:75]\n" \
"ds_read_b128 v[36:39], v12 offset:59392\n" \
"v_mfma_f32_16x16x32_bf16 v[76:79], a[220:223], v[44:47], v[76:79]\n" \
"s_waitcnt lgkmcnt(4)\n" \
"v_mfma_f32_16x16x32_bf16 v[64:67], a[96:99], v[16:19], v[64:67]\n" \
"v_mfma_f32_16x16x32_bf16 v[68:71], a[96:99], v[20:23], v[68:71]\n" \
"ds_read_b128 v[40:43], v12 offset:44032\n" \
"v_mfma_f32_16x16x32_bf16 v[72:75], a[224:227], v[16:19], v[72:75]\n" \
"ds_read_b128 v[44:47], v12 offset:60416\n" \
"v_mfma_f32_16x16x32_bf16 v[76:79], a[224:227], v[20:23], v[76:79]\n" \
"s_waitcnt lgkmcnt(4)\n" \
"v_mfma_f32_16x16x32_bf16 v[64:67], a[100:103], v[24:27], v[64:67]\n" \
"buffer_load_dwordx4 v4, s[36:39], s61 offen offset:2048 sc1 lds\n" \
"v_mfma_f32_16x16x32_bf16 v[68:71], a[100:103], v[28:31], v[68:71]\n" \
"ds_read_b128 v[16:19], v12 offset:45056\n" \
"v_mfma_f32_16x16x32_bf16 v[72:75], a[228:231], v[24:27], v[72:75]\n" \
"ds_read_b128 v[20:23], v12 offset:61440\n" \
"v_mfma_f32_16x16x32_bf16 v[76:79], a[228:231], v[28:31], v[76:79]\n" \
"s_waitcnt lgkmcnt(4)\n" \
"v_mfma_f32_16x16x32_bf16 v[64:67], a[104:107], v[32:35], v[64:67]\n" \
"buffer_load_dwordx4 v4, s[36:39], s61 offen offset:3072 sc1 lds\n" \
"v_mfma_f32_16x16x32_bf16 v[68:71], a[104:107], v[36:39], v[68:71]\n" \
"ds_read_b128 v[24:27], v12 offset:46080\n" \
"v_mfma_f32_16x16x32_bf16 v[72:75], a[232:235], v[32:35], v[72:75]\n" \
"ds_read_b128 v[28:31], v12 offset:62464\n" \
"v_mfma_f32_16x16x32_bf16 v[76:79], a[232:235], v[36:39], v[76:79]\n" \
"s_waitcnt lgkmcnt(4)\n" \
"v_mfma_f32_16x16x32_bf16 v[64:67], a[108:111], v[40:43], v[64:67]\n" \
"s_lshl_b32 s66, s59, s49\n" \
"s_lshr_b32 s65, s66, 8\n" \
"v_mfma_f32_16x16x32_bf16 v[68:71], a[108:111], v[44:47], v[68:71]\n" \
"ds_read_b128 v[32:35], v12 offset:47104\n" \
"v_mfma_f32_16x16x32_bf16 v[72:75], a[236:239], v[40:43], v[72:75]\n" \
"ds_read_b128 v[36:39], v12 offset:63488\n" \
"v_mfma_f32_16x16x32_bf16 v[76:79], a[236:239], v[44:47], v[76:79]\n" \
"s_waitcnt lgkmcnt(4)\n" \
"s_and_b32 s65, s65, 0x2000\n" \
"s_xor_b32 s66, s66, s65\n" \
"v_mfma_f32_16x16x32_bf16 v[64:67], a[112:115], v[16:19], v[64:67]\n" \
"v_mfma_f32_16x16x32_bf16 v[68:71], a[112:115], v[20:23], v[68:71]\n" \
"ds_read_b128 v[40:43], v12 offset:48128\n" \
"v_mfma_f32_16x16x32_bf16 v[72:75], a[240:243], v[16:19], v[72:75]\n" \
"ds_read_b128 v[44:47], v12 offset:64512\n" \
"v_mfma_f32_16x16x32_bf16 v[76:79], a[240:243], v[20:23], v[76:79]\n" \
"s_waitcnt lgkmcnt(4)\n" \
"v_mfma_f32_16x16x32_bf16 v[64:67], a[116:119], v[24:27], v[64:67]\n" \
"s_waitcnt vmcnt(8)\n" \
"s_barrier\n" \
"v_mfma_f32_16x16x32_bf16 v[68:71], a[116:119], v[28:31], v[68:71]\n" \
"ds_read_b128 v[16:19], v10 offset:0\n" \
"v_mfma_f32_16x16x32_bf16 v[72:75], a[244:247], v[24:27], v[72:75]\n" \
"ds_read_b128 v[20:23], v10 offset:16384\n" \
"v_mfma_f32_16x16x32_bf16 v[76:79], a[244:247], v[28:31], v[76:79]\n" \
"s_waitcnt lgkmcnt(4)\n" \
"buffer_store_dword v13, v6, s[36:39], 0 offen sc1\n" \
"v_mfma_f32_16x16x32_bf16 v[64:67], a[120:123], v[32:35], v[64:67]\n" \
"v_cmp_gt_u32_e64 vcc, 16, v15\n" \
"v_mfma_f32_16x16x32_bf16 v[68:71], a[120:123], v[36:39], v[68:71]\n" \
"ds_read_b128 v[24:27], v10 offset:1024\n" \
"v_mfma_f32_16x16x32_bf16 v[72:75], a[248:251], v[32:35], v[72:75]\n" \
"ds_read_b128 v[28:31], v10 offset:17408\n" \
"v_mfma_f32_16x16x32_bf16 v[76:79], a[248:251], v[36:39], v[76:79]\n" \
"s_waitcnt lgkmcnt(4)\n" \
"v_mfma_f32_16x16x32_bf16 v[64:67], a[124:127], v[40:43], v[64:67]\n" \
"s_cbranch_vccnz .Lp1qk_m1_%=\n" \
".Lp1qk_m1k_%=:\n" \
"v_mfma_f32_16x16x32_bf16 v[68:71], a[124:127], v[44:47], v[68:71]\n" \
"ds_read_b128 v[32:35], v10 offset:2048\n" \
"v_mfma_f32_16x16x32_bf16 v[72:75], a[252:255], v[40:43], v[72:75]\n" \
"ds_read_b128 v[36:39], v10 offset:18432\n" \
"v_mfma_f32_16x16x32_bf16 v[76:79], a[252:255], v[44:47], v[76:79]\n" \
"s_waitcnt lgkmcnt(4)\n" \
"s_xor_b32 s57, s66, s64\n" \
"s_add_u32 s58, s57, s50\n" \
"s_add_u32 s59, s59, 1\n" \
"v_mov_b32_e64 v5, v7\n" \
"v_mov_b32_e64 v6, v11\n" \
"s_cmp_ge_u32 s59, s47\n" \
"s_nop 0\n" \
"s_cbranch_scc0 .Lp1qk_loop_%=\n" \
".Lp1qk_out1_%=:\n" \
"s_nop 7\n" \
"s_nop 7\n" \
"v_mul_f32 v64, s51, v64\n" \
"v_mul_f32 v65, s51, v65\n" \
"v_mul_f32 v66, s51, v66\n" \
"v_mul_f32 v67, s51, v67\n" \
"v_mul_f32 v72, s51, v72\n" \
"v_mul_f32 v73, s51, v73\n" \
"v_mul_f32 v74, s51, v74\n" \
"v_mul_f32 v75, s51, v75\n" \
"v_cvt_pk_bf16_f32 v80, v64, v65\n" \
"v_cvt_pk_bf16_f32 v81, v66, v67\n" \
"v_cvt_pk_bf16_f32 v82, v72, v73\n" \
"v_cvt_pk_bf16_f32 v83, v74, v75\n" \
"buffer_store_dwordx4 v[80:83], v5, s[36:39], s57 offen sc1\n" \
"v_mul_f32 v68, s51, v68\n" \
"v_mul_f32 v69, s51, v69\n" \
"v_mul_f32 v70, s51, v70\n" \
"v_mul_f32 v71, s51, v71\n" \
"v_mul_f32 v76, s51, v76\n" \
"v_mul_f32 v77, s51, v77\n" \
"v_mul_f32 v78, s51, v78\n" \
"v_mul_f32 v79, s51, v79\n" \
"v_cvt_pk_bf16_f32 v84, v68, v69\n" \
"v_cvt_pk_bf16_f32 v85, v70, v71\n" \
"v_cvt_pk_bf16_f32 v86, v76, v77\n" \
"v_cvt_pk_bf16_f32 v87, v78, v79\n" \
"buffer_store_dwordx4 v[84:87], v5, s[36:39], s58 offen sc1\n" \
"s_branch .Lp1qk_end_%=\n" \
".Lp1qk_out0_%=:\n" \
"s_nop 7\n" \
"s_nop 7\n" \
"v_mul_f32 v48, s51, v48\n" \
"v_mul_f32 v49, s51, v49\n" \
"v_mul_f32 v50, s51, v50\n" \
"v_mul_f32 v51, s51, v51\n" \
"v_mul_f32 v56, s51, v56\n" \
"v_mul_f32 v57, s51, v57\n" \
"v_mul_f32 v58, s51, v58\n" \
"v_mul_f32 v59, s51, v59\n" \
"v_cvt_pk_bf16_f32 v80, v48, v49\n" \
"v_cvt_pk_bf16_f32 v81, v50, v51\n" \
"v_cvt_pk_bf16_f32 v82, v56, v57\n" \
"v_cvt_pk_bf16_f32 v83, v58, v59\n" \
"buffer_store_dwordx4 v[80:83], v5, s[36:39], s57 offen sc1\n" \
"v_mul_f32 v52, s51, v52\n" \
"v_mul_f32 v53, s51, v53\n" \
"v_mul_f32 v54, s51, v54\n" \
"v_mul_f32 v55, s51, v55\n" \
"v_mul_f32 v60, s51, v60\n" \
"v_mul_f32 v61, s51, v61\n" \
"v_mul_f32 v62, s51, v62\n" \
"v_mul_f32 v63, s51, v63\n" \
"v_cvt_pk_bf16_f32 v84, v52, v53\n" \
"v_cvt_pk_bf16_f32 v85, v54, v55\n" \
"v_cvt_pk_bf16_f32 v86, v60, v61\n" \
"v_cvt_pk_bf16_f32 v87, v62, v63\n" \
"buffer_store_dwordx4 v[84:87], v5, s[36:39], s58 offen sc1\n" \
".Lp1qk_end_%=:\n" \
"s_waitcnt vmcnt(0) lgkmcnt(0)\n" \
"s_barrier\n" \
"v_mov_b32 v13, s59\n" \
"buffer_store_dword v13, v6, s[36:39], 0 offen sc1\n" \
"s_waitcnt vmcnt(0) lgkmcnt(0)\n" \
"s_branch .Lp1qk_x_%=\n" \
".Lp1qk_l0_%=:\n" \
"s_nop 7\n" \
"s_nop 7\n" \
"s_waitcnt vmcnt(0) lgkmcnt(0)\n" \
"s_mov_b32 s62, 0\n" \
".Lp1qk_l0a_%=:\n" \
"buffer_load_dword v15, v14, s[36:39], s63 offen sc1\n" \
"s_waitcnt vmcnt(0)\n" \
"v_cmp_gt_u32 vcc, 16, v15\n" \
"s_nop 4\n" \
"s_cbranch_vccz .Lp1qk_l0k_%=\n" \
"s_sleep 4\n" \
"s_add_u32 s62, s62, 1\n" \
"s_cmp_lt_u32 s62, 0x10000\n" \
"s_cbranch_scc1 .Lp1qk_l0a_%=\n" \
"v_mov_b32 v15, 256\n" \
"buffer_store_dword v15, v15, s[36:39], 0 offen sc1\n" \
"s_waitcnt vmcnt(0) lgkmcnt(0)\n" \
"s_branch .Lp1qk_l0k_%=\n" \
".Lp1qk_l1_%=:\n" \
"s_nop 7\n" \
"s_nop 7\n" \
"s_waitcnt vmcnt(0) lgkmcnt(0)\n" \
"s_mov_b32 s62, 0\n" \
".Lp1qk_l1a_%=:\n" \
"buffer_load_dword v15, v14, s[36:39], s63 offen sc1\n" \
"s_waitcnt vmcnt(0)\n" \
"v_cmp_gt_u32 vcc, 16, v15\n" \
"s_nop 4\n" \
"s_cbranch_vccz .Lp1qk_l1k_%=\n" \
"s_sleep 4\n" \
"s_add_u32 s62, s62, 1\n" \
"s_cmp_lt_u32 s62, 0x10000\n" \
"s_cbranch_scc1 .Lp1qk_l1a_%=\n" \
"v_mov_b32 v15, 256\n" \
"buffer_store_dword v15, v15, s[36:39], 0 offen sc1\n" \
"s_waitcnt vmcnt(0) lgkmcnt(0)\n" \
"s_branch .Lp1qk_l1k_%=\n" \
".Lp1qk_m0_%=:\n" \
"s_nop 7\n" \
"s_nop 7\n" \
"s_waitcnt vmcnt(0) lgkmcnt(0)\n" \
"s_mov_b32 s62, 0\n" \
".Lp1qk_m0a_%=:\n" \
"buffer_load_dword v15, v14, s[36:39], s63 offen sc1\n" \
"s_waitcnt vmcnt(0)\n" \
"v_cmp_gt_u32 vcc, 16, v15\n" \
"s_nop 4\n" \
"s_cbranch_vccz .Lp1qk_m0k_%=\n" \
"s_sleep 4\n" \
"s_add_u32 s62, s62, 1\n" \
"s_cmp_lt_u32 s62, 0x10000\n" \
"s_cbranch_scc1 .Lp1qk_m0a_%=\n" \
"v_mov_b32 v15, 256\n" \
"buffer_store_dword v15, v15, s[36:39], 0 offen sc1\n" \
"s_waitcnt vmcnt(0) lgkmcnt(0)\n" \
"s_branch .Lp1qk_m0k_%=\n" \
".Lp1qk_m1_%=:\n" \
"s_nop 7\n" \
"s_nop 7\n" \
"s_waitcnt vmcnt(0) lgkmcnt(0)\n" \
"s_mov_b32 s62, 0\n" \
".Lp1qk_m1a_%=:\n" \
"buffer_load_dword v15, v14, s[36:39], s63 offen sc1\n" \
"s_waitcnt vmcnt(0)\n" \
"v_cmp_gt_u32 vcc, 16, v15\n" \
"s_nop 4\n" \
"s_cbranch_vccz .Lp1qk_m1k_%=\n" \
"s_sleep 4\n" \
"s_add_u32 s62, s62, 1\n" \
"s_cmp_lt_u32 s62, 0x10000\n" \
"s_cbranch_scc1 .Lp1qk_m1a_%=\n" \
"v_mov_b32 v15, 256\n" \
"buffer_store_dword v15, v15, s[36:39], 0 offen sc1\n" \
"s_waitcnt vmcnt(0) lgkmcnt(0)\n" \
"s_branch .Lp1qk_m1k_%=\n" \
".Lp1qk_x_%=:\n"
#define ASM_P1_V \
"s_nop 7\n" \
"s_nop 7\n" \
"s_lshr_b32 s52, s48, 1\n" \
"s_lshl_b32 s52, s52, 13\n" \
"s_and_b32 s53, s48, 1\n" \
"s_lshl_b32 s53, s53, 10\n" \
"s_add_u32 s52, s52, s53\n" \
"s_mov_b32 s54, 0\n" \
"s_add_u32 m0, s52, 0\n" \
"s_nop 0\n" \
"buffer_load_dwordx4 v1, s[40:43], s54 offen nt lds\n" \
"s_add_u32 s54, s54, s44\n" \
"s_add_u32 m0, s52, 2048\n" \
"s_nop 0\n" \
"buffer_load_dwordx4 v1, s[40:43], s54 offen nt lds\n" \
"s_add_u32 s54, s54, s44\n" \
"s_add_u32 m0, s52, 4096\n" \
"s_nop 0\n" \
"buffer_load_dwordx4 v1, s[40:43], s54 offen nt lds\n" \
"s_add_u32 s54, s54, s44\n" \
"s_add_u32 m0, s52, 6144\n" \
"s_nop 0\n" \
"buffer_load_dwordx4 v1, s[40:43], s54 offen nt lds\n" \
"s_add_u32 s54, s54, s44\n" \
"s_add_u32 m0, s52, 16384\n" \
"s_nop 0\n" \
"buffer_load_dwordx4 v1, s[40:43], s54 offen nt lds\n" \
"s_add_u32 s54, s54, s44\n" \
"s_add_u32 m0, s52, 18432\n" \
"s_nop 0\n" \
"buffer_load_dwordx4 v1, s[40:43], s54 offen nt lds\n" \
"s_add_u32 s54, s54, s44\n" \
"s_add_u32 m0, s52, 20480\n" \
"s_nop 0\n" \
"buffer_load_dwordx4 v1, s[40:43], s54 offen nt lds\n" \
"s_add_u32 s54, s54, s44\n" \
"s_add_u32 m0, s52, 22528\n" \
"s_nop 0\n" \
"buffer_load_dwordx4 v1, s[40:43], s54 offen nt lds\n" \
"s_add_u32 s54, s54, s44\n" \
"s_add_u32 m0, s52, 32768\n" \
"s_nop 0\n" \
"buffer_load_dwordx4 v1, s[40:43], s54 offen nt lds\n" \
"s_add_u32 s54, s54, s44\n" \
"s_add_u32 m0, s52, 34816\n" \
"s_nop 0\n" \
"buffer_load_dwordx4 v1, s[40:43], s54 offen nt lds\n" \
"s_add_u32 s54, s54, s44\n" \
"s_add_u32 m0, s52, 36864\n" \
"s_nop 0\n" \
"buffer_load_dwordx4 v1, s[40:43], s54 offen nt lds\n" \
"s_add_u32 s54, s54, s44\n" \
"s_add_u32 m0, s52, 38912\n" \
"s_nop 0\n" \
"buffer_load_dwordx4 v1, s[40:43], s54 offen nt lds\n" \
"s_add_u32 s54, s54, s44\n" \
"s_add_u32 m0, s52, 49152\n" \
"s_nop 0\n" \
"buffer_load_dwordx4 v1, s[40:43], s54 offen nt lds\n" \
"s_add_u32 s54, s54, s44\n" \
"s_add_u32 m0, s52, 51200\n" \
"s_nop 0\n" \
"buffer_load_dwordx4 v1, s[40:43], s54 offen nt lds\n" \
"s_add_u32 s54, s54, s44\n" \
"s_add_u32 m0, s52, 53248\n" \
"s_nop 0\n" \
"buffer_load_dwordx4 v1, s[40:43], s54 offen nt lds\n" \
"s_add_u32 s54, s54, s44\n" \
"s_add_u32 m0, s52, 55296\n" \
"s_nop 0\n" \
"buffer_load_dwordx4 v1, s[40:43], s54 offen nt lds\n" \
"s_add_u32 s54, s54, s44\n" \
"s_add_u32 m0, s52, 65536\n" \
"s_nop 0\n" \
"buffer_load_dwordx4 v1, s[40:43], s54 offen nt lds\n" \
"s_add_u32 s54, s54, s44\n" \
"s_add_u32 m0, s52, 67584\n" \
"s_nop 0\n" \
"buffer_load_dwordx4 v1, s[40:43], s54 offen nt lds\n" \
"s_add_u32 s54, s54, s44\n" \
"s_add_u32 m0, s52, 69632\n" \
"s_nop 0\n" \
"buffer_load_dwordx4 v1, s[40:43], s54 offen nt lds\n" \
"s_add_u32 s54, s54, s44\n" \
"s_add_u32 m0, s52, 71680\n" \
"s_nop 0\n" \
"buffer_load_dwordx4 v1, s[40:43], s54 offen nt lds\n" \
"s_add_u32 s54, s54, s44\n" \
"s_add_u32 m0, s52, 81920\n" \
"s_nop 0\n" \
"buffer_load_dwordx4 v1, s[40:43], s54 offen nt lds\n" \
"s_add_u32 s54, s54, s44\n" \
"s_add_u32 m0, s52, 83968\n" \
"s_nop 0\n" \
"buffer_load_dwordx4 v1, s[40:43], s54 offen nt lds\n" \
"s_add_u32 s54, s54, s44\n" \
"s_add_u32 m0, s52, 86016\n" \
"s_nop 0\n" \
"buffer_load_dwordx4 v1, s[40:43], s54 offen nt lds\n" \
"s_add_u32 s54, s54, s44\n" \
"s_add_u32 m0, s52, 88064\n" \
"s_nop 0\n" \
"buffer_load_dwordx4 v1, s[40:43], s54 offen nt lds\n" \
"s_add_u32 s54, s54, s44\n" \
"s_add_u32 m0, s52, 98304\n" \
"s_nop 0\n" \
"buffer_load_dwordx4 v1, s[40:43], s54 offen nt lds\n" \
"s_add_u32 s54, s54, s44\n" \
"s_add_u32 m0, s52, 100352\n" \
"s_nop 0\n" \
"buffer_load_dwordx4 v1, s[40:43], s54 offen nt lds\n" \
"s_add_u32 s54, s54, s44\n" \
"s_add_u32 m0, s52, 102400\n" \
"s_nop 0\n" \
"buffer_load_dwordx4 v1, s[40:43], s54 offen nt lds\n" \
"s_add_u32 s54, s54, s44\n" \
"s_add_u32 m0, s52, 104448\n" \
"s_nop 0\n" \
"buffer_load_dwordx4 v1, s[40:43], s54 offen nt lds\n" \
"s_add_u32 s54, s54, s44\n" \
"s_add_u32 m0, s52, 114688\n" \
"s_nop 0\n" \
"buffer_load_dwordx4 v1, s[40:43], s54 offen nt lds\n" \
"s_add_u32 s54, s54, s44\n" \
"s_add_u32 m0, s52, 116736\n" \
"s_nop 0\n" \
"buffer_load_dwordx4 v1, s[40:43], s54 offen nt lds\n" \
"s_add_u32 s54, s54, s44\n" \
"s_add_u32 m0, s52, 118784\n" \
"s_nop 0\n" \
"buffer_load_dwordx4 v1, s[40:43], s54 offen nt lds\n" \
"s_add_u32 s54, s54, s44\n" \
"s_add_u32 m0, s52, 120832\n" \
"s_nop 0\n" \
"buffer_load_dwordx4 v1, s[40:43], s54 offen nt lds\n" \
"s_add_u32 s54, s54, s44\n" \
"v_add_u32 v8, 0x10000, v2\n" \
"v_add_u32 v9, 0x10000, v3\n" \
"s_waitcnt vmcnt(28)\n" \
"s_barrier\n" \
"ds_read_b64_tr_b16 a[0:1], v2 offset:0\n" \
"ds_read_b64_tr_b16 a[2:3], v2 offset:512\n" \
"ds_read_b64_tr_b16 a[128:129], v3 offset:0\n" \
"ds_read_b64_tr_b16 a[130:131], v3 offset:512\n" \
"ds_read_b64_tr_b16 a[4:5], v2 offset:4096\n" \
"ds_read_b64_tr_b16 a[6:7], v2 offset:4608\n" \
"ds_read_b64_tr_b16 a[132:133], v3 offset:4096\n" \
"ds_read_b64_tr_b16 a[134:135], v3 offset:4608\n" \
"s_waitcnt lgkmcnt(0)\n" \
"s_barrier\n" \
"s_add_u32 m0, s52, 0\n" \
"s_nop 0\n" \
"buffer_load_dwordx4 v1, s[40:43], s54 offen nt lds\n" \
"s_add_u32 s54, s54, s44\n" \
"s_add_u32 m0, s52, 2048\n" \
"s_nop 0\n" \
"buffer_load_dwordx4 v1, s[40:43], s54 offen nt lds\n" \
"s_add_u32 s54, s54, s44\n" \
"s_add_u32 m0, s52, 4096\n" \
"s_nop 0\n" \
"buffer_load_dwordx4 v1, s[40:43], s54 offen nt lds\n" \
"s_add_u32 s54, s54, s44\n" \
"s_add_u32 m0, s52, 6144\n" \
"s_nop 0\n" \
"buffer_load_dwordx4 v1, s[40:43], s54 offen nt lds\n" \
"s_add_u32 s54, s54, s44\n" \
"s_waitcnt vmcnt(28)\n" \
"s_barrier\n" \
"ds_read_b64_tr_b16 a[8:9], v2 offset:16384\n" \
"ds_read_b64_tr_b16 a[10:11], v2 offset:16896\n" \
"ds_read_b64_tr_b16 a[136:137], v3 offset:16384\n" \
"ds_read_b64_tr_b16 a[138:139], v3 offset:16896\n" \
"ds_read_b64_tr_b16 a[12:13], v2 offset:20480\n" \
"ds_read_b64_tr_b16 a[14:15], v2 offset:20992\n" \
"ds_read_b64_tr_b16 a[140:141], v3 offset:20480\n" \
"ds_read_b64_tr_b16 a[142:143], v3 offset:20992\n" \
"s_waitcnt lgkmcnt(0)\n" \
"s_barrier\n" \
"s_add_u32 m0, s52, 16384\n" \
"s_nop 0\n" \
"buffer_load_dwordx4 v1, s[40:43], s54 offen nt lds\n" \
"s_add_u32 s54, s54, s44\n" \
"s_add_u32 m0, s52, 18432\n" \
"s_nop 0\n" \
"buffer_load_dwordx4 v1, s[40:43], s54 offen nt lds\n" \
"s_add_u32 s54, s54, s44\n" \
"s_add_u32 m0, s52, 20480\n" \
"s_nop 0\n" \
"buffer_load_dwordx4 v1, s[40:43], s54 offen nt lds\n" \
"s_add_u32 s54, s54, s44\n" \
"s_add_u32 m0, s52, 22528\n" \
"s_nop 0\n" \
"buffer_load_dwordx4 v1, s[40:43], s54 offen nt lds\n" \
"s_add_u32 s54, s54, s44\n" \
"s_waitcnt vmcnt(28)\n" \
"s_barrier\n" \
"ds_read_b64_tr_b16 a[16:17], v2 offset:32768\n" \
"ds_read_b64_tr_b16 a[18:19], v2 offset:33280\n" \
"ds_read_b64_tr_b16 a[144:145], v3 offset:32768\n" \
"ds_read_b64_tr_b16 a[146:147], v3 offset:33280\n" \
"ds_read_b64_tr_b16 a[20:21], v2 offset:36864\n" \
"ds_read_b64_tr_b16 a[22:23], v2 offset:37376\n" \
"ds_read_b64_tr_b16 a[148:149], v3 offset:36864\n" \
"ds_read_b64_tr_b16 a[150:151], v3 offset:37376\n" \
"s_waitcnt lgkmcnt(0)\n" \
"s_barrier\n" \
"s_add_u32 m0, s52, 32768\n" \
"s_nop 0\n" \
"buffer_load_dwordx4 v1, s[40:43], s54 offen nt lds\n" \
"s_add_u32 s54, s54, s44\n" \
"s_add_u32 m0, s52, 34816\n" \
"s_nop 0\n" \
"buffer_load_dwordx4 v1, s[40:43], s54 offen nt lds\n" \
"s_add_u32 s54, s54, s44\n" \
"s_add_u32 m0, s52, 36864\n" \
"s_nop 0\n" \
"buffer_load_dwordx4 v1, s[40:43], s54 offen nt lds\n" \
"s_add_u32 s54, s54, s44\n" \
"s_add_u32 m0, s52, 38912\n" \
"s_nop 0\n" \
"buffer_load_dwordx4 v1, s[40:43], s54 offen nt lds\n" \
"s_add_u32 s54, s54, s44\n" \
"s_waitcnt vmcnt(28)\n" \
"s_barrier\n" \
"ds_read_b64_tr_b16 a[24:25], v2 offset:49152\n" \
"ds_read_b64_tr_b16 a[26:27], v2 offset:49664\n" \
"ds_read_b64_tr_b16 a[152:153], v3 offset:49152\n" \
"ds_read_b64_tr_b16 a[154:155], v3 offset:49664\n" \
"ds_read_b64_tr_b16 a[28:29], v2 offset:53248\n" \
"ds_read_b64_tr_b16 a[30:31], v2 offset:53760\n" \
"ds_read_b64_tr_b16 a[156:157], v3 offset:53248\n" \
"ds_read_b64_tr_b16 a[158:159], v3 offset:53760\n" \
"s_waitcnt lgkmcnt(0)\n" \
"s_barrier\n" \
"s_add_u32 m0, s52, 49152\n" \
"s_nop 0\n" \
"buffer_load_dwordx4 v1, s[40:43], s54 offen nt lds\n" \
"s_add_u32 s54, s54, s44\n" \
"s_add_u32 m0, s52, 51200\n" \
"s_nop 0\n" \
"buffer_load_dwordx4 v1, s[40:43], s54 offen nt lds\n" \
"s_add_u32 s54, s54, s44\n" \
"s_add_u32 m0, s52, 53248\n" \
"s_nop 0\n" \
"buffer_load_dwordx4 v1, s[40:43], s54 offen nt lds\n" \
"s_add_u32 s54, s54, s44\n" \
"s_add_u32 m0, s52, 55296\n" \
"s_nop 0\n" \
"buffer_load_dwordx4 v1, s[40:43], s54 offen nt lds\n" \
"s_add_u32 s54, s54, s44\n" \
"s_waitcnt vmcnt(28)\n" \
"s_barrier\n" \
"ds_read_b64_tr_b16 a[32:33], v8 offset:0\n" \
"ds_read_b64_tr_b16 a[34:35], v8 offset:512\n" \
"ds_read_b64_tr_b16 a[160:161], v9 offset:0\n" \
"ds_read_b64_tr_b16 a[162:163], v9 offset:512\n" \
"ds_read_b64_tr_b16 a[36:37], v8 offset:4096\n" \
"ds_read_b64_tr_b16 a[38:39], v8 offset:4608\n" \
"ds_read_b64_tr_b16 a[164:165], v9 offset:4096\n" \
"ds_read_b64_tr_b16 a[166:167], v9 offset:4608\n" \
"s_waitcnt lgkmcnt(0)\n" \
"s_barrier\n" \
"s_add_u32 m0, s52, 65536\n" \
"s_nop 0\n" \
"buffer_load_dwordx4 v1, s[40:43], s54 offen nt lds\n" \
"s_add_u32 s54, s54, s44\n" \
"s_add_u32 m0, s52, 67584\n" \
"s_nop 0\n" \
"buffer_load_dwordx4 v1, s[40:43], s54 offen nt lds\n" \
"s_add_u32 s54, s54, s44\n" \
"s_add_u32 m0, s52, 69632\n" \
"s_nop 0\n" \
"buffer_load_dwordx4 v1, s[40:43], s54 offen nt lds\n" \
"s_add_u32 s54, s54, s44\n" \
"s_add_u32 m0, s52, 71680\n" \
"s_nop 0\n" \
"buffer_load_dwordx4 v1, s[40:43], s54 offen nt lds\n" \
"s_add_u32 s54, s54, s44\n" \
"s_waitcnt vmcnt(28)\n" \
"s_barrier\n" \
"ds_read_b64_tr_b16 a[40:41], v8 offset:16384\n" \
"ds_read_b64_tr_b16 a[42:43], v8 offset:16896\n" \
"ds_read_b64_tr_b16 a[168:169], v9 offset:16384\n" \
"ds_read_b64_tr_b16 a[170:171], v9 offset:16896\n" \
"ds_read_b64_tr_b16 a[44:45], v8 offset:20480\n" \
"ds_read_b64_tr_b16 a[46:47], v8 offset:20992\n" \
"ds_read_b64_tr_b16 a[172:173], v9 offset:20480\n" \
"ds_read_b64_tr_b16 a[174:175], v9 offset:20992\n" \
"s_waitcnt lgkmcnt(0)\n" \
"s_barrier\n" \
"s_add_u32 m0, s52, 81920\n" \
"s_nop 0\n" \
"buffer_load_dwordx4 v1, s[40:43], s54 offen nt lds\n" \
"s_add_u32 s54, s54, s44\n" \
"s_add_u32 m0, s52, 83968\n" \
"s_nop 0\n" \
"buffer_load_dwordx4 v1, s[40:43], s54 offen nt lds\n" \
"s_add_u32 s54, s54, s44\n" \
"s_add_u32 m0, s52, 86016\n" \
"s_nop 0\n" \
"buffer_load_dwordx4 v1, s[40:43], s54 offen nt lds\n" \
"s_add_u32 s54, s54, s44\n" \
"s_add_u32 m0, s52, 88064\n" \
"s_nop 0\n" \
"buffer_load_dwordx4 v1, s[40:43], s54 offen nt lds\n" \
"s_add_u32 s54, s54, s44\n" \
"s_waitcnt vmcnt(28)\n" \
"s_barrier\n" \
"ds_read_b64_tr_b16 a[48:49], v8 offset:32768\n" \
"ds_read_b64_tr_b16 a[50:51], v8 offset:33280\n" \
"ds_read_b64_tr_b16 a[176:177], v9 offset:32768\n" \
"ds_read_b64_tr_b16 a[178:179], v9 offset:33280\n" \
"ds_read_b64_tr_b16 a[52:53], v8 offset:36864\n" \
"ds_read_b64_tr_b16 a[54:55], v8 offset:37376\n" \
"ds_read_b64_tr_b16 a[180:181], v9 offset:36864\n" \
"ds_read_b64_tr_b16 a[182:183], v9 offset:37376\n" \
"s_waitcnt lgkmcnt(0)\n" \
"s_barrier\n" \
"s_add_u32 m0, s52, 98304\n" \
"s_nop 0\n" \
"buffer_load_dwordx4 v1, s[40:43], s54 offen nt lds\n" \
"s_add_u32 s54, s54, s44\n" \
"s_add_u32 m0, s52, 100352\n" \
"s_nop 0\n" \
"buffer_load_dwordx4 v1, s[40:43], s54 offen nt lds\n" \
"s_add_u32 s54, s54, s44\n" \
"s_add_u32 m0, s52, 102400\n" \
"s_nop 0\n" \
"buffer_load_dwordx4 v1, s[40:43], s54 offen nt lds\n" \
"s_add_u32 s54, s54, s44\n" \
"s_add_u32 m0, s52, 104448\n" \
"s_nop 0\n" \
"buffer_load_dwordx4 v1, s[40:43], s54 offen nt lds\n" \
"s_add_u32 s54, s54, s44\n" \
"s_waitcnt vmcnt(28)\n" \
"s_barrier\n" \
"ds_read_b64_tr_b16 a[56:57], v8 offset:49152\n" \
"ds_read_b64_tr_b16 a[58:59], v8 offset:49664\n" \
"ds_read_b64_tr_b16 a[184:185], v9 offset:49152\n" \
"ds_read_b64_tr_b16 a[186:187], v9 offset:49664\n" \
"ds_read_b64_tr_b16 a[60:61], v8 offset:53248\n" \
"ds_read_b64_tr_b16 a[62:63], v8 offset:53760\n" \
"ds_read_b64_tr_b16 a[188:189], v9 offset:53248\n" \
"ds_read_b64_tr_b16 a[190:191], v9 offset:53760\n" \
"s_waitcnt lgkmcnt(0)\n" \
"s_barrier\n" \
"s_add_u32 m0, s52, 114688\n" \
"s_nop 0\n" \
"buffer_load_dwordx4 v1, s[40:43], s54 offen nt lds\n" \
"s_add_u32 s54, s54, s44\n" \
"s_add_u32 m0, s52, 116736\n" \
"s_nop 0\n" \
"buffer_load_dwordx4 v1, s[40:43], s54 offen nt lds\n" \
"s_add_u32 s54, s54, s44\n" \
"s_add_u32 m0, s52, 118784\n" \
"s_nop 0\n" \
"buffer_load_dwordx4 v1, s[40:43], s54 offen nt lds\n" \
"s_add_u32 s54, s54, s44\n" \
"s_add_u32 m0, s52, 120832\n" \
"s_nop 0\n" \
"buffer_load_dwordx4 v1, s[40:43], s54 offen nt lds\n" \
"s_add_u32 s54, s54, s44\n" \
"s_waitcnt vmcnt(28)\n" \
"s_barrier\n" \
"ds_read_b64_tr_b16 a[64:65], v2 offset:0\n" \
"ds_read_b64_tr_b16 a[66:67], v2 offset:512\n" \
"ds_read_b64_tr_b16 a[192:193], v3 offset:0\n" \
"ds_read_b64_tr_b16 a[194:195], v3 offset:512\n" \
"ds_read_b64_tr_b16 a[68:69], v2 offset:4096\n" \
"ds_read_b64_tr_b16 a[70:71], v2 offset:4608\n" \
"ds_read_b64_tr_b16 a[196:197], v3 offset:4096\n" \
"ds_read_b64_tr_b16 a[198:199], v3 offset:4608\n" \
"s_waitcnt vmcnt(24)\n" \
"s_barrier\n" \
"ds_read_b64_tr_b16 a[72:73], v2 offset:16384\n" \
"ds_read_b64_tr_b16 a[74:75], v2 offset:16896\n" \
"ds_read_b64_tr_b16 a[200:201], v3 offset:16384\n" \
"ds_read_b64_tr_b16 a[202:203], v3 offset:16896\n" \
"ds_read_b64_tr_b16 a[76:77], v2 offset:20480\n" \
"ds_read_b64_tr_b16 a[78:79], v2 offset:20992\n" \
"ds_read_b64_tr_b16 a[204:205], v3 offset:20480\n" \
"ds_read_b64_tr_b16 a[206:207], v3 offset:20992\n" \
"s_waitcnt vmcnt(20)\n" \
"s_barrier\n" \
"ds_read_b64_tr_b16 a[80:81], v2 offset:32768\n" \
"ds_read_b64_tr_b16 a[82:83], v2 offset:33280\n" \
"ds_read_b64_tr_b16 a[208:209], v3 offset:32768\n" \
"ds_read_b64_tr_b16 a[210:211], v3 offset:33280\n" \
"ds_read_b64_tr_b16 a[84:85], v2 offset:36864\n" \
"ds_read_b64_tr_b16 a[86:87], v2 offset:37376\n" \
"ds_read_b64_tr_b16 a[212:213], v3 offset:36864\n" \
"ds_read_b64_tr_b16 a[214:215], v3 offset:37376\n" \
"s_waitcnt vmcnt(16)\n" \
"s_barrier\n" \
"ds_read_b64_tr_b16 a[88:89], v2 offset:49152\n" \
"ds_read_b64_tr_b16 a[90:91], v2 offset:49664\n" \
"ds_read_b64_tr_b16 a[216:217], v3 offset:49152\n" \
"ds_read_b64_tr_b16 a[218:219], v3 offset:49664\n" \
"ds_read_b64_tr_b16 a[92:93], v2 offset:53248\n" \
"ds_read_b64_tr_b16 a[94:95], v2 offset:53760\n" \
"ds_read_b64_tr_b16 a[220:221], v3 offset:53248\n" \
"ds_read_b64_tr_b16 a[222:223], v3 offset:53760\n" \
"s_waitcnt vmcnt(12)\n" \
"s_barrier\n" \
"ds_read_b64_tr_b16 a[96:97], v8 offset:0\n" \
"ds_read_b64_tr_b16 a[98:99], v8 offset:512\n" \
"ds_read_b64_tr_b16 a[224:225], v9 offset:0\n" \
"ds_read_b64_tr_b16 a[226:227], v9 offset:512\n" \
"ds_read_b64_tr_b16 a[100:101], v8 offset:4096\n" \
"ds_read_b64_tr_b16 a[102:103], v8 offset:4608\n" \
"ds_read_b64_tr_b16 a[228:229], v9 offset:4096\n" \
"ds_read_b64_tr_b16 a[230:231], v9 offset:4608\n" \
"s_waitcnt vmcnt(8)\n" \
"s_barrier\n" \
"ds_read_b64_tr_b16 a[104:105], v8 offset:16384\n" \
"ds_read_b64_tr_b16 a[106:107], v8 offset:16896\n" \
"ds_read_b64_tr_b16 a[232:233], v9 offset:16384\n" \
"ds_read_b64_tr_b16 a[234:235], v9 offset:16896\n" \
"ds_read_b64_tr_b16 a[108:109], v8 offset:20480\n" \
"ds_read_b64_tr_b16 a[110:111], v8 offset:20992\n" \
"ds_read_b64_tr_b16 a[236:237], v9 offset:20480\n" \
"ds_read_b64_tr_b16 a[238:239], v9 offset:20992\n" \
"s_waitcnt vmcnt(4)\n" \
"s_barrier\n" \
"ds_read_b64_tr_b16 a[112:113], v8 offset:32768\n" \
"ds_read_b64_tr_b16 a[114:115], v8 offset:33280\n" \
"ds_read_b64_tr_b16 a[240:241], v9 offset:32768\n" \
"ds_read_b64_tr_b16 a[242:243], v9 offset:33280\n" \
"ds_read_b64_tr_b16 a[116:117], v8 offset:36864\n" \
"ds_read_b64_tr_b16 a[118:119], v8 offset:37376\n" \
"ds_read_b64_tr_b16 a[244:245], v9 offset:36864\n" \
"ds_read_b64_tr_b16 a[246:247], v9 offset:37376\n" \
"s_waitcnt vmcnt(0)\n" \
"s_barrier\n" \
"ds_read_b64_tr_b16 a[120:121], v8 offset:49152\n" \
"ds_read_b64_tr_b16 a[122:123], v8 offset:49664\n" \
"ds_read_b64_tr_b16 a[248:249], v9 offset:49152\n" \
"ds_read_b64_tr_b16 a[250:251], v9 offset:49664\n" \
"ds_read_b64_tr_b16 a[124:125], v8 offset:53248\n" \
"ds_read_b64_tr_b16 a[126:127], v8 offset:53760\n" \
"ds_read_b64_tr_b16 a[252:253], v9 offset:53248\n" \
"ds_read_b64_tr_b16 a[254:255], v9 offset:53760\n" \
"s_waitcnt lgkmcnt(0)\n" \
"s_barrier\n" \
"v_mov_b32 v14, 4096\n" \
"s_mov_b32 s59, s46\n" \
"s_add_u32 s63, s59, 0\n" \
"s_min_u32 s63, s63, 63\n" \
"s_lshr_b32 s63, s63, 3\n" \
"s_lshl_b32 s63, s63, 7\n" \
"s_branch .Lp1v_l0_%=\n" \
".Lp1v_l0k_%=:\n" \
"s_add_u32 s63, s59, 1\n" \
"s_min_u32 s63, s63, 63\n" \
"s_lshr_b32 s63, s63, 3\n" \
"s_lshl_b32 s63, s63, 7\n" \
"s_branch .Lp1v_l1_%=\n" \
".Lp1v_l1k_%=:\n" \
"s_lshl_b32 s55, s48, 13\n" \
"s_lshl_b32 s56, s46, 17\n" \
"s_add_u32 s56, s56, s45\n" \
"s_lshl_b32 s60, s47, 17\n" \
"s_sub_u32 s60, s60, 0x20000\n" \
"s_add_u32 s60, s60, s45\n" \
"s_mov_b32 s57, 0\n" \
"s_mov_b32 s58, 0\n" \
"v_mov_b32 v7, v5\n" \
"v_mov_b32 v5, 0x7ffffff0\n" \
"v_mov_b32 v10, v0\n" \
"v_mov_b32 v11, v6\n" \
"v_mov_b32 v6, 0x7ffffff0\n" \
"s_mov_b32 s59, s46\n" \
"s_min_u32 s61, s56, s60\n" \
"s_lshr_b32 s65, s61, 8\n" \
"s_and_b32 s65, s65, 0x2000\n" \
"s_xor_b32 s61, s61, s65\n" \
"s_add_u32 m0, s55, 0\n" \
"s_nop 0\n" \
"buffer_load_dwordx4 v4, s[36:39], s61 offen offset:0 sc1 lds\n" \
"buffer_load_dwordx4 v4, s[36:39], s61 offen offset:1024 sc1 lds\n" \
"buffer_load_dwordx4 v4, s[36:39], s61 offen offset:2048 sc1 lds\n" \
"buffer_load_dwordx4 v4, s[36:39], s61 offen offset:3072 sc1 lds\n" \
"s_add_u32 m0, s55, 4096\n" \
"s_add_u32 s61, s61, 4096\n" \
"buffer_load_dwordx4 v4, s[36:39], s61 offen offset:0 sc1 lds\n" \
"buffer_load_dwordx4 v4, s[36:39], s61 offen offset:1024 sc1 lds\n" \
"buffer_load_dwordx4 v4, s[36:39], s61 offen offset:2048 sc1 lds\n" \
"buffer_load_dwordx4 v4, s[36:39], s61 offen offset:3072 sc1 lds\n" \
"s_add_u32 s61, s61, 28672\n" \
"s_add_u32 s56, s56, 0x20000\n" \
"s_add_u32 m0, s55, 32768\n" \
"s_nop 0\n" \
"buffer_load_dwordx4 v4, s[36:39], s61 offen offset:0 sc1 lds\n" \
"buffer_load_dwordx4 v4, s[36:39], s61 offen offset:1024 sc1 lds\n" \
"buffer_load_dwordx4 v4, s[36:39], s61 offen offset:2048 sc1 lds\n" \
"buffer_load_dwordx4 v4, s[36:39], s61 offen offset:3072 sc1 lds\n" \
"s_add_u32 m0, s55, 36864\n" \
"s_add_u32 s61, s61, 4096\n" \
"buffer_load_dwordx4 v4, s[36:39], s61 offen offset:0 sc1 lds\n" \
"buffer_load_dwordx4 v4, s[36:39], s61 offen offset:1024 sc1 lds\n" \
"buffer_load_dwordx4 v4, s[36:39], s61 offen offset:2048 sc1 lds\n" \
"buffer_load_dwordx4 v4, s[36:39], s61 offen offset:3072 sc1 lds\n" \
"s_mov_b32 s70, 0\n" \
"s_mov_b32 s72, 0\n" \
"s_waitcnt vmcnt(0) lgkmcnt(0)\n" \
"s_barrier\n" \
"v_add_u32 v12, 0x10000, v10\n" \
"ds_read_b128 v[16:19], v10 offset:0\n" \
"ds_read_b128 v[20:23], v10 offset:16384\n" \
"ds_read_b128 v[24:27], v10 offset:1024\n" \
"ds_read_b128 v[28:31], v10 offset:17408\n" \
"ds_read_b128 v[32:35], v10 offset:2048\n" \
"ds_read_b128 v[36:39], v10 offset:18432\n" \
"s_waitcnt lgkmcnt(4)\n" \
"s_nop 7\n" \
"s_nop 7\n" \
".p2align 6\n" \
".Lp1v_loop_%=:\n" \
"v_mfma_f32_16x16x32_bf16 v[48:51], v[16:19], a[0:3], 0\n" \
"s_min_u32 s61, s56, s60\n" \
"s_lshr_b32 s65, s61, 8\n" \
"s_and_b32 s65, s65, 0x2000\n" \
"s_xor_b32 s61, s61, s65\n" \
"s_add_u32 m0, s55, 65536\n" \
"s_nop 0\n" \
"buffer_load_dwordx4 v4, s[36:39], s61 offen offset:0 sc1 lds\n" \
"v_mov_b32_e64 v13, s59\n" \
"v_mfma_f32_16x16x32_bf16 v[52:55], v[20:23], a[0:3], 0\n" \
"ds_read_b128 v[40:43], v10 offset:3072\n" \
"s_nop 0\n" \
"v_mfma_f32_16x16x32_bf16 v[56:59], v[16:19], a[128:131], 0\n" \
"ds_read_b128 v[44:47], v10 offset:19456\n" \
"s_nop 0\n" \
"v_mfma_f32_16x16x32_bf16 v[60:63], v[20:23], a[128:131], 0\n" \
"s_waitcnt lgkmcnt(4)\n" \
"v_cvt_pk_bf16_f32 v80, v64, v65\n" \
"v_cvt_pk_bf16_f32 v81, v66, v67\n" \
"v_mfma_f32_16x16x32_bf16 v[48:51], v[24:27], a[4:7], v[48:51]\n" \
"buffer_load_dwordx4 v4, s[36:39], s61 offen offset:1024 sc1 lds\n" \
"v_mfma_f32_16x16x32_bf16 v[52:55], v[28:31], a[4:7], v[52:55]\n" \
"ds_read_b128 v[16:19], v10 offset:4096\n" \
"v_cvt_pk_bf16_f32 v82, v68, v69\n" \
"v_mfma_f32_16x16x32_bf16 v[56:59], v[24:27], a[132:135], v[56:59]\n" \
"ds_read_b128 v[20:23], v10 offset:20480\n" \
"v_cvt_pk_bf16_f32 v83, v70, v71\n" \
"v_mfma_f32_16x16x32_bf16 v[60:63], v[28:31], a[132:135], v[60:63]\n" \
"s_waitcnt lgkmcnt(4)\n" \
"buffer_store_dwordx4 v[80:83], v5, s[36:39], s57 offen sc1\n" \
"v_cvt_pk_bf16_f32 v84, v72, v73\n" \
"v_mfma_f32_16x16x32_bf16 v[48:51], v[32:35], a[8:11], v[48:51]\n" \
"s_add_u32 s63, s59, 2\n" \
"s_min_u32 s63, s63, 63\n" \
"v_mfma_f32_16x16x32_bf16 v[52:55], v[36:39], a[8:11], v[52:55]\n" \
"ds_read_b128 v[24:27], v10 offset:5120\n" \
"s_lshr_b32 s63, s63, 3\n" \
"s_lshl_b32 s63, s63, 7\n" \
"v_cvt_pk_bf16_f32 v85, v74, v75\n" \
"v_mfma_f32_16x16x32_bf16 v[56:59], v[32:35], a[136:139], v[56:59]\n" \
"ds_read_b128 v[28:31], v10 offset:21504\n" \
"v_cvt_pk_bf16_f32 v86, v76, v77\n" \
"v_mfma_f32_16x16x32_bf16 v[60:63], v[36:39], a[136:139], v[60:63]\n" \
"s_waitcnt lgkmcnt(4)\n" \
"v_cvt_pk_bf16_f32 v87, v78, v79\n" \
"buffer_store_dwordx4 v[84:87], v5, s[36:39], s58 offen sc1\n" \
"v_mfma_f32_16x16x32_bf16 v[48:51], v[40:43], a[12:15], v[48:51]\n" \
"buffer_load_dwordx4 v4, s[36:39], s61 offen offset:2048 sc1 lds\n" \
"v_mfma_f32_16x16x32_bf16 v[52:55], v[44:47], a[12:15], v[52:55]\n" \
"ds_read_b128 v[32:35], v10 offset:6144\n" \
"v_mfma_f32_16x16x32_bf16 v[56:59], v[40:43], a[140:143], v[56:59]\n" \
"ds_read_b128 v[36:39], v10 offset:22528\n" \
"v_mfma_f32_16x16x32_bf16 v[60:63], v[44:47], a[140:143], v[60:63]\n" \
"s_waitcnt lgkmcnt(4)\n" \
"v_mfma_f32_16x16x32_bf16 v[48:51], v[16:19], a[16:19], v[48:51]\n" \
"buffer_load_dwordx4 v4, s[36:39], s61 offen offset:3072 sc1 lds\n" \
"v_mfma_f32_16x16x32_bf16 v[52:55], v[20:23], a[16:19], v[52:55]\n" \
"ds_read_b128 v[40:43], v10 offset:7168\n" \
"v_mfma_f32_16x16x32_bf16 v[56:59], v[16:19], a[144:147], v[56:59]\n" \
"ds_read_b128 v[44:47], v10 offset:23552\n" \
"v_mfma_f32_16x16x32_bf16 v[60:63], v[20:23], a[144:147], v[60:63]\n" \
"s_waitcnt lgkmcnt(4)\n" \
"v_mfma_f32_16x16x32_bf16 v[48:51], v[24:27], a[20:23], v[48:51]\n" \
"buffer_load_dword v15, v14, s[36:39], s63 offen sc1\n" \
"v_mfma_f32_16x16x32_bf16 v[52:55], v[28:31], a[20:23], v[52:55]\n" \
"ds_read_b128 v[16:19], v10 offset:8192\n" \
"v_mfma_f32_16x16x32_bf16 v[56:59], v[24:27], a[148:151], v[56:59]\n" \
"ds_read_b128 v[20:23], v10 offset:24576\n" \
"v_mfma_f32_16x16x32_bf16 v[60:63], v[28:31], a[148:151], v[60:63]\n" \
"s_waitcnt lgkmcnt(4)\n" \
"v_mfma_f32_16x16x32_bf16 v[48:51], v[32:35], a[24:27], v[48:51]\n" \
"s_add_u32 m0, s55, 69632\n" \
"s_add_u32 s61, s61, 4096\n" \
"buffer_load_dwordx4 v4, s[36:39], s61 offen offset:0 sc1 lds\n" \
"v_mfma_f32_16x16x32_bf16 v[52:55], v[36:39], a[24:27], v[52:55]\n" \
"ds_read_b128 v[24:27], v10 offset:9216\n" \
"v_mfma_f32_16x16x32_bf16 v[56:59], v[32:35], a[152:155], v[56:59]\n" \
"ds_read_b128 v[28:31], v10 offset:25600\n" \
"v_mfma_f32_16x16x32_bf16 v[60:63], v[36:39], a[152:155], v[60:63]\n" \
"s_waitcnt lgkmcnt(4)\n" \
"v_mfma_f32_16x16x32_bf16 v[48:51], v[40:43], a[28:31], v[48:51]\n" \
"buffer_load_dwordx4 v4, s[36:39], s61 offen offset:1024 sc1 lds\n" \
"v_mfma_f32_16x16x32_bf16 v[52:55], v[44:47], a[28:31], v[52:55]\n" \
"ds_read_b128 v[32:35], v10 offset:10240\n" \
"v_mfma_f32_16x16x32_bf16 v[56:59], v[40:43], a[156:159], v[56:59]\n" \
"ds_read_b128 v[36:39], v10 offset:26624\n" \
"v_mfma_f32_16x16x32_bf16 v[60:63], v[44:47], a[156:159], v[60:63]\n" \
"s_waitcnt lgkmcnt(4)\n" \
"v_mfma_f32_16x16x32_bf16 v[48:51], v[16:19], a[32:35], v[48:51]\n" \
"v_mfma_f32_16x16x32_bf16 v[52:55], v[20:23], a[32:35], v[52:55]\n" \
"ds_read_b128 v[40:43], v10 offset:11264\n" \
"v_mfma_f32_16x16x32_bf16 v[56:59], v[16:19], a[160:163], v[56:59]\n" \
"ds_read_b128 v[44:47], v10 offset:27648\n" \
"v_mfma_f32_16x16x32_bf16 v[60:63], v[20:23], a[160:163], v[60:63]\n" \
"s_waitcnt lgkmcnt(4)\n" \
"v_mfma_f32_16x16x32_bf16 v[48:51], v[24:27], a[36:39], v[48:51]\n" \
"buffer_load_dwordx4 v4, s[36:39], s61 offen offset:2048 sc1 lds\n" \
"v_mfma_f32_16x16x32_bf16 v[52:55], v[28:31], a[36:39], v[52:55]\n" \
"ds_read_b128 v[16:19], v10 offset:12288\n" \
"v_mfma_f32_16x16x32_bf16 v[56:59], v[24:27], a[164:167], v[56:59]\n" \
"ds_read_b128 v[20:23], v10 offset:28672\n" \
"v_mfma_f32_16x16x32_bf16 v[60:63], v[28:31], a[164:167], v[60:63]\n" \
"s_waitcnt lgkmcnt(4)\n" \
"v_mfma_f32_16x16x32_bf16 v[48:51], v[32:35], a[40:43], v[48:51]\n" \
"buffer_load_dwordx4 v4, s[36:39], s61 offen offset:3072 sc1 lds\n" \
"v_mfma_f32_16x16x32_bf16 v[52:55], v[36:39], a[40:43], v[52:55]\n" \
"ds_read_b128 v[24:27], v10 offset:13312\n" \
"v_mfma_f32_16x16x32_bf16 v[56:59], v[32:35], a[168:171], v[56:59]\n" \
"ds_read_b128 v[28:31], v10 offset:29696\n" \
"v_mfma_f32_16x16x32_bf16 v[60:63], v[36:39], a[168:171], v[60:63]\n" \
"s_waitcnt lgkmcnt(4)\n" \
"v_mfma_f32_16x16x32_bf16 v[48:51], v[40:43], a[44:47], v[48:51]\n" \
"v_mfma_f32_16x16x32_bf16 v[52:55], v[44:47], a[44:47], v[52:55]\n" \
"ds_read_b128 v[32:35], v10 offset:14336\n" \
"v_mfma_f32_16x16x32_bf16 v[56:59], v[40:43], a[172:175], v[56:59]\n" \
"ds_read_b128 v[36:39], v10 offset:30720\n" \
"v_mfma_f32_16x16x32_bf16 v[60:63], v[44:47], a[172:175], v[60:63]\n" \
"s_waitcnt lgkmcnt(4)\n" \
"v_mfma_f32_16x16x32_bf16 v[48:51], v[16:19], a[48:51], v[48:51]\n" \
"v_mfma_f32_16x16x32_bf16 v[52:55], v[20:23], a[48:51], v[52:55]\n" \
"ds_read_b128 v[40:43], v10 offset:15360\n" \
"v_mfma_f32_16x16x32_bf16 v[56:59], v[16:19], a[176:179], v[56:59]\n" \
"ds_read_b128 v[44:47], v10 offset:31744\n" \
"v_mfma_f32_16x16x32_bf16 v[60:63], v[20:23], a[176:179], v[60:63]\n" \
"s_waitcnt lgkmcnt(4)\n" \
"v_mfma_f32_16x16x32_bf16 v[48:51], v[24:27], a[52:55], v[48:51]\n" \
"s_waitcnt vmcnt(12)\n" \
"s_barrier\n" \
"v_mfma_f32_16x16x32_bf16 v[52:55], v[28:31], a[52:55], v[52:55]\n" \
"ds_read_b128 v[16:19], v10 offset:32768\n" \
"v_mfma_f32_16x16x32_bf16 v[56:59], v[24:27], a[180:183], v[56:59]\n" \
"ds_read_b128 v[20:23], v10 offset:49152\n" \
"v_mfma_f32_16x16x32_bf16 v[60:63], v[28:31], a[180:183], v[60:63]\n" \
"s_waitcnt lgkmcnt(4)\n" \
"v_mfma_f32_16x16x32_bf16 v[48:51], v[32:35], a[56:59], v[48:51]\n" \
"v_mfma_f32_16x16x32_bf16 v[52:55], v[36:39], a[56:59], v[52:55]\n" \
"ds_read_b128 v[24:27], v10 offset:33792\n" \
"v_mfma_f32_16x16x32_bf16 v[56:59], v[32:35], a[184:187], v[56:59]\n" \
"ds_read_b128 v[28:31], v10 offset:50176\n" \
"v_mfma_f32_16x16x32_bf16 v[60:63], v[36:39], a[184:187], v[60:63]\n" \
"s_waitcnt lgkmcnt(4)\n" \
"v_mfma_f32_16x16x32_bf16 v[48:51], v[40:43], a[60:63], v[48:51]\n" \
"v_mfma_f32_16x16x32_bf16 v[52:55], v[44:47], a[60:63], v[52:55]\n" \
"ds_read_b128 v[32:35], v10 offset:34816\n" \
"v_mfma_f32_16x16x32_bf16 v[56:59], v[40:43], a[188:191], v[56:59]\n" \
"ds_read_b128 v[36:39], v10 offset:51200\n" \
"v_mfma_f32_16x16x32_bf16 v[60:63], v[44:47], a[188:191], v[60:63]\n" \
"s_waitcnt lgkmcnt(4)\n" \
"v_mfma_f32_16x16x32_bf16 v[48:51], v[16:19], a[64:67], v[48:51]\n" \
"s_add_u32 s61, s61, 28672\n" \
"s_add_u32 s56, s56, 0x20000\n" \
"s_add_u32 m0, s55, 98304\n" \
"s_nop 0\n" \
"buffer_load_dwordx4 v4, s[36:39], s61 offen offset:0 sc1 lds\n" \
"v_mfma_f32_16x16x32_bf16 v[52:55], v[20:23], a[64:67], v[52:55]\n" \
"ds_read_b128 v[40:43], v10 offset:35840\n" \
"v_mfma_f32_16x16x32_bf16 v[56:59], v[16:19], a[192:195], v[56:59]\n" \
"ds_read_b128 v[44:47], v10 offset:52224\n" \
"v_mfma_f32_16x16x32_bf16 v[60:63], v[20:23], a[192:195], v[60:63]\n" \
"s_waitcnt lgkmcnt(4)\n" \
"v_mfma_f32_16x16x32_bf16 v[48:51], v[24:27], a[68:71], v[48:51]\n" \
"buffer_load_dwordx4 v4, s[36:39], s61 offen offset:1024 sc1 lds\n" \
"v_mfma_f32_16x16x32_bf16 v[52:55], v[28:31], a[68:71], v[52:55]\n" \
"ds_read_b128 v[16:19], v10 offset:36864\n" \
"v_mfma_f32_16x16x32_bf16 v[56:59], v[24:27], a[196:199], v[56:59]\n" \
"ds_read_b128 v[20:23], v10 offset:53248\n" \
"v_mfma_f32_16x16x32_bf16 v[60:63], v[28:31], a[196:199], v[60:63]\n" \
"s_waitcnt lgkmcnt(4)\n" \
"v_mfma_f32_16x16x32_bf16 v[48:51], v[32:35], a[72:75], v[48:51]\n" \
"v_mfma_f32_16x16x32_bf16 v[52:55], v[36:39], a[72:75], v[52:55]\n" \
"ds_read_b128 v[24:27], v10 offset:37888\n" \
"v_mfma_f32_16x16x32_bf16 v[56:59], v[32:35], a[200:203], v[56:59]\n" \
"ds_read_b128 v[28:31], v10 offset:54272\n" \
"v_mfma_f32_16x16x32_bf16 v[60:63], v[36:39], a[200:203], v[60:63]\n" \
"s_waitcnt lgkmcnt(4)\n" \
"v_mfma_f32_16x16x32_bf16 v[48:51], v[40:43], a[76:79], v[48:51]\n" \
"buffer_load_dwordx4 v4, s[36:39], s61 offen offset:2048 sc1 lds\n" \
"v_mfma_f32_16x16x32_bf16 v[52:55], v[44:47], a[76:79], v[52:55]\n" \
"ds_read_b128 v[32:35], v10 offset:38912\n" \
"v_mfma_f32_16x16x32_bf16 v[56:59], v[40:43], a[204:207], v[56:59]\n" \
"ds_read_b128 v[36:39], v10 offset:55296\n" \
"v_mfma_f32_16x16x32_bf16 v[60:63], v[44:47], a[204:207], v[60:63]\n" \
"s_waitcnt lgkmcnt(4)\n" \
"v_mfma_f32_16x16x32_bf16 v[48:51], v[16:19], a[80:83], v[48:51]\n" \
"buffer_load_dwordx4 v4, s[36:39], s61 offen offset:3072 sc1 lds\n" \
"v_mfma_f32_16x16x32_bf16 v[52:55], v[20:23], a[80:83], v[52:55]\n" \
"ds_read_b128 v[40:43], v10 offset:39936\n" \
"v_mfma_f32_16x16x32_bf16 v[56:59], v[16:19], a[208:211], v[56:59]\n" \
"ds_read_b128 v[44:47], v10 offset:56320\n" \
"v_mfma_f32_16x16x32_bf16 v[60:63], v[20:23], a[208:211], v[60:63]\n" \
"s_waitcnt lgkmcnt(4)\n" \
"v_mfma_f32_16x16x32_bf16 v[48:51], v[24:27], a[84:87], v[48:51]\n" \
"v_mfma_f32_16x16x32_bf16 v[52:55], v[28:31], a[84:87], v[52:55]\n" \
"ds_read_b128 v[16:19], v10 offset:40960\n" \
"v_mfma_f32_16x16x32_bf16 v[56:59], v[24:27], a[212:215], v[56:59]\n" \
"ds_read_b128 v[20:23], v10 offset:57344\n" \
"v_mfma_f32_16x16x32_bf16 v[60:63], v[28:31], a[212:215], v[60:63]\n" \
"s_waitcnt lgkmcnt(4)\n" \
"v_mfma_f32_16x16x32_bf16 v[48:51], v[32:35], a[88:91], v[48:51]\n" \
"s_add_u32 m0, s55, 102400\n" \
"s_add_u32 s61, s61, 4096\n" \
"buffer_load_dwordx4 v4, s[36:39], s61 offen offset:0 sc1 lds\n" \
"v_mfma_f32_16x16x32_bf16 v[52:55], v[36:39], a[88:91], v[52:55]\n" \
"ds_read_b128 v[24:27], v10 offset:41984\n" \
"v_mfma_f32_16x16x32_bf16 v[56:59], v[32:35], a[216:219], v[56:59]\n" \
"ds_read_b128 v[28:31], v10 offset:58368\n" \
"v_mfma_f32_16x16x32_bf16 v[60:63], v[36:39], a[216:219], v[60:63]\n" \
"s_waitcnt lgkmcnt(4)\n" \
"v_mfma_f32_16x16x32_bf16 v[48:51], v[40:43], a[92:95], v[48:51]\n" \
"buffer_load_dwordx4 v4, s[36:39], s61 offen offset:1024 sc1 lds\n" \
"v_mfma_f32_16x16x32_bf16 v[52:55], v[44:47], a[92:95], v[52:55]\n" \
"ds_read_b128 v[32:35], v10 offset:43008\n" \
"v_mfma_f32_16x16x32_bf16 v[56:59], v[40:43], a[220:223], v[56:59]\n" \
"ds_read_b128 v[36:39], v10 offset:59392\n" \
"v_mfma_f32_16x16x32_bf16 v[60:63], v[44:47], a[220:223], v[60:63]\n" \
"s_waitcnt lgkmcnt(4)\n" \
"v_mfma_f32_16x16x32_bf16 v[48:51], v[16:19], a[96:99], v[48:51]\n" \
"v_mfma_f32_16x16x32_bf16 v[52:55], v[20:23], a[96:99], v[52:55]\n" \
"ds_read_b128 v[40:43], v10 offset:44032\n" \
"v_mfma_f32_16x16x32_bf16 v[56:59], v[16:19], a[224:227], v[56:59]\n" \
"ds_read_b128 v[44:47], v10 offset:60416\n" \
"v_mfma_f32_16x16x32_bf16 v[60:63], v[20:23], a[224:227], v[60:63]\n" \
"s_waitcnt lgkmcnt(4)\n" \
"v_mfma_f32_16x16x32_bf16 v[48:51], v[24:27], a[100:103], v[48:51]\n" \
"buffer_load_dwordx4 v4, s[36:39], s61 offen offset:2048 sc1 lds\n" \
"v_mfma_f32_16x16x32_bf16 v[52:55], v[28:31], a[100:103], v[52:55]\n" \
"ds_read_b128 v[16:19], v10 offset:45056\n" \
"v_mfma_f32_16x16x32_bf16 v[56:59], v[24:27], a[228:231], v[56:59]\n" \
"ds_read_b128 v[20:23], v10 offset:61440\n" \
"v_mfma_f32_16x16x32_bf16 v[60:63], v[28:31], a[228:231], v[60:63]\n" \
"s_waitcnt lgkmcnt(4)\n" \
"v_mfma_f32_16x16x32_bf16 v[48:51], v[32:35], a[104:107], v[48:51]\n" \
"buffer_load_dwordx4 v4, s[36:39], s61 offen offset:3072 sc1 lds\n" \
"v_mfma_f32_16x16x32_bf16 v[52:55], v[36:39], a[104:107], v[52:55]\n" \
"ds_read_b128 v[24:27], v10 offset:46080\n" \
"v_mfma_f32_16x16x32_bf16 v[56:59], v[32:35], a[232:235], v[56:59]\n" \
"ds_read_b128 v[28:31], v10 offset:62464\n" \
"v_mfma_f32_16x16x32_bf16 v[60:63], v[36:39], a[232:235], v[60:63]\n" \
"s_waitcnt lgkmcnt(4)\n" \
"v_mfma_f32_16x16x32_bf16 v[48:51], v[40:43], a[108:111], v[48:51]\n" \
"s_lshl_b32 s66, s59, s49\n" \
"s_lshr_b32 s65, s66, 8\n" \
"v_mfma_f32_16x16x32_bf16 v[52:55], v[44:47], a[108:111], v[52:55]\n" \
"ds_read_b128 v[32:35], v10 offset:47104\n" \
"v_mfma_f32_16x16x32_bf16 v[56:59], v[40:43], a[236:239], v[56:59]\n" \
"ds_read_b128 v[36:39], v10 offset:63488\n" \
"v_mfma_f32_16x16x32_bf16 v[60:63], v[44:47], a[236:239], v[60:63]\n" \
"s_waitcnt lgkmcnt(4)\n" \
"s_and_b32 s65, s65, 0x2000\n" \
"s_xor_b32 s66, s66, s65\n" \
"v_mfma_f32_16x16x32_bf16 v[48:51], v[16:19], a[112:115], v[48:51]\n" \
"v_mfma_f32_16x16x32_bf16 v[52:55], v[20:23], a[112:115], v[52:55]\n" \
"ds_read_b128 v[40:43], v10 offset:48128\n" \
"v_mfma_f32_16x16x32_bf16 v[56:59], v[16:19], a[240:243], v[56:59]\n" \
"ds_read_b128 v[44:47], v10 offset:64512\n" \
"v_mfma_f32_16x16x32_bf16 v[60:63], v[20:23], a[240:243], v[60:63]\n" \
"s_waitcnt lgkmcnt(4)\n" \
"v_mfma_f32_16x16x32_bf16 v[48:51], v[24:27], a[116:119], v[48:51]\n" \
"s_waitcnt vmcnt(8)\n" \
"s_barrier\n" \
"v_mfma_f32_16x16x32_bf16 v[52:55], v[28:31], a[116:119], v[52:55]\n" \
"ds_read_b128 v[16:19], v12 offset:0\n" \
"v_mfma_f32_16x16x32_bf16 v[56:59], v[24:27], a[244:247], v[56:59]\n" \
"ds_read_b128 v[20:23], v12 offset:16384\n" \
"v_mfma_f32_16x16x32_bf16 v[60:63], v[28:31], a[244:247], v[60:63]\n" \
"s_waitcnt lgkmcnt(4)\n" \
"buffer_store_dword v13, v6, s[36:39], 0 offen sc1\n" \
"v_mfma_f32_16x16x32_bf16 v[48:51], v[32:35], a[120:123], v[48:51]\n" \
"v_cmp_gt_u32_e64 vcc, 16, v15\n" \
"v_mfma_f32_16x16x32_bf16 v[52:55], v[36:39], a[120:123], v[52:55]\n" \
"ds_read_b128 v[24:27], v12 offset:1024\n" \
"v_mfma_f32_16x16x32_bf16 v[56:59], v[32:35], a[248:251], v[56:59]\n" \
"ds_read_b128 v[28:31], v12 offset:17408\n" \
"v_mfma_f32_16x16x32_bf16 v[60:63], v[36:39], a[248:251], v[60:63]\n" \
"s_waitcnt lgkmcnt(4)\n" \
"v_mfma_f32_16x16x32_bf16 v[48:51], v[40:43], a[124:127], v[48:51]\n" \
"s_nop 0\n" \
"s_cbranch_vccnz .Lp1v_m0_%=\n" \
".Lp1v_m0k_%=:\n" \
"v_mfma_f32_16x16x32_bf16 v[52:55], v[44:47], a[124:127], v[52:55]\n" \
"ds_read_b128 v[32:35], v12 offset:2048\n" \
"v_mfma_f32_16x16x32_bf16 v[56:59], v[40:43], a[252:255], v[56:59]\n" \
"ds_read_b128 v[36:39], v12 offset:18432\n" \
"v_mfma_f32_16x16x32_bf16 v[60:63], v[44:47], a[252:255], v[60:63]\n" \
"s_waitcnt lgkmcnt(4)\n" \
"s_xor_b32 s57, s66, s64\n" \
"s_add_u32 s58, s57, s50\n" \
"s_add_u32 s59, s59, 1\n" \
"v_mov_b32 v5, v7\n" \
"v_mov_b32 v6, v11\n" \
"s_cmp_ge_u32 s59, s47\n" \
"s_cbranch_scc1 .Lp1v_out0_%=\n" \
"v_mfma_f32_16x16x32_bf16 v[64:67], v[16:19], a[0:3], 0\n" \
"s_min_u32 s61, s56, s60\n" \
"s_lshr_b32 s65, s61, 8\n" \
"s_and_b32 s65, s65, 0x2000\n" \
"s_xor_b32 s61, s61, s65\n" \
"s_add_u32 m0, s55, 0\n" \
"s_nop 0\n" \
"buffer_load_dwordx4 v4, s[36:39], s61 offen offset:0 sc1 lds\n" \
"v_mov_b32_e64 v13, s59\n" \
"v_mfma_f32_16x16x32_bf16 v[68:71], v[20:23], a[0:3], 0\n" \
"ds_read_b128 v[40:43], v12 offset:3072\n" \
"s_nop 0\n" \
"v_mfma_f32_16x16x32_bf16 v[72:75], v[16:19], a[128:131], 0\n" \
"ds_read_b128 v[44:47], v12 offset:19456\n" \
"s_nop 0\n" \
"v_mfma_f32_16x16x32_bf16 v[76:79], v[20:23], a[128:131], 0\n" \
"s_waitcnt lgkmcnt(4)\n" \
"v_cvt_pk_bf16_f32 v80, v48, v49\n" \
"v_cvt_pk_bf16_f32 v81, v50, v51\n" \
"v_mfma_f32_16x16x32_bf16 v[64:67], v[24:27], a[4:7], v[64:67]\n" \
"buffer_load_dwordx4 v4, s[36:39], s61 offen offset:1024 sc1 lds\n" \
"v_mfma_f32_16x16x32_bf16 v[68:71], v[28:31], a[4:7], v[68:71]\n" \
"ds_read_b128 v[16:19], v12 offset:4096\n" \
"v_cvt_pk_bf16_f32 v82, v52, v53\n" \
"v_mfma_f32_16x16x32_bf16 v[72:75], v[24:27], a[132:135], v[72:75]\n" \
"ds_read_b128 v[20:23], v12 offset:20480\n" \
"v_cvt_pk_bf16_f32 v83, v54, v55\n" \
"v_mfma_f32_16x16x32_bf16 v[76:79], v[28:31], a[132:135], v[76:79]\n" \
"s_waitcnt lgkmcnt(4)\n" \
"buffer_store_dwordx4 v[80:83], v5, s[36:39], s57 offen sc1\n" \
"v_cvt_pk_bf16_f32 v84, v56, v57\n" \
"v_mfma_f32_16x16x32_bf16 v[64:67], v[32:35], a[8:11], v[64:67]\n" \
"s_add_u32 s63, s59, 2\n" \
"s_min_u32 s63, s63, 63\n" \
"v_mfma_f32_16x16x32_bf16 v[68:71], v[36:39], a[8:11], v[68:71]\n" \
"ds_read_b128 v[24:27], v12 offset:5120\n" \
"s_lshr_b32 s63, s63, 3\n" \
"s_lshl_b32 s63, s63, 7\n" \
"v_cvt_pk_bf16_f32 v85, v58, v59\n" \
"v_mfma_f32_16x16x32_bf16 v[72:75], v[32:35], a[136:139], v[72:75]\n" \
"ds_read_b128 v[28:31], v12 offset:21504\n" \
"v_cvt_pk_bf16_f32 v86, v60, v61\n" \
"v_mfma_f32_16x16x32_bf16 v[76:79], v[36:39], a[136:139], v[76:79]\n" \
"s_waitcnt lgkmcnt(4)\n" \
"v_cvt_pk_bf16_f32 v87, v62, v63\n" \
"buffer_store_dwordx4 v[84:87], v5, s[36:39], s58 offen sc1\n" \
"v_mfma_f32_16x16x32_bf16 v[64:67], v[40:43], a[12:15], v[64:67]\n" \
"buffer_load_dwordx4 v4, s[36:39], s61 offen offset:2048 sc1 lds\n" \
"v_mfma_f32_16x16x32_bf16 v[68:71], v[44:47], a[12:15], v[68:71]\n" \
"ds_read_b128 v[32:35], v12 offset:6144\n" \
"v_mfma_f32_16x16x32_bf16 v[72:75], v[40:43], a[140:143], v[72:75]\n" \
"ds_read_b128 v[36:39], v12 offset:22528\n" \
"v_mfma_f32_16x16x32_bf16 v[76:79], v[44:47], a[140:143], v[76:79]\n" \
"s_waitcnt lgkmcnt(4)\n" \
"v_mfma_f32_16x16x32_bf16 v[64:67], v[16:19], a[16:19], v[64:67]\n" \
"buffer_load_dwordx4 v4, s[36:39], s61 offen offset:3072 sc1 lds\n" \
"v_mfma_f32_16x16x32_bf16 v[68:71], v[20:23], a[16:19], v[68:71]\n" \
"ds_read_b128 v[40:43], v12 offset:7168\n" \
"v_mfma_f32_16x16x32_bf16 v[72:75], v[16:19], a[144:147], v[72:75]\n" \
"ds_read_b128 v[44:47], v12 offset:23552\n" \
"v_mfma_f32_16x16x32_bf16 v[76:79], v[20:23], a[144:147], v[76:79]\n" \
"s_waitcnt lgkmcnt(4)\n" \
"v_mfma_f32_16x16x32_bf16 v[64:67], v[24:27], a[20:23], v[64:67]\n" \
"buffer_load_dword v15, v14, s[36:39], s63 offen sc1\n" \
"v_mfma_f32_16x16x32_bf16 v[68:71], v[28:31], a[20:23], v[68:71]\n" \
"ds_read_b128 v[16:19], v12 offset:8192\n" \
"v_mfma_f32_16x16x32_bf16 v[72:75], v[24:27], a[148:151], v[72:75]\n" \
"ds_read_b128 v[20:23], v12 offset:24576\n" \
"v_mfma_f32_16x16x32_bf16 v[76:79], v[28:31], a[148:151], v[76:79]\n" \
"s_waitcnt lgkmcnt(4)\n" \
"v_mfma_f32_16x16x32_bf16 v[64:67], v[32:35], a[24:27], v[64:67]\n" \
"s_add_u32 m0, s55, 4096\n" \
"s_add_u32 s61, s61, 4096\n" \
"buffer_load_dwordx4 v4, s[36:39], s61 offen offset:0 sc1 lds\n" \
"v_mfma_f32_16x16x32_bf16 v[68:71], v[36:39], a[24:27], v[68:71]\n" \
"ds_read_b128 v[24:27], v12 offset:9216\n" \
"v_mfma_f32_16x16x32_bf16 v[72:75], v[32:35], a[152:155], v[72:75]\n" \
"ds_read_b128 v[28:31], v12 offset:25600\n" \
"v_mfma_f32_16x16x32_bf16 v[76:79], v[36:39], a[152:155], v[76:79]\n" \
"s_waitcnt lgkmcnt(4)\n" \
"v_mfma_f32_16x16x32_bf16 v[64:67], v[40:43], a[28:31], v[64:67]\n" \
"buffer_load_dwordx4 v4, s[36:39], s61 offen offset:1024 sc1 lds\n" \
"v_mfma_f32_16x16x32_bf16 v[68:71], v[44:47], a[28:31], v[68:71]\n" \
"ds_read_b128 v[32:35], v12 offset:10240\n" \
"v_mfma_f32_16x16x32_bf16 v[72:75], v[40:43], a[156:159], v[72:75]\n" \
"ds_read_b128 v[36:39], v12 offset:26624\n" \
"v_mfma_f32_16x16x32_bf16 v[76:79], v[44:47], a[156:159], v[76:79]\n" \
"s_waitcnt lgkmcnt(4)\n" \
"v_mfma_f32_16x16x32_bf16 v[64:67], v[16:19], a[32:35], v[64:67]\n" \
"v_mfma_f32_16x16x32_bf16 v[68:71], v[20:23], a[32:35], v[68:71]\n" \
"ds_read_b128 v[40:43], v12 offset:11264\n" \
"v_mfma_f32_16x16x32_bf16 v[72:75], v[16:19], a[160:163], v[72:75]\n" \
"ds_read_b128 v[44:47], v12 offset:27648\n" \
"v_mfma_f32_16x16x32_bf16 v[76:79], v[20:23], a[160:163], v[76:79]\n" \
"s_waitcnt lgkmcnt(4)\n" \
"v_mfma_f32_16x16x32_bf16 v[64:67], v[24:27], a[36:39], v[64:67]\n" \
"buffer_load_dwordx4 v4, s[36:39], s61 offen offset:2048 sc1 lds\n" \
"v_mfma_f32_16x16x32_bf16 v[68:71], v[28:31], a[36:39], v[68:71]\n" \
"ds_read_b128 v[16:19], v12 offset:12288\n" \
"v_mfma_f32_16x16x32_bf16 v[72:75], v[24:27], a[164:167], v[72:75]\n" \
"ds_read_b128 v[20:23], v12 offset:28672\n" \
"v_mfma_f32_16x16x32_bf16 v[76:79], v[28:31], a[164:167], v[76:79]\n" \
"s_waitcnt lgkmcnt(4)\n" \
"v_mfma_f32_16x16x32_bf16 v[64:67], v[32:35], a[40:43], v[64:67]\n" \
"buffer_load_dwordx4 v4, s[36:39], s61 offen offset:3072 sc1 lds\n" \
"v_mfma_f32_16x16x32_bf16 v[68:71], v[36:39], a[40:43], v[68:71]\n" \
"ds_read_b128 v[24:27], v12 offset:13312\n" \
"v_mfma_f32_16x16x32_bf16 v[72:75], v[32:35], a[168:171], v[72:75]\n" \
"ds_read_b128 v[28:31], v12 offset:29696\n" \
"v_mfma_f32_16x16x32_bf16 v[76:79], v[36:39], a[168:171], v[76:79]\n" \
"s_waitcnt lgkmcnt(4)\n" \
"v_mfma_f32_16x16x32_bf16 v[64:67], v[40:43], a[44:47], v[64:67]\n" \
"v_mfma_f32_16x16x32_bf16 v[68:71], v[44:47], a[44:47], v[68:71]\n" \
"ds_read_b128 v[32:35], v12 offset:14336\n" \
"v_mfma_f32_16x16x32_bf16 v[72:75], v[40:43], a[172:175], v[72:75]\n" \
"ds_read_b128 v[36:39], v12 offset:30720\n" \
"v_mfma_f32_16x16x32_bf16 v[76:79], v[44:47], a[172:175], v[76:79]\n" \
"s_waitcnt lgkmcnt(4)\n" \
"v_mfma_f32_16x16x32_bf16 v[64:67], v[16:19], a[48:51], v[64:67]\n" \
"v_mfma_f32_16x16x32_bf16 v[68:71], v[20:23], a[48:51], v[68:71]\n" \
"ds_read_b128 v[40:43], v12 offset:15360\n" \
"v_mfma_f32_16x16x32_bf16 v[72:75], v[16:19], a[176:179], v[72:75]\n" \
"ds_read_b128 v[44:47], v12 offset:31744\n" \
"v_mfma_f32_16x16x32_bf16 v[76:79], v[20:23], a[176:179], v[76:79]\n" \
"s_waitcnt lgkmcnt(4)\n" \
"v_mfma_f32_16x16x32_bf16 v[64:67], v[24:27], a[52:55], v[64:67]\n" \
"s_waitcnt vmcnt(12)\n" \
"s_barrier\n" \
"v_mfma_f32_16x16x32_bf16 v[68:71], v[28:31], a[52:55], v[68:71]\n" \
"ds_read_b128 v[16:19], v12 offset:32768\n" \
"v_mfma_f32_16x16x32_bf16 v[72:75], v[24:27], a[180:183], v[72:75]\n" \
"ds_read_b128 v[20:23], v12 offset:49152\n" \
"v_mfma_f32_16x16x32_bf16 v[76:79], v[28:31], a[180:183], v[76:79]\n" \
"s_waitcnt lgkmcnt(4)\n" \
"v_mfma_f32_16x16x32_bf16 v[64:67], v[32:35], a[56:59], v[64:67]\n" \
"v_mfma_f32_16x16x32_bf16 v[68:71], v[36:39], a[56:59], v[68:71]\n" \
"ds_read_b128 v[24:27], v12 offset:33792\n" \
"v_mfma_f32_16x16x32_bf16 v[72:75], v[32:35], a[184:187], v[72:75]\n" \
"ds_read_b128 v[28:31], v12 offset:50176\n" \
"v_mfma_f32_16x16x32_bf16 v[76:79], v[36:39], a[184:187], v[76:79]\n" \
"s_waitcnt lgkmcnt(4)\n" \
"v_mfma_f32_16x16x32_bf16 v[64:67], v[40:43], a[60:63], v[64:67]\n" \
"v_mfma_f32_16x16x32_bf16 v[68:71], v[44:47], a[60:63], v[68:71]\n" \
"ds_read_b128 v[32:35], v12 offset:34816\n" \
"v_mfma_f32_16x16x32_bf16 v[72:75], v[40:43], a[188:191], v[72:75]\n" \
"ds_read_b128 v[36:39], v12 offset:51200\n" \
"v_mfma_f32_16x16x32_bf16 v[76:79], v[44:47], a[188:191], v[76:79]\n" \
"s_waitcnt lgkmcnt(4)\n" \
"v_mfma_f32_16x16x32_bf16 v[64:67], v[16:19], a[64:67], v[64:67]\n" \
"s_add_u32 s61, s61, 28672\n" \
"s_add_u32 s56, s56, 0x20000\n" \
"s_add_u32 m0, s55, 32768\n" \
"s_nop 0\n" \
"buffer_load_dwordx4 v4, s[36:39], s61 offen offset:0 sc1 lds\n" \
"v_mfma_f32_16x16x32_bf16 v[68:71], v[20:23], a[64:67], v[68:71]\n" \
"ds_read_b128 v[40:43], v12 offset:35840\n" \
"v_mfma_f32_16x16x32_bf16 v[72:75], v[16:19], a[192:195], v[72:75]\n" \
"ds_read_b128 v[44:47], v12 offset:52224\n" \
"v_mfma_f32_16x16x32_bf16 v[76:79], v[20:23], a[192:195], v[76:79]\n" \
"s_waitcnt lgkmcnt(4)\n" \
"v_mfma_f32_16x16x32_bf16 v[64:67], v[24:27], a[68:71], v[64:67]\n" \
"buffer_load_dwordx4 v4, s[36:39], s61 offen offset:1024 sc1 lds\n" \
"v_mfma_f32_16x16x32_bf16 v[68:71], v[28:31], a[68:71], v[68:71]\n" \
"ds_read_b128 v[16:19], v12 offset:36864\n" \
"v_mfma_f32_16x16x32_bf16 v[72:75], v[24:27], a[196:199], v[72:75]\n" \
"ds_read_b128 v[20:23], v12 offset:53248\n" \
"v_mfma_f32_16x16x32_bf16 v[76:79], v[28:31], a[196:199], v[76:79]\n" \
"s_waitcnt lgkmcnt(4)\n" \
"v_mfma_f32_16x16x32_bf16 v[64:67], v[32:35], a[72:75], v[64:67]\n" \
"v_mfma_f32_16x16x32_bf16 v[68:71], v[36:39], a[72:75], v[68:71]\n" \
"ds_read_b128 v[24:27], v12 offset:37888\n" \
"v_mfma_f32_16x16x32_bf16 v[72:75], v[32:35], a[200:203], v[72:75]\n" \
"ds_read_b128 v[28:31], v12 offset:54272\n" \
"v_mfma_f32_16x16x32_bf16 v[76:79], v[36:39], a[200:203], v[76:79]\n" \
"s_waitcnt lgkmcnt(4)\n" \
"v_mfma_f32_16x16x32_bf16 v[64:67], v[40:43], a[76:79], v[64:67]\n" \
"buffer_load_dwordx4 v4, s[36:39], s61 offen offset:2048 sc1 lds\n" \
"v_mfma_f32_16x16x32_bf16 v[68:71], v[44:47], a[76:79], v[68:71]\n" \
"ds_read_b128 v[32:35], v12 offset:38912\n" \
"v_mfma_f32_16x16x32_bf16 v[72:75], v[40:43], a[204:207], v[72:75]\n" \
"ds_read_b128 v[36:39], v12 offset:55296\n" \
"v_mfma_f32_16x16x32_bf16 v[76:79], v[44:47], a[204:207], v[76:79]\n" \
"s_waitcnt lgkmcnt(4)\n" \
"v_mfma_f32_16x16x32_bf16 v[64:67], v[16:19], a[80:83], v[64:67]\n" \
"buffer_load_dwordx4 v4, s[36:39], s61 offen offset:3072 sc1 lds\n" \
"v_mfma_f32_16x16x32_bf16 v[68:71], v[20:23], a[80:83], v[68:71]\n" \
"ds_read_b128 v[40:43], v12 offset:39936\n" \
"v_mfma_f32_16x16x32_bf16 v[72:75], v[16:19], a[208:211], v[72:75]\n" \
"ds_read_b128 v[44:47], v12 offset:56320\n" \
"v_mfma_f32_16x16x32_bf16 v[76:79], v[20:23], a[208:211], v[76:79]\n" \
"s_waitcnt lgkmcnt(4)\n" \
"v_mfma_f32_16x16x32_bf16 v[64:67], v[24:27], a[84:87], v[64:67]\n" \
"v_mfma_f32_16x16x32_bf16 v[68:71], v[28:31], a[84:87], v[68:71]\n" \
"ds_read_b128 v[16:19], v12 offset:40960\n" \
"v_mfma_f32_16x16x32_bf16 v[72:75], v[24:27], a[212:215], v[72:75]\n" \
"ds_read_b128 v[20:23], v12 offset:57344\n" \
"v_mfma_f32_16x16x32_bf16 v[76:79], v[28:31], a[212:215], v[76:79]\n" \
"s_waitcnt lgkmcnt(4)\n" \
"v_mfma_f32_16x16x32_bf16 v[64:67], v[32:35], a[88:91], v[64:67]\n" \
"s_add_u32 m0, s55, 36864\n" \
"s_add_u32 s61, s61, 4096\n" \
"buffer_load_dwordx4 v4, s[36:39], s61 offen offset:0 sc1 lds\n" \
"v_mfma_f32_16x16x32_bf16 v[68:71], v[36:39], a[88:91], v[68:71]\n" \
"ds_read_b128 v[24:27], v12 offset:41984\n" \
"v_mfma_f32_16x16x32_bf16 v[72:75], v[32:35], a[216:219], v[72:75]\n" \
"ds_read_b128 v[28:31], v12 offset:58368\n" \
"v_mfma_f32_16x16x32_bf16 v[76:79], v[36:39], a[216:219], v[76:79]\n" \
"s_waitcnt lgkmcnt(4)\n" \
"v_mfma_f32_16x16x32_bf16 v[64:67], v[40:43], a[92:95], v[64:67]\n" \
"buffer_load_dwordx4 v4, s[36:39], s61 offen offset:1024 sc1 lds\n" \
"v_mfma_f32_16x16x32_bf16 v[68:71], v[44:47], a[92:95], v[68:71]\n" \
"ds_read_b128 v[32:35], v12 offset:43008\n" \
"v_mfma_f32_16x16x32_bf16 v[72:75], v[40:43], a[220:223], v[72:75]\n" \
"ds_read_b128 v[36:39], v12 offset:59392\n" \
"v_mfma_f32_16x16x32_bf16 v[76:79], v[44:47], a[220:223], v[76:79]\n" \
"s_waitcnt lgkmcnt(4)\n" \
"v_mfma_f32_16x16x32_bf16 v[64:67], v[16:19], a[96:99], v[64:67]\n" \
"v_mfma_f32_16x16x32_bf16 v[68:71], v[20:23], a[96:99], v[68:71]\n" \
"ds_read_b128 v[40:43], v12 offset:44032\n" \
"v_mfma_f32_16x16x32_bf16 v[72:75], v[16:19], a[224:227], v[72:75]\n" \
"ds_read_b128 v[44:47], v12 offset:60416\n" \
"v_mfma_f32_16x16x32_bf16 v[76:79], v[20:23], a[224:227], v[76:79]\n" \
"s_waitcnt lgkmcnt(4)\n" \
"v_mfma_f32_16x16x32_bf16 v[64:67], v[24:27], a[100:103], v[64:67]\n" \
"buffer_load_dwordx4 v4, s[36:39], s61 offen offset:2048 sc1 lds\n" \
"v_mfma_f32_16x16x32_bf16 v[68:71], v[28:31], a[100:103], v[68:71]\n" \
"ds_read_b128 v[16:19], v12 offset:45056\n" \
"v_mfma_f32_16x16x32_bf16 v[72:75], v[24:27], a[228:231], v[72:75]\n" \
"ds_read_b128 v[20:23], v12 offset:61440\n" \
"v_mfma_f32_16x16x32_bf16 v[76:79], v[28:31], a[228:231], v[76:79]\n" \
"s_waitcnt lgkmcnt(4)\n" \
"v_mfma_f32_16x16x32_bf16 v[64:67], v[32:35], a[104:107], v[64:67]\n" \
"buffer_load_dwordx4 v4, s[36:39], s61 offen offset:3072 sc1 lds\n" \
"v_mfma_f32_16x16x32_bf16 v[68:71], v[36:39], a[104:107], v[68:71]\n" \
"ds_read_b128 v[24:27], v12 offset:46080\n" \
"v_mfma_f32_16x16x32_bf16 v[72:75], v[32:35], a[232:235], v[72:75]\n" \
"ds_read_b128 v[28:31], v12 offset:62464\n" \
"v_mfma_f32_16x16x32_bf16 v[76:79], v[36:39], a[232:235], v[76:79]\n" \
"s_waitcnt lgkmcnt(4)\n" \
"v_mfma_f32_16x16x32_bf16 v[64:67], v[40:43], a[108:111], v[64:67]\n" \
"s_lshl_b32 s66, s59, s49\n" \
"s_lshr_b32 s65, s66, 8\n" \
"v_mfma_f32_16x16x32_bf16 v[68:71], v[44:47], a[108:111], v[68:71]\n" \
"ds_read_b128 v[32:35], v12 offset:47104\n" \
"v_mfma_f32_16x16x32_bf16 v[72:75], v[40:43], a[236:239], v[72:75]\n" \
"ds_read_b128 v[36:39], v12 offset:63488\n" \
"v_mfma_f32_16x16x32_bf16 v[76:79], v[44:47], a[236:239], v[76:79]\n" \
"s_waitcnt lgkmcnt(4)\n" \
"s_and_b32 s65, s65, 0x2000\n" \
"s_xor_b32 s66, s66, s65\n" \
"v_mfma_f32_16x16x32_bf16 v[64:67], v[16:19], a[112:115], v[64:67]\n" \
"v_mfma_f32_16x16x32_bf16 v[68:71], v[20:23], a[112:115], v[68:71]\n" \
"ds_read_b128 v[40:43], v12 offset:48128\n" \
"v_mfma_f32_16x16x32_bf16 v[72:75], v[16:19], a[240:243], v[72:75]\n" \
"ds_read_b128 v[44:47], v12 offset:64512\n" \
"v_mfma_f32_16x16x32_bf16 v[76:79], v[20:23], a[240:243], v[76:79]\n" \
"s_waitcnt lgkmcnt(4)\n" \
"v_mfma_f32_16x16x32_bf16 v[64:67], v[24:27], a[116:119], v[64:67]\n" \
"s_waitcnt vmcnt(8)\n" \
"s_barrier\n" \
"v_mfma_f32_16x16x32_bf16 v[68:71], v[28:31], a[116:119], v[68:71]\n" \
"ds_read_b128 v[16:19], v10 offset:0\n" \
"v_mfma_f32_16x16x32_bf16 v[72:75], v[24:27], a[244:247], v[72:75]\n" \
"ds_read_b128 v[20:23], v10 offset:16384\n" \
"v_mfma_f32_16x16x32_bf16 v[76:79], v[28:31], a[244:247], v[76:79]\n" \
"s_waitcnt lgkmcnt(4)\n" \
"buffer_store_dword v13, v6, s[36:39], 0 offen sc1\n" \
"v_mfma_f32_16x16x32_bf16 v[64:67], v[32:35], a[120:123], v[64:67]\n" \
"v_cmp_gt_u32_e64 vcc, 16, v15\n" \
"v_mfma_f32_16x16x32_bf16 v[68:71], v[36:39], a[120:123], v[68:71]\n" \
"ds_read_b128 v[24:27], v10 offset:1024\n" \
"v_mfma_f32_16x16x32_bf16 v[72:75], v[32:35], a[248:251], v[72:75]\n" \
"ds_read_b128 v[28:31], v10 offset:17408\n" \
"v_mfma_f32_16x16x32_bf16 v[76:79], v[36:39], a[248:251], v[76:79]\n" \
"s_waitcnt lgkmcnt(4)\n" \
"v_mfma_f32_16x16x32_bf16 v[64:67], v[40:43], a[124:127], v[64:67]\n" \
"s_cbranch_vccnz .Lp1v_m1_%=\n" \
".Lp1v_m1k_%=:\n" \
"v_mfma_f32_16x16x32_bf16 v[68:71], v[44:47], a[124:127], v[68:71]\n" \
"ds_read_b128 v[32:35], v10 offset:2048\n" \
"v_mfma_f32_16x16x32_bf16 v[72:75], v[40:43], a[252:255], v[72:75]\n" \
"ds_read_b128 v[36:39], v10 offset:18432\n" \
"v_mfma_f32_16x16x32_bf16 v[76:79], v[44:47], a[252:255], v[76:79]\n" \
"s_waitcnt lgkmcnt(4)\n" \
"s_xor_b32 s57, s66, s64\n" \
"s_add_u32 s58, s57, s50\n" \
"s_add_u32 s59, s59, 1\n" \
"v_mov_b32 v5, v7\n" \
"v_mov_b32 v6, v11\n" \
"s_cmp_ge_u32 s59, s47\n" \
"s_cbranch_scc0 .Lp1v_loop_%=\n" \
".Lp1v_out1_%=:\n" \
"s_nop 7\n" \
"s_nop 7\n" \
"v_cvt_pk_bf16_f32 v80, v64, v65\n" \
"v_cvt_pk_bf16_f32 v81, v66, v67\n" \
"v_cvt_pk_bf16_f32 v82, v68, v69\n" \
"v_cvt_pk_bf16_f32 v83, v70, v71\n" \
"buffer_store_dwordx4 v[80:83], v5, s[36:39], s57 offen sc1\n" \
"v_cvt_pk_bf16_f32 v84, v72, v73\n" \
"v_cvt_pk_bf16_f32 v85, v74, v75\n" \
"v_cvt_pk_bf16_f32 v86, v76, v77\n" \
"v_cvt_pk_bf16_f32 v87, v78, v79\n" \
"buffer_store_dwordx4 v[84:87], v5, s[36:39], s58 offen sc1\n" \
"s_branch .Lp1v_end_%=\n" \
".Lp1v_out0_%=:\n" \
"s_nop 7\n" \
"s_nop 7\n" \
"v_cvt_pk_bf16_f32 v80, v48, v49\n" \
"v_cvt_pk_bf16_f32 v81, v50, v51\n" \
"v_cvt_pk_bf16_f32 v82, v52, v53\n" \
"v_cvt_pk_bf16_f32 v83, v54, v55\n" \
"buffer_store_dwordx4 v[80:83], v5, s[36:39], s57 offen sc1\n" \
"v_cvt_pk_bf16_f32 v84, v56, v57\n" \
"v_cvt_pk_bf16_f32 v85, v58, v59\n" \
"v_cvt_pk_bf16_f32 v86, v60, v61\n" \
"v_cvt_pk_bf16_f32 v87, v62, v63\n" \
"buffer_store_dwordx4 v[84:87], v5, s[36:39], s58 offen sc1\n" \
".Lp1v_end_%=:\n" \
"s_waitcnt vmcnt(0) lgkmcnt(0)\n" \
"s_barrier\n" \
"v_mov_b32 v13, s59\n" \
"buffer_store_dword v13, v6, s[36:39], 0 offen sc1\n" \
"s_waitcnt vmcnt(0) lgkmcnt(0)\n" \
"s_branch .Lp1v_x_%=\n" \
".Lp1v_l0_%=:\n" \
"s_nop 7\n" \
"s_nop 7\n" \
"s_waitcnt vmcnt(0) lgkmcnt(0)\n" \
"s_mov_b32 s62, 0\n" \
".Lp1v_l0a_%=:\n" \
"buffer_load_dword v15, v14, s[36:39], s63 offen sc1\n" \
"s_waitcnt vmcnt(0)\n" \
"v_cmp_gt_u32 vcc, 16, v15\n" \
"s_nop 4\n" \
"s_cbranch_vccz .Lp1v_l0k_%=\n" \
"s_sleep 4\n" \
"s_add_u32 s62, s62, 1\n" \
"s_cmp_lt_u32 s62, 0x10000\n" \
"s_cbranch_scc1 .Lp1v_l0a_%=\n" \
"v_mov_b32 v15, 256\n" \
"buffer_store_dword v15, v15, s[36:39], 0 offen sc1\n" \
"s_waitcnt vmcnt(0) lgkmcnt(0)\n" \
"s_branch .Lp1v_l0k_%=\n" \
".Lp1v_l1_%=:\n" \
"s_nop 7\n" \
"s_nop 7\n" \
"s_waitcnt vmcnt(0) lgkmcnt(0)\n" \
"s_mov_b32 s62, 0\n" \
".Lp1v_l1a_%=:\n" \
"buffer_load_dword v15, v14, s[36:39], s63 offen sc1\n" \
"s_waitcnt vmcnt(0)\n" \
"v_cmp_gt_u32 vcc, 16, v15\n" \
"s_nop 4\n" \
"s_cbranch_vccz .Lp1v_l1k_%=\n" \
"s_sleep 4\n" \
"s_add_u32 s62, s62, 1\n" \
"s_cmp_lt_u32 s62, 0x10000\n" \
"s_cbranch_scc1 .Lp1v_l1a_%=\n" \
"v_mov_b32 v15, 256\n" \
"buffer_store_dword v15, v15, s[36:39], 0 offen sc1\n" \
"s_waitcnt vmcnt(0) lgkmcnt(0)\n" \
"s_branch .Lp1v_l1k_%=\n" \
".Lp1v_m0_%=:\n" \
"s_nop 7\n" \
"s_nop 7\n" \
"s_waitcnt vmcnt(0) lgkmcnt(0)\n" \
"s_mov_b32 s62, 0\n" \
".Lp1v_m0a_%=:\n" \
"buffer_load_dword v15, v14, s[36:39], s63 offen sc1\n" \
"s_waitcnt vmcnt(0)\n" \
"v_cmp_gt_u32 vcc, 16, v15\n" \
"s_nop 4\n" \
"s_cbranch_vccz .Lp1v_m0k_%=\n" \
"s_sleep 4\n" \
"s_add_u32 s62, s62, 1\n" \
"s_cmp_lt_u32 s62, 0x10000\n" \
"s_cbranch_scc1 .Lp1v_m0a_%=\n" \
"v_mov_b32 v15, 256\n" \
"buffer_store_dword v15, v15, s[36:39], 0 offen sc1\n" \
"s_waitcnt vmcnt(0) lgkmcnt(0)\n" \
"s_branch .Lp1v_m0k_%=\n" \
".Lp1v_m1_%=:\n" \
"s_nop 7\n" \
"s_nop 7\n" \
"s_waitcnt vmcnt(0) lgkmcnt(0)\n" \
"s_mov_b32 s62, 0\n" \
".Lp1v_m1a_%=:\n" \
"buffer_load_dword v15, v14, s[36:39], s63 offen sc1\n" \
"s_waitcnt vmcnt(0)\n" \
"v_cmp_gt_u32 vcc, 16, v15\n" \
"s_nop 4\n" \
"s_cbranch_vccz .Lp1v_m1k_%=\n" \
"s_sleep 4\n" \
"s_add_u32 s62, s62, 1\n" \
"s_cmp_lt_u32 s62, 0x10000\n" \
"s_cbranch_scc1 .Lp1v_m1a_%=\n" \
"v_mov_b32 v15, 256\n" \
"buffer_store_dword v15, v15, s[36:39], 0 offen sc1\n" \
"s_waitcnt vmcnt(0) lgkmcnt(0)\n" \
"s_branch .Lp1v_m1k_%=\n" \
".Lp1v_x_%=:\n"
#define ASM_P1_G1 \
"s_nop 7\n" \
"s_nop 7\n" \
"s_lshr_b32 s52, s48, 1\n" \
"s_lshl_b32 s52, s52, 13\n" \
"s_and_b32 s53, s48, 1\n" \
"s_lshl_b32 s53, s53, 10\n" \
"s_add_u32 s52, s52, s53\n" \
"s_mov_b32 s54, 0\n" \
"s_add_u32 m0, s52, 0\n" \
"s_nop 0\n" \
"buffer_load_dwordx4 v1, s[40:43], s54 offen nt lds\n" \
"s_add_u32 s54, s54, s44\n" \
"s_add_u32 m0, s52, 2048\n" \
"s_nop 0\n" \
"buffer_load_dwordx4 v1, s[40:43], s54 offen nt lds\n" \
"s_add_u32 s54, s54, s44\n" \
"s_add_u32 m0, s52, 4096\n" \
"s_nop 0\n" \
"buffer_load_dwordx4 v1, s[40:43], s54 offen nt lds\n" \
"s_add_u32 s54, s54, s44\n" \
"s_add_u32 m0, s52, 6144\n" \
"s_nop 0\n" \
"buffer_load_dwordx4 v1, s[40:43], s54 offen nt lds\n" \
"s_add_u32 s54, s54, s44\n" \
"s_add_u32 m0, s52, 16384\n" \
"s_nop 0\n" \
"buffer_load_dwordx4 v1, s[40:43], s54 offen nt lds\n" \
"s_add_u32 s54, s54, s44\n" \
"s_add_u32 m0, s52, 18432\n" \
"s_nop 0\n" \
"buffer_load_dwordx4 v1, s[40:43], s54 offen nt lds\n" \
"s_add_u32 s54, s54, s44\n" \
"s_add_u32 m0, s52, 20480\n" \
"s_nop 0\n" \
"buffer_load_dwordx4 v1, s[40:43], s54 offen nt lds\n" \
"s_add_u32 s54, s54, s44\n" \
"s_add_u32 m0, s52, 22528\n" \
"s_nop 0\n" \
"buffer_load_dwordx4 v1, s[40:43], s54 offen nt lds\n" \
"s_add_u32 s54, s54, s44\n" \
"s_add_u32 m0, s52, 32768\n" \
"s_nop 0\n" \
"buffer_load_dwordx4 v1, s[40:43], s54 offen nt lds\n" \
"s_add_u32 s54, s54, s44\n" \
"s_add_u32 m0, s52, 34816\n" \
"s_nop 0\n" \
"buffer_load_dwordx4 v1, s[40:43], s54 offen nt lds\n" \
"s_add_u32 s54, s54, s44\n" \
"s_add_u32 m0, s52, 36864\n" \
"s_nop 0\n" \
"buffer_load_dwordx4 v1, s[40:43], s54 offen nt lds\n" \
"s_add_u32 s54, s54, s44\n" \
"s_add_u32 m0, s52, 38912\n" \
"s_nop 0\n" \
"buffer_load_dwordx4 v1, s[40:43], s54 offen nt lds\n" \
"s_add_u32 s54, s54, s44\n" \
"s_add_u32 m0, s52, 49152\n" \
"s_nop 0\n" \
"buffer_load_dwordx4 v1, s[40:43], s54 offen nt lds\n" \
"s_add_u32 s54, s54, s44\n" \
"s_add_u32 m0, s52, 51200\n" \
"s_nop 0\n" \
"buffer_load_dwordx4 v1, s[40:43], s54 offen nt lds\n" \
"s_add_u32 s54, s54, s44\n" \
"s_add_u32 m0, s52, 53248\n" \
"s_nop 0\n" \
"buffer_load_dwordx4 v1, s[40:43], s54 offen nt lds\n" \
"s_add_u32 s54, s54, s44\n" \
"s_add_u32 m0, s52, 55296\n" \
"s_nop 0\n" \
"buffer_load_dwordx4 v1, s[40:43], s54 offen nt lds\n" \
"s_add_u32 s54, s54, s44\n" \
"s_add_u32 m0, s52, 65536\n" \
"s_nop 0\n" \
"buffer_load_dwordx4 v1, s[40:43], s54 offen nt lds\n" \
"s_add_u32 s54, s54, s44\n" \
"s_add_u32 m0, s52, 67584\n" \
"s_nop 0\n" \
"buffer_load_dwordx4 v1, s[40:43], s54 offen nt lds\n" \
"s_add_u32 s54, s54, s44\n" \
"s_add_u32 m0, s52, 69632\n" \
"s_nop 0\n" \
"buffer_load_dwordx4 v1, s[40:43], s54 offen nt lds\n" \
"s_add_u32 s54, s54, s44\n" \
"s_add_u32 m0, s52, 71680\n" \
"s_nop 0\n" \
"buffer_load_dwordx4 v1, s[40:43], s54 offen nt lds\n" \
"s_add_u32 s54, s54, s44\n" \
"s_add_u32 m0, s52, 81920\n" \
"s_nop 0\n" \
"buffer_load_dwordx4 v1, s[40:43], s54 offen nt lds\n" \
"s_add_u32 s54, s54, s44\n" \
"s_add_u32 m0, s52, 83968\n" \
"s_nop 0\n" \
"buffer_load_dwordx4 v1, s[40:43], s54 offen nt lds\n" \
"s_add_u32 s54, s54, s44\n" \
"s_add_u32 m0, s52, 86016\n" \
"s_nop 0\n" \
"buffer_load_dwordx4 v1, s[40:43], s54 offen nt lds\n" \
"s_add_u32 s54, s54, s44\n" \
"s_add_u32 m0, s52, 88064\n" \
"s_nop 0\n" \
"buffer_load_dwordx4 v1, s[40:43], s54 offen nt lds\n" \
"s_add_u32 s54, s54, s44\n" \
"s_add_u32 m0, s52, 98304\n" \
"s_nop 0\n" \
"buffer_load_dwordx4 v1, s[40:43], s54 offen nt lds\n" \
"s_add_u32 s54, s54, s44\n" \
"s_add_u32 m0, s52, 100352\n" \
"s_nop 0\n" \
"buffer_load_dwordx4 v1, s[40:43], s54 offen nt lds\n" \
"s_add_u32 s54, s54, s44\n" \
"s_add_u32 m0, s52, 102400\n" \
"s_nop 0\n" \
"buffer_load_dwordx4 v1, s[40:43], s54 offen nt lds\n" \
"s_add_u32 s54, s54, s44\n" \
"s_add_u32 m0, s52, 104448\n" \
"s_nop 0\n" \
"buffer_load_dwordx4 v1, s[40:43], s54 offen nt lds\n" \
"s_add_u32 s54, s54, s44\n" \
"s_add_u32 m0, s52, 114688\n" \
"s_nop 0\n" \
"buffer_load_dwordx4 v1, s[40:43], s54 offen nt lds\n" \
"s_add_u32 s54, s54, s44\n" \
"s_add_u32 m0, s52, 116736\n" \
"s_nop 0\n" \
"buffer_load_dwordx4 v1, s[40:43], s54 offen nt lds\n" \
"s_add_u32 s54, s54, s44\n" \
"s_add_u32 m0, s52, 118784\n" \
"s_nop 0\n" \
"buffer_load_dwordx4 v1, s[40:43], s54 offen nt lds\n" \
"s_add_u32 s54, s54, s44\n" \
"s_add_u32 m0, s52, 120832\n" \
"s_nop 0\n" \
"buffer_load_dwordx4 v1, s[40:43], s54 offen nt lds\n" \
"s_add_u32 s54, s54, s44\n" \
"v_add_u32 v8, 0x10000, v2\n" \
"v_add_u32 v9, 0x10000, v3\n" \
"s_waitcnt vmcnt(28)\n" \
"s_barrier\n" \
"ds_read_b64_tr_b16 a[0:1], v2 offset:0\n" \
"ds_read_b64_tr_b16 a[2:3], v2 offset:512\n" \
"ds_read_b64_tr_b16 a[128:129], v3 offset:0\n" \
"ds_read_b64_tr_b16 a[130:131], v3 offset:512\n" \
"ds_read_b64_tr_b16 a[4:5], v2 offset:4096\n" \
"ds_read_b64_tr_b16 a[6:7], v2 offset:4608\n" \
"ds_read_b64_tr_b16 a[132:133], v3 offset:4096\n" \
"ds_read_b64_tr_b16 a[134:135], v3 offset:4608\n" \
"s_waitcnt lgkmcnt(0)\n" \
"s_barrier\n" \
"s_add_u32 m0, s52, 0\n" \
"s_nop 0\n" \
"buffer_load_dwordx4 v1, s[40:43], s54 offen nt lds\n" \
"s_add_u32 s54, s54, s44\n" \
"s_add_u32 m0, s52, 2048\n" \
"s_nop 0\n" \
"buffer_load_dwordx4 v1, s[40:43], s54 offen nt lds\n" \
"s_add_u32 s54, s54, s44\n" \
"s_add_u32 m0, s52, 4096\n" \
"s_nop 0\n" \
"buffer_load_dwordx4 v1, s[40:43], s54 offen nt lds\n" \
"s_add_u32 s54, s54, s44\n" \
"s_add_u32 m0, s52, 6144\n" \
"s_nop 0\n" \
"buffer_load_dwordx4 v1, s[40:43], s54 offen nt lds\n" \
"s_add_u32 s54, s54, s44\n" \
"s_waitcnt vmcnt(28)\n" \
"s_barrier\n" \
"ds_read_b64_tr_b16 a[8:9], v2 offset:16384\n" \
"ds_read_b64_tr_b16 a[10:11], v2 offset:16896\n" \
"ds_read_b64_tr_b16 a[136:137], v3 offset:16384\n" \
"ds_read_b64_tr_b16 a[138:139], v3 offset:16896\n" \
"ds_read_b64_tr_b16 a[12:13], v2 offset:20480\n" \
"ds_read_b64_tr_b16 a[14:15], v2 offset:20992\n" \
"ds_read_b64_tr_b16 a[140:141], v3 offset:20480\n" \
"ds_read_b64_tr_b16 a[142:143], v3 offset:20992\n" \
"s_waitcnt lgkmcnt(0)\n" \
"s_barrier\n" \
"s_add_u32 m0, s52, 16384\n" \
"s_nop 0\n" \
"buffer_load_dwordx4 v1, s[40:43], s54 offen nt lds\n" \
"s_add_u32 s54, s54, s44\n" \
"s_add_u32 m0, s52, 18432\n" \
"s_nop 0\n" \
"buffer_load_dwordx4 v1, s[40:43], s54 offen nt lds\n" \
"s_add_u32 s54, s54, s44\n" \
"s_add_u32 m0, s52, 20480\n" \
"s_nop 0\n" \
"buffer_load_dwordx4 v1, s[40:43], s54 offen nt lds\n" \
"s_add_u32 s54, s54, s44\n" \
"s_add_u32 m0, s52, 22528\n" \
"s_nop 0\n" \
"buffer_load_dwordx4 v1, s[40:43], s54 offen nt lds\n" \
"s_add_u32 s54, s54, s44\n" \
"s_waitcnt vmcnt(28)\n" \
"s_barrier\n" \
"ds_read_b64_tr_b16 a[16:17], v2 offset:32768\n" \
"ds_read_b64_tr_b16 a[18:19], v2 offset:33280\n" \
"ds_read_b64_tr_b16 a[144:145], v3 offset:32768\n" \
"ds_read_b64_tr_b16 a[146:147], v3 offset:33280\n" \
"ds_read_b64_tr_b16 a[20:21], v2 offset:36864\n" \
"ds_read_b64_tr_b16 a[22:23], v2 offset:37376\n" \
"ds_read_b64_tr_b16 a[148:149], v3 offset:36864\n" \
"ds_read_b64_tr_b16 a[150:151], v3 offset:37376\n" \
"s_waitcnt lgkmcnt(0)\n" \
"s_barrier\n" \
"s_add_u32 m0, s52, 32768\n" \
"s_nop 0\n" \
"buffer_load_dwordx4 v1, s[40:43], s54 offen nt lds\n" \
"s_add_u32 s54, s54, s44\n" \
"s_add_u32 m0, s52, 34816\n" \
"s_nop 0\n" \
"buffer_load_dwordx4 v1, s[40:43], s54 offen nt lds\n" \
"s_add_u32 s54, s54, s44\n" \
"s_add_u32 m0, s52, 36864\n" \
"s_nop 0\n" \
"buffer_load_dwordx4 v1, s[40:43], s54 offen nt lds\n" \
"s_add_u32 s54, s54, s44\n" \
"s_add_u32 m0, s52, 38912\n" \
"s_nop 0\n" \
"buffer_load_dwordx4 v1, s[40:43], s54 offen nt lds\n" \
"s_add_u32 s54, s54, s44\n" \
"s_waitcnt vmcnt(28)\n" \
"s_barrier\n" \
"ds_read_b64_tr_b16 a[24:25], v2 offset:49152\n" \
"ds_read_b64_tr_b16 a[26:27], v2 offset:49664\n" \
"ds_read_b64_tr_b16 a[152:153], v3 offset:49152\n" \
"ds_read_b64_tr_b16 a[154:155], v3 offset:49664\n" \
"ds_read_b64_tr_b16 a[28:29], v2 offset:53248\n" \
"ds_read_b64_tr_b16 a[30:31], v2 offset:53760\n" \
"ds_read_b64_tr_b16 a[156:157], v3 offset:53248\n" \
"ds_read_b64_tr_b16 a[158:159], v3 offset:53760\n" \
"s_waitcnt lgkmcnt(0)\n" \
"s_barrier\n" \
"s_add_u32 m0, s52, 49152\n" \
"s_nop 0\n" \
"buffer_load_dwordx4 v1, s[40:43], s54 offen nt lds\n" \
"s_add_u32 s54, s54, s44\n" \
"s_add_u32 m0, s52, 51200\n" \
"s_nop 0\n" \
"buffer_load_dwordx4 v1, s[40:43], s54 offen nt lds\n" \
"s_add_u32 s54, s54, s44\n" \
"s_add_u32 m0, s52, 53248\n" \
"s_nop 0\n" \
"buffer_load_dwordx4 v1, s[40:43], s54 offen nt lds\n" \
"s_add_u32 s54, s54, s44\n" \
"s_add_u32 m0, s52, 55296\n" \
"s_nop 0\n" \
"buffer_load_dwordx4 v1, s[40:43], s54 offen nt lds\n" \
"s_add_u32 s54, s54, s44\n" \
"s_waitcnt vmcnt(28)\n" \
"s_barrier\n" \
"ds_read_b64_tr_b16 a[32:33], v8 offset:0\n" \
"ds_read_b64_tr_b16 a[34:35], v8 offset:512\n" \
"ds_read_b64_tr_b16 a[160:161], v9 offset:0\n" \
"ds_read_b64_tr_b16 a[162:163], v9 offset:512\n" \
"ds_read_b64_tr_b16 a[36:37], v8 offset:4096\n" \
"ds_read_b64_tr_b16 a[38:39], v8 offset:4608\n" \
"ds_read_b64_tr_b16 a[164:165], v9 offset:4096\n" \
"ds_read_b64_tr_b16 a[166:167], v9 offset:4608\n" \
"s_waitcnt lgkmcnt(0)\n" \
"s_barrier\n" \
"s_add_u32 m0, s52, 65536\n" \
"s_nop 0\n" \
"buffer_load_dwordx4 v1, s[40:43], s54 offen nt lds\n" \
"s_add_u32 s54, s54, s44\n" \
"s_add_u32 m0, s52, 67584\n" \
"s_nop 0\n" \
"buffer_load_dwordx4 v1, s[40:43], s54 offen nt lds\n" \
"s_add_u32 s54, s54, s44\n" \
"s_add_u32 m0, s52, 69632\n" \
"s_nop 0\n" \
"buffer_load_dwordx4 v1, s[40:43], s54 offen nt lds\n" \
"s_add_u32 s54, s54, s44\n" \
"s_add_u32 m0, s52, 71680\n" \
"s_nop 0\n" \
"buffer_load_dwordx4 v1, s[40:43], s54 offen nt lds\n" \
"s_add_u32 s54, s54, s44\n" \
"s_waitcnt vmcnt(28)\n" \
"s_barrier\n" \
"ds_read_b64_tr_b16 a[40:41], v8 offset:16384\n" \
"ds_read_b64_tr_b16 a[42:43], v8 offset:16896\n" \
"ds_read_b64_tr_b16 a[168:169], v9 offset:16384\n" \
"ds_read_b64_tr_b16 a[170:171], v9 offset:16896\n" \
"ds_read_b64_tr_b16 a[44:45], v8 offset:20480\n" \
"ds_read_b64_tr_b16 a[46:47], v8 offset:20992\n" \
"ds_read_b64_tr_b16 a[172:173], v9 offset:20480\n" \
"ds_read_b64_tr_b16 a[174:175], v9 offset:20992\n" \
"s_waitcnt lgkmcnt(0)\n" \
"s_barrier\n" \
"s_add_u32 m0, s52, 81920\n" \
"s_nop 0\n" \
"buffer_load_dwordx4 v1, s[40:43], s54 offen nt lds\n" \
"s_add_u32 s54, s54, s44\n" \
"s_add_u32 m0, s52, 83968\n" \
"s_nop 0\n" \
"buffer_load_dwordx4 v1, s[40:43], s54 offen nt lds\n" \
"s_add_u32 s54, s54, s44\n" \
"s_add_u32 m0, s52, 86016\n" \
"s_nop 0\n" \
"buffer_load_dwordx4 v1, s[40:43], s54 offen nt lds\n" \
"s_add_u32 s54, s54, s44\n" \
"s_add_u32 m0, s52, 88064\n" \
"s_nop 0\n" \
"buffer_load_dwordx4 v1, s[40:43], s54 offen nt lds\n" \
"s_add_u32 s54, s54, s44\n" \
"s_waitcnt vmcnt(28)\n" \
"s_barrier\n" \
"ds_read_b64_tr_b16 a[48:49], v8 offset:32768\n" \
"ds_read_b64_tr_b16 a[50:51], v8 offset:33280\n" \
"ds_read_b64_tr_b16 a[176:177], v9 offset:32768\n" \
"ds_read_b64_tr_b16 a[178:179], v9 offset:33280\n" \
"ds_read_b64_tr_b16 a[52:53], v8 offset:36864\n" \
"ds_read_b64_tr_b16 a[54:55], v8 offset:37376\n" \
"ds_read_b64_tr_b16 a[180:181], v9 offset:36864\n" \
"ds_read_b64_tr_b16 a[182:183], v9 offset:37376\n" \
"s_waitcnt lgkmcnt(0)\n" \
"s_barrier\n" \
"s_add_u32 m0, s52, 98304\n" \
"s_nop 0\n" \
"buffer_load_dwordx4 v1, s[40:43], s54 offen nt lds\n" \
"s_add_u32 s54, s54, s44\n" \
"s_add_u32 m0, s52, 100352\n" \
"s_nop 0\n" \
"buffer_load_dwordx4 v1, s[40:43], s54 offen nt lds\n" \
"s_add_u32 s54, s54, s44\n" \
"s_add_u32 m0, s52, 102400\n" \
"s_nop 0\n" \
"buffer_load_dwordx4 v1, s[40:43], s54 offen nt lds\n" \
"s_add_u32 s54, s54, s44\n" \
"s_add_u32 m0, s52, 104448\n" \
"s_nop 0\n" \
"buffer_load_dwordx4 v1, s[40:43], s54 offen nt lds\n" \
"s_add_u32 s54, s54, s44\n" \
"s_waitcnt vmcnt(28)\n" \
"s_barrier\n" \
"ds_read_b64_tr_b16 a[56:57], v8 offset:49152\n" \
"ds_read_b64_tr_b16 a[58:59], v8 offset:49664\n" \
"ds_read_b64_tr_b16 a[184:185], v9 offset:49152\n" \
"ds_read_b64_tr_b16 a[186:187], v9 offset:49664\n" \
"ds_read_b64_tr_b16 a[60:61], v8 offset:53248\n" \
"ds_read_b64_tr_b16 a[62:63], v8 offset:53760\n" \
"ds_read_b64_tr_b16 a[188:189], v9 offset:53248\n" \
"ds_read_b64_tr_b16 a[190:191], v9 offset:53760\n" \
"s_waitcnt lgkmcnt(0)\n" \
"s_barrier\n" \
"s_add_u32 m0, s52, 114688\n" \
"s_nop 0\n" \
"buffer_load_dwordx4 v1, s[40:43], s54 offen nt lds\n" \
"s_add_u32 s54, s54, s44\n" \
"s_add_u32 m0, s52, 116736\n" \
"s_nop 0\n" \
"buffer_load_dwordx4 v1, s[40:43], s54 offen nt lds\n" \
"s_add_u32 s54, s54, s44\n" \
"s_add_u32 m0, s52, 118784\n" \
"s_nop 0\n" \
"buffer_load_dwordx4 v1, s[40:43], s54 offen nt lds\n" \
"s_add_u32 s54, s54, s44\n" \
"s_add_u32 m0, s52, 120832\n" \
"s_nop 0\n" \
"buffer_load_dwordx4 v1, s[40:43], s54 offen nt lds\n" \
"s_add_u32 s54, s54, s44\n" \
"s_waitcnt vmcnt(28)\n" \
"s_barrier\n" \
"ds_read_b64_tr_b16 a[64:65], v2 offset:0\n" \
"ds_read_b64_tr_b16 a[66:67], v2 offset:512\n" \
"ds_read_b64_tr_b16 a[192:193], v3 offset:0\n" \
"ds_read_b64_tr_b16 a[194:195], v3 offset:512\n" \
"ds_read_b64_tr_b16 a[68:69], v2 offset:4096\n" \
"ds_read_b64_tr_b16 a[70:71], v2 offset:4608\n" \
"ds_read_b64_tr_b16 a[196:197], v3 offset:4096\n" \
"ds_read_b64_tr_b16 a[198:199], v3 offset:4608\n" \
"s_waitcnt vmcnt(24)\n" \
"s_barrier\n" \
"ds_read_b64_tr_b16 a[72:73], v2 offset:16384\n" \
"ds_read_b64_tr_b16 a[74:75], v2 offset:16896\n" \
"ds_read_b64_tr_b16 a[200:201], v3 offset:16384\n" \
"ds_read_b64_tr_b16 a[202:203], v3 offset:16896\n" \
"ds_read_b64_tr_b16 a[76:77], v2 offset:20480\n" \
"ds_read_b64_tr_b16 a[78:79], v2 offset:20992\n" \
"ds_read_b64_tr_b16 a[204:205], v3 offset:20480\n" \
"ds_read_b64_tr_b16 a[206:207], v3 offset:20992\n" \
"s_waitcnt vmcnt(20)\n" \
"s_barrier\n" \
"ds_read_b64_tr_b16 a[80:81], v2 offset:32768\n" \
"ds_read_b64_tr_b16 a[82:83], v2 offset:33280\n" \
"ds_read_b64_tr_b16 a[208:209], v3 offset:32768\n" \
"ds_read_b64_tr_b16 a[210:211], v3 offset:33280\n" \
"ds_read_b64_tr_b16 a[84:85], v2 offset:36864\n" \
"ds_read_b64_tr_b16 a[86:87], v2 offset:37376\n" \
"ds_read_b64_tr_b16 a[212:213], v3 offset:36864\n" \
"ds_read_b64_tr_b16 a[214:215], v3 offset:37376\n" \
"s_waitcnt vmcnt(16)\n" \
"s_barrier\n" \
"ds_read_b64_tr_b16 a[88:89], v2 offset:49152\n" \
"ds_read_b64_tr_b16 a[90:91], v2 offset:49664\n" \
"ds_read_b64_tr_b16 a[216:217], v3 offset:49152\n" \
"ds_read_b64_tr_b16 a[218:219], v3 offset:49664\n" \
"ds_read_b64_tr_b16 a[92:93], v2 offset:53248\n" \
"ds_read_b64_tr_b16 a[94:95], v2 offset:53760\n" \
"ds_read_b64_tr_b16 a[220:221], v3 offset:53248\n" \
"ds_read_b64_tr_b16 a[222:223], v3 offset:53760\n" \
"s_waitcnt vmcnt(12)\n" \
"s_barrier\n" \
"ds_read_b64_tr_b16 a[96:97], v8 offset:0\n" \
"ds_read_b64_tr_b16 a[98:99], v8 offset:512\n" \
"ds_read_b64_tr_b16 a[224:225], v9 offset:0\n" \
"ds_read_b64_tr_b16 a[226:227], v9 offset:512\n" \
"ds_read_b64_tr_b16 a[100:101], v8 offset:4096\n" \
"ds_read_b64_tr_b16 a[102:103], v8 offset:4608\n" \
"ds_read_b64_tr_b16 a[228:229], v9 offset:4096\n" \
"ds_read_b64_tr_b16 a[230:231], v9 offset:4608\n" \
"s_waitcnt vmcnt(8)\n" \
"s_barrier\n" \
"ds_read_b64_tr_b16 a[104:105], v8 offset:16384\n" \
"ds_read_b64_tr_b16 a[106:107], v8 offset:16896\n" \
"ds_read_b64_tr_b16 a[232:233], v9 offset:16384\n" \
"ds_read_b64_tr_b16 a[234:235], v9 offset:16896\n" \
"ds_read_b64_tr_b16 a[108:109], v8 offset:20480\n" \
"ds_read_b64_tr_b16 a[110:111], v8 offset:20992\n" \
"ds_read_b64_tr_b16 a[236:237], v9 offset:20480\n" \
"ds_read_b64_tr_b16 a[238:239], v9 offset:20992\n" \
"s_waitcnt vmcnt(4)\n" \
"s_barrier\n" \
"ds_read_b64_tr_b16 a[112:113], v8 offset:32768\n" \
"ds_read_b64_tr_b16 a[114:115], v8 offset:33280\n" \
"ds_read_b64_tr_b16 a[240:241], v9 offset:32768\n" \
"ds_read_b64_tr_b16 a[242:243], v9 offset:33280\n" \
"ds_read_b64_tr_b16 a[116:117], v8 offset:36864\n" \
"ds_read_b64_tr_b16 a[118:119], v8 offset:37376\n" \
"ds_read_b64_tr_b16 a[244:245], v9 offset:36864\n" \
"ds_read_b64_tr_b16 a[246:247], v9 offset:37376\n" \
"s_waitcnt vmcnt(0)\n" \
"s_barrier\n" \
"ds_read_b64_tr_b16 a[120:121], v8 offset:49152\n" \
"ds_read_b64_tr_b16 a[122:123], v8 offset:49664\n" \
"ds_read_b64_tr_b16 a[248:249], v9 offset:49152\n" \
"ds_read_b64_tr_b16 a[250:251], v9 offset:49664\n" \
"ds_read_b64_tr_b16 a[124:125], v8 offset:53248\n" \
"ds_read_b64_tr_b16 a[126:127], v8 offset:53760\n" \
"ds_read_b64_tr_b16 a[252:253], v9 offset:53248\n" \
"ds_read_b64_tr_b16 a[254:255], v9 offset:53760\n" \
"s_waitcnt lgkmcnt(0)\n" \
"s_barrier\n" \
"v_mov_b32 v14, 4096\n" \
"s_mov_b32 s59, s46\n" \
"s_add_u32 s63, s59, 0\n" \
"s_min_u32 s63, s63, 63\n" \
"s_lshr_b32 s63, s63, 3\n" \
"s_lshl_b32 s63, s63, 7\n" \
"s_branch .Lp1g1_l0_%=\n" \
".Lp1g1_l0k_%=:\n" \
"s_add_u32 s63, s59, 1\n" \
"s_min_u32 s63, s63, 63\n" \
"s_lshr_b32 s63, s63, 3\n" \
"s_lshl_b32 s63, s63, 7\n" \
"s_branch .Lp1g1_l1_%=\n" \
".Lp1g1_l1k_%=:\n" \
"s_lshl_b32 s55, s48, 13\n" \
"s_lshl_b32 s56, s46, 17\n" \
"s_add_u32 s56, s56, s45\n" \
"s_lshl_b32 s60, s47, 17\n" \
"s_sub_u32 s60, s60, 0x20000\n" \
"s_add_u32 s60, s60, s45\n" \
"s_mov_b32 s57, 0\n" \
"s_mov_b32 s58, 0\n" \
"v_mov_b32 v7, v5\n" \
"v_mov_b32 v5, 0x7ffffff0\n" \
"v_mov_b32 v10, v0\n" \
"v_mov_b32 v11, v6\n" \
"v_mov_b32 v6, 0x7ffffff0\n" \
"s_mov_b32 s59, s46\n" \
"s_min_u32 s61, s56, s60\n" \
"s_lshr_b32 s65, s61, 8\n" \
"s_and_b32 s65, s65, 0x2000\n" \
"s_xor_b32 s61, s61, s65\n" \
"s_add_u32 m0, s55, 0\n" \
"s_nop 0\n" \
"buffer_load_dwordx4 v4, s[36:39], s61 offen offset:0 sc1 lds\n" \
"buffer_load_dwordx4 v4, s[36:39], s61 offen offset:1024 sc1 lds\n" \
"buffer_load_dwordx4 v4, s[36:39], s61 offen offset:2048 sc1 lds\n" \
"buffer_load_dwordx4 v4, s[36:39], s61 offen offset:3072 sc1 lds\n" \
"s_add_u32 m0, s55, 4096\n" \
"s_add_u32 s61, s61, 4096\n" \
"buffer_load_dwordx4 v4, s[36:39], s61 offen offset:0 sc1 lds\n" \
"buffer_load_dwordx4 v4, s[36:39], s61 offen offset:1024 sc1 lds\n" \
"buffer_load_dwordx4 v4, s[36:39], s61 offen offset:2048 sc1 lds\n" \
"buffer_load_dwordx4 v4, s[36:39], s61 offen offset:3072 sc1 lds\n" \
"s_add_u32 s61, s61, 28672\n" \
"s_add_u32 s56, s56, 0x20000\n" \
"s_add_u32 m0, s55, 32768\n" \
"s_nop 0\n" \
"buffer_load_dwordx4 v4, s[36:39], s61 offen offset:0 sc1 lds\n" \
"buffer_load_dwordx4 v4, s[36:39], s61 offen offset:1024 sc1 lds\n" \
"buffer_load_dwordx4 v4, s[36:39], s61 offen offset:2048 sc1 lds\n" \
"buffer_load_dwordx4 v4, s[36:39], s61 offen offset:3072 sc1 lds\n" \
"s_add_u32 m0, s55, 36864\n" \
"s_add_u32 s61, s61, 4096\n" \
"buffer_load_dwordx4 v4, s[36:39], s61 offen offset:0 sc1 lds\n" \
"buffer_load_dwordx4 v4, s[36:39], s61 offen offset:1024 sc1 lds\n" \
"buffer_load_dwordx4 v4, s[36:39], s61 offen offset:2048 sc1 lds\n" \
"buffer_load_dwordx4 v4, s[36:39], s61 offen offset:3072 sc1 lds\n" \
"s_mov_b32 s70, 0\n" \
"s_mov_b32 s72, 0\n" \
"s_waitcnt vmcnt(0) lgkmcnt(0)\n" \
"s_barrier\n" \
"v_add_u32 v12, 0x10000, v10\n" \
"ds_read_b128 v[16:19], v10 offset:0\n" \
"ds_read_b128 v[20:23], v10 offset:16384\n" \
"ds_read_b128 v[24:27], v10 offset:1024\n" \
"ds_read_b128 v[28:31], v10 offset:17408\n" \
"ds_read_b128 v[32:35], v10 offset:2048\n" \
"ds_read_b128 v[36:39], v10 offset:18432\n" \
"s_waitcnt lgkmcnt(4)\n" \
"s_nop 7\n" \
"s_nop 7\n" \
".p2align 6\n" \
".Lp1g1_loop_%=:\n" \
"v_mfma_f32_16x16x32_bf16 v[48:51], a[0:3], v[16:19], 0\n" \
"s_min_u32 s61, s56, s60\n" \
"s_lshr_b32 s65, s61, 8\n" \
"s_and_b32 s65, s65, 0x2000\n" \
"s_xor_b32 s61, s61, s65\n" \
"s_add_u32 m0, s55, 65536\n" \
"s_nop 0\n" \
"buffer_load_dwordx4 v4, s[36:39], s61 offen offset:0 sc1 lds\n" \
"v_add_u32_e64 v13, -1, s59\n" \
"v_mfma_f32_16x16x32_bf16 v[52:55], a[0:3], v[20:23], 0\n" \
"ds_read_b128 v[40:43], v10 offset:3072\n" \
"s_nop 0\n" \
"v_mfma_f32_16x16x32_bf16 v[56:59], a[128:131], v[16:19], 0\n" \
"ds_read_b128 v[44:47], v10 offset:19456\n" \
"s_nop 0\n" \
"v_mfma_f32_16x16x32_bf16 v[60:63], a[128:131], v[20:23], 0\n" \
"s_waitcnt lgkmcnt(4)\n" \
"v_mul_f32 v64, 0xbfb8aa3b, v64\n" \
"v_mul_f32 v65, 0xbfb8aa3b, v65\n" \
"v_mfma_f32_16x16x32_bf16 v[48:51], a[4:7], v[24:27], v[48:51]\n" \
"buffer_load_dwordx4 v4, s[36:39], s61 offen offset:1024 sc1 lds\n" \
"v_mfma_f32_16x16x32_bf16 v[52:55], a[4:7], v[28:31], v[52:55]\n" \
"ds_read_b128 v[16:19], v10 offset:4096\n" \
"v_mul_f32 v66, 0xbfb8aa3b, v66\n" \
"v_mfma_f32_16x16x32_bf16 v[56:59], a[132:135], v[24:27], v[56:59]\n" \
"ds_read_b128 v[20:23], v10 offset:20480\n" \
"v_mul_f32 v67, 0xbfb8aa3b, v67\n" \
"v_mfma_f32_16x16x32_bf16 v[60:63], a[132:135], v[28:31], v[60:63]\n" \
"s_waitcnt lgkmcnt(4)\n" \
"v_exp_f32_e64 v64, v64\n" \
"v_mfma_f32_16x16x32_bf16 v[48:51], a[8:11], v[32:35], v[48:51]\n" \
"s_add_u32 s63, s59, 2\n" \
"s_min_u32 s63, s63, 63\n" \
"v_mfma_f32_16x16x32_bf16 v[52:55], a[8:11], v[36:39], v[52:55]\n" \
"ds_read_b128 v[24:27], v10 offset:5120\n" \
"s_lshr_b32 s63, s63, 3\n" \
"s_lshl_b32 s63, s63, 7\n" \
"v_mfma_f32_16x16x32_bf16 v[56:59], a[136:139], v[32:35], v[56:59]\n" \
"ds_read_b128 v[28:31], v10 offset:21504\n" \
"v_mfma_f32_16x16x32_bf16 v[60:63], a[136:139], v[36:39], v[60:63]\n" \
"s_waitcnt lgkmcnt(4)\n" \
"v_exp_f32 v65, v65\n" \
"v_mfma_f32_16x16x32_bf16 v[48:51], a[12:15], v[40:43], v[48:51]\n" \
"buffer_load_dwordx4 v4, s[36:39], s61 offen offset:2048 sc1 lds\n" \
"v_mfma_f32_16x16x32_bf16 v[52:55], a[12:15], v[44:47], v[52:55]\n" \
"ds_read_b128 v[32:35], v10 offset:6144\n" \
"v_mfma_f32_16x16x32_bf16 v[56:59], a[140:143], v[40:43], v[56:59]\n" \
"ds_read_b128 v[36:39], v10 offset:22528\n" \
"v_mfma_f32_16x16x32_bf16 v[60:63], a[140:143], v[44:47], v[60:63]\n" \
"s_waitcnt lgkmcnt(4)\n" \
"v_exp_f32 v66, v66\n" \
"v_mfma_f32_16x16x32_bf16 v[48:51], a[16:19], v[16:19], v[48:51]\n" \
"buffer_load_dwordx4 v4, s[36:39], s61 offen offset:3072 sc1 lds\n" \
"v_mfma_f32_16x16x32_bf16 v[52:55], a[16:19], v[20:23], v[52:55]\n" \
"ds_read_b128 v[40:43], v10 offset:7168\n" \
"v_mfma_f32_16x16x32_bf16 v[56:59], a[144:147], v[16:19], v[56:59]\n" \
"ds_read_b128 v[44:47], v10 offset:23552\n" \
"v_mfma_f32_16x16x32_bf16 v[60:63], a[144:147], v[20:23], v[60:63]\n" \
"s_waitcnt lgkmcnt(4)\n" \
"v_exp_f32 v67, v67\n" \
"v_mfma_f32_16x16x32_bf16 v[48:51], a[20:23], v[24:27], v[48:51]\n" \
"buffer_load_dword v15, v14, s[36:39], s63 offen sc1\n" \
"v_mfma_f32_16x16x32_bf16 v[52:55], a[20:23], v[28:31], v[52:55]\n" \
"ds_read_b128 v[16:19], v10 offset:8192\n" \
"v_add_f32_e64 v64, 1.0, v64\n" \
"v_mfma_f32_16x16x32_bf16 v[56:59], a[148:151], v[24:27], v[56:59]\n" \
"ds_read_b128 v[20:23], v10 offset:24576\n" \
"v_add_f32_e64 v65, 1.0, v65\n" \
"v_mfma_f32_16x16x32_bf16 v[60:63], a[148:151], v[28:31], v[60:63]\n" \
"s_waitcnt lgkmcnt(4)\n" \
"v_add_f32 v66, 1.0, v66\n" \
"v_add_f32_e64 v67, 1.0, v67\n" \
"v_mfma_f32_16x16x32_bf16 v[48:51], a[24:27], v[32:35], v[48:51]\n" \
"s_add_u32 m0, s55, 69632\n" \
"s_add_u32 s61, s61, 4096\n" \
"buffer_load_dwordx4 v4, s[36:39], s61 offen offset:0 sc1 lds\n" \
"v_mfma_f32_16x16x32_bf16 v[52:55], a[24:27], v[36:39], v[52:55]\n" \
"ds_read_b128 v[24:27], v10 offset:9216\n" \
"v_mfma_f32_16x16x32_bf16 v[56:59], a[152:155], v[32:35], v[56:59]\n" \
"ds_read_b128 v[28:31], v10 offset:25600\n" \
"v_mfma_f32_16x16x32_bf16 v[60:63], a[152:155], v[36:39], v[60:63]\n" \
"s_waitcnt lgkmcnt(4)\n" \
"v_rcp_f32 v64, v64\n" \
"v_mfma_f32_16x16x32_bf16 v[48:51], a[28:31], v[40:43], v[48:51]\n" \
"buffer_load_dwordx4 v4, s[36:39], s61 offen offset:1024 sc1 lds\n" \
"v_mfma_f32_16x16x32_bf16 v[52:55], a[28:31], v[44:47], v[52:55]\n" \
"ds_read_b128 v[32:35], v10 offset:10240\n" \
"v_mfma_f32_16x16x32_bf16 v[56:59], a[156:159], v[40:43], v[56:59]\n" \
"ds_read_b128 v[36:39], v10 offset:26624\n" \
"v_mfma_f32_16x16x32_bf16 v[60:63], a[156:159], v[44:47], v[60:63]\n" \
"s_waitcnt lgkmcnt(4)\n" \
"v_rcp_f32 v65, v65\n" \
"v_mfma_f32_16x16x32_bf16 v[48:51], a[32:35], v[16:19], v[48:51]\n" \
"v_rcp_f32_e64 v66, v66\n" \
"v_mfma_f32_16x16x32_bf16 v[52:55], a[32:35], v[20:23], v[52:55]\n" \
"ds_read_b128 v[40:43], v10 offset:11264\n" \
"v_mfma_f32_16x16x32_bf16 v[56:59], a[160:163], v[16:19], v[56:59]\n" \
"ds_read_b128 v[44:47], v10 offset:27648\n" \
"v_mfma_f32_16x16x32_bf16 v[60:63], a[160:163], v[20:23], v[60:63]\n" \
"s_waitcnt lgkmcnt(4)\n" \
"v_rcp_f32 v67, v67\n" \
"v_mfma_f32_16x16x32_bf16 v[48:51], a[36:39], v[24:27], v[48:51]\n" \
"buffer_load_dwordx4 v4, s[36:39], s61 offen offset:2048 sc1 lds\n" \
"v_mfma_f32_16x16x32_bf16 v[52:55], a[36:39], v[28:31], v[52:55]\n" \
"ds_read_b128 v[16:19], v10 offset:12288\n" \
"v_mul_f32_e64 v64, v64, v72\n" \
"v_mfma_f32_16x16x32_bf16 v[56:59], a[164:167], v[24:27], v[56:59]\n" \
"ds_read_b128 v[20:23], v10 offset:28672\n" \
"v_mul_f32_e64 v65, v65, v73\n" \
"v_mfma_f32_16x16x32_bf16 v[60:63], a[164:167], v[28:31], v[60:63]\n" \
"s_waitcnt lgkmcnt(4)\n" \
"v_mul_f32 v66, v66, v74\n" \
"v_mul_f32_e64 v67, v67, v75\n" \
"v_mfma_f32_16x16x32_bf16 v[48:51], a[40:43], v[32:35], v[48:51]\n" \
"buffer_load_dwordx4 v4, s[36:39], s61 offen offset:3072 sc1 lds\n" \
"v_mfma_f32_16x16x32_bf16 v[52:55], a[40:43], v[36:39], v[52:55]\n" \
"ds_read_b128 v[24:27], v10 offset:13312\n" \
"v_cvt_pk_bf16_f32 v80, v64, v65\n" \
"v_mfma_f32_16x16x32_bf16 v[56:59], a[168:171], v[32:35], v[56:59]\n" \
"ds_read_b128 v[28:31], v10 offset:29696\n" \
"v_cvt_pk_bf16_f32 v81, v66, v67\n" \
"v_mfma_f32_16x16x32_bf16 v[60:63], a[168:171], v[36:39], v[60:63]\n" \
"s_waitcnt lgkmcnt(4)\n" \
"v_mul_f32 v68, 0xbfb8aa3b, v68\n" \
"v_mul_f32 v69, 0xbfb8aa3b, v69\n" \
"v_mfma_f32_16x16x32_bf16 v[48:51], a[44:47], v[40:43], v[48:51]\n" \
"v_mul_f32 v70, 0xbfb8aa3b, v70\n" \
"v_mul_f32 v71, 0xbfb8aa3b, v71\n" \
"v_mfma_f32_16x16x32_bf16 v[52:55], a[44:47], v[44:47], v[52:55]\n" \
"ds_read_b128 v[32:35], v10 offset:14336\n" \
"v_mfma_f32_16x16x32_bf16 v[56:59], a[172:175], v[40:43], v[56:59]\n" \
"ds_read_b128 v[36:39], v10 offset:30720\n" \
"v_mfma_f32_16x16x32_bf16 v[60:63], a[172:175], v[44:47], v[60:63]\n" \
"s_waitcnt lgkmcnt(4)\n" \
"v_exp_f32_e64 v68, v68\n" \
"v_mfma_f32_16x16x32_bf16 v[48:51], a[48:51], v[16:19], v[48:51]\n" \
"v_exp_f32_e64 v69, v69\n" \
"v_mfma_f32_16x16x32_bf16 v[52:55], a[48:51], v[20:23], v[52:55]\n" \
"ds_read_b128 v[40:43], v10 offset:15360\n" \
"v_mfma_f32_16x16x32_bf16 v[56:59], a[176:179], v[16:19], v[56:59]\n" \
"ds_read_b128 v[44:47], v10 offset:31744\n" \
"v_mfma_f32_16x16x32_bf16 v[60:63], a[176:179], v[20:23], v[60:63]\n" \
"s_waitcnt lgkmcnt(4)\n" \
"v_exp_f32 v70, v70\n" \
"v_mfma_f32_16x16x32_bf16 v[48:51], a[52:55], v[24:27], v[48:51]\n" \
"s_waitcnt vmcnt(9)\n" \
"s_barrier\n" \
"v_mfma_f32_16x16x32_bf16 v[52:55], a[52:55], v[28:31], v[52:55]\n" \
"ds_read_b128 v[16:19], v10 offset:32768\n" \
"v_mfma_f32_16x16x32_bf16 v[56:59], a[180:183], v[24:27], v[56:59]\n" \
"ds_read_b128 v[20:23], v10 offset:49152\n" \
"v_mfma_f32_16x16x32_bf16 v[60:63], a[180:183], v[28:31], v[60:63]\n" \
"s_waitcnt lgkmcnt(4)\n" \
"buffer_store_dword v13, v6, s[36:39], 0 offen sc1\n" \
"v_exp_f32 v71, v71\n" \
"v_mfma_f32_16x16x32_bf16 v[48:51], a[56:59], v[32:35], v[48:51]\n" \
"v_add_f32 v68, 1.0, v68\n" \
"v_add_f32 v69, 1.0, v69\n" \
"v_mfma_f32_16x16x32_bf16 v[52:55], a[56:59], v[36:39], v[52:55]\n" \
"ds_read_b128 v[24:27], v10 offset:33792\n" \
"v_add_f32_e64 v70, 1.0, v70\n" \
"v_mfma_f32_16x16x32_bf16 v[56:59], a[184:187], v[32:35], v[56:59]\n" \
"ds_read_b128 v[28:31], v10 offset:50176\n" \
"v_add_f32_e64 v71, 1.0, v71\n" \
"v_mfma_f32_16x16x32_bf16 v[60:63], a[184:187], v[36:39], v[60:63]\n" \
"s_waitcnt lgkmcnt(4)\n" \
"v_rcp_f32 v68, v68\n" \
"v_mfma_f32_16x16x32_bf16 v[48:51], a[60:63], v[40:43], v[48:51]\n" \
"v_rcp_f32_e64 v69, v69\n" \
"v_mfma_f32_16x16x32_bf16 v[52:55], a[60:63], v[44:47], v[52:55]\n" \
"ds_read_b128 v[32:35], v10 offset:34816\n" \
"v_mfma_f32_16x16x32_bf16 v[56:59], a[188:191], v[40:43], v[56:59]\n" \
"ds_read_b128 v[36:39], v10 offset:51200\n" \
"v_mfma_f32_16x16x32_bf16 v[60:63], a[188:191], v[44:47], v[60:63]\n" \
"s_waitcnt lgkmcnt(4)\n" \
"v_rcp_f32 v70, v70\n" \
"v_mfma_f32_16x16x32_bf16 v[48:51], a[64:67], v[16:19], v[48:51]\n" \
"s_add_u32 s61, s61, 28672\n" \
"s_add_u32 s56, s56, 0x20000\n" \
"s_add_u32 m0, s55, 98304\n" \
"s_nop 0\n" \
"buffer_load_dwordx4 v4, s[36:39], s61 offen offset:0 sc1 lds\n" \
"v_mfma_f32_16x16x32_bf16 v[52:55], a[64:67], v[20:23], v[52:55]\n" \
"ds_read_b128 v[40:43], v10 offset:35840\n" \
"v_mfma_f32_16x16x32_bf16 v[56:59], a[192:195], v[16:19], v[56:59]\n" \
"ds_read_b128 v[44:47], v10 offset:52224\n" \
"v_mfma_f32_16x16x32_bf16 v[60:63], a[192:195], v[20:23], v[60:63]\n" \
"s_waitcnt lgkmcnt(4)\n" \
"v_rcp_f32_e64 v71, v71\n" \
"v_mfma_f32_16x16x32_bf16 v[48:51], a[68:71], v[24:27], v[48:51]\n" \
"buffer_load_dwordx4 v4, s[36:39], s61 offen offset:1024 sc1 lds\n" \
"v_mfma_f32_16x16x32_bf16 v[52:55], a[68:71], v[28:31], v[52:55]\n" \
"ds_read_b128 v[16:19], v10 offset:36864\n" \
"v_mul_f32_e64 v68, v68, v76\n" \
"v_mfma_f32_16x16x32_bf16 v[56:59], a[196:199], v[24:27], v[56:59]\n" \
"ds_read_b128 v[20:23], v10 offset:53248\n" \
"v_mul_f32_e64 v69, v69, v77\n" \
"v_mfma_f32_16x16x32_bf16 v[60:63], a[196:199], v[28:31], v[60:63]\n" \
"s_waitcnt lgkmcnt(4)\n" \
"v_mul_f32 v70, v70, v78\n" \
"v_mul_f32_e64 v71, v71, v79\n" \
"v_mfma_f32_16x16x32_bf16 v[48:51], a[72:75], v[32:35], v[48:51]\n" \
"v_cvt_pk_bf16_f32 v82, v68, v69\n" \
"v_cvt_pk_bf16_f32 v83, v70, v71\n" \
"v_mfma_f32_16x16x32_bf16 v[52:55], a[72:75], v[36:39], v[52:55]\n" \
"ds_read_b128 v[24:27], v10 offset:37888\n" \
"s_nop 0\n" \
"v_mfma_f32_16x16x32_bf16 v[56:59], a[200:203], v[32:35], v[56:59]\n" \
"ds_read_b128 v[28:31], v10 offset:54272\n" \
"s_nop 0\n" \
"v_mfma_f32_16x16x32_bf16 v[60:63], a[200:203], v[36:39], v[60:63]\n" \
"s_waitcnt lgkmcnt(4)\n" \
"v_permlane16_swap_b32 v80, v82\n" \
"v_permlane16_swap_b32 v81, v83\n" \
"v_mfma_f32_16x16x32_bf16 v[48:51], a[76:79], v[40:43], v[48:51]\n" \
"buffer_load_dwordx4 v4, s[36:39], s61 offen offset:2048 sc1 lds\n" \
"v_mfma_f32_16x16x32_bf16 v[52:55], a[76:79], v[44:47], v[52:55]\n" \
"ds_read_b128 v[32:35], v10 offset:38912\n" \
"buffer_store_dwordx4 v[80:83], v5, s[36:39], s57 offen sc1\n" \
"v_mfma_f32_16x16x32_bf16 v[56:59], a[204:207], v[40:43], v[56:59]\n" \
"ds_read_b128 v[36:39], v10 offset:55296\n" \
"v_mfma_f32_16x16x32_bf16 v[60:63], a[204:207], v[44:47], v[60:63]\n" \
"s_waitcnt lgkmcnt(4)\n" \
"v_mfma_f32_16x16x32_bf16 v[48:51], a[80:83], v[16:19], v[48:51]\n" \
"buffer_load_dwordx4 v4, s[36:39], s61 offen offset:3072 sc1 lds\n" \
"v_mfma_f32_16x16x32_bf16 v[52:55], a[80:83], v[20:23], v[52:55]\n" \
"ds_read_b128 v[40:43], v10 offset:39936\n" \
"v_mfma_f32_16x16x32_bf16 v[56:59], a[208:211], v[16:19], v[56:59]\n" \
"ds_read_b128 v[44:47], v10 offset:56320\n" \
"v_mfma_f32_16x16x32_bf16 v[60:63], a[208:211], v[20:23], v[60:63]\n" \
"s_waitcnt lgkmcnt(4)\n" \
"v_mfma_f32_16x16x32_bf16 v[48:51], a[84:87], v[24:27], v[48:51]\n" \
"v_mfma_f32_16x16x32_bf16 v[52:55], a[84:87], v[28:31], v[52:55]\n" \
"ds_read_b128 v[16:19], v10 offset:40960\n" \
"v_mfma_f32_16x16x32_bf16 v[56:59], a[212:215], v[24:27], v[56:59]\n" \
"ds_read_b128 v[20:23], v10 offset:57344\n" \
"v_mfma_f32_16x16x32_bf16 v[60:63], a[212:215], v[28:31], v[60:63]\n" \
"s_waitcnt lgkmcnt(4)\n" \
"v_mfma_f32_16x16x32_bf16 v[48:51], a[88:91], v[32:35], v[48:51]\n" \
"s_add_u32 m0, s55, 102400\n" \
"s_add_u32 s61, s61, 4096\n" \
"buffer_load_dwordx4 v4, s[36:39], s61 offen offset:0 sc1 lds\n" \
"v_mfma_f32_16x16x32_bf16 v[52:55], a[88:91], v[36:39], v[52:55]\n" \
"ds_read_b128 v[24:27], v10 offset:41984\n" \
"v_mfma_f32_16x16x32_bf16 v[56:59], a[216:219], v[32:35], v[56:59]\n" \
"ds_read_b128 v[28:31], v10 offset:58368\n" \
"v_mfma_f32_16x16x32_bf16 v[60:63], a[216:219], v[36:39], v[60:63]\n" \
"s_waitcnt lgkmcnt(4)\n" \
"v_mfma_f32_16x16x32_bf16 v[48:51], a[92:95], v[40:43], v[48:51]\n" \
"buffer_load_dwordx4 v4, s[36:39], s61 offen offset:1024 sc1 lds\n" \
"v_mfma_f32_16x16x32_bf16 v[52:55], a[92:95], v[44:47], v[52:55]\n" \
"ds_read_b128 v[32:35], v10 offset:43008\n" \
"v_mfma_f32_16x16x32_bf16 v[56:59], a[220:223], v[40:43], v[56:59]\n" \
"ds_read_b128 v[36:39], v10 offset:59392\n" \
"v_mfma_f32_16x16x32_bf16 v[60:63], a[220:223], v[44:47], v[60:63]\n" \
"s_waitcnt lgkmcnt(4)\n" \
"v_mfma_f32_16x16x32_bf16 v[48:51], a[96:99], v[16:19], v[48:51]\n" \
"v_mfma_f32_16x16x32_bf16 v[52:55], a[96:99], v[20:23], v[52:55]\n" \
"ds_read_b128 v[40:43], v10 offset:44032\n" \
"v_mfma_f32_16x16x32_bf16 v[56:59], a[224:227], v[16:19], v[56:59]\n" \
"ds_read_b128 v[44:47], v10 offset:60416\n" \
"v_mfma_f32_16x16x32_bf16 v[60:63], a[224:227], v[20:23], v[60:63]\n" \
"s_waitcnt lgkmcnt(4)\n" \
"v_mfma_f32_16x16x32_bf16 v[48:51], a[100:103], v[24:27], v[48:51]\n" \
"buffer_load_dwordx4 v4, s[36:39], s61 offen offset:2048 sc1 lds\n" \
"v_mfma_f32_16x16x32_bf16 v[52:55], a[100:103], v[28:31], v[52:55]\n" \
"ds_read_b128 v[16:19], v10 offset:45056\n" \
"v_mfma_f32_16x16x32_bf16 v[56:59], a[228:231], v[24:27], v[56:59]\n" \
"ds_read_b128 v[20:23], v10 offset:61440\n" \
"v_mfma_f32_16x16x32_bf16 v[60:63], a[228:231], v[28:31], v[60:63]\n" \
"s_waitcnt lgkmcnt(4)\n" \
"v_mfma_f32_16x16x32_bf16 v[48:51], a[104:107], v[32:35], v[48:51]\n" \
"buffer_load_dwordx4 v4, s[36:39], s61 offen offset:3072 sc1 lds\n" \
"v_mfma_f32_16x16x32_bf16 v[52:55], a[104:107], v[36:39], v[52:55]\n" \
"ds_read_b128 v[24:27], v10 offset:46080\n" \
"v_mfma_f32_16x16x32_bf16 v[56:59], a[232:235], v[32:35], v[56:59]\n" \
"ds_read_b128 v[28:31], v10 offset:62464\n" \
"v_mfma_f32_16x16x32_bf16 v[60:63], a[232:235], v[36:39], v[60:63]\n" \
"s_waitcnt lgkmcnt(4)\n" \
"v_mfma_f32_16x16x32_bf16 v[48:51], a[108:111], v[40:43], v[48:51]\n" \
"s_lshl_b32 s66, s59, s49\n" \
"s_lshr_b32 s65, s66, 8\n" \
"v_mfma_f32_16x16x32_bf16 v[52:55], a[108:111], v[44:47], v[52:55]\n" \
"ds_read_b128 v[32:35], v10 offset:47104\n" \
"v_mfma_f32_16x16x32_bf16 v[56:59], a[236:239], v[40:43], v[56:59]\n" \
"ds_read_b128 v[36:39], v10 offset:63488\n" \
"v_mfma_f32_16x16x32_bf16 v[60:63], a[236:239], v[44:47], v[60:63]\n" \
"s_waitcnt lgkmcnt(4)\n" \
"s_and_b32 s65, s65, 0x2000\n" \
"s_xor_b32 s66, s66, s65\n" \
"v_mfma_f32_16x16x32_bf16 v[48:51], a[112:115], v[16:19], v[48:51]\n" \
"v_mfma_f32_16x16x32_bf16 v[52:55], a[112:115], v[20:23], v[52:55]\n" \
"ds_read_b128 v[40:43], v10 offset:48128\n" \
"v_mfma_f32_16x16x32_bf16 v[56:59], a[240:243], v[16:19], v[56:59]\n" \
"ds_read_b128 v[44:47], v10 offset:64512\n" \
"v_mfma_f32_16x16x32_bf16 v[60:63], a[240:243], v[20:23], v[60:63]\n" \
"s_waitcnt lgkmcnt(4)\n" \
"v_mfma_f32_16x16x32_bf16 v[48:51], a[116:119], v[24:27], v[48:51]\n" \
"s_waitcnt vmcnt(10)\n" \
"s_barrier\n" \
"v_mfma_f32_16x16x32_bf16 v[52:55], a[116:119], v[28:31], v[52:55]\n" \
"ds_read_b128 v[16:19], v12 offset:0\n" \
"v_mfma_f32_16x16x32_bf16 v[56:59], a[244:247], v[24:27], v[56:59]\n" \
"ds_read_b128 v[20:23], v12 offset:16384\n" \
"v_mfma_f32_16x16x32_bf16 v[60:63], a[244:247], v[28:31], v[60:63]\n" \
"s_waitcnt lgkmcnt(4)\n" \
"v_mfma_f32_16x16x32_bf16 v[48:51], a[120:123], v[32:35], v[48:51]\n" \
"v_cmp_gt_u32 vcc, 16, v15\n" \
"v_mfma_f32_16x16x32_bf16 v[52:55], a[120:123], v[36:39], v[52:55]\n" \
"ds_read_b128 v[24:27], v12 offset:1024\n" \
"v_mfma_f32_16x16x32_bf16 v[56:59], a[248:251], v[32:35], v[56:59]\n" \
"ds_read_b128 v[28:31], v12 offset:17408\n" \
"v_mfma_f32_16x16x32_bf16 v[60:63], a[248:251], v[36:39], v[60:63]\n" \
"s_waitcnt lgkmcnt(4)\n" \
"v_mfma_f32_16x16x32_bf16 v[48:51], a[124:127], v[40:43], v[48:51]\n" \
"s_cbranch_vccnz .Lp1g1_m0_%=\n" \
".Lp1g1_m0k_%=:\n" \
"v_mfma_f32_16x16x32_bf16 v[52:55], a[124:127], v[44:47], v[52:55]\n" \
"ds_read_b128 v[32:35], v12 offset:2048\n" \
"v_mfma_f32_16x16x32_bf16 v[56:59], a[252:255], v[40:43], v[56:59]\n" \
"ds_read_b128 v[36:39], v12 offset:18432\n" \
"v_mfma_f32_16x16x32_bf16 v[60:63], a[252:255], v[44:47], v[60:63]\n" \
"s_waitcnt lgkmcnt(4)\n" \
"s_xor_b32 s57, s66, s64\n" \
"s_add_u32 s58, s57, s50\n" \
"s_add_u32 s59, s59, 1\n" \
"v_mov_b32 v5, v7\n" \
"v_mov_b32 v6, v11\n" \
"s_cmp_ge_u32 s59, s47\n" \
"s_cbranch_scc1 .Lp1g1_out0_%=\n" \
"v_mfma_f32_16x16x32_bf16 v[64:67], a[0:3], v[16:19], 0\n" \
"s_min_u32 s61, s56, s60\n" \
"s_lshr_b32 s65, s61, 8\n" \
"s_and_b32 s65, s65, 0x2000\n" \
"s_xor_b32 s61, s61, s65\n" \
"s_add_u32 m0, s55, 0\n" \
"s_nop 0\n" \
"buffer_load_dwordx4 v4, s[36:39], s61 offen offset:0 sc1 lds\n" \
"v_add_u32_e64 v13, -1, s59\n" \
"v_mfma_f32_16x16x32_bf16 v[68:71], a[0:3], v[20:23], 0\n" \
"ds_read_b128 v[40:43], v12 offset:3072\n" \
"s_nop 0\n" \
"v_mfma_f32_16x16x32_bf16 v[72:75], a[128:131], v[16:19], 0\n" \
"ds_read_b128 v[44:47], v12 offset:19456\n" \
"s_nop 0\n" \
"v_mfma_f32_16x16x32_bf16 v[76:79], a[128:131], v[20:23], 0\n" \
"s_waitcnt lgkmcnt(4)\n" \
"v_mul_f32 v48, 0xbfb8aa3b, v48\n" \
"v_mul_f32 v49, 0xbfb8aa3b, v49\n" \
"v_mfma_f32_16x16x32_bf16 v[64:67], a[4:7], v[24:27], v[64:67]\n" \
"buffer_load_dwordx4 v4, s[36:39], s61 offen offset:1024 sc1 lds\n" \
"v_mfma_f32_16x16x32_bf16 v[68:71], a[4:7], v[28:31], v[68:71]\n" \
"ds_read_b128 v[16:19], v12 offset:4096\n" \
"v_mul_f32 v50, 0xbfb8aa3b, v50\n" \
"v_mfma_f32_16x16x32_bf16 v[72:75], a[132:135], v[24:27], v[72:75]\n" \
"ds_read_b128 v[20:23], v12 offset:20480\n" \
"v_mul_f32 v51, 0xbfb8aa3b, v51\n" \
"v_mfma_f32_16x16x32_bf16 v[76:79], a[132:135], v[28:31], v[76:79]\n" \
"s_waitcnt lgkmcnt(4)\n" \
"v_exp_f32 v48, v48\n" \
"v_mfma_f32_16x16x32_bf16 v[64:67], a[8:11], v[32:35], v[64:67]\n" \
"s_add_u32 s63, s59, 2\n" \
"s_min_u32 s63, s63, 63\n" \
"v_mfma_f32_16x16x32_bf16 v[68:71], a[8:11], v[36:39], v[68:71]\n" \
"ds_read_b128 v[24:27], v12 offset:5120\n" \
"s_lshr_b32 s63, s63, 3\n" \
"s_lshl_b32 s63, s63, 7\n" \
"v_mfma_f32_16x16x32_bf16 v[72:75], a[136:139], v[32:35], v[72:75]\n" \
"ds_read_b128 v[28:31], v12 offset:21504\n" \
"v_mfma_f32_16x16x32_bf16 v[76:79], a[136:139], v[36:39], v[76:79]\n" \
"s_waitcnt lgkmcnt(4)\n" \
"v_exp_f32 v49, v49\n" \
"v_mfma_f32_16x16x32_bf16 v[64:67], a[12:15], v[40:43], v[64:67]\n" \
"buffer_load_dwordx4 v4, s[36:39], s61 offen offset:2048 sc1 lds\n" \
"v_mfma_f32_16x16x32_bf16 v[68:71], a[12:15], v[44:47], v[68:71]\n" \
"ds_read_b128 v[32:35], v12 offset:6144\n" \
"v_mfma_f32_16x16x32_bf16 v[72:75], a[140:143], v[40:43], v[72:75]\n" \
"ds_read_b128 v[36:39], v12 offset:22528\n" \
"v_mfma_f32_16x16x32_bf16 v[76:79], a[140:143], v[44:47], v[76:79]\n" \
"s_waitcnt lgkmcnt(4)\n" \
"v_exp_f32 v50, v50\n" \
"v_mfma_f32_16x16x32_bf16 v[64:67], a[16:19], v[16:19], v[64:67]\n" \
"buffer_load_dwordx4 v4, s[36:39], s61 offen offset:3072 sc1 lds\n" \
"v_mfma_f32_16x16x32_bf16 v[68:71], a[16:19], v[20:23], v[68:71]\n" \
"ds_read_b128 v[40:43], v12 offset:7168\n" \
"v_mfma_f32_16x16x32_bf16 v[72:75], a[144:147], v[16:19], v[72:75]\n" \
"ds_read_b128 v[44:47], v12 offset:23552\n" \
"v_mfma_f32_16x16x32_bf16 v[76:79], a[144:147], v[20:23], v[76:79]\n" \
"s_waitcnt lgkmcnt(4)\n" \
"v_exp_f32 v51, v51\n" \
"v_mfma_f32_16x16x32_bf16 v[64:67], a[20:23], v[24:27], v[64:67]\n" \
"buffer_load_dword v15, v14, s[36:39], s63 offen sc1\n" \
"v_mfma_f32_16x16x32_bf16 v[68:71], a[20:23], v[28:31], v[68:71]\n" \
"ds_read_b128 v[16:19], v12 offset:8192\n" \
"v_add_f32_e64 v48, 1.0, v48\n" \
"v_mfma_f32_16x16x32_bf16 v[72:75], a[148:151], v[24:27], v[72:75]\n" \
"ds_read_b128 v[20:23], v12 offset:24576\n" \
"v_add_f32_e64 v49, 1.0, v49\n" \
"v_mfma_f32_16x16x32_bf16 v[76:79], a[148:151], v[28:31], v[76:79]\n" \
"s_waitcnt lgkmcnt(4)\n" \
"v_add_f32 v50, 1.0, v50\n" \
"v_add_f32_e64 v51, 1.0, v51\n" \
"v_mfma_f32_16x16x32_bf16 v[64:67], a[24:27], v[32:35], v[64:67]\n" \
"s_add_u32 m0, s55, 4096\n" \
"s_add_u32 s61, s61, 4096\n" \
"buffer_load_dwordx4 v4, s[36:39], s61 offen offset:0 sc1 lds\n" \
"v_mfma_f32_16x16x32_bf16 v[68:71], a[24:27], v[36:39], v[68:71]\n" \
"ds_read_b128 v[24:27], v12 offset:9216\n" \
"v_mfma_f32_16x16x32_bf16 v[72:75], a[152:155], v[32:35], v[72:75]\n" \
"ds_read_b128 v[28:31], v12 offset:25600\n" \
"v_mfma_f32_16x16x32_bf16 v[76:79], a[152:155], v[36:39], v[76:79]\n" \
"s_waitcnt lgkmcnt(4)\n" \
"v_rcp_f32 v48, v48\n" \
"v_mfma_f32_16x16x32_bf16 v[64:67], a[28:31], v[40:43], v[64:67]\n" \
"buffer_load_dwordx4 v4, s[36:39], s61 offen offset:1024 sc1 lds\n" \
"v_mfma_f32_16x16x32_bf16 v[68:71], a[28:31], v[44:47], v[68:71]\n" \
"ds_read_b128 v[32:35], v12 offset:10240\n" \
"v_mfma_f32_16x16x32_bf16 v[72:75], a[156:159], v[40:43], v[72:75]\n" \
"ds_read_b128 v[36:39], v12 offset:26624\n" \
"v_mfma_f32_16x16x32_bf16 v[76:79], a[156:159], v[44:47], v[76:79]\n" \
"s_waitcnt lgkmcnt(4)\n" \
"v_rcp_f32 v49, v49\n" \
"v_mfma_f32_16x16x32_bf16 v[64:67], a[32:35], v[16:19], v[64:67]\n" \
"v_rcp_f32_e64 v50, v50\n" \
"v_mfma_f32_16x16x32_bf16 v[68:71], a[32:35], v[20:23], v[68:71]\n" \
"ds_read_b128 v[40:43], v12 offset:11264\n" \
"v_mfma_f32_16x16x32_bf16 v[72:75], a[160:163], v[16:19], v[72:75]\n" \
"ds_read_b128 v[44:47], v12 offset:27648\n" \
"v_mfma_f32_16x16x32_bf16 v[76:79], a[160:163], v[20:23], v[76:79]\n" \
"s_waitcnt lgkmcnt(4)\n" \
"v_rcp_f32 v51, v51\n" \
"v_mfma_f32_16x16x32_bf16 v[64:67], a[36:39], v[24:27], v[64:67]\n" \
"buffer_load_dwordx4 v4, s[36:39], s61 offen offset:2048 sc1 lds\n" \
"v_mfma_f32_16x16x32_bf16 v[68:71], a[36:39], v[28:31], v[68:71]\n" \
"ds_read_b128 v[16:19], v12 offset:12288\n" \
"v_mul_f32_e64 v48, v48, v56\n" \
"v_mfma_f32_16x16x32_bf16 v[72:75], a[164:167], v[24:27], v[72:75]\n" \
"ds_read_b128 v[20:23], v12 offset:28672\n" \
"v_mul_f32_e64 v49, v49, v57\n" \
"v_mfma_f32_16x16x32_bf16 v[76:79], a[164:167], v[28:31], v[76:79]\n" \
"s_waitcnt lgkmcnt(4)\n" \
"v_mul_f32 v50, v50, v58\n" \
"v_mul_f32_e64 v51, v51, v59\n" \
"v_mfma_f32_16x16x32_bf16 v[64:67], a[40:43], v[32:35], v[64:67]\n" \
"buffer_load_dwordx4 v4, s[36:39], s61 offen offset:3072 sc1 lds\n" \
"v_mfma_f32_16x16x32_bf16 v[68:71], a[40:43], v[36:39], v[68:71]\n" \
"ds_read_b128 v[24:27], v12 offset:13312\n" \
"v_cvt_pk_bf16_f32 v80, v48, v49\n" \
"v_mfma_f32_16x16x32_bf16 v[72:75], a[168:171], v[32:35], v[72:75]\n" \
"ds_read_b128 v[28:31], v12 offset:29696\n" \
"v_cvt_pk_bf16_f32 v81, v50, v51\n" \
"v_mfma_f32_16x16x32_bf16 v[76:79], a[168:171], v[36:39], v[76:79]\n" \
"s_waitcnt lgkmcnt(4)\n" \
"v_mul_f32 v52, 0xbfb8aa3b, v52\n" \
"v_mul_f32 v53, 0xbfb8aa3b, v53\n" \
"v_mfma_f32_16x16x32_bf16 v[64:67], a[44:47], v[40:43], v[64:67]\n" \
"v_mul_f32 v54, 0xbfb8aa3b, v54\n" \
"v_mul_f32 v55, 0xbfb8aa3b, v55\n" \
"v_mfma_f32_16x16x32_bf16 v[68:71], a[44:47], v[44:47], v[68:71]\n" \
"ds_read_b128 v[32:35], v12 offset:14336\n" \
"v_mfma_f32_16x16x32_bf16 v[72:75], a[172:175], v[40:43], v[72:75]\n" \
"ds_read_b128 v[36:39], v12 offset:30720\n" \
"v_mfma_f32_16x16x32_bf16 v[76:79], a[172:175], v[44:47], v[76:79]\n" \
"s_waitcnt lgkmcnt(4)\n" \
"v_exp_f32_e64 v52, v52\n" \
"v_mfma_f32_16x16x32_bf16 v[64:67], a[48:51], v[16:19], v[64:67]\n" \
"v_exp_f32_e64 v53, v53\n" \
"v_mfma_f32_16x16x32_bf16 v[68:71], a[48:51], v[20:23], v[68:71]\n" \
"ds_read_b128 v[40:43], v12 offset:15360\n" \
"v_mfma_f32_16x16x32_bf16 v[72:75], a[176:179], v[16:19], v[72:75]\n" \
"ds_read_b128 v[44:47], v12 offset:31744\n" \
"v_mfma_f32_16x16x32_bf16 v[76:79], a[176:179], v[20:23], v[76:79]\n" \
"s_waitcnt lgkmcnt(4)\n" \
"v_exp_f32 v54, v54\n" \
"v_mfma_f32_16x16x32_bf16 v[64:67], a[52:55], v[24:27], v[64:67]\n" \
"s_waitcnt vmcnt(9)\n" \
"s_barrier\n" \
"v_mfma_f32_16x16x32_bf16 v[68:71], a[52:55], v[28:31], v[68:71]\n" \
"ds_read_b128 v[16:19], v12 offset:32768\n" \
"v_mfma_f32_16x16x32_bf16 v[72:75], a[180:183], v[24:27], v[72:75]\n" \
"ds_read_b128 v[20:23], v12 offset:49152\n" \
"v_mfma_f32_16x16x32_bf16 v[76:79], a[180:183], v[28:31], v[76:79]\n" \
"s_waitcnt lgkmcnt(4)\n" \
"buffer_store_dword v13, v6, s[36:39], 0 offen sc1\n" \
"v_exp_f32 v55, v55\n" \
"v_mfma_f32_16x16x32_bf16 v[64:67], a[56:59], v[32:35], v[64:67]\n" \
"v_add_f32 v52, 1.0, v52\n" \
"v_add_f32 v53, 1.0, v53\n" \
"v_mfma_f32_16x16x32_bf16 v[68:71], a[56:59], v[36:39], v[68:71]\n" \
"ds_read_b128 v[24:27], v12 offset:33792\n" \
"v_add_f32_e64 v54, 1.0, v54\n" \
"v_mfma_f32_16x16x32_bf16 v[72:75], a[184:187], v[32:35], v[72:75]\n" \
"ds_read_b128 v[28:31], v12 offset:50176\n" \
"v_add_f32_e64 v55, 1.0, v55\n" \
"v_mfma_f32_16x16x32_bf16 v[76:79], a[184:187], v[36:39], v[76:79]\n" \
"s_waitcnt lgkmcnt(4)\n" \
"v_rcp_f32 v52, v52\n" \
"v_mfma_f32_16x16x32_bf16 v[64:67], a[60:63], v[40:43], v[64:67]\n" \
"v_rcp_f32_e64 v53, v53\n" \
"v_mfma_f32_16x16x32_bf16 v[68:71], a[60:63], v[44:47], v[68:71]\n" \
"ds_read_b128 v[32:35], v12 offset:34816\n" \
"v_mfma_f32_16x16x32_bf16 v[72:75], a[188:191], v[40:43], v[72:75]\n" \
"ds_read_b128 v[36:39], v12 offset:51200\n" \
"v_mfma_f32_16x16x32_bf16 v[76:79], a[188:191], v[44:47], v[76:79]\n" \
"s_waitcnt lgkmcnt(4)\n" \
"v_rcp_f32 v54, v54\n" \
"v_mfma_f32_16x16x32_bf16 v[64:67], a[64:67], v[16:19], v[64:67]\n" \
"s_add_u32 s61, s61, 28672\n" \
"s_add_u32 s56, s56, 0x20000\n" \
"s_add_u32 m0, s55, 32768\n" \
"s_nop 0\n" \
"buffer_load_dwordx4 v4, s[36:39], s61 offen offset:0 sc1 lds\n" \
"v_mfma_f32_16x16x32_bf16 v[68:71], a[64:67], v[20:23], v[68:71]\n" \
"ds_read_b128 v[40:43], v12 offset:35840\n" \
"v_mfma_f32_16x16x32_bf16 v[72:75], a[192:195], v[16:19], v[72:75]\n" \
"ds_read_b128 v[44:47], v12 offset:52224\n" \
"v_mfma_f32_16x16x32_bf16 v[76:79], a[192:195], v[20:23], v[76:79]\n" \
"s_waitcnt lgkmcnt(4)\n" \
"v_rcp_f32_e64 v55, v55\n" \
"v_mfma_f32_16x16x32_bf16 v[64:67], a[68:71], v[24:27], v[64:67]\n" \
"buffer_load_dwordx4 v4, s[36:39], s61 offen offset:1024 sc1 lds\n" \
"v_mfma_f32_16x16x32_bf16 v[68:71], a[68:71], v[28:31], v[68:71]\n" \
"ds_read_b128 v[16:19], v12 offset:36864\n" \
"v_mul_f32_e64 v52, v52, v60\n" \
"v_mfma_f32_16x16x32_bf16 v[72:75], a[196:199], v[24:27], v[72:75]\n" \
"ds_read_b128 v[20:23], v12 offset:53248\n" \
"v_mul_f32_e64 v53, v53, v61\n" \
"v_mfma_f32_16x16x32_bf16 v[76:79], a[196:199], v[28:31], v[76:79]\n" \
"s_waitcnt lgkmcnt(4)\n" \
"v_mul_f32 v54, v54, v62\n" \
"v_mul_f32_e64 v55, v55, v63\n" \
"v_mfma_f32_16x16x32_bf16 v[64:67], a[72:75], v[32:35], v[64:67]\n" \
"v_cvt_pk_bf16_f32 v82, v52, v53\n" \
"v_cvt_pk_bf16_f32 v83, v54, v55\n" \
"v_mfma_f32_16x16x32_bf16 v[68:71], a[72:75], v[36:39], v[68:71]\n" \
"ds_read_b128 v[24:27], v12 offset:37888\n" \
"s_nop 0\n" \
"v_mfma_f32_16x16x32_bf16 v[72:75], a[200:203], v[32:35], v[72:75]\n" \
"ds_read_b128 v[28:31], v12 offset:54272\n" \
"s_nop 0\n" \
"v_mfma_f32_16x16x32_bf16 v[76:79], a[200:203], v[36:39], v[76:79]\n" \
"s_waitcnt lgkmcnt(4)\n" \
"v_permlane16_swap_b32 v80, v82\n" \
"v_permlane16_swap_b32 v81, v83\n" \
"v_mfma_f32_16x16x32_bf16 v[64:67], a[76:79], v[40:43], v[64:67]\n" \
"buffer_load_dwordx4 v4, s[36:39], s61 offen offset:2048 sc1 lds\n" \
"v_mfma_f32_16x16x32_bf16 v[68:71], a[76:79], v[44:47], v[68:71]\n" \
"ds_read_b128 v[32:35], v12 offset:38912\n" \
"buffer_store_dwordx4 v[80:83], v5, s[36:39], s57 offen sc1\n" \
"v_mfma_f32_16x16x32_bf16 v[72:75], a[204:207], v[40:43], v[72:75]\n" \
"ds_read_b128 v[36:39], v12 offset:55296\n" \
"v_mfma_f32_16x16x32_bf16 v[76:79], a[204:207], v[44:47], v[76:79]\n" \
"s_waitcnt lgkmcnt(4)\n" \
"v_mfma_f32_16x16x32_bf16 v[64:67], a[80:83], v[16:19], v[64:67]\n" \
"buffer_load_dwordx4 v4, s[36:39], s61 offen offset:3072 sc1 lds\n" \
"v_mfma_f32_16x16x32_bf16 v[68:71], a[80:83], v[20:23], v[68:71]\n" \
"ds_read_b128 v[40:43], v12 offset:39936\n" \
"v_mfma_f32_16x16x32_bf16 v[72:75], a[208:211], v[16:19], v[72:75]\n" \
"ds_read_b128 v[44:47], v12 offset:56320\n" \
"v_mfma_f32_16x16x32_bf16 v[76:79], a[208:211], v[20:23], v[76:79]\n" \
"s_waitcnt lgkmcnt(4)\n" \
"v_mfma_f32_16x16x32_bf16 v[64:67], a[84:87], v[24:27], v[64:67]\n" \
"v_mfma_f32_16x16x32_bf16 v[68:71], a[84:87], v[28:31], v[68:71]\n" \
"ds_read_b128 v[16:19], v12 offset:40960\n" \
"v_mfma_f32_16x16x32_bf16 v[72:75], a[212:215], v[24:27], v[72:75]\n" \
"ds_read_b128 v[20:23], v12 offset:57344\n" \
"v_mfma_f32_16x16x32_bf16 v[76:79], a[212:215], v[28:31], v[76:79]\n" \
"s_waitcnt lgkmcnt(4)\n" \
"v_mfma_f32_16x16x32_bf16 v[64:67], a[88:91], v[32:35], v[64:67]\n" \
"s_add_u32 m0, s55, 36864\n" \
"s_add_u32 s61, s61, 4096\n" \
"buffer_load_dwordx4 v4, s[36:39], s61 offen offset:0 sc1 lds\n" \
"v_mfma_f32_16x16x32_bf16 v[68:71], a[88:91], v[36:39], v[68:71]\n" \
"ds_read_b128 v[24:27], v12 offset:41984\n" \
"v_mfma_f32_16x16x32_bf16 v[72:75], a[216:219], v[32:35], v[72:75]\n" \
"ds_read_b128 v[28:31], v12 offset:58368\n" \
"v_mfma_f32_16x16x32_bf16 v[76:79], a[216:219], v[36:39], v[76:79]\n" \
"s_waitcnt lgkmcnt(4)\n" \
"v_mfma_f32_16x16x32_bf16 v[64:67], a[92:95], v[40:43], v[64:67]\n" \
"buffer_load_dwordx4 v4, s[36:39], s61 offen offset:1024 sc1 lds\n" \
"v_mfma_f32_16x16x32_bf16 v[68:71], a[92:95], v[44:47], v[68:71]\n" \
"ds_read_b128 v[32:35], v12 offset:43008\n" \
"v_mfma_f32_16x16x32_bf16 v[72:75], a[220:223], v[40:43], v[72:75]\n" \
"ds_read_b128 v[36:39], v12 offset:59392\n" \
"v_mfma_f32_16x16x32_bf16 v[76:79], a[220:223], v[44:47], v[76:79]\n" \
"s_waitcnt lgkmcnt(4)\n" \
"v_mfma_f32_16x16x32_bf16 v[64:67], a[96:99], v[16:19], v[64:67]\n" \
"v_mfma_f32_16x16x32_bf16 v[68:71], a[96:99], v[20:23], v[68:71]\n" \
"ds_read_b128 v[40:43], v12 offset:44032\n" \
"v_mfma_f32_16x16x32_bf16 v[72:75], a[224:227], v[16:19], v[72:75]\n" \
"ds_read_b128 v[44:47], v12 offset:60416\n" \
"v_mfma_f32_16x16x32_bf16 v[76:79], a[224:227], v[20:23], v[76:79]\n" \
"s_waitcnt lgkmcnt(4)\n" \
"v_mfma_f32_16x16x32_bf16 v[64:67], a[100:103], v[24:27], v[64:67]\n" \
"buffer_load_dwordx4 v4, s[36:39], s61 offen offset:2048 sc1 lds\n" \
"v_mfma_f32_16x16x32_bf16 v[68:71], a[100:103], v[28:31], v[68:71]\n" \
"ds_read_b128 v[16:19], v12 offset:45056\n" \
"v_mfma_f32_16x16x32_bf16 v[72:75], a[228:231], v[24:27], v[72:75]\n" \
"ds_read_b128 v[20:23], v12 offset:61440\n" \
"v_mfma_f32_16x16x32_bf16 v[76:79], a[228:231], v[28:31], v[76:79]\n" \
"s_waitcnt lgkmcnt(4)\n" \
"v_mfma_f32_16x16x32_bf16 v[64:67], a[104:107], v[32:35], v[64:67]\n" \
"buffer_load_dwordx4 v4, s[36:39], s61 offen offset:3072 sc1 lds\n" \
"v_mfma_f32_16x16x32_bf16 v[68:71], a[104:107], v[36:39], v[68:71]\n" \
"ds_read_b128 v[24:27], v12 offset:46080\n" \
"v_mfma_f32_16x16x32_bf16 v[72:75], a[232:235], v[32:35], v[72:75]\n" \
"ds_read_b128 v[28:31], v12 offset:62464\n" \
"v_mfma_f32_16x16x32_bf16 v[76:79], a[232:235], v[36:39], v[76:79]\n" \
"s_waitcnt lgkmcnt(4)\n" \
"v_mfma_f32_16x16x32_bf16 v[64:67], a[108:111], v[40:43], v[64:67]\n" \
"s_lshl_b32 s66, s59, s49\n" \
"s_lshr_b32 s65, s66, 8\n" \
"v_mfma_f32_16x16x32_bf16 v[68:71], a[108:111], v[44:47], v[68:71]\n" \
"ds_read_b128 v[32:35], v12 offset:47104\n" \
"v_mfma_f32_16x16x32_bf16 v[72:75], a[236:239], v[40:43], v[72:75]\n" \
"ds_read_b128 v[36:39], v12 offset:63488\n" \
"v_mfma_f32_16x16x32_bf16 v[76:79], a[236:239], v[44:47], v[76:79]\n" \
"s_waitcnt lgkmcnt(4)\n" \
"s_and_b32 s65, s65, 0x2000\n" \
"s_xor_b32 s66, s66, s65\n" \
"v_mfma_f32_16x16x32_bf16 v[64:67], a[112:115], v[16:19], v[64:67]\n" \
"v_mfma_f32_16x16x32_bf16 v[68:71], a[112:115], v[20:23], v[68:71]\n" \
"ds_read_b128 v[40:43], v12 offset:48128\n" \
"v_mfma_f32_16x16x32_bf16 v[72:75], a[240:243], v[16:19], v[72:75]\n" \
"ds_read_b128 v[44:47], v12 offset:64512\n" \
"v_mfma_f32_16x16x32_bf16 v[76:79], a[240:243], v[20:23], v[76:79]\n" \
"s_waitcnt lgkmcnt(4)\n" \
"v_mfma_f32_16x16x32_bf16 v[64:67], a[116:119], v[24:27], v[64:67]\n" \
"s_waitcnt vmcnt(10)\n" \
"s_barrier\n" \
"v_mfma_f32_16x16x32_bf16 v[68:71], a[116:119], v[28:31], v[68:71]\n" \
"ds_read_b128 v[16:19], v10 offset:0\n" \
"v_mfma_f32_16x16x32_bf16 v[72:75], a[244:247], v[24:27], v[72:75]\n" \
"ds_read_b128 v[20:23], v10 offset:16384\n" \
"v_mfma_f32_16x16x32_bf16 v[76:79], a[244:247], v[28:31], v[76:79]\n" \
"s_waitcnt lgkmcnt(4)\n" \
"v_mfma_f32_16x16x32_bf16 v[64:67], a[120:123], v[32:35], v[64:67]\n" \
"v_cmp_gt_u32 vcc, 16, v15\n" \
"v_mfma_f32_16x16x32_bf16 v[68:71], a[120:123], v[36:39], v[68:71]\n" \
"ds_read_b128 v[24:27], v10 offset:1024\n" \
"v_mfma_f32_16x16x32_bf16 v[72:75], a[248:251], v[32:35], v[72:75]\n" \
"ds_read_b128 v[28:31], v10 offset:17408\n" \
"v_mfma_f32_16x16x32_bf16 v[76:79], a[248:251], v[36:39], v[76:79]\n" \
"s_waitcnt lgkmcnt(4)\n" \
"v_mfma_f32_16x16x32_bf16 v[64:67], a[124:127], v[40:43], v[64:67]\n" \
"s_cbranch_vccnz .Lp1g1_m1_%=\n" \
".Lp1g1_m1k_%=:\n" \
"v_mfma_f32_16x16x32_bf16 v[68:71], a[124:127], v[44:47], v[68:71]\n" \
"ds_read_b128 v[32:35], v10 offset:2048\n" \
"v_mfma_f32_16x16x32_bf16 v[72:75], a[252:255], v[40:43], v[72:75]\n" \
"ds_read_b128 v[36:39], v10 offset:18432\n" \
"v_mfma_f32_16x16x32_bf16 v[76:79], a[252:255], v[44:47], v[76:79]\n" \
"s_waitcnt lgkmcnt(4)\n" \
"s_xor_b32 s57, s66, s64\n" \
"s_add_u32 s58, s57, s50\n" \
"s_add_u32 s59, s59, 1\n" \
"v_mov_b32_e64 v5, v7\n" \
"v_mov_b32_e64 v6, v11\n" \
"s_cmp_ge_u32 s59, s47\n" \
"s_nop 0\n" \
"s_cbranch_scc0 .Lp1g1_loop_%=\n" \
".Lp1g1_out1_%=:\n" \
"s_nop 7\n" \
"s_nop 7\n" \
"v_mul_f32 v64, 0xbfb8aa3b, v64\n" \
"v_mul_f32 v65, 0xbfb8aa3b, v65\n" \
"v_mul_f32 v66, 0xbfb8aa3b, v66\n" \
"v_mul_f32 v67, 0xbfb8aa3b, v67\n" \
"v_exp_f32 v64, v64\n" \
"v_exp_f32 v65, v65\n" \
"v_exp_f32 v66, v66\n" \
"v_exp_f32 v67, v67\n" \
"v_add_f32 v64, 1.0, v64\n" \
"v_add_f32 v65, 1.0, v65\n" \
"v_add_f32 v66, 1.0, v66\n" \
"v_add_f32 v67, 1.0, v67\n" \
"v_rcp_f32 v64, v64\n" \
"v_rcp_f32 v65, v65\n" \
"v_rcp_f32 v66, v66\n" \
"v_rcp_f32 v67, v67\n" \
"v_mul_f32 v64, v64, v72\n" \
"v_mul_f32 v65, v65, v73\n" \
"v_mul_f32 v66, v66, v74\n" \
"v_mul_f32 v67, v67, v75\n" \
"v_cvt_pk_bf16_f32 v80, v64, v65\n" \
"v_cvt_pk_bf16_f32 v81, v66, v67\n" \
"v_mul_f32 v68, 0xbfb8aa3b, v68\n" \
"v_mul_f32 v69, 0xbfb8aa3b, v69\n" \
"v_mul_f32 v70, 0xbfb8aa3b, v70\n" \
"v_mul_f32 v71, 0xbfb8aa3b, v71\n" \
"v_exp_f32 v68, v68\n" \
"v_exp_f32 v69, v69\n" \
"v_exp_f32 v70, v70\n" \
"v_exp_f32 v71, v71\n" \
"v_add_f32 v68, 1.0, v68\n" \
"v_add_f32 v69, 1.0, v69\n" \
"v_add_f32 v70, 1.0, v70\n" \
"v_add_f32 v71, 1.0, v71\n" \
"v_rcp_f32 v68, v68\n" \
"v_rcp_f32 v69, v69\n" \
"v_rcp_f32 v70, v70\n" \
"v_rcp_f32 v71, v71\n" \
"v_mul_f32 v68, v68, v76\n" \
"v_mul_f32 v69, v69, v77\n" \
"v_mul_f32 v70, v70, v78\n" \
"v_mul_f32 v71, v71, v79\n" \
"v_cvt_pk_bf16_f32 v82, v68, v69\n" \
"v_cvt_pk_bf16_f32 v83, v70, v71\n" \
"s_nop 0\n" \
"s_nop 0\n" \
"v_permlane16_swap_b32 v80, v82\n" \
"v_permlane16_swap_b32 v81, v83\n" \
"buffer_store_dwordx4 v[80:83], v5, s[36:39], s57 offen sc1\n" \
"s_branch .Lp1g1_end_%=\n" \
".Lp1g1_out0_%=:\n" \
"s_nop 7\n" \
"s_nop 7\n" \
"v_mul_f32 v48, 0xbfb8aa3b, v48\n" \
"v_mul_f32 v49, 0xbfb8aa3b, v49\n" \
"v_mul_f32 v50, 0xbfb8aa3b, v50\n" \
"v_mul_f32 v51, 0xbfb8aa3b, v51\n" \
"v_exp_f32 v48, v48\n" \
"v_exp_f32 v49, v49\n" \
"v_exp_f32 v50, v50\n" \
"v_exp_f32 v51, v51\n" \
"v_add_f32 v48, 1.0, v48\n" \
"v_add_f32 v49, 1.0, v49\n" \
"v_add_f32 v50, 1.0, v50\n" \
"v_add_f32 v51, 1.0, v51\n" \
"v_rcp_f32 v48, v48\n" \
"v_rcp_f32 v49, v49\n" \
"v_rcp_f32 v50, v50\n" \
"v_rcp_f32 v51, v51\n" \
"v_mul_f32 v48, v48, v56\n" \
"v_mul_f32 v49, v49, v57\n" \
"v_mul_f32 v50, v50, v58\n" \
"v_mul_f32 v51, v51, v59\n" \
"v_cvt_pk_bf16_f32 v80, v48, v49\n" \
"v_cvt_pk_bf16_f32 v81, v50, v51\n" \
"v_mul_f32 v52, 0xbfb8aa3b, v52\n" \
"v_mul_f32 v53, 0xbfb8aa3b, v53\n" \
"v_mul_f32 v54, 0xbfb8aa3b, v54\n" \
"v_mul_f32 v55, 0xbfb8aa3b, v55\n" \
"v_exp_f32 v52, v52\n" \
"v_exp_f32 v53, v53\n" \
"v_exp_f32 v54, v54\n" \
"v_exp_f32 v55, v55\n" \
"v_add_f32 v52, 1.0, v52\n" \
"v_add_f32 v53, 1.0, v53\n" \
"v_add_f32 v54, 1.0, v54\n" \
"v_add_f32 v55, 1.0, v55\n" \
"v_rcp_f32 v52, v52\n" \
"v_rcp_f32 v53, v53\n" \
"v_rcp_f32 v54, v54\n" \
"v_rcp_f32 v55, v55\n" \
"v_mul_f32 v52, v52, v60\n" \
"v_mul_f32 v53, v53, v61\n" \
"v_mul_f32 v54, v54, v62\n" \
"v_mul_f32 v55, v55, v63\n" \
"v_cvt_pk_bf16_f32 v82, v52, v53\n" \
"v_cvt_pk_bf16_f32 v83, v54, v55\n" \
"s_nop 0\n" \
"s_nop 0\n" \
"v_permlane16_swap_b32 v80, v82\n" \
"v_permlane16_swap_b32 v81, v83\n" \
"buffer_store_dwordx4 v[80:83], v5, s[36:39], s57 offen sc1\n" \
".Lp1g1_end_%=:\n" \
"s_waitcnt vmcnt(0) lgkmcnt(0)\n" \
"s_barrier\n" \
"v_mov_b32 v13, s59\n" \
"buffer_store_dword v13, v6, s[36:39], 0 offen sc1\n" \
"s_waitcnt vmcnt(0) lgkmcnt(0)\n" \
"s_branch .Lp1g1_x_%=\n" \
".Lp1g1_l0_%=:\n" \
"s_nop 7\n" \
"s_nop 7\n" \
"s_waitcnt vmcnt(0) lgkmcnt(0)\n" \
"s_mov_b32 s62, 0\n" \
".Lp1g1_l0a_%=:\n" \
"buffer_load_dword v15, v14, s[36:39], s63 offen sc1\n" \
"s_waitcnt vmcnt(0)\n" \
"v_cmp_gt_u32 vcc, 16, v15\n" \
"s_nop 4\n" \
"s_cbranch_vccz .Lp1g1_l0k_%=\n" \
"s_sleep 4\n" \
"s_add_u32 s62, s62, 1\n" \
"s_cmp_lt_u32 s62, 0x10000\n" \
"s_cbranch_scc1 .Lp1g1_l0a_%=\n" \
"v_mov_b32 v15, 256\n" \
"buffer_store_dword v15, v15, s[36:39], 0 offen sc1\n" \
"s_waitcnt vmcnt(0) lgkmcnt(0)\n" \
"s_branch .Lp1g1_l0k_%=\n" \
".Lp1g1_l1_%=:\n" \
"s_nop 7\n" \
"s_nop 7\n" \
"s_waitcnt vmcnt(0) lgkmcnt(0)\n" \
"s_mov_b32 s62, 0\n" \
".Lp1g1_l1a_%=:\n" \
"buffer_load_dword v15, v14, s[36:39], s63 offen sc1\n" \
"s_waitcnt vmcnt(0)\n" \
"v_cmp_gt_u32 vcc, 16, v15\n" \
"s_nop 4\n" \
"s_cbranch_vccz .Lp1g1_l1k_%=\n" \
"s_sleep 4\n" \
"s_add_u32 s62, s62, 1\n" \
"s_cmp_lt_u32 s62, 0x10000\n" \
"s_cbranch_scc1 .Lp1g1_l1a_%=\n" \
"v_mov_b32 v15, 256\n" \
"buffer_store_dword v15, v15, s[36:39], 0 offen sc1\n" \
"s_waitcnt vmcnt(0) lgkmcnt(0)\n" \
"s_branch .Lp1g1_l1k_%=\n" \
".Lp1g1_m0_%=:\n" \
"s_nop 7\n" \
"s_nop 7\n" \
"s_waitcnt vmcnt(0) lgkmcnt(0)\n" \
"s_mov_b32 s62, 0\n" \
".Lp1g1_m0a_%=:\n" \
"buffer_load_dword v15, v14, s[36:39], s63 offen sc1\n" \
"s_waitcnt vmcnt(0)\n" \
"v_cmp_gt_u32 vcc, 16, v15\n" \
"s_nop 4\n" \
"s_cbranch_vccz .Lp1g1_m0k_%=\n" \
"s_sleep 4\n" \
"s_add_u32 s62, s62, 1\n" \
"s_cmp_lt_u32 s62, 0x10000\n" \
"s_cbranch_scc1 .Lp1g1_m0a_%=\n" \
"v_mov_b32 v15, 256\n" \
"buffer_store_dword v15, v15, s[36:39], 0 offen sc1\n" \
"s_waitcnt vmcnt(0) lgkmcnt(0)\n" \
"s_branch .Lp1g1_m0k_%=\n" \
".Lp1g1_m1_%=:\n" \
"s_nop 7\n" \
"s_nop 7\n" \
"s_waitcnt vmcnt(0) lgkmcnt(0)\n" \
"s_mov_b32 s62, 0\n" \
".Lp1g1_m1a_%=:\n" \
"buffer_load_dword v15, v14, s[36:39], s63 offen sc1\n" \
"s_waitcnt vmcnt(0)\n" \
"v_cmp_gt_u32 vcc, 16, v15\n" \
"s_nop 4\n" \
"s_cbranch_vccz .Lp1g1_m1k_%=\n" \
"s_sleep 4\n" \
"s_add_u32 s62, s62, 1\n" \
"s_cmp_lt_u32 s62, 0x10000\n" \
"s_cbranch_scc1 .Lp1g1_m1a_%=\n" \
"v_mov_b32 v15, 256\n" \
"buffer_store_dword v15, v15, s[36:39], 0 offen sc1\n" \
"s_waitcnt vmcnt(0) lgkmcnt(0)\n" \
"s_branch .Lp1g1_m1k_%=\n" \
".Lp1g1_x_%=:\n"
#define CLOB_P1 \
"a0", "a1", "a2", "a3", "a4", "a5", "a6", "a7", "a8", "a9", "a10", "a11", "a12", "a13", "a14", "a15", "a16", "a17", "a18", "a19", "a20", "a21", "a22", "a23", "a24", "a25", "a26", "a27", "a28", "a29", "a30", "a31", "a32", "a33", "a34", "a35", "a36", "a37", "a38", "a39", "a40", "a41", "a42", "a43", "a44", "a45", "a46", "a47", "a48", "a49", "a50", "a51", "a52", "a53", "a54", "a55", "a56", "a57", "a58", "a59", "a60", "a61", "a62", "a63", "a64", "a65", "a66", "a67", "a68", "a69", "a70", "a71", "a72", "a73", "a74", "a75", "a76", "a77", "a78", "a79", "a80", "a81", "a82", "a83", "a84", "a85", "a86", "a87", "a88", "a89", "a90", "a91", "a92", "a93", "a94", "a95", "a96", "a97", "a98", "a99", "a100", "a101", "a102", "a103", "a104", "a105", "a106", "a107", "a108", "a109", "a110", "a111", "a112", "a113", "a114", "a115", "a116", "a117", "a118", "a119", "a120", "a121", "a122", "a123", "a124", "a125", "a126", "a127", "a128", "a129", "a130", "a131", "a132", "a133", "a134", "a135", "a136", "a137", "a138", "a139", "a140", "a141", "a142", "a143", "a144", "a145", "a146", "a147", "a148", "a149", "a150", "a151", "a152", "a153", "a154", "a155", "a156", "a157", "a158", "a159", "a160", "a161", "a162", "a163", "a164", "a165", "a166", "a167", "a168", "a169", "a170", "a171", "a172", "a173", "a174", "a175", "a176", "a177", "a178", "a179", "a180", "a181", "a182", "a183", "a184", "a185", "a186", "a187", "a188", "a189", "a190", "a191", "a192", "a193", "a194", "a195", "a196", "a197", "a198", "a199", "a200", "a201", "a202", "a203", "a204", "a205", "a206", "a207", "a208", "a209", "a210", "a211", "a212", "a213", "a214", "a215", "a216", "a217", "a218", "a219", "a220", "a221", "a222", "a223", "a224", "a225", "a226", "a227", "a228", "a229", "a230", "a231", "a232", "a233", "a234", "a235", "a236", "a237", "a238", "a239", "a240", "a241", "a242", "a243", "a244", "a245", "a246", "a247", "a248", "a249", "a250", "a251", "a252", "a253", "a254", "a255", "s52", "s53", "s54", "s55", "s56", "s57", "s58", "s59", "s60", "s61", "s62", "s63", "s65", "s66", "v7", "v8", "v9", "v10", "v11", "v12", "v13", "v14", "v15", "v16", "v17", "v18", "v19", "v20", "v21", "v22", "v23", "v24", "v25", "v26", "v27", "v28", "v29", "v30", "v31", "v32", "v33", "v34", "v35", "v36", "v37", "v38", "v39", "v40", "v41", "v42", "v43", "v44", "v45", "v46", "v47", "v48", "v49", "v50", "v51", "v52", "v53", "v54", "v55", "v56", "v57", "v58", "v59", "v60", "v61", "v62", "v63", "v64", "v65", "v66", "v67", "v68", "v69", "v70", "v71", "v72", "v73", "v74", "v75", "v76", "v77", "v78", "v79", "v80", "v81", "v82", "v83", "v84", "v85", "v86", "v87", "vcc", "scc", "memory"
#define ASM_QK \
"s_nop 7\n" \
"s_nop 7\n" \
"v_add_u32 v36, 0x10000, v34\n" \
"v_add_u32 v38, 0x20000, v34\n" \
"v_add_u32 v37, 0x10000, v35\n" \
"v_add_u32 v39, 0x20000, v35\n" \
"s_lshl_b32 s62, s48, 12\n" \
"s_add_u32 m0, s62, 147456\n" \
"s_xor_b32 s54, s15, 524288\n" \
"buffer_load_dwordx4 v32, s[40:43], s54 offen nt lds\n" \
"s_add_u32 m0, s62, 148480\n" \
"s_xor_b32 s54, s15, 524288\n" \
"buffer_load_dwordx4 v33, s[40:43], s54 offen nt lds\n" \
"s_add_u32 m0, s62, 149504\n" \
"s_xor_b32 s54, s15, 655360\n" \
"buffer_load_dwordx4 v32, s[40:43], s54 offen nt lds\n" \
"s_add_u32 m0, s62, 150528\n" \
"s_xor_b32 s54, s15, 655360\n" \
"buffer_load_dwordx4 v33, s[40:43], s54 offen nt lds\n" \
"s_add_u32 m0, s62, 131072\n" \
"s_xor_b32 s54, s15, 786432\n" \
"buffer_load_dwordx4 v32, s[40:43], s54 offen nt lds\n" \
"s_add_u32 m0, s62, 132096\n" \
"s_xor_b32 s54, s15, 786432\n" \
"buffer_load_dwordx4 v33, s[40:43], s54 offen nt lds\n" \
"s_add_u32 m0, s62, 133120\n" \
"s_xor_b32 s54, s15, 917504\n" \
"buffer_load_dwordx4 v32, s[40:43], s54 offen nt lds\n" \
"s_add_u32 m0, s62, 134144\n" \
"s_xor_b32 s54, s15, 917504\n" \
"buffer_load_dwordx4 v33, s[40:43], s54 offen nt lds\n" \
"s_add_u32 m0, s62, 114688\n" \
"s_xor_b32 s54, s15, 1572864\n" \
"buffer_load_dwordx4 v32, s[40:43], s54 offen nt lds\n" \
"s_add_u32 m0, s62, 115712\n" \
"s_xor_b32 s54, s15, 1572864\n" \
"buffer_load_dwordx4 v33, s[40:43], s54 offen nt lds\n" \
"s_add_u32 m0, s62, 116736\n" \
"s_xor_b32 s54, s15, 1703936\n" \
"buffer_load_dwordx4 v32, s[40:43], s54 offen nt lds\n" \
"s_add_u32 m0, s62, 117760\n" \
"s_xor_b32 s54, s15, 1703936\n" \
"buffer_load_dwordx4 v33, s[40:43], s54 offen nt lds\n" \
"s_add_u32 m0, s62, 98304\n" \
"s_xor_b32 s54, s15, 1835008\n" \
"buffer_load_dwordx4 v32, s[40:43], s54 offen nt lds\n" \
"s_add_u32 m0, s62, 99328\n" \
"s_xor_b32 s54, s15, 1835008\n" \
"buffer_load_dwordx4 v33, s[40:43], s54 offen nt lds\n" \
"s_add_u32 m0, s62, 100352\n" \
"s_xor_b32 s54, s15, 1966080\n" \
"buffer_load_dwordx4 v32, s[40:43], s54 offen nt lds\n" \
"s_add_u32 m0, s62, 101376\n" \
"s_xor_b32 s54, s15, 1966080\n" \
"buffer_load_dwordx4 v33, s[40:43], s54 offen nt lds\n" \
"s_add_u32 m0, s62, 81920\n" \
"s_xor_b32 s54, s15, 2621440\n" \
"buffer_load_dwordx4 v32, s[40:43], s54 offen nt lds\n" \
"s_add_u32 m0, s62, 82944\n" \
"s_xor_b32 s54, s15, 2621440\n" \
"buffer_load_dwordx4 v33, s[40:43], s54 offen nt lds\n" \
"s_add_u32 m0, s62, 83968\n" \
"s_xor_b32 s54, s15, 2752512\n" \
"buffer_load_dwordx4 v32, s[40:43], s54 offen nt lds\n" \
"s_add_u32 m0, s62, 84992\n" \
"s_xor_b32 s54, s15, 2752512\n" \
"buffer_load_dwordx4 v33, s[40:43], s54 offen nt lds\n" \
"s_add_u32 m0, s62, 65536\n" \
"s_xor_b32 s54, s15, 2883584\n" \
"buffer_load_dwordx4 v32, s[40:43], s54 offen nt lds\n" \
"s_add_u32 m0, s62, 66560\n" \
"s_xor_b32 s54, s15, 2883584\n" \
"buffer_load_dwordx4 v33, s[40:43], s54 offen nt lds\n" \
"s_add_u32 m0, s62, 67584\n" \
"s_xor_b32 s54, s15, 3014656\n" \
"buffer_load_dwordx4 v32, s[40:43], s54 offen nt lds\n" \
"s_add_u32 m0, s62, 68608\n" \
"s_xor_b32 s54, s15, 3014656\n" \
"buffer_load_dwordx4 v33, s[40:43], s54 offen nt lds\n" \
"s_add_u32 m0, s62, 49152\n" \
"s_xor_b32 s54, s15, 3670016\n" \
"buffer_load_dwordx4 v32, s[40:43], s54 offen nt lds\n" \
"s_add_u32 m0, s62, 50176\n" \
"s_xor_b32 s54, s15, 3670016\n" \
"buffer_load_dwordx4 v33, s[40:43], s54 offen nt lds\n" \
"s_add_u32 m0, s62, 51200\n" \
"s_xor_b32 s54, s15, 3801088\n" \
"buffer_load_dwordx4 v32, s[40:43], s54 offen nt lds\n" \
"s_add_u32 m0, s62, 52224\n" \
"s_xor_b32 s54, s15, 3801088\n" \
"buffer_load_dwordx4 v33, s[40:43], s54 offen nt lds\n" \
"s_add_u32 m0, s62, 32768\n" \
"s_xor_b32 s54, s15, 3932160\n" \
"buffer_load_dwordx4 v32, s[40:43], s54 offen nt lds\n" \
"s_add_u32 m0, s62, 33792\n" \
"s_xor_b32 s54, s15, 3932160\n" \
"buffer_load_dwordx4 v33, s[40:43], s54 offen nt lds\n" \
"s_add_u32 m0, s62, 34816\n" \
"s_xor_b32 s54, s15, 4063232\n" \
"buffer_load_dwordx4 v32, s[40:43], s54 offen nt lds\n" \
"s_add_u32 m0, s62, 35840\n" \
"s_xor_b32 s54, s15, 4063232\n" \
"buffer_load_dwordx4 v33, s[40:43], s54 offen nt lds\n" \
"s_add_u32 m0, s62, 16384\n" \
"s_xor_b32 s54, s15, 4718592\n" \
"buffer_load_dwordx4 v32, s[40:43], s54 offen nt lds\n" \
"s_add_u32 m0, s62, 17408\n" \
"s_xor_b32 s54, s15, 4718592\n" \
"buffer_load_dwordx4 v33, s[40:43], s54 offen nt lds\n" \
"s_add_u32 m0, s62, 18432\n" \
"s_xor_b32 s54, s15, 4849664\n" \
"buffer_load_dwordx4 v32, s[40:43], s54 offen nt lds\n" \
"s_add_u32 m0, s62, 19456\n" \
"s_xor_b32 s54, s15, 4849664\n" \
"buffer_load_dwordx4 v33, s[40:43], s54 offen nt lds\n" \
"s_add_u32 m0, s62, 0\n" \
"s_xor_b32 s54, s15, 4980736\n" \
"buffer_load_dwordx4 v32, s[40:43], s54 offen nt lds\n" \
"s_add_u32 m0, s62, 1024\n" \
"s_xor_b32 s54, s15, 4980736\n" \
"buffer_load_dwordx4 v33, s[40:43], s54 offen nt lds\n" \
"s_add_u32 m0, s62, 2048\n" \
"s_xor_b32 s54, s15, 5111808\n" \
"buffer_load_dwordx4 v32, s[40:43], s54 offen nt lds\n" \
"s_add_u32 m0, s62, 3072\n" \
"s_xor_b32 s54, s15, 5111808\n" \
"buffer_load_dwordx4 v33, s[40:43], s54 offen nt lds\n" \
"s_waitcnt vmcnt(32)\n" \
"s_barrier\n" \
"ds_read_b64_tr_b16 a[16:17], v38 offset:16384\n" \
"ds_read_b64_tr_b16 a[18:19], v38 offset:16896\n" \
"ds_read_b64_tr_b16 a[24:25], v39 offset:16384\n" \
"ds_read_b64_tr_b16 a[26:27], v39 offset:16896\n" \
"ds_read_b64_tr_b16 v[112:113], v38 offset:24576\n" \
"ds_read_b64_tr_b16 v[114:115], v38 offset:25088\n" \
"ds_read_b64_tr_b16 v[120:121], v39 offset:24576\n" \
"ds_read_b64_tr_b16 v[122:123], v39 offset:25088\n" \
"ds_read_b64_tr_b16 a[20:21], v38 offset:0\n" \
"ds_read_b64_tr_b16 a[22:23], v38 offset:512\n" \
"ds_read_b64_tr_b16 a[28:29], v39 offset:0\n" \
"ds_read_b64_tr_b16 a[30:31], v39 offset:512\n" \
"ds_read_b64_tr_b16 v[116:117], v38 offset:8192\n" \
"ds_read_b64_tr_b16 v[118:119], v38 offset:8704\n" \
"ds_read_b64_tr_b16 v[124:125], v39 offset:8192\n" \
"ds_read_b64_tr_b16 v[126:127], v39 offset:8704\n" \
"s_waitcnt lgkmcnt(0)\n" \
"s_barrier\n" \
"s_add_u32 m0, s62, 147456\n" \
"s_xor_b32 s54, s15, 5767168\n" \
"buffer_load_dwordx4 v32, s[40:43], s54 offen nt lds\n" \
"s_add_u32 m0, s62, 148480\n" \
"s_xor_b32 s54, s15, 5767168\n" \
"buffer_load_dwordx4 v33, s[40:43], s54 offen nt lds\n" \
"s_add_u32 m0, s62, 149504\n" \
"s_xor_b32 s54, s15, 5898240\n" \
"buffer_load_dwordx4 v32, s[40:43], s54 offen nt lds\n" \
"s_add_u32 m0, s62, 150528\n" \
"s_xor_b32 s54, s15, 5898240\n" \
"buffer_load_dwordx4 v33, s[40:43], s54 offen nt lds\n" \
"s_add_u32 m0, s62, 131072\n" \
"s_xor_b32 s54, s15, 6029312\n" \
"buffer_load_dwordx4 v32, s[40:43], s54 offen nt lds\n" \
"s_add_u32 m0, s62, 132096\n" \
"s_xor_b32 s54, s15, 6029312\n" \
"buffer_load_dwordx4 v33, s[40:43], s54 offen nt lds\n" \
"s_add_u32 m0, s62, 133120\n" \
"s_xor_b32 s54, s15, 6160384\n" \
"buffer_load_dwordx4 v32, s[40:43], s54 offen nt lds\n" \
"s_add_u32 m0, s62, 134144\n" \
"s_xor_b32 s54, s15, 6160384\n" \
"buffer_load_dwordx4 v33, s[40:43], s54 offen nt lds\n" \
"s_waitcnt vmcnt(32)\n" \
"s_barrier\n" \
"ds_read_b64_tr_b16 a[48:49], v36 offset:49152\n" \
"ds_read_b64_tr_b16 a[50:51], v36 offset:49664\n" \
"ds_read_b64_tr_b16 a[56:57], v37 offset:49152\n" \
"ds_read_b64_tr_b16 a[58:59], v37 offset:49664\n" \
"ds_read_b64_tr_b16 v[128:129], v36 offset:57344\n" \
"ds_read_b64_tr_b16 v[130:131], v36 offset:57856\n" \
"ds_read_b64_tr_b16 v[136:137], v37 offset:57344\n" \
"ds_read_b64_tr_b16 v[138:139], v37 offset:57856\n" \
"ds_read_b64_tr_b16 a[52:53], v36 offset:32768\n" \
"ds_read_b64_tr_b16 a[54:55], v36 offset:33280\n" \
"ds_read_b64_tr_b16 a[60:61], v37 offset:32768\n" \
"ds_read_b64_tr_b16 a[62:63], v37 offset:33280\n" \
"ds_read_b64_tr_b16 v[132:133], v36 offset:40960\n" \
"ds_read_b64_tr_b16 v[134:135], v36 offset:41472\n" \
"ds_read_b64_tr_b16 v[140:141], v37 offset:40960\n" \
"ds_read_b64_tr_b16 v[142:143], v37 offset:41472\n" \
"s_waitcnt lgkmcnt(0)\n" \
"s_barrier\n" \
"s_add_u32 m0, s62, 114688\n" \
"s_xor_b32 s54, s15, 6815744\n" \
"buffer_load_dwordx4 v32, s[40:43], s54 offen nt lds\n" \
"s_add_u32 m0, s62, 115712\n" \
"s_xor_b32 s54, s15, 6815744\n" \
"buffer_load_dwordx4 v33, s[40:43], s54 offen nt lds\n" \
"s_add_u32 m0, s62, 116736\n" \
"s_xor_b32 s54, s15, 6946816\n" \
"buffer_load_dwordx4 v32, s[40:43], s54 offen nt lds\n" \
"s_add_u32 m0, s62, 117760\n" \
"s_xor_b32 s54, s15, 6946816\n" \
"buffer_load_dwordx4 v33, s[40:43], s54 offen nt lds\n" \
"s_add_u32 m0, s62, 98304\n" \
"s_xor_b32 s54, s15, 7077888\n" \
"buffer_load_dwordx4 v32, s[40:43], s54 offen nt lds\n" \
"s_add_u32 m0, s62, 99328\n" \
"s_xor_b32 s54, s15, 7077888\n" \
"buffer_load_dwordx4 v33, s[40:43], s54 offen nt lds\n" \
"s_add_u32 m0, s62, 100352\n" \
"s_xor_b32 s54, s15, 7208960\n" \
"buffer_load_dwordx4 v32, s[40:43], s54 offen nt lds\n" \
"s_add_u32 m0, s62, 101376\n" \
"s_xor_b32 s54, s15, 7208960\n" \
"buffer_load_dwordx4 v33, s[40:43], s54 offen nt lds\n" \
"s_waitcnt vmcnt(32)\n" \
"s_barrier\n" \
"ds_read_b64_tr_b16 a[80:81], v36 offset:16384\n" \
"ds_read_b64_tr_b16 a[82:83], v36 offset:16896\n" \
"ds_read_b64_tr_b16 a[88:89], v37 offset:16384\n" \
"ds_read_b64_tr_b16 a[90:91], v37 offset:16896\n" \
"ds_read_b64_tr_b16 v[144:145], v36 offset:24576\n" \
"ds_read_b64_tr_b16 v[146:147], v36 offset:25088\n" \
"ds_read_b64_tr_b16 v[152:153], v37 offset:24576\n" \
"ds_read_b64_tr_b16 v[154:155], v37 offset:25088\n" \
"ds_read_b64_tr_b16 a[84:85], v36 offset:0\n" \
"ds_read_b64_tr_b16 a[86:87], v36 offset:512\n" \
"ds_read_b64_tr_b16 a[92:93], v37 offset:0\n" \
"ds_read_b64_tr_b16 a[94:95], v37 offset:512\n" \
"ds_read_b64_tr_b16 v[148:149], v36 offset:8192\n" \
"ds_read_b64_tr_b16 v[150:151], v36 offset:8704\n" \
"ds_read_b64_tr_b16 v[156:157], v37 offset:8192\n" \
"ds_read_b64_tr_b16 v[158:159], v37 offset:8704\n" \
"s_waitcnt lgkmcnt(0)\n" \
"s_barrier\n" \
"s_add_u32 m0, s62, 81920\n" \
"s_xor_b32 s54, s15, 7340032\n" \
"buffer_load_dwordx4 v32, s[40:43], s54 offen nt lds\n" \
"s_add_u32 m0, s62, 82944\n" \
"s_xor_b32 s54, s15, 7340032\n" \
"buffer_load_dwordx4 v33, s[40:43], s54 offen nt lds\n" \
"s_add_u32 m0, s62, 83968\n" \
"s_xor_b32 s54, s15, 7471104\n" \
"buffer_load_dwordx4 v32, s[40:43], s54 offen nt lds\n" \
"s_add_u32 m0, s62, 84992\n" \
"s_xor_b32 s54, s15, 7471104\n" \
"buffer_load_dwordx4 v33, s[40:43], s54 offen nt lds\n" \
"s_add_u32 m0, s62, 65536\n" \
"s_xor_b32 s54, s15, 7602176\n" \
"buffer_load_dwordx4 v32, s[40:43], s54 offen nt lds\n" \
"s_add_u32 m0, s62, 66560\n" \
"s_xor_b32 s54, s15, 7602176\n" \
"buffer_load_dwordx4 v33, s[40:43], s54 offen nt lds\n" \
"s_add_u32 m0, s62, 67584\n" \
"s_xor_b32 s54, s15, 7733248\n" \
"buffer_load_dwordx4 v32, s[40:43], s54 offen nt lds\n" \
"s_add_u32 m0, s62, 68608\n" \
"s_xor_b32 s54, s15, 7733248\n" \
"buffer_load_dwordx4 v33, s[40:43], s54 offen nt lds\n" \
"s_waitcnt vmcnt(32)\n" \
"s_barrier\n" \
"ds_read_b64_tr_b16 a[112:113], v34 offset:49152\n" \
"ds_read_b64_tr_b16 a[114:115], v34 offset:49664\n" \
"ds_read_b64_tr_b16 a[120:121], v35 offset:49152\n" \
"ds_read_b64_tr_b16 a[122:123], v35 offset:49664\n" \
"ds_read_b64_tr_b16 v[160:161], v34 offset:57344\n" \
"ds_read_b64_tr_b16 v[162:163], v34 offset:57856\n" \
"ds_read_b64_tr_b16 v[168:169], v35 offset:57344\n" \
"ds_read_b64_tr_b16 v[170:171], v35 offset:57856\n" \
"ds_read_b64_tr_b16 a[116:117], v34 offset:32768\n" \
"ds_read_b64_tr_b16 a[118:119], v34 offset:33280\n" \
"ds_read_b64_tr_b16 a[124:125], v35 offset:32768\n" \
"ds_read_b64_tr_b16 a[126:127], v35 offset:33280\n" \
"ds_read_b64_tr_b16 v[164:165], v34 offset:40960\n" \
"ds_read_b64_tr_b16 v[166:167], v34 offset:41472\n" \
"ds_read_b64_tr_b16 v[172:173], v35 offset:40960\n" \
"ds_read_b64_tr_b16 v[174:175], v35 offset:41472\n" \
"s_waitcnt lgkmcnt(0)\n" \
"s_barrier\n" \
"s_add_u32 m0, s62, 49152\n" \
"s_xor_b32 s54, s15, 7864320\n" \
"buffer_load_dwordx4 v32, s[40:43], s54 offen nt lds\n" \
"s_add_u32 m0, s62, 50176\n" \
"s_xor_b32 s54, s15, 7864320\n" \
"buffer_load_dwordx4 v33, s[40:43], s54 offen nt lds\n" \
"s_add_u32 m0, s62, 51200\n" \
"s_xor_b32 s54, s15, 7995392\n" \
"buffer_load_dwordx4 v32, s[40:43], s54 offen nt lds\n" \
"s_add_u32 m0, s62, 52224\n" \
"s_xor_b32 s54, s15, 7995392\n" \
"buffer_load_dwordx4 v33, s[40:43], s54 offen nt lds\n" \
"s_add_u32 m0, s62, 32768\n" \
"s_xor_b32 s54, s15, 8126464\n" \
"buffer_load_dwordx4 v32, s[40:43], s54 offen nt lds\n" \
"s_add_u32 m0, s62, 33792\n" \
"s_xor_b32 s54, s15, 8126464\n" \
"buffer_load_dwordx4 v33, s[40:43], s54 offen nt lds\n" \
"s_add_u32 m0, s62, 34816\n" \
"s_xor_b32 s54, s15, 8257536\n" \
"buffer_load_dwordx4 v32, s[40:43], s54 offen nt lds\n" \
"s_add_u32 m0, s62, 35840\n" \
"s_xor_b32 s54, s15, 8257536\n" \
"buffer_load_dwordx4 v33, s[40:43], s54 offen nt lds\n" \
"s_waitcnt vmcnt(32)\n" \
"s_barrier\n" \
"ds_read_b64_tr_b16 a[144:145], v34 offset:16384\n" \
"ds_read_b64_tr_b16 a[146:147], v34 offset:16896\n" \
"ds_read_b64_tr_b16 a[152:153], v35 offset:16384\n" \
"ds_read_b64_tr_b16 a[154:155], v35 offset:16896\n" \
"ds_read_b64_tr_b16 v[176:177], v34 offset:24576\n" \
"ds_read_b64_tr_b16 v[178:179], v34 offset:25088\n" \
"ds_read_b64_tr_b16 v[184:185], v35 offset:24576\n" \
"ds_read_b64_tr_b16 v[186:187], v35 offset:25088\n" \
"ds_read_b64_tr_b16 a[148:149], v34 offset:0\n" \
"ds_read_b64_tr_b16 a[150:151], v34 offset:512\n" \
"ds_read_b64_tr_b16 a[156:157], v35 offset:0\n" \
"ds_read_b64_tr_b16 a[158:159], v35 offset:512\n" \
"ds_read_b64_tr_b16 v[180:181], v34 offset:8192\n" \
"ds_read_b64_tr_b16 v[182:183], v34 offset:8704\n" \
"ds_read_b64_tr_b16 v[188:189], v35 offset:8192\n" \
"ds_read_b64_tr_b16 v[190:191], v35 offset:8704\n" \
"s_waitcnt lgkmcnt(0)\n" \
"s_barrier\n" \
"s_add_u32 m0, s62, 0\n" \
"s_xor_b32 s54, s15, 0\n" \
"buffer_load_dwordx4 v32, s[40:43], s54 offen nt lds\n" \
"s_add_u32 m0, s62, 1024\n" \
"s_xor_b32 s54, s15, 0\n" \
"buffer_load_dwordx4 v33, s[40:43], s54 offen nt lds\n" \
"s_add_u32 m0, s62, 2048\n" \
"s_xor_b32 s54, s15, 131072\n" \
"buffer_load_dwordx4 v32, s[40:43], s54 offen nt lds\n" \
"s_add_u32 m0, s62, 3072\n" \
"s_xor_b32 s54, s15, 131072\n" \
"buffer_load_dwordx4 v33, s[40:43], s54 offen nt lds\n" \
"s_add_u32 m0, s62, 16384\n" \
"s_xor_b32 s54, s15, 262144\n" \
"buffer_load_dwordx4 v32, s[40:43], s54 offen nt lds\n" \
"s_add_u32 m0, s62, 17408\n" \
"s_xor_b32 s54, s15, 262144\n" \
"buffer_load_dwordx4 v33, s[40:43], s54 offen nt lds\n" \
"s_add_u32 m0, s62, 18432\n" \
"s_xor_b32 s54, s15, 393216\n" \
"buffer_load_dwordx4 v32, s[40:43], s54 offen nt lds\n" \
"s_add_u32 m0, s62, 19456\n" \
"s_xor_b32 s54, s15, 393216\n" \
"buffer_load_dwordx4 v33, s[40:43], s54 offen nt lds\n" \
"s_waitcnt vmcnt(32)\n" \
"s_barrier\n" \
"ds_read_b64_tr_b16 a[176:177], v38 offset:16384\n" \
"ds_read_b64_tr_b16 a[178:179], v38 offset:16896\n" \
"ds_read_b64_tr_b16 a[184:185], v39 offset:16384\n" \
"ds_read_b64_tr_b16 a[186:187], v39 offset:16896\n" \
"ds_read_b64_tr_b16 v[192:193], v38 offset:24576\n" \
"ds_read_b64_tr_b16 v[194:195], v38 offset:25088\n" \
"ds_read_b64_tr_b16 v[200:201], v39 offset:24576\n" \
"ds_read_b64_tr_b16 v[202:203], v39 offset:25088\n" \
"ds_read_b64_tr_b16 a[180:181], v38 offset:0\n" \
"ds_read_b64_tr_b16 a[182:183], v38 offset:512\n" \
"ds_read_b64_tr_b16 a[188:189], v39 offset:0\n" \
"ds_read_b64_tr_b16 a[190:191], v39 offset:512\n" \
"ds_read_b64_tr_b16 v[196:197], v38 offset:8192\n" \
"ds_read_b64_tr_b16 v[198:199], v38 offset:8704\n" \
"ds_read_b64_tr_b16 v[204:205], v39 offset:8192\n" \
"ds_read_b64_tr_b16 v[206:207], v39 offset:8704\n" \
"s_waitcnt lgkmcnt(0)\n" \
"s_barrier\n" \
"s_waitcnt vmcnt(24)\n" \
"s_barrier\n" \
"ds_read_b64_tr_b16 a[208:209], v36 offset:49152\n" \
"ds_read_b64_tr_b16 a[210:211], v36 offset:49664\n" \
"ds_read_b64_tr_b16 a[216:217], v37 offset:49152\n" \
"ds_read_b64_tr_b16 a[218:219], v37 offset:49664\n" \
"ds_read_b64_tr_b16 v[208:209], v36 offset:57344\n" \
"ds_read_b64_tr_b16 v[210:211], v36 offset:57856\n" \
"ds_read_b64_tr_b16 v[216:217], v37 offset:57344\n" \
"ds_read_b64_tr_b16 v[218:219], v37 offset:57856\n" \
"ds_read_b64_tr_b16 a[212:213], v36 offset:32768\n" \
"ds_read_b64_tr_b16 a[214:215], v36 offset:33280\n" \
"ds_read_b64_tr_b16 a[220:221], v37 offset:32768\n" \
"ds_read_b64_tr_b16 a[222:223], v37 offset:33280\n" \
"ds_read_b64_tr_b16 v[212:213], v36 offset:40960\n" \
"ds_read_b64_tr_b16 v[214:215], v36 offset:41472\n" \
"ds_read_b64_tr_b16 v[220:221], v37 offset:40960\n" \
"ds_read_b64_tr_b16 v[222:223], v37 offset:41472\n" \
"s_waitcnt lgkmcnt(0)\n" \
"s_barrier\n" \
"s_add_u32 m0, s62, 98304\n" \
"s_xor_b32 s54, s15, 1048576\n" \
"buffer_load_dwordx4 v32, s[40:43], s54 offen nt lds\n" \
"s_add_u32 m0, s62, 99328\n" \
"s_xor_b32 s54, s15, 1048576\n" \
"buffer_load_dwordx4 v33, s[40:43], s54 offen nt lds\n" \
"s_add_u32 m0, s62, 100352\n" \
"s_xor_b32 s54, s15, 1179648\n" \
"buffer_load_dwordx4 v32, s[40:43], s54 offen nt lds\n" \
"s_add_u32 m0, s62, 101376\n" \
"s_xor_b32 s54, s15, 1179648\n" \
"buffer_load_dwordx4 v33, s[40:43], s54 offen nt lds\n" \
"s_add_u32 m0, s62, 147456\n" \
"s_xor_b32 s54, s15, 1310720\n" \
"buffer_load_dwordx4 v32, s[40:43], s54 offen nt lds\n" \
"s_add_u32 m0, s62, 148480\n" \
"s_xor_b32 s54, s15, 1310720\n" \
"buffer_load_dwordx4 v33, s[40:43], s54 offen nt lds\n" \
"s_add_u32 m0, s62, 149504\n" \
"s_xor_b32 s54, s15, 1441792\n" \
"buffer_load_dwordx4 v32, s[40:43], s54 offen nt lds\n" \
"s_add_u32 m0, s62, 150528\n" \
"s_xor_b32 s54, s15, 1441792\n" \
"buffer_load_dwordx4 v33, s[40:43], s54 offen nt lds\n" \
"s_waitcnt vmcnt(24)\n" \
"s_barrier\n" \
"ds_read_b64_tr_b16 a[224:225], v36 offset:16384\n" \
"ds_read_b64_tr_b16 a[226:227], v36 offset:16896\n" \
"ds_read_b64_tr_b16 a[232:233], v37 offset:16384\n" \
"ds_read_b64_tr_b16 a[234:235], v37 offset:16896\n" \
"ds_read_b64_tr_b16 v[224:225], v36 offset:24576\n" \
"ds_read_b64_tr_b16 v[226:227], v36 offset:25088\n" \
"ds_read_b64_tr_b16 v[232:233], v37 offset:24576\n" \
"ds_read_b64_tr_b16 v[234:235], v37 offset:25088\n" \
"ds_read_b64_tr_b16 a[228:229], v36 offset:0\n" \
"ds_read_b64_tr_b16 a[230:231], v36 offset:512\n" \
"ds_read_b64_tr_b16 a[236:237], v37 offset:0\n" \
"ds_read_b64_tr_b16 a[238:239], v37 offset:512\n" \
"ds_read_b64_tr_b16 v[228:229], v36 offset:8192\n" \
"ds_read_b64_tr_b16 v[230:231], v36 offset:8704\n" \
"ds_read_b64_tr_b16 v[236:237], v37 offset:8192\n" \
"ds_read_b64_tr_b16 v[238:239], v37 offset:8704\n" \
"s_waitcnt lgkmcnt(0)\n" \
"s_barrier\n" \
"s_add_u32 m0, s62, 65536\n" \
"s_xor_b32 s54, s15, 2097152\n" \
"buffer_load_dwordx4 v32, s[40:43], s54 offen nt lds\n" \
"s_add_u32 m0, s62, 66560\n" \
"s_xor_b32 s54, s15, 2097152\n" \
"buffer_load_dwordx4 v33, s[40:43], s54 offen nt lds\n" \
"s_add_u32 m0, s62, 67584\n" \
"s_xor_b32 s54, s15, 2228224\n" \
"buffer_load_dwordx4 v32, s[40:43], s54 offen nt lds\n" \
"s_add_u32 m0, s62, 68608\n" \
"s_xor_b32 s54, s15, 2228224\n" \
"buffer_load_dwordx4 v33, s[40:43], s54 offen nt lds\n" \
"s_add_u32 m0, s62, 131072\n" \
"s_xor_b32 s54, s15, 2359296\n" \
"buffer_load_dwordx4 v32, s[40:43], s54 offen nt lds\n" \
"s_add_u32 m0, s62, 132096\n" \
"s_xor_b32 s54, s15, 2359296\n" \
"buffer_load_dwordx4 v33, s[40:43], s54 offen nt lds\n" \
"s_add_u32 m0, s62, 133120\n" \
"s_xor_b32 s54, s15, 2490368\n" \
"buffer_load_dwordx4 v32, s[40:43], s54 offen nt lds\n" \
"s_add_u32 m0, s62, 134144\n" \
"s_xor_b32 s54, s15, 2490368\n" \
"buffer_load_dwordx4 v33, s[40:43], s54 offen nt lds\n" \
"s_add_u32 m0, s62, 81920\n" \
"s_xor_b32 s54, s15, 3145728\n" \
"buffer_load_dwordx4 v32, s[40:43], s54 offen nt lds\n" \
"s_add_u32 m0, s62, 82944\n" \
"s_xor_b32 s54, s15, 3145728\n" \
"buffer_load_dwordx4 v33, s[40:43], s54 offen nt lds\n" \
"s_add_u32 m0, s62, 83968\n" \
"s_xor_b32 s54, s15, 3276800\n" \
"buffer_load_dwordx4 v32, s[40:43], s54 offen nt lds\n" \
"s_add_u32 m0, s62, 84992\n" \
"s_xor_b32 s54, s15, 3276800\n" \
"buffer_load_dwordx4 v33, s[40:43], s54 offen nt lds\n" \
"s_add_u32 m0, s62, 114688\n" \
"s_xor_b32 s54, s15, 3407872\n" \
"buffer_load_dwordx4 v32, s[40:43], s54 offen nt lds\n" \
"s_add_u32 m0, s62, 115712\n" \
"s_xor_b32 s54, s15, 3407872\n" \
"buffer_load_dwordx4 v33, s[40:43], s54 offen nt lds\n" \
"s_add_u32 m0, s62, 116736\n" \
"s_xor_b32 s54, s15, 3538944\n" \
"buffer_load_dwordx4 v32, s[40:43], s54 offen nt lds\n" \
"s_add_u32 m0, s62, 117760\n" \
"s_xor_b32 s54, s15, 3538944\n" \
"buffer_load_dwordx4 v33, s[40:43], s54 offen nt lds\n" \
"s_waitcnt vmcnt(32)\n" \
"s_barrier\n" \
"ds_read_b64_tr_b16 a[240:241], v34 offset:49152\n" \
"ds_read_b64_tr_b16 a[242:243], v34 offset:49664\n" \
"ds_read_b64_tr_b16 a[248:249], v35 offset:49152\n" \
"ds_read_b64_tr_b16 a[250:251], v35 offset:49664\n" \
"ds_read_b64_tr_b16 v[240:241], v34 offset:57344\n" \
"ds_read_b64_tr_b16 v[242:243], v34 offset:57856\n" \
"ds_read_b64_tr_b16 v[248:249], v35 offset:57344\n" \
"ds_read_b64_tr_b16 v[250:251], v35 offset:57856\n" \
"ds_read_b64_tr_b16 a[244:245], v34 offset:32768\n" \
"ds_read_b64_tr_b16 a[246:247], v34 offset:33280\n" \
"ds_read_b64_tr_b16 a[252:253], v35 offset:32768\n" \
"ds_read_b64_tr_b16 a[254:255], v35 offset:33280\n" \
"ds_read_b64_tr_b16 v[244:245], v34 offset:40960\n" \
"ds_read_b64_tr_b16 v[246:247], v34 offset:41472\n" \
"ds_read_b64_tr_b16 v[252:253], v35 offset:40960\n" \
"ds_read_b64_tr_b16 v[254:255], v35 offset:41472\n" \
"s_waitcnt lgkmcnt(0)\n" \
"s_barrier\n" \
"s_add_u32 m0, s62, 32768\n" \
"s_xor_b32 s54, s15, 4194304\n" \
"buffer_load_dwordx4 v32, s[40:43], s54 offen nt lds\n" \
"s_add_u32 m0, s62, 33792\n" \
"s_xor_b32 s54, s15, 4194304\n" \
"buffer_load_dwordx4 v33, s[40:43], s54 offen nt lds\n" \
"s_add_u32 m0, s62, 34816\n" \
"s_xor_b32 s54, s15, 4325376\n" \
"buffer_load_dwordx4 v32, s[40:43], s54 offen nt lds\n" \
"s_add_u32 m0, s62, 35840\n" \
"s_xor_b32 s54, s15, 4325376\n" \
"buffer_load_dwordx4 v33, s[40:43], s54 offen nt lds\n" \
"s_add_u32 m0, s62, 49152\n" \
"s_xor_b32 s54, s15, 4456448\n" \
"buffer_load_dwordx4 v32, s[40:43], s54 offen nt lds\n" \
"s_add_u32 m0, s62, 50176\n" \
"s_xor_b32 s54, s15, 4456448\n" \
"buffer_load_dwordx4 v33, s[40:43], s54 offen nt lds\n" \
"s_add_u32 m0, s62, 51200\n" \
"s_xor_b32 s54, s15, 4587520\n" \
"buffer_load_dwordx4 v32, s[40:43], s54 offen nt lds\n" \
"s_add_u32 m0, s62, 52224\n" \
"s_xor_b32 s54, s15, 4587520\n" \
"buffer_load_dwordx4 v33, s[40:43], s54 offen nt lds\n" \
"s_waitcnt vmcnt(32)\n" \
"s_barrier\n" \
"ds_read_b64_tr_b16 a[0:1], v34 offset:0\n" \
"ds_read_b64_tr_b16 a[2:3], v34 offset:512\n" \
"ds_read_b64_tr_b16 a[8:9], v35 offset:0\n" \
"ds_read_b64_tr_b16 a[10:11], v35 offset:512\n" \
"ds_read_b64_tr_b16 v[48:49], v34 offset:8192\n" \
"ds_read_b64_tr_b16 v[50:51], v34 offset:8704\n" \
"ds_read_b64_tr_b16 v[56:57], v35 offset:8192\n" \
"ds_read_b64_tr_b16 v[58:59], v35 offset:8704\n" \
"ds_read_b64_tr_b16 a[4:5], v34 offset:16384\n" \
"ds_read_b64_tr_b16 a[6:7], v34 offset:16896\n" \
"ds_read_b64_tr_b16 a[12:13], v35 offset:16384\n" \
"ds_read_b64_tr_b16 a[14:15], v35 offset:16896\n" \
"ds_read_b64_tr_b16 v[52:53], v34 offset:24576\n" \
"ds_read_b64_tr_b16 v[54:55], v34 offset:25088\n" \
"ds_read_b64_tr_b16 v[60:61], v35 offset:24576\n" \
"ds_read_b64_tr_b16 v[62:63], v35 offset:25088\n" \
"s_waitcnt lgkmcnt(0)\n" \
"s_barrier\n" \
"ds_write_b128 v1, v[48:51] offset:0\n" \
"ds_write_b128 v1, v[52:55] offset:1024\n" \
"ds_write_b128 v1, v[56:59] offset:2048\n" \
"ds_write_b128 v1, v[60:63] offset:3072\n" \
"s_add_u32 m0, s62, 16384\n" \
"s_xor_b32 s54, s15, 5242880\n" \
"buffer_load_dwordx4 v32, s[40:43], s54 offen nt lds\n" \
"s_add_u32 m0, s62, 17408\n" \
"s_xor_b32 s54, s15, 5242880\n" \
"buffer_load_dwordx4 v33, s[40:43], s54 offen nt lds\n" \
"s_add_u32 m0, s62, 18432\n" \
"s_xor_b32 s54, s15, 5373952\n" \
"buffer_load_dwordx4 v32, s[40:43], s54 offen nt lds\n" \
"s_add_u32 m0, s62, 19456\n" \
"s_xor_b32 s54, s15, 5373952\n" \
"buffer_load_dwordx4 v33, s[40:43], s54 offen nt lds\n" \
"s_waitcnt vmcnt(28)\n" \
"s_barrier\n" \
"ds_read_b64_tr_b16 a[32:33], v36 offset:32768\n" \
"ds_read_b64_tr_b16 a[34:35], v36 offset:33280\n" \
"ds_read_b64_tr_b16 a[40:41], v37 offset:32768\n" \
"ds_read_b64_tr_b16 a[42:43], v37 offset:33280\n" \
"ds_read_b64_tr_b16 v[48:49], v36 offset:40960\n" \
"ds_read_b64_tr_b16 v[50:51], v36 offset:41472\n" \
"ds_read_b64_tr_b16 v[56:57], v37 offset:40960\n" \
"ds_read_b64_tr_b16 v[58:59], v37 offset:41472\n" \
"ds_read_b64_tr_b16 a[36:37], v38 offset:16384\n" \
"ds_read_b64_tr_b16 a[38:39], v38 offset:16896\n" \
"ds_read_b64_tr_b16 a[44:45], v39 offset:16384\n" \
"ds_read_b64_tr_b16 a[46:47], v39 offset:16896\n" \
"ds_read_b64_tr_b16 v[52:53], v38 offset:24576\n" \
"ds_read_b64_tr_b16 v[54:55], v38 offset:25088\n" \
"ds_read_b64_tr_b16 v[60:61], v39 offset:24576\n" \
"ds_read_b64_tr_b16 v[62:63], v39 offset:25088\n" \
"s_waitcnt lgkmcnt(0)\n" \
"s_barrier\n" \
"ds_write_b128 v2, v[48:51] offset:32768\n" \
"ds_write_b128 v2, v[52:55] offset:33792\n" \
"ds_write_b128 v2, v[56:59] offset:34816\n" \
"ds_write_b128 v2, v[60:63] offset:35840\n" \
"s_add_u32 m0, s62, 147456\n" \
"s_xor_b32 s54, s15, 5505024\n" \
"buffer_load_dwordx4 v32, s[40:43], s54 offen nt lds\n" \
"s_add_u32 m0, s62, 148480\n" \
"s_xor_b32 s54, s15, 5505024\n" \
"buffer_load_dwordx4 v33, s[40:43], s54 offen nt lds\n" \
"s_add_u32 m0, s62, 149504\n" \
"s_xor_b32 s54, s15, 5636096\n" \
"buffer_load_dwordx4 v32, s[40:43], s54 offen nt lds\n" \
"s_add_u32 m0, s62, 150528\n" \
"s_xor_b32 s54, s15, 5636096\n" \
"buffer_load_dwordx4 v33, s[40:43], s54 offen nt lds\n" \
"s_waitcnt vmcnt(24)\n" \
"s_barrier\n" \
"ds_read_b64_tr_b16 a[64:65], v36 offset:0\n" \
"ds_read_b64_tr_b16 a[66:67], v36 offset:512\n" \
"ds_read_b64_tr_b16 a[72:73], v37 offset:0\n" \
"ds_read_b64_tr_b16 a[74:75], v37 offset:512\n" \
"ds_read_b64_tr_b16 v[48:49], v36 offset:8192\n" \
"ds_read_b64_tr_b16 v[50:51], v36 offset:8704\n" \
"ds_read_b64_tr_b16 v[56:57], v37 offset:8192\n" \
"ds_read_b64_tr_b16 v[58:59], v37 offset:8704\n" \
"ds_read_b64_tr_b16 a[68:69], v38 offset:0\n" \
"ds_read_b64_tr_b16 a[70:71], v38 offset:512\n" \
"ds_read_b64_tr_b16 a[76:77], v39 offset:0\n" \
"ds_read_b64_tr_b16 a[78:79], v39 offset:512\n" \
"ds_read_b64_tr_b16 v[52:53], v38 offset:8192\n" \
"ds_read_b64_tr_b16 v[54:55], v38 offset:8704\n" \
"ds_read_b64_tr_b16 v[60:61], v39 offset:8192\n" \
"ds_read_b64_tr_b16 v[62:63], v39 offset:8704\n" \
"s_waitcnt lgkmcnt(0)\n" \
"s_barrier\n" \
"ds_write_b128 v2, v[48:51] offset:0\n" \
"ds_write_b128 v2, v[52:55] offset:1024\n" \
"ds_write_b128 v2, v[56:59] offset:2048\n" \
"ds_write_b128 v2, v[60:63] offset:3072\n" \
"s_waitcnt vmcnt(16)\n" \
"s_barrier\n" \
"ds_read_b64_tr_b16 a[96:97], v36 offset:16384\n" \
"ds_read_b64_tr_b16 a[98:99], v36 offset:16896\n" \
"ds_read_b64_tr_b16 a[104:105], v37 offset:16384\n" \
"ds_read_b64_tr_b16 a[106:107], v37 offset:16896\n" \
"ds_read_b64_tr_b16 v[48:49], v36 offset:24576\n" \
"ds_read_b64_tr_b16 v[50:51], v36 offset:25088\n" \
"ds_read_b64_tr_b16 v[56:57], v37 offset:24576\n" \
"ds_read_b64_tr_b16 v[58:59], v37 offset:25088\n" \
"ds_read_b64_tr_b16 a[100:101], v36 offset:49152\n" \
"ds_read_b64_tr_b16 a[102:103], v36 offset:49664\n" \
"ds_read_b64_tr_b16 a[108:109], v37 offset:49152\n" \
"ds_read_b64_tr_b16 a[110:111], v37 offset:49664\n" \
"ds_read_b64_tr_b16 v[52:53], v36 offset:57344\n" \
"ds_read_b64_tr_b16 v[54:55], v36 offset:57856\n" \
"ds_read_b64_tr_b16 v[60:61], v37 offset:57344\n" \
"ds_read_b64_tr_b16 v[62:63], v37 offset:57856\n" \
"s_waitcnt lgkmcnt(0)\n" \
"s_barrier\n" \
"ds_write_b128 v2, v[48:51] offset:16384\n" \
"ds_write_b128 v2, v[52:55] offset:17408\n" \
"ds_write_b128 v2, v[56:59] offset:18432\n" \
"ds_write_b128 v2, v[60:63] offset:19456\n" \
"s_waitcnt vmcnt(8)\n" \
"s_barrier\n" \
"ds_read_b64_tr_b16 a[128:129], v34 offset:32768\n" \
"ds_read_b64_tr_b16 a[130:131], v34 offset:33280\n" \
"ds_read_b64_tr_b16 a[136:137], v35 offset:32768\n" \
"ds_read_b64_tr_b16 a[138:139], v35 offset:33280\n" \
"ds_read_b64_tr_b16 v[48:49], v34 offset:40960\n" \
"ds_read_b64_tr_b16 v[50:51], v34 offset:41472\n" \
"ds_read_b64_tr_b16 v[56:57], v35 offset:40960\n" \
"ds_read_b64_tr_b16 v[58:59], v35 offset:41472\n" \
"ds_read_b64_tr_b16 a[132:133], v34 offset:49152\n" \
"ds_read_b64_tr_b16 a[134:135], v34 offset:49664\n" \
"ds_read_b64_tr_b16 a[140:141], v35 offset:49152\n" \
"ds_read_b64_tr_b16 a[142:143], v35 offset:49664\n" \
"ds_read_b64_tr_b16 v[52:53], v34 offset:57344\n" \
"ds_read_b64_tr_b16 v[54:55], v34 offset:57856\n" \
"ds_read_b64_tr_b16 v[60:61], v35 offset:57344\n" \
"ds_read_b64_tr_b16 v[62:63], v35 offset:57856\n" \
"s_waitcnt lgkmcnt(0)\n" \
"s_barrier\n" \
"ds_write_b128 v1, v[48:51] offset:32768\n" \
"ds_write_b128 v1, v[52:55] offset:33792\n" \
"ds_write_b128 v1, v[56:59] offset:34816\n" \
"ds_write_b128 v1, v[60:63] offset:35840\n" \
"s_add_u32 m0, s62, 49152\n" \
"s_xor_b32 s54, s15, 6291456\n" \
"buffer_load_dwordx4 v32, s[40:43], s54 offen nt lds\n" \
"s_add_u32 m0, s62, 50176\n" \
"s_xor_b32 s54, s15, 6291456\n" \
"buffer_load_dwordx4 v33, s[40:43], s54 offen nt lds\n" \
"s_add_u32 m0, s62, 51200\n" \
"s_xor_b32 s54, s15, 6422528\n" \
"buffer_load_dwordx4 v32, s[40:43], s54 offen nt lds\n" \
"s_add_u32 m0, s62, 52224\n" \
"s_xor_b32 s54, s15, 6422528\n" \
"buffer_load_dwordx4 v33, s[40:43], s54 offen nt lds\n" \
"s_add_u32 m0, s62, 131072\n" \
"s_xor_b32 s54, s15, 6553600\n" \
"buffer_load_dwordx4 v32, s[40:43], s54 offen nt lds\n" \
"s_add_u32 m0, s62, 132096\n" \
"s_xor_b32 s54, s15, 6553600\n" \
"buffer_load_dwordx4 v33, s[40:43], s54 offen nt lds\n" \
"s_add_u32 m0, s62, 133120\n" \
"s_xor_b32 s54, s15, 6684672\n" \
"buffer_load_dwordx4 v32, s[40:43], s54 offen nt lds\n" \
"s_add_u32 m0, s62, 134144\n" \
"s_xor_b32 s54, s15, 6684672\n" \
"buffer_load_dwordx4 v33, s[40:43], s54 offen nt lds\n" \
"s_waitcnt vmcnt(8)\n" \
"s_barrier\n" \
"ds_read_b64_tr_b16 a[160:161], v34 offset:16384\n" \
"ds_read_b64_tr_b16 a[162:163], v34 offset:16896\n" \
"ds_read_b64_tr_b16 a[168:169], v35 offset:16384\n" \
"ds_read_b64_tr_b16 a[170:171], v35 offset:16896\n" \
"ds_read_b64_tr_b16 v[48:49], v34 offset:24576\n" \
"ds_read_b64_tr_b16 v[50:51], v34 offset:25088\n" \
"ds_read_b64_tr_b16 v[56:57], v35 offset:24576\n" \
"ds_read_b64_tr_b16 v[58:59], v35 offset:25088\n" \
"ds_read_b64_tr_b16 a[164:165], v38 offset:16384\n" \
"ds_read_b64_tr_b16 a[166:167], v38 offset:16896\n" \
"ds_read_b64_tr_b16 a[172:173], v39 offset:16384\n" \
"ds_read_b64_tr_b16 a[174:175], v39 offset:16896\n" \
"ds_read_b64_tr_b16 v[52:53], v38 offset:24576\n" \
"ds_read_b64_tr_b16 v[54:55], v38 offset:25088\n" \
"ds_read_b64_tr_b16 v[60:61], v39 offset:24576\n" \
"ds_read_b64_tr_b16 v[62:63], v39 offset:25088\n" \
"s_waitcnt lgkmcnt(0)\n" \
"s_barrier\n" \
"ds_write_b128 v1, v[48:51] offset:16384\n" \
"ds_write_b128 v1, v[52:55] offset:17408\n" \
"ds_write_b128 v1, v[56:59] offset:18432\n" \
"ds_write_b128 v1, v[60:63] offset:19456\n" \
"s_waitcnt vmcnt(0)\n" \
"s_barrier\n" \
"ds_read_b64_tr_b16 a[192:193], v34 offset:49152\n" \
"ds_read_b64_tr_b16 a[194:195], v34 offset:49664\n" \
"ds_read_b64_tr_b16 a[200:201], v35 offset:49152\n" \
"ds_read_b64_tr_b16 a[202:203], v35 offset:49664\n" \
"ds_read_b64_tr_b16 v[48:49], v34 offset:57344\n" \
"ds_read_b64_tr_b16 v[50:51], v34 offset:57856\n" \
"ds_read_b64_tr_b16 v[56:57], v35 offset:57344\n" \
"ds_read_b64_tr_b16 v[58:59], v35 offset:57856\n" \
"ds_read_b64_tr_b16 a[196:197], v38 offset:0\n" \
"ds_read_b64_tr_b16 a[198:199], v38 offset:512\n" \
"ds_read_b64_tr_b16 a[204:205], v39 offset:0\n" \
"ds_read_b64_tr_b16 a[206:207], v39 offset:512\n" \
"ds_read_b64_tr_b16 v[52:53], v38 offset:8192\n" \
"ds_read_b64_tr_b16 v[54:55], v38 offset:8704\n" \
"ds_read_b64_tr_b16 v[60:61], v39 offset:8192\n" \
"ds_read_b64_tr_b16 v[62:63], v39 offset:8704\n" \
"s_waitcnt lgkmcnt(0)\n" \
"s_barrier\n" \
"ds_write_b128 v1, v[48:51] offset:49152\n" \
"ds_write_b128 v1, v[52:55] offset:50176\n" \
"ds_write_b128 v1, v[56:59] offset:51200\n" \
"ds_write_b128 v1, v[60:63] offset:52224\n" \
"s_waitcnt vmcnt(0) lgkmcnt(0)\n" \
"s_barrier\n" \
"s_getpc_b64 s[18:19]\n" \
".Lq_pc_%=:\n" \
"v_mbcnt_lo_u32_b32 v40, -1, 0\n" \
"v_mbcnt_hi_u32_b32 v40, -1, v40\n" \
"v_lshl_add_u32 v40, s48, 6, v40\n" \
"v_lshlrev_b32 v40, 6, v40\n" \
"v_min_u32 v40, .Lq_fin_%=-.Lq_pc_%=, v40\n" \
"global_load_dword v41, v40, s[18:19]\n" \
"s_lshl_b32 s62, s48, 11\n" \
"s_mov_b32 s21, s12\n" \
"s_add_u32 s12, s63, s62\n" \
"s_mov_b32 s49, 0\n" \
"s_mov_b32 s52, 0\n" \
"s_mov_b32 s81, 1\n" \
"s_mov_b32 s88, 0\n" \
"s_mov_b32 s95, 0x7ffffff0\n" \
"s_mov_b32 s98, 0x3e0293ee\n" \
"s_mov_b32 s99, 0x3e0293ee\n" \
"v_mov_b32 v20, 0x3f803f80\n" \
"v_mov_b32 v21, 0x3f803f80\n" \
"v_mov_b32 v22, 0x3f803f80\n" \
"v_mov_b32 v23, 0x3f803f80\n" \
"v_mov_b32 v24, 0\n" \
"v_mov_b32 v25, 0\n" \
"v_mov_b32 v26, 0\n" \
"v_mov_b32 v27, 0\n" \
"v_mov_b32 v28, 0\n" \
"v_mov_b32 v29, 0\n" \
"v_mov_b32 v104, 0\n" \
"v_mov_b32 v105, 0\n" \
"v_mov_b32 v106, 0\n" \
"v_mov_b32 v107, 0\n" \
"v_mov_b32 v108, 0\n" \
"v_mov_b32 v109, 0\n" \
"v_mov_b32 v110, 0\n" \
"v_mov_b32 v111, 0\n" \
"v_cmp_le_i32_e64 s[64:65], 0, v14\n" \
"v_cmp_le_i32_e64 s[66:67], 1, v14\n" \
"v_cmp_le_i32_e64 s[68:69], 2, v14\n" \
"v_cmp_le_i32_e64 s[70:71], 3, v14\n" \
"v_cmp_le_i32_e64 s[72:73], 16, v14\n" \
"v_cmp_le_i32_e64 s[74:75], 17, v14\n" \
"v_cmp_le_i32_e64 s[76:77], 18, v14\n" \
"v_cmp_le_i32_e64 s[78:79], 19, v14\n" \
"s_mov_b32 s82, -1\n" \
"s_mov_b32 s83, 0\n" \
"s_mov_b32 s6, 0x33333333\n" \
"s_mov_b32 s7, 0x33333333\n" \
"s_mov_b32 s8, 0x53800000\n" \
"s_mov_b32 s41, s10\n" \
"s_mov_b32 s22, 0xfffffc00\n" \
"s_mov_b32 s23, -1\n" \
"s_mul_i32 s32, s46, 1\n" \
"s_mov_b32 s33, s32\n" \
"s_mov_b32 s34, s46\n" \
"s_add_u32 s35, s17, s13\n" \
"s_mov_b32 s10, s14\n" \
"s_mov_b32 s14, s21\n" \
"s_lshl_b32 s42, s48, 10\n" \
"s_add_u32 s42, s42, 114688\n" \
"s_and_b32 s43, s48, 1\n" \
"s_lshl_b32 s43, s43, 12\n" \
"s_mov_b32 s54, 0\n" \
"buffer_store_dwordx4 v[24:27], v7, s[36:39], s54 offen sc1\n" \
"s_mov_b32 s54, 131072\n" \
"buffer_store_dwordx4 v[24:27], v7, s[36:39], s54 offen sc1\n" \
"s_add_u32 s54, s94, 65536\n" \
"buffer_store_dword v24, v10, s[36:39], s54 offen sc1\n" \
"s_mov_b32 s54, 262144\n" \
"buffer_store_dwordx4 v[24:27], v7, s[36:39], s54 offen sc1\n" \
"s_add_u32 s54, s94, 131072\n" \
"buffer_store_dword v24, v10, s[36:39], s54 offen sc1\n" \
"s_mov_b32 s54, 393216\n" \
"buffer_store_dwordx4 v[24:27], v7, s[36:39], s54 offen sc1\n" \
"s_add_u32 s54, s94, 196608\n" \
"buffer_store_dword v24, v10, s[36:39], s54 offen sc1\n" \
"s_cmp_eq_u32 s48, 0\n" \
"s_cselect_b32 s9, 7, 0\n" \
"buffer_load_dword v30, v15, s[36:39], s80 offen sc1\n" \
"s_waitcnt vmcnt(0)\n" \
"v_cmp_ge_u32_e64 s[96:97], s81, v30\n" \
"s_nop 4\n" \
"s_and_b32 s96, s96, s9\n" \
"s_cbranch_scc1 .Lq_f0_cold_%=\n" \
".Lq_f0_ok_%=:\n" \
"s_barrier\n" \
"s_add_u32 m0, s62, 131072\n" \
"s_sub_u32 s15, s12, 114688\n" \
"buffer_load_dwordx4 v3, s[36:39], s15 offen offset:0 sc1 lds\n" \
"buffer_load_dwordx4 v3, s[36:39], s15 offen offset:1024 sc1 lds\n" \
"s_memrealtime s[56:57]\n" \
"s_waitcnt lgkmcnt(0)\n" \
"s_sub_u32 s50, s56, 20\n" \
"s_waitcnt vmcnt(0) lgkmcnt(0)\n" \
"s_add_u32 s54, s49, 0\n" \
"s_and_b32 s54, s54, 3\n" \
"s_mul_i32 s58, s54, 131072\n" \
"s_add_u32 s54, s49, 1\n" \
"s_and_b32 s54, s54, 3\n" \
"s_mul_i32 s59, s54, 131072\n" \
"s_add_u32 s54, s49, 2\n" \
"s_and_b32 s54, s54, 3\n" \
"s_mul_i32 s60, s54, 131072\n" \
"s_add_u32 s54, s49, 3\n" \
"s_and_b32 s54, s54, 3\n" \
"s_mul_i32 s61, s54, 131072\n" \
"s_lshr_b32 s53, s58, 1\n" \
"s_add_u32 s92, s53, s94\n" \
"s_add_u32 s55, s53, s41\n" \
"s_xor_b32 s89, s55, 0x4800\n" \
"s_xor_b32 s90, s55, 0x800\n" \
"s_xor_b32 s91, s55, 0x4000\n" \
"s_lshr_b32 s93, s59, 1\n" \
"s_add_u32 s93, s93, s94\n" \
"s_add_u32 s81, s49, 2\n" \
"s_min_u32 s81, s81, 63\n" \
"ds_read_b128 v[32:35], v1 offset:0\n" \
"ds_read_b128 v[36:39], v1 offset:1024\n" \
"ds_read_b128 v[40:43], v1 offset:2048\n" \
"ds_read_b128 v[44:47], v1 offset:3072\n" \
"ds_read_b128 v[48:51], v2 offset:32768\n" \
"ds_read_b128 v[52:55], v2 offset:33792\n" \
"ds_read_b128 v[56:59], v2 offset:34816\n" \
"ds_read_b128 v[60:63], v2 offset:35840\n" \
"v_mov_b64 v[80:81], 0\n" \
"v_mov_b64 v[82:83], 0\n" \
"v_mov_b64 v[84:85], 0\n" \
"v_mov_b64 v[86:87], 0\n" \
"v_mov_b64 v[88:89], 0\n" \
"v_mov_b64 v[90:91], 0\n" \
"v_mov_b64 v[92:93], 0\n" \
"v_mov_b64 v[94:95], 0\n" \
"v_mov_b32 v13, -1\n" \
"v_add_u32 v14, s62, v3\n" \
"v_xor_b32 v31, 0x400, v11\n" \
"s_max_u32 s53, s49, 1\n" \
"s_sub_u32 s53, s53, 1\n" \
"v_mov_b32 v30, s53\n" \
"s_mov_b32 m0, s42\n" \
"s_waitcnt vmcnt(0) lgkmcnt(0)\n" \
"s_nop 7\n" \
"s_nop 7\n" \
".p2align 6\n" \
".Lq_loop_%=:\n" \
"s_getpc_b64 s[18:19]\n" \
".Lq_wp_%=:\n" \
"s_add_u32 s18, s18, .Lq_ws_%=-.Lq_wp_%=\n" \
"s_addc_u32 s19, s19, 0\n" \
"s_sub_u32 s21, s50, 5\n" \
"s_nop 0\n" \
".Lq_w_%=:\n" \
"s_memrealtime s[56:57]\n" \
"s_waitcnt lgkmcnt(0)\n" \
"s_sub_u32 s54, s56, s21\n" \
"s_cmp_gt_u32 s54, s22\n" \
"s_cbranch_scc1 .Lq_w_%=\n" \
"s_min_u32 s21, s54, 3\n" \
"s_lshl2_add_u32 s18, s21, s18\n" \
"s_setpc_b64 s[18:19]\n" \
"s_nop 0\n" \
".Lq_ws_%=:\n" \
"s_nop 4\n" \
"s_nop 5\n" \
"s_nop 4\n" \
".Lq_w4_%=:\n" \
"s_barrier\n" \
".Lq_go_%=:\n" \
"buffer_load_dwordx4 v4, s[36:39], s55 offen sc1 lds\n" \
"s_memtime s[0:1]\n" \
"s_cmp_lt_u32 s54, 8\n" \
"s_cselect_b32 s51, s50, s56\n" \
"s_cselect_b32 s23, s23, s49\n" \
"s_cselect_b32 s53, 0, s14\n" \
"s_add_u32 s53, s53, s34\n" \
"v_mov_b32 v17, s35\n" \
"s_nop 7\n" \
"s_nop 7\n" \
"s_add_u32 m0, s42, 4096\n" \
"s_nop 0\n" \
"buffer_load_dwordx4 v4, s[36:39], s89 offen sc1 lds\n" \
"s_nop 7\n" \
"s_nop 7\n" \
"s_nop 7\n" \
"s_add_u32 m0, s42, 8192\n" \
"s_nop 0\n" \
"buffer_load_dwordx4 v4, s[36:39], s90 offen sc1 lds\n" \
"s_add_u32 m0, s42, 12288\n" \
"s_nop 0\n" \
"buffer_load_dwordx4 v4, s[36:39], s91 offen sc1 lds\n" \
"s_add_u32 m0, s62, 147456\n" \
"s_sub_u32 s15, s12, 81920\n" \
"buffer_load_dwordx4 v3, s[36:39], s15 offen offset:0 sc1 lds\n" \
"buffer_load_dwordx4 v3, s[36:39], s15 offen offset:1024 sc1 lds\n" \
"s_add_u32 m0, s62, 139264\n" \
"s_sub_u32 s15, s12, 98304\n" \
"buffer_load_dwordx4 v3, s[36:39], s15 offen offset:0 sc1 lds\n" \
"buffer_load_dwordx4 v3, s[36:39], s15 offen offset:1024 sc1 lds\n" \
"s_waitcnt lgkmcnt(0)\n" \
"s_waitcnt vmcnt(7)\n" \
"s_barrier\n" \
"s_memtime s[2:3]\n" \
"ds_read_b128 v[64:67], v3 offset:0\n" \
"ds_read_b128 v[68:71], v3 offset:1024\n" \
"ds_read_b128 v[72:75], v3 offset:2048\n" \
"ds_read_b128 v[76:79], v3 offset:3072\n" \
"s_waitcnt lgkmcnt(3)\n" \
"v_smfmac_f32_16x16x64_bf16 v[80:83], v[64:67], a[0:7], v12\n" \
"v_min3_u32 v13, v13, v64, v66\n" \
"v_smfmac_f32_16x16x64_bf16 v[84:87], v[64:67], a[8:15], v12\n" \
"s_waitcnt lgkmcnt(2)\n" \
"v_min3_u32 v13, v13, v68, v70\n" \
"v_smfmac_f32_16x16x64_bf16 v[88:91], v[64:67], v[32:39], v12\n" \
"s_waitcnt lgkmcnt(1)\n" \
"v_min3_u32 v13, v13, v72, v74\n" \
"v_smfmac_f32_16x16x64_bf16 v[92:95], v[64:67], v[40:47], v12\n" \
"s_waitcnt lgkmcnt(0)\n" \
"v_min3_u32 v13, v13, v76, v78\n" \
"v_smfmac_f32_16x16x64_bf16 v[80:83], v[68:71], a[16:23], v12\n" \
"s_sub_u32 s2, s2, s0\n" \
"s_mul_i32 s2, s2, 23\n" \
"v_cmp_eq_u32 vcc, 0, v13\n" \
"v_smfmac_f32_16x16x64_bf16 v[84:87], v[68:71], a[24:31], v12\n" \
"ds_read_b128 v[32:35], v2 offset:0\n" \
"v_smfmac_f32_16x16x64_bf16 v[88:91], v[68:71], v[112:119], v12\n" \
"ds_read_b128 v[36:39], v2 offset:1024\n" \
"v_smfmac_f32_16x16x64_bf16 v[92:95], v[68:71], v[120:127], v12\n" \
"s_cbranch_vccnz .Lq_retry_%=\n" \
"s_waitcnt vmcnt(6)\n" \
"s_barrier\n" \
"ds_read_b128 v[64:67], v3 offset:4096\n" \
"v_smfmac_f32_16x16x64_bf16 v[80:83], v[72:75], a[32:39], v12\n" \
"ds_read_b128 v[68:71], v3 offset:5120\n" \
"s_lshr_b32 s2, s2, 9\n" \
"v_smfmac_f32_16x16x64_bf16 v[84:87], v[72:75], a[40:47], v12\n" \
"ds_read_b128 v[40:43], v2 offset:2048\n" \
"s_add_u32 s3, s51, s2\n" \
"v_smfmac_f32_16x16x64_bf16 v[88:91], v[72:75], v[48:55], v12\n" \
"ds_read_b128 v[44:47], v2 offset:3072\n" \
"s_add_u32 s53, s53, s3\n" \
"v_smfmac_f32_16x16x64_bf16 v[92:95], v[72:75], v[56:63], v12\n" \
"ds_read_b128 v[72:75], v3 offset:6144\n" \
"s_max_u32 s53, s53, 1\n" \
"v_mov_b32 v16, s53\n" \
"v_smfmac_f32_16x16x64_bf16 v[80:83], v[76:79], a[48:55], v12\n" \
"buffer_store_dwordx2 v[16:17], v10, s[36:39], s92 offen sc1\n" \
"v_smfmac_f32_16x16x64_bf16 v[84:87], v[76:79], a[56:63], v12\n" \
"ds_read_b128 v[48:51], v2 offset:16384\n" \
"v_smfmac_f32_16x16x64_bf16 v[88:91], v[76:79], v[128:135], v12\n" \
"ds_read_b128 v[52:55], v2 offset:17408\n" \
"v_smfmac_f32_16x16x64_bf16 v[92:95], v[76:79], v[136:143], v12\n" \
"ds_read_b128 v[76:79], v3 offset:7168\n" \
"s_waitcnt lgkmcnt(7)\n" \
"v_smfmac_f32_16x16x64_bf16 v[80:83], v[64:67], a[64:71], v12\n" \
"v_min3_u32 v13, v13, v64, v66\n" \
"v_smfmac_f32_16x16x64_bf16 v[84:87], v[64:67], a[72:79], v12\n" \
"ds_read_b128 v[56:59], v2 offset:18432\n" \
"v_smfmac_f32_16x16x64_bf16 v[88:91], v[64:67], v[32:39], v12\n" \
"ds_read_b128 v[60:63], v2 offset:19456\n" \
"s_waitcnt lgkmcnt(6)\n" \
"v_smfmac_f32_16x16x64_bf16 v[92:95], v[64:67], v[40:47], v12\n" \
"v_smfmac_f32_16x16x64_bf16 v[80:83], v[68:71], a[80:87], v12\n" \
"v_min3_u32 v13, v13, v68, v70\n" \
"v_smfmac_f32_16x16x64_bf16 v[84:87], v[68:71], a[88:95], v12\n" \
"ds_read_b128 v[32:35], v1 offset:32768\n" \
"v_smfmac_f32_16x16x64_bf16 v[88:91], v[68:71], v[144:151], v12\n" \
"ds_read_b128 v[36:39], v1 offset:33792\n" \
"v_smfmac_f32_16x16x64_bf16 v[92:95], v[68:71], v[152:159], v12\n" \
"s_waitcnt lgkmcnt(7)\n" \
"v_smfmac_f32_16x16x64_bf16 v[80:83], v[72:75], a[96:103], v12\n" \
"v_min3_u32 v13, v13, v72, v74\n" \
"v_smfmac_f32_16x16x64_bf16 v[84:87], v[72:75], a[104:111], v12\n" \
"ds_read_b128 v[40:43], v1 offset:34816\n" \
"s_waitcnt lgkmcnt(6)\n" \
"v_smfmac_f32_16x16x64_bf16 v[88:91], v[72:75], v[48:55], v12\n" \
"ds_read_b128 v[44:47], v1 offset:35840\n" \
"s_waitcnt lgkmcnt(4)\n" \
"v_smfmac_f32_16x16x64_bf16 v[92:95], v[72:75], v[56:63], v12\n" \
"s_waitcnt vmcnt(5)\n" \
"s_barrier\n" \
"ds_read_b128 v[64:67], v3 offset:8192\n" \
"v_smfmac_f32_16x16x64_bf16 v[80:83], v[76:79], a[112:119], v12\n" \
"ds_read_b128 v[68:71], v3 offset:9216\n" \
"v_min3_u32 v13, v13, v76, v78\n" \
"v_smfmac_f32_16x16x64_bf16 v[84:87], v[76:79], a[120:127], v12\n" \
"ds_read_b128 v[48:51], v1 offset:16384\n" \
"v_smfmac_f32_16x16x64_bf16 v[88:91], v[76:79], v[160:167], v12\n" \
"ds_read_b128 v[52:55], v1 offset:17408\n" \
"v_smfmac_f32_16x16x64_bf16 v[92:95], v[76:79], v[168:175], v12\n" \
"ds_read_b128 v[72:75], v3 offset:10240\n" \
"ds_read_b128 v[76:79], v3 offset:11264\n" \
"s_waitcnt lgkmcnt(5)\n" \
"v_smfmac_f32_16x16x64_bf16 v[80:83], v[64:67], a[128:135], v12\n" \
"buffer_store_dwordx4 v[96:99], v11, s[36:39], s95 offen sc1\n" \
"v_min3_u32 v13, v13, v64, v66\n" \
"v_smfmac_f32_16x16x64_bf16 v[84:87], v[64:67], a[136:143], v12\n" \
"ds_read_b128 v[56:59], v1 offset:18432\n" \
"v_smfmac_f32_16x16x64_bf16 v[88:91], v[64:67], v[32:39], v12\n" \
"ds_read_b128 v[60:63], v1 offset:19456\n" \
"v_smfmac_f32_16x16x64_bf16 v[92:95], v[64:67], v[40:47], v12\n" \
"ds_read_b128 v[64:67], v3 offset:12288\n" \
"s_waitcnt lgkmcnt(7)\n" \
"v_smfmac_f32_16x16x64_bf16 v[80:83], v[68:71], a[144:151], v12\n" \
"v_min3_u32 v13, v13, v68, v70\n" \
"v_smfmac_f32_16x16x64_bf16 v[84:87], v[68:71], a[152:159], v12\n" \
"ds_read_b128 v[32:35], v1 offset:49152\n" \
"v_smfmac_f32_16x16x64_bf16 v[88:91], v[68:71], v[176:183], v12\n" \
"ds_read_b128 v[36:39], v1 offset:50176\n" \
"v_smfmac_f32_16x16x64_bf16 v[92:95], v[68:71], v[184:191], v12\n" \
"ds_read_b128 v[68:71], v3 offset:13312\n" \
"s_waitcnt lgkmcnt(7)\n" \
"v_smfmac_f32_16x16x64_bf16 v[80:83], v[72:75], a[160:167], v12\n" \
"v_min3_u32 v13, v13, v72, v74\n" \
"v_smfmac_f32_16x16x64_bf16 v[84:87], v[72:75], a[168:175], v12\n" \
"ds_read_b128 v[40:43], v1 offset:51200\n" \
"v_smfmac_f32_16x16x64_bf16 v[88:91], v[72:75], v[48:55], v12\n" \
"ds_read_b128 v[44:47], v1 offset:52224\n" \
"s_waitcnt lgkmcnt(6)\n" \
"v_smfmac_f32_16x16x64_bf16 v[92:95], v[72:75], v[56:63], v12\n" \
"ds_read_b128 v[72:75], v3 offset:14336\n" \
"v_smfmac_f32_16x16x64_bf16 v[80:83], v[76:79], a[176:183], v12\n" \
"buffer_store_dwordx4 v[100:103], v31, s[36:39], s95 offen sc1\n" \
"v_min3_u32 v13, v13, v76, v78\n" \
"v_smfmac_f32_16x16x64_bf16 v[84:87], v[76:79], a[184:191], v12\n" \
"ds_read_b128 v[48:51], v3 offset:20480\n" \
"v_smfmac_f32_16x16x64_bf16 v[88:91], v[76:79], v[192:199], v12\n" \
"ds_read_b128 v[52:55], v3 offset:21504\n" \
"v_smfmac_f32_16x16x64_bf16 v[92:95], v[76:79], v[200:207], v12\n" \
"ds_read_b128 v[76:79], v3 offset:15360\n" \
"s_waitcnt lgkmcnt(9)\n" \
"v_smfmac_f32_16x16x64_bf16 v[80:83], v[64:67], a[192:199], v12\n" \
"s_mov_b32 s95, 0x7ffffff0\n" \
"v_min3_u32 v13, v13, v64, v66\n" \
"v_smfmac_f32_16x16x64_bf16 v[84:87], v[64:67], a[200:207], v12\n" \
"ds_read_b128 v[56:59], v3 offset:22528\n" \
"s_waitcnt lgkmcnt(8)\n" \
"v_smfmac_f32_16x16x64_bf16 v[88:91], v[64:67], v[32:39], v12\n" \
"ds_read_b128 v[60:63], v3 offset:23552\n" \
"s_waitcnt lgkmcnt(6)\n" \
"v_smfmac_f32_16x16x64_bf16 v[92:95], v[64:67], v[40:47], v12\n" \
"v_smfmac_f32_16x16x64_bf16 v[80:83], v[68:71], a[208:215], v12\n" \
"v_min3_u32 v13, v13, v68, v70\n" \
"v_smfmac_f32_16x16x64_bf16 v[84:87], v[68:71], a[216:223], v12\n" \
"ds_read_b128 v[32:35], v3 offset:16384\n" \
"v_smfmac_f32_16x16x64_bf16 v[88:91], v[68:71], v[208:215], v12\n" \
"ds_read_b128 v[36:39], v3 offset:17408\n" \
"v_smfmac_f32_16x16x64_bf16 v[92:95], v[68:71], v[216:223], v12\n" \
"s_waitcnt lgkmcnt(7)\n" \
"v_smfmac_f32_16x16x64_bf16 v[80:83], v[72:75], a[224:231], v12\n" \
"v_min3_u32 v13, v13, v72, v74\n" \
"v_smfmac_f32_16x16x64_bf16 v[84:87], v[72:75], a[232:239], v12\n" \
"ds_read_b128 v[40:43], v3 offset:18432\n" \
"s_waitcnt lgkmcnt(5)\n" \
"v_smfmac_f32_16x16x64_bf16 v[80:83], v[76:79], a[240:247], v12\n" \
"ds_read_b128 v[44:47], v3 offset:19456\n" \
"v_min3_u32 v13, v13, v76, v78\n" \
"v_smfmac_f32_16x16x64_bf16 v[84:87], v[76:79], a[248:255], v12\n" \
"v_cmp_eq_u32_e64 vcc, 0, v13\n" \
"v_smfmac_f32_16x16x64_bf16 v[88:91], v[72:75], v[224:231], v12\n" \
"v_add_f32_e64 v80, v80, v82\n" \
"v_smfmac_f32_16x16x64_bf16 v[92:95], v[72:75], v[232:239], v12\n" \
"v_add_f32_e64 v81, v81, v83\n" \
"v_smfmac_f32_16x16x64_bf16 v[88:91], v[76:79], v[240:247], v12\n" \
"s_cbranch_vccnz .Lq_retry_%=\n" \
"v_add_f32 v84, v84, v86\n" \
"v_smfmac_f32_16x16x64_bf16 v[92:95], v[76:79], v[248:255], v12\n" \
"v_add_f32_e64 v85, v85, v87\n" \
"v_pk_mul_f32 v[80:81], v[80:81], s[98:99]\n" \
"v_pk_mul_f32 v[84:85], v[84:85], s[98:99]\n" \
"v_cvt_pk_bf16_f32 v80, v80, v81\n" \
"v_cvt_pk_bf16_f32 v84, v84, v85\n" \
"v_pk_add_f32 v[88:89], v[88:89], v[90:91]\n" \
"v_cvt_pk_bf16_f32 v81, v88, v89\n" \
"s_nop 0\n" \
"v_pk_add_f32 v[92:93], v[92:93], v[94:95]\n" \
"v_cvt_pk_bf16_f32 v85, v92, v93\n" \
"ds_write2_b64 v5, v[80:81], v[84:85] offset1:64\n" \
"buffer_load_dword v104, v8, s[36:39], s58 offen offset:0 sc1\n" \
"buffer_load_dword v106, v8, s[36:39], s58 offen offset:256 sc1\n" \
"s_cmp_eq_u32 s49, 63\n" \
"s_waitcnt vmcnt(5)\n" \
"s_waitcnt lgkmcnt(0)\n" \
"s_barrier\n" \
"ds_read_b64_tr_b16 v[64:65], v6 offset:0\n" \
"ds_read_b64_tr_b16 v[66:67], v6 offset:512\n" \
"ds_read_b64_tr_b16 v[68:69], v6 offset:1024\n" \
"ds_read_b64_tr_b16 v[70:71], v6 offset:1536\n" \
"ds_read_b64_tr_b16 v[72:73], v6 offset:2048\n" \
"ds_read_b64_tr_b16 v[74:75], v6 offset:2560\n" \
"s_nop 0\n" \
"s_cbranch_scc1 .Lq_last_%=\n" \
"s_waitcnt lgkmcnt(4)\n" \
"v_mfma_f32_16x16x32_bf16 v[88:91], v[64:67], v[64:67], 0\n" \
"ds_read_b64_tr_b16 v[76:77], v6 offset:3072\n" \
"v_mfma_f32_16x16x32_bf16 v[80:83], v[32:35], v[64:67], 0\n" \
"ds_read_b64_tr_b16 v[78:79], v6 offset:3584\n" \
"s_waitcnt lgkmcnt(4)\n" \
"v_mfma_f32_16x16x32_bf16 v[88:91], v[68:71], v[68:71], v[88:91]\n" \
"ds_read_b128 v[96:99], v14 offset:32768\n" \
"s_waitcnt lgkmcnt(3)\n" \
"v_mfma_f32_16x16x32_bf16 v[88:91], v[72:75], v[72:75], v[88:91]\n" \
"ds_read_b128 v[100:103], v14 offset:33792\n" \
"v_mfma_f32_16x16x32_bf16 v[80:83], v[36:39], v[68:71], v[80:83]\n" \
"buffer_store_dword v30, v10, s[36:39], s87 offen sc1\n" \
"s_waitcnt lgkmcnt(2)\n" \
"v_mfma_f32_16x16x32_bf16 v[88:91], v[76:79], v[76:79], v[88:91]\n" \
"buffer_load_dword v30, v15, s[36:39], s80 offen sc1\n" \
"v_mfma_f32_16x16x32_bf16 v[80:83], v[40:43], v[72:75], v[80:83]\n" \
"v_mfma_f32_16x16x32_bf16 v[80:83], v[44:47], v[76:79], v[80:83]\n" \
"v_max_f32 v13, v90, v91\n" \
"v_max_f32_e64 v14, v90, v91\n" \
"v_mfma_f32_16x16x32_bf16 v[84:87], v[48:51], v[64:67], 0\n" \
"s_nop 0\n" \
"v_permlane16_swap_b32 v13, v14\n" \
"v_mfma_f32_16x16x32_bf16 v[84:87], v[52:55], v[68:71], v[84:87]\n" \
"v_max_f32 v13, v13, v14\n" \
"v_max_f32 v14, v13, v14\n" \
"v_mfma_f32_16x16x32_bf16 v[84:87], v[56:59], v[72:75], v[84:87]\n" \
"s_nop 0\n" \
"v_permlane32_swap_b32 v13, v14\n" \
"v_mfma_f32_16x16x32_bf16 v[84:87], v[60:63], v[76:79], v[84:87]\n" \
"v_max_f32 v14, v13, v14\n" \
"v_sub_f32 v80, v80, v14\n" \
"v_sub_f32 v81, v81, v14\n" \
"v_exp_f32 v80, v80\n" \
"v_pk_add_f32 v[82:83], v[82:83], v[14:15] op_sel_hi:[1,0] neg_lo:[0,1] neg_hi:[0,1]\n" \
"v_exp_f32 v81, v81\n" \
"v_exp_f32 v82, v82\n" \
"v_exp_f32_e64 v83, v83\n" \
"v_pk_add_f32 v[84:85], v[84:85], v[14:15] op_sel_hi:[1,0] neg_lo:[0,1] neg_hi:[0,1]\n" \
"v_pk_add_f32 v[86:87], v[86:87], v[14:15] op_sel_hi:[1,0] neg_lo:[0,1] neg_hi:[0,1]\n" \
"v_pk_add_f32 v[90:91], v[90:91], v[14:15] op_sel_hi:[1,0] neg_lo:[0,1] neg_hi:[0,1]\n" \
"v_exp_f32 v84, v84\n" \
"v_exp_f32 v85, v85\n" \
"v_exp_f32 v86, v86\n" \
"v_exp_f32 v87, v87\n" \
"v_cvt_pk_bf16_f32 v80, v80, v81\n" \
"v_cvt_pk_bf16_f32 v81, v82, v83\n" \
"v_cvt_pk_bf16_f32 v82, v84, v85\n" \
"v_cvt_pk_bf16_f32 v83, v86, v87\n" \
"v_exp_f32 v90, v90\n" \
"s_waitcnt lgkmcnt(1)\n" \
"v_mfma_f32_16x16x32_bf16 v[84:87], v[96:99], v[80:83], 0\n" \
"v_exp_f32 v91, v91\n" \
"s_waitcnt lgkmcnt(0)\n" \
"v_mfma_f32_16x16x32_bf16 v[92:95], v[100:103], v[80:83], 0\n" \
"v_cvt_pk_bf16_f32 v26, v90, v91\n" \
"v_mfma_f32_16x16x32_bf16 v[16:19], v[20:23], v[80:83], 0\n" \
"s_nop 0\n" \
"v_mfma_f32_16x16x32_bf16 v[16:19], v[20:23], v[26:29], v[16:19]\n" \
"s_waitcnt vmcnt(2)\n" \
"v_mfma_f32_16x16x32_bf16 v[84:87], v[104:107], v[26:29], v[84:87]\n" \
"v_mfma_f32_16x16x32_bf16 v[92:95], v[104:107], v[24:27], v[92:95]\n" \
"v_min_u32 v13, v104, v106\n" \
"v_cmp_eq_u32 vcc, 0, v13\n" \
"v_rcp_f32_e64 v14, v16\n" \
"v_cmp_nlt_f32_e64 s[26:27], v16, s8\n" \
".Lq_vr2_%=:\n" \
"v_pk_mul_f32 v[84:85], v[84:85], v[14:15] op_sel_hi:[1,0]\n" \
"v_pk_mul_f32 v[86:87], v[86:87], v[14:15] op_sel_hi:[1,0]\n" \
"v_cvt_pk_bf16_f32 v88, v84, v85\n" \
"v_cvt_pk_bf16_f32 v89, v86, v87\n" \
"v_max_u32 v88, 1, v88\n" \
"s_and_b64 s[26:27], s[26:27], s[6:7]\n" \
"s_or_b64 vcc, vcc, s[26:27]\n" \
"s_cbranch_vccnz .Lq_vr_%=\n" \
"v_pk_mul_f32 v[92:93], v[92:93], v[14:15] op_sel_hi:[1,0]\n" \
"v_pk_mul_f32 v[94:95], v[94:95], v[14:15] op_sel_hi:[1,0]\n" \
"v_cvt_pk_bf16_f32 v90, v92, v93\n" \
"v_cvt_pk_bf16_f32 v91, v94, v95\n" \
"v_max_u32_e64 v90, 1, v90\n" \
"buffer_store_dwordx4 v[88:91], v7, s[36:39], s58 offen sc1\n" \
".Lq_tok_%=:\n" \
"s_memrealtime s[24:25]\n" \
"v_add_u32_e64 v14, s43, v3\n" \
"ds_read_b128 v[96:99], v14 offset:24576\n" \
"ds_read_b128 v[100:103], v14 offset:25600\n" \
"ds_read_b128 v[92:95], v14 offset:26624\n" \
"s_memrealtime s[26:27]\n" \
"ds_read_b128 v[16:19], v14 offset:27648\n" \
"v_xor_b32 v0, 512, v8\n" \
"s_nop 2\n" \
"s_memrealtime s[28:29]\n" \
"s_nop 4\n" \
"s_memrealtime s[30:31]\n" \
"s_waitcnt lgkmcnt(0)\n" \
"v_mfma_f32_16x16x32_bf16 v[88:91], v[64:67], v[96:99], 0\n" \
"s_sub_u32 s13, s12, s62\n" \
"s_add_u32 s54, s49, 1\n" \
"s_min_u32 s54, s54, 63\n" \
"s_lshl_b32 s54, s54, 16\n" \
"v_mfma_f32_16x16x32_bf16 v[80:83], v[32:35], v[96:99], 0\n" \
"s_lshr_b32 s53, s54, 8\n" \
"s_and_b32 s53, s53, 0x2000\n" \
"s_xor_b32 s54, s54, s53\n" \
"buffer_load_dword v108, v0, s[36:39], s58 offen offset:0 sc1\n" \
"v_mfma_f32_16x16x32_bf16 v[88:91], v[68:71], v[100:103], v[88:91]\n" \
"s_xor_b32 s12, s54, s63\n" \
"s_add_u32 s12, s12, s62\n" \
"buffer_load_dword v110, v0, s[36:39], s58 offen offset:256 sc1\n" \
"v_mfma_f32_16x16x32_bf16 v[88:91], v[72:75], v[92:95], v[88:91]\n" \
"s_add_u32 m0, s62, 131072\n" \
"s_sub_u32 s15, s12, 114688\n" \
"buffer_load_dwordx4 v3, s[36:39], s15 offen offset:0 sc1 lds\n" \
"v_mfma_f32_16x16x32_bf16 v[80:83], v[36:39], v[100:103], v[80:83]\n" \
"buffer_load_dwordx4 v3, s[36:39], s15 offen offset:1024 sc1 lds\n" \
"v_mfma_f32_16x16x32_bf16 v[88:91], v[76:79], v[16:19], v[88:91]\n" \
"s_add_u32 s25, s24, 3\n" \
"s_cmp_eq_u32 s26, s24\n" \
"s_subb_u32 s25, s25, 0\n" \
"v_mfma_f32_16x16x32_bf16 v[80:83], v[40:43], v[92:95], v[80:83]\n" \
"s_cmp_eq_u32 s28, s24\n" \
"s_subb_u32 s25, s25, 0\n" \
"v_mfma_f32_16x16x32_bf16 v[80:83], v[44:47], v[16:19], v[80:83]\n" \
"s_cmp_eq_u32 s30, s24\n" \
"s_subb_u32 s25, s25, 0\n" \
"s_max_u32 s16, s25, 1\n" \
"v_add_u32 v14, s62, v3\n" \
"v_mfma_f32_16x16x32_bf16 v[84:87], v[48:51], v[96:99], 0\n" \
"s_sub_u32 s24, s25, s3\n" \
"s_min_u32 s24, s24, 255\n" \
"v_xor_b32 v0, 0x800, v14\n" \
"v_mfma_f32_16x16x32_bf16 v[84:87], v[52:55], v[100:103], v[84:87]\n" \
"ds_read_b128 v[32:35], v14 offset:32768\n" \
"ds_read_b128 v[36:39], v14 offset:33792\n" \
"s_cmp_eq_u32 s23, s49\n" \
"s_cselect_b32 s24, s34, s24\n" \
"v_mov_b32 v13, 0xceabdc6e\n" \
"v_mfma_f32_16x16x32_bf16 v[84:87], v[56:59], v[92:95], v[84:87]\n" \
"ds_read_b128 v[40:43], v0 offset:32768\n" \
"ds_read_b128 v[44:47], v0 offset:33792\n" \
"s_min_u32 s34, s24, s32\n" \
"s_max_u32 s25, s24, s32\n" \
"v_mfma_f32_16x16x32_bf16 v[84:87], v[60:63], v[16:19], v[84:87]\n" \
"s_min_u32 s25, s25, s33\n" \
"s_max_u32 s34, s34, s25\n" \
"s_mov_b32 s33, s32\n" \
"s_mov_b32 s32, s24\n" \
"v_cndmask_b32_e64 v80, v13, v80, s[64:65]\n" \
"v_cndmask_b32_e64 v81, v13, v81, s[66:67]\n" \
"v_cndmask_b32_e64 v82, v13, v82, s[68:69]\n" \
"v_cndmask_b32_e64 v83, v13, v83, s[70:71]\n" \
"s_waitcnt vmcnt(5)\n" \
"v_cmp_ge_u32_e64 s[96:97], s81, v30\n" \
"v_readlane_b32 s35, v30, 3\n" \
"v_readlane_b32 s53, v30, 4\n" \
"v_cndmask_b32_e64 v84, v13, v84, s[72:73]\n" \
"v_cndmask_b32_e64 v85, v13, v85, s[74:75]\n" \
"v_cndmask_b32_e64 v86, v13, v86, s[76:77]\n" \
"v_cndmask_b32_e64 v87, v13, v87, s[78:79]\n" \
"s_max_u32 s35, s35, s53\n" \
"s_cmp_eq_u32 s35, 0\n" \
"s_cselect_b32 s35, s17, s35\n" \
"s_and_b32 s96, s96, s9\n" \
"s_cbranch_scc1 .Lq_f_cold_%=\n" \
".Lq_f_ok_%=:\n" \
"s_lshr_b32 s11, s58, 1\n" \
"s_mov_b32 s54, s58\n" \
"s_mov_b32 s58, s59\n" \
"s_mov_b32 s59, s60\n" \
"s_mov_b32 s60, s61\n" \
"s_mov_b32 s61, s54\n" \
"buffer_load_dwordx2 v[64:65], v9, s[36:39], s11 offen sc1\n" \
"v_max3_f32 v13, v90, v91, v80\n" \
"v_max3_f32 v14, v81, v82, v83\n" \
"v_max3_f32 v14, v14, v84, v85\n" \
"v_max3_f32 v13, v13, v86, v87\n" \
"v_max_f32 v13, v13, v14\n" \
"v_max_f32 v14, v13, v14\n" \
"s_nop 1\n" \
"v_permlane16_swap_b32 v13, v14\n" \
"v_max_f32 v13, v13, v14\n" \
"v_max_f32 v14, v13, v14\n" \
"s_nop 1\n" \
"v_permlane32_swap_b32 v13, v14\n" \
"v_max_f32 v14, v13, v14\n" \
"v_pk_add_f32 v[80:81], v[80:81], v[14:15] op_sel_hi:[1,0] neg_lo:[0,1] neg_hi:[0,1]\n" \
"v_pk_add_f32 v[82:83], v[82:83], v[14:15] op_sel_hi:[1,0] neg_lo:[0,1] neg_hi:[0,1]\n" \
"v_pk_add_f32 v[84:85], v[84:85], v[14:15] op_sel_hi:[1,0] neg_lo:[0,1] neg_hi:[0,1]\n" \
"v_pk_add_f32 v[86:87], v[86:87], v[14:15] op_sel_hi:[1,0] neg_lo:[0,1] neg_hi:[0,1]\n" \
"v_pk_add_f32 v[90:91], v[90:91], v[14:15] op_sel_hi:[1,0] neg_lo:[0,1] neg_hi:[0,1]\n" \
"v_exp_f32 v80, v80\n" \
"v_exp_f32 v81, v81\n" \
"v_exp_f32 v82, v82\n" \
"v_exp_f32 v83, v83\n" \
"v_exp_f32 v84, v84\n" \
"v_exp_f32 v85, v85\n" \
"v_exp_f32 v86, v86\n" \
"v_exp_f32 v87, v87\n" \
"v_cvt_pk_bf16_f32 v80, v80, v81\n" \
"v_cvt_pk_bf16_f32 v81, v82, v83\n" \
"v_cvt_pk_bf16_f32 v82, v84, v85\n" \
"v_cvt_pk_bf16_f32 v83, v86, v87\n" \
"v_exp_f32 v90, v90\n" \
"s_waitcnt lgkmcnt(3)\n" \
"v_mfma_f32_16x16x32_bf16 v[48:51], v[32:35], v[80:83], 0\n" \
"v_exp_f32 v91, v91\n" \
"s_waitcnt lgkmcnt(2)\n" \
"v_mfma_f32_16x16x32_bf16 v[52:55], v[36:39], v[80:83], 0\n" \
"v_cvt_pk_bf16_f32 v26, v90, v91\n" \
"s_waitcnt lgkmcnt(1)\n" \
"v_mfma_f32_16x16x32_bf16 v[56:59], v[40:43], v[80:83], 0\n" \
"s_waitcnt lgkmcnt(0)\n" \
"v_mfma_f32_16x16x32_bf16 v[60:63], v[44:47], v[80:83], 0\n" \
"v_mfma_f32_16x16x32_bf16 v[92:95], v[20:23], v[80:83], 0\n" \
"s_waitcnt vmcnt(4)\n" \
"v_min3_u32 v13, v104, v106, v108\n" \
"s_waitcnt vmcnt(3)\n" \
"v_min3_u32 v13, v13, v110, v110\n" \
"v_cmp_eq_u32_e64 vcc, 0, v13\n" \
"s_nop 0\n" \
"s_nop 0\n" \
"s_cbranch_vccnz .Lq_v2_cold_%=\n" \
".Lq_v2_ok_%=:\n" \
"v_mfma_f32_16x16x32_bf16 v[92:95], v[20:23], v[26:29], v[92:95]\n" \
"v_mfma_f32_16x16x32_bf16 v[48:51], v[104:107], v[26:29], v[48:51]\n" \
"ds_read_b128 v[32:35], v1 offset:0\n" \
"s_mov_b32 s95, s13\n" \
"s_add_u32 s49, s49, 1\n" \
"v_mfma_f32_16x16x32_bf16 v[52:55], v[104:107], v[24:27], v[52:55]\n" \
"ds_read_b128 v[36:39], v1 offset:1024\n" \
"s_lshr_b32 s53, s58, 1\n" \
"s_add_u32 s92, s53, s94\n" \
"v_mfma_f32_16x16x32_bf16 v[56:59], v[108:111], v[26:29], v[56:59]\n" \
"ds_read_b128 v[40:43], v1 offset:2048\n" \
"s_add_u32 s55, s53, s41\n" \
"s_xor_b32 s89, s55, 0x4800\n" \
"v_mfma_f32_16x16x32_bf16 v[60:63], v[108:111], v[24:27], v[60:63]\n" \
"ds_read_b128 v[44:47], v1 offset:3072\n" \
"s_xor_b32 s90, s55, 0x800\n" \
"s_xor_b32 s91, s55, 0x4000\n" \
"s_lshr_b32 s93, s59, 1\n" \
"s_add_u32 s93, s93, s94\n" \
"s_add_u32 s81, s49, 2\n" \
"s_min_u32 s81, s81, 63\n" \
"s_mov_b32 s14, s10\n" \
"v_rcp_f32 v14, v92\n" \
"s_nop 0\n" \
"v_pk_mul_f32 v[48:49], v[48:49], v[14:15] op_sel_hi:[1,0]\n" \
"v_pk_mul_f32 v[50:51], v[50:51], v[14:15] op_sel_hi:[1,0]\n" \
"v_pk_mul_f32 v[52:53], v[52:53], v[14:15] op_sel_hi:[1,0]\n" \
"v_pk_mul_f32 v[54:55], v[54:55], v[14:15] op_sel_hi:[1,0]\n" \
"v_cvt_pk_bf16_f32 v96, v48, v49\n" \
"v_cvt_pk_bf16_f32 v97, v50, v51\n" \
"v_cvt_pk_bf16_f32 v98, v52, v53\n" \
"v_cvt_pk_bf16_f32 v99, v54, v55\n" \
"ds_read_b128 v[48:51], v2 offset:32768\n" \
"ds_read_b128 v[52:55], v2 offset:33792\n" \
"v_pk_mul_f32 v[56:57], v[56:57], v[14:15] op_sel_hi:[1,0]\n" \
"v_pk_mul_f32 v[58:59], v[58:59], v[14:15] op_sel_hi:[1,0]\n" \
"v_pk_mul_f32 v[60:61], v[60:61], v[14:15] op_sel_hi:[1,0]\n" \
"v_pk_mul_f32 v[62:63], v[62:63], v[14:15] op_sel_hi:[1,0]\n" \
"v_cvt_pk_bf16_f32 v100, v56, v57\n" \
"v_cvt_pk_bf16_f32 v101, v58, v59\n" \
"v_cvt_pk_bf16_f32 v102, v60, v61\n" \
"v_cvt_pk_bf16_f32 v103, v62, v63\n" \
"ds_read_b128 v[56:59], v2 offset:34816\n" \
"ds_read_b128 v[60:63], v2 offset:35840\n" \
"s_cmp_eq_u32 s49, 64\n" \
"s_nop 0\n" \
"s_nop 0\n" \
"s_cbranch_scc1 .Lq_end_%=\n" \
"v_mov_b64 v[80:81], 0\n" \
"v_mov_b64 v[82:83], 0\n" \
"v_mov_b64 v[84:85], 0\n" \
"v_mov_b64 v[86:87], 0\n" \
"v_mov_b64 v[88:89], 0\n" \
"v_mov_b64 v[90:91], 0\n" \
"v_mov_b64 v[92:93], 0\n" \
"v_mov_b64 v[94:95], 0\n" \
"v_mov_b32 v13, -1\n" \
"v_add_u32_e64 v14, s62, v3\n" \
"v_xor_b32 v31, 0x400, v11\n" \
"s_max_u32 s53, s49, 1\n" \
"s_sub_u32 s53, s53, 1\n" \
"v_mov_b32 v30, s53\n" \
"s_mov_b32 m0, s42\n" \
"buffer_store_dwordx4 v[80:83], v7, s[36:39], s59 offen sc1\n" \
"buffer_store_dword v24, v10, s[36:39], s93 offen sc1\n" \
"s_waitcnt lgkmcnt(0)\n" \
"s_mov_b32 s54, 0\n" \
"s_cmp_lg_u32 s48, 0\n" \
"s_cbranch_scc1 .Lq_w4_%=\n" \
"s_waitcnt vmcnt(2)\n" \
"v_cmp_eq_u32_e64 vcc, 0, v64\n" \
"s_nop 0\n" \
"s_and_b64 vcc, vcc, s[82:83]\n" \
"s_nop 0\n" \
"s_cbranch_vccnz .Lq_h_cold_%=\n" \
".Lq_h_ok_%=:\n" \
"v_subrev_u32 v64, s51, v64\n" \
"v_max_i32_dpp v65, v65, v65 row_ror:8 row_mask:0xf bank_mask:0xf\n" \
"s_nop 0\n" \
"v_max_i32_dpp v64, v64, v64 row_ror:8 row_mask:0xf bank_mask:0xf\n" \
"v_max_i32_dpp v65, v65, v65 row_ror:4 row_mask:0xf bank_mask:0xf\n" \
"s_nop 0\n" \
"v_max_i32_dpp v64, v64, v64 row_ror:4 row_mask:0xf bank_mask:0xf\n" \
"v_max_i32_dpp v65, v65, v65 row_ror:2 row_mask:0xf bank_mask:0xf\n" \
"s_nop 0\n" \
"v_max_i32_dpp v64, v64, v64 row_ror:2 row_mask:0xf bank_mask:0xf\n" \
"v_max_i32_dpp v65, v65, v65 row_ror:1 row_mask:0xf bank_mask:0xf\n" \
"s_nop 0\n" \
"v_max_i32_dpp v64, v64, v64 row_ror:1 row_mask:0xf bank_mask:0xf\n" \
"v_readlane_b32 s53, v65, 0\n" \
"v_readlane_b32 s50, v65, 16\n" \
"v_readlane_b32 s54, v64, 0\n" \
"v_readlane_b32 s40, v64, 16\n" \
"s_max_i32 s50, s50, s53\n" \
"s_max_i32 s40, s40, s54\n" \
"s_add_u32 s40, s40, s51\n" \
"s_add_u32 s40, s40, s44\n" \
"s_add_u32 s50, s50, s40\n" \
"s_add_u32 s50, s50, s45\n" \
"s_branch .Lq_loop_%=\n" \
".Lq_f0_cold_%=:\n" \
"s_nop 7\n" \
"s_nop 7\n" \
"s_waitcnt vmcnt(0) lgkmcnt(0)\n" \
"s_mov_b32 s53, 0\n" \
".Lq_f0_again_%=:\n" \
"s_cmp_lg_u32 s52, 0\n" \
"s_cbranch_scc1 .Lq_f0_ok_%=\n" \
"s_sleep 4\n" \
"buffer_load_dword v30, v15, s[36:39], s80 offen sc1\n" \
"s_waitcnt vmcnt(0)\n" \
"v_cmp_ge_u32_e64 s[96:97], s81, v30\n" \
"s_nop 4\n" \
"s_and_b32 s96, s96, s9\n" \
"s_cbranch_scc0 .Lq_f0_ok_%=\n" \
"s_add_u32 s53, s53, 1\n" \
"s_cmp_lt_u32 s53, 32768\n" \
"s_cbranch_scc1 .Lq_f0_again_%=\n" \
"s_mov_b32 s52, 1\n" \
"v_mov_b32 v30, 256\n" \
"v_mov_b32 v31, 1\n" \
"buffer_store_dword v31, v30, s[36:39], 0 offen sc1\n" \
"s_waitcnt vmcnt(0) lgkmcnt(0)\n" \
"s_branch .Lq_f0_ok_%=\n" \
".Lq_last_%=:\n" \
"ds_read_b64_tr_b16 v[76:77], v6 offset:3072\n" \
"ds_read_b64_tr_b16 v[78:79], v6 offset:3584\n" \
"ds_read_b128 v[96:99], v14 offset:32768\n" \
"ds_read_b128 v[100:103], v14 offset:33792\n" \
"buffer_store_dword v30, v10, s[36:39], s87 offen sc1\n" \
"buffer_load_dword v30, v15, s[36:39], s80 offen sc1\n" \
"s_waitcnt vmcnt(0) lgkmcnt(0)\n" \
"s_nop 7\n" \
"s_nop 7\n" \
"s_branch .Lq_tok_%=\n" \
".Lq_vr_%=:\n" \
"s_nop 7\n" \
"s_nop 7\n" \
"s_waitcnt vmcnt(0) lgkmcnt(0)\n" \
"v_min3_u32 v13, v104, v106, v106\n" \
"v_cmp_eq_u32 vcc, 0, v13\n" \
"s_nop 0\n" \
"s_cbranch_vccnz .Lq_v_cold_%=\n" \
".Lq_v_ok_%=:\n" \
"s_mov_b32 s23, s49\n" \
"s_cmp_eq_u64 s[26:27], 0\n" \
"s_cbranch_scc1 .Lq_vr3_%=\n" \
"v_mfma_f32_16x16x32_bf16 v[88:91], v[64:67], v[64:67], 0\n" \
"v_mfma_f32_16x16x32_bf16 v[80:83], v[32:35], v[64:67], 0\n" \
"v_mfma_f32_16x16x32_bf16 v[88:91], v[68:71], v[68:71], v[88:91]\n" \
"v_mfma_f32_16x16x32_bf16 v[88:91], v[72:75], v[72:75], v[88:91]\n" \
"v_mfma_f32_16x16x32_bf16 v[80:83], v[36:39], v[68:71], v[80:83]\n" \
"v_mfma_f32_16x16x32_bf16 v[88:91], v[76:79], v[76:79], v[88:91]\n" \
"v_mfma_f32_16x16x32_bf16 v[80:83], v[40:43], v[72:75], v[80:83]\n" \
"v_mfma_f32_16x16x32_bf16 v[80:83], v[44:47], v[76:79], v[80:83]\n" \
"v_mfma_f32_16x16x32_bf16 v[84:87], v[48:51], v[64:67], 0\n" \
"v_mfma_f32_16x16x32_bf16 v[84:87], v[52:55], v[68:71], v[84:87]\n" \
"v_mfma_f32_16x16x32_bf16 v[84:87], v[56:59], v[72:75], v[84:87]\n" \
"v_max3_f32 v13, v90, v91, v80\n" \
"v_mfma_f32_16x16x32_bf16 v[84:87], v[60:63], v[76:79], v[84:87]\n" \
"v_max3_f32 v14, v81, v82, v83\n" \
"s_nop 6\n" \
"v_max3_f32 v14, v14, v84, v85\n" \
"v_max3_f32 v13, v13, v86, v87\n" \
"v_max_f32 v13, v13, v14\n" \
"v_max_f32 v14, v13, v14\n" \
"s_nop 1\n" \
"v_permlane16_swap_b32 v13, v14\n" \
"v_max_f32 v13, v13, v14\n" \
"v_max_f32 v14, v13, v14\n" \
"s_nop 1\n" \
"v_permlane32_swap_b32 v13, v14\n" \
"v_max_f32 v14, v13, v14\n" \
"v_pk_add_f32 v[80:81], v[80:81], v[14:15] op_sel_hi:[1,0] neg_lo:[0,1] neg_hi:[0,1]\n" \
"v_pk_add_f32 v[82:83], v[82:83], v[14:15] op_sel_hi:[1,0] neg_lo:[0,1] neg_hi:[0,1]\n" \
"v_pk_add_f32 v[84:85], v[84:85], v[14:15] op_sel_hi:[1,0] neg_lo:[0,1] neg_hi:[0,1]\n" \
"v_pk_add_f32 v[86:87], v[86:87], v[14:15] op_sel_hi:[1,0] neg_lo:[0,1] neg_hi:[0,1]\n" \
"v_pk_add_f32 v[90:91], v[90:91], v[14:15] op_sel_hi:[1,0] neg_lo:[0,1] neg_hi:[0,1]\n" \
"v_exp_f32 v80, v80\n" \
"v_exp_f32 v81, v81\n" \
"v_exp_f32 v82, v82\n" \
"v_exp_f32 v83, v83\n" \
"v_exp_f32 v84, v84\n" \
"v_exp_f32 v85, v85\n" \
"v_exp_f32 v86, v86\n" \
"v_exp_f32 v87, v87\n" \
"v_cvt_pk_bf16_f32 v80, v80, v81\n" \
"v_cvt_pk_bf16_f32 v81, v82, v83\n" \
"v_cvt_pk_bf16_f32 v82, v84, v85\n" \
"v_cvt_pk_bf16_f32 v83, v86, v87\n" \
"v_exp_f32 v90, v90\n" \
"s_nop 0\n" \
"v_mfma_f32_16x16x32_bf16 v[84:87], v[96:99], v[80:83], 0\n" \
"v_exp_f32 v91, v91\n" \
"v_mfma_f32_16x16x32_bf16 v[92:95], v[100:103], v[80:83], 0\n" \
"v_cvt_pk_bf16_f32 v26, v90, v91\n" \
"v_mfma_f32_16x16x32_bf16 v[16:19], v[20:23], v[80:83], 0\n" \
".Lq_vr3_%=:\n" \
"s_nop 7\n" \
"s_nop 7\n" \
"v_mfma_f32_16x16x32_bf16 v[84:87], v[96:99], v[80:83], 0\n" \
"v_mfma_f32_16x16x32_bf16 v[92:95], v[100:103], v[80:83], 0\n" \
"v_mfma_f32_16x16x32_bf16 v[16:19], v[20:23], v[80:83], 0\n" \
"v_mfma_f32_16x16x32_bf16 v[16:19], v[20:23], v[26:29], v[16:19]\n" \
"v_mfma_f32_16x16x32_bf16 v[84:87], v[104:107], v[26:29], v[84:87]\n" \
"v_mfma_f32_16x16x32_bf16 v[92:95], v[104:107], v[24:27], v[92:95]\n" \
"v_rcp_f32 v14, v16\n" \
"s_mov_b64 vcc, 0\n" \
"s_mov_b64 s[26:27], 0\n" \
"s_nop 7\n" \
"s_nop 7\n" \
"s_branch .Lq_vr2_%=\n" \
".Lq_v_cold_%=:\n" \
"s_nop 7\n" \
"s_nop 7\n" \
"s_waitcnt vmcnt(0) lgkmcnt(0)\n" \
"s_mov_b32 s53, 0\n" \
".Lq_v_again_%=:\n" \
"s_cmp_lg_u32 s52, 0\n" \
"s_cbranch_scc1 .Lq_v_ok_%=\n" \
"buffer_load_dword v104, v8, s[36:39], s58 offen offset:0 sc1\n" \
"buffer_load_dword v106, v8, s[36:39], s58 offen offset:256 sc1\n" \
"s_waitcnt vmcnt(0)\n" \
"v_min3_u32 v13, v104, v106, v106\n" \
"v_cmp_eq_u32 vcc, 0, v13\n" \
"s_nop 4\n" \
"s_cbranch_vccz .Lq_v_ok_%=\n" \
"s_add_u32 s53, s53, 1\n" \
"s_cmp_lt_u32 s53, 32768\n" \
"s_cbranch_scc1 .Lq_v_again_%=\n" \
"s_mov_b32 s52, 1\n" \
"v_mov_b32 v13, 256\n" \
"v_mov_b32 v14, 1\n" \
"buffer_store_dword v14, v13, s[36:39], 0 offen sc1\n" \
"s_waitcnt vmcnt(0) lgkmcnt(0)\n" \
"s_branch .Lq_v_ok_%=\n" \
".Lq_f_cold_%=:\n" \
"s_nop 7\n" \
"s_nop 7\n" \
"s_waitcnt vmcnt(0) lgkmcnt(0)\n" \
"s_mov_b32 s53, 0\n" \
".Lq_f_again_%=:\n" \
"s_cmp_lg_u32 s52, 0\n" \
"s_cbranch_scc1 .Lq_f_ok_%=\n" \
"s_sleep 4\n" \
"buffer_load_dword v30, v15, s[36:39], s80 offen sc1\n" \
"s_waitcnt vmcnt(0)\n" \
"v_cmp_ge_u32_e64 s[96:97], s81, v30\n" \
"s_nop 4\n" \
"s_and_b32 s96, s96, s9\n" \
"s_cbranch_scc0 .Lq_f_ok_%=\n" \
"s_add_u32 s53, s53, 1\n" \
"s_cmp_lt_u32 s53, 32768\n" \
"s_cbranch_scc1 .Lq_f_again_%=\n" \
"s_mov_b32 s52, 1\n" \
"v_mov_b32 v30, 256\n" \
"v_mov_b32 v31, 1\n" \
"buffer_store_dword v31, v30, s[36:39], 0 offen sc1\n" \
"s_waitcnt vmcnt(0) lgkmcnt(0)\n" \
"s_branch .Lq_f_ok_%=\n" \
".Lq_v2_cold_%=:\n" \
"s_nop 7\n" \
"s_nop 7\n" \
"s_waitcnt vmcnt(0) lgkmcnt(0)\n" \
"s_mov_b32 s53, 0\n" \
".Lq_v2_again_%=:\n" \
"s_cmp_lg_u32 s52, 0\n" \
"s_cbranch_scc1 .Lq_v2_ok_%=\n" \
"s_sleep 1\n" \
"v_xor_b32 v31, 512, v8\n" \
"buffer_load_dword v104, v8, s[36:39], s61 offen offset:0 sc1\n" \
"buffer_load_dword v106, v8, s[36:39], s61 offen offset:256 sc1\n" \
"buffer_load_dword v108, v31, s[36:39], s61 offen offset:0 sc1\n" \
"buffer_load_dword v110, v31, s[36:39], s61 offen offset:256 sc1\n" \
"s_waitcnt vmcnt(1)\n" \
"v_min3_u32 v13, v104, v106, v108\n" \
"s_waitcnt vmcnt(0)\n" \
"v_min3_u32 v13, v13, v110, v110\n" \
"v_cmp_eq_u32 vcc, 0, v13\n" \
"s_nop 4\n" \
"s_cbranch_vccz .Lq_v2_ok_%=\n" \
"s_add_u32 s53, s53, 1\n" \
"s_cmp_lt_u32 s53, 32768\n" \
"s_cbranch_scc1 .Lq_v2_again_%=\n" \
"s_mov_b32 s52, 1\n" \
"v_mov_b32 v13, 256\n" \
"v_mov_b32 v14, 1\n" \
"buffer_store_dword v14, v13, s[36:39], 0 offen sc1\n" \
"s_waitcnt vmcnt(0) lgkmcnt(0)\n" \
"s_branch .Lq_v2_ok_%=\n" \
".Lq_h_cold_%=:\n" \
"s_nop 7\n" \
"s_nop 7\n" \
"s_waitcnt vmcnt(0) lgkmcnt(0)\n" \
"s_mov_b32 s53, 0\n" \
".Lq_h_again_%=:\n" \
"s_cmp_lg_u32 s52, 0\n" \
"s_cbranch_scc1 .Lq_h_ok_%=\n" \
"s_sleep 1\n" \
"buffer_load_dwordx2 v[64:65], v9, s[36:39], s11 offen sc1\n" \
"s_waitcnt vmcnt(0)\n" \
"v_cmp_eq_u32 vcc, 0, v64\n" \
"s_nop 0\n" \
"s_and_b64 vcc, vcc, s[82:83]\n" \
"s_nop 4\n" \
"s_cbranch_vccz .Lq_h_ok_%=\n" \
"s_add_u32 s53, s53, 1\n" \
"s_cmp_lt_u32 s53, 32768\n" \
"s_cbranch_scc1 .Lq_h_again_%=\n" \
"s_mov_b32 s52, 1\n" \
"v_mov_b32 v18, 256\n" \
"v_mov_b32 v19, 1\n" \
"buffer_store_dword v19, v18, s[36:39], 0 offen sc1\n" \
"s_waitcnt vmcnt(0) lgkmcnt(0)\n" \
"s_branch .Lq_h_ok_%=\n" \
".Lq_retry_%=:\n" \
"s_nop 7\n" \
"s_nop 7\n" \
"s_waitcnt vmcnt(0) lgkmcnt(0)\n" \
"ds_read_b128 v[32:35], v1 offset:0\n" \
"ds_read_b128 v[36:39], v1 offset:1024\n" \
"ds_read_b128 v[40:43], v1 offset:2048\n" \
"ds_read_b128 v[44:47], v1 offset:3072\n" \
"ds_read_b128 v[48:51], v2 offset:32768\n" \
"ds_read_b128 v[52:55], v2 offset:33792\n" \
"ds_read_b128 v[56:59], v2 offset:34816\n" \
"ds_read_b128 v[60:63], v2 offset:35840\n" \
"v_mov_b64 v[80:81], 0\n" \
"v_mov_b64 v[82:83], 0\n" \
"v_mov_b64 v[84:85], 0\n" \
"v_mov_b64 v[86:87], 0\n" \
"v_mov_b64 v[88:89], 0\n" \
"v_mov_b64 v[90:91], 0\n" \
"v_mov_b64 v[92:93], 0\n" \
"v_mov_b64 v[94:95], 0\n" \
"v_mov_b32 v13, -1\n" \
"v_add_u32 v14, s62, v3\n" \
"v_xor_b32 v31, 0x400, v11\n" \
"s_max_u32 s53, s49, 1\n" \
"s_sub_u32 s53, s53, 1\n" \
"v_mov_b32 v30, s53\n" \
"s_mov_b32 m0, s42\n" \
"s_memrealtime s[56:57]\n" \
"s_mov_b32 s54, 100\n" \
"s_waitcnt vmcnt(0) lgkmcnt(0)\n" \
"s_barrier\n" \
"s_add_u32 s88, s88, 1\n" \
"s_cmp_lt_u32 s88, 32768\n" \
"s_cbranch_scc1 .Lq_r1_%=\n" \
"s_mov_b32 s52, 1\n" \
"v_mov_b32 v13, 256\n" \
"v_mov_b32 v14, 1\n" \
"buffer_store_dword v14, v13, s[36:39], 0 offen sc1\n" \
"s_waitcnt vmcnt(0) lgkmcnt(0)\n" \
"s_endpgm\n" \
".Lq_r1_%=:\n" \
"s_cmp_lg_u32 s49, 0\n" \
"s_cbranch_scc1 .Lq_r2_%=\n" \
"s_sleep 8\n" \
".Lq_r2_%=:\n" \
"s_branch .Lq_go_%=\n" \
".Lq_end_%=:\n" \
"s_nop 7\n" \
"s_nop 7\n" \
"v_xor_b32 v31, 0x400, v11\n" \
"buffer_store_dwordx4 v[96:99], v11, s[36:39], s95 offen sc1\n" \
"buffer_store_dwordx4 v[100:103], v31, s[36:39], s95 offen sc1\n" \
"s_waitcnt vmcnt(0) lgkmcnt(0)\n" \
"s_barrier\n" \
"v_mov_b32 v31, 64\n" \
"buffer_store_dword v31, v10, s[36:39], s87 offen sc1\n" \
"s_waitcnt vmcnt(0) lgkmcnt(0)\n" \
".Lq_fin_%=:\n" \
"s_endpgm\n"
#define ASM_VO \
"s_nop 7\n" \
"s_nop 7\n" \
"buffer_load_dword v20, v13, s[68:71], 0 offen offset:0\n" \
"buffer_load_dword v21, v13, s[68:71], 0 offen offset:8\n" \
"buffer_load_dword v22, v13, s[68:71], 0 offen offset:16\n" \
"buffer_load_dword v23, v13, s[68:71], 0 offen offset:24\n" \
"v_mov_b32 v16, 0\n" \
"v_mov_b32 v17, 0\n" \
"v_mov_b32 v18, 0\n" \
"v_mov_b32 v19, 0\n" \
"s_mov_b32 s54, 0\n" \
"buffer_store_dword v16, v7, s[36:39], s54 offen sc1\n" \
"s_mov_b32 s54, 131072\n" \
"buffer_store_dword v16, v7, s[36:39], s54 offen sc1\n" \
"s_mov_b32 s54, 65536\n" \
"buffer_store_dwordx4 v[16:19], v8, s[36:39], s54 offen sc1\n" \
"s_mov_b32 s54, 262144\n" \
"buffer_store_dword v16, v7, s[36:39], s54 offen sc1\n" \
"s_mov_b32 s54, 131072\n" \
"buffer_store_dwordx4 v[16:19], v8, s[36:39], s54 offen sc1\n" \
"s_mov_b32 s54, 393216\n" \
"buffer_store_dword v16, v7, s[36:39], s54 offen sc1\n" \
"s_mov_b32 s54, 196608\n" \
"buffer_store_dwordx4 v[16:19], v8, s[36:39], s54 offen sc1\n" \
"s_mul_i32 s62, s48, 40960\n" \
"v_add_u32 v32, 40960, v3\n" \
"v_add_u32 v33, 81920, v3\n" \
"v_add_u32 v34, 122880, v3\n" \
"s_add_u32 m0, s62, 0\n" \
"s_mov_b32 s54, 0\n" \
"buffer_load_dwordx4 v1, s[40:43], s54 offen nt lds\n" \
"s_add_u32 m0, s62, 1024\n" \
"s_mov_b32 s54, 65536\n" \
"buffer_load_dwordx4 v1, s[40:43], s54 offen nt lds\n" \
"s_add_u32 m0, s62, 2048\n" \
"s_mov_b32 s54, 131072\n" \
"buffer_load_dwordx4 v1, s[40:43], s54 offen nt lds\n" \
"s_add_u32 m0, s62, 3072\n" \
"s_mov_b32 s54, 196608\n" \
"buffer_load_dwordx4 v1, s[40:43], s54 offen nt lds\n" \
"s_add_u32 m0, s62, 4096\n" \
"s_mov_b32 s54, 262144\n" \
"buffer_load_dwordx4 v1, s[40:43], s54 offen nt lds\n" \
"s_add_u32 m0, s62, 5120\n" \
"s_mov_b32 s54, 327680\n" \
"buffer_load_dwordx4 v1, s[40:43], s54 offen nt lds\n" \
"s_add_u32 m0, s62, 6144\n" \
"s_mov_b32 s54, 393216\n" \
"buffer_load_dwordx4 v1, s[40:43], s54 offen nt lds\n" \
"s_add_u32 m0, s62, 7168\n" \
"s_mov_b32 s54, 458752\n" \
"buffer_load_dwordx4 v1, s[40:43], s54 offen nt lds\n" \
"s_add_u32 m0, s62, 8192\n" \
"s_mov_b32 s54, 524288\n" \
"buffer_load_dwordx4 v1, s[40:43], s54 offen nt lds\n" \
"s_add_u32 m0, s62, 9216\n" \
"s_mov_b32 s54, 589824\n" \
"buffer_load_dwordx4 v1, s[40:43], s54 offen nt lds\n" \
"s_add_u32 m0, s62, 10240\n" \
"s_mov_b32 s54, 655360\n" \
"buffer_load_dwordx4 v1, s[40:43], s54 offen nt lds\n" \
"s_add_u32 m0, s62, 11264\n" \
"s_mov_b32 s54, 720896\n" \
"buffer_load_dwordx4 v1, s[40:43], s54 offen nt lds\n" \
"s_add_u32 m0, s62, 12288\n" \
"s_mov_b32 s54, 786432\n" \
"buffer_load_dwordx4 v1, s[40:43], s54 offen nt lds\n" \
"s_add_u32 m0, s62, 13312\n" \
"s_mov_b32 s54, 851968\n" \
"buffer_load_dwordx4 v1, s[40:43], s54 offen nt lds\n" \
"s_add_u32 m0, s62, 14336\n" \
"s_mov_b32 s54, 917504\n" \
"buffer_load_dwordx4 v1, s[40:43], s54 offen nt lds\n" \
"s_add_u32 m0, s62, 15360\n" \
"s_mov_b32 s54, 983040\n" \
"buffer_load_dwordx4 v1, s[40:43], s54 offen nt lds\n" \
"s_add_u32 m0, s62, 16384\n" \
"s_mov_b32 s54, 1048576\n" \
"buffer_load_dwordx4 v1, s[40:43], s54 offen nt lds\n" \
"s_add_u32 m0, s62, 17408\n" \
"s_mov_b32 s54, 1114112\n" \
"buffer_load_dwordx4 v1, s[40:43], s54 offen nt lds\n" \
"s_add_u32 m0, s62, 18432\n" \
"s_mov_b32 s54, 1179648\n" \
"buffer_load_dwordx4 v1, s[40:43], s54 offen nt lds\n" \
"s_add_u32 m0, s62, 19456\n" \
"s_mov_b32 s54, 1245184\n" \
"buffer_load_dwordx4 v1, s[40:43], s54 offen nt lds\n" \
"s_add_u32 m0, s62, 20480\n" \
"s_mov_b32 s54, 1310720\n" \
"buffer_load_dwordx4 v1, s[40:43], s54 offen nt lds\n" \
"s_add_u32 m0, s62, 21504\n" \
"s_mov_b32 s54, 1376256\n" \
"buffer_load_dwordx4 v1, s[40:43], s54 offen nt lds\n" \
"s_add_u32 m0, s62, 22528\n" \
"s_mov_b32 s54, 1441792\n" \
"buffer_load_dwordx4 v1, s[40:43], s54 offen nt lds\n" \
"s_add_u32 m0, s62, 23552\n" \
"s_mov_b32 s54, 1507328\n" \
"buffer_load_dwordx4 v1, s[40:43], s54 offen nt lds\n" \
"s_add_u32 m0, s62, 24576\n" \
"s_mov_b32 s54, 1572864\n" \
"buffer_load_dwordx4 v1, s[40:43], s54 offen nt lds\n" \
"s_add_u32 m0, s62, 25600\n" \
"s_mov_b32 s54, 1638400\n" \
"buffer_load_dwordx4 v1, s[40:43], s54 offen nt lds\n" \
"s_add_u32 m0, s62, 26624\n" \
"s_mov_b32 s54, 1703936\n" \
"buffer_load_dwordx4 v1, s[40:43], s54 offen nt lds\n" \
"s_add_u32 m0, s62, 27648\n" \
"s_mov_b32 s54, 1769472\n" \
"buffer_load_dwordx4 v1, s[40:43], s54 offen nt lds\n" \
"s_add_u32 m0, s62, 28672\n" \
"s_mov_b32 s54, 1835008\n" \
"buffer_load_dwordx4 v1, s[40:43], s54 offen nt lds\n" \
"s_add_u32 m0, s62, 29696\n" \
"s_mov_b32 s54, 1900544\n" \
"buffer_load_dwordx4 v1, s[40:43], s54 offen nt lds\n" \
"s_add_u32 m0, s62, 30720\n" \
"s_mov_b32 s54, 1966080\n" \
"buffer_load_dwordx4 v1, s[40:43], s54 offen nt lds\n" \
"s_add_u32 m0, s62, 31744\n" \
"s_mov_b32 s54, 2031616\n" \
"buffer_load_dwordx4 v1, s[40:43], s54 offen nt lds\n" \
"s_add_u32 m0, s62, 32768\n" \
"s_mov_b32 s54, 0\n" \
"buffer_load_dwordx4 v2, s[64:67], s54 offen lds\n" \
"s_add_u32 m0, s62, 33792\n" \
"s_mov_b32 s54, 32768\n" \
"buffer_load_dwordx4 v2, s[64:67], s54 offen lds\n" \
"s_add_u32 m0, s62, 34816\n" \
"s_mov_b32 s54, 65536\n" \
"buffer_load_dwordx4 v2, s[64:67], s54 offen lds\n" \
"s_add_u32 m0, s62, 35840\n" \
"s_mov_b32 s54, 98304\n" \
"buffer_load_dwordx4 v2, s[64:67], s54 offen lds\n" \
"s_add_u32 m0, s62, 36864\n" \
"s_mov_b32 s54, 131072\n" \
"buffer_load_dwordx4 v2, s[64:67], s54 offen lds\n" \
"s_add_u32 m0, s62, 37888\n" \
"s_mov_b32 s54, 163840\n" \
"buffer_load_dwordx4 v2, s[64:67], s54 offen lds\n" \
"s_add_u32 m0, s62, 38912\n" \
"s_mov_b32 s54, 196608\n" \
"buffer_load_dwordx4 v2, s[64:67], s54 offen lds\n" \
"s_add_u32 m0, s62, 39936\n" \
"s_mov_b32 s54, 229376\n" \
"buffer_load_dwordx4 v2, s[64:67], s54 offen lds\n" \
"s_waitcnt vmcnt(32)\n" \
"s_barrier\n" \
"ds_read_b64_tr_b16 v[128:129], v3 offset:0\n" \
"ds_read_b64_tr_b16 v[130:131], v3 offset:512\n" \
"ds_read_b64_tr_b16 v[132:133], v3 offset:4096\n" \
"ds_read_b64_tr_b16 v[134:135], v3 offset:4608\n" \
"ds_read_b64_tr_b16 v[160:161], v32 offset:0\n" \
"ds_read_b64_tr_b16 v[162:163], v32 offset:512\n" \
"ds_read_b64_tr_b16 v[164:165], v32 offset:4096\n" \
"ds_read_b64_tr_b16 v[166:167], v32 offset:4608\n" \
"ds_read_b64_tr_b16 v[192:193], v33 offset:0\n" \
"ds_read_b64_tr_b16 v[194:195], v33 offset:512\n" \
"ds_read_b64_tr_b16 v[196:197], v33 offset:4096\n" \
"ds_read_b64_tr_b16 v[198:199], v33 offset:4608\n" \
"ds_read_b64_tr_b16 v[224:225], v34 offset:0\n" \
"ds_read_b64_tr_b16 v[226:227], v34 offset:512\n" \
"ds_read_b64_tr_b16 v[228:229], v34 offset:4096\n" \
"ds_read_b64_tr_b16 v[230:231], v34 offset:4608\n" \
"s_waitcnt lgkmcnt(0)\n" \
"s_barrier\n" \
"s_add_u32 m0, s62, 0\n" \
"s_mov_b32 s54, 262144\n" \
"buffer_load_dwordx4 v2, s[64:67], s54 offen lds\n" \
"s_add_u32 m0, s62, 1024\n" \
"s_mov_b32 s54, 294912\n" \
"buffer_load_dwordx4 v2, s[64:67], s54 offen lds\n" \
"s_add_u32 m0, s62, 2048\n" \
"s_mov_b32 s54, 327680\n" \
"buffer_load_dwordx4 v2, s[64:67], s54 offen lds\n" \
"s_add_u32 m0, s62, 3072\n" \
"s_mov_b32 s54, 360448\n" \
"buffer_load_dwordx4 v2, s[64:67], s54 offen lds\n" \
"s_add_u32 m0, s62, 4096\n" \
"s_mov_b32 s54, 393216\n" \
"buffer_load_dwordx4 v2, s[64:67], s54 offen lds\n" \
"s_add_u32 m0, s62, 5120\n" \
"s_mov_b32 s54, 425984\n" \
"buffer_load_dwordx4 v2, s[64:67], s54 offen lds\n" \
"s_add_u32 m0, s62, 6144\n" \
"s_mov_b32 s54, 458752\n" \
"buffer_load_dwordx4 v2, s[64:67], s54 offen lds\n" \
"s_add_u32 m0, s62, 7168\n" \
"s_mov_b32 s54, 491520\n" \
"buffer_load_dwordx4 v2, s[64:67], s54 offen lds\n" \
"s_waitcnt vmcnt(32)\n" \
"s_barrier\n" \
"ds_read_b64_tr_b16 v[136:137], v3 offset:8192\n" \
"ds_read_b64_tr_b16 v[138:139], v3 offset:8704\n" \
"ds_read_b64_tr_b16 v[140:141], v3 offset:12288\n" \
"ds_read_b64_tr_b16 v[142:143], v3 offset:12800\n" \
"ds_read_b64_tr_b16 v[168:169], v32 offset:8192\n" \
"ds_read_b64_tr_b16 v[170:171], v32 offset:8704\n" \
"ds_read_b64_tr_b16 v[172:173], v32 offset:12288\n" \
"ds_read_b64_tr_b16 v[174:175], v32 offset:12800\n" \
"ds_read_b64_tr_b16 v[200:201], v33 offset:8192\n" \
"ds_read_b64_tr_b16 v[202:203], v33 offset:8704\n" \
"ds_read_b64_tr_b16 v[204:205], v33 offset:12288\n" \
"ds_read_b64_tr_b16 v[206:207], v33 offset:12800\n" \
"ds_read_b64_tr_b16 v[232:233], v34 offset:8192\n" \
"ds_read_b64_tr_b16 v[234:235], v34 offset:8704\n" \
"ds_read_b64_tr_b16 v[236:237], v34 offset:12288\n" \
"ds_read_b64_tr_b16 v[238:239], v34 offset:12800\n" \
"s_waitcnt lgkmcnt(0)\n" \
"s_barrier\n" \
"s_add_u32 m0, s62, 8192\n" \
"s_mov_b32 s54, 524288\n" \
"buffer_load_dwordx4 v2, s[64:67], s54 offen lds\n" \
"s_add_u32 m0, s62, 9216\n" \
"s_mov_b32 s54, 557056\n" \
"buffer_load_dwordx4 v2, s[64:67], s54 offen lds\n" \
"s_add_u32 m0, s62, 10240\n" \
"s_mov_b32 s54, 589824\n" \
"buffer_load_dwordx4 v2, s[64:67], s54 offen lds\n" \
"s_add_u32 m0, s62, 11264\n" \
"s_mov_b32 s54, 622592\n" \
"buffer_load_dwordx4 v2, s[64:67], s54 offen lds\n" \
"s_add_u32 m0, s62, 12288\n" \
"s_mov_b32 s54, 655360\n" \
"buffer_load_dwordx4 v2, s[64:67], s54 offen lds\n" \
"s_add_u32 m0, s62, 13312\n" \
"s_mov_b32 s54, 688128\n" \
"buffer_load_dwordx4 v2, s[64:67], s54 offen lds\n" \
"s_add_u32 m0, s62, 14336\n" \
"s_mov_b32 s54, 720896\n" \
"buffer_load_dwordx4 v2, s[64:67], s54 offen lds\n" \
"s_add_u32 m0, s62, 15360\n" \
"s_mov_b32 s54, 753664\n" \
"buffer_load_dwordx4 v2, s[64:67], s54 offen lds\n" \
"s_waitcnt vmcnt(32)\n" \
"s_barrier\n" \
"ds_read_b64_tr_b16 v[144:145], v3 offset:16384\n" \
"ds_read_b64_tr_b16 v[146:147], v3 offset:16896\n" \
"ds_read_b64_tr_b16 v[148:149], v3 offset:20480\n" \
"ds_read_b64_tr_b16 v[150:151], v3 offset:20992\n" \
"ds_read_b64_tr_b16 v[176:177], v32 offset:16384\n" \
"ds_read_b64_tr_b16 v[178:179], v32 offset:16896\n" \
"ds_read_b64_tr_b16 v[180:181], v32 offset:20480\n" \
"ds_read_b64_tr_b16 v[182:183], v32 offset:20992\n" \
"ds_read_b64_tr_b16 v[208:209], v33 offset:16384\n" \
"ds_read_b64_tr_b16 v[210:211], v33 offset:16896\n" \
"ds_read_b64_tr_b16 v[212:213], v33 offset:20480\n" \
"ds_read_b64_tr_b16 v[214:215], v33 offset:20992\n" \
"ds_read_b64_tr_b16 v[240:241], v34 offset:16384\n" \
"ds_read_b64_tr_b16 v[242:243], v34 offset:16896\n" \
"ds_read_b64_tr_b16 v[244:245], v34 offset:20480\n" \
"ds_read_b64_tr_b16 v[246:247], v34 offset:20992\n" \
"s_waitcnt lgkmcnt(0)\n" \
"s_barrier\n" \
"s_add_u32 m0, s62, 16384\n" \
"s_mov_b32 s54, 786432\n" \
"buffer_load_dwordx4 v2, s[64:67], s54 offen lds\n" \
"s_add_u32 m0, s62, 17408\n" \
"s_mov_b32 s54, 819200\n" \
"buffer_load_dwordx4 v2, s[64:67], s54 offen lds\n" \
"s_add_u32 m0, s62, 18432\n" \
"s_mov_b32 s54, 851968\n" \
"buffer_load_dwordx4 v2, s[64:67], s54 offen lds\n" \
"s_add_u32 m0, s62, 19456\n" \
"s_mov_b32 s54, 884736\n" \
"buffer_load_dwordx4 v2, s[64:67], s54 offen lds\n" \
"s_add_u32 m0, s62, 20480\n" \
"s_mov_b32 s54, 917504\n" \
"buffer_load_dwordx4 v2, s[64:67], s54 offen lds\n" \
"s_add_u32 m0, s62, 21504\n" \
"s_mov_b32 s54, 950272\n" \
"buffer_load_dwordx4 v2, s[64:67], s54 offen lds\n" \
"s_add_u32 m0, s62, 22528\n" \
"s_mov_b32 s54, 983040\n" \
"buffer_load_dwordx4 v2, s[64:67], s54 offen lds\n" \
"s_add_u32 m0, s62, 23552\n" \
"s_mov_b32 s54, 1015808\n" \
"buffer_load_dwordx4 v2, s[64:67], s54 offen lds\n" \
"s_waitcnt vmcnt(32)\n" \
"s_barrier\n" \
"ds_read_b64_tr_b16 v[152:153], v3 offset:24576\n" \
"ds_read_b64_tr_b16 v[154:155], v3 offset:25088\n" \
"ds_read_b64_tr_b16 v[156:157], v3 offset:28672\n" \
"ds_read_b64_tr_b16 v[158:159], v3 offset:29184\n" \
"ds_read_b64_tr_b16 v[184:185], v32 offset:24576\n" \
"ds_read_b64_tr_b16 v[186:187], v32 offset:25088\n" \
"ds_read_b64_tr_b16 v[188:189], v32 offset:28672\n" \
"ds_read_b64_tr_b16 v[190:191], v32 offset:29184\n" \
"ds_read_b64_tr_b16 v[216:217], v33 offset:24576\n" \
"ds_read_b64_tr_b16 v[218:219], v33 offset:25088\n" \
"ds_read_b64_tr_b16 v[220:221], v33 offset:28672\n" \
"ds_read_b64_tr_b16 v[222:223], v33 offset:29184\n" \
"ds_read_b64_tr_b16 v[248:249], v34 offset:24576\n" \
"ds_read_b64_tr_b16 v[250:251], v34 offset:25088\n" \
"ds_read_b64_tr_b16 v[252:253], v34 offset:28672\n" \
"ds_read_b64_tr_b16 v[254:255], v34 offset:29184\n" \
"s_waitcnt lgkmcnt(0)\n" \
"s_barrier\n" \
"s_add_u32 m0, s62, 24576\n" \
"s_mov_b32 s54, 1048576\n" \
"buffer_load_dwordx4 v2, s[64:67], s54 offen lds\n" \
"s_add_u32 m0, s62, 25600\n" \
"s_mov_b32 s54, 1081344\n" \
"buffer_load_dwordx4 v2, s[64:67], s54 offen lds\n" \
"s_add_u32 m0, s62, 26624\n" \
"s_mov_b32 s54, 1114112\n" \
"buffer_load_dwordx4 v2, s[64:67], s54 offen lds\n" \
"s_add_u32 m0, s62, 27648\n" \
"s_mov_b32 s54, 1146880\n" \
"buffer_load_dwordx4 v2, s[64:67], s54 offen lds\n" \
"s_add_u32 m0, s62, 28672\n" \
"s_mov_b32 s54, 1179648\n" \
"buffer_load_dwordx4 v2, s[64:67], s54 offen lds\n" \
"s_add_u32 m0, s62, 29696\n" \
"s_mov_b32 s54, 1212416\n" \
"buffer_load_dwordx4 v2, s[64:67], s54 offen lds\n" \
"s_add_u32 m0, s62, 30720\n" \
"s_mov_b32 s54, 1245184\n" \
"buffer_load_dwordx4 v2, s[64:67], s54 offen lds\n" \
"s_add_u32 m0, s62, 31744\n" \
"s_mov_b32 s54, 1277952\n" \
"buffer_load_dwordx4 v2, s[64:67], s54 offen lds\n" \
"s_waitcnt vmcnt(36)\n" \
"ds_read_b64_tr_b16 a[0:1], v4 offset:32768\n" \
"ds_read_b64_tr_b16 a[2:3], v4 offset:32896\n" \
"ds_read_b64_tr_b16 a[4:5], v4 offset:34816\n" \
"ds_read_b64_tr_b16 a[6:7], v4 offset:34944\n" \
"ds_read_b64_tr_b16 a[8:9], v4 offset:32800\n" \
"ds_read_b64_tr_b16 a[10:11], v4 offset:32928\n" \
"ds_read_b64_tr_b16 a[12:13], v4 offset:34848\n" \
"ds_read_b64_tr_b16 a[14:15], v4 offset:34976\n" \
"s_waitcnt lgkmcnt(0)\n" \
"s_add_u32 m0, s62, 32768\n" \
"s_mov_b32 s54, 1310720\n" \
"buffer_load_dwordx4 v2, s[64:67], s54 offen lds\n" \
"s_add_u32 m0, s62, 33792\n" \
"s_mov_b32 s54, 1343488\n" \
"buffer_load_dwordx4 v2, s[64:67], s54 offen lds\n" \
"s_add_u32 m0, s62, 34816\n" \
"s_mov_b32 s54, 1376256\n" \
"buffer_load_dwordx4 v2, s[64:67], s54 offen lds\n" \
"s_add_u32 m0, s62, 35840\n" \
"s_mov_b32 s54, 1409024\n" \
"buffer_load_dwordx4 v2, s[64:67], s54 offen lds\n" \
"s_waitcnt vmcnt(36)\n" \
"ds_read_b64_tr_b16 a[16:17], v4 offset:36864\n" \
"ds_read_b64_tr_b16 a[18:19], v4 offset:36992\n" \
"ds_read_b64_tr_b16 a[20:21], v4 offset:38912\n" \
"ds_read_b64_tr_b16 a[22:23], v4 offset:39040\n" \
"ds_read_b64_tr_b16 a[24:25], v4 offset:36896\n" \
"ds_read_b64_tr_b16 a[26:27], v4 offset:37024\n" \
"ds_read_b64_tr_b16 a[28:29], v4 offset:38944\n" \
"ds_read_b64_tr_b16 a[30:31], v4 offset:39072\n" \
"s_waitcnt lgkmcnt(0)\n" \
"s_add_u32 m0, s62, 36864\n" \
"s_mov_b32 s54, 1441792\n" \
"buffer_load_dwordx4 v2, s[64:67], s54 offen lds\n" \
"s_add_u32 m0, s62, 37888\n" \
"s_mov_b32 s54, 1474560\n" \
"buffer_load_dwordx4 v2, s[64:67], s54 offen lds\n" \
"s_add_u32 m0, s62, 38912\n" \
"s_mov_b32 s54, 1507328\n" \
"buffer_load_dwordx4 v2, s[64:67], s54 offen lds\n" \
"s_add_u32 m0, s62, 39936\n" \
"s_mov_b32 s54, 1540096\n" \
"buffer_load_dwordx4 v2, s[64:67], s54 offen lds\n" \
"s_waitcnt vmcnt(36)\n" \
"ds_read_b64_tr_b16 a[32:33], v4 offset:0\n" \
"ds_read_b64_tr_b16 a[34:35], v4 offset:128\n" \
"ds_read_b64_tr_b16 a[36:37], v4 offset:2048\n" \
"ds_read_b64_tr_b16 a[38:39], v4 offset:2176\n" \
"ds_read_b64_tr_b16 a[40:41], v4 offset:32\n" \
"ds_read_b64_tr_b16 a[42:43], v4 offset:160\n" \
"ds_read_b64_tr_b16 a[44:45], v4 offset:2080\n" \
"ds_read_b64_tr_b16 a[46:47], v4 offset:2208\n" \
"s_waitcnt lgkmcnt(0)\n" \
"s_add_u32 m0, s62, 0\n" \
"s_mov_b32 s54, 1572864\n" \
"buffer_load_dwordx4 v2, s[64:67], s54 offen lds\n" \
"s_add_u32 m0, s62, 1024\n" \
"s_mov_b32 s54, 1605632\n" \
"buffer_load_dwordx4 v2, s[64:67], s54 offen lds\n" \
"s_add_u32 m0, s62, 2048\n" \
"s_mov_b32 s54, 1638400\n" \
"buffer_load_dwordx4 v2, s[64:67], s54 offen lds\n" \
"s_add_u32 m0, s62, 3072\n" \
"s_mov_b32 s54, 1671168\n" \
"buffer_load_dwordx4 v2, s[64:67], s54 offen lds\n" \
"s_waitcnt vmcnt(36)\n" \
"ds_read_b64_tr_b16 a[48:49], v4 offset:4096\n" \
"ds_read_b64_tr_b16 a[50:51], v4 offset:4224\n" \
"ds_read_b64_tr_b16 a[52:53], v4 offset:6144\n" \
"ds_read_b64_tr_b16 a[54:55], v4 offset:6272\n" \
"ds_read_b64_tr_b16 a[56:57], v4 offset:4128\n" \
"ds_read_b64_tr_b16 a[58:59], v4 offset:4256\n" \
"ds_read_b64_tr_b16 a[60:61], v4 offset:6176\n" \
"ds_read_b64_tr_b16 a[62:63], v4 offset:6304\n" \
"s_waitcnt lgkmcnt(0)\n" \
"s_add_u32 m0, s62, 4096\n" \
"s_mov_b32 s54, 1703936\n" \
"buffer_load_dwordx4 v2, s[64:67], s54 offen lds\n" \
"s_add_u32 m0, s62, 5120\n" \
"s_mov_b32 s54, 1736704\n" \
"buffer_load_dwordx4 v2, s[64:67], s54 offen lds\n" \
"s_add_u32 m0, s62, 6144\n" \
"s_mov_b32 s54, 1769472\n" \
"buffer_load_dwordx4 v2, s[64:67], s54 offen lds\n" \
"s_add_u32 m0, s62, 7168\n" \
"s_mov_b32 s54, 1802240\n" \
"buffer_load_dwordx4 v2, s[64:67], s54 offen lds\n" \
"s_waitcnt vmcnt(36)\n" \
"ds_read_b64_tr_b16 a[64:65], v4 offset:8192\n" \
"ds_read_b64_tr_b16 a[66:67], v4 offset:8320\n" \
"ds_read_b64_tr_b16 a[68:69], v4 offset:10240\n" \
"ds_read_b64_tr_b16 a[70:71], v4 offset:10368\n" \
"ds_read_b64_tr_b16 a[72:73], v4 offset:8224\n" \
"ds_read_b64_tr_b16 a[74:75], v4 offset:8352\n" \
"ds_read_b64_tr_b16 a[76:77], v4 offset:10272\n" \
"ds_read_b64_tr_b16 a[78:79], v4 offset:10400\n" \
"s_waitcnt lgkmcnt(0)\n" \
"s_add_u32 m0, s62, 8192\n" \
"s_mov_b32 s54, 1835008\n" \
"buffer_load_dwordx4 v2, s[64:67], s54 offen lds\n" \
"s_add_u32 m0, s62, 9216\n" \
"s_mov_b32 s54, 1867776\n" \
"buffer_load_dwordx4 v2, s[64:67], s54 offen lds\n" \
"s_add_u32 m0, s62, 10240\n" \
"s_mov_b32 s54, 1900544\n" \
"buffer_load_dwordx4 v2, s[64:67], s54 offen lds\n" \
"s_add_u32 m0, s62, 11264\n" \
"s_mov_b32 s54, 1933312\n" \
"buffer_load_dwordx4 v2, s[64:67], s54 offen lds\n" \
"s_waitcnt vmcnt(36)\n" \
"ds_read_b64_tr_b16 a[80:81], v4 offset:12288\n" \
"ds_read_b64_tr_b16 a[82:83], v4 offset:12416\n" \
"ds_read_b64_tr_b16 a[84:85], v4 offset:14336\n" \
"ds_read_b64_tr_b16 a[86:87], v4 offset:14464\n" \
"ds_read_b64_tr_b16 a[88:89], v4 offset:12320\n" \
"ds_read_b64_tr_b16 a[90:91], v4 offset:12448\n" \
"ds_read_b64_tr_b16 a[92:93], v4 offset:14368\n" \
"ds_read_b64_tr_b16 a[94:95], v4 offset:14496\n" \
"s_waitcnt lgkmcnt(0)\n" \
"s_add_u32 m0, s62, 12288\n" \
"s_mov_b32 s54, 1966080\n" \
"buffer_load_dwordx4 v2, s[64:67], s54 offen lds\n" \
"s_add_u32 m0, s62, 13312\n" \
"s_mov_b32 s54, 1998848\n" \
"buffer_load_dwordx4 v2, s[64:67], s54 offen lds\n" \
"s_add_u32 m0, s62, 14336\n" \
"s_mov_b32 s54, 2031616\n" \
"buffer_load_dwordx4 v2, s[64:67], s54 offen lds\n" \
"s_add_u32 m0, s62, 15360\n" \
"s_mov_b32 s54, 2064384\n" \
"buffer_load_dwordx4 v2, s[64:67], s54 offen lds\n" \
"s_waitcnt vmcnt(36)\n" \
"ds_read_b64_tr_b16 a[96:97], v4 offset:16384\n" \
"ds_read_b64_tr_b16 a[98:99], v4 offset:16512\n" \
"ds_read_b64_tr_b16 a[100:101], v4 offset:18432\n" \
"ds_read_b64_tr_b16 a[102:103], v4 offset:18560\n" \
"ds_read_b64_tr_b16 a[104:105], v4 offset:16416\n" \
"ds_read_b64_tr_b16 a[106:107], v4 offset:16544\n" \
"ds_read_b64_tr_b16 a[108:109], v4 offset:18464\n" \
"ds_read_b64_tr_b16 a[110:111], v4 offset:18592\n" \
"s_waitcnt vmcnt(32)\n" \
"ds_read_b64_tr_b16 a[112:113], v4 offset:20480\n" \
"ds_read_b64_tr_b16 a[114:115], v4 offset:20608\n" \
"ds_read_b64_tr_b16 a[116:117], v4 offset:22528\n" \
"ds_read_b64_tr_b16 a[118:119], v4 offset:22656\n" \
"ds_read_b64_tr_b16 a[120:121], v4 offset:20512\n" \
"ds_read_b64_tr_b16 a[122:123], v4 offset:20640\n" \
"ds_read_b64_tr_b16 a[124:125], v4 offset:22560\n" \
"ds_read_b64_tr_b16 a[126:127], v4 offset:22688\n" \
"s_waitcnt vmcnt(28)\n" \
"ds_read_b64_tr_b16 a[128:129], v4 offset:24576\n" \
"ds_read_b64_tr_b16 a[130:131], v4 offset:24704\n" \
"ds_read_b64_tr_b16 a[132:133], v4 offset:26624\n" \
"ds_read_b64_tr_b16 a[134:135], v4 offset:26752\n" \
"ds_read_b64_tr_b16 a[136:137], v4 offset:24608\n" \
"ds_read_b64_tr_b16 a[138:139], v4 offset:24736\n" \
"ds_read_b64_tr_b16 a[140:141], v4 offset:26656\n" \
"ds_read_b64_tr_b16 a[142:143], v4 offset:26784\n" \
"s_waitcnt vmcnt(24)\n" \
"ds_read_b64_tr_b16 a[144:145], v4 offset:28672\n" \
"ds_read_b64_tr_b16 a[146:147], v4 offset:28800\n" \
"ds_read_b64_tr_b16 a[148:149], v4 offset:30720\n" \
"ds_read_b64_tr_b16 a[150:151], v4 offset:30848\n" \
"ds_read_b64_tr_b16 a[152:153], v4 offset:28704\n" \
"ds_read_b64_tr_b16 a[154:155], v4 offset:28832\n" \
"ds_read_b64_tr_b16 a[156:157], v4 offset:30752\n" \
"ds_read_b64_tr_b16 a[158:159], v4 offset:30880\n" \
"s_waitcnt vmcnt(20)\n" \
"ds_read_b64_tr_b16 a[160:161], v4 offset:32768\n" \
"ds_read_b64_tr_b16 a[162:163], v4 offset:32896\n" \
"ds_read_b64_tr_b16 a[164:165], v4 offset:34816\n" \
"ds_read_b64_tr_b16 a[166:167], v4 offset:34944\n" \
"ds_read_b64_tr_b16 a[168:169], v4 offset:32800\n" \
"ds_read_b64_tr_b16 a[170:171], v4 offset:32928\n" \
"ds_read_b64_tr_b16 a[172:173], v4 offset:34848\n" \
"ds_read_b64_tr_b16 a[174:175], v4 offset:34976\n" \
"s_waitcnt vmcnt(16)\n" \
"ds_read_b64_tr_b16 a[176:177], v4 offset:36864\n" \
"ds_read_b64_tr_b16 a[178:179], v4 offset:36992\n" \
"ds_read_b64_tr_b16 a[180:181], v4 offset:38912\n" \
"ds_read_b64_tr_b16 a[182:183], v4 offset:39040\n" \
"ds_read_b64_tr_b16 a[184:185], v4 offset:36896\n" \
"ds_read_b64_tr_b16 a[186:187], v4 offset:37024\n" \
"ds_read_b64_tr_b16 a[188:189], v4 offset:38944\n" \
"ds_read_b64_tr_b16 a[190:191], v4 offset:39072\n" \
"s_waitcnt vmcnt(12)\n" \
"ds_read_b64_tr_b16 a[192:193], v4 offset:0\n" \
"ds_read_b64_tr_b16 a[194:195], v4 offset:128\n" \
"ds_read_b64_tr_b16 a[196:197], v4 offset:2048\n" \
"ds_read_b64_tr_b16 a[198:199], v4 offset:2176\n" \
"ds_read_b64_tr_b16 a[200:201], v4 offset:32\n" \
"ds_read_b64_tr_b16 a[202:203], v4 offset:160\n" \
"ds_read_b64_tr_b16 a[204:205], v4 offset:2080\n" \
"ds_read_b64_tr_b16 a[206:207], v4 offset:2208\n" \
"s_waitcnt vmcnt(8)\n" \
"ds_read_b64_tr_b16 a[208:209], v4 offset:4096\n" \
"ds_read_b64_tr_b16 a[210:211], v4 offset:4224\n" \
"ds_read_b64_tr_b16 a[212:213], v4 offset:6144\n" \
"ds_read_b64_tr_b16 a[214:215], v4 offset:6272\n" \
"ds_read_b64_tr_b16 a[216:217], v4 offset:4128\n" \
"ds_read_b64_tr_b16 a[218:219], v4 offset:4256\n" \
"ds_read_b64_tr_b16 a[220:221], v4 offset:6176\n" \
"ds_read_b64_tr_b16 a[222:223], v4 offset:6304\n" \
"s_waitcnt vmcnt(4)\n" \
"ds_read_b64_tr_b16 a[224:225], v4 offset:8192\n" \
"ds_read_b64_tr_b16 a[226:227], v4 offset:8320\n" \
"ds_read_b64_tr_b16 a[228:229], v4 offset:10240\n" \
"ds_read_b64_tr_b16 a[230:231], v4 offset:10368\n" \
"ds_read_b64_tr_b16 a[232:233], v4 offset:8224\n" \
"ds_read_b64_tr_b16 a[234:235], v4 offset:8352\n" \
"ds_read_b64_tr_b16 a[236:237], v4 offset:10272\n" \
"ds_read_b64_tr_b16 a[238:239], v4 offset:10400\n" \
"s_waitcnt vmcnt(0)\n" \
"ds_read_b64_tr_b16 a[240:241], v4 offset:12288\n" \
"ds_read_b64_tr_b16 a[242:243], v4 offset:12416\n" \
"ds_read_b64_tr_b16 a[244:245], v4 offset:14336\n" \
"ds_read_b64_tr_b16 a[246:247], v4 offset:14464\n" \
"ds_read_b64_tr_b16 a[248:249], v4 offset:12320\n" \
"ds_read_b64_tr_b16 a[250:251], v4 offset:12448\n" \
"ds_read_b64_tr_b16 a[252:253], v4 offset:14368\n" \
"ds_read_b64_tr_b16 a[254:255], v4 offset:14496\n" \
"s_waitcnt vmcnt(0) lgkmcnt(0)\n" \
"s_barrier\n" \
"s_getpc_b64 s[18:19]\n" \
".Lv_pc_%=:\n" \
"v_mbcnt_lo_u32_b32 v40, -1, 0\n" \
"v_mbcnt_hi_u32_b32 v40, -1, v40\n" \
"v_lshl_add_u32 v40, s48, 6, v40\n" \
"v_lshlrev_b32 v40, 6, v40\n" \
"v_min_u32 v40, .Lv_fin_%=-.Lv_pc_%=, v40\n" \
"global_load_dword v41, v40, s[18:19]\n" \
"s_mov_b32 s49, 0\n" \
"s_mov_b32 s52, 0\n" \
"s_mov_b32 s88, 0\n" \
"s_mov_b32 s76, 0\n" \
"s_mov_b32 s22, 0xfffffc00\n" \
"s_mov_b32 s23, -1\n" \
"s_mul_i32 s32, s17, 1\n" \
"s_mov_b32 s33, s32\n" \
"s_mov_b32 s34, s17\n" \
"v_max_u32 v20, 1, v20\n" \
"v_max_u32 v21, 1, v21\n" \
"v_max_u32 v22, 1, v22\n" \
"v_max_u32 v23, 1, v23\n" \
"buffer_store_dwordx4 v[20:23], v8, s[36:39], 0 offen sc1\n" \
"s_mov_b32 s72, 0\n" \
"s_mov_b32 s73, -1\n" \
"v_lshlrev_b32 v31, 1, v0\n" \
"v_add_u32 v31, 16384, v31\n" \
"s_lshl_b32 s79, s48, 10\n" \
"s_add_u32 s79, s79, 0\n" \
"v_xor_b32 v30, 0x1000, v6\n" \
"s_memrealtime s[56:57]\n" \
"s_waitcnt lgkmcnt(0)\n" \
"s_mov_b32 s50, s56\n" \
"s_waitcnt vmcnt(0) lgkmcnt(0)\n" \
"s_add_u32 s54, s49, 0\n" \
"s_and_b32 s54, s54, 3\n" \
"s_mul_i32 s58, s54, 131072\n" \
"s_add_u32 s54, s49, 1\n" \
"s_and_b32 s54, s54, 3\n" \
"s_mul_i32 s59, s54, 131072\n" \
"s_add_u32 s54, s49, 2\n" \
"s_and_b32 s54, s54, 3\n" \
"s_mul_i32 s60, s54, 131072\n" \
"s_add_u32 s54, s49, 3\n" \
"s_and_b32 s54, s54, 3\n" \
"s_mul_i32 s61, s54, 131072\n" \
"s_mov_b32 s77, 0\n" \
"s_add_u32 s78, s77, s63\n" \
"s_xor_b32 s89, s78, 0x4800\n" \
"s_xor_b32 s90, s78, 0x800\n" \
"s_xor_b32 s91, s78, 0x4000\n" \
"s_mov_b32 m0, s79\n" \
"s_add_u32 s51, s50, s46\n" \
"v_mov_b64 v[80:81], 0\n" \
"v_mov_b64 v[82:83], 0\n" \
"v_mov_b32 v13, -1\n" \
"s_mov_b32 s23, -1\n" \
"s_cmp_eq_u32 s48, 0\n" \
"s_cselect_b32 s80, -1, 0\n" \
"s_mov_b32 s81, 0\n" \
"s_nop 7\n" \
"s_nop 7\n" \
".p2align 6\n" \
".Lv_loop_%=:\n" \
"s_getpc_b64 s[18:19]\n" \
".Lv_wp_%=:\n" \
"s_add_u32 s18, s18, .Lv_ws_%=-.Lv_wp_%=\n" \
"s_addc_u32 s19, s19, 0\n" \
"s_sub_u32 s21, s51, 5\n" \
"s_nop 0\n" \
".Lv_w_%=:\n" \
"s_memrealtime s[56:57]\n" \
"s_waitcnt lgkmcnt(0)\n" \
"s_sub_u32 s54, s56, s21\n" \
"s_cmp_gt_u32 s54, s22\n" \
"s_cbranch_scc1 .Lv_w_%=\n" \
"s_min_u32 s21, s54, 3\n" \
"s_lshl2_add_u32 s18, s21, s18\n" \
"s_setpc_b64 s[18:19]\n" \
"s_nop 0\n" \
".Lv_ws_%=:\n" \
"s_nop 4\n" \
"s_nop 5\n" \
"s_nop 4\n" \
".Lv_w4_%=:\n" \
"s_barrier\n" \
".Lv_go_%=:\n" \
"buffer_load_dwordx4 v5, s[36:39], s78 offen sc1 lds\n" \
"s_nop 7\n" \
"s_add_u32 m0, s79, 4096\n" \
"s_nop 0\n" \
"buffer_load_dwordx4 v5, s[36:39], s89 offen sc1 lds\n" \
"s_nop 7\n" \
"s_add_u32 m0, s79, 8192\n" \
"s_nop 0\n" \
"buffer_load_dwordx4 v5, s[36:39], s90 offen sc1 lds\n" \
"s_nop 7\n" \
"s_add_u32 m0, s79, 12288\n" \
"s_nop 0\n" \
"buffer_load_dwordx4 v5, s[36:39], s91 offen sc1 lds\n" \
".Lv_x_%=:\n" \
"s_waitcnt vmcnt(2)\n" \
"s_barrier\n" \
"ds_read_b128 v[32:35], v0 offset:0\n" \
"ds_read_b128 v[36:39], v0 offset:1024\n" \
"ds_read_b128 v[40:43], v0 offset:2048\n" \
"ds_read_b128 v[44:47], v0 offset:3072\n" \
"ds_read_b128 v[48:51], v0 offset:4096\n" \
"s_waitcnt lgkmcnt(4)\n" \
"v_smfmac_f32_16x16x64_bf16 v[80:83], v[32:35], v[128:135], v12\n" \
"ds_read_b128 v[52:55], v0 offset:5120\n" \
"s_waitcnt lgkmcnt(4)\n" \
"v_min3_u32 v13, v13, v32, v34\n" \
"v_smfmac_f32_16x16x64_bf16 v[80:83], v[36:39], v[136:143], v12\n" \
"ds_read_b128 v[56:59], v0 offset:6144\n" \
"s_waitcnt lgkmcnt(4)\n" \
"v_min3_u32 v13, v13, v36, v38\n" \
"v_smfmac_f32_16x16x64_bf16 v[80:83], v[40:43], v[144:151], v12\n" \
"ds_read_b128 v[60:63], v0 offset:7168\n" \
"s_waitcnt lgkmcnt(4)\n" \
"v_min3_u32 v13, v13, v40, v42\n" \
"v_smfmac_f32_16x16x64_bf16 v[80:83], v[44:47], v[152:159], v12\n" \
"s_waitcnt vmcnt(0)\n" \
"s_barrier\n" \
"ds_read_b128 v[32:35], v0 offset:8192\n" \
"s_waitcnt lgkmcnt(4)\n" \
"v_min3_u32 v13, v13, v44, v46\n" \
"v_smfmac_f32_16x16x64_bf16 v[80:83], v[48:51], v[160:167], v12\n" \
"ds_read_b128 v[36:39], v0 offset:9216\n" \
"s_waitcnt lgkmcnt(4)\n" \
"v_min3_u32 v13, v13, v48, v50\n" \
"v_cmp_eq_u32_e64 vcc, 0, v13\n" \
"v_smfmac_f32_16x16x64_bf16 v[80:83], v[52:55], v[168:175], v12\n" \
"ds_read_b128 v[40:43], v0 offset:10240\n" \
"s_waitcnt lgkmcnt(4)\n" \
"v_min3_u32 v13, v13, v52, v54\n" \
"v_smfmac_f32_16x16x64_bf16 v[80:83], v[56:59], v[176:183], v12\n" \
"ds_read_b128 v[44:47], v0 offset:11264\n" \
"s_waitcnt lgkmcnt(4)\n" \
"v_min3_u32 v13, v13, v56, v58\n" \
"v_smfmac_f32_16x16x64_bf16 v[80:83], v[60:63], v[184:191], v12\n" \
"ds_read_b128 v[48:51], v0 offset:12288\n" \
"s_waitcnt lgkmcnt(4)\n" \
"s_nop 0\n" \
"s_cbranch_vccnz .Lv_mretry_%=\n" \
"v_min3_u32 v13, v13, v60, v62\n" \
"v_smfmac_f32_16x16x64_bf16 v[80:83], v[32:35], v[192:199], v12\n" \
"ds_read_b128 v[52:55], v0 offset:13312\n" \
"s_waitcnt lgkmcnt(4)\n" \
"v_min3_u32 v13, v13, v32, v34\n" \
"v_smfmac_f32_16x16x64_bf16 v[80:83], v[36:39], v[200:207], v12\n" \
"ds_read_b128 v[56:59], v0 offset:14336\n" \
"s_waitcnt lgkmcnt(4)\n" \
"v_min3_u32 v13, v13, v36, v38\n" \
"v_smfmac_f32_16x16x64_bf16 v[80:83], v[40:43], v[208:215], v12\n" \
"ds_read_b128 v[60:63], v0 offset:15360\n" \
"s_waitcnt lgkmcnt(4)\n" \
"v_min3_u32 v13, v13, v40, v42\n" \
"v_smfmac_f32_16x16x64_bf16 v[80:83], v[44:47], v[216:223], v12\n" \
"s_waitcnt lgkmcnt(3)\n" \
"v_min3_u32 v13, v13, v44, v46\n" \
"v_smfmac_f32_16x16x64_bf16 v[80:83], v[48:51], v[224:231], v12\n" \
"s_waitcnt lgkmcnt(2)\n" \
"v_min3_u32 v13, v13, v48, v50\n" \
"v_smfmac_f32_16x16x64_bf16 v[80:83], v[52:55], v[232:239], v12\n" \
"s_waitcnt lgkmcnt(1)\n" \
"v_min3_u32 v13, v13, v52, v54\n" \
"v_smfmac_f32_16x16x64_bf16 v[80:83], v[56:59], v[240:247], v12\n" \
"s_waitcnt lgkmcnt(0)\n" \
"v_min3_u32 v13, v13, v56, v58\n" \
"v_smfmac_f32_16x16x64_bf16 v[80:83], v[60:63], v[248:255], v12\n" \
"v_min3_u32 v13, v13, v60, v62\n" \
"v_cmp_eq_u32 vcc, 0, v13\n" \
"s_nop 5\n" \
"v_pk_add_f32 v[80:81], v[80:81], v[82:83]\n" \
"s_cbranch_vccnz .Lv_mretry_%=\n" \
"v_cvt_pk_bf16_f32 v84, v80, v81\n" \
"v_max_u32 v84, 1, v84\n" \
"buffer_store_dword v84, v7, s[36:39], s58 offen sc1\n" \
"s_mov_b32 s75, s77\n" \
"s_lshr_b32 s77, s59, 1\n" \
"buffer_load_dwordx2 v[64:65], v9, s[36:39], s75 offen sc1\n" \
"s_sub_u32 s53, s49, 1\n" \
"s_add_u32 s25, s24, 3\n" \
"s_cmp_eq_u32 s26, s24\n" \
"s_subb_u32 s25, s25, 0\n" \
"s_cmp_eq_u32 s28, s24\n" \
"s_subb_u32 s25, s25, 0\n" \
"s_cmp_eq_u32 s30, s24\n" \
"s_subb_u32 s25, s25, 0\n" \
"s_max_u32 s16, s25, 1\n" \
"s_sub_u32 s24, s25, s55\n" \
"s_min_u32 s24, s24, 255\n" \
"s_cmp_eq_u32 s23, s53\n" \
"s_cselect_b32 s24, s34, s24\n" \
"s_min_u32 s34, s24, s32\n" \
"s_max_u32 s25, s24, s32\n" \
"s_min_u32 s25, s25, s33\n" \
"s_max_u32 s34, s34, s25\n" \
"s_mov_b32 s33, s32\n" \
"s_mov_b32 s32, s24\n" \
"s_cmp_eq_u32 s49, 63\n" \
"s_cbranch_scc1 .Lv_end_%=\n" \
"v_mov_b64 v[80:81], 0\n" \
"v_mov_b64 v[82:83], 0\n" \
"v_mov_b64 v[84:85], 0\n" \
"v_mov_b64 v[86:87], 0\n" \
"v_mov_b64 v[88:89], 0\n" \
"v_mov_b64 v[90:91], 0\n" \
"v_mov_b64 v[92:93], 0\n" \
"v_mov_b64 v[94:95], 0\n" \
"buffer_store_dword v16, v7, s[36:39], s60 offen sc1\n" \
"s_lshr_b32 s54, s61, 1\n" \
"buffer_store_dwordx4 v[16:19], v8, s[36:39], s54 offen sc1\n" \
"s_sub_u32 s54, s34, s74\n" \
"v_mov_b32_e64 v13, s54\n" \
"buffer_store_dword v13, v28, s[36:39], 0 offen sc1\n" \
"v_mov_b32_e64 v13, -1\n" \
"s_mov_b32 s54, 0\n" \
"s_cmp_lg_u32 s48, 0\n" \
"s_cbranch_scc1 .Lv_wo4_%=\n" \
"s_waitcnt vmcnt(3)\n" \
"v_cmp_eq_u32_e64 vcc, 0, v64\n" \
"s_nop 0\n" \
"s_and_b64 vcc, vcc, s[80:81]\n" \
"s_cbranch_vccnz .Lv_h_cold_%=\n" \
".Lv_h_ok_%=:\n" \
"v_subrev_u32 v64, s51, v64\n" \
"v_max_i32_dpp v65, v65, v65 row_ror:8 row_mask:0xf bank_mask:0xf\n" \
"s_nop 0\n" \
"v_max_i32_dpp v64, v64, v64 row_ror:8 row_mask:0xf bank_mask:0xf\n" \
"v_max_i32_dpp v65, v65, v65 row_ror:4 row_mask:0xf bank_mask:0xf\n" \
"s_nop 0\n" \
"v_max_i32_dpp v64, v64, v64 row_ror:4 row_mask:0xf bank_mask:0xf\n" \
"v_max_i32_dpp v65, v65, v65 row_ror:2 row_mask:0xf bank_mask:0xf\n" \
"s_nop 0\n" \
"v_max_i32_dpp v64, v64, v64 row_ror:2 row_mask:0xf bank_mask:0xf\n" \
"v_max_i32_dpp v65, v65, v65 row_ror:1 row_mask:0xf bank_mask:0xf\n" \
"s_nop 0\n" \
"v_max_i32_dpp v64, v64, v64 row_ror:1 row_mask:0xf bank_mask:0xf\n" \
"v_readlane_b32 s53, v65, 0\n" \
"v_readlane_b32 s50, v65, 16\n" \
"v_readlane_b32 s54, v64, 0\n" \
"v_readlane_b32 s55, v64, 16\n" \
"s_max_i32 s50, s50, s53\n" \
"s_max_i32 s55, s55, s54\n" \
"s_add_u32 s55, s55, s51\n" \
"s_add_u32 s55, s55, s44\n" \
"s_add_u32 s50, s50, s55\n" \
"s_add_u32 s50, s50, s45\n" \
"s_getpc_b64 s[18:19]\n" \
".Lv_wop_%=:\n" \
"s_add_u32 s18, s18, .Lv_wos_%=-.Lv_wop_%=\n" \
"s_addc_u32 s19, s19, 0\n" \
"s_sub_u32 s21, s55, 5\n" \
"s_nop 0\n" \
"s_nop 0\n" \
"s_nop 0\n" \
"s_nop 0\n" \
"s_nop 0\n" \
".Lv_wo_%=:\n" \
"s_memrealtime s[56:57]\n" \
"s_waitcnt lgkmcnt(0)\n" \
"s_sub_u32 s54, s56, s21\n" \
"s_cmp_gt_u32 s54, s22\n" \
"s_cbranch_scc1 .Lv_wo_%=\n" \
"s_min_u32 s21, s54, 3\n" \
"s_lshl2_add_u32 s18, s21, s18\n" \
"s_setpc_b64 s[18:19]\n" \
"s_nop 0\n" \
".Lv_wos_%=:\n" \
"s_nop 4\n" \
"s_nop 5\n" \
"s_nop 4\n" \
".Lv_wo4_%=:\n" \
"s_barrier\n" \
".Lv_o_%=:\n" \
"buffer_load_dwordx4 v[32:35], v6, s[36:39], s58 offen offset:0 sc1\n" \
"s_cmp_lt_u32 s54, 8\n" \
"s_cselect_b32 s23, s23, s49\n" \
"s_nop 8\n" \
"buffer_load_dwordx4 v[36:39], v6, s[36:39], s58 offen offset:1024 sc1\n" \
"s_nop 10\n" \
"buffer_load_dwordx4 v[40:43], v6, s[36:39], s58 offen offset:2048 sc1\n" \
"s_nop 10\n" \
"buffer_load_dwordx4 v[44:47], v6, s[36:39], s58 offen offset:3072 sc1\n" \
"s_nop 10\n" \
"buffer_load_dwordx4 v[48:51], v30, s[36:39], s58 offen offset:0 sc1\n" \
"s_nop 10\n" \
"buffer_load_dwordx4 v[52:55], v30, s[36:39], s58 offen offset:1024 sc1\n" \
"s_nop 10\n" \
"buffer_load_dwordx4 v[56:59], v30, s[36:39], s58 offen offset:2048 sc1\n" \
"s_nop 10\n" \
"buffer_load_dwordx4 v[60:63], v30, s[36:39], s58 offen offset:3072 sc1\n" \
".Lv_y_%=:\n" \
"s_waitcnt vmcnt(7)\n" \
"v_smfmac_f32_16x16x64_bf16 v[80:83], v[32:35], a[0:7], v12\n" \
"v_min3_u32 v13, v13, v32, v34\n" \
"v_smfmac_f32_16x16x64_bf16 v[84:87], v[32:35], a[8:15], v12\n" \
"v_smfmac_f32_16x16x64_bf16 v[88:91], v[32:35], a[16:23], v12\n" \
"v_smfmac_f32_16x16x64_bf16 v[92:95], v[32:35], a[24:31], v12\n" \
"s_waitcnt vmcnt(6)\n" \
"v_smfmac_f32_16x16x64_bf16 v[80:83], v[36:39], a[32:39], v12\n" \
"v_min3_u32 v13, v13, v36, v38\n" \
"v_smfmac_f32_16x16x64_bf16 v[84:87], v[36:39], a[40:47], v12\n" \
"v_cmp_eq_u32 vcc, 0, v13\n" \
"v_smfmac_f32_16x16x64_bf16 v[88:91], v[36:39], a[48:55], v12\n" \
"v_smfmac_f32_16x16x64_bf16 v[92:95], v[36:39], a[56:63], v12\n" \
"s_cbranch_vccnz .Lv_retry_%=\n" \
"s_waitcnt vmcnt(5)\n" \
"v_smfmac_f32_16x16x64_bf16 v[80:83], v[40:43], a[64:71], v12\n" \
"v_min3_u32 v13, v13, v40, v42\n" \
"v_smfmac_f32_16x16x64_bf16 v[84:87], v[40:43], a[72:79], v12\n" \
"v_smfmac_f32_16x16x64_bf16 v[88:91], v[40:43], a[80:87], v12\n" \
"v_smfmac_f32_16x16x64_bf16 v[92:95], v[40:43], a[88:95], v12\n" \
"s_waitcnt vmcnt(4)\n" \
"v_smfmac_f32_16x16x64_bf16 v[80:83], v[44:47], a[96:103], v12\n" \
"v_min3_u32 v13, v13, v44, v46\n" \
"v_smfmac_f32_16x16x64_bf16 v[84:87], v[44:47], a[104:111], v12\n" \
"v_smfmac_f32_16x16x64_bf16 v[88:91], v[44:47], a[112:119], v12\n" \
"v_smfmac_f32_16x16x64_bf16 v[92:95], v[44:47], a[120:127], v12\n" \
"s_waitcnt vmcnt(3)\n" \
"v_smfmac_f32_16x16x64_bf16 v[80:83], v[48:51], a[128:135], v12\n" \
"v_min3_u32 v13, v13, v48, v50\n" \
"v_smfmac_f32_16x16x64_bf16 v[84:87], v[48:51], a[136:143], v12\n" \
"v_smfmac_f32_16x16x64_bf16 v[88:91], v[48:51], a[144:151], v12\n" \
"v_smfmac_f32_16x16x64_bf16 v[92:95], v[48:51], a[152:159], v12\n" \
"s_waitcnt vmcnt(2)\n" \
"v_smfmac_f32_16x16x64_bf16 v[80:83], v[52:55], a[160:167], v12\n" \
"v_min3_u32 v13, v13, v52, v54\n" \
"v_smfmac_f32_16x16x64_bf16 v[84:87], v[52:55], a[168:175], v12\n" \
"v_smfmac_f32_16x16x64_bf16 v[88:91], v[52:55], a[176:183], v12\n" \
"v_smfmac_f32_16x16x64_bf16 v[92:95], v[52:55], a[184:191], v12\n" \
"s_waitcnt vmcnt(1)\n" \
"v_smfmac_f32_16x16x64_bf16 v[80:83], v[56:59], a[192:199], v12\n" \
"v_min3_u32 v13, v13, v56, v58\n" \
"v_smfmac_f32_16x16x64_bf16 v[84:87], v[56:59], a[200:207], v12\n" \
"v_smfmac_f32_16x16x64_bf16 v[88:91], v[56:59], a[208:215], v12\n" \
"v_smfmac_f32_16x16x64_bf16 v[92:95], v[56:59], a[216:223], v12\n" \
"s_waitcnt vmcnt(0)\n" \
"v_smfmac_f32_16x16x64_bf16 v[80:83], v[60:63], a[224:231], v12\n" \
"v_min3_u32 v13, v13, v60, v62\n" \
"v_smfmac_f32_16x16x64_bf16 v[84:87], v[60:63], a[232:239], v12\n" \
"v_cmp_eq_u32 vcc, 0, v13\n" \
"v_smfmac_f32_16x16x64_bf16 v[88:91], v[60:63], a[240:247], v12\n" \
"v_smfmac_f32_16x16x64_bf16 v[92:95], v[60:63], a[248:255], v12\n" \
"s_cbranch_vccnz .Lv_retry_%=\n" \
".Lv_y2_%=:\n" \
"v_pk_add_f32 v[80:81], v[80:81], v[82:83]\n" \
"v_pk_add_f32 v[84:85], v[84:85], v[86:87]\n" \
"s_nop 0\n" \
"v_pk_add_f32 v[88:89], v[88:89], v[90:91]\n" \
"s_nop 2\n" \
"v_pk_add_f32 v[92:93], v[92:93], v[94:95]\n" \
"v_cndmask_b32_e64 v80, v80, v88, s[72:73]\n" \
"v_cndmask_b32_e64 v81, v81, v89, s[72:73]\n" \
"v_cndmask_b32_e64 v84, v84, v92, s[72:73]\n" \
"v_cndmask_b32_e64 v85, v85, v93, s[72:73]\n" \
"s_nop 0\n" \
"v_permlane32_swap_b32 v80, v84\n" \
"v_permlane32_swap_b32 v81, v85\n" \
"v_pk_add_f32 v[80:81], v[80:81], v[84:85]\n" \
"ds_write2_b32 v15, v80, v81 offset1:32\n" \
"s_cmp_lg_u32 s48, 0\n" \
"s_waitcnt lgkmcnt(0)\n" \
"s_barrier\n" \
"s_nop 0\n" \
"s_nop 0\n" \
"s_nop 0\n" \
"s_cbranch_scc1 .Lv_skip_%=\n" \
"ds_read_b128 v[64:67], v31\n" \
"ds_read_b128 v[68:71], v31 offset:16\n" \
"s_waitcnt lgkmcnt(1)\n" \
"v_pk_add_f32 v[64:65], v[64:65], v[66:67]\n" \
"s_waitcnt lgkmcnt(0)\n" \
"v_pk_add_f32 v[68:69], v[68:69], v[70:71]\n" \
"v_pk_add_f32 v[64:65], v[64:65], v[68:69]\n" \
"v_cvt_pk_bf16_f32 v72, v64, v65\n" \
"v_max_u32 v72, 1, v72\n" \
"s_nop 1\n" \
"v_mov_b32_dpp v73, v72 quad_perm:[1,1,1,1] row_mask:0xf bank_mask:0xf\n" \
"v_mov_b32_dpp v74, v72 quad_perm:[2,2,2,2] row_mask:0xf bank_mask:0xf\n" \
"v_mov_b32_dpp v75, v72 quad_perm:[3,3,3,3] row_mask:0xf bank_mask:0xf\n" \
".Lv_skip_%=:\n" \
"buffer_store_dwordx4 v[72:75], v8, s[36:39], s77 offen sc1\n" \
"s_memrealtime s[24:25]\n" \
"s_mov_b32 s54, s58\n" \
"s_mov_b32 s58, s59\n" \
"s_mov_b32 s59, s60\n" \
"s_mov_b32 s60, s61\n" \
"s_mov_b32 s61, s54\n" \
"s_memrealtime s[26:27]\n" \
"s_add_u32 s78, s77, s63\n" \
"s_xor_b32 s89, s78, 0x4800\n" \
"s_xor_b32 s90, s78, 0x800\n" \
"s_xor_b32 s91, s78, 0x4000\n" \
"s_mov_b32 m0, s79\n" \
"s_memrealtime s[28:29]\n" \
"s_add_u32 s51, s50, s46\n" \
"v_mov_b64 v[80:81], 0\n" \
"v_mov_b64 v[82:83], 0\n" \
"v_mov_b32 v13, -1\n" \
"s_memrealtime s[30:31]\n" \
"s_add_u32 s49, s49, 1\n" \
"s_waitcnt lgkmcnt(0)\n" \
"s_cmp_lg_u32 s48, 0\n" \
"s_nop 0\n" \
"s_nop 0\n" \
"s_nop 0\n" \
"s_cbranch_scc1 .Lv_w4_%=\n" \
"s_branch .Lv_loop_%=\n" \
".Lv_h_cold_%=:\n" \
"s_nop 7\n" \
"s_nop 7\n" \
"s_waitcnt vmcnt(0) lgkmcnt(0)\n" \
"s_mov_b32 s53, 0\n" \
".Lv_h_again_%=:\n" \
"s_cmp_lg_u32 s52, 0\n" \
"s_cbranch_scc1 .Lv_h_ok_%=\n" \
"s_sleep 1\n" \
"buffer_load_dwordx2 v[64:65], v9, s[36:39], s75 offen sc1\n" \
"s_waitcnt vmcnt(0)\n" \
"v_cmp_eq_u32 vcc, 0, v64\n" \
"s_nop 0\n" \
"s_and_b64 vcc, vcc, s[80:81]\n" \
"s_nop 4\n" \
"s_cbranch_vccz .Lv_h_ok_%=\n" \
"s_add_u32 s53, s53, 1\n" \
"s_cmp_lt_u32 s53, 32768\n" \
"s_cbranch_scc1 .Lv_h_again_%=\n" \
"s_mov_b32 s52, 1\n" \
"v_mov_b32 v10, 256\n" \
"v_mov_b32 v11, 1\n" \
"buffer_store_dword v11, v10, s[36:39], 0 offen sc1\n" \
"s_waitcnt vmcnt(0) lgkmcnt(0)\n" \
"s_branch .Lv_h_ok_%=\n" \
".Lv_retry_%=:\n" \
"s_nop 7\n" \
"s_nop 7\n" \
"s_cmp_lg_u32 s52, 0\n" \
"s_cbranch_scc1 .Lv_dead_%=\n" \
"v_mov_b64 v[80:81], 0\n" \
"v_mov_b64 v[82:83], 0\n" \
"v_mov_b64 v[84:85], 0\n" \
"v_mov_b64 v[86:87], 0\n" \
"v_mov_b64 v[88:89], 0\n" \
"v_mov_b64 v[90:91], 0\n" \
"v_mov_b64 v[92:93], 0\n" \
"v_mov_b64 v[94:95], 0\n" \
"v_mov_b32 v13, -1\n" \
"s_mov_b32 s54, 100\n" \
"s_add_u32 s76, s76, 1\n" \
"s_cmp_lt_u32 s76, 32768\n" \
"s_cbranch_scc1 .Lv_o_%=\n" \
"s_mov_b32 s52, 1\n" \
"v_mov_b32 v13, 256\n" \
"v_mov_b32 v14, 1\n" \
"buffer_store_dword v14, v13, s[36:39], 0 offen sc1\n" \
".Lv_dead_%=:\n" \
"s_waitcnt vmcnt(0) lgkmcnt(0)\n" \
"s_branch .Lv_y2_%=\n" \
".Lv_mretry_%=:\n" \
"s_nop 7\n" \
"s_nop 7\n" \
"v_mov_b64 v[80:81], 0\n" \
"v_mov_b64 v[82:83], 0\n" \
"v_mov_b32 v13, -1\n" \
"s_mov_b32 m0, s79\n" \
"s_waitcnt vmcnt(0) lgkmcnt(0)\n" \
"s_barrier\n" \
"s_add_u32 s88, s88, 1\n" \
"s_cmp_lt_u32 s88, 32768\n" \
"s_cbranch_scc1 .Lv_r1_%=\n" \
"s_mov_b32 s52, 1\n" \
"v_mov_b32 v13, 256\n" \
"v_mov_b32 v14, 1\n" \
"buffer_store_dword v14, v13, s[36:39], 0 offen sc1\n" \
"s_waitcnt vmcnt(0) lgkmcnt(0)\n" \
"s_endpgm\n" \
".Lv_r1_%=:\n" \
"s_cmp_lg_u32 s49, 0\n" \
"s_cbranch_scc1 .Lv_r2_%=\n" \
"s_sleep 8\n" \
".Lv_r2_%=:\n" \
"s_branch .Lv_go_%=\n" \
".Lv_end_%=:\n" \
"s_nop 7\n" \
"s_nop 7\n" \
"s_waitcnt vmcnt(0) lgkmcnt(0)\n" \
"s_waitcnt vmcnt(0) lgkmcnt(0)\n" \
".Lv_fin_%=:\n" \
"s_endpgm\n"
#define ASM_S64_WO \
"s_nop 7\n" \
"s_nop 7\n" \
"s_lshl_b32 s52, s48, 15\n" \
"s_lshl_b32 s53, s48, 13\n" \
"s_add_u32 s53, s53, 98304\n" \
"s_sub_u32 s54, s53, s52\n" \
"v_add_u32 v9, s54, v1\n" \
"s_add_u32 m0, s52, 0\n" \
"s_mov_b32 s54, 0\n" \
"buffer_load_dwordx4 v0, s[40:43], s54 offen sc1 lds\n" \
"s_add_u32 m0, s52, 1024\n" \
"s_mov_b32 s54, 16384\n" \
"buffer_load_dwordx4 v0, s[40:43], s54 offen sc1 lds\n" \
"s_add_u32 m0, s52, 2048\n" \
"s_mov_b32 s54, 32768\n" \
"buffer_load_dwordx4 v0, s[40:43], s54 offen sc1 lds\n" \
"s_add_u32 m0, s52, 3072\n" \
"s_mov_b32 s54, 49152\n" \
"buffer_load_dwordx4 v0, s[40:43], s54 offen sc1 lds\n" \
"s_mov_b32 s54, s99\n" \
".Lswo_z0_%=:\n" \
"s_sleep 1\n" \
"s_sub_u32 s54, s54, 1\n" \
"s_cbranch_scc0 .Lswo_z0_%=\n" \
"s_add_u32 m0, s52, 4096\n" \
"s_mov_b32 s54, 65536\n" \
"buffer_load_dwordx4 v0, s[40:43], s54 offen sc1 lds\n" \
"s_add_u32 m0, s52, 5120\n" \
"s_mov_b32 s54, 81920\n" \
"buffer_load_dwordx4 v0, s[40:43], s54 offen sc1 lds\n" \
"s_add_u32 m0, s52, 6144\n" \
"s_mov_b32 s54, 98304\n" \
"buffer_load_dwordx4 v0, s[40:43], s54 offen sc1 lds\n" \
"s_add_u32 m0, s52, 7168\n" \
"s_mov_b32 s54, 114688\n" \
"buffer_load_dwordx4 v0, s[40:43], s54 offen sc1 lds\n" \
"s_mov_b32 s54, s99\n" \
".Lswo_z1_%=:\n" \
"s_sleep 1\n" \
"s_sub_u32 s54, s54, 1\n" \
"s_cbranch_scc0 .Lswo_z1_%=\n" \
"s_add_u32 m0, s52, 8192\n" \
"s_mov_b32 s54, 131072\n" \
"buffer_load_dwordx4 v0, s[40:43], s54 offen sc1 lds\n" \
"s_add_u32 m0, s52, 9216\n" \
"s_mov_b32 s54, 147456\n" \
"buffer_load_dwordx4 v0, s[40:43], s54 offen sc1 lds\n" \
"s_add_u32 m0, s52, 10240\n" \
"s_mov_b32 s54, 163840\n" \
"buffer_load_dwordx4 v0, s[40:43], s54 offen sc1 lds\n" \
"s_add_u32 m0, s52, 11264\n" \
"s_mov_b32 s54, 180224\n" \
"buffer_load_dwordx4 v0, s[40:43], s54 offen sc1 lds\n" \
"s_mov_b32 s54, s99\n" \
".Lswo_z2_%=:\n" \
"s_sleep 1\n" \
"s_sub_u32 s54, s54, 1\n" \
"s_cbranch_scc0 .Lswo_z2_%=\n" \
"s_add_u32 m0, s52, 12288\n" \
"s_mov_b32 s54, 196608\n" \
"buffer_load_dwordx4 v0, s[40:43], s54 offen sc1 lds\n" \
"s_add_u32 m0, s52, 13312\n" \
"s_mov_b32 s54, 212992\n" \
"buffer_load_dwordx4 v0, s[40:43], s54 offen sc1 lds\n" \
"s_add_u32 m0, s52, 14336\n" \
"s_mov_b32 s54, 229376\n" \
"buffer_load_dwordx4 v0, s[40:43], s54 offen sc1 lds\n" \
"s_add_u32 m0, s52, 15360\n" \
"s_mov_b32 s54, 245760\n" \
"buffer_load_dwordx4 v0, s[40:43], s54 offen sc1 lds\n" \
"s_mov_b32 s54, s99\n" \
".Lswo_z3_%=:\n" \
"s_sleep 1\n" \
"s_sub_u32 s54, s54, 1\n" \
"s_cbranch_scc0 .Lswo_z3_%=\n" \
"s_add_u32 m0, s52, 16384\n" \
"s_mov_b32 s54, 262144\n" \
"buffer_load_dwordx4 v0, s[40:43], s54 offen sc1 lds\n" \
"s_add_u32 m0, s52, 17408\n" \
"s_mov_b32 s54, 278528\n" \
"buffer_load_dwordx4 v0, s[40:43], s54 offen sc1 lds\n" \
"s_add_u32 m0, s52, 18432\n" \
"s_mov_b32 s54, 294912\n" \
"buffer_load_dwordx4 v0, s[40:43], s54 offen sc1 lds\n" \
"s_add_u32 m0, s52, 19456\n" \
"s_mov_b32 s54, 311296\n" \
"buffer_load_dwordx4 v0, s[40:43], s54 offen sc1 lds\n" \
"s_mov_b32 s54, s99\n" \
".Lswo_z4_%=:\n" \
"s_sleep 1\n" \
"s_sub_u32 s54, s54, 1\n" \
"s_cbranch_scc0 .Lswo_z4_%=\n" \
"s_add_u32 m0, s52, 20480\n" \
"s_mov_b32 s54, 327680\n" \
"buffer_load_dwordx4 v0, s[40:43], s54 offen sc1 lds\n" \
"s_add_u32 m0, s52, 21504\n" \
"s_mov_b32 s54, 344064\n" \
"buffer_load_dwordx4 v0, s[40:43], s54 offen sc1 lds\n" \
"s_add_u32 m0, s52, 22528\n" \
"s_mov_b32 s54, 360448\n" \
"buffer_load_dwordx4 v0, s[40:43], s54 offen sc1 lds\n" \
"s_add_u32 m0, s52, 23552\n" \
"s_mov_b32 s54, 376832\n" \
"buffer_load_dwordx4 v0, s[40:43], s54 offen sc1 lds\n" \
"s_mov_b32 s54, s99\n" \
".Lswo_z5_%=:\n" \
"s_sleep 1\n" \
"s_sub_u32 s54, s54, 1\n" \
"s_cbranch_scc0 .Lswo_z5_%=\n" \
"s_add_u32 m0, s52, 24576\n" \
"s_mov_b32 s54, 393216\n" \
"buffer_load_dwordx4 v0, s[40:43], s54 offen sc1 lds\n" \
"s_add_u32 m0, s52, 25600\n" \
"s_mov_b32 s54, 409600\n" \
"buffer_load_dwordx4 v0, s[40:43], s54 offen sc1 lds\n" \
"s_add_u32 m0, s52, 26624\n" \
"s_mov_b32 s54, 425984\n" \
"buffer_load_dwordx4 v0, s[40:43], s54 offen sc1 lds\n" \
"s_add_u32 m0, s52, 27648\n" \
"s_mov_b32 s54, 442368\n" \
"buffer_load_dwordx4 v0, s[40:43], s54 offen sc1 lds\n" \
"s_mov_b32 s54, s99\n" \
".Lswo_z6_%=:\n" \
"s_sleep 1\n" \
"s_sub_u32 s54, s54, 1\n" \
"s_cbranch_scc0 .Lswo_z6_%=\n" \
"s_add_u32 m0, s52, 28672\n" \
"s_mov_b32 s54, 458752\n" \
"buffer_load_dwordx4 v0, s[40:43], s54 offen sc1 lds\n" \
"s_add_u32 m0, s52, 29696\n" \
"s_mov_b32 s54, 475136\n" \
"buffer_load_dwordx4 v0, s[40:43], s54 offen sc1 lds\n" \
"s_add_u32 m0, s52, 30720\n" \
"s_mov_b32 s54, 491520\n" \
"buffer_load_dwordx4 v0, s[40:43], s54 offen sc1 lds\n" \
"s_add_u32 m0, s52, 31744\n" \
"s_mov_b32 s54, 507904\n" \
"buffer_load_dwordx4 v0, s[40:43], s54 offen sc1 lds\n" \
"s_mov_b32 s54, s99\n" \
".Lswo_z7_%=:\n" \
"s_sleep 1\n" \
"s_sub_u32 s54, s54, 1\n" \
"s_cbranch_scc0 .Lswo_z7_%=\n" \
"s_add_u32 m0, s53, 32768\n" \
"s_mov_b32 s54, 524288\n" \
"buffer_load_dwordx4 v0, s[40:43], s54 offen sc1 lds\n" \
"s_add_u32 m0, s53, 33792\n" \
"s_mov_b32 s54, 540672\n" \
"buffer_load_dwordx4 v0, s[40:43], s54 offen sc1 lds\n" \
"s_add_u32 m0, s53, 34816\n" \
"s_mov_b32 s54, 557056\n" \
"buffer_load_dwordx4 v0, s[40:43], s54 offen sc1 lds\n" \
"s_add_u32 m0, s53, 35840\n" \
"s_mov_b32 s54, 573440\n" \
"buffer_load_dwordx4 v0, s[40:43], s54 offen sc1 lds\n" \
"s_mov_b32 s54, s99\n" \
".Lswo_z8_%=:\n" \
"s_sleep 1\n" \
"s_sub_u32 s54, s54, 1\n" \
"s_cbranch_scc0 .Lswo_z8_%=\n" \
"s_add_u32 m0, s53, 36864\n" \
"s_mov_b32 s54, 589824\n" \
"buffer_load_dwordx4 v0, s[40:43], s54 offen sc1 lds\n" \
"s_add_u32 m0, s53, 37888\n" \
"s_mov_b32 s54, 606208\n" \
"buffer_load_dwordx4 v0, s[40:43], s54 offen sc1 lds\n" \
"s_add_u32 m0, s53, 38912\n" \
"s_mov_b32 s54, 622592\n" \
"buffer_load_dwordx4 v0, s[40:43], s54 offen sc1 lds\n" \
"s_add_u32 m0, s53, 39936\n" \
"s_mov_b32 s54, 638976\n" \
"buffer_load_dwordx4 v0, s[40:43], s54 offen sc1 lds\n" \
"s_mov_b32 s54, s99\n" \
".Lswo_z9_%=:\n" \
"s_sleep 1\n" \
"s_sub_u32 s54, s54, 1\n" \
"s_cbranch_scc0 .Lswo_z9_%=\n" \
"s_waitcnt vmcnt(36)\n" \
"ds_read_b64_tr_b16 a[0:1], v1 offset:0\n" \
"ds_read_b64_tr_b16 a[2:3], v1 offset:2048\n" \
"ds_read_b64_tr_b16 a[128:129], v1 offset:32\n" \
"ds_read_b64_tr_b16 a[130:131], v1 offset:2080\n" \
"ds_read_b64_tr_b16 v[128:129], v1 offset:64\n" \
"ds_read_b64_tr_b16 v[130:131], v1 offset:2112\n" \
"ds_read_b64_tr_b16 v[16:17], v1 offset:96\n" \
"ds_read_b64_tr_b16 v[18:19], v1 offset:2144\n" \
"s_waitcnt lgkmcnt(0)\n" \
"s_add_u32 m0, s52, 0\n" \
"s_mov_b32 s54, 655360\n" \
"buffer_load_dwordx4 v0, s[40:43], s54 offen sc1 lds\n" \
"s_add_u32 m0, s52, 1024\n" \
"s_mov_b32 s54, 671744\n" \
"buffer_load_dwordx4 v0, s[40:43], s54 offen sc1 lds\n" \
"s_add_u32 m0, s52, 2048\n" \
"s_mov_b32 s54, 688128\n" \
"buffer_load_dwordx4 v0, s[40:43], s54 offen sc1 lds\n" \
"s_add_u32 m0, s52, 3072\n" \
"s_mov_b32 s54, 704512\n" \
"buffer_load_dwordx4 v0, s[40:43], s54 offen sc1 lds\n" \
"s_mov_b32 s54, s99\n" \
".Lswo_z10_%=:\n" \
"s_sleep 1\n" \
"s_sub_u32 s54, s54, 1\n" \
"s_cbranch_scc0 .Lswo_z10_%=\n" \
"s_waitcnt vmcnt(36)\n" \
"ds_read_b64_tr_b16 a[4:5], v1 offset:4096\n" \
"ds_read_b64_tr_b16 a[6:7], v1 offset:6144\n" \
"ds_read_b64_tr_b16 a[132:133], v1 offset:4128\n" \
"ds_read_b64_tr_b16 a[134:135], v1 offset:6176\n" \
"ds_read_b64_tr_b16 v[132:133], v1 offset:4160\n" \
"ds_read_b64_tr_b16 v[134:135], v1 offset:6208\n" \
"ds_read_b64_tr_b16 v[20:21], v1 offset:4192\n" \
"ds_read_b64_tr_b16 v[22:23], v1 offset:6240\n" \
"s_waitcnt lgkmcnt(0)\n" \
"s_add_u32 m0, s52, 4096\n" \
"s_mov_b32 s54, 720896\n" \
"buffer_load_dwordx4 v0, s[40:43], s54 offen sc1 lds\n" \
"s_add_u32 m0, s52, 5120\n" \
"s_mov_b32 s54, 737280\n" \
"buffer_load_dwordx4 v0, s[40:43], s54 offen sc1 lds\n" \
"s_add_u32 m0, s52, 6144\n" \
"s_mov_b32 s54, 753664\n" \
"buffer_load_dwordx4 v0, s[40:43], s54 offen sc1 lds\n" \
"s_add_u32 m0, s52, 7168\n" \
"s_mov_b32 s54, 770048\n" \
"buffer_load_dwordx4 v0, s[40:43], s54 offen sc1 lds\n" \
"s_mov_b32 s54, s99\n" \
".Lswo_z11_%=:\n" \
"s_sleep 1\n" \
"s_sub_u32 s54, s54, 1\n" \
"s_cbranch_scc0 .Lswo_z11_%=\n" \
"s_waitcnt vmcnt(36)\n" \
"ds_read_b64_tr_b16 a[8:9], v1 offset:8192\n" \
"ds_read_b64_tr_b16 a[10:11], v1 offset:10240\n" \
"ds_read_b64_tr_b16 a[136:137], v1 offset:8224\n" \
"ds_read_b64_tr_b16 a[138:139], v1 offset:10272\n" \
"ds_read_b64_tr_b16 v[136:137], v1 offset:8256\n" \
"ds_read_b64_tr_b16 v[138:139], v1 offset:10304\n" \
"ds_read_b64_tr_b16 v[24:25], v1 offset:8288\n" \
"ds_read_b64_tr_b16 v[26:27], v1 offset:10336\n" \
"s_waitcnt lgkmcnt(0)\n" \
"s_add_u32 m0, s52, 8192\n" \
"s_mov_b32 s54, 786432\n" \
"buffer_load_dwordx4 v0, s[40:43], s54 offen sc1 lds\n" \
"s_add_u32 m0, s52, 9216\n" \
"s_mov_b32 s54, 802816\n" \
"buffer_load_dwordx4 v0, s[40:43], s54 offen sc1 lds\n" \
"s_add_u32 m0, s52, 10240\n" \
"s_mov_b32 s54, 819200\n" \
"buffer_load_dwordx4 v0, s[40:43], s54 offen sc1 lds\n" \
"s_add_u32 m0, s52, 11264\n" \
"s_mov_b32 s54, 835584\n" \
"buffer_load_dwordx4 v0, s[40:43], s54 offen sc1 lds\n" \
"s_mov_b32 s54, s99\n" \
".Lswo_z12_%=:\n" \
"s_sleep 1\n" \
"s_sub_u32 s54, s54, 1\n" \
"s_cbranch_scc0 .Lswo_z12_%=\n" \
"s_waitcnt vmcnt(36)\n" \
"ds_read_b64_tr_b16 a[12:13], v1 offset:12288\n" \
"ds_read_b64_tr_b16 a[14:15], v1 offset:14336\n" \
"ds_read_b64_tr_b16 a[140:141], v1 offset:12320\n" \
"ds_read_b64_tr_b16 a[142:143], v1 offset:14368\n" \
"ds_read_b64_tr_b16 v[140:141], v1 offset:12352\n" \
"ds_read_b64_tr_b16 v[142:143], v1 offset:14400\n" \
"ds_read_b64_tr_b16 v[28:29], v1 offset:12384\n" \
"ds_read_b64_tr_b16 v[30:31], v1 offset:14432\n" \
"s_waitcnt lgkmcnt(0)\n" \
"s_add_u32 m0, s52, 12288\n" \
"s_mov_b32 s54, 851968\n" \
"buffer_load_dwordx4 v0, s[40:43], s54 offen sc1 lds\n" \
"s_add_u32 m0, s52, 13312\n" \
"s_mov_b32 s54, 868352\n" \
"buffer_load_dwordx4 v0, s[40:43], s54 offen sc1 lds\n" \
"s_add_u32 m0, s52, 14336\n" \
"s_mov_b32 s54, 884736\n" \
"buffer_load_dwordx4 v0, s[40:43], s54 offen sc1 lds\n" \
"s_add_u32 m0, s52, 15360\n" \
"s_mov_b32 s54, 901120\n" \
"buffer_load_dwordx4 v0, s[40:43], s54 offen sc1 lds\n" \
"s_mov_b32 s54, s99\n" \
".Lswo_z13_%=:\n" \
"s_sleep 1\n" \
"s_sub_u32 s54, s54, 1\n" \
"s_cbranch_scc0 .Lswo_z13_%=\n" \
"s_waitcnt vmcnt(36)\n" \
"ds_read_b64_tr_b16 a[16:17], v1 offset:16384\n" \
"ds_read_b64_tr_b16 a[18:19], v1 offset:18432\n" \
"ds_read_b64_tr_b16 a[144:145], v1 offset:16416\n" \
"ds_read_b64_tr_b16 a[146:147], v1 offset:18464\n" \
"ds_read_b64_tr_b16 v[144:145], v1 offset:16448\n" \
"ds_read_b64_tr_b16 v[146:147], v1 offset:18496\n" \
"ds_read_b64_tr_b16 v[32:33], v1 offset:16480\n" \
"ds_read_b64_tr_b16 v[34:35], v1 offset:18528\n" \
"s_waitcnt lgkmcnt(0)\n" \
"s_add_u32 m0, s52, 16384\n" \
"s_mov_b32 s54, 917504\n" \
"buffer_load_dwordx4 v0, s[40:43], s54 offen sc1 lds\n" \
"s_add_u32 m0, s52, 17408\n" \
"s_mov_b32 s54, 933888\n" \
"buffer_load_dwordx4 v0, s[40:43], s54 offen sc1 lds\n" \
"s_add_u32 m0, s52, 18432\n" \
"s_mov_b32 s54, 950272\n" \
"buffer_load_dwordx4 v0, s[40:43], s54 offen sc1 lds\n" \
"s_add_u32 m0, s52, 19456\n" \
"s_mov_b32 s54, 966656\n" \
"buffer_load_dwordx4 v0, s[40:43], s54 offen sc1 lds\n" \
"s_mov_b32 s54, s99\n" \
".Lswo_z14_%=:\n" \
"s_sleep 1\n" \
"s_sub_u32 s54, s54, 1\n" \
"s_cbranch_scc0 .Lswo_z14_%=\n" \
"s_waitcnt vmcnt(36)\n" \
"ds_read_b64_tr_b16 a[20:21], v1 offset:20480\n" \
"ds_read_b64_tr_b16 a[22:23], v1 offset:22528\n" \
"ds_read_b64_tr_b16 a[148:149], v1 offset:20512\n" \
"ds_read_b64_tr_b16 a[150:151], v1 offset:22560\n" \
"ds_read_b64_tr_b16 v[148:149], v1 offset:20544\n" \
"ds_read_b64_tr_b16 v[150:151], v1 offset:22592\n" \
"ds_read_b64_tr_b16 v[36:37], v1 offset:20576\n" \
"ds_read_b64_tr_b16 v[38:39], v1 offset:22624\n" \
"s_waitcnt lgkmcnt(0)\n" \
"s_add_u32 m0, s52, 20480\n" \
"s_mov_b32 s54, 983040\n" \
"buffer_load_dwordx4 v0, s[40:43], s54 offen sc1 lds\n" \
"s_add_u32 m0, s52, 21504\n" \
"s_mov_b32 s54, 999424\n" \
"buffer_load_dwordx4 v0, s[40:43], s54 offen sc1 lds\n" \
"s_add_u32 m0, s52, 22528\n" \
"s_mov_b32 s54, 1015808\n" \
"buffer_load_dwordx4 v0, s[40:43], s54 offen sc1 lds\n" \
"s_add_u32 m0, s52, 23552\n" \
"s_mov_b32 s54, 1032192\n" \
"buffer_load_dwordx4 v0, s[40:43], s54 offen sc1 lds\n" \
"s_mov_b32 s54, s99\n" \
".Lswo_z15_%=:\n" \
"s_sleep 1\n" \
"s_sub_u32 s54, s54, 1\n" \
"s_cbranch_scc0 .Lswo_z15_%=\n" \
"s_waitcnt vmcnt(36)\n" \
"ds_read_b64_tr_b16 a[24:25], v1 offset:24576\n" \
"ds_read_b64_tr_b16 a[26:27], v1 offset:26624\n" \
"ds_read_b64_tr_b16 a[152:153], v1 offset:24608\n" \
"ds_read_b64_tr_b16 a[154:155], v1 offset:26656\n" \
"ds_read_b64_tr_b16 v[152:153], v1 offset:24640\n" \
"ds_read_b64_tr_b16 v[154:155], v1 offset:26688\n" \
"ds_read_b64_tr_b16 v[40:41], v1 offset:24672\n" \
"ds_read_b64_tr_b16 v[42:43], v1 offset:26720\n" \
"s_waitcnt lgkmcnt(0)\n" \
"s_add_u32 m0, s52, 24576\n" \
"s_mov_b32 s54, 1048576\n" \
"buffer_load_dwordx4 v0, s[40:43], s54 offen sc1 lds\n" \
"s_add_u32 m0, s52, 25600\n" \
"s_mov_b32 s54, 1064960\n" \
"buffer_load_dwordx4 v0, s[40:43], s54 offen sc1 lds\n" \
"s_add_u32 m0, s52, 26624\n" \
"s_mov_b32 s54, 1081344\n" \
"buffer_load_dwordx4 v0, s[40:43], s54 offen sc1 lds\n" \
"s_add_u32 m0, s52, 27648\n" \
"s_mov_b32 s54, 1097728\n" \
"buffer_load_dwordx4 v0, s[40:43], s54 offen sc1 lds\n" \
"s_mov_b32 s54, s99\n" \
".Lswo_z16_%=:\n" \
"s_sleep 1\n" \
"s_sub_u32 s54, s54, 1\n" \
"s_cbranch_scc0 .Lswo_z16_%=\n" \
"s_waitcnt vmcnt(36)\n" \
"ds_read_b64_tr_b16 a[28:29], v1 offset:28672\n" \
"ds_read_b64_tr_b16 a[30:31], v1 offset:30720\n" \
"ds_read_b64_tr_b16 a[156:157], v1 offset:28704\n" \
"ds_read_b64_tr_b16 a[158:159], v1 offset:30752\n" \
"ds_read_b64_tr_b16 v[156:157], v1 offset:28736\n" \
"ds_read_b64_tr_b16 v[158:159], v1 offset:30784\n" \
"ds_read_b64_tr_b16 v[44:45], v1 offset:28768\n" \
"ds_read_b64_tr_b16 v[46:47], v1 offset:30816\n" \
"s_waitcnt lgkmcnt(0)\n" \
"s_add_u32 m0, s52, 28672\n" \
"s_mov_b32 s54, 1114112\n" \
"buffer_load_dwordx4 v0, s[40:43], s54 offen sc1 lds\n" \
"s_add_u32 m0, s52, 29696\n" \
"s_mov_b32 s54, 1130496\n" \
"buffer_load_dwordx4 v0, s[40:43], s54 offen sc1 lds\n" \
"s_add_u32 m0, s52, 30720\n" \
"s_mov_b32 s54, 1146880\n" \
"buffer_load_dwordx4 v0, s[40:43], s54 offen sc1 lds\n" \
"s_add_u32 m0, s52, 31744\n" \
"s_mov_b32 s54, 1163264\n" \
"buffer_load_dwordx4 v0, s[40:43], s54 offen sc1 lds\n" \
"s_mov_b32 s54, s99\n" \
".Lswo_z17_%=:\n" \
"s_sleep 1\n" \
"s_sub_u32 s54, s54, 1\n" \
"s_cbranch_scc0 .Lswo_z17_%=\n" \
"s_waitcnt vmcnt(36)\n" \
"ds_read_b64_tr_b16 a[32:33], v9 offset:32768\n" \
"ds_read_b64_tr_b16 a[34:35], v9 offset:34816\n" \
"ds_read_b64_tr_b16 a[160:161], v9 offset:32800\n" \
"ds_read_b64_tr_b16 a[162:163], v9 offset:34848\n" \
"ds_read_b64_tr_b16 v[160:161], v9 offset:32832\n" \
"ds_read_b64_tr_b16 v[162:163], v9 offset:34880\n" \
"ds_read_b64_tr_b16 v[48:49], v9 offset:32864\n" \
"ds_read_b64_tr_b16 v[50:51], v9 offset:34912\n" \
"s_waitcnt lgkmcnt(0)\n" \
"s_add_u32 m0, s53, 32768\n" \
"s_mov_b32 s54, 1179648\n" \
"buffer_load_dwordx4 v0, s[40:43], s54 offen sc1 lds\n" \
"s_add_u32 m0, s53, 33792\n" \
"s_mov_b32 s54, 1196032\n" \
"buffer_load_dwordx4 v0, s[40:43], s54 offen sc1 lds\n" \
"s_add_u32 m0, s53, 34816\n" \
"s_mov_b32 s54, 1212416\n" \
"buffer_load_dwordx4 v0, s[40:43], s54 offen sc1 lds\n" \
"s_add_u32 m0, s53, 35840\n" \
"s_mov_b32 s54, 1228800\n" \
"buffer_load_dwordx4 v0, s[40:43], s54 offen sc1 lds\n" \
"s_mov_b32 s54, s99\n" \
".Lswo_z18_%=:\n" \
"s_sleep 1\n" \
"s_sub_u32 s54, s54, 1\n" \
"s_cbranch_scc0 .Lswo_z18_%=\n" \
"s_waitcnt vmcnt(36)\n" \
"ds_read_b64_tr_b16 a[36:37], v9 offset:36864\n" \
"ds_read_b64_tr_b16 a[38:39], v9 offset:38912\n" \
"ds_read_b64_tr_b16 a[164:165], v9 offset:36896\n" \
"ds_read_b64_tr_b16 a[166:167], v9 offset:38944\n" \
"ds_read_b64_tr_b16 v[164:165], v9 offset:36928\n" \
"ds_read_b64_tr_b16 v[166:167], v9 offset:38976\n" \
"ds_read_b64_tr_b16 v[52:53], v9 offset:36960\n" \
"ds_read_b64_tr_b16 v[54:55], v9 offset:39008\n" \
"s_waitcnt lgkmcnt(0)\n" \
"s_add_u32 m0, s53, 36864\n" \
"s_mov_b32 s54, 1245184\n" \
"buffer_load_dwordx4 v0, s[40:43], s54 offen sc1 lds\n" \
"s_add_u32 m0, s53, 37888\n" \
"s_mov_b32 s54, 1261568\n" \
"buffer_load_dwordx4 v0, s[40:43], s54 offen sc1 lds\n" \
"s_add_u32 m0, s53, 38912\n" \
"s_mov_b32 s54, 1277952\n" \
"buffer_load_dwordx4 v0, s[40:43], s54 offen sc1 lds\n" \
"s_add_u32 m0, s53, 39936\n" \
"s_mov_b32 s54, 1294336\n" \
"buffer_load_dwordx4 v0, s[40:43], s54 offen sc1 lds\n" \
"s_mov_b32 s54, s99\n" \
".Lswo_z19_%=:\n" \
"s_sleep 1\n" \
"s_sub_u32 s54, s54, 1\n" \
"s_cbranch_scc0 .Lswo_z19_%=\n" \
"s_waitcnt vmcnt(36)\n" \
"ds_read_b64_tr_b16 a[40:41], v1 offset:0\n" \
"ds_read_b64_tr_b16 a[42:43], v1 offset:2048\n" \
"ds_read_b64_tr_b16 a[168:169], v1 offset:32\n" \
"ds_read_b64_tr_b16 a[170:171], v1 offset:2080\n" \
"ds_read_b64_tr_b16 v[168:169], v1 offset:64\n" \
"ds_read_b64_tr_b16 v[170:171], v1 offset:2112\n" \
"ds_read_b64_tr_b16 v[56:57], v1 offset:96\n" \
"ds_read_b64_tr_b16 v[58:59], v1 offset:2144\n" \
"s_waitcnt lgkmcnt(0)\n" \
"s_add_u32 m0, s52, 0\n" \
"s_mov_b32 s54, 1310720\n" \
"buffer_load_dwordx4 v0, s[40:43], s54 offen sc1 lds\n" \
"s_add_u32 m0, s52, 1024\n" \
"s_mov_b32 s54, 1327104\n" \
"buffer_load_dwordx4 v0, s[40:43], s54 offen sc1 lds\n" \
"s_add_u32 m0, s52, 2048\n" \
"s_mov_b32 s54, 1343488\n" \
"buffer_load_dwordx4 v0, s[40:43], s54 offen sc1 lds\n" \
"s_add_u32 m0, s52, 3072\n" \
"s_mov_b32 s54, 1359872\n" \
"buffer_load_dwordx4 v0, s[40:43], s54 offen sc1 lds\n" \
"s_mov_b32 s54, s99\n" \
".Lswo_z20_%=:\n" \
"s_sleep 1\n" \
"s_sub_u32 s54, s54, 1\n" \
"s_cbranch_scc0 .Lswo_z20_%=\n" \
"s_waitcnt vmcnt(36)\n" \
"ds_read_b64_tr_b16 a[44:45], v1 offset:4096\n" \
"ds_read_b64_tr_b16 a[46:47], v1 offset:6144\n" \
"ds_read_b64_tr_b16 a[172:173], v1 offset:4128\n" \
"ds_read_b64_tr_b16 a[174:175], v1 offset:6176\n" \
"ds_read_b64_tr_b16 v[172:173], v1 offset:4160\n" \
"ds_read_b64_tr_b16 v[174:175], v1 offset:6208\n" \
"ds_read_b64_tr_b16 v[60:61], v1 offset:4192\n" \
"ds_read_b64_tr_b16 v[62:63], v1 offset:6240\n" \
"s_waitcnt lgkmcnt(0)\n" \
"s_add_u32 m0, s52, 4096\n" \
"s_mov_b32 s54, 1376256\n" \
"buffer_load_dwordx4 v0, s[40:43], s54 offen sc1 lds\n" \
"s_add_u32 m0, s52, 5120\n" \
"s_mov_b32 s54, 1392640\n" \
"buffer_load_dwordx4 v0, s[40:43], s54 offen sc1 lds\n" \
"s_add_u32 m0, s52, 6144\n" \
"s_mov_b32 s54, 1409024\n" \
"buffer_load_dwordx4 v0, s[40:43], s54 offen sc1 lds\n" \
"s_add_u32 m0, s52, 7168\n" \
"s_mov_b32 s54, 1425408\n" \
"buffer_load_dwordx4 v0, s[40:43], s54 offen sc1 lds\n" \
"s_mov_b32 s54, s99\n" \
".Lswo_z21_%=:\n" \
"s_sleep 1\n" \
"s_sub_u32 s54, s54, 1\n" \
"s_cbranch_scc0 .Lswo_z21_%=\n" \
"s_waitcnt vmcnt(36)\n" \
"ds_read_b64_tr_b16 a[48:49], v1 offset:8192\n" \
"ds_read_b64_tr_b16 a[50:51], v1 offset:10240\n" \
"ds_read_b64_tr_b16 a[176:177], v1 offset:8224\n" \
"ds_read_b64_tr_b16 a[178:179], v1 offset:10272\n" \
"ds_read_b64_tr_b16 v[176:177], v1 offset:8256\n" \
"ds_read_b64_tr_b16 v[178:179], v1 offset:10304\n" \
"ds_read_b64_tr_b16 v[64:65], v1 offset:8288\n" \
"ds_read_b64_tr_b16 v[66:67], v1 offset:10336\n" \
"s_waitcnt lgkmcnt(0)\n" \
"s_add_u32 m0, s52, 8192\n" \
"s_mov_b32 s54, 1441792\n" \
"buffer_load_dwordx4 v0, s[40:43], s54 offen sc1 lds\n" \
"s_add_u32 m0, s52, 9216\n" \
"s_mov_b32 s54, 1458176\n" \
"buffer_load_dwordx4 v0, s[40:43], s54 offen sc1 lds\n" \
"s_add_u32 m0, s52, 10240\n" \
"s_mov_b32 s54, 1474560\n" \
"buffer_load_dwordx4 v0, s[40:43], s54 offen sc1 lds\n" \
"s_add_u32 m0, s52, 11264\n" \
"s_mov_b32 s54, 1490944\n" \
"buffer_load_dwordx4 v0, s[40:43], s54 offen sc1 lds\n" \
"s_mov_b32 s54, s99\n" \
".Lswo_z22_%=:\n" \
"s_sleep 1\n" \
"s_sub_u32 s54, s54, 1\n" \
"s_cbranch_scc0 .Lswo_z22_%=\n" \
"s_waitcnt vmcnt(36)\n" \
"ds_read_b64_tr_b16 a[52:53], v1 offset:12288\n" \
"ds_read_b64_tr_b16 a[54:55], v1 offset:14336\n" \
"ds_read_b64_tr_b16 a[180:181], v1 offset:12320\n" \
"ds_read_b64_tr_b16 a[182:183], v1 offset:14368\n" \
"ds_read_b64_tr_b16 v[180:181], v1 offset:12352\n" \
"ds_read_b64_tr_b16 v[182:183], v1 offset:14400\n" \
"ds_read_b64_tr_b16 v[68:69], v1 offset:12384\n" \
"ds_read_b64_tr_b16 v[70:71], v1 offset:14432\n" \
"s_waitcnt lgkmcnt(0)\n" \
"s_add_u32 m0, s52, 12288\n" \
"s_mov_b32 s54, 1507328\n" \
"buffer_load_dwordx4 v0, s[40:43], s54 offen sc1 lds\n" \
"s_add_u32 m0, s52, 13312\n" \
"s_mov_b32 s54, 1523712\n" \
"buffer_load_dwordx4 v0, s[40:43], s54 offen sc1 lds\n" \
"s_add_u32 m0, s52, 14336\n" \
"s_mov_b32 s54, 1540096\n" \
"buffer_load_dwordx4 v0, s[40:43], s54 offen sc1 lds\n" \
"s_add_u32 m0, s52, 15360\n" \
"s_mov_b32 s54, 1556480\n" \
"buffer_load_dwordx4 v0, s[40:43], s54 offen sc1 lds\n" \
"s_mov_b32 s54, s99\n" \
".Lswo_z23_%=:\n" \
"s_sleep 1\n" \
"s_sub_u32 s54, s54, 1\n" \
"s_cbranch_scc0 .Lswo_z23_%=\n" \
"s_waitcnt vmcnt(36)\n" \
"ds_read_b64_tr_b16 a[56:57], v1 offset:16384\n" \
"ds_read_b64_tr_b16 a[58:59], v1 offset:18432\n" \
"ds_read_b64_tr_b16 a[184:185], v1 offset:16416\n" \
"ds_read_b64_tr_b16 a[186:187], v1 offset:18464\n" \
"ds_read_b64_tr_b16 v[184:185], v1 offset:16448\n" \
"ds_read_b64_tr_b16 v[186:187], v1 offset:18496\n" \
"ds_read_b64_tr_b16 v[72:73], v1 offset:16480\n" \
"ds_read_b64_tr_b16 v[74:75], v1 offset:18528\n" \
"s_waitcnt lgkmcnt(0)\n" \
"s_add_u32 m0, s52, 16384\n" \
"s_mov_b32 s54, 1572864\n" \
"buffer_load_dwordx4 v0, s[40:43], s54 offen sc1 lds\n" \
"s_add_u32 m0, s52, 17408\n" \
"s_mov_b32 s54, 1589248\n" \
"buffer_load_dwordx4 v0, s[40:43], s54 offen sc1 lds\n" \
"s_add_u32 m0, s52, 18432\n" \
"s_mov_b32 s54, 1605632\n" \
"buffer_load_dwordx4 v0, s[40:43], s54 offen sc1 lds\n" \
"s_add_u32 m0, s52, 19456\n" \
"s_mov_b32 s54, 1622016\n" \
"buffer_load_dwordx4 v0, s[40:43], s54 offen sc1 lds\n" \
"s_mov_b32 s54, s99\n" \
".Lswo_z24_%=:\n" \
"s_sleep 1\n" \
"s_sub_u32 s54, s54, 1\n" \
"s_cbranch_scc0 .Lswo_z24_%=\n" \
"s_waitcnt vmcnt(36)\n" \
"ds_read_b64_tr_b16 a[60:61], v1 offset:20480\n" \
"ds_read_b64_tr_b16 a[62:63], v1 offset:22528\n" \
"ds_read_b64_tr_b16 a[188:189], v1 offset:20512\n" \
"ds_read_b64_tr_b16 a[190:191], v1 offset:22560\n" \
"ds_read_b64_tr_b16 v[188:189], v1 offset:20544\n" \
"ds_read_b64_tr_b16 v[190:191], v1 offset:22592\n" \
"ds_read_b64_tr_b16 v[76:77], v1 offset:20576\n" \
"ds_read_b64_tr_b16 v[78:79], v1 offset:22624\n" \
"s_waitcnt lgkmcnt(0)\n" \
"s_add_u32 m0, s52, 20480\n" \
"s_mov_b32 s54, 1638400\n" \
"buffer_load_dwordx4 v0, s[40:43], s54 offen sc1 lds\n" \
"s_add_u32 m0, s52, 21504\n" \
"s_mov_b32 s54, 1654784\n" \
"buffer_load_dwordx4 v0, s[40:43], s54 offen sc1 lds\n" \
"s_add_u32 m0, s52, 22528\n" \
"s_mov_b32 s54, 1671168\n" \
"buffer_load_dwordx4 v0, s[40:43], s54 offen sc1 lds\n" \
"s_add_u32 m0, s52, 23552\n" \
"s_mov_b32 s54, 1687552\n" \
"buffer_load_dwordx4 v0, s[40:43], s54 offen sc1 lds\n" \
"s_mov_b32 s54, s99\n" \
".Lswo_z25_%=:\n" \
"s_sleep 1\n" \
"s_sub_u32 s54, s54, 1\n" \
"s_cbranch_scc0 .Lswo_z25_%=\n" \
"s_waitcnt vmcnt(36)\n" \
"ds_read_b64_tr_b16 a[64:65], v1 offset:24576\n" \
"ds_read_b64_tr_b16 a[66:67], v1 offset:26624\n" \
"ds_read_b64_tr_b16 a[192:193], v1 offset:24608\n" \
"ds_read_b64_tr_b16 a[194:195], v1 offset:26656\n" \
"ds_read_b64_tr_b16 v[192:193], v1 offset:24640\n" \
"ds_read_b64_tr_b16 v[194:195], v1 offset:26688\n" \
"ds_read_b64_tr_b16 v[80:81], v1 offset:24672\n" \
"ds_read_b64_tr_b16 v[82:83], v1 offset:26720\n" \
"s_waitcnt lgkmcnt(0)\n" \
"s_add_u32 m0, s52, 24576\n" \
"s_mov_b32 s54, 1703936\n" \
"buffer_load_dwordx4 v0, s[40:43], s54 offen sc1 lds\n" \
"s_add_u32 m0, s52, 25600\n" \
"s_mov_b32 s54, 1720320\n" \
"buffer_load_dwordx4 v0, s[40:43], s54 offen sc1 lds\n" \
"s_add_u32 m0, s52, 26624\n" \
"s_mov_b32 s54, 1736704\n" \
"buffer_load_dwordx4 v0, s[40:43], s54 offen sc1 lds\n" \
"s_add_u32 m0, s52, 27648\n" \
"s_mov_b32 s54, 1753088\n" \
"buffer_load_dwordx4 v0, s[40:43], s54 offen sc1 lds\n" \
"s_mov_b32 s54, s99\n" \
".Lswo_z26_%=:\n" \
"s_sleep 1\n" \
"s_sub_u32 s54, s54, 1\n" \
"s_cbranch_scc0 .Lswo_z26_%=\n" \
"s_waitcnt vmcnt(36)\n" \
"ds_read_b64_tr_b16 a[68:69], v1 offset:28672\n" \
"ds_read_b64_tr_b16 a[70:71], v1 offset:30720\n" \
"ds_read_b64_tr_b16 a[196:197], v1 offset:28704\n" \
"ds_read_b64_tr_b16 a[198:199], v1 offset:30752\n" \
"ds_read_b64_tr_b16 v[196:197], v1 offset:28736\n" \
"ds_read_b64_tr_b16 v[198:199], v1 offset:30784\n" \
"ds_read_b64_tr_b16 v[84:85], v1 offset:28768\n" \
"ds_read_b64_tr_b16 v[86:87], v1 offset:30816\n" \
"s_waitcnt lgkmcnt(0)\n" \
"s_add_u32 m0, s52, 28672\n" \
"s_mov_b32 s54, 1769472\n" \
"buffer_load_dwordx4 v0, s[40:43], s54 offen sc1 lds\n" \
"s_add_u32 m0, s52, 29696\n" \
"s_mov_b32 s54, 1785856\n" \
"buffer_load_dwordx4 v0, s[40:43], s54 offen sc1 lds\n" \
"s_add_u32 m0, s52, 30720\n" \
"s_mov_b32 s54, 1802240\n" \
"buffer_load_dwordx4 v0, s[40:43], s54 offen sc1 lds\n" \
"s_add_u32 m0, s52, 31744\n" \
"s_mov_b32 s54, 1818624\n" \
"buffer_load_dwordx4 v0, s[40:43], s54 offen sc1 lds\n" \
"s_mov_b32 s54, s99\n" \
".Lswo_z27_%=:\n" \
"s_sleep 1\n" \
"s_sub_u32 s54, s54, 1\n" \
"s_cbranch_scc0 .Lswo_z27_%=\n" \
"s_waitcnt vmcnt(36)\n" \
"ds_read_b64_tr_b16 a[72:73], v9 offset:32768\n" \
"ds_read_b64_tr_b16 a[74:75], v9 offset:34816\n" \
"ds_read_b64_tr_b16 a[200:201], v9 offset:32800\n" \
"ds_read_b64_tr_b16 a[202:203], v9 offset:34848\n" \
"ds_read_b64_tr_b16 v[200:201], v9 offset:32832\n" \
"ds_read_b64_tr_b16 v[202:203], v9 offset:34880\n" \
"ds_read_b64_tr_b16 v[88:89], v9 offset:32864\n" \
"ds_read_b64_tr_b16 v[90:91], v9 offset:34912\n" \
"s_waitcnt lgkmcnt(0)\n" \
"s_add_u32 m0, s53, 32768\n" \
"s_mov_b32 s54, 1835008\n" \
"buffer_load_dwordx4 v0, s[40:43], s54 offen sc1 lds\n" \
"s_add_u32 m0, s53, 33792\n" \
"s_mov_b32 s54, 1851392\n" \
"buffer_load_dwordx4 v0, s[40:43], s54 offen sc1 lds\n" \
"s_add_u32 m0, s53, 34816\n" \
"s_mov_b32 s54, 1867776\n" \
"buffer_load_dwordx4 v0, s[40:43], s54 offen sc1 lds\n" \
"s_add_u32 m0, s53, 35840\n" \
"s_mov_b32 s54, 1884160\n" \
"buffer_load_dwordx4 v0, s[40:43], s54 offen sc1 lds\n" \
"s_mov_b32 s54, s99\n" \
".Lswo_z28_%=:\n" \
"s_sleep 1\n" \
"s_sub_u32 s54, s54, 1\n" \
"s_cbranch_scc0 .Lswo_z28_%=\n" \
"s_waitcnt vmcnt(36)\n" \
"ds_read_b64_tr_b16 a[76:77], v9 offset:36864\n" \
"ds_read_b64_tr_b16 a[78:79], v9 offset:38912\n" \
"ds_read_b64_tr_b16 a[204:205], v9 offset:36896\n" \
"ds_read_b64_tr_b16 a[206:207], v9 offset:38944\n" \
"ds_read_b64_tr_b16 v[204:205], v9 offset:36928\n" \
"ds_read_b64_tr_b16 v[206:207], v9 offset:38976\n" \
"ds_read_b64_tr_b16 v[92:93], v9 offset:36960\n" \
"ds_read_b64_tr_b16 v[94:95], v9 offset:39008\n" \
"s_waitcnt lgkmcnt(0)\n" \
"s_add_u32 m0, s53, 36864\n" \
"s_mov_b32 s54, 1900544\n" \
"buffer_load_dwordx4 v0, s[40:43], s54 offen sc1 lds\n" \
"s_add_u32 m0, s53, 37888\n" \
"s_mov_b32 s54, 1916928\n" \
"buffer_load_dwordx4 v0, s[40:43], s54 offen sc1 lds\n" \
"s_add_u32 m0, s53, 38912\n" \
"s_mov_b32 s54, 1933312\n" \
"buffer_load_dwordx4 v0, s[40:43], s54 offen sc1 lds\n" \
"s_add_u32 m0, s53, 39936\n" \
"s_mov_b32 s54, 1949696\n" \
"buffer_load_dwordx4 v0, s[40:43], s54 offen sc1 lds\n" \
"s_mov_b32 s54, s99\n" \
".Lswo_z29_%=:\n" \
"s_sleep 1\n" \
"s_sub_u32 s54, s54, 1\n" \
"s_cbranch_scc0 .Lswo_z29_%=\n" \
"s_waitcnt vmcnt(36)\n" \
"ds_read_b64_tr_b16 a[80:81], v1 offset:0\n" \
"ds_read_b64_tr_b16 a[82:83], v1 offset:2048\n" \
"ds_read_b64_tr_b16 a[208:209], v1 offset:32\n" \
"ds_read_b64_tr_b16 a[210:211], v1 offset:2080\n" \
"ds_read_b64_tr_b16 v[208:209], v1 offset:64\n" \
"ds_read_b64_tr_b16 v[210:211], v1 offset:2112\n" \
"ds_read_b64_tr_b16 v[96:97], v1 offset:96\n" \
"ds_read_b64_tr_b16 v[98:99], v1 offset:2144\n" \
"s_waitcnt lgkmcnt(0)\n" \
"s_add_u32 m0, s52, 0\n" \
"s_mov_b32 s54, 1966080\n" \
"buffer_load_dwordx4 v0, s[40:43], s54 offen sc1 lds\n" \
"s_add_u32 m0, s52, 1024\n" \
"s_mov_b32 s54, 1982464\n" \
"buffer_load_dwordx4 v0, s[40:43], s54 offen sc1 lds\n" \
"s_add_u32 m0, s52, 2048\n" \
"s_mov_b32 s54, 1998848\n" \
"buffer_load_dwordx4 v0, s[40:43], s54 offen sc1 lds\n" \
"s_add_u32 m0, s52, 3072\n" \
"s_mov_b32 s54, 2015232\n" \
"buffer_load_dwordx4 v0, s[40:43], s54 offen sc1 lds\n" \
"s_mov_b32 s54, s99\n" \
".Lswo_z30_%=:\n" \
"s_sleep 1\n" \
"s_sub_u32 s54, s54, 1\n" \
"s_cbranch_scc0 .Lswo_z30_%=\n" \
"s_waitcnt vmcnt(36)\n" \
"ds_read_b64_tr_b16 a[84:85], v1 offset:4096\n" \
"ds_read_b64_tr_b16 a[86:87], v1 offset:6144\n" \
"ds_read_b64_tr_b16 a[212:213], v1 offset:4128\n" \
"ds_read_b64_tr_b16 a[214:215], v1 offset:6176\n" \
"ds_read_b64_tr_b16 v[212:213], v1 offset:4160\n" \
"ds_read_b64_tr_b16 v[214:215], v1 offset:6208\n" \
"ds_read_b64_tr_b16 v[100:101], v1 offset:4192\n" \
"ds_read_b64_tr_b16 v[102:103], v1 offset:6240\n" \
"s_waitcnt lgkmcnt(0)\n" \
"s_add_u32 m0, s52, 4096\n" \
"s_mov_b32 s54, 2031616\n" \
"buffer_load_dwordx4 v0, s[40:43], s54 offen sc1 lds\n" \
"s_add_u32 m0, s52, 5120\n" \
"s_mov_b32 s54, 2048000\n" \
"buffer_load_dwordx4 v0, s[40:43], s54 offen sc1 lds\n" \
"s_add_u32 m0, s52, 6144\n" \
"s_mov_b32 s54, 2064384\n" \
"buffer_load_dwordx4 v0, s[40:43], s54 offen sc1 lds\n" \
"s_add_u32 m0, s52, 7168\n" \
"s_mov_b32 s54, 2080768\n" \
"buffer_load_dwordx4 v0, s[40:43], s54 offen sc1 lds\n" \
"s_mov_b32 s54, s99\n" \
".Lswo_z31_%=:\n" \
"s_sleep 1\n" \
"s_sub_u32 s54, s54, 1\n" \
"s_cbranch_scc0 .Lswo_z31_%=\n" \
"s_waitcnt vmcnt(36)\n" \
"ds_read_b64_tr_b16 a[88:89], v1 offset:8192\n" \
"ds_read_b64_tr_b16 a[90:91], v1 offset:10240\n" \
"ds_read_b64_tr_b16 a[216:217], v1 offset:8224\n" \
"ds_read_b64_tr_b16 a[218:219], v1 offset:10272\n" \
"ds_read_b64_tr_b16 v[216:217], v1 offset:8256\n" \
"ds_read_b64_tr_b16 v[218:219], v1 offset:10304\n" \
"ds_read_b64_tr_b16 v[104:105], v1 offset:8288\n" \
"ds_read_b64_tr_b16 v[106:107], v1 offset:10336\n" \
"s_waitcnt lgkmcnt(0)\n" \
"ds_write_b128 v2, v[16:19] offset:8192\n" \
"ds_write_b128 v2, v[20:23] offset:9216\n" \
"ds_write_b128 v2, v[24:27] offset:10240\n" \
"ds_write_b128 v2, v[28:31] offset:11264\n" \
"s_waitcnt vmcnt(32)\n" \
"ds_read_b64_tr_b16 a[92:93], v1 offset:12288\n" \
"ds_read_b64_tr_b16 a[94:95], v1 offset:14336\n" \
"ds_read_b64_tr_b16 a[220:221], v1 offset:12320\n" \
"ds_read_b64_tr_b16 a[222:223], v1 offset:14368\n" \
"ds_read_b64_tr_b16 v[220:221], v1 offset:12352\n" \
"ds_read_b64_tr_b16 v[222:223], v1 offset:14400\n" \
"ds_read_b64_tr_b16 v[108:109], v1 offset:12384\n" \
"ds_read_b64_tr_b16 v[110:111], v1 offset:14432\n" \
"s_waitcnt lgkmcnt(0)\n" \
"ds_write_b128 v2, v[32:35] offset:12288\n" \
"ds_write_b128 v2, v[36:39] offset:13312\n" \
"ds_write_b128 v2, v[40:43] offset:14336\n" \
"ds_write_b128 v2, v[44:47] offset:15360\n" \
"s_waitcnt vmcnt(28)\n" \
"ds_read_b64_tr_b16 a[96:97], v1 offset:16384\n" \
"ds_read_b64_tr_b16 a[98:99], v1 offset:18432\n" \
"ds_read_b64_tr_b16 a[224:225], v1 offset:16416\n" \
"ds_read_b64_tr_b16 a[226:227], v1 offset:18464\n" \
"ds_read_b64_tr_b16 v[224:225], v1 offset:16448\n" \
"ds_read_b64_tr_b16 v[226:227], v1 offset:18496\n" \
"ds_read_b64_tr_b16 v[16:17], v1 offset:16480\n" \
"ds_read_b64_tr_b16 v[18:19], v1 offset:18528\n" \
"s_waitcnt lgkmcnt(0)\n" \
"ds_write_b128 v2, v[48:51] offset:16384\n" \
"ds_write_b128 v2, v[52:55] offset:17408\n" \
"ds_write_b128 v2, v[56:59] offset:18432\n" \
"ds_write_b128 v2, v[60:63] offset:19456\n" \
"s_waitcnt vmcnt(24)\n" \
"ds_read_b64_tr_b16 a[100:101], v1 offset:20480\n" \
"ds_read_b64_tr_b16 a[102:103], v1 offset:22528\n" \
"ds_read_b64_tr_b16 a[228:229], v1 offset:20512\n" \
"ds_read_b64_tr_b16 a[230:231], v1 offset:22560\n" \
"ds_read_b64_tr_b16 v[228:229], v1 offset:20544\n" \
"ds_read_b64_tr_b16 v[230:231], v1 offset:22592\n" \
"ds_read_b64_tr_b16 v[20:21], v1 offset:20576\n" \
"ds_read_b64_tr_b16 v[22:23], v1 offset:22624\n" \
"s_waitcnt lgkmcnt(0)\n" \
"ds_write_b128 v2, v[64:67] offset:20480\n" \
"ds_write_b128 v2, v[68:71] offset:21504\n" \
"ds_write_b128 v2, v[72:75] offset:22528\n" \
"ds_write_b128 v2, v[76:79] offset:23552\n" \
"s_waitcnt vmcnt(20)\n" \
"ds_read_b64_tr_b16 a[104:105], v1 offset:24576\n" \
"ds_read_b64_tr_b16 a[106:107], v1 offset:26624\n" \
"ds_read_b64_tr_b16 a[232:233], v1 offset:24608\n" \
"ds_read_b64_tr_b16 a[234:235], v1 offset:26656\n" \
"ds_read_b64_tr_b16 v[232:233], v1 offset:24640\n" \
"ds_read_b64_tr_b16 v[234:235], v1 offset:26688\n" \
"ds_read_b64_tr_b16 v[24:25], v1 offset:24672\n" \
"ds_read_b64_tr_b16 v[26:27], v1 offset:26720\n" \
"s_waitcnt lgkmcnt(0)\n" \
"ds_write_b128 v2, v[80:83] offset:24576\n" \
"ds_write_b128 v2, v[84:87] offset:25600\n" \
"ds_write_b128 v2, v[88:91] offset:26624\n" \
"ds_write_b128 v2, v[92:95] offset:27648\n" \
"s_waitcnt vmcnt(16)\n" \
"ds_read_b64_tr_b16 a[108:109], v1 offset:28672\n" \
"ds_read_b64_tr_b16 a[110:111], v1 offset:30720\n" \
"ds_read_b64_tr_b16 a[236:237], v1 offset:28704\n" \
"ds_read_b64_tr_b16 a[238:239], v1 offset:30752\n" \
"ds_read_b64_tr_b16 v[236:237], v1 offset:28736\n" \
"ds_read_b64_tr_b16 v[238:239], v1 offset:30784\n" \
"ds_read_b64_tr_b16 v[28:29], v1 offset:28768\n" \
"ds_read_b64_tr_b16 v[30:31], v1 offset:30816\n" \
"s_waitcnt lgkmcnt(0)\n" \
"ds_write_b128 v2, v[96:99] offset:28672\n" \
"ds_write_b128 v2, v[100:103] offset:29696\n" \
"ds_write_b128 v2, v[104:107] offset:30720\n" \
"ds_write_b128 v2, v[108:111] offset:31744\n" \
"s_waitcnt vmcnt(12)\n" \
"ds_read_b64_tr_b16 a[112:113], v9 offset:32768\n" \
"ds_read_b64_tr_b16 a[114:115], v9 offset:34816\n" \
"ds_read_b64_tr_b16 a[240:241], v9 offset:32800\n" \
"ds_read_b64_tr_b16 a[242:243], v9 offset:34848\n" \
"ds_read_b64_tr_b16 v[240:241], v9 offset:32832\n" \
"ds_read_b64_tr_b16 v[242:243], v9 offset:34880\n" \
"ds_read_b64_tr_b16 v[32:33], v9 offset:32864\n" \
"ds_read_b64_tr_b16 v[34:35], v9 offset:34912\n" \
"s_waitcnt lgkmcnt(0)\n" \
"s_waitcnt vmcnt(8)\n" \
"ds_read_b64_tr_b16 a[116:117], v9 offset:36864\n" \
"ds_read_b64_tr_b16 a[118:119], v9 offset:38912\n" \
"ds_read_b64_tr_b16 a[244:245], v9 offset:36896\n" \
"ds_read_b64_tr_b16 a[246:247], v9 offset:38944\n" \
"ds_read_b64_tr_b16 v[244:245], v9 offset:36928\n" \
"ds_read_b64_tr_b16 v[246:247], v9 offset:38976\n" \
"ds_read_b64_tr_b16 v[36:37], v9 offset:36960\n" \
"ds_read_b64_tr_b16 v[38:39], v9 offset:39008\n" \
"s_waitcnt lgkmcnt(0)\n" \
"s_waitcnt vmcnt(4)\n" \
"ds_read_b64_tr_b16 a[120:121], v1 offset:0\n" \
"ds_read_b64_tr_b16 a[122:123], v1 offset:2048\n" \
"ds_read_b64_tr_b16 a[248:249], v1 offset:32\n" \
"ds_read_b64_tr_b16 a[250:251], v1 offset:2080\n" \
"ds_read_b64_tr_b16 v[248:249], v1 offset:64\n" \
"ds_read_b64_tr_b16 v[250:251], v1 offset:2112\n" \
"ds_read_b64_tr_b16 v[40:41], v1 offset:96\n" \
"ds_read_b64_tr_b16 v[42:43], v1 offset:2144\n" \
"s_waitcnt lgkmcnt(0)\n" \
"ds_write_b128 v2, v[16:19] offset:0\n" \
"ds_write_b128 v2, v[20:23] offset:1024\n" \
"ds_write_b128 v2, v[24:27] offset:2048\n" \
"ds_write_b128 v2, v[28:31] offset:3072\n" \
"s_waitcnt vmcnt(0)\n" \
"ds_read_b64_tr_b16 a[124:125], v1 offset:4096\n" \
"ds_read_b64_tr_b16 a[126:127], v1 offset:6144\n" \
"ds_read_b64_tr_b16 a[252:253], v1 offset:4128\n" \
"ds_read_b64_tr_b16 a[254:255], v1 offset:6176\n" \
"ds_read_b64_tr_b16 v[252:253], v1 offset:4160\n" \
"ds_read_b64_tr_b16 v[254:255], v1 offset:6208\n" \
"ds_read_b64_tr_b16 v[44:45], v1 offset:4192\n" \
"ds_read_b64_tr_b16 v[46:47], v1 offset:6240\n" \
"s_waitcnt lgkmcnt(0)\n" \
"ds_write_b128 v2, v[32:35] offset:4096\n" \
"ds_write_b128 v2, v[36:39] offset:5120\n" \
"ds_write_b128 v2, v[40:43] offset:6144\n" \
"ds_write_b128 v2, v[44:47] offset:7168\n" \
"s_waitcnt vmcnt(0) lgkmcnt(0)\n" \
"s_barrier\n" \
"s_add_u32 s86, s85, 0\n" \
"s_add_u32 s87, s85, 4259840\n" \
"s_add_u32 s88, s85, 8519680\n" \
"s_add_u32 s89, s85, 12779520\n" \
"s_add_u32 s90, s85, 17039360\n" \
"s_add_u32 s91, s85, 21299200\n" \
"s_add_u32 s92, s85, 25559040\n" \
"s_add_u32 s93, s85, 29818880\n" \
"s_mov_b32 s55, 0\n" \
"s_mov_b32 s60, 0\n" \
"s_mov_b32 s79, s95\n" \
"s_mov_b32 s98, 0x70000000\n" \
".Lswo_prime_%=:\n" \
"s_lshr_b32 s59, s44, 1\n" \
"s_add_u32 s59, s59, 1\n" \
"s_lshr_b32 s56, s44, 1\n" \
"s_lshl_b32 s56, s56, 16\n" \
"s_lshr_b32 s58, s56, 8\n" \
"s_and_b32 s58, s58, 0x2000\n" \
"s_xor_b32 s56, s56, s58\n" \
"s_and_b32 s58, s44, 1\n" \
"s_lshl_b32 s58, s58, 12\n" \
"s_or_b32 s56, s56, s58\n" \
"s_lshl_b32 s54, s44, 16\n" \
"s_add_u32 s54, s54, s78\n" \
"s_lshr_b32 s58, s54, 8\n" \
"s_and_b32 s58, s58, 0x2000\n" \
"s_xor_b32 s54, s54, s58\n" \
"s_add_u32 s54, s54, 8388608\n" \
"s_lshl_b32 s77, s44, 15\n" \
"s_or_b32 s77, s77, s98\n" \
"s_or_b32 s54, s54, s98\n" \
"buffer_load_dwordx2 v[14:15], v6, s[36:39], s54 offen sc1\n" \
"buffer_load_dwordx2 v[12:13], v5, s[64:67], s77 offen\n" \
"buffer_load_dword v11, v4, s[36:39], 0 offen sc1\n" \
"s_waitcnt vmcnt(0)\n" \
"v_cmp_gt_u32_e64 s[62:63], s59, v11\n" \
"s_nop 4\n" \
"s_cmp_lg_u64 s[62:63], 0\n" \
"s_cbranch_scc1 .Lswo_w_%=\n" \
".Lswo_wk_%=:\n" \
"s_mov_b32 s76, 0x7ffffff0\n" \
"s_mov_b32 s81, 1\n" \
"s_mov_b32 s84, 0\n" \
"s_waitcnt vmcnt(0) lgkmcnt(0)\n" \
"s_xor_b32 s57, s56, s86\n" \
"buffer_load_dwordx4 v[16:19], v3, s[36:39], s57 offen offset:0 sc1\n" \
"buffer_load_dwordx4 v[20:23], v3, s[36:39], s57 offen offset:1024 sc1\n" \
"buffer_load_dwordx4 v[24:27], v3, s[36:39], s57 offen offset:2048 sc1\n" \
"buffer_load_dwordx4 v[28:31], v3, s[36:39], s57 offen offset:3072 sc1\n" \
"s_xor_b32 s57, s56, s87\n" \
"buffer_load_dwordx4 v[32:35], v3, s[36:39], s57 offen offset:0 sc1\n" \
"buffer_load_dwordx4 v[36:39], v3, s[36:39], s57 offen offset:1024 sc1\n" \
"buffer_load_dwordx4 v[40:43], v3, s[36:39], s57 offen offset:2048 sc1\n" \
"buffer_load_dwordx4 v[44:47], v3, s[36:39], s57 offen offset:3072 sc1\n" \
"s_xor_b32 s57, s56, s88\n" \
"buffer_load_dwordx4 v[48:51], v3, s[36:39], s57 offen offset:0 sc1\n" \
"buffer_load_dwordx4 v[52:55], v3, s[36:39], s57 offen offset:1024 sc1\n" \
"buffer_load_dwordx4 v[56:59], v3, s[36:39], s57 offen offset:2048 sc1\n" \
"buffer_load_dwordx4 v[60:63], v3, s[36:39], s57 offen offset:3072 sc1\n" \
"s_xor_b32 s57, s56, s89\n" \
"buffer_load_dwordx4 v[64:67], v3, s[36:39], s57 offen offset:0 sc1\n" \
"buffer_load_dwordx4 v[68:71], v3, s[36:39], s57 offen offset:1024 sc1\n" \
"buffer_load_dwordx4 v[72:75], v3, s[36:39], s57 offen offset:2048 sc1\n" \
"buffer_load_dwordx4 v[76:79], v3, s[36:39], s57 offen offset:3072 sc1\n" \
"ds_read_b128 v[80:83], v2 offset:8192\n" \
"ds_read_b128 v[84:87], v2 offset:9216\n" \
"ds_read_b128 v[88:91], v2 offset:10240\n" \
"s_waitcnt lgkmcnt(0)\n" \
"v_mov_b32 v9, 0x7ffffff0\n" \
"buffer_store_dword v9, v9, s[36:39], 0 offen\n" \
"s_waitcnt vmcnt(16)\n" \
"s_nop 7\n" \
"s_nop 7\n" \
".p2align 6\n" \
".Lswo_loop_%=:\n" \
"v_mfma_f32_16x16x32_bf16 v[96:99], a[0:3], v[16:19], 0\n" \
"ds_write_b128 v7, v[104:107] offset:2048\n" \
"v_xor_b32_e64 v7, 64, v7\n" \
"v_mfma_f32_16x16x32_bf16 v[100:103], a[128:131], v[16:19], 0\n" \
"ds_write_b128 v7, v[108:111] offset:3072\n" \
"ds_read_b128 v[92:95], v2 offset:11264\n" \
"v_xor_b32 v7, 192, v7\n" \
"v_mfma_f32_16x16x32_bf16 v[104:107], v[128:131], v[16:19], 0\n" \
"s_waitcnt vmcnt(15) lgkmcnt(7)\n" \
"v_mfma_f32_16x16x32_bf16 v[108:111], v[80:83], v[16:19], 0\n" \
"s_xor_b32 s57, s56, s90\n" \
"buffer_load_dwordx4 v[16:19], v3, s[36:39], s57 offen offset:0 sc1\n" \
"v_mfma_f32_16x16x32_bf16 v[96:99], a[4:7], v[20:23], v[96:99]\n" \
"s_cmp_lt_i32 s44, s50\n" \
"s_cselect_b32 s82, s45, s47\n" \
"v_mfma_f32_16x16x32_bf16 v[100:103], a[132:135], v[20:23], v[100:103]\n" \
"ds_read_b128 v[80:83], v2 offset:12288\n" \
"v_mfma_f32_16x16x32_bf16 v[104:107], v[132:135], v[20:23], v[104:107]\n" \
"s_waitcnt vmcnt(15) lgkmcnt(7)\n" \
"v_mfma_f32_16x16x32_bf16 v[108:111], v[84:87], v[20:23], v[108:111]\n" \
"buffer_load_dwordx4 v[20:23], v3, s[36:39], s57 offen offset:1024 sc1\n" \
"v_mfma_f32_16x16x32_bf16 v[96:99], a[8:11], v[24:27], v[96:99]\n" \
"s_add_u32 s82, s82, s44\n" \
"s_cmp_lt_u32 s82, 128\n" \
"v_mfma_f32_16x16x32_bf16 v[100:103], a[136:139], v[24:27], v[100:103]\n" \
"ds_read_b128 v[84:87], v2 offset:13312\n" \
"s_cselect_b32 s82, s82, s94\n" \
"v_mfma_f32_16x16x32_bf16 v[104:107], v[136:139], v[24:27], v[104:107]\n" \
"s_waitcnt vmcnt(15) lgkmcnt(7)\n" \
"v_mfma_f32_16x16x32_bf16 v[108:111], v[88:91], v[24:27], v[108:111]\n" \
"buffer_load_dwordx4 v[24:27], v3, s[36:39], s57 offen offset:2048 sc1\n" \
"v_mfma_f32_16x16x32_bf16 v[96:99], a[12:15], v[28:31], v[96:99]\n" \
"s_waitcnt lgkmcnt(3)\n" \
"s_cmp_lg_u32 s81, 0\n" \
"s_cbranch_scc1 .Lswo_nb_%=\n" \
"s_barrier\n" \
".Lswo_nb_%=:\n" \
"v_mfma_f32_16x16x32_bf16 v[100:103], a[140:143], v[28:31], v[100:103]\n" \
"ds_read_b128 v[88:91], v2 offset:14336\n" \
"v_xor_b32_e64 v7, s84, v7\n" \
"v_mfma_f32_16x16x32_bf16 v[104:107], v[140:143], v[28:31], v[104:107]\n" \
"s_waitcnt vmcnt(15) lgkmcnt(3)\n" \
"s_lshr_b32 s83, s82, 1\n" \
"v_mfma_f32_16x16x32_bf16 v[108:111], v[92:95], v[28:31], v[108:111]\n" \
"buffer_load_dwordx4 v[28:31], v3, s[36:39], s57 offen offset:3072 sc1\n" \
"v_mfma_f32_16x16x32_bf16 v[96:99], a[16:19], v[32:35], v[96:99]\n" \
"ds_read_b128 v[112:115], v8 offset:0\n" \
"s_lshl_b32 s83, s83, 16\n" \
"v_mfma_f32_16x16x32_bf16 v[100:103], a[144:147], v[32:35], v[100:103]\n" \
"ds_read_b128 v[92:95], v2 offset:15360\n" \
"v_mfma_f32_16x16x32_bf16 v[104:107], v[144:147], v[32:35], v[104:107]\n" \
"s_waitcnt vmcnt(15) lgkmcnt(4)\n" \
"s_lshr_b32 s58, s83, 8\n" \
"v_mfma_f32_16x16x32_bf16 v[108:111], v[80:83], v[32:35], v[108:111]\n" \
"s_xor_b32 s57, s56, s91\n" \
"buffer_load_dwordx4 v[32:35], v3, s[36:39], s57 offen offset:0 sc1\n" \
"v_mfma_f32_16x16x32_bf16 v[96:99], a[20:23], v[36:39], v[96:99]\n" \
"ds_read_b128 v[116:119], v8 offset:4096\n" \
"s_and_b32 s58, s58, 0x2000\n" \
"v_mfma_f32_16x16x32_bf16 v[100:103], a[148:151], v[36:39], v[100:103]\n" \
"ds_read_b128 v[80:83], v2 offset:16384\n" \
"v_mfma_f32_16x16x32_bf16 v[104:107], v[148:151], v[36:39], v[104:107]\n" \
"s_waitcnt vmcnt(15) lgkmcnt(5)\n" \
"s_xor_b32 s83, s83, s58\n" \
"v_mfma_f32_16x16x32_bf16 v[108:111], v[84:87], v[36:39], v[108:111]\n" \
"buffer_load_dwordx4 v[36:39], v3, s[36:39], s57 offen offset:1024 sc1\n" \
"v_mfma_f32_16x16x32_bf16 v[96:99], a[24:27], v[40:43], v[96:99]\n" \
"ds_read_b128 v[120:123], v8 offset:8192\n" \
"s_and_b32 s58, s82, 1\n" \
"v_mfma_f32_16x16x32_bf16 v[100:103], a[152:155], v[40:43], v[100:103]\n" \
"ds_read_b128 v[84:87], v2 offset:17408\n" \
"v_mfma_f32_16x16x32_bf16 v[104:107], v[152:155], v[40:43], v[104:107]\n" \
"s_waitcnt vmcnt(15) lgkmcnt(6)\n" \
"s_lshl_b32 s58, s58, 12\n" \
"v_mfma_f32_16x16x32_bf16 v[108:111], v[88:91], v[40:43], v[108:111]\n" \
"buffer_load_dwordx4 v[40:43], v3, s[36:39], s57 offen offset:2048 sc1\n" \
"v_mfma_f32_16x16x32_bf16 v[96:99], a[28:31], v[44:47], v[96:99]\n" \
"ds_read_b128 v[124:127], v8 offset:12288\n" \
"s_or_b32 s83, s83, s58\n" \
"v_mfma_f32_16x16x32_bf16 v[100:103], a[156:159], v[44:47], v[100:103]\n" \
"ds_read_b128 v[88:91], v2 offset:18432\n" \
"v_mfma_f32_16x16x32_bf16 v[104:107], v[156:159], v[44:47], v[104:107]\n" \
"s_waitcnt vmcnt(15) lgkmcnt(6)\n" \
"v_mfma_f32_16x16x32_bf16 v[108:111], v[92:95], v[44:47], v[108:111]\n" \
"buffer_load_dwordx4 v[44:47], v3, s[36:39], s57 offen offset:3072 sc1\n" \
"v_mfma_f32_16x16x32_bf16 v[96:99], a[32:35], v[48:51], v[96:99]\n" \
"s_lshr_b32 s59, s82, 1\n" \
"s_add_u32 s59, s59, 1\n" \
"v_mfma_f32_16x16x32_bf16 v[100:103], a[160:163], v[48:51], v[100:103]\n" \
"ds_read_b128 v[92:95], v2 offset:19456\n" \
"v_mfma_f32_16x16x32_bf16 v[104:107], v[160:163], v[48:51], v[104:107]\n" \
"s_waitcnt vmcnt(15) lgkmcnt(5)\n" \
"v_mfma_f32_16x16x32_bf16 v[108:111], v[80:83], v[48:51], v[108:111]\n" \
"s_xor_b32 s57, s56, s92\n" \
"buffer_load_dwordx4 v[48:51], v3, s[36:39], s57 offen offset:0 sc1\n" \
"v_mfma_f32_16x16x32_bf16 v[96:99], a[36:39], v[52:55], v[96:99]\n" \
"v_add_f32 v112, v112, v116\n" \
"v_mfma_f32_16x16x32_bf16 v[100:103], a[164:167], v[52:55], v[100:103]\n" \
"ds_read_b128 v[80:83], v2 offset:20480\n" \
"v_add_f32_e64 v113, v113, v117\n" \
"v_mfma_f32_16x16x32_bf16 v[104:107], v[164:167], v[52:55], v[104:107]\n" \
"s_waitcnt vmcnt(15) lgkmcnt(4)\n" \
"v_add_f32 v114, v114, v118\n" \
"v_mfma_f32_16x16x32_bf16 v[108:111], v[84:87], v[52:55], v[108:111]\n" \
"buffer_load_dwordx4 v[52:55], v3, s[36:39], s57 offen offset:1024 sc1\n" \
"v_add_f32_e64 v115, v115, v119\n" \
"v_mfma_f32_16x16x32_bf16 v[96:99], a[40:43], v[56:59], v[96:99]\n" \
"s_waitcnt lgkmcnt(3)\n" \
"v_add_f32 v120, v120, v124\n" \
"v_mfma_f32_16x16x32_bf16 v[100:103], a[168:171], v[56:59], v[100:103]\n" \
"ds_read_b128 v[84:87], v2 offset:21504\n" \
"v_add_f32_e64 v121, v121, v125\n" \
"v_mfma_f32_16x16x32_bf16 v[104:107], v[168:171], v[56:59], v[104:107]\n" \
"s_waitcnt vmcnt(15) lgkmcnt(3)\n" \
"v_add_f32 v122, v122, v126\n" \
"v_mfma_f32_16x16x32_bf16 v[108:111], v[88:91], v[56:59], v[108:111]\n" \
"buffer_load_dwordx4 v[56:59], v3, s[36:39], s57 offen offset:2048 sc1\n" \
"v_add_f32_e64 v123, v123, v127\n" \
"v_mfma_f32_16x16x32_bf16 v[96:99], a[44:47], v[60:63], v[96:99]\n" \
"v_add_f32_e64 v112, v112, v120\n" \
"v_mfma_f32_16x16x32_bf16 v[100:103], a[172:175], v[60:63], v[100:103]\n" \
"ds_read_b128 v[88:91], v2 offset:22528\n" \
"v_add_f32_e64 v113, v113, v121\n" \
"v_mfma_f32_16x16x32_bf16 v[104:107], v[172:175], v[60:63], v[104:107]\n" \
"s_waitcnt vmcnt(15) lgkmcnt(3)\n" \
"v_add_f32 v114, v114, v122\n" \
"v_mfma_f32_16x16x32_bf16 v[108:111], v[92:95], v[60:63], v[108:111]\n" \
"buffer_load_dwordx4 v[60:63], v3, s[36:39], s57 offen offset:3072 sc1\n" \
"v_add_f32_e64 v115, v115, v123\n" \
"v_mfma_f32_16x16x32_bf16 v[96:99], a[48:51], v[64:67], v[96:99]\n" \
"v_xor_b32_e64 v8, s84, v8\n" \
"v_mfma_f32_16x16x32_bf16 v[100:103], a[176:179], v[64:67], v[100:103]\n" \
"ds_read_b128 v[92:95], v2 offset:23552\n" \
"v_cvt_pk_bf16_f32 v0, v112, v113\n" \
"v_mfma_f32_16x16x32_bf16 v[104:107], v[176:179], v[64:67], v[104:107]\n" \
"s_waitcnt vmcnt(14) lgkmcnt(3)\n" \
"v_cvt_pk_bf16_f32 v1, v114, v115\n" \
"v_mfma_f32_16x16x32_bf16 v[108:111], v[80:83], v[64:67], v[108:111]\n" \
"s_xor_b32 s57, s56, s93\n" \
"buffer_load_dwordx4 v[64:67], v3, s[36:39], s57 offen offset:0 sc1\n" \
"v_mfma_f32_16x16x32_bf16 v[96:99], a[52:55], v[68:71], v[96:99]\n" \
"buffer_store_dwordx2 v[0:1], v6, s[36:39], s76 offen sc1\n" \
"v_cmp_gt_u32_e64 s[62:63], s59, v11\n" \
"v_mfma_f32_16x16x32_bf16 v[100:103], a[180:183], v[68:71], v[100:103]\n" \
"ds_read_b128 v[80:83], v2 offset:24576\n" \
"v_mfma_f32_16x16x32_bf16 v[104:107], v[180:183], v[68:71], v[104:107]\n" \
"s_waitcnt vmcnt(15) lgkmcnt(3)\n" \
"v_mfma_f32_16x16x32_bf16 v[108:111], v[84:87], v[68:71], v[108:111]\n" \
"buffer_load_dwordx4 v[68:71], v3, s[36:39], s57 offen offset:1024 sc1\n" \
"v_mfma_f32_16x16x32_bf16 v[96:99], a[56:59], v[72:75], v[96:99]\n" \
"s_add_u32 s54, s55, 1\n" \
"s_cmp_lt_u32 s54, s79\n" \
"v_mfma_f32_16x16x32_bf16 v[100:103], a[184:187], v[72:75], v[100:103]\n" \
"ds_read_b128 v[84:87], v2 offset:25600\n" \
"s_cselect_b64 s[62:63], s[62:63], -1\n" \
"v_mfma_f32_16x16x32_bf16 v[104:107], v[184:187], v[72:75], v[104:107]\n" \
"s_waitcnt vmcnt(15) lgkmcnt(3)\n" \
"v_mfma_f32_16x16x32_bf16 v[108:111], v[88:91], v[72:75], v[108:111]\n" \
"buffer_load_dwordx4 v[72:75], v3, s[36:39], s57 offen offset:2048 sc1\n" \
"v_mfma_f32_16x16x32_bf16 v[96:99], a[60:63], v[76:79], v[96:99]\n" \
"s_cmp_lg_u64 s[62:63], 0\n" \
"s_cselect_b32 s81, 1, 0\n" \
"v_mfma_f32_16x16x32_bf16 v[100:103], a[188:191], v[76:79], v[100:103]\n" \
"ds_read_b128 v[88:91], v2 offset:26624\n" \
"s_cselect_b32 s83, 0x70000000, s83\n" \
"v_mfma_f32_16x16x32_bf16 v[104:107], v[188:191], v[76:79], v[104:107]\n" \
"s_waitcnt vmcnt(15) lgkmcnt(3)\n" \
"v_mfma_f32_16x16x32_bf16 v[108:111], v[92:95], v[76:79], v[108:111]\n" \
"buffer_load_dwordx4 v[76:79], v3, s[36:39], s57 offen offset:3072 sc1\n" \
"v_mfma_f32_16x16x32_bf16 v[96:99], a[64:67], v[16:19], v[96:99]\n" \
"v_mfma_f32_16x16x32_bf16 v[100:103], a[192:195], v[16:19], v[100:103]\n" \
"ds_read_b128 v[92:95], v2 offset:27648\n" \
"v_mfma_f32_16x16x32_bf16 v[104:107], v[192:195], v[16:19], v[104:107]\n" \
"s_waitcnt vmcnt(15) lgkmcnt(3)\n" \
"s_mov_b32 s56, s83\n" \
"v_mfma_f32_16x16x32_bf16 v[108:111], v[80:83], v[16:19], v[108:111]\n" \
"s_xor_b32 s57, s56, s86\n" \
"buffer_load_dwordx4 v[16:19], v3, s[36:39], s57 offen offset:0 sc1\n" \
"v_mfma_f32_16x16x32_bf16 v[96:99], a[68:71], v[20:23], v[96:99]\n" \
"v_mfma_f32_16x16x32_bf16 v[100:103], a[196:199], v[20:23], v[100:103]\n" \
"ds_read_b128 v[80:83], v2 offset:28672\n" \
"v_mfma_f32_16x16x32_bf16 v[104:107], v[196:199], v[20:23], v[104:107]\n" \
"s_waitcnt vmcnt(15) lgkmcnt(3)\n" \
"v_mfma_f32_16x16x32_bf16 v[108:111], v[84:87], v[20:23], v[108:111]\n" \
"buffer_load_dwordx4 v[20:23], v3, s[36:39], s57 offen offset:1024 sc1\n" \
"v_mfma_f32_16x16x32_bf16 v[96:99], a[72:75], v[24:27], v[96:99]\n" \
"v_mfma_f32_16x16x32_bf16 v[100:103], a[200:203], v[24:27], v[100:103]\n" \
"ds_read_b128 v[84:87], v2 offset:29696\n" \
"v_mfma_f32_16x16x32_bf16 v[104:107], v[200:203], v[24:27], v[104:107]\n" \
"s_waitcnt vmcnt(15) lgkmcnt(3)\n" \
"v_mfma_f32_16x16x32_bf16 v[108:111], v[88:91], v[24:27], v[108:111]\n" \
"buffer_load_dwordx4 v[24:27], v3, s[36:39], s57 offen offset:2048 sc1\n" \
"v_mfma_f32_16x16x32_bf16 v[96:99], a[76:79], v[28:31], v[96:99]\n" \
"v_mfma_f32_16x16x32_bf16 v[100:103], a[204:207], v[28:31], v[100:103]\n" \
"ds_read_b128 v[88:91], v2 offset:30720\n" \
"v_mfma_f32_16x16x32_bf16 v[104:107], v[204:207], v[28:31], v[104:107]\n" \
"s_waitcnt vmcnt(15) lgkmcnt(3)\n" \
"v_mfma_f32_16x16x32_bf16 v[108:111], v[92:95], v[28:31], v[108:111]\n" \
"buffer_load_dwordx4 v[28:31], v3, s[36:39], s57 offen offset:3072 sc1\n" \
"v_mfma_f32_16x16x32_bf16 v[96:99], a[80:83], v[32:35], v[96:99]\n" \
"v_mfma_f32_16x16x32_bf16 v[100:103], a[208:211], v[32:35], v[100:103]\n" \
"ds_read_b128 v[92:95], v2 offset:31744\n" \
"v_mfma_f32_16x16x32_bf16 v[104:107], v[208:211], v[32:35], v[104:107]\n" \
"s_waitcnt vmcnt(15) lgkmcnt(3)\n" \
"v_mfma_f32_16x16x32_bf16 v[108:111], v[80:83], v[32:35], v[108:111]\n" \
"s_xor_b32 s57, s56, s87\n" \
"buffer_load_dwordx4 v[32:35], v3, s[36:39], s57 offen offset:0 sc1\n" \
"v_mfma_f32_16x16x32_bf16 v[96:99], a[84:87], v[36:39], v[96:99]\n" \
"v_mfma_f32_16x16x32_bf16 v[100:103], a[212:215], v[36:39], v[100:103]\n" \
"ds_read_b128 v[80:83], v2 offset:0\n" \
"v_mfma_f32_16x16x32_bf16 v[104:107], v[212:215], v[36:39], v[104:107]\n" \
"s_waitcnt vmcnt(15) lgkmcnt(3)\n" \
"v_mfma_f32_16x16x32_bf16 v[108:111], v[84:87], v[36:39], v[108:111]\n" \
"buffer_load_dwordx4 v[36:39], v3, s[36:39], s57 offen offset:1024 sc1\n" \
"v_mfma_f32_16x16x32_bf16 v[96:99], a[88:91], v[40:43], v[96:99]\n" \
"v_mfma_f32_16x16x32_bf16 v[100:103], a[216:219], v[40:43], v[100:103]\n" \
"ds_read_b128 v[84:87], v2 offset:1024\n" \
"v_mfma_f32_16x16x32_bf16 v[104:107], v[216:219], v[40:43], v[104:107]\n" \
"s_waitcnt vmcnt(15) lgkmcnt(3)\n" \
"v_mfma_f32_16x16x32_bf16 v[108:111], v[88:91], v[40:43], v[108:111]\n" \
"buffer_load_dwordx4 v[40:43], v3, s[36:39], s57 offen offset:2048 sc1\n" \
"v_mfma_f32_16x16x32_bf16 v[96:99], a[92:95], v[44:47], v[96:99]\n" \
"v_mfma_f32_16x16x32_bf16 v[100:103], a[220:223], v[44:47], v[100:103]\n" \
"ds_read_b128 v[88:91], v2 offset:2048\n" \
"v_mfma_f32_16x16x32_bf16 v[104:107], v[220:223], v[44:47], v[104:107]\n" \
"s_waitcnt vmcnt(15) lgkmcnt(3)\n" \
"v_mfma_f32_16x16x32_bf16 v[108:111], v[92:95], v[44:47], v[108:111]\n" \
"buffer_load_dwordx4 v[44:47], v3, s[36:39], s57 offen offset:3072 sc1\n" \
"v_mfma_f32_16x16x32_bf16 v[96:99], a[96:99], v[48:51], v[96:99]\n" \
"v_mfma_f32_16x16x32_bf16 v[100:103], a[224:227], v[48:51], v[100:103]\n" \
"ds_read_b128 v[92:95], v2 offset:3072\n" \
"v_mfma_f32_16x16x32_bf16 v[104:107], v[224:227], v[48:51], v[104:107]\n" \
"s_waitcnt vmcnt(15) lgkmcnt(3)\n" \
"v_mfma_f32_16x16x32_bf16 v[108:111], v[80:83], v[48:51], v[108:111]\n" \
"s_xor_b32 s57, s56, s88\n" \
"buffer_load_dwordx4 v[48:51], v3, s[36:39], s57 offen offset:0 sc1\n" \
"v_mfma_f32_16x16x32_bf16 v[96:99], a[100:103], v[52:55], v[96:99]\n" \
"s_lshl_b32 s80, s44, 16\n" \
"v_mfma_f32_16x16x32_bf16 v[100:103], a[228:231], v[52:55], v[100:103]\n" \
"ds_read_b128 v[80:83], v2 offset:4096\n" \
"v_mfma_f32_16x16x32_bf16 v[104:107], v[228:231], v[52:55], v[104:107]\n" \
"s_waitcnt vmcnt(15) lgkmcnt(3)\n" \
"s_add_u32 s80, s80, s78\n" \
"v_mfma_f32_16x16x32_bf16 v[108:111], v[84:87], v[52:55], v[108:111]\n" \
"buffer_load_dwordx4 v[52:55], v3, s[36:39], s57 offen offset:1024 sc1\n" \
"v_mfma_f32_16x16x32_bf16 v[96:99], a[104:107], v[56:59], v[96:99]\n" \
"s_lshr_b32 s58, s80, 8\n" \
"v_mfma_f32_16x16x32_bf16 v[100:103], a[232:235], v[56:59], v[100:103]\n" \
"ds_read_b128 v[84:87], v2 offset:5120\n" \
"v_mfma_f32_16x16x32_bf16 v[104:107], v[232:235], v[56:59], v[104:107]\n" \
"s_waitcnt vmcnt(15) lgkmcnt(3)\n" \
"s_and_b32 s58, s58, 0x2000\n" \
"v_mfma_f32_16x16x32_bf16 v[108:111], v[88:91], v[56:59], v[108:111]\n" \
"buffer_load_dwordx4 v[56:59], v3, s[36:39], s57 offen offset:2048 sc1\n" \
"v_mfma_f32_16x16x32_bf16 v[96:99], a[108:111], v[60:63], v[96:99]\n" \
"s_xor_b32 s80, s80, s58\n" \
"v_mfma_f32_16x16x32_bf16 v[100:103], a[236:239], v[60:63], v[100:103]\n" \
"ds_read_b128 v[88:91], v2 offset:6144\n" \
"v_mfma_f32_16x16x32_bf16 v[104:107], v[236:239], v[60:63], v[104:107]\n" \
"s_waitcnt vmcnt(15) lgkmcnt(3)\n" \
"v_mfma_f32_16x16x32_bf16 v[108:111], v[92:95], v[60:63], v[108:111]\n" \
"buffer_load_dwordx4 v[60:63], v3, s[36:39], s57 offen offset:3072 sc1\n" \
"v_mfma_f32_16x16x32_bf16 v[96:99], a[112:115], v[64:67], v[96:99]\n" \
"v_mfma_f32_16x16x32_bf16 v[100:103], a[240:243], v[64:67], v[100:103]\n" \
"ds_read_b128 v[92:95], v2 offset:7168\n" \
"v_mfma_f32_16x16x32_bf16 v[104:107], v[240:243], v[64:67], v[104:107]\n" \
"s_waitcnt vmcnt(14) lgkmcnt(3)\n" \
"v_mfma_f32_16x16x32_bf16 v[108:111], v[80:83], v[64:67], v[108:111]\n" \
"s_xor_b32 s57, s56, s89\n" \
"buffer_load_dwordx4 v[64:67], v3, s[36:39], s57 offen offset:0 sc1\n" \
"v_mfma_f32_16x16x32_bf16 v[96:99], a[116:119], v[68:71], v[96:99]\n" \
"buffer_load_dword v11, v4, s[36:39], 0 offen sc1\n" \
"v_mfma_f32_16x16x32_bf16 v[100:103], a[244:247], v[68:71], v[100:103]\n" \
"ds_read_b128 v[80:83], v2 offset:8192\n" \
"v_mfma_f32_16x16x32_bf16 v[104:107], v[244:247], v[68:71], v[104:107]\n" \
"s_waitcnt vmcnt(15) lgkmcnt(3)\n" \
"v_mfma_f32_16x16x32_bf16 v[108:111], v[84:87], v[68:71], v[108:111]\n" \
"buffer_load_dwordx4 v[68:71], v3, s[36:39], s57 offen offset:1024 sc1\n" \
"v_mfma_f32_16x16x32_bf16 v[96:99], a[120:123], v[72:75], v[96:99]\n" \
"s_add_u32 s55, s55, 1\n" \
"s_mov_b32 s84, 0x4000\n" \
"v_mfma_f32_16x16x32_bf16 v[100:103], a[248:251], v[72:75], v[100:103]\n" \
"ds_read_b128 v[84:87], v2 offset:9216\n" \
"v_mfma_f32_16x16x32_bf16 v[104:107], v[248:251], v[72:75], v[104:107]\n" \
"s_waitcnt vmcnt(15) lgkmcnt(3)\n" \
"v_mfma_f32_16x16x32_bf16 v[108:111], v[88:91], v[72:75], v[108:111]\n" \
"buffer_load_dwordx4 v[72:75], v3, s[36:39], s57 offen offset:2048 sc1\n" \
"v_mfma_f32_16x16x32_bf16 v[96:99], a[124:127], v[76:79], v[96:99]\n" \
"s_mov_b32 s76, s80\n" \
"v_mfma_f32_16x16x32_bf16 v[100:103], a[252:255], v[76:79], v[100:103]\n" \
"ds_read_b128 v[88:91], v2 offset:10240\n" \
"s_mov_b32 s44, s82\n" \
"v_mfma_f32_16x16x32_bf16 v[104:107], v[252:255], v[76:79], v[104:107]\n" \
"s_waitcnt vmcnt(15) lgkmcnt(3)\n" \
"ds_write_b128 v7, v[96:99] offset:0\n" \
"v_xor_b32_e64 v7, 64, v7\n" \
"v_mfma_f32_16x16x32_bf16 v[108:111], v[92:95], v[76:79], v[108:111]\n" \
"buffer_load_dwordx4 v[76:79], v3, s[36:39], s57 offen offset:3072 sc1\n" \
"ds_write_b128 v7, v[100:103] offset:1024\n" \
"v_xor_b32 v7, 192, v7\n" \
"s_cmp_eq_u32 s81, 0\n" \
"s_cbranch_scc1 .Lswo_loop_%=\n" \
"s_nop 7\n" \
"s_nop 7\n" \
"s_waitcnt vmcnt(0) lgkmcnt(0)\n" \
"ds_write_b128 v7, v[104:107] offset:2048\n" \
"v_xor_b32 v7, 64, v7\n" \
"ds_write_b128 v7, v[108:111] offset:3072\n" \
"v_xor_b32 v7, 64, v7\n" \
"s_waitcnt lgkmcnt(0)\n" \
"s_barrier\n" \
"ds_read_b128 v[112:115], v8 offset:0\n" \
"ds_read_b128 v[116:119], v8 offset:4096\n" \
"ds_read_b128 v[120:123], v8 offset:8192\n" \
"ds_read_b128 v[124:127], v8 offset:12288\n" \
"s_waitcnt lgkmcnt(2)\n" \
"v_add_f32 v112, v112, v116\n" \
"v_add_f32 v113, v113, v117\n" \
"v_add_f32 v114, v114, v118\n" \
"v_add_f32 v115, v115, v119\n" \
"s_waitcnt lgkmcnt(0)\n" \
"v_add_f32 v120, v120, v124\n" \
"v_add_f32 v121, v121, v125\n" \
"v_add_f32 v122, v122, v126\n" \
"v_add_f32 v123, v123, v127\n" \
"v_add_f32 v112, v112, v120\n" \
"v_add_f32 v113, v113, v121\n" \
"v_add_f32 v114, v114, v122\n" \
"v_add_f32 v115, v115, v123\n" \
"v_xor_b32 v8, s84, v8\n" \
"v_cvt_pk_bf16_f32 v0, v112, v113\n" \
"v_cvt_pk_bf16_f32 v1, v114, v115\n" \
"v_xor_b32 v7, 0x4000, v7\n" \
"s_cmp_eq_u32 s98, 0\n" \
"s_cbranch_scc0 .Lswo_f_%=\n" \
"v_lshlrev_b32 v112, 16, v0\n" \
"v_and_b32 v113, 0xffff0000, v0\n" \
"v_lshlrev_b32 v114, 16, v1\n" \
"v_and_b32 v115, 0xffff0000, v1\n" \
"v_lshlrev_b32 v116, 16, v14\n" \
"v_and_b32 v117, 0xffff0000, v14\n" \
"v_lshlrev_b32 v118, 16, v15\n" \
"v_and_b32 v119, 0xffff0000, v15\n" \
"v_lshlrev_b32 v120, 16, v12\n" \
"v_and_b32 v121, 0xffff0000, v12\n" \
"v_lshlrev_b32 v122, 16, v13\n" \
"v_and_b32 v123, 0xffff0000, v13\n" \
"v_fmac_f32 v120, v112, v116\n" \
"v_fmac_f32 v121, v113, v117\n" \
"v_fmac_f32 v122, v114, v118\n" \
"v_fmac_f32 v123, v115, v119\n" \
"v_cvt_pk_bf16_f32 v12, v120, v121\n" \
"v_cvt_pk_bf16_f32 v13, v122, v123\n" \
"buffer_store_dwordx2 v[12:13], v5, s[68:71], s77 offen\n" \
"s_branch .Lswo_g_%=\n" \
".Lswo_f_%=:\n" \
"buffer_store_dwordx2 v[0:1], v6, s[36:39], s76 offen sc1\n" \
"s_cmp_lg_u32 s55, s95\n" \
"s_cbranch_scc1 .Lswo_g_%=\n" \
"s_waitcnt vmcnt(0) lgkmcnt(0)\n" \
"v_mov_b32 v9, 1\n" \
"s_mov_b64 exec, 1\n" \
"buffer_atomic_add v9, off, s[36:39], s49 offset:0\n" \
"s_mov_b64 exec, -1\n" \
"s_mov_b32 s79, 0\n" \
"s_mov_b32 s98, 0\n" \
"s_branch .Lswo_o_%=\n" \
".Lswo_ok_%=:\n" \
".Lswo_g_%=:\n" \
"s_cmp_lt_u32 s55, s46\n" \
"s_cbranch_scc1 .Lswo_prime_%=\n" \
"s_branch .Lswo_end_%=\n" \
".Lswo_w_%=:\n" \
"s_nop 7\n" \
"s_nop 7\n" \
"s_waitcnt vmcnt(0) lgkmcnt(0)\n" \
"s_mov_b32 s61, 0\n" \
".Lswo_wa_%=:\n" \
"s_cmp_lg_u32 s60, 0\n" \
"s_cbranch_scc1 .Lswo_wk_%=\n" \
"buffer_load_dword v11, v4, s[36:39], 0 offen sc1\n" \
"s_waitcnt vmcnt(0)\n" \
"v_cmp_gt_u32_e64 s[62:63], s59, v11\n" \
"s_nop 4\n" \
"s_cmp_eq_u64 s[62:63], 0\n" \
"s_cbranch_scc1 .Lswo_wk_%=\n" \
"s_sleep 1\n" \
"s_add_u32 s61, s61, 1\n" \
"s_cmp_lt_u32 s61, 65536\n" \
"s_cbranch_scc1 .Lswo_wa_%=\n" \
"s_mov_b32 s60, 1\n" \
"v_mov_b32 v11, 256\n" \
"v_mov_b32 v9, 1\n" \
"buffer_store_dword v9, v11, s[36:39], 0 offen sc1\n" \
"s_waitcnt vmcnt(0) lgkmcnt(0)\n" \
"s_branch .Lswo_wk_%=\n" \
".Lswo_o_%=:\n" \
"s_nop 7\n" \
"s_nop 7\n" \
"s_waitcnt vmcnt(0) lgkmcnt(0)\n" \
"s_mov_b32 s61, 0\n" \
".Lswo_oa_%=:\n" \
"s_cmp_lg_u32 s60, 0\n" \
"s_cbranch_scc1 .Lswo_ok_%=\n" \
"buffer_load_dword v9, off, s[36:39], s49 offset:64 sc1\n" \
"s_waitcnt vmcnt(0)\n" \
"v_cmp_gt_u32 vcc, 6, v9\n" \
"s_nop 4\n" \
"s_cbranch_vccz .Lswo_ok_%=\n" \
"s_sleep 1\n" \
"s_add_u32 s61, s61, 1\n" \
"s_cmp_lt_u32 s61, 65536\n" \
"s_cbranch_scc1 .Lswo_oa_%=\n" \
"s_mov_b32 s60, 1\n" \
"v_mov_b32 v11, 256\n" \
"v_mov_b32 v9, 1\n" \
"buffer_store_dword v9, v11, s[36:39], 0 offen sc1\n" \
"s_waitcnt vmcnt(0) lgkmcnt(0)\n" \
"s_branch .Lswo_ok_%=\n" \
".Lswo_end_%=:\n" \
"s_endpgm\n"
#define ASM_S64_G2 \
"s_nop 7\n" \
"s_nop 7\n" \
"s_lshl_b32 s52, s48, 15\n" \
"s_lshl_b32 s53, s48, 13\n" \
"s_add_u32 s53, s53, 98304\n" \
"s_sub_u32 s54, s53, s52\n" \
"v_add_u32 v9, s54, v1\n" \
"s_add_u32 m0, s52, 0\n" \
"s_add_u32 s54, s97, 0\n" \
"s_lshr_b32 s55, s54, 8\n" \
"s_and_b32 s55, s55, 0x2000\n" \
"s_xor_b32 s54, s54, s55\n" \
"buffer_load_dwordx4 v0, s[40:43], s54 offen sc1 lds\n" \
"s_add_u32 m0, s52, 1024\n" \
"s_add_u32 s54, s97, 32768\n" \
"s_lshr_b32 s55, s54, 8\n" \
"s_and_b32 s55, s55, 0x2000\n" \
"s_xor_b32 s54, s54, s55\n" \
"buffer_load_dwordx4 v0, s[40:43], s54 offen sc1 lds\n" \
"s_add_u32 m0, s52, 2048\n" \
"s_add_u32 s54, s97, 65536\n" \
"s_lshr_b32 s55, s54, 8\n" \
"s_and_b32 s55, s55, 0x2000\n" \
"s_xor_b32 s54, s54, s55\n" \
"buffer_load_dwordx4 v0, s[40:43], s54 offen sc1 lds\n" \
"s_add_u32 m0, s52, 3072\n" \
"s_add_u32 s54, s97, 98304\n" \
"s_lshr_b32 s55, s54, 8\n" \
"s_and_b32 s55, s55, 0x2000\n" \
"s_xor_b32 s54, s54, s55\n" \
"buffer_load_dwordx4 v0, s[40:43], s54 offen sc1 lds\n" \
"s_mov_b32 s54, s99\n" \
".Lsg2_z0_%=:\n" \
"s_sleep 1\n" \
"s_sub_u32 s54, s54, 1\n" \
"s_cbranch_scc0 .Lsg2_z0_%=\n" \
"s_add_u32 m0, s52, 4096\n" \
"s_add_u32 s54, s97, 131072\n" \
"s_lshr_b32 s55, s54, 8\n" \
"s_and_b32 s55, s55, 0x2000\n" \
"s_xor_b32 s54, s54, s55\n" \
"buffer_load_dwordx4 v0, s[40:43], s54 offen sc1 lds\n" \
"s_add_u32 m0, s52, 5120\n" \
"s_add_u32 s54, s97, 163840\n" \
"s_lshr_b32 s55, s54, 8\n" \
"s_and_b32 s55, s55, 0x2000\n" \
"s_xor_b32 s54, s54, s55\n" \
"buffer_load_dwordx4 v0, s[40:43], s54 offen sc1 lds\n" \
"s_add_u32 m0, s52, 6144\n" \
"s_add_u32 s54, s97, 196608\n" \
"s_lshr_b32 s55, s54, 8\n" \
"s_and_b32 s55, s55, 0x2000\n" \
"s_xor_b32 s54, s54, s55\n" \
"buffer_load_dwordx4 v0, s[40:43], s54 offen sc1 lds\n" \
"s_add_u32 m0, s52, 7168\n" \
"s_add_u32 s54, s97, 229376\n" \
"s_lshr_b32 s55, s54, 8\n" \
"s_and_b32 s55, s55, 0x2000\n" \
"s_xor_b32 s54, s54, s55\n" \
"buffer_load_dwordx4 v0, s[40:43], s54 offen sc1 lds\n" \
"s_mov_b32 s54, s99\n" \
".Lsg2_z1_%=:\n" \
"s_sleep 1\n" \
"s_sub_u32 s54, s54, 1\n" \
"s_cbranch_scc0 .Lsg2_z1_%=\n" \
"s_add_u32 m0, s52, 8192\n" \
"s_add_u32 s54, s97, 262144\n" \
"s_lshr_b32 s55, s54, 8\n" \
"s_and_b32 s55, s55, 0x2000\n" \
"s_xor_b32 s54, s54, s55\n" \
"buffer_load_dwordx4 v0, s[40:43], s54 offen sc1 lds\n" \
"s_add_u32 m0, s52, 9216\n" \
"s_add_u32 s54, s97, 294912\n" \
"s_lshr_b32 s55, s54, 8\n" \
"s_and_b32 s55, s55, 0x2000\n" \
"s_xor_b32 s54, s54, s55\n" \
"buffer_load_dwordx4 v0, s[40:43], s54 offen sc1 lds\n" \
"s_add_u32 m0, s52, 10240\n" \
"s_add_u32 s54, s97, 327680\n" \
"s_lshr_b32 s55, s54, 8\n" \
"s_and_b32 s55, s55, 0x2000\n" \
"s_xor_b32 s54, s54, s55\n" \
"buffer_load_dwordx4 v0, s[40:43], s54 offen sc1 lds\n" \
"s_add_u32 m0, s52, 11264\n" \
"s_add_u32 s54, s97, 360448\n" \
"s_lshr_b32 s55, s54, 8\n" \
"s_and_b32 s55, s55, 0x2000\n" \
"s_xor_b32 s54, s54, s55\n" \
"buffer_load_dwordx4 v0, s[40:43], s54 offen sc1 lds\n" \
"s_mov_b32 s54, s99\n" \
".Lsg2_z2_%=:\n" \
"s_sleep 1\n" \
"s_sub_u32 s54, s54, 1\n" \
"s_cbranch_scc0 .Lsg2_z2_%=\n" \
"s_add_u32 m0, s52, 12288\n" \
"s_add_u32 s54, s97, 393216\n" \
"s_lshr_b32 s55, s54, 8\n" \
"s_and_b32 s55, s55, 0x2000\n" \
"s_xor_b32 s54, s54, s55\n" \
"buffer_load_dwordx4 v0, s[40:43], s54 offen sc1 lds\n" \
"s_add_u32 m0, s52, 13312\n" \
"s_add_u32 s54, s97, 425984\n" \
"s_lshr_b32 s55, s54, 8\n" \
"s_and_b32 s55, s55, 0x2000\n" \
"s_xor_b32 s54, s54, s55\n" \
"buffer_load_dwordx4 v0, s[40:43], s54 offen sc1 lds\n" \
"s_add_u32 m0, s52, 14336\n" \
"s_add_u32 s54, s97, 458752\n" \
"s_lshr_b32 s55, s54, 8\n" \
"s_and_b32 s55, s55, 0x2000\n" \
"s_xor_b32 s54, s54, s55\n" \
"buffer_load_dwordx4 v0, s[40:43], s54 offen sc1 lds\n" \
"s_add_u32 m0, s52, 15360\n" \
"s_add_u32 s54, s97, 491520\n" \
"s_lshr_b32 s55, s54, 8\n" \
"s_and_b32 s55, s55, 0x2000\n" \
"s_xor_b32 s54, s54, s55\n" \
"buffer_load_dwordx4 v0, s[40:43], s54 offen sc1 lds\n" \
"s_mov_b32 s54, s99\n" \
".Lsg2_z3_%=:\n" \
"s_sleep 1\n" \
"s_sub_u32 s54, s54, 1\n" \
"s_cbranch_scc0 .Lsg2_z3_%=\n" \
"s_add_u32 m0, s52, 16384\n" \
"s_add_u32 s54, s97, 524288\n" \
"s_lshr_b32 s55, s54, 8\n" \
"s_and_b32 s55, s55, 0x2000\n" \
"s_xor_b32 s54, s54, s55\n" \
"buffer_load_dwordx4 v0, s[40:43], s54 offen sc1 lds\n" \
"s_add_u32 m0, s52, 17408\n" \
"s_add_u32 s54, s97, 557056\n" \
"s_lshr_b32 s55, s54, 8\n" \
"s_and_b32 s55, s55, 0x2000\n" \
"s_xor_b32 s54, s54, s55\n" \
"buffer_load_dwordx4 v0, s[40:43], s54 offen sc1 lds\n" \
"s_add_u32 m0, s52, 18432\n" \
"s_add_u32 s54, s97, 589824\n" \
"s_lshr_b32 s55, s54, 8\n" \
"s_and_b32 s55, s55, 0x2000\n" \
"s_xor_b32 s54, s54, s55\n" \
"buffer_load_dwordx4 v0, s[40:43], s54 offen sc1 lds\n" \
"s_add_u32 m0, s52, 19456\n" \
"s_add_u32 s54, s97, 622592\n" \
"s_lshr_b32 s55, s54, 8\n" \
"s_and_b32 s55, s55, 0x2000\n" \
"s_xor_b32 s54, s54, s55\n" \
"buffer_load_dwordx4 v0, s[40:43], s54 offen sc1 lds\n" \
"s_mov_b32 s54, s99\n" \
".Lsg2_z4_%=:\n" \
"s_sleep 1\n" \
"s_sub_u32 s54, s54, 1\n" \
"s_cbranch_scc0 .Lsg2_z4_%=\n" \
"s_add_u32 m0, s52, 20480\n" \
"s_add_u32 s54, s97, 655360\n" \
"s_lshr_b32 s55, s54, 8\n" \
"s_and_b32 s55, s55, 0x2000\n" \
"s_xor_b32 s54, s54, s55\n" \
"buffer_load_dwordx4 v0, s[40:43], s54 offen sc1 lds\n" \
"s_add_u32 m0, s52, 21504\n" \
"s_add_u32 s54, s97, 688128\n" \
"s_lshr_b32 s55, s54, 8\n" \
"s_and_b32 s55, s55, 0x2000\n" \
"s_xor_b32 s54, s54, s55\n" \
"buffer_load_dwordx4 v0, s[40:43], s54 offen sc1 lds\n" \
"s_add_u32 m0, s52, 22528\n" \
"s_add_u32 s54, s97, 720896\n" \
"s_lshr_b32 s55, s54, 8\n" \
"s_and_b32 s55, s55, 0x2000\n" \
"s_xor_b32 s54, s54, s55\n" \
"buffer_load_dwordx4 v0, s[40:43], s54 offen sc1 lds\n" \
"s_add_u32 m0, s52, 23552\n" \
"s_add_u32 s54, s97, 753664\n" \
"s_lshr_b32 s55, s54, 8\n" \
"s_and_b32 s55, s55, 0x2000\n" \
"s_xor_b32 s54, s54, s55\n" \
"buffer_load_dwordx4 v0, s[40:43], s54 offen sc1 lds\n" \
"s_mov_b32 s54, s99\n" \
".Lsg2_z5_%=:\n" \
"s_sleep 1\n" \
"s_sub_u32 s54, s54, 1\n" \
"s_cbranch_scc0 .Lsg2_z5_%=\n" \
"s_add_u32 m0, s52, 24576\n" \
"s_add_u32 s54, s97, 786432\n" \
"s_lshr_b32 s55, s54, 8\n" \
"s_and_b32 s55, s55, 0x2000\n" \
"s_xor_b32 s54, s54, s55\n" \
"buffer_load_dwordx4 v0, s[40:43], s54 offen sc1 lds\n" \
"s_add_u32 m0, s52, 25600\n" \
"s_add_u32 s54, s97, 819200\n" \
"s_lshr_b32 s55, s54, 8\n" \
"s_and_b32 s55, s55, 0x2000\n" \
"s_xor_b32 s54, s54, s55\n" \
"buffer_load_dwordx4 v0, s[40:43], s54 offen sc1 lds\n" \
"s_add_u32 m0, s52, 26624\n" \
"s_add_u32 s54, s97, 851968\n" \
"s_lshr_b32 s55, s54, 8\n" \
"s_and_b32 s55, s55, 0x2000\n" \
"s_xor_b32 s54, s54, s55\n" \
"buffer_load_dwordx4 v0, s[40:43], s54 offen sc1 lds\n" \
"s_add_u32 m0, s52, 27648\n" \
"s_add_u32 s54, s97, 884736\n" \
"s_lshr_b32 s55, s54, 8\n" \
"s_and_b32 s55, s55, 0x2000\n" \
"s_xor_b32 s54, s54, s55\n" \
"buffer_load_dwordx4 v0, s[40:43], s54 offen sc1 lds\n" \
"s_mov_b32 s54, s99\n" \
".Lsg2_z6_%=:\n" \
"s_sleep 1\n" \
"s_sub_u32 s54, s54, 1\n" \
"s_cbranch_scc0 .Lsg2_z6_%=\n" \
"s_add_u32 m0, s52, 28672\n" \
"s_add_u32 s54, s97, 917504\n" \
"s_lshr_b32 s55, s54, 8\n" \
"s_and_b32 s55, s55, 0x2000\n" \
"s_xor_b32 s54, s54, s55\n" \
"buffer_load_dwordx4 v0, s[40:43], s54 offen sc1 lds\n" \
"s_add_u32 m0, s52, 29696\n" \
"s_add_u32 s54, s97, 950272\n" \
"s_lshr_b32 s55, s54, 8\n" \
"s_and_b32 s55, s55, 0x2000\n" \
"s_xor_b32 s54, s54, s55\n" \
"buffer_load_dwordx4 v0, s[40:43], s54 offen sc1 lds\n" \
"s_add_u32 m0, s52, 30720\n" \
"s_add_u32 s54, s97, 983040\n" \
"s_lshr_b32 s55, s54, 8\n" \
"s_and_b32 s55, s55, 0x2000\n" \
"s_xor_b32 s54, s54, s55\n" \
"buffer_load_dwordx4 v0, s[40:43], s54 offen sc1 lds\n" \
"s_add_u32 m0, s52, 31744\n" \
"s_add_u32 s54, s97, 1015808\n" \
"s_lshr_b32 s55, s54, 8\n" \
"s_and_b32 s55, s55, 0x2000\n" \
"s_xor_b32 s54, s54, s55\n" \
"buffer_load_dwordx4 v0, s[40:43], s54 offen sc1 lds\n" \
"s_mov_b32 s54, s99\n" \
".Lsg2_z7_%=:\n" \
"s_sleep 1\n" \
"s_sub_u32 s54, s54, 1\n" \
"s_cbranch_scc0 .Lsg2_z7_%=\n" \
"s_add_u32 m0, s53, 32768\n" \
"s_add_u32 s54, s97, 1048576\n" \
"s_lshr_b32 s55, s54, 8\n" \
"s_and_b32 s55, s55, 0x2000\n" \
"s_xor_b32 s54, s54, s55\n" \
"buffer_load_dwordx4 v0, s[40:43], s54 offen sc1 lds\n" \
"s_add_u32 m0, s53, 33792\n" \
"s_add_u32 s54, s97, 1081344\n" \
"s_lshr_b32 s55, s54, 8\n" \
"s_and_b32 s55, s55, 0x2000\n" \
"s_xor_b32 s54, s54, s55\n" \
"buffer_load_dwordx4 v0, s[40:43], s54 offen sc1 lds\n" \
"s_add_u32 m0, s53, 34816\n" \
"s_add_u32 s54, s97, 1114112\n" \
"s_lshr_b32 s55, s54, 8\n" \
"s_and_b32 s55, s55, 0x2000\n" \
"s_xor_b32 s54, s54, s55\n" \
"buffer_load_dwordx4 v0, s[40:43], s54 offen sc1 lds\n" \
"s_add_u32 m0, s53, 35840\n" \
"s_add_u32 s54, s97, 1146880\n" \
"s_lshr_b32 s55, s54, 8\n" \
"s_and_b32 s55, s55, 0x2000\n" \
"s_xor_b32 s54, s54, s55\n" \
"buffer_load_dwordx4 v0, s[40:43], s54 offen sc1 lds\n" \
"s_mov_b32 s54, s99\n" \
".Lsg2_z8_%=:\n" \
"s_sleep 1\n" \
"s_sub_u32 s54, s54, 1\n" \
"s_cbranch_scc0 .Lsg2_z8_%=\n" \
"s_add_u32 m0, s53, 36864\n" \
"s_add_u32 s54, s97, 1179648\n" \
"s_lshr_b32 s55, s54, 8\n" \
"s_and_b32 s55, s55, 0x2000\n" \
"s_xor_b32 s54, s54, s55\n" \
"buffer_load_dwordx4 v0, s[40:43], s54 offen sc1 lds\n" \
"s_add_u32 m0, s53, 37888\n" \
"s_add_u32 s54, s97, 1212416\n" \
"s_lshr_b32 s55, s54, 8\n" \
"s_and_b32 s55, s55, 0x2000\n" \
"s_xor_b32 s54, s54, s55\n" \
"buffer_load_dwordx4 v0, s[40:43], s54 offen sc1 lds\n" \
"s_add_u32 m0, s53, 38912\n" \
"s_add_u32 s54, s97, 1245184\n" \
"s_lshr_b32 s55, s54, 8\n" \
"s_and_b32 s55, s55, 0x2000\n" \
"s_xor_b32 s54, s54, s55\n" \
"buffer_load_dwordx4 v0, s[40:43], s54 offen sc1 lds\n" \
"s_add_u32 m0, s53, 39936\n" \
"s_add_u32 s54, s97, 1277952\n" \
"s_lshr_b32 s55, s54, 8\n" \
"s_and_b32 s55, s55, 0x2000\n" \
"s_xor_b32 s54, s54, s55\n" \
"buffer_load_dwordx4 v0, s[40:43], s54 offen sc1 lds\n" \
"s_mov_b32 s54, s99\n" \
".Lsg2_z9_%=:\n" \
"s_sleep 1\n" \
"s_sub_u32 s54, s54, 1\n" \
"s_cbranch_scc0 .Lsg2_z9_%=\n" \
"s_waitcnt vmcnt(36)\n" \
"ds_read_b64_tr_b16 a[0:1], v1 offset:0\n" \
"ds_read_b64_tr_b16 a[2:3], v1 offset:512\n" \
"ds_read_b64_tr_b16 a[128:129], v1 offset:32\n" \
"ds_read_b64_tr_b16 a[130:131], v1 offset:544\n" \
"ds_read_b64_tr_b16 v[128:129], v1 offset:64\n" \
"ds_read_b64_tr_b16 v[130:131], v1 offset:576\n" \
"ds_read_b64_tr_b16 v[16:17], v1 offset:96\n" \
"ds_read_b64_tr_b16 v[18:19], v1 offset:608\n" \
"s_waitcnt lgkmcnt(0)\n" \
"s_add_u32 m0, s52, 0\n" \
"s_add_u32 s54, s97, 1310720\n" \
"s_lshr_b32 s55, s54, 8\n" \
"s_and_b32 s55, s55, 0x2000\n" \
"s_xor_b32 s54, s54, s55\n" \
"buffer_load_dwordx4 v0, s[40:43], s54 offen sc1 lds\n" \
"s_add_u32 m0, s52, 1024\n" \
"s_add_u32 s54, s97, 1343488\n" \
"s_lshr_b32 s55, s54, 8\n" \
"s_and_b32 s55, s55, 0x2000\n" \
"s_xor_b32 s54, s54, s55\n" \
"buffer_load_dwordx4 v0, s[40:43], s54 offen sc1 lds\n" \
"s_add_u32 m0, s52, 2048\n" \
"s_add_u32 s54, s97, 1376256\n" \
"s_lshr_b32 s55, s54, 8\n" \
"s_and_b32 s55, s55, 0x2000\n" \
"s_xor_b32 s54, s54, s55\n" \
"buffer_load_dwordx4 v0, s[40:43], s54 offen sc1 lds\n" \
"s_add_u32 m0, s52, 3072\n" \
"s_add_u32 s54, s97, 1409024\n" \
"s_lshr_b32 s55, s54, 8\n" \
"s_and_b32 s55, s55, 0x2000\n" \
"s_xor_b32 s54, s54, s55\n" \
"buffer_load_dwordx4 v0, s[40:43], s54 offen sc1 lds\n" \
"s_mov_b32 s54, s99\n" \
".Lsg2_z10_%=:\n" \
"s_sleep 1\n" \
"s_sub_u32 s54, s54, 1\n" \
"s_cbranch_scc0 .Lsg2_z10_%=\n" \
"s_waitcnt vmcnt(36)\n" \
"ds_read_b64_tr_b16 a[4:5], v1 offset:4096\n" \
"ds_read_b64_tr_b16 a[6:7], v1 offset:4608\n" \
"ds_read_b64_tr_b16 a[132:133], v1 offset:4128\n" \
"ds_read_b64_tr_b16 a[134:135], v1 offset:4640\n" \
"ds_read_b64_tr_b16 v[132:133], v1 offset:4160\n" \
"ds_read_b64_tr_b16 v[134:135], v1 offset:4672\n" \
"ds_read_b64_tr_b16 v[20:21], v1 offset:4192\n" \
"ds_read_b64_tr_b16 v[22:23], v1 offset:4704\n" \
"s_waitcnt lgkmcnt(0)\n" \
"s_add_u32 m0, s52, 4096\n" \
"s_add_u32 s54, s97, 1441792\n" \
"s_lshr_b32 s55, s54, 8\n" \
"s_and_b32 s55, s55, 0x2000\n" \
"s_xor_b32 s54, s54, s55\n" \
"buffer_load_dwordx4 v0, s[40:43], s54 offen sc1 lds\n" \
"s_add_u32 m0, s52, 5120\n" \
"s_add_u32 s54, s97, 1474560\n" \
"s_lshr_b32 s55, s54, 8\n" \
"s_and_b32 s55, s55, 0x2000\n" \
"s_xor_b32 s54, s54, s55\n" \
"buffer_load_dwordx4 v0, s[40:43], s54 offen sc1 lds\n" \
"s_add_u32 m0, s52, 6144\n" \
"s_add_u32 s54, s97, 1507328\n" \
"s_lshr_b32 s55, s54, 8\n" \
"s_and_b32 s55, s55, 0x2000\n" \
"s_xor_b32 s54, s54, s55\n" \
"buffer_load_dwordx4 v0, s[40:43], s54 offen sc1 lds\n" \
"s_add_u32 m0, s52, 7168\n" \
"s_add_u32 s54, s97, 1540096\n" \
"s_lshr_b32 s55, s54, 8\n" \
"s_and_b32 s55, s55, 0x2000\n" \
"s_xor_b32 s54, s54, s55\n" \
"buffer_load_dwordx4 v0, s[40:43], s54 offen sc1 lds\n" \
"s_mov_b32 s54, s99\n" \
".Lsg2_z11_%=:\n" \
"s_sleep 1\n" \
"s_sub_u32 s54, s54, 1\n" \
"s_cbranch_scc0 .Lsg2_z11_%=\n" \
"s_waitcnt vmcnt(36)\n" \
"ds_read_b64_tr_b16 a[8:9], v1 offset:8192\n" \
"ds_read_b64_tr_b16 a[10:11], v1 offset:8704\n" \
"ds_read_b64_tr_b16 a[136:137], v1 offset:8224\n" \
"ds_read_b64_tr_b16 a[138:139], v1 offset:8736\n" \
"ds_read_b64_tr_b16 v[136:137], v1 offset:8256\n" \
"ds_read_b64_tr_b16 v[138:139], v1 offset:8768\n" \
"ds_read_b64_tr_b16 v[24:25], v1 offset:8288\n" \
"ds_read_b64_tr_b16 v[26:27], v1 offset:8800\n" \
"s_waitcnt lgkmcnt(0)\n" \
"s_add_u32 m0, s52, 8192\n" \
"s_add_u32 s54, s97, 1572864\n" \
"s_lshr_b32 s55, s54, 8\n" \
"s_and_b32 s55, s55, 0x2000\n" \
"s_xor_b32 s54, s54, s55\n" \
"buffer_load_dwordx4 v0, s[40:43], s54 offen sc1 lds\n" \
"s_add_u32 m0, s52, 9216\n" \
"s_add_u32 s54, s97, 1605632\n" \
"s_lshr_b32 s55, s54, 8\n" \
"s_and_b32 s55, s55, 0x2000\n" \
"s_xor_b32 s54, s54, s55\n" \
"buffer_load_dwordx4 v0, s[40:43], s54 offen sc1 lds\n" \
"s_add_u32 m0, s52, 10240\n" \
"s_add_u32 s54, s97, 1638400\n" \
"s_lshr_b32 s55, s54, 8\n" \
"s_and_b32 s55, s55, 0x2000\n" \
"s_xor_b32 s54, s54, s55\n" \
"buffer_load_dwordx4 v0, s[40:43], s54 offen sc1 lds\n" \
"s_add_u32 m0, s52, 11264\n" \
"s_add_u32 s54, s97, 1671168\n" \
"s_lshr_b32 s55, s54, 8\n" \
"s_and_b32 s55, s55, 0x2000\n" \
"s_xor_b32 s54, s54, s55\n" \
"buffer_load_dwordx4 v0, s[40:43], s54 offen sc1 lds\n" \
"s_mov_b32 s54, s99\n" \
".Lsg2_z12_%=:\n" \
"s_sleep 1\n" \
"s_sub_u32 s54, s54, 1\n" \
"s_cbranch_scc0 .Lsg2_z12_%=\n" \
"s_waitcnt vmcnt(36)\n" \
"ds_read_b64_tr_b16 a[12:13], v1 offset:12288\n" \
"ds_read_b64_tr_b16 a[14:15], v1 offset:12800\n" \
"ds_read_b64_tr_b16 a[140:141], v1 offset:12320\n" \
"ds_read_b64_tr_b16 a[142:143], v1 offset:12832\n" \
"ds_read_b64_tr_b16 v[140:141], v1 offset:12352\n" \
"ds_read_b64_tr_b16 v[142:143], v1 offset:12864\n" \
"ds_read_b64_tr_b16 v[28:29], v1 offset:12384\n" \
"ds_read_b64_tr_b16 v[30:31], v1 offset:12896\n" \
"s_waitcnt lgkmcnt(0)\n" \
"s_add_u32 m0, s52, 12288\n" \
"s_add_u32 s54, s97, 1703936\n" \
"s_lshr_b32 s55, s54, 8\n" \
"s_and_b32 s55, s55, 0x2000\n" \
"s_xor_b32 s54, s54, s55\n" \
"buffer_load_dwordx4 v0, s[40:43], s54 offen sc1 lds\n" \
"s_add_u32 m0, s52, 13312\n" \
"s_add_u32 s54, s97, 1736704\n" \
"s_lshr_b32 s55, s54, 8\n" \
"s_and_b32 s55, s55, 0x2000\n" \
"s_xor_b32 s54, s54, s55\n" \
"buffer_load_dwordx4 v0, s[40:43], s54 offen sc1 lds\n" \
"s_add_u32 m0, s52, 14336\n" \
"s_add_u32 s54, s97, 1769472\n" \
"s_lshr_b32 s55, s54, 8\n" \
"s_and_b32 s55, s55, 0x2000\n" \
"s_xor_b32 s54, s54, s55\n" \
"buffer_load_dwordx4 v0, s[40:43], s54 offen sc1 lds\n" \
"s_add_u32 m0, s52, 15360\n" \
"s_add_u32 s54, s97, 1802240\n" \
"s_lshr_b32 s55, s54, 8\n" \
"s_and_b32 s55, s55, 0x2000\n" \
"s_xor_b32 s54, s54, s55\n" \
"buffer_load_dwordx4 v0, s[40:43], s54 offen sc1 lds\n" \
"s_mov_b32 s54, s99\n" \
".Lsg2_z13_%=:\n" \
"s_sleep 1\n" \
"s_sub_u32 s54, s54, 1\n" \
"s_cbranch_scc0 .Lsg2_z13_%=\n" \
"s_waitcnt vmcnt(36)\n" \
"ds_read_b64_tr_b16 a[16:17], v1 offset:16384\n" \
"ds_read_b64_tr_b16 a[18:19], v1 offset:16896\n" \
"ds_read_b64_tr_b16 a[144:145], v1 offset:16416\n" \
"ds_read_b64_tr_b16 a[146:147], v1 offset:16928\n" \
"ds_read_b64_tr_b16 v[144:145], v1 offset:16448\n" \
"ds_read_b64_tr_b16 v[146:147], v1 offset:16960\n" \
"ds_read_b64_tr_b16 v[32:33], v1 offset:16480\n" \
"ds_read_b64_tr_b16 v[34:35], v1 offset:16992\n" \
"s_waitcnt lgkmcnt(0)\n" \
"s_add_u32 m0, s52, 16384\n" \
"s_add_u32 s54, s97, 1835008\n" \
"s_lshr_b32 s55, s54, 8\n" \
"s_and_b32 s55, s55, 0x2000\n" \
"s_xor_b32 s54, s54, s55\n" \
"buffer_load_dwordx4 v0, s[40:43], s54 offen sc1 lds\n" \
"s_add_u32 m0, s52, 17408\n" \
"s_add_u32 s54, s97, 1867776\n" \
"s_lshr_b32 s55, s54, 8\n" \
"s_and_b32 s55, s55, 0x2000\n" \
"s_xor_b32 s54, s54, s55\n" \
"buffer_load_dwordx4 v0, s[40:43], s54 offen sc1 lds\n" \
"s_add_u32 m0, s52, 18432\n" \
"s_add_u32 s54, s97, 1900544\n" \
"s_lshr_b32 s55, s54, 8\n" \
"s_and_b32 s55, s55, 0x2000\n" \
"s_xor_b32 s54, s54, s55\n" \
"buffer_load_dwordx4 v0, s[40:43], s54 offen sc1 lds\n" \
"s_add_u32 m0, s52, 19456\n" \
"s_add_u32 s54, s97, 1933312\n" \
"s_lshr_b32 s55, s54, 8\n" \
"s_and_b32 s55, s55, 0x2000\n" \
"s_xor_b32 s54, s54, s55\n" \
"buffer_load_dwordx4 v0, s[40:43], s54 offen sc1 lds\n" \
"s_mov_b32 s54, s99\n" \
".Lsg2_z14_%=:\n" \
"s_sleep 1\n" \
"s_sub_u32 s54, s54, 1\n" \
"s_cbranch_scc0 .Lsg2_z14_%=\n" \
"s_waitcnt vmcnt(36)\n" \
"ds_read_b64_tr_b16 a[20:21], v1 offset:20480\n" \
"ds_read_b64_tr_b16 a[22:23], v1 offset:20992\n" \
"ds_read_b64_tr_b16 a[148:149], v1 offset:20512\n" \
"ds_read_b64_tr_b16 a[150:151], v1 offset:21024\n" \
"ds_read_b64_tr_b16 v[148:149], v1 offset:20544\n" \
"ds_read_b64_tr_b16 v[150:151], v1 offset:21056\n" \
"ds_read_b64_tr_b16 v[36:37], v1 offset:20576\n" \
"ds_read_b64_tr_b16 v[38:39], v1 offset:21088\n" \
"s_waitcnt lgkmcnt(0)\n" \
"s_add_u32 m0, s52, 20480\n" \
"s_add_u32 s54, s97, 1966080\n" \
"s_lshr_b32 s55, s54, 8\n" \
"s_and_b32 s55, s55, 0x2000\n" \
"s_xor_b32 s54, s54, s55\n" \
"buffer_load_dwordx4 v0, s[40:43], s54 offen sc1 lds\n" \
"s_add_u32 m0, s52, 21504\n" \
"s_add_u32 s54, s97, 1998848\n" \
"s_lshr_b32 s55, s54, 8\n" \
"s_and_b32 s55, s55, 0x2000\n" \
"s_xor_b32 s54, s54, s55\n" \
"buffer_load_dwordx4 v0, s[40:43], s54 offen sc1 lds\n" \
"s_add_u32 m0, s52, 22528\n" \
"s_add_u32 s54, s97, 2031616\n" \
"s_lshr_b32 s55, s54, 8\n" \
"s_and_b32 s55, s55, 0x2000\n" \
"s_xor_b32 s54, s54, s55\n" \
"buffer_load_dwordx4 v0, s[40:43], s54 offen sc1 lds\n" \
"s_add_u32 m0, s52, 23552\n" \
"s_add_u32 s54, s97, 2064384\n" \
"s_lshr_b32 s55, s54, 8\n" \
"s_and_b32 s55, s55, 0x2000\n" \
"s_xor_b32 s54, s54, s55\n" \
"buffer_load_dwordx4 v0, s[40:43], s54 offen sc1 lds\n" \
"s_mov_b32 s54, s99\n" \
".Lsg2_z15_%=:\n" \
"s_sleep 1\n" \
"s_sub_u32 s54, s54, 1\n" \
"s_cbranch_scc0 .Lsg2_z15_%=\n" \
"s_waitcnt vmcnt(36)\n" \
"ds_read_b64_tr_b16 a[24:25], v1 offset:24576\n" \
"ds_read_b64_tr_b16 a[26:27], v1 offset:25088\n" \
"ds_read_b64_tr_b16 a[152:153], v1 offset:24608\n" \
"ds_read_b64_tr_b16 a[154:155], v1 offset:25120\n" \
"ds_read_b64_tr_b16 v[152:153], v1 offset:24640\n" \
"ds_read_b64_tr_b16 v[154:155], v1 offset:25152\n" \
"ds_read_b64_tr_b16 v[40:41], v1 offset:24672\n" \
"ds_read_b64_tr_b16 v[42:43], v1 offset:25184\n" \
"s_waitcnt lgkmcnt(0)\n" \
"s_add_u32 m0, s52, 24576\n" \
"s_add_u32 s54, s97, 2097152\n" \
"s_lshr_b32 s55, s54, 8\n" \
"s_and_b32 s55, s55, 0x2000\n" \
"s_xor_b32 s54, s54, s55\n" \
"buffer_load_dwordx4 v0, s[40:43], s54 offen sc1 lds\n" \
"s_add_u32 m0, s52, 25600\n" \
"s_add_u32 s54, s97, 2129920\n" \
"s_lshr_b32 s55, s54, 8\n" \
"s_and_b32 s55, s55, 0x2000\n" \
"s_xor_b32 s54, s54, s55\n" \
"buffer_load_dwordx4 v0, s[40:43], s54 offen sc1 lds\n" \
"s_add_u32 m0, s52, 26624\n" \
"s_add_u32 s54, s97, 2162688\n" \
"s_lshr_b32 s55, s54, 8\n" \
"s_and_b32 s55, s55, 0x2000\n" \
"s_xor_b32 s54, s54, s55\n" \
"buffer_load_dwordx4 v0, s[40:43], s54 offen sc1 lds\n" \
"s_add_u32 m0, s52, 27648\n" \
"s_add_u32 s54, s97, 2195456\n" \
"s_lshr_b32 s55, s54, 8\n" \
"s_and_b32 s55, s55, 0x2000\n" \
"s_xor_b32 s54, s54, s55\n" \
"buffer_load_dwordx4 v0, s[40:43], s54 offen sc1 lds\n" \
"s_mov_b32 s54, s99\n" \
".Lsg2_z16_%=:\n" \
"s_sleep 1\n" \
"s_sub_u32 s54, s54, 1\n" \
"s_cbranch_scc0 .Lsg2_z16_%=\n" \
"s_waitcnt vmcnt(36)\n" \
"ds_read_b64_tr_b16 a[28:29], v1 offset:28672\n" \
"ds_read_b64_tr_b16 a[30:31], v1 offset:29184\n" \
"ds_read_b64_tr_b16 a[156:157], v1 offset:28704\n" \
"ds_read_b64_tr_b16 a[158:159], v1 offset:29216\n" \
"ds_read_b64_tr_b16 v[156:157], v1 offset:28736\n" \
"ds_read_b64_tr_b16 v[158:159], v1 offset:29248\n" \
"ds_read_b64_tr_b16 v[44:45], v1 offset:28768\n" \
"ds_read_b64_tr_b16 v[46:47], v1 offset:29280\n" \
"s_waitcnt lgkmcnt(0)\n" \
"s_add_u32 m0, s52, 28672\n" \
"s_add_u32 s54, s97, 2228224\n" \
"s_lshr_b32 s55, s54, 8\n" \
"s_and_b32 s55, s55, 0x2000\n" \
"s_xor_b32 s54, s54, s55\n" \
"buffer_load_dwordx4 v0, s[40:43], s54 offen sc1 lds\n" \
"s_add_u32 m0, s52, 29696\n" \
"s_add_u32 s54, s97, 2260992\n" \
"s_lshr_b32 s55, s54, 8\n" \
"s_and_b32 s55, s55, 0x2000\n" \
"s_xor_b32 s54, s54, s55\n" \
"buffer_load_dwordx4 v0, s[40:43], s54 offen sc1 lds\n" \
"s_add_u32 m0, s52, 30720\n" \
"s_add_u32 s54, s97, 2293760\n" \
"s_lshr_b32 s55, s54, 8\n" \
"s_and_b32 s55, s55, 0x2000\n" \
"s_xor_b32 s54, s54, s55\n" \
"buffer_load_dwordx4 v0, s[40:43], s54 offen sc1 lds\n" \
"s_add_u32 m0, s52, 31744\n" \
"s_add_u32 s54, s97, 2326528\n" \
"s_lshr_b32 s55, s54, 8\n" \
"s_and_b32 s55, s55, 0x2000\n" \
"s_xor_b32 s54, s54, s55\n" \
"buffer_load_dwordx4 v0, s[40:43], s54 offen sc1 lds\n" \
"s_mov_b32 s54, s99\n" \
".Lsg2_z17_%=:\n" \
"s_sleep 1\n" \
"s_sub_u32 s54, s54, 1\n" \
"s_cbranch_scc0 .Lsg2_z17_%=\n" \
"s_waitcnt vmcnt(36)\n" \
"ds_read_b64_tr_b16 a[32:33], v9 offset:32768\n" \
"ds_read_b64_tr_b16 a[34:35], v9 offset:33280\n" \
"ds_read_b64_tr_b16 a[160:161], v9 offset:32800\n" \
"ds_read_b64_tr_b16 a[162:163], v9 offset:33312\n" \
"ds_read_b64_tr_b16 v[160:161], v9 offset:32832\n" \
"ds_read_b64_tr_b16 v[162:163], v9 offset:33344\n" \
"ds_read_b64_tr_b16 v[48:49], v9 offset:32864\n" \
"ds_read_b64_tr_b16 v[50:51], v9 offset:33376\n" \
"s_waitcnt lgkmcnt(0)\n" \
"s_add_u32 m0, s53, 32768\n" \
"s_add_u32 s54, s97, 2359296\n" \
"s_lshr_b32 s55, s54, 8\n" \
"s_and_b32 s55, s55, 0x2000\n" \
"s_xor_b32 s54, s54, s55\n" \
"buffer_load_dwordx4 v0, s[40:43], s54 offen sc1 lds\n" \
"s_add_u32 m0, s53, 33792\n" \
"s_add_u32 s54, s97, 2392064\n" \
"s_lshr_b32 s55, s54, 8\n" \
"s_and_b32 s55, s55, 0x2000\n" \
"s_xor_b32 s54, s54, s55\n" \
"buffer_load_dwordx4 v0, s[40:43], s54 offen sc1 lds\n" \
"s_add_u32 m0, s53, 34816\n" \
"s_add_u32 s54, s97, 2424832\n" \
"s_lshr_b32 s55, s54, 8\n" \
"s_and_b32 s55, s55, 0x2000\n" \
"s_xor_b32 s54, s54, s55\n" \
"buffer_load_dwordx4 v0, s[40:43], s54 offen sc1 lds\n" \
"s_add_u32 m0, s53, 35840\n" \
"s_add_u32 s54, s97, 2457600\n" \
"s_lshr_b32 s55, s54, 8\n" \
"s_and_b32 s55, s55, 0x2000\n" \
"s_xor_b32 s54, s54, s55\n" \
"buffer_load_dwordx4 v0, s[40:43], s54 offen sc1 lds\n" \
"s_mov_b32 s54, s99\n" \
".Lsg2_z18_%=:\n" \
"s_sleep 1\n" \
"s_sub_u32 s54, s54, 1\n" \
"s_cbranch_scc0 .Lsg2_z18_%=\n" \
"s_waitcnt vmcnt(36)\n" \
"ds_read_b64_tr_b16 a[36:37], v9 offset:36864\n" \
"ds_read_b64_tr_b16 a[38:39], v9 offset:37376\n" \
"ds_read_b64_tr_b16 a[164:165], v9 offset:36896\n" \
"ds_read_b64_tr_b16 a[166:167], v9 offset:37408\n" \
"ds_read_b64_tr_b16 v[164:165], v9 offset:36928\n" \
"ds_read_b64_tr_b16 v[166:167], v9 offset:37440\n" \
"ds_read_b64_tr_b16 v[52:53], v9 offset:36960\n" \
"ds_read_b64_tr_b16 v[54:55], v9 offset:37472\n" \
"s_waitcnt lgkmcnt(0)\n" \
"s_add_u32 m0, s53, 36864\n" \
"s_add_u32 s54, s97, 2490368\n" \
"s_lshr_b32 s55, s54, 8\n" \
"s_and_b32 s55, s55, 0x2000\n" \
"s_xor_b32 s54, s54, s55\n" \
"buffer_load_dwordx4 v0, s[40:43], s54 offen sc1 lds\n" \
"s_add_u32 m0, s53, 37888\n" \
"s_add_u32 s54, s97, 2523136\n" \
"s_lshr_b32 s55, s54, 8\n" \
"s_and_b32 s55, s55, 0x2000\n" \
"s_xor_b32 s54, s54, s55\n" \
"buffer_load_dwordx4 v0, s[40:43], s54 offen sc1 lds\n" \
"s_add_u32 m0, s53, 38912\n" \
"s_add_u32 s54, s97, 2555904\n" \
"s_lshr_b32 s55, s54, 8\n" \
"s_and_b32 s55, s55, 0x2000\n" \
"s_xor_b32 s54, s54, s55\n" \
"buffer_load_dwordx4 v0, s[40:43], s54 offen sc1 lds\n" \
"s_add_u32 m0, s53, 39936\n" \
"s_add_u32 s54, s97, 2588672\n" \
"s_lshr_b32 s55, s54, 8\n" \
"s_and_b32 s55, s55, 0x2000\n" \
"s_xor_b32 s54, s54, s55\n" \
"buffer_load_dwordx4 v0, s[40:43], s54 offen sc1 lds\n" \
"s_mov_b32 s54, s99\n" \
".Lsg2_z19_%=:\n" \
"s_sleep 1\n" \
"s_sub_u32 s54, s54, 1\n" \
"s_cbranch_scc0 .Lsg2_z19_%=\n" \
"s_waitcnt vmcnt(36)\n" \
"ds_read_b64_tr_b16 a[40:41], v1 offset:0\n" \
"ds_read_b64_tr_b16 a[42:43], v1 offset:512\n" \
"ds_read_b64_tr_b16 a[168:169], v1 offset:32\n" \
"ds_read_b64_tr_b16 a[170:171], v1 offset:544\n" \
"ds_read_b64_tr_b16 v[168:169], v1 offset:64\n" \
"ds_read_b64_tr_b16 v[170:171], v1 offset:576\n" \
"ds_read_b64_tr_b16 v[56:57], v1 offset:96\n" \
"ds_read_b64_tr_b16 v[58:59], v1 offset:608\n" \
"s_waitcnt lgkmcnt(0)\n" \
"s_add_u32 m0, s52, 0\n" \
"s_add_u32 s54, s97, 2621440\n" \
"s_lshr_b32 s55, s54, 8\n" \
"s_and_b32 s55, s55, 0x2000\n" \
"s_xor_b32 s54, s54, s55\n" \
"buffer_load_dwordx4 v0, s[40:43], s54 offen sc1 lds\n" \
"s_add_u32 m0, s52, 1024\n" \
"s_add_u32 s54, s97, 2654208\n" \
"s_lshr_b32 s55, s54, 8\n" \
"s_and_b32 s55, s55, 0x2000\n" \
"s_xor_b32 s54, s54, s55\n" \
"buffer_load_dwordx4 v0, s[40:43], s54 offen sc1 lds\n" \
"s_add_u32 m0, s52, 2048\n" \
"s_add_u32 s54, s97, 2686976\n" \
"s_lshr_b32 s55, s54, 8\n" \
"s_and_b32 s55, s55, 0x2000\n" \
"s_xor_b32 s54, s54, s55\n" \
"buffer_load_dwordx4 v0, s[40:43], s54 offen sc1 lds\n" \
"s_add_u32 m0, s52, 3072\n" \
"s_add_u32 s54, s97, 2719744\n" \
"s_lshr_b32 s55, s54, 8\n" \
"s_and_b32 s55, s55, 0x2000\n" \
"s_xor_b32 s54, s54, s55\n" \
"buffer_load_dwordx4 v0, s[40:43], s54 offen sc1 lds\n" \
"s_mov_b32 s54, s99\n" \
".Lsg2_z20_%=:\n" \
"s_sleep 1\n" \
"s_sub_u32 s54, s54, 1\n" \
"s_cbranch_scc0 .Lsg2_z20_%=\n" \
"s_waitcnt vmcnt(36)\n" \
"ds_read_b64_tr_b16 a[44:45], v1 offset:4096\n" \
"ds_read_b64_tr_b16 a[46:47], v1 offset:4608\n" \
"ds_read_b64_tr_b16 a[172:173], v1 offset:4128\n" \
"ds_read_b64_tr_b16 a[174:175], v1 offset:4640\n" \
"ds_read_b64_tr_b16 v[172:173], v1 offset:4160\n" \
"ds_read_b64_tr_b16 v[174:175], v1 offset:4672\n" \
"ds_read_b64_tr_b16 v[60:61], v1 offset:4192\n" \
"ds_read_b64_tr_b16 v[62:63], v1 offset:4704\n" \
"s_waitcnt lgkmcnt(0)\n" \
"s_add_u32 m0, s52, 4096\n" \
"s_add_u32 s54, s97, 2752512\n" \
"s_lshr_b32 s55, s54, 8\n" \
"s_and_b32 s55, s55, 0x2000\n" \
"s_xor_b32 s54, s54, s55\n" \
"buffer_load_dwordx4 v0, s[40:43], s54 offen sc1 lds\n" \
"s_add_u32 m0, s52, 5120\n" \
"s_add_u32 s54, s97, 2785280\n" \
"s_lshr_b32 s55, s54, 8\n" \
"s_and_b32 s55, s55, 0x2000\n" \
"s_xor_b32 s54, s54, s55\n" \
"buffer_load_dwordx4 v0, s[40:43], s54 offen sc1 lds\n" \
"s_add_u32 m0, s52, 6144\n" \
"s_add_u32 s54, s97, 2818048\n" \
"s_lshr_b32 s55, s54, 8\n" \
"s_and_b32 s55, s55, 0x2000\n" \
"s_xor_b32 s54, s54, s55\n" \
"buffer_load_dwordx4 v0, s[40:43], s54 offen sc1 lds\n" \
"s_add_u32 m0, s52, 7168\n" \
"s_add_u32 s54, s97, 2850816\n" \
"s_lshr_b32 s55, s54, 8\n" \
"s_and_b32 s55, s55, 0x2000\n" \
"s_xor_b32 s54, s54, s55\n" \
"buffer_load_dwordx4 v0, s[40:43], s54 offen sc1 lds\n" \
"s_mov_b32 s54, s99\n" \
".Lsg2_z21_%=:\n" \
"s_sleep 1\n" \
"s_sub_u32 s54, s54, 1\n" \
"s_cbranch_scc0 .Lsg2_z21_%=\n" \
"s_waitcnt vmcnt(36)\n" \
"ds_read_b64_tr_b16 a[48:49], v1 offset:8192\n" \
"ds_read_b64_tr_b16 a[50:51], v1 offset:8704\n" \
"ds_read_b64_tr_b16 a[176:177], v1 offset:8224\n" \
"ds_read_b64_tr_b16 a[178:179], v1 offset:8736\n" \
"ds_read_b64_tr_b16 v[176:177], v1 offset:8256\n" \
"ds_read_b64_tr_b16 v[178:179], v1 offset:8768\n" \
"ds_read_b64_tr_b16 v[64:65], v1 offset:8288\n" \
"ds_read_b64_tr_b16 v[66:67], v1 offset:8800\n" \
"s_waitcnt lgkmcnt(0)\n" \
"s_add_u32 m0, s52, 8192\n" \
"s_add_u32 s54, s97, 2883584\n" \
"s_lshr_b32 s55, s54, 8\n" \
"s_and_b32 s55, s55, 0x2000\n" \
"s_xor_b32 s54, s54, s55\n" \
"buffer_load_dwordx4 v0, s[40:43], s54 offen sc1 lds\n" \
"s_add_u32 m0, s52, 9216\n" \
"s_add_u32 s54, s97, 2916352\n" \
"s_lshr_b32 s55, s54, 8\n" \
"s_and_b32 s55, s55, 0x2000\n" \
"s_xor_b32 s54, s54, s55\n" \
"buffer_load_dwordx4 v0, s[40:43], s54 offen sc1 lds\n" \
"s_add_u32 m0, s52, 10240\n" \
"s_add_u32 s54, s97, 2949120\n" \
"s_lshr_b32 s55, s54, 8\n" \
"s_and_b32 s55, s55, 0x2000\n" \
"s_xor_b32 s54, s54, s55\n" \
"buffer_load_dwordx4 v0, s[40:43], s54 offen sc1 lds\n" \
"s_add_u32 m0, s52, 11264\n" \
"s_add_u32 s54, s97, 2981888\n" \
"s_lshr_b32 s55, s54, 8\n" \
"s_and_b32 s55, s55, 0x2000\n" \
"s_xor_b32 s54, s54, s55\n" \
"buffer_load_dwordx4 v0, s[40:43], s54 offen sc1 lds\n" \
"s_mov_b32 s54, s99\n" \
".Lsg2_z22_%=:\n" \
"s_sleep 1\n" \
"s_sub_u32 s54, s54, 1\n" \
"s_cbranch_scc0 .Lsg2_z22_%=\n" \
"s_waitcnt vmcnt(36)\n" \
"ds_read_b64_tr_b16 a[52:53], v1 offset:12288\n" \
"ds_read_b64_tr_b16 a[54:55], v1 offset:12800\n" \
"ds_read_b64_tr_b16 a[180:181], v1 offset:12320\n" \
"ds_read_b64_tr_b16 a[182:183], v1 offset:12832\n" \
"ds_read_b64_tr_b16 v[180:181], v1 offset:12352\n" \
"ds_read_b64_tr_b16 v[182:183], v1 offset:12864\n" \
"ds_read_b64_tr_b16 v[68:69], v1 offset:12384\n" \
"ds_read_b64_tr_b16 v[70:71], v1 offset:12896\n" \
"s_waitcnt lgkmcnt(0)\n" \
"s_add_u32 m0, s52, 12288\n" \
"s_add_u32 s54, s97, 3014656\n" \
"s_lshr_b32 s55, s54, 8\n" \
"s_and_b32 s55, s55, 0x2000\n" \
"s_xor_b32 s54, s54, s55\n" \
"buffer_load_dwordx4 v0, s[40:43], s54 offen sc1 lds\n" \
"s_add_u32 m0, s52, 13312\n" \
"s_add_u32 s54, s97, 3047424\n" \
"s_lshr_b32 s55, s54, 8\n" \
"s_and_b32 s55, s55, 0x2000\n" \
"s_xor_b32 s54, s54, s55\n" \
"buffer_load_dwordx4 v0, s[40:43], s54 offen sc1 lds\n" \
"s_add_u32 m0, s52, 14336\n" \
"s_add_u32 s54, s97, 3080192\n" \
"s_lshr_b32 s55, s54, 8\n" \
"s_and_b32 s55, s55, 0x2000\n" \
"s_xor_b32 s54, s54, s55\n" \
"buffer_load_dwordx4 v0, s[40:43], s54 offen sc1 lds\n" \
"s_add_u32 m0, s52, 15360\n" \
"s_add_u32 s54, s97, 3112960\n" \
"s_lshr_b32 s55, s54, 8\n" \
"s_and_b32 s55, s55, 0x2000\n" \
"s_xor_b32 s54, s54, s55\n" \
"buffer_load_dwordx4 v0, s[40:43], s54 offen sc1 lds\n" \
"s_mov_b32 s54, s99\n" \
".Lsg2_z23_%=:\n" \
"s_sleep 1\n" \
"s_sub_u32 s54, s54, 1\n" \
"s_cbranch_scc0 .Lsg2_z23_%=\n" \
"s_waitcnt vmcnt(36)\n" \
"ds_read_b64_tr_b16 a[56:57], v1 offset:16384\n" \
"ds_read_b64_tr_b16 a[58:59], v1 offset:16896\n" \
"ds_read_b64_tr_b16 a[184:185], v1 offset:16416\n" \
"ds_read_b64_tr_b16 a[186:187], v1 offset:16928\n" \
"ds_read_b64_tr_b16 v[184:185], v1 offset:16448\n" \
"ds_read_b64_tr_b16 v[186:187], v1 offset:16960\n" \
"ds_read_b64_tr_b16 v[72:73], v1 offset:16480\n" \
"ds_read_b64_tr_b16 v[74:75], v1 offset:16992\n" \
"s_waitcnt lgkmcnt(0)\n" \
"s_add_u32 m0, s52, 16384\n" \
"s_add_u32 s54, s97, 3145728\n" \
"s_lshr_b32 s55, s54, 8\n" \
"s_and_b32 s55, s55, 0x2000\n" \
"s_xor_b32 s54, s54, s55\n" \
"buffer_load_dwordx4 v0, s[40:43], s54 offen sc1 lds\n" \
"s_add_u32 m0, s52, 17408\n" \
"s_add_u32 s54, s97, 3178496\n" \
"s_lshr_b32 s55, s54, 8\n" \
"s_and_b32 s55, s55, 0x2000\n" \
"s_xor_b32 s54, s54, s55\n" \
"buffer_load_dwordx4 v0, s[40:43], s54 offen sc1 lds\n" \
"s_add_u32 m0, s52, 18432\n" \
"s_add_u32 s54, s97, 3211264\n" \
"s_lshr_b32 s55, s54, 8\n" \
"s_and_b32 s55, s55, 0x2000\n" \
"s_xor_b32 s54, s54, s55\n" \
"buffer_load_dwordx4 v0, s[40:43], s54 offen sc1 lds\n" \
"s_add_u32 m0, s52, 19456\n" \
"s_add_u32 s54, s97, 3244032\n" \
"s_lshr_b32 s55, s54, 8\n" \
"s_and_b32 s55, s55, 0x2000\n" \
"s_xor_b32 s54, s54, s55\n" \
"buffer_load_dwordx4 v0, s[40:43], s54 offen sc1 lds\n" \
"s_mov_b32 s54, s99\n" \
".Lsg2_z24_%=:\n" \
"s_sleep 1\n" \
"s_sub_u32 s54, s54, 1\n" \
"s_cbranch_scc0 .Lsg2_z24_%=\n" \
"s_waitcnt vmcnt(36)\n" \
"ds_read_b64_tr_b16 a[60:61], v1 offset:20480\n" \
"ds_read_b64_tr_b16 a[62:63], v1 offset:20992\n" \
"ds_read_b64_tr_b16 a[188:189], v1 offset:20512\n" \
"ds_read_b64_tr_b16 a[190:191], v1 offset:21024\n" \
"ds_read_b64_tr_b16 v[188:189], v1 offset:20544\n" \
"ds_read_b64_tr_b16 v[190:191], v1 offset:21056\n" \
"ds_read_b64_tr_b16 v[76:77], v1 offset:20576\n" \
"ds_read_b64_tr_b16 v[78:79], v1 offset:21088\n" \
"s_waitcnt lgkmcnt(0)\n" \
"s_add_u32 m0, s52, 20480\n" \
"s_add_u32 s54, s97, 3276800\n" \
"s_lshr_b32 s55, s54, 8\n" \
"s_and_b32 s55, s55, 0x2000\n" \
"s_xor_b32 s54, s54, s55\n" \
"buffer_load_dwordx4 v0, s[40:43], s54 offen sc1 lds\n" \
"s_add_u32 m0, s52, 21504\n" \
"s_add_u32 s54, s97, 3309568\n" \
"s_lshr_b32 s55, s54, 8\n" \
"s_and_b32 s55, s55, 0x2000\n" \
"s_xor_b32 s54, s54, s55\n" \
"buffer_load_dwordx4 v0, s[40:43], s54 offen sc1 lds\n" \
"s_add_u32 m0, s52, 22528\n" \
"s_add_u32 s54, s97, 3342336\n" \
"s_lshr_b32 s55, s54, 8\n" \
"s_and_b32 s55, s55, 0x2000\n" \
"s_xor_b32 s54, s54, s55\n" \
"buffer_load_dwordx4 v0, s[40:43], s54 offen sc1 lds\n" \
"s_add_u32 m0, s52, 23552\n" \
"s_add_u32 s54, s97, 3375104\n" \
"s_lshr_b32 s55, s54, 8\n" \
"s_and_b32 s55, s55, 0x2000\n" \
"s_xor_b32 s54, s54, s55\n" \
"buffer_load_dwordx4 v0, s[40:43], s54 offen sc1 lds\n" \
"s_mov_b32 s54, s99\n" \
".Lsg2_z25_%=:\n" \
"s_sleep 1\n" \
"s_sub_u32 s54, s54, 1\n" \
"s_cbranch_scc0 .Lsg2_z25_%=\n" \
"s_waitcnt vmcnt(36)\n" \
"ds_read_b64_tr_b16 a[64:65], v1 offset:24576\n" \
"ds_read_b64_tr_b16 a[66:67], v1 offset:25088\n" \
"ds_read_b64_tr_b16 a[192:193], v1 offset:24608\n" \
"ds_read_b64_tr_b16 a[194:195], v1 offset:25120\n" \
"ds_read_b64_tr_b16 v[192:193], v1 offset:24640\n" \
"ds_read_b64_tr_b16 v[194:195], v1 offset:25152\n" \
"ds_read_b64_tr_b16 v[80:81], v1 offset:24672\n" \
"ds_read_b64_tr_b16 v[82:83], v1 offset:25184\n" \
"s_waitcnt lgkmcnt(0)\n" \
"s_add_u32 m0, s52, 24576\n" \
"s_add_u32 s54, s97, 3407872\n" \
"s_lshr_b32 s55, s54, 8\n" \
"s_and_b32 s55, s55, 0x2000\n" \
"s_xor_b32 s54, s54, s55\n" \
"buffer_load_dwordx4 v0, s[40:43], s54 offen sc1 lds\n" \
"s_add_u32 m0, s52, 25600\n" \
"s_add_u32 s54, s97, 3440640\n" \
"s_lshr_b32 s55, s54, 8\n" \
"s_and_b32 s55, s55, 0x2000\n" \
"s_xor_b32 s54, s54, s55\n" \
"buffer_load_dwordx4 v0, s[40:43], s54 offen sc1 lds\n" \
"s_add_u32 m0, s52, 26624\n" \
"s_add_u32 s54, s97, 3473408\n" \
"s_lshr_b32 s55, s54, 8\n" \
"s_and_b32 s55, s55, 0x2000\n" \
"s_xor_b32 s54, s54, s55\n" \
"buffer_load_dwordx4 v0, s[40:43], s54 offen sc1 lds\n" \
"s_add_u32 m0, s52, 27648\n" \
"s_add_u32 s54, s97, 3506176\n" \
"s_lshr_b32 s55, s54, 8\n" \
"s_and_b32 s55, s55, 0x2000\n" \
"s_xor_b32 s54, s54, s55\n" \
"buffer_load_dwordx4 v0, s[40:43], s54 offen sc1 lds\n" \
"s_mov_b32 s54, s99\n" \
".Lsg2_z26_%=:\n" \
"s_sleep 1\n" \
"s_sub_u32 s54, s54, 1\n" \
"s_cbranch_scc0 .Lsg2_z26_%=\n" \
"s_waitcnt vmcnt(36)\n" \
"ds_read_b64_tr_b16 a[68:69], v1 offset:28672\n" \
"ds_read_b64_tr_b16 a[70:71], v1 offset:29184\n" \
"ds_read_b64_tr_b16 a[196:197], v1 offset:28704\n" \
"ds_read_b64_tr_b16 a[198:199], v1 offset:29216\n" \
"ds_read_b64_tr_b16 v[196:197], v1 offset:28736\n" \
"ds_read_b64_tr_b16 v[198:199], v1 offset:29248\n" \
"ds_read_b64_tr_b16 v[84:85], v1 offset:28768\n" \
"ds_read_b64_tr_b16 v[86:87], v1 offset:29280\n" \
"s_waitcnt lgkmcnt(0)\n" \
"s_add_u32 m0, s52, 28672\n" \
"s_add_u32 s54, s97, 3538944\n" \
"s_lshr_b32 s55, s54, 8\n" \
"s_and_b32 s55, s55, 0x2000\n" \
"s_xor_b32 s54, s54, s55\n" \
"buffer_load_dwordx4 v0, s[40:43], s54 offen sc1 lds\n" \
"s_add_u32 m0, s52, 29696\n" \
"s_add_u32 s54, s97, 3571712\n" \
"s_lshr_b32 s55, s54, 8\n" \
"s_and_b32 s55, s55, 0x2000\n" \
"s_xor_b32 s54, s54, s55\n" \
"buffer_load_dwordx4 v0, s[40:43], s54 offen sc1 lds\n" \
"s_add_u32 m0, s52, 30720\n" \
"s_add_u32 s54, s97, 3604480\n" \
"s_lshr_b32 s55, s54, 8\n" \
"s_and_b32 s55, s55, 0x2000\n" \
"s_xor_b32 s54, s54, s55\n" \
"buffer_load_dwordx4 v0, s[40:43], s54 offen sc1 lds\n" \
"s_add_u32 m0, s52, 31744\n" \
"s_add_u32 s54, s97, 3637248\n" \
"s_lshr_b32 s55, s54, 8\n" \
"s_and_b32 s55, s55, 0x2000\n" \
"s_xor_b32 s54, s54, s55\n" \
"buffer_load_dwordx4 v0, s[40:43], s54 offen sc1 lds\n" \
"s_mov_b32 s54, s99\n" \
".Lsg2_z27_%=:\n" \
"s_sleep 1\n" \
"s_sub_u32 s54, s54, 1\n" \
"s_cbranch_scc0 .Lsg2_z27_%=\n" \
"s_waitcnt vmcnt(36)\n" \
"ds_read_b64_tr_b16 a[72:73], v9 offset:32768\n" \
"ds_read_b64_tr_b16 a[74:75], v9 offset:33280\n" \
"ds_read_b64_tr_b16 a[200:201], v9 offset:32800\n" \
"ds_read_b64_tr_b16 a[202:203], v9 offset:33312\n" \
"ds_read_b64_tr_b16 v[200:201], v9 offset:32832\n" \
"ds_read_b64_tr_b16 v[202:203], v9 offset:33344\n" \
"ds_read_b64_tr_b16 v[88:89], v9 offset:32864\n" \
"ds_read_b64_tr_b16 v[90:91], v9 offset:33376\n" \
"s_waitcnt lgkmcnt(0)\n" \
"s_waitcnt vmcnt(32)\n" \
"ds_read_b64_tr_b16 a[76:77], v9 offset:36864\n" \
"ds_read_b64_tr_b16 a[78:79], v9 offset:37376\n" \
"ds_read_b64_tr_b16 a[204:205], v9 offset:36896\n" \
"ds_read_b64_tr_b16 a[206:207], v9 offset:37408\n" \
"ds_read_b64_tr_b16 v[204:205], v9 offset:36928\n" \
"ds_read_b64_tr_b16 v[206:207], v9 offset:37440\n" \
"ds_read_b64_tr_b16 v[92:93], v9 offset:36960\n" \
"ds_read_b64_tr_b16 v[94:95], v9 offset:37472\n" \
"s_waitcnt lgkmcnt(0)\n" \
"s_waitcnt vmcnt(28)\n" \
"ds_read_b64_tr_b16 a[80:81], v1 offset:0\n" \
"ds_read_b64_tr_b16 a[82:83], v1 offset:512\n" \
"ds_read_b64_tr_b16 a[208:209], v1 offset:32\n" \
"ds_read_b64_tr_b16 a[210:211], v1 offset:544\n" \
"ds_read_b64_tr_b16 v[208:209], v1 offset:64\n" \
"ds_read_b64_tr_b16 v[210:211], v1 offset:576\n" \
"ds_read_b64_tr_b16 v[96:97], v1 offset:96\n" \
"ds_read_b64_tr_b16 v[98:99], v1 offset:608\n" \
"s_waitcnt lgkmcnt(0)\n" \
"ds_write_b128 v2, v[16:19] offset:0\n" \
"ds_write_b128 v2, v[20:23] offset:1024\n" \
"ds_write_b128 v2, v[24:27] offset:2048\n" \
"ds_write_b128 v2, v[28:31] offset:3072\n" \
"s_waitcnt vmcnt(24)\n" \
"ds_read_b64_tr_b16 a[84:85], v1 offset:4096\n" \
"ds_read_b64_tr_b16 a[86:87], v1 offset:4608\n" \
"ds_read_b64_tr_b16 a[212:213], v1 offset:4128\n" \
"ds_read_b64_tr_b16 a[214:215], v1 offset:4640\n" \
"ds_read_b64_tr_b16 v[212:213], v1 offset:4160\n" \
"ds_read_b64_tr_b16 v[214:215], v1 offset:4672\n" \
"ds_read_b64_tr_b16 v[100:101], v1 offset:4192\n" \
"ds_read_b64_tr_b16 v[102:103], v1 offset:4704\n" \
"s_waitcnt lgkmcnt(0)\n" \
"ds_write_b128 v2, v[32:35] offset:4096\n" \
"ds_write_b128 v2, v[36:39] offset:5120\n" \
"ds_write_b128 v2, v[40:43] offset:6144\n" \
"ds_write_b128 v2, v[44:47] offset:7168\n" \
"s_waitcnt vmcnt(20)\n" \
"ds_read_b64_tr_b16 a[88:89], v1 offset:8192\n" \
"ds_read_b64_tr_b16 a[90:91], v1 offset:8704\n" \
"ds_read_b64_tr_b16 a[216:217], v1 offset:8224\n" \
"ds_read_b64_tr_b16 a[218:219], v1 offset:8736\n" \
"ds_read_b64_tr_b16 v[216:217], v1 offset:8256\n" \
"ds_read_b64_tr_b16 v[218:219], v1 offset:8768\n" \
"ds_read_b64_tr_b16 v[104:105], v1 offset:8288\n" \
"ds_read_b64_tr_b16 v[106:107], v1 offset:8800\n" \
"s_waitcnt lgkmcnt(0)\n" \
"ds_write_b128 v2, v[48:51] offset:8192\n" \
"ds_write_b128 v2, v[52:55] offset:9216\n" \
"ds_write_b128 v2, v[56:59] offset:10240\n" \
"ds_write_b128 v2, v[60:63] offset:11264\n" \
"s_waitcnt vmcnt(16)\n" \
"ds_read_b64_tr_b16 a[92:93], v1 offset:12288\n" \
"ds_read_b64_tr_b16 a[94:95], v1 offset:12800\n" \
"ds_read_b64_tr_b16 a[220:221], v1 offset:12320\n" \
"ds_read_b64_tr_b16 a[222:223], v1 offset:12832\n" \
"ds_read_b64_tr_b16 v[220:221], v1 offset:12352\n" \
"ds_read_b64_tr_b16 v[222:223], v1 offset:12864\n" \
"ds_read_b64_tr_b16 v[108:109], v1 offset:12384\n" \
"ds_read_b64_tr_b16 v[110:111], v1 offset:12896\n" \
"s_waitcnt lgkmcnt(0)\n" \
"ds_write_b128 v2, v[64:67] offset:12288\n" \
"ds_write_b128 v2, v[68:71] offset:13312\n" \
"ds_write_b128 v2, v[72:75] offset:14336\n" \
"ds_write_b128 v2, v[76:79] offset:15360\n" \
"s_waitcnt vmcnt(12)\n" \
"ds_read_b64_tr_b16 a[96:97], v1 offset:16384\n" \
"ds_read_b64_tr_b16 a[98:99], v1 offset:16896\n" \
"ds_read_b64_tr_b16 a[224:225], v1 offset:16416\n" \
"ds_read_b64_tr_b16 a[226:227], v1 offset:16928\n" \
"ds_read_b64_tr_b16 v[224:225], v1 offset:16448\n" \
"ds_read_b64_tr_b16 v[226:227], v1 offset:16960\n" \
"ds_read_b64_tr_b16 v[16:17], v1 offset:16480\n" \
"ds_read_b64_tr_b16 v[18:19], v1 offset:16992\n" \
"s_waitcnt lgkmcnt(0)\n" \
"ds_write_b128 v2, v[80:83] offset:16384\n" \
"ds_write_b128 v2, v[84:87] offset:17408\n" \
"ds_write_b128 v2, v[88:91] offset:18432\n" \
"ds_write_b128 v2, v[92:95] offset:19456\n" \
"s_waitcnt vmcnt(8)\n" \
"ds_read_b64_tr_b16 a[100:101], v1 offset:20480\n" \
"ds_read_b64_tr_b16 a[102:103], v1 offset:20992\n" \
"ds_read_b64_tr_b16 a[228:229], v1 offset:20512\n" \
"ds_read_b64_tr_b16 a[230:231], v1 offset:21024\n" \
"ds_read_b64_tr_b16 v[228:229], v1 offset:20544\n" \
"ds_read_b64_tr_b16 v[230:231], v1 offset:21056\n" \
"ds_read_b64_tr_b16 v[20:21], v1 offset:20576\n" \
"ds_read_b64_tr_b16 v[22:23], v1 offset:21088\n" \
"s_waitcnt lgkmcnt(0)\n" \
"ds_write_b128 v2, v[96:99] offset:20480\n" \
"ds_write_b128 v2, v[100:103] offset:21504\n" \
"ds_write_b128 v2, v[104:107] offset:22528\n" \
"ds_write_b128 v2, v[108:111] offset:23552\n" \
"s_waitcnt vmcnt(4)\n" \
"ds_read_b64_tr_b16 a[104:105], v1 offset:24576\n" \
"ds_read_b64_tr_b16 a[106:107], v1 offset:25088\n" \
"ds_read_b64_tr_b16 a[232:233], v1 offset:24608\n" \
"ds_read_b64_tr_b16 a[234:235], v1 offset:25120\n" \
"ds_read_b64_tr_b16 v[232:233], v1 offset:24640\n" \
"ds_read_b64_tr_b16 v[234:235], v1 offset:25152\n" \
"ds_read_b64_tr_b16 v[24:25], v1 offset:24672\n" \
"ds_read_b64_tr_b16 v[26:27], v1 offset:25184\n" \
"s_waitcnt lgkmcnt(0)\n" \
"ds_write_b128 v2, v[16:19] offset:24576\n" \
"ds_write_b128 v2, v[20:23] offset:25600\n" \
"ds_write_b128 v2, v[24:27] offset:26624\n" \
"s_waitcnt vmcnt(0)\n" \
"ds_read_b64_tr_b16 a[108:109], v1 offset:28672\n" \
"ds_read_b64_tr_b16 a[110:111], v1 offset:29184\n" \
"ds_read_b64_tr_b16 a[236:237], v1 offset:28704\n" \
"ds_read_b64_tr_b16 a[238:239], v1 offset:29216\n" \
"ds_read_b64_tr_b16 v[236:237], v1 offset:28736\n" \
"ds_read_b64_tr_b16 v[238:239], v1 offset:29248\n" \
"ds_read_b64_tr_b16 v[28:29], v1 offset:28768\n" \
"ds_read_b64_tr_b16 v[30:31], v1 offset:29280\n" \
"s_waitcnt lgkmcnt(0)\n" \
"ds_write_b128 v2, v[28:31] offset:28672\n" \
"s_waitcnt vmcnt(0) lgkmcnt(0)\n" \
"s_barrier\n" \
"s_mul_i32 s54, s48, 7\n" \
"s_add_u32 s54, s54, 0\n" \
"s_lshr_b32 s55, s54, 1\n" \
"s_lshl_b32 s55, s55, 14\n" \
"s_and_b32 s54, s54, 1\n" \
"s_lshl_b32 s54, s54, 12\n" \
"s_add_u32 s54, s54, s55\n" \
"s_add_u32 s86, s54, s85\n" \
"s_mul_i32 s54, s48, 7\n" \
"s_add_u32 s54, s54, 1\n" \
"s_lshr_b32 s55, s54, 1\n" \
"s_lshl_b32 s55, s55, 14\n" \
"s_and_b32 s54, s54, 1\n" \
"s_lshl_b32 s54, s54, 12\n" \
"s_add_u32 s54, s54, s55\n" \
"s_add_u32 s87, s54, s85\n" \
"s_mul_i32 s54, s48, 7\n" \
"s_add_u32 s54, s54, 2\n" \
"s_lshr_b32 s55, s54, 1\n" \
"s_lshl_b32 s55, s55, 14\n" \
"s_and_b32 s54, s54, 1\n" \
"s_lshl_b32 s54, s54, 12\n" \
"s_add_u32 s54, s54, s55\n" \
"s_add_u32 s88, s54, s85\n" \
"s_mul_i32 s54, s48, 7\n" \
"s_add_u32 s54, s54, 3\n" \
"s_lshr_b32 s55, s54, 1\n" \
"s_lshl_b32 s55, s55, 14\n" \
"s_and_b32 s54, s54, 1\n" \
"s_lshl_b32 s54, s54, 12\n" \
"s_add_u32 s54, s54, s55\n" \
"s_add_u32 s89, s54, s85\n" \
"s_mul_i32 s54, s48, 7\n" \
"s_add_u32 s54, s54, 4\n" \
"s_lshr_b32 s55, s54, 1\n" \
"s_lshl_b32 s55, s55, 14\n" \
"s_and_b32 s54, s54, 1\n" \
"s_lshl_b32 s54, s54, 12\n" \
"s_add_u32 s54, s54, s55\n" \
"s_add_u32 s90, s54, s85\n" \
"s_mul_i32 s54, s48, 7\n" \
"s_add_u32 s54, s54, 5\n" \
"s_lshr_b32 s55, s54, 1\n" \
"s_lshl_b32 s55, s55, 14\n" \
"s_and_b32 s54, s54, 1\n" \
"s_lshl_b32 s54, s54, 12\n" \
"s_add_u32 s54, s54, s55\n" \
"s_add_u32 s91, s54, s85\n" \
"s_mul_i32 s54, s48, 7\n" \
"s_add_u32 s54, s54, 6\n" \
"s_lshr_b32 s55, s54, 1\n" \
"s_lshl_b32 s55, s55, 14\n" \
"s_and_b32 s54, s54, 1\n" \
"s_lshl_b32 s54, s54, 12\n" \
"s_add_u32 s54, s54, s55\n" \
"s_add_u32 s92, s54, s85\n" \
"s_mov_b32 s55, 0\n" \
"s_mov_b32 s60, 0\n" \
"s_mov_b32 s79, s95\n" \
"s_mov_b32 s98, 0x70000000\n" \
".Lsg2_prime_%=:\n" \
"s_lshr_b32 s59, s44, 1\n" \
"s_add_u32 s59, s59, 1\n" \
"s_lshl_b32 s56, s44, 18\n" \
"s_lshr_b32 s58, s56, 8\n" \
"s_and_b32 s58, s58, 0x2000\n" \
"s_xor_b32 s56, s56, s58\n" \
"buffer_load_dword v11, v4, s[36:39], 0 offen sc1\n" \
"s_waitcnt vmcnt(0)\n" \
"v_cmp_gt_u32_e64 s[62:63], s59, v11\n" \
"s_nop 4\n" \
"s_cmp_lg_u64 s[62:63], 0\n" \
"s_cbranch_scc1 .Lsg2_w_%=\n" \
".Lsg2_wk_%=:\n" \
"s_mov_b32 s76, 0x7ffffff0\n" \
"s_mov_b32 s81, 1\n" \
"s_mov_b32 s84, 0\n" \
"s_waitcnt vmcnt(0) lgkmcnt(0)\n" \
"s_xor_b32 s57, s56, s86\n" \
"buffer_load_dwordx4 v[16:19], v3, s[36:39], s57 offen offset:0 sc1\n" \
"buffer_load_dwordx4 v[20:23], v3, s[36:39], s57 offen offset:1024 sc1\n" \
"buffer_load_dwordx4 v[24:27], v3, s[36:39], s57 offen offset:2048 sc1\n" \
"buffer_load_dwordx4 v[28:31], v3, s[36:39], s57 offen offset:3072 sc1\n" \
"s_xor_b32 s57, s56, s87\n" \
"buffer_load_dwordx4 v[32:35], v3, s[36:39], s57 offen offset:0 sc1\n" \
"buffer_load_dwordx4 v[36:39], v3, s[36:39], s57 offen offset:1024 sc1\n" \
"buffer_load_dwordx4 v[40:43], v3, s[36:39], s57 offen offset:2048 sc1\n" \
"buffer_load_dwordx4 v[44:47], v3, s[36:39], s57 offen offset:3072 sc1\n" \
"s_xor_b32 s57, s56, s88\n" \
"buffer_load_dwordx4 v[48:51], v3, s[36:39], s57 offen offset:0 sc1\n" \
"buffer_load_dwordx4 v[52:55], v3, s[36:39], s57 offen offset:1024 sc1\n" \
"buffer_load_dwordx4 v[56:59], v3, s[36:39], s57 offen offset:2048 sc1\n" \
"buffer_load_dwordx4 v[60:63], v3, s[36:39], s57 offen offset:3072 sc1\n" \
"s_xor_b32 s57, s56, s89\n" \
"buffer_load_dwordx4 v[64:67], v3, s[36:39], s57 offen offset:0 sc1\n" \
"buffer_load_dwordx4 v[68:71], v3, s[36:39], s57 offen offset:1024 sc1\n" \
"ds_read_b128 v[80:83], v2 offset:0\n" \
"ds_read_b128 v[84:87], v2 offset:1024\n" \
"ds_read_b128 v[88:91], v2 offset:2048\n" \
"s_waitcnt lgkmcnt(0)\n" \
"v_mov_b32 v9, 0x7ffffff0\n" \
"buffer_store_dword v9, v9, s[36:39], 0 offen\n" \
"buffer_store_dword v9, v9, s[36:39], 0 offen\n" \
"s_waitcnt vmcnt(15)\n" \
"s_nop 7\n" \
"s_nop 7\n" \
".p2align 6\n" \
".Lsg2_loop_%=:\n" \
"v_mfma_f32_16x16x32_bf16 v[96:99], a[0:3], v[16:19], 0\n" \
"ds_write_b128 v7, v[104:107] offset:2048\n" \
"v_xor_b32_e64 v7, 64, v7\n" \
"v_mfma_f32_16x16x32_bf16 v[100:103], a[128:131], v[16:19], 0\n" \
"ds_write_b128 v7, v[108:111] offset:3072\n" \
"ds_read_b128 v[92:95], v2 offset:3072\n" \
"v_xor_b32 v7, 192, v7\n" \
"v_mfma_f32_16x16x32_bf16 v[104:107], v[128:131], v[16:19], 0\n" \
"s_waitcnt vmcnt(14) lgkmcnt(7)\n" \
"v_mfma_f32_16x16x32_bf16 v[108:111], v[80:83], v[16:19], 0\n" \
"buffer_load_dwordx4 v[16:19], v3, s[36:39], s57 offen offset:2048 sc1\n" \
"v_mfma_f32_16x16x32_bf16 v[96:99], a[4:7], v[20:23], v[96:99]\n" \
"s_cmp_lt_i32 s44, s50\n" \
"s_cselect_b32 s82, s45, s47\n" \
"v_mfma_f32_16x16x32_bf16 v[100:103], a[132:135], v[20:23], v[100:103]\n" \
"ds_read_b128 v[80:83], v2 offset:4096\n" \
"v_mfma_f32_16x16x32_bf16 v[104:107], v[132:135], v[20:23], v[104:107]\n" \
"s_waitcnt vmcnt(14) lgkmcnt(7)\n" \
"v_mfma_f32_16x16x32_bf16 v[108:111], v[84:87], v[20:23], v[108:111]\n" \
"buffer_load_dwordx4 v[20:23], v3, s[36:39], s57 offen offset:3072 sc1\n" \
"v_mfma_f32_16x16x32_bf16 v[96:99], a[8:11], v[24:27], v[96:99]\n" \
"s_add_u32 s82, s82, s44\n" \
"s_cmp_lt_u32 s82, 128\n" \
"v_mfma_f32_16x16x32_bf16 v[100:103], a[136:139], v[24:27], v[100:103]\n" \
"ds_read_b128 v[84:87], v2 offset:5120\n" \
"s_cselect_b32 s82, s82, s94\n" \
"v_mfma_f32_16x16x32_bf16 v[104:107], v[136:139], v[24:27], v[104:107]\n" \
"s_waitcnt vmcnt(14) lgkmcnt(7)\n" \
"v_mfma_f32_16x16x32_bf16 v[108:111], v[88:91], v[24:27], v[108:111]\n" \
"s_xor_b32 s57, s56, s90\n" \
"buffer_load_dwordx4 v[24:27], v3, s[36:39], s57 offen offset:0 sc1\n" \
"v_mfma_f32_16x16x32_bf16 v[96:99], a[12:15], v[28:31], v[96:99]\n" \
"s_waitcnt lgkmcnt(3)\n" \
"s_cmp_lg_u32 s81, 0\n" \
"s_cbranch_scc1 .Lsg2_nb_%=\n" \
"s_barrier\n" \
".Lsg2_nb_%=:\n" \
"v_mfma_f32_16x16x32_bf16 v[100:103], a[140:143], v[28:31], v[100:103]\n" \
"ds_read_b128 v[88:91], v2 offset:6144\n" \
"v_xor_b32_e64 v7, s84, v7\n" \
"v_mfma_f32_16x16x32_bf16 v[104:107], v[140:143], v[28:31], v[104:107]\n" \
"s_waitcnt vmcnt(14) lgkmcnt(3)\n" \
"s_lshl_b32 s83, s82, 18\n" \
"v_mfma_f32_16x16x32_bf16 v[108:111], v[92:95], v[28:31], v[108:111]\n" \
"buffer_load_dwordx4 v[28:31], v3, s[36:39], s57 offen offset:1024 sc1\n" \
"v_mfma_f32_16x16x32_bf16 v[96:99], a[16:19], v[32:35], v[96:99]\n" \
"ds_read_b128 v[112:115], v8 offset:0\n" \
"s_lshr_b32 s58, s83, 8\n" \
"v_mfma_f32_16x16x32_bf16 v[100:103], a[144:147], v[32:35], v[100:103]\n" \
"ds_read_b128 v[92:95], v2 offset:7168\n" \
"v_mfma_f32_16x16x32_bf16 v[104:107], v[144:147], v[32:35], v[104:107]\n" \
"s_waitcnt vmcnt(14) lgkmcnt(4)\n" \
"s_and_b32 s58, s58, 0x2000\n" \
"v_mfma_f32_16x16x32_bf16 v[108:111], v[80:83], v[32:35], v[108:111]\n" \
"buffer_load_dwordx4 v[32:35], v3, s[36:39], s57 offen offset:2048 sc1\n" \
"v_mfma_f32_16x16x32_bf16 v[96:99], a[20:23], v[36:39], v[96:99]\n" \
"ds_read_b128 v[116:119], v8 offset:4096\n" \
"s_xor_b32 s83, s83, s58\n" \
"v_mfma_f32_16x16x32_bf16 v[100:103], a[148:151], v[36:39], v[100:103]\n" \
"ds_read_b128 v[80:83], v2 offset:8192\n" \
"v_mfma_f32_16x16x32_bf16 v[104:107], v[148:151], v[36:39], v[104:107]\n" \
"s_waitcnt vmcnt(14) lgkmcnt(5)\n" \
"v_mfma_f32_16x16x32_bf16 v[108:111], v[84:87], v[36:39], v[108:111]\n" \
"buffer_load_dwordx4 v[36:39], v3, s[36:39], s57 offen offset:3072 sc1\n" \
"v_mfma_f32_16x16x32_bf16 v[96:99], a[24:27], v[40:43], v[96:99]\n" \
"ds_read_b128 v[120:123], v8 offset:8192\n" \
"v_mfma_f32_16x16x32_bf16 v[100:103], a[152:155], v[40:43], v[100:103]\n" \
"ds_read_b128 v[84:87], v2 offset:9216\n" \
"v_mfma_f32_16x16x32_bf16 v[104:107], v[152:155], v[40:43], v[104:107]\n" \
"s_waitcnt vmcnt(14) lgkmcnt(6)\n" \
"v_mfma_f32_16x16x32_bf16 v[108:111], v[88:91], v[40:43], v[108:111]\n" \
"s_xor_b32 s57, s56, s91\n" \
"buffer_load_dwordx4 v[40:43], v3, s[36:39], s57 offen offset:0 sc1\n" \
"v_mfma_f32_16x16x32_bf16 v[96:99], a[28:31], v[44:47], v[96:99]\n" \
"ds_read_b128 v[124:127], v8 offset:12288\n" \
"v_mfma_f32_16x16x32_bf16 v[100:103], a[156:159], v[44:47], v[100:103]\n" \
"ds_read_b128 v[88:91], v2 offset:10240\n" \
"v_mfma_f32_16x16x32_bf16 v[104:107], v[156:159], v[44:47], v[104:107]\n" \
"s_waitcnt vmcnt(14) lgkmcnt(6)\n" \
"v_mfma_f32_16x16x32_bf16 v[108:111], v[92:95], v[44:47], v[108:111]\n" \
"buffer_load_dwordx4 v[44:47], v3, s[36:39], s57 offen offset:1024 sc1\n" \
"v_mfma_f32_16x16x32_bf16 v[96:99], a[32:35], v[48:51], v[96:99]\n" \
"s_lshr_b32 s59, s82, 1\n" \
"s_add_u32 s59, s59, 1\n" \
"v_mfma_f32_16x16x32_bf16 v[100:103], a[160:163], v[48:51], v[100:103]\n" \
"ds_read_b128 v[92:95], v2 offset:11264\n" \
"v_mfma_f32_16x16x32_bf16 v[104:107], v[160:163], v[48:51], v[104:107]\n" \
"s_waitcnt vmcnt(14) lgkmcnt(5)\n" \
"v_mfma_f32_16x16x32_bf16 v[108:111], v[80:83], v[48:51], v[108:111]\n" \
"buffer_load_dwordx4 v[48:51], v3, s[36:39], s57 offen offset:2048 sc1\n" \
"v_mfma_f32_16x16x32_bf16 v[96:99], a[36:39], v[52:55], v[96:99]\n" \
"v_add_f32_e64 v112, v112, v116\n" \
"v_mfma_f32_16x16x32_bf16 v[100:103], a[164:167], v[52:55], v[100:103]\n" \
"ds_read_b128 v[80:83], v2 offset:12288\n" \
"v_add_f32_e64 v113, v113, v117\n" \
"v_mfma_f32_16x16x32_bf16 v[104:107], v[164:167], v[52:55], v[104:107]\n" \
"s_waitcnt vmcnt(13) lgkmcnt(4)\n" \
"v_add_f32 v114, v114, v118\n" \
"v_mfma_f32_16x16x32_bf16 v[108:111], v[84:87], v[52:55], v[108:111]\n" \
"buffer_load_dwordx4 v[52:55], v3, s[36:39], s57 offen offset:3072 sc1\n" \
"v_add_f32_e64 v115, v115, v119\n" \
"v_mfma_f32_16x16x32_bf16 v[96:99], a[40:43], v[56:59], v[96:99]\n" \
"s_waitcnt lgkmcnt(3)\n" \
"v_add_f32 v120, v120, v124\n" \
"v_mfma_f32_16x16x32_bf16 v[100:103], a[168:171], v[56:59], v[100:103]\n" \
"ds_read_b128 v[84:87], v2 offset:13312\n" \
"v_add_f32_e64 v121, v121, v125\n" \
"v_mfma_f32_16x16x32_bf16 v[104:107], v[168:171], v[56:59], v[104:107]\n" \
"s_waitcnt vmcnt(12) lgkmcnt(3)\n" \
"v_add_f32 v122, v122, v126\n" \
"v_mfma_f32_16x16x32_bf16 v[108:111], v[88:91], v[56:59], v[108:111]\n" \
"s_xor_b32 s57, s56, s92\n" \
"buffer_load_dwordx4 v[56:59], v3, s[36:39], s57 offen offset:0 sc1\n" \
"v_mfma_f32_16x16x32_bf16 v[96:99], a[44:47], v[60:63], v[96:99]\n" \
"v_cmp_gt_u32_e64 s[62:63], s59, v11\n" \
"v_mfma_f32_16x16x32_bf16 v[100:103], a[172:175], v[60:63], v[100:103]\n" \
"ds_read_b128 v[88:91], v2 offset:14336\n" \
"v_add_f32 v123, v123, v127\n" \
"v_mfma_f32_16x16x32_bf16 v[104:107], v[172:175], v[60:63], v[104:107]\n" \
"s_waitcnt vmcnt(12) lgkmcnt(3)\n" \
"v_add_f32 v112, v112, v120\n" \
"v_mfma_f32_16x16x32_bf16 v[108:111], v[92:95], v[60:63], v[108:111]\n" \
"buffer_load_dwordx4 v[60:63], v3, s[36:39], s57 offen offset:1024 sc1\n" \
"v_add_f32_e64 v113, v113, v121\n" \
"v_mfma_f32_16x16x32_bf16 v[96:99], a[48:51], v[64:67], v[96:99]\n" \
"s_add_u32 s54, s55, 1\n" \
"s_cmp_lt_u32 s54, s79\n" \
"v_mfma_f32_16x16x32_bf16 v[100:103], a[176:179], v[64:67], v[100:103]\n" \
"ds_read_b128 v[92:95], v2 offset:15360\n" \
"s_cselect_b64 s[62:63], s[62:63], -1\n" \
"v_mfma_f32_16x16x32_bf16 v[104:107], v[176:179], v[64:67], v[104:107]\n" \
"s_waitcnt vmcnt(12) lgkmcnt(3)\n" \
"v_add_f32_e64 v114, v114, v122\n" \
"v_mfma_f32_16x16x32_bf16 v[108:111], v[80:83], v[64:67], v[108:111]\n" \
"buffer_load_dwordx4 v[64:67], v3, s[36:39], s57 offen offset:2048 sc1\n" \
"v_add_f32_e64 v115, v115, v123\n" \
"v_mfma_f32_16x16x32_bf16 v[96:99], a[52:55], v[68:71], v[96:99]\n" \
"s_cmp_lg_u64 s[62:63], 0\n" \
"s_cselect_b32 s81, 1, 0\n" \
"v_mfma_f32_16x16x32_bf16 v[100:103], a[180:183], v[68:71], v[100:103]\n" \
"ds_read_b128 v[80:83], v2 offset:16384\n" \
"s_cselect_b32 s83, 0x70000000, s83\n" \
"v_mfma_f32_16x16x32_bf16 v[104:107], v[180:183], v[68:71], v[104:107]\n" \
"s_waitcnt vmcnt(12) lgkmcnt(3)\n" \
"v_xor_b32 v8, s84, v8\n" \
"v_mfma_f32_16x16x32_bf16 v[108:111], v[84:87], v[68:71], v[108:111]\n" \
"buffer_load_dwordx4 v[68:71], v3, s[36:39], s57 offen offset:3072 sc1\n" \
"v_mul_f32 v116, 0xbfb8aa3b, v112\n" \
"v_mfma_f32_16x16x32_bf16 v[96:99], a[56:59], v[16:19], v[96:99]\n" \
"v_mul_f32 v117, 0xbfb8aa3b, v113\n" \
"v_mfma_f32_16x16x32_bf16 v[100:103], a[184:187], v[16:19], v[100:103]\n" \
"ds_read_b128 v[84:87], v2 offset:17408\n" \
"v_mul_f32 v118, 0xbfb8aa3b, v114\n" \
"v_mfma_f32_16x16x32_bf16 v[104:107], v[184:187], v[16:19], v[104:107]\n" \
"s_waitcnt vmcnt(12) lgkmcnt(3)\n" \
"s_mov_b32 s56, s83\n" \
"v_mfma_f32_16x16x32_bf16 v[108:111], v[88:91], v[16:19], v[108:111]\n" \
"s_xor_b32 s57, s56, s86\n" \
"buffer_load_dwordx4 v[16:19], v3, s[36:39], s57 offen offset:0 sc1\n" \
"v_mfma_f32_16x16x32_bf16 v[96:99], a[60:63], v[20:23], v[96:99]\n" \
"v_mul_f32 v119, 0xbfb8aa3b, v115\n" \
"v_mfma_f32_16x16x32_bf16 v[100:103], a[188:191], v[20:23], v[100:103]\n" \
"ds_read_b128 v[88:91], v2 offset:18432\n" \
"v_mfma_f32_16x16x32_bf16 v[104:107], v[188:191], v[20:23], v[104:107]\n" \
"s_waitcnt vmcnt(12) lgkmcnt(3)\n" \
"v_exp_f32_e64 v116, v116\n" \
"v_mfma_f32_16x16x32_bf16 v[108:111], v[92:95], v[20:23], v[108:111]\n" \
"buffer_load_dwordx4 v[20:23], v3, s[36:39], s57 offen offset:1024 sc1\n" \
"v_mfma_f32_16x16x32_bf16 v[96:99], a[64:67], v[24:27], v[96:99]\n" \
"v_exp_f32_e64 v117, v117\n" \
"v_mfma_f32_16x16x32_bf16 v[100:103], a[192:195], v[24:27], v[100:103]\n" \
"ds_read_b128 v[92:95], v2 offset:19456\n" \
"v_mfma_f32_16x16x32_bf16 v[104:107], v[192:195], v[24:27], v[104:107]\n" \
"s_waitcnt vmcnt(12) lgkmcnt(3)\n" \
"v_exp_f32 v118, v118\n" \
"v_mfma_f32_16x16x32_bf16 v[108:111], v[80:83], v[24:27], v[108:111]\n" \
"buffer_load_dwordx4 v[24:27], v3, s[36:39], s57 offen offset:2048 sc1\n" \
"v_mfma_f32_16x16x32_bf16 v[96:99], a[68:71], v[28:31], v[96:99]\n" \
"v_exp_f32_e64 v119, v119\n" \
"v_mfma_f32_16x16x32_bf16 v[100:103], a[196:199], v[28:31], v[100:103]\n" \
"ds_read_b128 v[80:83], v2 offset:20480\n" \
"v_add_f32_e64 v116, 1.0, v116\n" \
"v_mfma_f32_16x16x32_bf16 v[104:107], v[196:199], v[28:31], v[104:107]\n" \
"s_waitcnt vmcnt(12) lgkmcnt(3)\n" \
"v_add_f32 v117, 1.0, v117\n" \
"v_mfma_f32_16x16x32_bf16 v[108:111], v[84:87], v[28:31], v[108:111]\n" \
"buffer_load_dwordx4 v[28:31], v3, s[36:39], s57 offen offset:3072 sc1\n" \
"v_add_f32_e64 v118, 1.0, v118\n" \
"v_mfma_f32_16x16x32_bf16 v[96:99], a[72:75], v[32:35], v[96:99]\n" \
"v_add_f32_e64 v119, 1.0, v119\n" \
"v_mfma_f32_16x16x32_bf16 v[100:103], a[200:203], v[32:35], v[100:103]\n" \
"ds_read_b128 v[84:87], v2 offset:21504\n" \
"v_mfma_f32_16x16x32_bf16 v[104:107], v[200:203], v[32:35], v[104:107]\n" \
"s_waitcnt vmcnt(12) lgkmcnt(3)\n" \
"v_rcp_f32 v116, v116\n" \
"v_mfma_f32_16x16x32_bf16 v[108:111], v[88:91], v[32:35], v[108:111]\n" \
"s_xor_b32 s57, s56, s87\n" \
"buffer_load_dwordx4 v[32:35], v3, s[36:39], s57 offen offset:0 sc1\n" \
"v_mfma_f32_16x16x32_bf16 v[96:99], a[76:79], v[36:39], v[96:99]\n" \
"v_rcp_f32 v117, v117\n" \
"v_mfma_f32_16x16x32_bf16 v[100:103], a[204:207], v[36:39], v[100:103]\n" \
"ds_read_b128 v[88:91], v2 offset:22528\n" \
"v_mfma_f32_16x16x32_bf16 v[104:107], v[204:207], v[36:39], v[104:107]\n" \
"s_waitcnt vmcnt(12) lgkmcnt(3)\n" \
"v_rcp_f32 v118, v118\n" \
"v_mfma_f32_16x16x32_bf16 v[108:111], v[92:95], v[36:39], v[108:111]\n" \
"buffer_load_dwordx4 v[36:39], v3, s[36:39], s57 offen offset:1024 sc1\n" \
"v_mfma_f32_16x16x32_bf16 v[96:99], a[80:83], v[40:43], v[96:99]\n" \
"v_rcp_f32_e64 v119, v119\n" \
"v_mfma_f32_16x16x32_bf16 v[100:103], a[208:211], v[40:43], v[100:103]\n" \
"ds_read_b128 v[92:95], v2 offset:23552\n" \
"v_mul_f32_e64 v112, v112, v116\n" \
"v_mfma_f32_16x16x32_bf16 v[104:107], v[208:211], v[40:43], v[104:107]\n" \
"s_waitcnt vmcnt(12) lgkmcnt(3)\n" \
"v_mul_f32 v113, v113, v117\n" \
"v_mfma_f32_16x16x32_bf16 v[108:111], v[80:83], v[40:43], v[108:111]\n" \
"buffer_load_dwordx4 v[40:43], v3, s[36:39], s57 offen offset:2048 sc1\n" \
"v_mul_f32_e64 v114, v114, v118\n" \
"v_mfma_f32_16x16x32_bf16 v[96:99], a[84:87], v[44:47], v[96:99]\n" \
"s_lshl_b32 s80, s44, 16\n" \
"v_mul_f32 v115, v115, v119\n" \
"v_mfma_f32_16x16x32_bf16 v[100:103], a[212:215], v[44:47], v[100:103]\n" \
"ds_read_b128 v[80:83], v2 offset:24576\n" \
"v_cvt_pk_bf16_f32 v0, v112, v113\n" \
"v_mfma_f32_16x16x32_bf16 v[104:107], v[212:215], v[44:47], v[104:107]\n" \
"s_waitcnt vmcnt(12) lgkmcnt(3)\n" \
"s_add_u32 s80, s80, s78\n" \
"v_mfma_f32_16x16x32_bf16 v[108:111], v[84:87], v[44:47], v[108:111]\n" \
"buffer_load_dwordx4 v[44:47], v3, s[36:39], s57 offen offset:3072 sc1\n" \
"v_cvt_pk_bf16_f32 v1, v114, v115\n" \
"v_mfma_f32_16x16x32_bf16 v[96:99], a[88:91], v[48:51], v[96:99]\n" \
"s_lshr_b32 s58, s80, 8\n" \
"v_mfma_f32_16x16x32_bf16 v[100:103], a[216:219], v[48:51], v[100:103]\n" \
"ds_read_b128 v[84:87], v2 offset:25600\n" \
"v_mfma_f32_16x16x32_bf16 v[104:107], v[216:219], v[48:51], v[104:107]\n" \
"s_waitcnt vmcnt(12) lgkmcnt(3)\n" \
"s_and_b32 s58, s58, 0x2000\n" \
"v_mfma_f32_16x16x32_bf16 v[108:111], v[88:91], v[48:51], v[108:111]\n" \
"s_xor_b32 s57, s56, s88\n" \
"buffer_load_dwordx4 v[48:51], v3, s[36:39], s57 offen offset:0 sc1\n" \
"v_mfma_f32_16x16x32_bf16 v[96:99], a[92:95], v[52:55], v[96:99]\n" \
"s_xor_b32 s80, s80, s58\n" \
"v_mfma_f32_16x16x32_bf16 v[100:103], a[220:223], v[52:55], v[100:103]\n" \
"ds_read_b128 v[88:91], v2 offset:26624\n" \
"v_mfma_f32_16x16x32_bf16 v[104:107], v[220:223], v[52:55], v[104:107]\n" \
"s_waitcnt vmcnt(12) lgkmcnt(3)\n" \
"s_add_u32 s80, s80, 8388608\n" \
"v_mfma_f32_16x16x32_bf16 v[108:111], v[92:95], v[52:55], v[108:111]\n" \
"buffer_load_dwordx4 v[52:55], v3, s[36:39], s57 offen offset:1024 sc1\n" \
"v_mfma_f32_16x16x32_bf16 v[96:99], a[96:99], v[56:59], v[96:99]\n" \
"buffer_store_dwordx2 v[0:1], v6, s[36:39], s76 offen sc1\n" \
"v_mfma_f32_16x16x32_bf16 v[100:103], a[224:227], v[56:59], v[100:103]\n" \
"ds_read_b128 v[92:95], v2 offset:28672\n" \
"v_mfma_f32_16x16x32_bf16 v[104:107], v[224:227], v[56:59], v[104:107]\n" \
"s_waitcnt vmcnt(13) lgkmcnt(3)\n" \
"v_mfma_f32_16x16x32_bf16 v[108:111], v[80:83], v[56:59], v[108:111]\n" \
"buffer_load_dwordx4 v[56:59], v3, s[36:39], s57 offen offset:2048 sc1\n" \
"v_mfma_f32_16x16x32_bf16 v[96:99], a[100:103], v[60:63], v[96:99]\n" \
"buffer_load_dword v11, v4, s[36:39], 0 offen sc1\n" \
"v_mfma_f32_16x16x32_bf16 v[100:103], a[228:231], v[60:63], v[100:103]\n" \
"ds_read_b128 v[80:83], v2 offset:0\n" \
"v_mfma_f32_16x16x32_bf16 v[104:107], v[228:231], v[60:63], v[104:107]\n" \
"s_waitcnt vmcnt(14) lgkmcnt(3)\n" \
"v_mfma_f32_16x16x32_bf16 v[108:111], v[84:87], v[60:63], v[108:111]\n" \
"buffer_load_dwordx4 v[60:63], v3, s[36:39], s57 offen offset:3072 sc1\n" \
"v_mfma_f32_16x16x32_bf16 v[96:99], a[104:107], v[64:67], v[96:99]\n" \
"s_add_u32 s55, s55, 1\n" \
"s_mov_b32 s84, 0x4000\n" \
"v_mfma_f32_16x16x32_bf16 v[100:103], a[232:235], v[64:67], v[100:103]\n" \
"ds_read_b128 v[84:87], v2 offset:1024\n" \
"v_mfma_f32_16x16x32_bf16 v[104:107], v[232:235], v[64:67], v[104:107]\n" \
"s_waitcnt vmcnt(14) lgkmcnt(3)\n" \
"v_mfma_f32_16x16x32_bf16 v[108:111], v[88:91], v[64:67], v[108:111]\n" \
"s_xor_b32 s57, s56, s89\n" \
"buffer_load_dwordx4 v[64:67], v3, s[36:39], s57 offen offset:0 sc1\n" \
"v_mfma_f32_16x16x32_bf16 v[96:99], a[108:111], v[68:71], v[96:99]\n" \
"s_mov_b32 s76, s80\n" \
"v_mfma_f32_16x16x32_bf16 v[100:103], a[236:239], v[68:71], v[100:103]\n" \
"ds_read_b128 v[88:91], v2 offset:2048\n" \
"s_mov_b32 s44, s82\n" \
"v_mfma_f32_16x16x32_bf16 v[104:107], v[236:239], v[68:71], v[104:107]\n" \
"s_waitcnt vmcnt(14) lgkmcnt(3)\n" \
"ds_write_b128 v7, v[96:99] offset:0\n" \
"v_xor_b32 v7, 64, v7\n" \
"v_mfma_f32_16x16x32_bf16 v[108:111], v[92:95], v[68:71], v[108:111]\n" \
"buffer_load_dwordx4 v[68:71], v3, s[36:39], s57 offen offset:1024 sc1\n" \
"ds_write_b128 v7, v[100:103] offset:1024\n" \
"v_xor_b32 v7, 192, v7\n" \
"s_cmp_eq_u32 s81, 0\n" \
"s_cbranch_scc1 .Lsg2_loop_%=\n" \
"s_nop 7\n" \
"s_nop 7\n" \
"s_waitcnt vmcnt(0) lgkmcnt(0)\n" \
"ds_write_b128 v7, v[104:107] offset:2048\n" \
"v_xor_b32 v7, 64, v7\n" \
"ds_write_b128 v7, v[108:111] offset:3072\n" \
"v_xor_b32 v7, 64, v7\n" \
"s_waitcnt lgkmcnt(0)\n" \
"s_barrier\n" \
"ds_read_b128 v[112:115], v8 offset:0\n" \
"ds_read_b128 v[116:119], v8 offset:4096\n" \
"ds_read_b128 v[120:123], v8 offset:8192\n" \
"ds_read_b128 v[124:127], v8 offset:12288\n" \
"s_waitcnt lgkmcnt(2)\n" \
"v_add_f32 v112, v112, v116\n" \
"v_add_f32 v113, v113, v117\n" \
"v_add_f32 v114, v114, v118\n" \
"v_add_f32 v115, v115, v119\n" \
"s_waitcnt lgkmcnt(0)\n" \
"v_add_f32 v120, v120, v124\n" \
"v_add_f32 v121, v121, v125\n" \
"v_add_f32 v122, v122, v126\n" \
"v_add_f32 v123, v123, v127\n" \
"v_add_f32 v112, v112, v120\n" \
"v_add_f32 v113, v113, v121\n" \
"v_add_f32 v114, v114, v122\n" \
"v_add_f32 v115, v115, v123\n" \
"v_xor_b32 v8, s84, v8\n" \
"v_mul_f32 v116, 0xbfb8aa3b, v112\n" \
"v_mul_f32 v117, 0xbfb8aa3b, v113\n" \
"v_mul_f32 v118, 0xbfb8aa3b, v114\n" \
"v_mul_f32 v119, 0xbfb8aa3b, v115\n" \
"v_exp_f32 v116, v116\n" \
"v_exp_f32 v117, v117\n" \
"v_exp_f32 v118, v118\n" \
"v_exp_f32 v119, v119\n" \
"v_add_f32 v116, 1.0, v116\n" \
"v_add_f32 v117, 1.0, v117\n" \
"v_add_f32 v118, 1.0, v118\n" \
"v_add_f32 v119, 1.0, v119\n" \
"v_rcp_f32 v116, v116\n" \
"v_rcp_f32 v117, v117\n" \
"v_rcp_f32 v118, v118\n" \
"v_rcp_f32 v119, v119\n" \
"v_mul_f32 v112, v112, v116\n" \
"v_mul_f32 v113, v113, v117\n" \
"v_mul_f32 v114, v114, v118\n" \
"v_mul_f32 v115, v115, v119\n" \
"v_cvt_pk_bf16_f32 v0, v112, v113\n" \
"v_cvt_pk_bf16_f32 v1, v114, v115\n" \
"v_xor_b32 v7, 0x4000, v7\n" \
"buffer_store_dwordx2 v[0:1], v6, s[36:39], s76 offen sc1\n" \
"s_cmp_lg_u32 s55, s95\n" \
"s_cbranch_scc1 .Lsg2_g_%=\n" \
"s_waitcnt vmcnt(0) lgkmcnt(0)\n" \
"v_mov_b32 v9, 1\n" \
"s_mov_b64 exec, 1\n" \
"buffer_atomic_add v9, off, s[36:39], s49 offset:64\n" \
"s_mov_b64 exec, -1\n" \
"s_mov_b32 s79, s46\n" \
".Lsg2_g_%=:\n" \
"s_cmp_lt_u32 s55, s46\n" \
"s_cbranch_scc1 .Lsg2_prime_%=\n" \
"s_branch .Lsg2_o_%=\n" \
".Lsg2_ok_%=:\n" \
"s_mov_b32 s44, s94\n" \
".Lsg2_c_%=:\n" \
"s_nop 7\n" \
"s_nop 7\n" \
"s_lshl_b32 s54, s44, 16\n" \
"s_add_u32 s54, s54, s78\n" \
"s_lshr_b32 s58, s54, 8\n" \
"s_and_b32 s58, s58, 0x2000\n" \
"s_xor_b32 s54, s54, s58\n" \
"s_lshl_b32 s80, s44, 15\n" \
"s_cmp_lt_u32 s44, s96\n" \
"s_cselect_b32 s80, s80, 0x70000000\n" \
"s_cselect_b32 s54, s54, 0x70000000\n" \
"s_add_u32 s55, s54, 8388608\n" \
"s_add_u32 s44, s44, 6\n" \
"buffer_load_dwordx2 v[128:129], v6, s[36:39], s54 offen sc1\n" \
"buffer_load_dwordx2 v[130:131], v6, s[36:39], s55 offen sc1\n" \
"buffer_load_dwordx2 v[132:133], v5, s[64:67], s80 offen\n" \
"s_lshl_b32 s54, s44, 16\n" \
"s_add_u32 s54, s54, s78\n" \
"s_lshr_b32 s58, s54, 8\n" \
"s_and_b32 s58, s58, 0x2000\n" \
"s_xor_b32 s54, s54, s58\n" \
"s_lshl_b32 s81, s44, 15\n" \
"s_cmp_lt_u32 s44, s96\n" \
"s_cselect_b32 s81, s81, 0x70000000\n" \
"s_cselect_b32 s54, s54, 0x70000000\n" \
"s_add_u32 s55, s54, 8388608\n" \
"s_add_u32 s44, s44, 6\n" \
"buffer_load_dwordx2 v[134:135], v6, s[36:39], s54 offen sc1\n" \
"buffer_load_dwordx2 v[136:137], v6, s[36:39], s55 offen sc1\n" \
"buffer_load_dwordx2 v[138:139], v5, s[64:67], s81 offen\n" \
"s_lshl_b32 s54, s44, 16\n" \
"s_add_u32 s54, s54, s78\n" \
"s_lshr_b32 s58, s54, 8\n" \
"s_and_b32 s58, s58, 0x2000\n" \
"s_xor_b32 s54, s54, s58\n" \
"s_lshl_b32 s82, s44, 15\n" \
"s_cmp_lt_u32 s44, s96\n" \
"s_cselect_b32 s82, s82, 0x70000000\n" \
"s_cselect_b32 s54, s54, 0x70000000\n" \
"s_add_u32 s55, s54, 8388608\n" \
"s_add_u32 s44, s44, 6\n" \
"buffer_load_dwordx2 v[140:141], v6, s[36:39], s54 offen sc1\n" \
"buffer_load_dwordx2 v[142:143], v6, s[36:39], s55 offen sc1\n" \
"buffer_load_dwordx2 v[144:145], v5, s[64:67], s82 offen\n" \
"s_lshl_b32 s54, s44, 16\n" \
"s_add_u32 s54, s54, s78\n" \
"s_lshr_b32 s58, s54, 8\n" \
"s_and_b32 s58, s58, 0x2000\n" \
"s_xor_b32 s54, s54, s58\n" \
"s_lshl_b32 s83, s44, 15\n" \
"s_cmp_lt_u32 s44, s96\n" \
"s_cselect_b32 s83, s83, 0x70000000\n" \
"s_cselect_b32 s54, s54, 0x70000000\n" \
"s_add_u32 s55, s54, 8388608\n" \
"s_add_u32 s44, s44, 6\n" \
"buffer_load_dwordx2 v[146:147], v6, s[36:39], s54 offen sc1\n" \
"buffer_load_dwordx2 v[148:149], v6, s[36:39], s55 offen sc1\n" \
"buffer_load_dwordx2 v[150:151], v5, s[64:67], s83 offen\n" \
"s_lshl_b32 s54, s44, 16\n" \
"s_add_u32 s54, s54, s78\n" \
"s_lshr_b32 s58, s54, 8\n" \
"s_and_b32 s58, s58, 0x2000\n" \
"s_xor_b32 s54, s54, s58\n" \
"s_lshl_b32 s84, s44, 15\n" \
"s_cmp_lt_u32 s44, s96\n" \
"s_cselect_b32 s84, s84, 0x70000000\n" \
"s_cselect_b32 s54, s54, 0x70000000\n" \
"s_add_u32 s55, s54, 8388608\n" \
"s_add_u32 s44, s44, 6\n" \
"buffer_load_dwordx2 v[152:153], v6, s[36:39], s54 offen sc1\n" \
"buffer_load_dwordx2 v[154:155], v6, s[36:39], s55 offen sc1\n" \
"buffer_load_dwordx2 v[156:157], v5, s[64:67], s84 offen\n" \
"s_lshl_b32 s54, s44, 16\n" \
"s_add_u32 s54, s54, s78\n" \
"s_lshr_b32 s58, s54, 8\n" \
"s_and_b32 s58, s58, 0x2000\n" \
"s_xor_b32 s54, s54, s58\n" \
"s_lshl_b32 s85, s44, 15\n" \
"s_cmp_lt_u32 s44, s96\n" \
"s_cselect_b32 s85, s85, 0x70000000\n" \
"s_cselect_b32 s54, s54, 0x70000000\n" \
"s_add_u32 s55, s54, 8388608\n" \
"s_add_u32 s44, s44, 6\n" \
"buffer_load_dwordx2 v[158:159], v6, s[36:39], s54 offen sc1\n" \
"buffer_load_dwordx2 v[160:161], v6, s[36:39], s55 offen sc1\n" \
"buffer_load_dwordx2 v[162:163], v5, s[64:67], s85 offen\n" \
"s_lshl_b32 s54, s44, 16\n" \
"s_add_u32 s54, s54, s78\n" \
"s_lshr_b32 s58, s54, 8\n" \
"s_and_b32 s58, s58, 0x2000\n" \
"s_xor_b32 s54, s54, s58\n" \
"s_lshl_b32 s86, s44, 15\n" \
"s_cmp_lt_u32 s44, s96\n" \
"s_cselect_b32 s86, s86, 0x70000000\n" \
"s_cselect_b32 s54, s54, 0x70000000\n" \
"s_add_u32 s55, s54, 8388608\n" \
"s_add_u32 s44, s44, 6\n" \
"buffer_load_dwordx2 v[164:165], v6, s[36:39], s54 offen sc1\n" \
"buffer_load_dwordx2 v[166:167], v6, s[36:39], s55 offen sc1\n" \
"buffer_load_dwordx2 v[168:169], v5, s[64:67], s86 offen\n" \
"s_lshl_b32 s54, s44, 16\n" \
"s_add_u32 s54, s54, s78\n" \
"s_lshr_b32 s58, s54, 8\n" \
"s_and_b32 s58, s58, 0x2000\n" \
"s_xor_b32 s54, s54, s58\n" \
"s_lshl_b32 s87, s44, 15\n" \
"s_cmp_lt_u32 s44, s96\n" \
"s_cselect_b32 s87, s87, 0x70000000\n" \
"s_cselect_b32 s54, s54, 0x70000000\n" \
"s_add_u32 s55, s54, 8388608\n" \
"s_add_u32 s44, s44, 6\n" \
"buffer_load_dwordx2 v[170:171], v6, s[36:39], s54 offen sc1\n" \
"buffer_load_dwordx2 v[172:173], v6, s[36:39], s55 offen sc1\n" \
"buffer_load_dwordx2 v[174:175], v5, s[64:67], s87 offen\n" \
"s_lshl_b32 s54, s44, 16\n" \
"s_add_u32 s54, s54, s78\n" \
"s_lshr_b32 s58, s54, 8\n" \
"s_and_b32 s58, s58, 0x2000\n" \
"s_xor_b32 s54, s54, s58\n" \
"s_lshl_b32 s88, s44, 15\n" \
"s_cmp_lt_u32 s44, s96\n" \
"s_cselect_b32 s88, s88, 0x70000000\n" \
"s_cselect_b32 s54, s54, 0x70000000\n" \
"s_add_u32 s55, s54, 8388608\n" \
"s_add_u32 s44, s44, 6\n" \
"buffer_load_dwordx2 v[176:177], v6, s[36:39], s54 offen sc1\n" \
"buffer_load_dwordx2 v[178:179], v6, s[36:39], s55 offen sc1\n" \
"buffer_load_dwordx2 v[180:181], v5, s[64:67], s88 offen\n" \
"s_lshl_b32 s54, s44, 16\n" \
"s_add_u32 s54, s54, s78\n" \
"s_lshr_b32 s58, s54, 8\n" \
"s_and_b32 s58, s58, 0x2000\n" \
"s_xor_b32 s54, s54, s58\n" \
"s_lshl_b32 s89, s44, 15\n" \
"s_cmp_lt_u32 s44, s96\n" \
"s_cselect_b32 s89, s89, 0x70000000\n" \
"s_cselect_b32 s54, s54, 0x70000000\n" \
"s_add_u32 s55, s54, 8388608\n" \
"s_add_u32 s44, s44, 6\n" \
"buffer_load_dwordx2 v[182:183], v6, s[36:39], s54 offen sc1\n" \
"buffer_load_dwordx2 v[184:185], v6, s[36:39], s55 offen sc1\n" \
"buffer_load_dwordx2 v[186:187], v5, s[64:67], s89 offen\n" \
"s_waitcnt vmcnt(29)\n" \
"v_lshlrev_b32 v112, 16, v128\n" \
"v_and_b32 v113, 0xffff0000, v128\n" \
"v_lshlrev_b32 v114, 16, v129\n" \
"v_and_b32 v115, 0xffff0000, v129\n" \
"s_waitcnt vmcnt(28)\n" \
"v_lshlrev_b32 v116, 16, v130\n" \
"v_and_b32 v117, 0xffff0000, v130\n" \
"v_lshlrev_b32 v118, 16, v131\n" \
"v_and_b32 v119, 0xffff0000, v131\n" \
"s_waitcnt vmcnt(27)\n" \
"v_lshlrev_b32 v120, 16, v132\n" \
"v_and_b32 v121, 0xffff0000, v132\n" \
"v_lshlrev_b32 v122, 16, v133\n" \
"v_and_b32 v123, 0xffff0000, v133\n" \
"v_fmac_f32 v120, v112, v116\n" \
"v_fmac_f32 v121, v113, v117\n" \
"v_fmac_f32 v122, v114, v118\n" \
"v_fmac_f32 v123, v115, v119\n" \
"v_cvt_pk_bf16_f32 v132, v120, v121\n" \
"v_cvt_pk_bf16_f32 v133, v122, v123\n" \
"buffer_store_dwordx2 v[132:133], v5, s[68:71], s80 offen\n" \
"s_waitcnt vmcnt(27)\n" \
"v_lshlrev_b32 v112, 16, v134\n" \
"v_and_b32 v113, 0xffff0000, v134\n" \
"v_lshlrev_b32 v114, 16, v135\n" \
"v_and_b32 v115, 0xffff0000, v135\n" \
"s_waitcnt vmcnt(26)\n" \
"v_lshlrev_b32 v116, 16, v136\n" \
"v_and_b32 v117, 0xffff0000, v136\n" \
"v_lshlrev_b32 v118, 16, v137\n" \
"v_and_b32 v119, 0xffff0000, v137\n" \
"s_waitcnt vmcnt(25)\n" \
"v_lshlrev_b32 v120, 16, v138\n" \
"v_and_b32 v121, 0xffff0000, v138\n" \
"v_lshlrev_b32 v122, 16, v139\n" \
"v_and_b32 v123, 0xffff0000, v139\n" \
"v_fmac_f32 v120, v112, v116\n" \
"v_fmac_f32 v121, v113, v117\n" \
"v_fmac_f32 v122, v114, v118\n" \
"v_fmac_f32 v123, v115, v119\n" \
"v_cvt_pk_bf16_f32 v138, v120, v121\n" \
"v_cvt_pk_bf16_f32 v139, v122, v123\n" \
"buffer_store_dwordx2 v[138:139], v5, s[68:71], s81 offen\n" \
"s_waitcnt vmcnt(25)\n" \
"v_lshlrev_b32 v112, 16, v140\n" \
"v_and_b32 v113, 0xffff0000, v140\n" \
"v_lshlrev_b32 v114, 16, v141\n" \
"v_and_b32 v115, 0xffff0000, v141\n" \
"s_waitcnt vmcnt(24)\n" \
"v_lshlrev_b32 v116, 16, v142\n" \
"v_and_b32 v117, 0xffff0000, v142\n" \
"v_lshlrev_b32 v118, 16, v143\n" \
"v_and_b32 v119, 0xffff0000, v143\n" \
"s_waitcnt vmcnt(23)\n" \
"v_lshlrev_b32 v120, 16, v144\n" \
"v_and_b32 v121, 0xffff0000, v144\n" \
"v_lshlrev_b32 v122, 16, v145\n" \
"v_and_b32 v123, 0xffff0000, v145\n" \
"v_fmac_f32 v120, v112, v116\n" \
"v_fmac_f32 v121, v113, v117\n" \
"v_fmac_f32 v122, v114, v118\n" \
"v_fmac_f32 v123, v115, v119\n" \
"v_cvt_pk_bf16_f32 v144, v120, v121\n" \
"v_cvt_pk_bf16_f32 v145, v122, v123\n" \
"buffer_store_dwordx2 v[144:145], v5, s[68:71], s82 offen\n" \
"s_waitcnt vmcnt(23)\n" \
"v_lshlrev_b32 v112, 16, v146\n" \
"v_and_b32 v113, 0xffff0000, v146\n" \
"v_lshlrev_b32 v114, 16, v147\n" \
"v_and_b32 v115, 0xffff0000, v147\n" \
"s_waitcnt vmcnt(22)\n" \
"v_lshlrev_b32 v116, 16, v148\n" \
"v_and_b32 v117, 0xffff0000, v148\n" \
"v_lshlrev_b32 v118, 16, v149\n" \
"v_and_b32 v119, 0xffff0000, v149\n" \
"s_waitcnt vmcnt(21)\n" \
"v_lshlrev_b32 v120, 16, v150\n" \
"v_and_b32 v121, 0xffff0000, v150\n" \
"v_lshlrev_b32 v122, 16, v151\n" \
"v_and_b32 v123, 0xffff0000, v151\n" \
"v_fmac_f32 v120, v112, v116\n" \
"v_fmac_f32 v121, v113, v117\n" \
"v_fmac_f32 v122, v114, v118\n" \
"v_fmac_f32 v123, v115, v119\n" \
"v_cvt_pk_bf16_f32 v150, v120, v121\n" \
"v_cvt_pk_bf16_f32 v151, v122, v123\n" \
"buffer_store_dwordx2 v[150:151], v5, s[68:71], s83 offen\n" \
"s_waitcnt vmcnt(21)\n" \
"v_lshlrev_b32 v112, 16, v152\n" \
"v_and_b32 v113, 0xffff0000, v152\n" \
"v_lshlrev_b32 v114, 16, v153\n" \
"v_and_b32 v115, 0xffff0000, v153\n" \
"s_waitcnt vmcnt(20)\n" \
"v_lshlrev_b32 v116, 16, v154\n" \
"v_and_b32 v117, 0xffff0000, v154\n" \
"v_lshlrev_b32 v118, 16, v155\n" \
"v_and_b32 v119, 0xffff0000, v155\n" \
"s_waitcnt vmcnt(19)\n" \
"v_lshlrev_b32 v120, 16, v156\n" \
"v_and_b32 v121, 0xffff0000, v156\n" \
"v_lshlrev_b32 v122, 16, v157\n" \
"v_and_b32 v123, 0xffff0000, v157\n" \
"v_fmac_f32 v120, v112, v116\n" \
"v_fmac_f32 v121, v113, v117\n" \
"v_fmac_f32 v122, v114, v118\n" \
"v_fmac_f32 v123, v115, v119\n" \
"v_cvt_pk_bf16_f32 v156, v120, v121\n" \
"v_cvt_pk_bf16_f32 v157, v122, v123\n" \
"buffer_store_dwordx2 v[156:157], v5, s[68:71], s84 offen\n" \
"s_waitcnt vmcnt(19)\n" \
"v_lshlrev_b32 v112, 16, v158\n" \
"v_and_b32 v113, 0xffff0000, v158\n" \
"v_lshlrev_b32 v114, 16, v159\n" \
"v_and_b32 v115, 0xffff0000, v159\n" \
"s_waitcnt vmcnt(18)\n" \
"v_lshlrev_b32 v116, 16, v160\n" \
"v_and_b32 v117, 0xffff0000, v160\n" \
"v_lshlrev_b32 v118, 16, v161\n" \
"v_and_b32 v119, 0xffff0000, v161\n" \
"s_waitcnt vmcnt(17)\n" \
"v_lshlrev_b32 v120, 16, v162\n" \
"v_and_b32 v121, 0xffff0000, v162\n" \
"v_lshlrev_b32 v122, 16, v163\n" \
"v_and_b32 v123, 0xffff0000, v163\n" \
"v_fmac_f32 v120, v112, v116\n" \
"v_fmac_f32 v121, v113, v117\n" \
"v_fmac_f32 v122, v114, v118\n" \
"v_fmac_f32 v123, v115, v119\n" \
"v_cvt_pk_bf16_f32 v162, v120, v121\n" \
"v_cvt_pk_bf16_f32 v163, v122, v123\n" \
"buffer_store_dwordx2 v[162:163], v5, s[68:71], s85 offen\n" \
"s_waitcnt vmcnt(17)\n" \
"v_lshlrev_b32 v112, 16, v164\n" \
"v_and_b32 v113, 0xffff0000, v164\n" \
"v_lshlrev_b32 v114, 16, v165\n" \
"v_and_b32 v115, 0xffff0000, v165\n" \
"s_waitcnt vmcnt(16)\n" \
"v_lshlrev_b32 v116, 16, v166\n" \
"v_and_b32 v117, 0xffff0000, v166\n" \
"v_lshlrev_b32 v118, 16, v167\n" \
"v_and_b32 v119, 0xffff0000, v167\n" \
"s_waitcnt vmcnt(15)\n" \
"v_lshlrev_b32 v120, 16, v168\n" \
"v_and_b32 v121, 0xffff0000, v168\n" \
"v_lshlrev_b32 v122, 16, v169\n" \
"v_and_b32 v123, 0xffff0000, v169\n" \
"v_fmac_f32 v120, v112, v116\n" \
"v_fmac_f32 v121, v113, v117\n" \
"v_fmac_f32 v122, v114, v118\n" \
"v_fmac_f32 v123, v115, v119\n" \
"v_cvt_pk_bf16_f32 v168, v120, v121\n" \
"v_cvt_pk_bf16_f32 v169, v122, v123\n" \
"buffer_store_dwordx2 v[168:169], v5, s[68:71], s86 offen\n" \
"s_waitcnt vmcnt(15)\n" \
"v_lshlrev_b32 v112, 16, v170\n" \
"v_and_b32 v113, 0xffff0000, v170\n" \
"v_lshlrev_b32 v114, 16, v171\n" \
"v_and_b32 v115, 0xffff0000, v171\n" \
"s_waitcnt vmcnt(14)\n" \
"v_lshlrev_b32 v116, 16, v172\n" \
"v_and_b32 v117, 0xffff0000, v172\n" \
"v_lshlrev_b32 v118, 16, v173\n" \
"v_and_b32 v119, 0xffff0000, v173\n" \
"s_waitcnt vmcnt(13)\n" \
"v_lshlrev_b32 v120, 16, v174\n" \
"v_and_b32 v121, 0xffff0000, v174\n" \
"v_lshlrev_b32 v122, 16, v175\n" \
"v_and_b32 v123, 0xffff0000, v175\n" \
"v_fmac_f32 v120, v112, v116\n" \
"v_fmac_f32 v121, v113, v117\n" \
"v_fmac_f32 v122, v114, v118\n" \
"v_fmac_f32 v123, v115, v119\n" \
"v_cvt_pk_bf16_f32 v174, v120, v121\n" \
"v_cvt_pk_bf16_f32 v175, v122, v123\n" \
"buffer_store_dwordx2 v[174:175], v5, s[68:71], s87 offen\n" \
"s_waitcnt vmcnt(13)\n" \
"v_lshlrev_b32 v112, 16, v176\n" \
"v_and_b32 v113, 0xffff0000, v176\n" \
"v_lshlrev_b32 v114, 16, v177\n" \
"v_and_b32 v115, 0xffff0000, v177\n" \
"s_waitcnt vmcnt(12)\n" \
"v_lshlrev_b32 v116, 16, v178\n" \
"v_and_b32 v117, 0xffff0000, v178\n" \
"v_lshlrev_b32 v118, 16, v179\n" \
"v_and_b32 v119, 0xffff0000, v179\n" \
"s_waitcnt vmcnt(11)\n" \
"v_lshlrev_b32 v120, 16, v180\n" \
"v_and_b32 v121, 0xffff0000, v180\n" \
"v_lshlrev_b32 v122, 16, v181\n" \
"v_and_b32 v123, 0xffff0000, v181\n" \
"v_fmac_f32 v120, v112, v116\n" \
"v_fmac_f32 v121, v113, v117\n" \
"v_fmac_f32 v122, v114, v118\n" \
"v_fmac_f32 v123, v115, v119\n" \
"v_cvt_pk_bf16_f32 v180, v120, v121\n" \
"v_cvt_pk_bf16_f32 v181, v122, v123\n" \
"buffer_store_dwordx2 v[180:181], v5, s[68:71], s88 offen\n" \
"s_waitcnt vmcnt(11)\n" \
"v_lshlrev_b32 v112, 16, v182\n" \
"v_and_b32 v113, 0xffff0000, v182\n" \
"v_lshlrev_b32 v114, 16, v183\n" \
"v_and_b32 v115, 0xffff0000, v183\n" \
"s_waitcnt vmcnt(10)\n" \
"v_lshlrev_b32 v116, 16, v184\n" \
"v_and_b32 v117, 0xffff0000, v184\n" \
"v_lshlrev_b32 v118, 16, v185\n" \
"v_and_b32 v119, 0xffff0000, v185\n" \
"s_waitcnt vmcnt(9)\n" \
"v_lshlrev_b32 v120, 16, v186\n" \
"v_and_b32 v121, 0xffff0000, v186\n" \
"v_lshlrev_b32 v122, 16, v187\n" \
"v_and_b32 v123, 0xffff0000, v187\n" \
"v_fmac_f32 v120, v112, v116\n" \
"v_fmac_f32 v121, v113, v117\n" \
"v_fmac_f32 v122, v114, v118\n" \
"v_fmac_f32 v123, v115, v119\n" \
"v_cvt_pk_bf16_f32 v186, v120, v121\n" \
"v_cvt_pk_bf16_f32 v187, v122, v123\n" \
"buffer_store_dwordx2 v[186:187], v5, s[68:71], s89 offen\n" \
"s_cmp_lt_u32 s44, s96\n" \
"s_cbranch_scc1 .Lsg2_c_%=\n" \
"s_branch .Lsg2_end_%=\n" \
".Lsg2_w_%=:\n" \
"s_nop 7\n" \
"s_nop 7\n" \
"s_waitcnt vmcnt(0) lgkmcnt(0)\n" \
"s_mov_b32 s61, 0\n" \
".Lsg2_wa_%=:\n" \
"s_cmp_lg_u32 s60, 0\n" \
"s_cbranch_scc1 .Lsg2_wk_%=\n" \
"buffer_load_dword v11, v4, s[36:39], 0 offen sc1\n" \
"s_waitcnt vmcnt(0)\n" \
"v_cmp_gt_u32_e64 s[62:63], s59, v11\n" \
"s_nop 4\n" \
"s_cmp_eq_u64 s[62:63], 0\n" \
"s_cbranch_scc1 .Lsg2_wk_%=\n" \
"s_sleep 1\n" \
"s_add_u32 s61, s61, 1\n" \
"s_cmp_lt_u32 s61, 65536\n" \
"s_cbranch_scc1 .Lsg2_wa_%=\n" \
"s_mov_b32 s60, 1\n" \
"v_mov_b32 v11, 256\n" \
"v_mov_b32 v9, 1\n" \
"buffer_store_dword v9, v11, s[36:39], 0 offen sc1\n" \
"s_waitcnt vmcnt(0) lgkmcnt(0)\n" \
"s_branch .Lsg2_wk_%=\n" \
".Lsg2_o_%=:\n" \
"s_nop 7\n" \
"s_nop 7\n" \
"s_waitcnt vmcnt(0) lgkmcnt(0)\n" \
"s_mov_b32 s61, 0\n" \
".Lsg2_oa_%=:\n" \
"s_cmp_lg_u32 s60, 0\n" \
"s_cbranch_scc1 .Lsg2_ok_%=\n" \
"buffer_load_dword v9, off, s[36:39], s49 offset:0 sc1\n" \
"s_waitcnt vmcnt(0)\n" \
"v_cmp_gt_u32 vcc, 4, v9\n" \
"s_nop 4\n" \
"s_cbranch_vccz .Lsg2_ok_%=\n" \
"s_sleep 1\n" \
"s_add_u32 s61, s61, 1\n" \
"s_cmp_lt_u32 s61, 65536\n" \
"s_cbranch_scc1 .Lsg2_oa_%=\n" \
"s_mov_b32 s60, 1\n" \
"v_mov_b32 v11, 256\n" \
"v_mov_b32 v9, 1\n" \
"buffer_store_dword v9, v11, s[36:39], 0 offen sc1\n" \
"s_waitcnt vmcnt(0) lgkmcnt(0)\n" \
"s_branch .Lsg2_ok_%=\n" \
".Lsg2_end_%=:\n" \
"s_endpgm\n"
#define TUNEV(i, c) (c)
#define C_MO 6
#define C_MM 6
#define FARP (TUNEV(11, 1) << 13)
#define C_X 112
#define C_Y 84
#define C_DS 36
#define C_VD 12
#define C_YL 9
#define C_D0 36
#define C_Y0 0
#define C_PW3 8
#define C_TK 0
__device__ __forceinline__ int tkbase(const Params& p, int h, int fp) { const int m = TUNEV(8, C_TK); return WS_TK + h * (4 * MB + 65536) + (fp ^ (m == 2 ? (h & 8) << 10 : (m ^ 1) << 13)); }

typedef u32 v32u __attribute__((ext_vector_type(32)));
typedef float f32x2 __attribute__((ext_vector_type(2)));
__device__ __forceinline__ float xsum(float s) {
    auto a = __builtin_amdgcn_permlane16_swap(__builtin_bit_cast(u32, s), __builtin_bit_cast(u32, s), false, false);
    s = __builtin_bit_cast(float, (u32)a[0]) + __builtin_bit_cast(float, (u32)a[1]);
    auto b = __builtin_amdgcn_permlane32_swap(__builtin_bit_cast(u32, s), __builtin_bit_cast(u32, s), false, false);
    return __builtin_bit_cast(float, (u32)b[0]) + __builtin_bit_cast(float, (u32)b[1]);
}
__device__ __forceinline__ void ln_tile(const Params& p, const v4u (&xv)[8], int rt, int w, int lane, int fp) {
    const int n = lane & 15, g = lane >> 4;
    const rsrc_t rs = __builtin_amdgcn_make_buffer_rsrc(p.ws, 0, WS_END, 0x00020000);
    LAS float* red = (LAS float*)(uintptr_t)(136 * 1024);
    f32x2 xf[32], s2 = {0.f, 0.f};
#pragma unroll
    for (int i = 0; i < 32; ++i) { xf[i] = f32x2{bflo(xv[i >> 2][i & 3]), bfhi(xv[i >> 2][i & 3])}; s2 += xf[i]; }
    const float s = xsum(s2.x + s2.y);
    if (g == 0) red[n * 4 + w] = s;
    BAR();
    f32x4 t = *(LAS f32x4*)(red + n * 4);
    const float mean = ((t.x + t.y) + (t.z + t.w)) * (1.0f / DM);
    f32x2 q2 = {0.f, 0.f};
#pragma unroll
    for (int i = 0; i < 32; ++i) { const f32x2 a = xf[i] - mean; q2 += a * a; }
    const float q = xsum(q2.x + q2.y);
    if (g == 0) red[64 + n * 4 + w] = q;
    BAR();
    t = *(LAS f32x4*)(red + 64 + n * 4);
    const float var = ((t.x + t.y) + (t.z + t.w)) * (1.0f / DM);
    const float mb = rbf(mean);
    const float rd = 1.0f / rbf(sqrtf(rbf(rbf(var) + 1e-5f)));
#pragma unroll
    for (int i = 0; i < 8; ++i) {
        v4u o;
#pragma unroll
        for (int e = 0; e < 4; ++e) { const f32x2 a = xf[4 * i + e] - mb; const u32 d = pack2(a.x, a.y); const f32x2 y = f32x2{bflo(d), bfhi(d)} * rd; o[e] = pack2(y.x, y.y); }
        st128s(o, rs, farx(WS_XF + rt * 65536 + w * 16384 + fp) + i * 1024 + lane * 16);
    }
    WAIT_V0();
    __builtin_amdgcn_s_barrier(); CFENCE();
    if (w == 0 && lane == 0) __hip_atomic_fetch_add((GAS u32*)(p.ws + WS_CT) + CT_LN + (rt >> 4) * 32, 1u, RLXA);
}
__device__ __forceinline__ void ln_role(const Params& p, int c, int w, int fp) {
    const int lane = threadIdx.x & 63;
    const u16* xr = p.x + (size_t)(c * 16 + (lane & 15)) * DM + w * 256 + (lane >> 4) * 8;
    v4u xa[8], xb[8];
#pragma unroll
    for (int i = 0; i < 8; ++i) { xa[i] = *(const v4u*)(xr + i * 32); xb[i] = *(const v4u*)(xr + 128 * DM + i * 32); }
#pragma unroll 1
    for (int k = 0; k < 16; ++k) {
        v4u xc[8];
#pragma unroll
        for (int i = 0; i < 8; ++i) xc[i] = *(const v4u*)(xr + (size_t)min(k + 2, 15) * 128 * DM + i * 32);
        ln_tile(p, xa, k * 8 + c, w, lane, fp);
#pragma unroll
        for (int i = 0; i < 8; ++i) { xa[i] = xb[i]; xb[i] = xc[i]; }
    }
}

__device__ __forceinline__ void p1_role(const Params& p, int b, int s0, int s1, int id, int w, int fp) {
    const int lane = __builtin_amdgcn_mbcnt_hi(~0u, __builtin_amdgcn_mbcnt_lo(~0u, 0u)), n = lane & 15, g = lane >> 4;
    const bool g1 = b >= 96;
    const int h = b & 31, kind = b >> 5;
    const int stride = g1 ? FF * 2 : 8192;
    const u16* src = g1 ? colsrc2(p, w >> 1, b - 96) : colsrc(p, kind == 0 ? 1 : (kind == 1 ? 0 : 2), 2 * h + (w >> 1));
    const v4u rs = mksrd(p.ws, WS_END), rw = mksrd(src, 1023 * stride + 128);
    const int v0 = lane * 16;
    const int v1 = (8 * (w & 1) + (lane >> 3)) * stride + ((lane & 7) ^ ((((lane >> 4) & 1) | ((w & 1) << 1)) << 1)) * 16;
    const int swz = ((n >> 3) | ((g & 1) << 1)), rowo = (8 * g + (n >> 2)) * 128 + (n & 3) * 8;
    const int v2 = (g1 ? 0 : (w >> 1) * 8192) + rowo + (((g1 ? w : 2 * (w & 1)) ^ swz) * 32);
    const int v3 = (g1 ? 8192 : (w >> 1) * 8192) + rowo + (((g1 ? w : 2 * (w & 1) + 1) ^ swz) * 32);
    const int v4 = (w >> 1) * 65536 + (w & 1) * 16384 + lane * 16, ks = (b - 96) * 2 + (w >> 1);
    int v5, ostr, od;
    if (g1) { v5 = (g & 1) * 262144 + (ks >> 3) * 16384 + (ks & 7) * 1024 + (2 * (w & 1) + (g >> 1)) * 256 + n * 16; ostr = 19; od = 0; }
    else if (kind == 2) { v5 = 32768 + w * 2048 + lane * 16; ostr = 16; od = 1024; }
    else { v5 = kind * 16384 + w * 1024 + lane * 16; ostr = 16; od = 4096; }
    const int v6 = (w == 0 && lane == 0) ? WS_CT + (g1 ? CT_G1 + (b - 96) * 32 : CT_P1 + h * 32 + kind * 4) * 4 : OOB;
#define P1_IN "{v0}"(v0), "{v1}"(v1), "{v2}"(v2), "{v3}"(v3), "{v4}"(v4), "{s[36:39]}"(rs), "{s[40:43]}"(rw), "{s44}"(16 * stride), "{s46}"(s0), "{s47}"(s1), "{s48}"(w), "{s49}"(ostr), "{s50}"(od), "{s45}"(WS_XF + fp), "{s64}"(g1 ? WS_HF + fp : tkbase(p, h, fp)), "{s51}"(rfl(kind == 1 ? 0x3e0293ee : 0x3f800000))
    int a5 = v5, a6 = v6; u32 tc, tr;
    if (g1) asm volatile(ASM_P1_G1 : "+{v5}"(a5), "+{v6}"(a6), "={s70}"(tc), "={s72}"(tr) : P1_IN : CLOB_P1);
    else if (kind == 2) asm volatile(ASM_P1_V : "+{v5}"(a5), "+{v6}"(a6), "={s70}"(tc), "={s72}"(tr) : P1_IN : CLOB_P1);
    else asm volatile(ASM_P1_QK : "+{v5}"(a5), "+{v6}"(a6), "={s70}"(tc), "={s72}"(tr) : P1_IN : CLOB_P1);
}

__device__ __forceinline__ int mlane(int n, int g) { return (g >> 1) * 512 + (n >> 3) * 256 + (g & 1) * 128 + (n & 7) * 16; }
__device__ __forceinline__ int mq(int j) { return ((j & 1) ? 0x4800 : 0) ^ ((j & 2) ? 0x800 : 0); }
__device__ __forceinline__ int hword(int i, int cls) { return WS_M + (cls ^ 0x1000 ^ ((i & 32) ? 0x4800 : 0)) + ((i >> 4) & 1) * 32768 + (i & 15) * 128; }
__device__ __forceinline__ int oblk(const Params& p, int rg, int q, int cls) { return rg * 32768 + q * 8192; }
__device__ __forceinline__ void qk_role(const Params& p, int h, int par, int rot) {
    const int lane = __builtin_amdgcn_mbcnt_hi(~0u, __builtin_amdgcn_mbcnt_lo(~0u, 0u)), w = rfl(threadIdx.x >> 6), n = lane & 15, g = lane >> 4;
    const v4u rs = mksrd(p.ws, WS_END), rw = mksrd(colsrc(p, w >> 1, 2 * h + (w & 1)), 1023 * 8192 + 128);
    const int l16 = lane * 16, v1 = l16 + w * 4096, v2 = v1 + 65536, v3 = l16 + 112 * 1024;
    const int v4 = WS_M + (w >> 1) * 32768 + (w & 1) * 1024 + mlane(n, g);
    const int v5 = 152 * 1024 + (32 * w + n) * 32 + (g ^ (n >> 2)) * 8;
    const int v6 = 152 * 1024 + (4 * g + (n >> 2)) * 32 + ((n & 3) ^ g) * 8;
    const int v15 = w == 0 ? min(l16, 64) : OOB;
    const int v7 = (n & 2) == 0 ? WS_O + amap(oblk(p, n >> 3, h >> 3, par) + (h & 7) * 1024 + w * 256 + (g >> 1) * 128 + ((n >> 2) & 1) * 64 + (g & 1) * 32 + (n & 1) * 16, par) : OOB;
    const int v8 = WS_V + amap(h * 2048 + w * 512 + lane * 4, par);
    const int v9 = (w == 0 && lane < 32) ? hword(lane, par) : OOB;
    const int v10 = (w == 0 && lane == 0) ? 0 : OOB;
    const int v11 = 49152 + (w & 1) * 4096 + w * 1024 + l16;
    const int v12 = (n & 2) ? 0xEEEE : 0x4444;
    const int v14 = 16 * (w & 1) + n - 4 * g;
    const int r8 = lane >> 3;
    const int v32 = r8 * 8192 + ((lane & 7) ^ (((r8 >> 1) & 1) << 1)) * 16, v33 = (8 + r8) * 8192 + ((lane & 7) ^ ((((r8 >> 1) & 1) | 2) << 1)) * 16;
    const int swz = (n >> 3) | ((g & 1) << 1), rowo = (w >> 1) * 4096 + (8 * g + (n >> 2)) * 128 + (n & 3) * 8;
    const int v34 = rowo + ((2 * (w & 1)) ^ swz) * 32, v35 = rowo + ((2 * (w & 1) + 1) ^ swz) * 32;
    asm volatile(ASM_QK :: "{v1}"(v1), "{v2}"(v2), "{v3}"(v3), "{v4}"(v4), "{v5}"(v5), "{v6}"(v6), "{v7}"(v7), "{v8}"(v8), "{v9}"(v9), "{v10}"(v10), "{v11}"(v11), "{v12}"(v12),
        "{v14}"(v14), "{v15}"(v15), "{v[32:35]}"(v4u{(u32)v32, (u32)v33, (u32)v34, (u32)v35}), "{s[36:39]}"(rs), "{s[40:43]}"(rw), "{s[44:45]}"(v2u{(u32)TUNEV(0, C_MO), (u32)TUNEV(1, C_MM)}), "{s46}"(TUNEV(2, C_X)), "{s17}"(TUNEV(3, C_Y)), "{s[12:15]}"(v4u{(u32)TUNEV(7, C_D0), (u32)TUNEV(5, C_Y0), (u32)TUNEV(4, C_DS), (u32)rot << 21}), "{s10}"(par ^ mq(rot)), "{s47}"(WS_DBG + 131072 + (h * 4 + w) * 2048), "{s48}"(w),
        "{s63}"(tkbase(p, h, (par & 8192) ^ FARP)), "{s80}"(WS_CT + (CT_P1 + h * 32) * 4), "{s87}"(WS_CT + (CT_AT + h * 32) * 4), "{s94}"(hword(h, par)) : "v255", "a255", "s99", "memory");
    __builtin_unreachable();
}
__device__ __forceinline__ void vo_role(const Params& p, int c, int par) {
    const int lane = __builtin_amdgcn_mbcnt_hi(~0u, __builtin_amdgcn_mbcnt_lo(~0u, 0u)), w = rfl(threadIdx.x >> 6), n = lane & 15, g = lane >> 4;
    const int rg = RGX ? c >> 5 : (c >> 4) & 1, cs = RGX ? c & 31 : (c & 15) + 16 * (c >> 5);
    const v4u rs = mksrd(p.ws, WS_END), rw = mksrd(colsrc(p, 2, c) + (size_t)(256 * w) * 4096, 255 * 8192 + 128), ro = mksrd(colsrc(p, 3, cs >> 1) + (size_t)(1024 * w) * 1024 + (cs & 1) * 32, 1023 * 2048 + 64), rm = mksrd(p.mem0, 16384);
    const int v0 = lane * 16, v1 = (lane >> 3) * 8192 + (lane & 7) * 16, v2 = (lane >> 2) * 2048 + (lane & 3) * 16;
    const int v3 = (8 * g + (n >> 2)) * 128 + w * 32 + (n & 3) * 8;
    const int v4 = w * 40960 + (16 * (g & 1) + 8 * (g >> 1) + 4 * (n >> 3) + ((n >> 2) & 1)) * 64 + (n & 3) * 8;
    const int v5 = WS_M + (w >> 1) * 32768 + (w & 1) * 1024 + mlane(n, g);
    const int v6 = WS_O + amap(oblk(p, rg, w, par) + (2 * (n >> 3) + (g >> 1)) * 256 + (g & 1) * 128 + (n & 7) * 16, par);
    const int v7 = WS_V + amap(c * 1024 + w * 256 + lane * 4, par);
    const int v8 = (w == 0 && (lane & 3) == 0) ? WS_M + (par ^ mq(cs >> 3)) + ((cs >> 2) & 1) * 32768 + ((cs & 3) * 2 + rg) * 256 + lane * 4 : OOB;
    const int v9 = (w == 0 && lane < 32) ? hword(lane, par) : OOB;
    const int v12 = (n & 2) ? 0xEEEE : 0x4444;
    const int v13 = (4 * rg + 2 * ((lane >> 4) & 1) + ((lane >> 2) & 1)) * 2048 + (32 * cs + 16 * (lane >> 5) + 2 * ((lane >> 3) & 1)) * 2;
    const int v15 = 16384 + (g * 16 + ((n >> 1) & 1) * 8 + (n >> 2)) * 32 + w * 8 + (n & 1) * 4;
    asm volatile(ASM_VO :: "{v0}"(v0), "{v1}"(v1), "{v2}"(v2), "{v3}"(v3), "{v4}"(v4), "{v5}"(v5), "{v6}"(v6), "{v7}"(v7), "{v8}"(v8), "{v9}"(v9), "{v12}"(v12),
        "{v13}"(v13), "{v15}"(v15), "{v28}"((w == 0 && lane == 0) ? WS_CT + (CT_P1 + (c >> 1) * 32 + 12 + (c & 1) * 4) * 4 : OOB), "{s[36:39]}"(rs), "{s[40:43]}"(rw), "{s[64:67]}"(ro), "{s[68:71]}"(rm), "{s[44:45]}"(v2u{(u32)TUNEV(0, C_MO), (u32)TUNEV(1, C_MM)}), "{s46}"(TUNEV(12, C_VD)), "{s17}"(TUNEV(3, C_Y)),
        "{s63}"(par), "{s74}"(cs >= 16 ? TUNEV(15, C_YL) : 0), "{s47}"(WS_DBG + 131072 + ((32 + c) * 4 + w) * 2048), "{s48}"(w) : "v255", "a255", "s99", "memory");
    __builtin_unreachable();
}
__device__ __forceinline__ void copy_wd(const Params& p, int u, int fp) {
    const int lane = threadIdx.x & 63;
    const rsrc_t rs = __builtin_amdgcn_make_buffer_rsrc(p.ws + WS_WD, 0, 16 * MB, 0x00020000);
#pragma unroll 1
    for (int k = 0; k < 14; ++k) {
        const int row = (k * 32 + u) * 8;
        v4u t[16];
#pragma unroll
        for (int i = 0; i < 16; ++i) t[i] = __builtin_nontemporal_load((const v4u*)(p.wd + (size_t)row * 1024 + i * 512 + lane * 8));
#pragma unroll
        for (int i = 0; i < 16; ++i) st128s(t[i], rs, farx(row * 4096 + fp) + (i >> 3) * 16384 + ((i >> 1) & 3) * 2048 + ((((i & 1) * 8 + (lane >> 3) + (i >> 1)) & 15) * 128) + (lane & 7) * 16);
    }
    WAIT_V0();
    if (lane == 0) __hip_atomic_fetch_add((GAS u32*)(p.ws + WS_CT) + CT_WD, 1u, RLXA);
}
__device__ __forceinline__ void s64_role(const Params& p, int kind, int cb, int r, int id, int w, int fp) {
    const int lane = __builtin_amdgcn_mbcnt_hi(~0u, __builtin_amdgcn_mbcnt_lo(~0u, 0u)), n = lane & 15, g = lane >> 4;

    if (kind) {
        GAS u32* ct = (GAS u32*)(p.ws + WS_CT);
        for (u32 spins = 0; (u32)rfl(__hip_atomic_load(ct + CT_WD, RLXA)) < 32u; ++spins) {
            if (spins > (1u << 18)) { __hip_atomic_store(ct + CT_TMO, 1u, RLXA); break; }
            __builtin_amdgcn_s_sleep(8);
        }
        CFENCE();
    }
    const v4u rs = mksrd(p.ws, WS_END), rw = mksrd(kind ? p.ws + WS_WD : (const u8*)(colsrc(p, 3, cb) + (size_t)(1024 * w) * 1024), kind ? 16 * MB : 1023 * 2048 + 128), rx = mksrd(p.x, 2048 * 2048), ro = mksrd(p.out, 2048 * 2048);
    const int v2 = lane * 16 + w * 32768, v0 = (kind ? (lane >> 5) * 16384 + ((lane >> 3) & 3) * 2048 + ((cb + (lane >> 3)) & 15) * 128 : (lane >> 3) * 2048) + (lane & 7) * 16;
    const int v1 = w * 32768 + ((kind ? 8 : 4) * g + (n >> 2)) * 128 + (n & 3) * 8;
    const int v3 = (kind ? 0 : 49152) + lane * 16;
    const int v4 = WS_CT + (kind ? CT_G1 + (14 * w + min(n, 13)) * 32 : CT_AT + (8 * w + (n & 7)) * 32) * 4;
    const int v5 = (4 * w + g) * 2048 + cb * 128 + n * 8, v6 = (cb >> 2) * 16384 + (cb & 3) * 2048 + w * 512 + lane * 8;
    const int v7 = 131072 + w * 4096 + g * 256 + (n ^ g ^ 8) * 16, v8 = 131072 + n * 256 + ((4 * w + g) ^ n) * 16;
    const int ha = TUNEV(6, S64_HEAD);
    const int nt = kind ? (r < 2 ? 22 : 21) : (128 - ha) / 4 + (r < 2 ? ha / 2 : 0), r0 = TUNEV(10, S64_R0);
    const int rt = kind ? r0 + r : (r < 2 ? r : ha + r), n1 = kind ? nt - r0 / 6 : nt - (128 - r0) / 4;
#define S64_IO "{s97}"(896 * 4096 * w + fp), "{s78}"(WS_GT + fp), "{v7}"(v7), "{v8}"(v8), "{s44}"(rt), "{v0}"(v0), "{v1}"(v1), "{v2}"(v2), "{v3}"(v3), "{v4}"(v4), "{v5}"(v5), "{v6}"(v6), "{s[36:39]}"(rs), "{s[40:43]}"(rw), "{s[64:67]}"(rx), "{s[68:71]}"(ro), \
        "{s45}"(2), "{s47}"(kind ? 6 : 4), "{s50}"(kind ? -128 : ha), "{s46}"(nt), "{s48}"(w), "{s49}"(WS_CT + CT_S * 4 + (cb * 4 + w) * 128), "{s94}"(r), "{s95}"(n1), "{s96}"(r0), "{s51}"(w ? OOB : WS_DBG + id * 128), "{s99}"(kind == 0 ? TUNEV(9, C_PW3) : 0), "{s85}"(kind ? WS_HF + fp : tkbase(p, 8 * w, fp))
    if (kind) asm volatile(ASM_S64_G2 :: S64_IO : "v255", "a255", "memory");
    else asm volatile(ASM_S64_WO :: S64_IO : "v255", "a255", "memory");
    __builtin_unreachable();
}
#define MAIN_BODY \
int gx = (id >> 5) - 4, gr = id & 31; \
    if (id < 128) { \
        const int x = id >> 5, t = id & 31; \
        const int nq = PLACE == 0 ? (x < 2 ? 16 : 0) : (PLACE == 1 ? 8 : (x < 2 ? 16 : 0)), nv = PLACE == 2 ? (x < 2 ? 16 : (x == 2 ? 32 : 0)) : 16; \
        if (t < nq) qk_role(p, nq * x + t, par, x & TUNEV(14, 1)); \
        else if (t < nq + nv) vo_role(p, 16 * x + t - nq, par); \
        gx = -1; gr = PLACE == 0 ? (x - 2) * 16 + t - 16 : (PLACE == 1 ? x * 8 + t - 24 : t); \
        id = 96 + gr; \
        STAMP(0); \
    } \
    const int fp = (par & 8192) ^ FARP; \
    if (gx < 0 || gr < 30) p1_role(p, gx < 0 ? 64 + gr : (gr < 16 ? gx * 8 + (gr & 7) + (gr >> 3) * 32 : 96 + gx * 14 + gr - 16), 0, 64, id, wv_, fp); \
    else { ln_role(p, gx * 2 + gr - 30, wv_, fp); copy_wd(p, (gx * 2 + gr - 30) * 4 + wv_, fp); } \
    STAMP(2); \
    s64_role(p, gx < 1 ? 0 : 1, gr & 15, (gx < 1 ? 2 * gx + 2 : 2 * gx - 2) + (gr >> 4), id, wv_, fp);
constexpr int LDS_BYTES = 160 * 1024;
__device__ __forceinline__ int take_role(const Params& p, LAS u32* L) {
    if (threadIdx.x == 0) {
        GAS u32* tk = (GAS u32*)(p.ws + WS_CT) + CT_TICK;
        const u32 x = xcc_id() & 7u;
        const u32 t = __hip_atomic_fetch_add(tk + 64 * x, 1u, RLXA);
        u32 id = 32 * x + t;
        if (t >= 32u) {
            u32 o = __hip_atomic_fetch_add(tk + 64 * 8, 1u, RLXA);
            for (u32 spins = 0; spins < (1u << 20); ++spins) {
                u32 tot = 0;
                for (int k = 0; k < 8; ++k) tot += __hip_atomic_load(tk + 64 * k, RLXA);
                if (tot >= 256u) break;
                __builtin_amdgcn_s_sleep(8);
            }
            id = 255u;
            for (int k = 0; k < 8; ++k) {
                const u32 ck = __hip_atomic_load(tk + 64 * k, RLXA);
                const u32 fr = ck < 32u ? 32u - ck : 0u;
                if (o < fr) { id = 32 * k + ck + o; break; }
                o -= fr;
            }
        }
        *L = id;
    }
    BAR();
    const int id = rfl(*L);
    BAR();
    return id;
}
#define STAMP(i) do {} while (0)
__global__ void __launch_bounds__(256, 1) k_main(Params p) {
    extern __shared__ __attribute__((aligned(16))) u8 smem[];
    int par = rfl(*(const GAS u32*)(p.ws + WS_CT + CT_PAR * 4)) & 1;
    par = (par << 13) | ((TUNEV(13, 0) & 3) << 11) | ((TUNEV(13, 0) & 4) << 12);
    int id = take_role(p, (LAS u32*)smem);
    const int wv_ = rfl(threadIdx.x >> 6);
    STAMP(0);
    MAIN_BODY
    STAMP(7);
}
__global__ void __launch_bounds__(256) k_zero(Params p) {
    __shared__ u32 vote[4];
    u8* ws = p.ws;
    v4u z = {0u, 0u, 0u, 0u};
    if (WARM && blockIdx.x != 0) {
        const int i = (blockIdx.x - 1) * 32 + (threadIdx.x >> 3), m2 = min(i & 7, 2);
        const rsrc_t rc = __builtin_amdgcn_make_buffer_rsrc(ws + WS_WC, 0, 16 * MB, 0x00020000);
        v4u t[7];
#pragma unroll
        for (int m = 0; m < 4; ++m) t[m] = __builtin_nontemporal_load((const v4u*)(wmat(p, m) + (size_t)i * 512 + slowpos(wmat(p, m)) * 64 + (threadIdx.x & 7) * 8));
        if ((i & 7) < 3) t[4] = __builtin_nontemporal_load((const v4u*)(wmat(p, m2) + (size_t)(i >> 3) * 4096 + slowpos2(wmat(p, m2)) * 64 + (threadIdx.x & 7) * 8));
        if (WARM > 1 && i < 7168) {
            t[5] = __builtin_nontemporal_load((const v4u*)(p.wg + (size_t)i * 512 + slowpos(p.wg) * 64 + (threadIdx.x & 7) * 8));
            t[6] = __builtin_nontemporal_load((const v4u*)(p.wu + (size_t)i * 512 + slowpos(p.wu) * 64 + (threadIdx.x & 7) * 8));
        }
#pragma unroll
        for (int m = 0; m < 4; ++m) __builtin_amdgcn_raw_buffer_store_b128(t[m], rc, i * 1024 + (m == 3 ? 4 : m) * 128 + (threadIdx.x & 7) * 16, 0, ZAUX);
        if ((i & 7) < 3) __builtin_amdgcn_raw_buffer_store_b128(t[4], rc, (i >> 3) * 8192 + (5 + m2) * 128 + (threadIdx.x & 7) * 16, 0, ZAUX);
        if (WARM > 1 && i < 7168) {
            __builtin_amdgcn_raw_buffer_store_b128(t[5], rc, WS_WG - WS_WC + i * 1024 + (threadIdx.x & 7) * 16, 0, ZAUX);
            __builtin_amdgcn_raw_buffer_store_b128(t[6], rc, WS_WG - WS_WC + i * 1024 + 128 + (threadIdx.x & 7) * 16, 0, ZAUX);
        }
    }
    if (blockIdx.x == 0) {
        const rsrc_t rs = __builtin_amdgcn_make_buffer_rsrc(ws, 0, 2 * MB, 0x00020000);
        const int lane = threadIdx.x & 63, w = threadIdx.x >> 6, base = 2 * MB - 32768 + (w & 1) * 16384 + (w >> 1) * 2048 + (lane & 31) * 4;
        st32s(0u, rs, base + 1024); st32s(0u, rs, base + 8192 + 1024);
        u32 a = 65535u, b = 65535u;
#pragma unroll 1
        for (int k = 0; k < ZR; ++k) {
            u64 t0, t1, t2;
            asm volatile("s_waitcnt vmcnt(0)\n\ts_memtime %0\n\ts_waitcnt lgkmcnt(0)" : "=s"(t0) :: "memory");
            st32s(0u, rs, base);
            asm volatile("s_waitcnt vmcnt(0)\n\ts_memtime %0\n\ts_waitcnt lgkmcnt(0)" : "=s"(t1) :: "memory");
            st32s(0u, rs, base + 8192);
            asm volatile("s_waitcnt vmcnt(0)\n\ts_memtime %0\n\ts_waitcnt lgkmcnt(0)" : "=s"(t2) :: "memory");
            a = min(a, (u32)(t1 - t0)); b = min(b, (u32)(t2 - t1));
            if (4 * max(a, b) > 5 * min(a, b) && min(a, b) < 600u && max(a, b) < 1000u) break;
        }
        vote[w] = (a << 16) | b;
        __syncthreads();
        if (threadIdx.x == 0) {
            for (int k = 0; k < 4; ++k) { a = min(a, vote[k] >> 16); b = min(b, vote[k] & 65535u); }
            z.x = (b < a ? 1u : 0u) ^ (xcc_id() >= 4 ? 1u : 0u);
        }
    }
    if (blockIdx.x < 32) ((v4u*)(ws + (blockIdx.x >> 4) * WS_M))[(blockIdx.x & 15) * 256 + threadIdx.x] = z;
}

extern "C" void kernel_launch(void* const* d_in, const int* in_sizes, int n_in, void* d_out, int out_size, void* d_ws, size_t ws_size, hipStream_t stream) {
    Params p;
    p.x = (const u16*)d_in[0]; p.mem0 = (const u16*)d_in[1]; p.wq = (const u16*)d_in[2]; p.wk = (const u16*)d_in[3]; p.wv = (const u16*)d_in[4]; p.wo = (const u16*)d_in[5];
    p.wg = (const u16*)d_in[8]; p.wu = (const u16*)d_in[9]; p.wd = (const u16*)d_in[10];
    p.out = (u16*)d_out;
    p.ws = (u8*)(((uintptr_t)d_ws + (2u * MB - 1)) & ~(uintptr_t)(2u * MB - 1));
    (void)hipFuncSetAttribute((const void*)k_main, hipFuncAttributeMaxDynamicSharedMemorySize, LDS_BYTES);
    hipLaunchKernelGGL(k_zero, dim3(257), dim3(256), 0, stream, p);
    hipLaunchKernelGGL(k_main, dim3(256), dim3(256), LDS_BYTES, stream, p);
}
```

```cpp
#define V2 0
#pragma clang diagnostic ignored "-Wint-to-pointer-cast"
#pragma clang diagnostic ignored "-Winline-asm"
#include <hip/hip_runtime.h>
#include <hip/hip_bf16.h>
#include <stdint.h>

#define LAS __attribute__((address_space(3)))
#define GAS __attribute__((address_space(1)))
typedef unsigned u32;
typedef unsigned char u8;
typedef unsigned short u16;
typedef unsigned long long u64;
typedef u32 v2u __attribute__((ext_vector_type(2)));
typedef u32 v4u __attribute__((ext_vector_type(4)));
typedef float f32x4 __attribute__((ext_vector_type(4)));
typedef __bf16 bf16x2 __attribute__((ext_vector_type(2)));
typedef __amdgpu_buffer_rsrc_t rsrc_t;

constexpr int NSEG = 64, DM = 1024, NH = 32, FF = 3584;
constexpr int MB = 1 << 20;
constexpr int WS_CT = 0;
constexpr int WS_M = 262144;
constexpr int WS_O = 524288, WS_V = 1048576;
constexpr int WS_DBG = 4 * MB;
constexpr int WS_XF = 8 * MB;
constexpr int WS_HF = 32 * MB;
constexpr int WS_GT = 16 * MB;
constexpr int WS_TK = 64 * MB;
constexpr int WS_END = 192 * MB;
__device__ __forceinline__ int farx(int off) { return off ^ ((off >> 8) & 0x2000); }
constexpr int CT_PAR = 0, CT_TMO = 64, CT_TICK = 256;
constexpr int CT_LN = 1024;
constexpr int CT_WD = 1536;
constexpr int CT_P1 = 2048;
constexpr int CT_G1 = 4096;
constexpr int CT_AT = 3072;
constexpr int CT_S = 8192;
#define NQ0 16
#define RGX 0
#define ZAUX 16
#define ZR 8
#define WARM 1
#define NWO 4
#define PMAP 1
#define S64_HEAD 22
#define S64_R0 112
constexpr int OOB = 0x7ffffff0;
constexpr int AUX_SC1 = 16;

struct Params {
    const u16 *x, *mem0, *wq, *wk, *wv, *wo, *wg, *wu, *wd;
    u16* out;
    u8* ws;
};
__device__ __forceinline__ u32 pack2(float a, float b) { bf16x2 v = {(__bf16)a, (__bf16)b}; return __builtin_bit_cast(u32, v); }
__device__ __forceinline__ float bflo(u32 d) { return __builtin_bit_cast(float, d << 16); }
__device__ __forceinline__ float bfhi(u32 d) { return __builtin_bit_cast(float, d & 0xFFFF0000u); }
__device__ __forceinline__ float rbf(float a) { return (float)(__bf16)a; }
__device__ __forceinline__ void st128s(v4u v, rsrc_t rs, int voff) { __builtin_amdgcn_raw_buffer_store_b128(v, rs, voff, 0, AUX_SC1); }
__device__ __forceinline__ void st32s(u32 v, rsrc_t rs, int voff) { __builtin_amdgcn_raw_buffer_store_b32(v, rs, voff, 0, AUX_SC1); }
__device__ __forceinline__ u32 ld32s(rsrc_t rs, int voff) { return __builtin_amdgcn_raw_buffer_load_b32(rs, voff, 0, AUX_SC1); }
__device__ __forceinline__ u32 xcc_id() { return (u32)__builtin_amdgcn_s_getreg(20 | (3 << 11)) & 0xfu; }
__device__ __forceinline__ int rfl(int x) { return __builtin_amdgcn_readfirstlane(x); }
__device__ __forceinline__ v4u mksrd(const void* p, u32 bytes) {
    const u64 a = (u64)p;
    return v4u{(u32)rfl((u32)a), (u32)rfl((u32)(a >> 32) & 0xFFFFu), (u32)rfl(bytes), 0x00020000u};
}
#define CFENCE() asm volatile("" ::: "memory")
#define WAIT_V0() asm volatile("s_waitcnt vmcnt(0)" ::: "memory")
#define BAR() do { asm volatile("s_waitcnt lgkmcnt(0)" ::: "memory"); __builtin_amdgcn_s_barrier(); asm volatile("" ::: "memory"); } while (0)
#define RLXA __ATOMIC_RELAXED, __HIP_MEMORY_SCOPE_AGENT
__device__ __forceinline__ int amap(int off, int cls) { return ((off >> 13) << 14) + (off & 8191) + (cls & 8192); }

constexpr int WS_WC = 208 * MB;
constexpr int WS_WG = 216 * MB;
constexpr int WS_WD = 192 * MB;
__device__ __forceinline__ const u16* wmat(const Params& p, int m) { return m == 0 ? p.wq : (m == 1 ? p.wk : (m == 2 ? p.wv : p.wo)); }
__device__ __forceinline__ int slowpos(const void* b) { return (3 - (int)((u64)b >> 7)) & 7; }
__device__ __forceinline__ int slowpos2(const void* b) { return (63 - (int)((u64)b >> 7)) & 63; }
__device__ __forceinline__ const u16* colsrc(const Params& p, int m, int j) {
    const u16* b = wmat(p, m);
    if (WARM && m < 3 && j == slowpos2(b)) return (const u16*)(p.ws + WS_WC) + (5 + m) * 64;
    return (WARM && (j & 7) == slowpos(b)) ? (const u16*)(p.ws + WS_WC) + (j >> 3) * 512 + (m == 3 ? 4 : m) * 64 : b + j * 64;
}
__device__ __forceinline__ const u16* colsrc2(const Params& p, int m, int j) {
    const u16* b = m ? p.wu : p.wg;
    return (WARM > 1 && (j & 7) == slowpos(b)) ? (const u16*)(p.ws + WS_WG) + (j >> 3) * 512 + m * 64 : b + j * 64;
}
#define ASM_P1_QK \
"s_nop 7\n" \
"s_nop 7\n" \
"s_lshr_b32 s52, s48, 1\n" \
"s_lshl_b32 s52, s52, 13\n" \
"s_and_b32 s53, s48, 1\n" \
"s_lshl_b32 s53, s53, 10\n" \
"s_add_u32 s52, s52, s53\n" \
"s_mov_b32 s54, 0\n" \
"s_add_u32 m0, s52, 0\n" \
"s_nop 0\n" \
"buffer_load_dwordx4 v1, s[40:43], s54 offen nt lds\n" \
"s_add_u32 s54, s54, s44\n" \
"s_add_u32 m0, s52, 2048\n" \
"s_nop 0\n" \
"buffer_load_dwordx4 v1, s[40:43], s54 offen nt lds\n" \
"s_add_u32 s54, s54, s44\n" \
"s_add_u32 m0, s52, 4096\n" \
"s_nop 0\n" \
"buffer_load_dwordx4 v1, s[40:43], s54 offen nt lds\n" \
"s_add_u32 s54, s54, s44\n" \
"s_add_u32 m0, s52, 6144\n" \
"s_nop 0\n" \
"buffer_load_dwordx4 v1, s[40:43], s54 offen nt lds\n" \
"s_add_u32 s54, s54, s44\n" \
"s_add_u32 m0, s52, 16384\n" \
"s_nop 0\n" \
"buffer_load_dwordx4 v1, s[40:43], s54 offen nt lds\n" \
"s_add_u32 s54, s54, s44\n" \
"s_add_u32 m0, s52, 18432\n" \
"s_nop 0\n" \
"buffer_load_dwordx4 v1, s[40:43], s54 offen nt lds\n" \
"s_add_u32 s54, s54, s44\n" \
"s_add_u32 m0, s52, 20480\n" \
"s_nop 0\n" \
"buffer_load_dwordx4 v1, s[40:43], s54 offen nt lds\n" \
"s_add_u32 s54, s54, s44\n" \
"s_add_u32 m0, s52, 22528\n" \
"s_nop 0\n" \
"buffer_load_dwordx4 v1, s[40:43], s54 offen nt lds\n" \
"s_add_u32 s54, s54, s44\n" \
"s_add_u32 m0, s52, 32768\n" \
"s_nop 0\n" \
"buffer_load_dwordx4 v1, s[40:43], s54 offen nt lds\n" \
"s_add_u32 s54, s54, s44\n" \
"s_add_u32 m0, s52, 34816\n" \
"s_nop 0\n" \
"buffer_load_dwordx4 v1, s[40:43], s54 offen nt lds\n" \
"s_add_u32 s54, s54, s44\n" \
"s_add_u32 m0, s52, 36864\n" \
"s_nop 0\n" \
"buffer_load_dwordx4 v1, s[40:43], s54 offen nt lds\n" \
"s_add_u32 s54, s54, s44\n" \
"s_add_u32 m0, s52, 38912\n" \
"s_nop 0\n" \
"buffer_load_dwordx4 v1, s[40:43], s54 offen nt lds\n" \
"s_add_u32 s54, s54, s44\n" \
"s_add_u32 m0, s52, 49152\n" \
"s_nop 0\n" \
"buffer_load_dwordx4 v1, s[40:43], s54 offen nt lds\n" \
"s_add_u32 s54, s54, s44\n" \
"s_add_u32 m0, s52, 51200\n" \
"s_nop 0\n" \
"buffer_load_dwordx4 v1, s[40:43], s54 offen nt lds\n" \
"s_add_u32 s54, s54, s44\n" \
"s_add_u32 m0, s52, 53248\n" \
"s_nop 0\n" \
"buffer_load_dwordx4 v1, s[40:43], s54 offen nt lds\n" \
"s_add_u32 s54, s54, s44\n" \
"s_add_u32 m0, s52, 55296\n" \
"s_nop 0\n" \
"buffer_load_dwordx4 v1, s[40:43], s54 offen nt lds\n" \
"s_add_u32 s54, s54, s44\n" \
"s_add_u32 m0, s52, 65536\n" \
"s_nop 0\n" \
"buffer_load_dwordx4 v1, s[40:43], s54 offen nt lds\n" \
"s_add_u32 s54, s54, s44\n" \
"s_add_u32 m0, s52, 67584\n" \
"s_nop 0\n" \
"buffer_load_dwordx4 v1, s[40:43], s54 offen nt lds\n" \
"s_add_u32 s54, s54, s44\n" \
"s_add_u32 m0, s52, 69632\n" \
"s_nop 0\n" \
"buffer_load_dwordx4 v1, s[40:43], s54 offen nt lds\n" \
"s_add_u32 s54, s54, s44\n" \
"s_add_u32 m0, s52, 71680\n" \
"s_nop 0\n" \
"buffer_load_dwordx4 v1, s[40:43], s54 offen nt lds\n" \
"s_add_u32 s54, s54, s44\n" \
"s_add_u32 m0, s52, 81920\n" \
"s_nop 0\n" \
"buffer_load_dwordx4 v1, s[40:43], s54 offen nt lds\n" \
"s_add_u32 s54, s54, s44\n" \
"s_add_u32 m0, s52, 83968\n" \
"s_nop 0\n" \
"buffer_load_dwordx4 v1, s[40:43], s54 offen nt lds\n" \
"s_add_u32 s54, s54, s44\n" \
"s_add_u32 m0, s52, 86016\n" \
"s_nop 0\n" \
"buffer_load_dwordx4 v1, s[40:43], s54 offen nt lds\n" \
"s_add_u32 s54, s54, s44\n" \
"s_add_u32 m0, s52, 88064\n" \
"s_nop 0\n" \
"buffer_load_dwordx4 v1, s[40:43], s54 offen nt lds\n" \
"s_add_u32 s54, s54, s44\n" \
"s_add_u32 m0, s52, 98304\n" \
"s_nop 0\n" \
"buffer_load_dwordx4 v1, s[40:43], s54 offen nt lds\n" \
"s_add_u32 s54, s54, s44\n" \
"s_add_u32 m0, s52, 100352\n" \
"s_nop 0\n" \
"buffer_load_dwordx4 v1, s[40:43], s54 offen nt lds\n" \
"s_add_u32 s54, s54, s44\n" \
"s_add_u32 m0, s52, 102400\n" \
"s_nop 0\n" \
"buffer_load_dwordx4 v1, s[40:43], s54 offen nt lds\n" \
"s_add_u32 s54, s54, s44\n" \
"s_add_u32 m0, s52, 104448\n" \
"s_nop 0\n" \
"buffer_load_dwordx4 v1, s[40:43], s54 offen nt lds\n" \
"s_add_u32 s54, s54, s44\n" \
"s_add_u32 m0, s52, 114688\n" \
"s_nop 0\n" \
"buffer_load_dwordx4 v1, s[40:43], s54 offen nt lds\n" \
"s_add_u32 s54, s54, s44\n" \
"s_add_u32 m0, s52, 116736\n" \
"s_nop 0\n" \
"buffer_load_dwordx4 v1, s[40:43], s54 offen nt lds\n" \
"s_add_u32 s54, s54, s44\n" \
"s_add_u32 m0, s52, 118784\n" \
"s_nop 0\n" \
"buffer_load_dwordx4 v1, s[40:43], s54 offen nt lds\n" \
"s_add_u32 s54, s54, s44\n" \
"s_add_u32 m0, s52, 120832\n" \
"s_nop 0\n" \
"buffer_load_dwordx4 v1, s[40:43], s54 offen nt lds\n" \
"s_add_u32 s54, s54, s44\n" \
"v_add_u32 v8, 0x10000, v2\n" \
"v_add_u32 v9, 0x10000, v3\n" \
"s_waitcnt vmcnt(28)\n" \
"s_barrier\n" \
"ds_read_b64_tr_b16 a[0:1], v2 offset:0\n" \
"ds_read_b64_tr_b16 a[2:3], v2 offset:512\n" \
"ds_read_b64_tr_b16 a[128:129], v3 offset:0\n" \
"ds_read_b64_tr_b16 a[130:131], v3 offset:512\n" \
"ds_read_b64_tr_b16 a[4:5], v2 offset:4096\n" \
"ds_read_b64_tr_b16 a[6:7], v2 offset:4608\n" \
"ds_read_b64_tr_b16 a[132:133], v3 offset:4096\n" \
"ds_read_b64_tr_b16 a[134:135], v3 offset:4608\n" \
"s_waitcnt lgkmcnt(0)\n" \
"s_barrier\n" \
"s_add_u32 m0, s52, 0\n" \
"s_nop 0\n" \
"buffer_load_dwordx4 v1, s[40:43], s54 offen nt lds\n" \
"s_add_u32 s54, s54, s44\n" \
"s_add_u32 m0, s52, 2048\n" \
"s_nop 0\n" \
"buffer_load_dwordx4 v1, s[40:43], s54 offen nt lds\n" \
"s_add_u32 s54, s54, s44\n" \
"s_add_u32 m0, s52, 4096\n" \
"s_nop 0\n" \
"buffer_load_dwordx4 v1, s[40:43], s54 offen nt lds\n" \
"s_add_u32 s54, s54, s44\n" \
"s_add_u32 m0, s52, 6144\n" \
"s_nop 0\n" \
"buffer_load_dwordx4 v1, s[40:43], s54 offen nt lds\n" \
"s_add_u32 s54, s54, s44\n" \
"s_waitcnt vmcnt(28)\n" \
"s_barrier\n" \
"ds_read_b64_tr_b16 a[8:9], v2 offset:16384\n" \
"ds_read_b64_tr_b16 a[10:11], v2 offset:16896\n" \
"ds_read_b64_tr_b16 a[136:137], v3 offset:16384\n" \
"ds_read_b64_tr_b16 a[138:139], v3 offset:16896\n" \
"ds_read_b64_tr_b16 a[12:13], v2 offset:20480\n" \
"ds_read_b64_tr_b16 a[14:15], v2 offset:20992\n" \
"ds_read_b64_tr_b16 a[140:141], v3 offset:20480\n" \
"ds_read_b64_tr_b16 a[142:143], v3 offset:20992\n" \
"s_waitcnt lgkmcnt(0)\n" \
"s_barrier\n" \
"s_add_u32 m0, s52, 16384\n" \
"s_nop 0\n" \
"buffer_load_dwordx4 v1, s[40:43], s54 offen nt lds\n" \
"s_add_u32 s54, s54, s44\n" \
"s_add_u32 m0, s52, 18432\n" \
"s_nop 0\n" \
"buffer_load_dwordx4 v1, s[40:43], s54 offen nt lds\n" \
"s_add_u32 s54, s54, s44\n" \
"s_add_u32 m0, s52, 20480\n" \
"s_nop 0\n" \
"buffer_load_dwordx4 v1, s[40:43], s54 offen nt lds\n" \
"s_add_u32 s54, s54, s44\n" \
"s_add_u32 m0, s52, 22528\n" \
"s_nop 0\n" \
"buffer_load_dwordx4 v1, s[40:43], s54 offen nt lds\n" \
"s_add_u32 s54, s54, s44\n" \
"s_waitcnt vmcnt(28)\n" \
"s_barrier\n" \
"ds_read_b64_tr_b16 a[16:17], v2 offset:32768\n" \
"ds_read_b64_tr_b16 a[18:19], v2 offset:33280\n" \
"ds_read_b64_tr_b16 a[144:145], v3 offset:32768\n" \
"ds_read_b64_tr_b16 a[146:147], v3 offset:33280\n" \
"ds_read_b64_tr_b16 a[20:21], v2 offset:36864\n" \
"ds_read_b64_tr_b16 a[22:23], v2 offset:37376\n" \
"ds_read_b64_tr_b16 a[148:149], v3 offset:36864\n" \
"ds_read_b64_tr_b16 a[150:151], v3 offset:37376\n" \
"s_waitcnt lgkmcnt(0)\n" \
"s_barrier\n" \
"s_add_u32 m0, s52, 32768\n" \
"s_nop 0\n" \
"buffer_load_dwordx4 v1, s[40:43], s54 offen nt lds\n" \
"s_add_u32 s54, s54, s44\n" \
"s_add_u32 m0, s52, 34816\n" \
"s_nop 0\n" \
"buffer_load_dwordx4 v1, s[40:43], s54 offen nt lds\n" \
"s_add_u32 s54, s54, s44\n" \
"s_add_u32 m0, s52, 36864\n" \
"s_nop 0\n" \
"buffer_load_dwordx4 v1, s[40:43], s54 offen nt lds\n" \
"s_add_u32 s54, s54, s44\n" \
"s_add_u32 m0, s52, 38912\n" \
"s_nop 0\n" \
"buffer_load_dwordx4 v1, s[40:43], s54 offen nt lds\n" \
"s_add_u32 s54, s54, s44\n" \
"s_waitcnt vmcnt(28)\n" \
"s_barrier\n" \
"ds_read_b64_tr_b16 a[24:25], v2 offset:49152\n" \
"ds_read_b64_tr_b16 a[26:27], v2 offset:49664\n" \
"ds_read_b64_tr_b16 a[152:153], v3 offset:49152\n" \
"ds_read_b64_tr_b16 a[154:155], v3 offset:49664\n" \
"ds_read_b64_tr_b16 a[28:29], v2 offset:53248\n" \
"ds_read_b64_tr_b16 a[30:31], v2 offset:53760\n" \
"ds_read_b64_tr_b16 a[156:157], v3 offset:53248\n" \
"ds_read_b64_tr_b16 a[158:159], v3 offset:53760\n" \
"s_waitcnt lgkmcnt(0)\n" \
"s_barrier\n" \
"s_add_u32 m0, s52, 49152\n" \
"s_nop 0\n" \
"buffer_load_dwordx4 v1, s[40:43], s54 offen nt lds\n" \
"s_add_u32 s54, s54, s44\n" \
"s_add_u32 m0, s52, 51200\n" \
"s_nop 0\n" \
"buffer_load_dwordx4 v1, s[40:43], s54 offen nt lds\n" \
"s_add_u32 s54, s54, s44\n" \
"s_add_u32 m0, s52, 53248\n" \
"s_nop 0\n" \
"buffer_load_dwordx4 v1, s[40:43], s54 offen nt lds\n" \
"s_add_u32 s54, s54, s44\n" \
"s_add_u32 m0, s52, 55296\n" \
"s_nop 0\n" \
"buffer_load_dwordx4 v1, s[40:43], s54 offen nt lds\n" \
"s_add_u32 s54, s54, s44\n" \
"s_waitcnt vmcnt(28)\n" \
"s_barrier\n" \
"ds_read_b64_tr_b16 a[32:33], v8 offset:0\n" \
"ds_read_b64_tr_b16 a[34:35], v8 offset:512\n" \
"ds_read_b64_tr_b16 a[160:161], v9 offset:0\n" \
"ds_read_b64_tr_b16 a[162:163], v9 offset:512\n" \
"ds_read_b64_tr_b16 a[36:37], v8 offset:4096\n" \
"ds_read_b64_tr_b16 a[38:39], v8 offset:4608\n" \
"ds_read_b64_tr_b16 a[164:165], v9 offset:4096\n" \
"ds_read_b64_tr_b16 a[166:167], v9 offset:4608\n" \
"s_waitcnt lgkmcnt(0)\n" \
"s_barrier\n" \
"s_add_u32 m0, s52, 65536\n" \
"s_nop 0\n" \
"buffer_load_dwordx4 v1, s[40:43], s54 offen nt lds\n" \
"s_add_u32 s54, s54, s44\n" \
"s_add_u32 m0, s52, 67584\n" \
"s_nop 0\n" \
"buffer_load_dwordx4 v1, s[40:43], s54 offen nt lds\n" \
"s_add_u32 s54, s54, s44\n" \
"s_add_u32 m0, s52, 69632\n" \
"s_nop 0\n" \
"buffer_load_dwordx4 v1, s[40:43], s54 offen nt lds\n" \
"s_add_u32 s54, s54, s44\n" \
"s_add_u32 m0, s52, 71680\n" \
"s_nop 0\n" \
"buffer_load_dwordx4 v1, s[40:43], s54 offen nt lds\n" \
"s_add_u32 s54, s54, s44\n" \
"s_waitcnt vmcnt(28)\n" \
"s_barrier\n" \
"ds_read_b64_tr_b16 a[40:41], v8 offset:16384\n" \
"ds_read_b64_tr_b16 a[42:43], v8 offset:16896\n" \
"ds_read_b64_tr_b16 a[168:169], v9 offset:16384\n" \
"ds_read_b64_tr_b16 a[170:171], v9 offset:16896\n" \
"ds_read_b64_tr_b16 a[44:45], v8 offset:20480\n" \
"ds_read_b64_tr_b16 a[46:47], v8 offset:20992\n" \
"ds_read_b64_tr_b16 a[172:173], v9 offset:20480\n" \
"ds_read_b64_tr_b16 a[174:175], v9 offset:20992\n" \
"s_waitcnt lgkmcnt(0)\n" \
"s_barrier\n" \
"s_add_u32 m0, s52, 81920\n" \
"s_nop 0\n" \
"buffer_load_dwordx4 v1, s[40:43], s54 offen nt lds\n" \
"s_add_u32 s54, s54, s44\n" \
"s_add_u32 m0, s52, 83968\n" \
"s_nop 0\n" \
"buffer_load_dwordx4 v1, s[40:43], s54 offen nt lds\n" \
"s_add_u32 s54, s54, s44\n" \
"s_add_u32 m0, s52, 86016\n" \
"s_nop 0\n" \
"buffer_load_dwordx4 v1, s[40:43], s54 offen nt lds\n" \
"s_add_u32 s54, s54, s44\n" \
"s_add_u32 m0, s52, 88064\n" \
"s_nop 0\n" \
"buffer_load_dwordx4 v1, s[40:43], s54 offen nt lds\n" \
"s_add_u32 s54, s54, s44\n" \
"s_waitcnt vmcnt(28)\n" \
"s_barrier\n" \
"ds_read_b64_tr_b16 a[48:49], v8 offset:32768\n" \
"ds_read_b64_tr_b16 a[50:51], v8 offset:33280\n" \
"ds_read_b64_tr_b16 a[176:177], v9 offset:32768\n" \
"ds_read_b64_tr_b16 a[178:179], v9 offset:33280\n" \
"ds_read_b64_tr_b16 a[52:53], v8 offset:36864\n" \
"ds_read_b64_tr_b16 a[54:55], v8 offset:37376\n" \
"ds_read_b64_tr_b16 a[180:181], v9 offset:36864\n" \
"ds_read_b64_tr_b16 a[182:183], v9 offset:37376\n" \
"s_waitcnt lgkmcnt(0)\n" \
"s_barrier\n" \
"s_add_u32 m0, s52, 98304\n" \
"s_nop 0\n" \
"buffer_load_dwordx4 v1, s[40:43], s54 offen nt lds\n" \
"s_add_u32 s54, s54, s44\n" \
"s_add_u32 m0, s52, 100352\n" \
"s_nop 0\n" \
"buffer_load_dwordx4 v1, s[40:43], s54 offen nt lds\n" \
"s_add_u32 s54, s54, s44\n" \
"s_add_u32 m0, s52, 102400\n" \
"s_nop 0\n" \
"buffer_load_dwordx4 v1, s[40:43], s54 offen nt lds\n" \
"s_add_u32 s54, s54, s44\n" \
"s_add_u32 m0, s52, 104448\n" \
"s_nop 0\n" \
"buffer_load_dwordx4 v1, s[40:43], s54 offen nt lds\n" \
"s_add_u32 s54, s54, s44\n" \
"s_waitcnt vmcnt(28)\n" \
"s_barrier\n" \
"ds_read_b64_tr_b16 a[56:57], v8 offset:49152\n" \
"ds_read_b64_tr_b16 a[58:59], v8 offset:49664\n" \
"ds_read_b64_tr_b16 a[184:185], v9 offset:49152\n" \
"ds_read_b64_tr_b16 a[186:187], v9 offset:49664\n" \
"ds_read_b64_tr_b16 a[60:61], v8 offset:53248\n" \
"ds_read_b64_tr_b16 a[62:63], v8 offset:53760\n" \
"ds_read_b64_tr_b16 a[188:189], v9 offset:53248\n" \
"ds_read_b64_tr_b16 a[190:191], v9 offset:53760\n" \
"s_waitcnt lgkmcnt(0)\n" \
"s_barrier\n" \
"s_add_u32 m0, s52, 114688\n" \
"s_nop 0\n" \
"buffer_load_dwordx4 v1, s[40:43], s54 offen nt lds\n" \
"s_add_u32 s54, s54, s44\n" \
"s_add_u32 m0, s52, 116736\n" \
"s_nop 0\n" \
"buffer_load_dwordx4 v1, s[40:43], s54 offen nt lds\n" \
"s_add_u32 s54, s54, s44\n" \
"s_add_u32 m0, s52, 118784\n" \
"s_nop 0\n" \
"buffer_load_dwordx4 v1, s[40:43], s54 offen nt lds\n" \
"s_add_u32 s54, s54, s44\n" \
"s_add_u32 m0, s52, 120832\n" \
"s_nop 0\n" \
"buffer_load_dwordx4 v1, s[40:43], s54 offen nt lds\n" \
"s_add_u32 s54, s54, s44\n" \
"s_waitcnt vmcnt(28)\n" \
"s_barrier\n" \
"ds_read_b64_tr_b16 a[64:65], v2 offset:0\n" \
"ds_read_b64_tr_b16 a[66:67], v2 offset:512\n" \
"ds_read_b64_tr_b16 a[192:193], v3 offset:0\n" \
"ds_read_b64_tr_b16 a[194:195], v3 offset:512\n" \
"ds_read_b64_tr_b16 a[68:69], v2 offset:4096\n" \
"ds_read_b64_tr_b16 a[70:71], v2 offset:4608\n" \
"ds_read_b64_tr_b16 a[196:197], v3 offset:4096\n" \
"ds_read_b64_tr_b16 a[198:199], v3 offset:4608\n" \
"s_waitcnt vmcnt(24)\n" \
"s_barrier\n" \
"ds_read_b64_tr_b16 a[72:73], v2 offset:16384\n" \
"ds_read_b64_tr_b16 a[74:75], v2 offset:16896\n" \
"ds_read_b64_tr_b16 a[200:201], v3 offset:16384\n" \
"ds_read_b64_tr_b16 a[202:203], v3 offset:16896\n" \
"ds_read_b64_tr_b16 a[76:77], v2 offset:20480\n" \
"ds_read_b64_tr_b16 a[78:79], v2 offset:20992\n" \
"ds_read_b64_tr_b16 a[204:205], v3 offset:20480\n" \
"ds_read_b64_tr_b16 a[206:207], v3 offset:20992\n" \
"s_waitcnt vmcnt(20)\n" \
"s_barrier\n" \
"ds_read_b64_tr_b16 a[80:81], v2 offset:32768\n" \
"ds_read_b64_tr_b16 a[82:83], v2 offset:33280\n" \
"ds_read_b64_tr_b16 a[208:209], v3 offset:32768\n" \
"ds_read_b64_tr_b16 a[210:211], v3 offset:33280\n" \
"ds_read_b64_tr_b16 a[84:85], v2 offset:36864\n" \
"ds_read_b64_tr_b16 a[86:87], v2 offset:37376\n" \
"ds_read_b64_tr_b16 a[212:213], v3 offset:36864\n" \
"ds_read_b64_tr_b16 a[214:215], v3 offset:37376\n" \
"s_waitcnt vmcnt(16)\n" \
"s_barrier\n" \
"ds_read_b64_tr_b16 a[88:89], v2 offset:49152\n" \
"ds_read_b64_tr_b16 a[90:91], v2 offset:49664\n" \
"ds_read_b64_tr_b16 a[216:217], v3 offset:49152\n" \
"ds_read_b64_tr_b16 a[218:219], v3 offset:49664\n" \
"ds_read_b64_tr_b16 a[92:93], v2 offset:53248\n" \
"ds_read_b64_tr_b16 a[94:95], v2 offset:53760\n" \
"ds_read_b64_tr_b16 a[220:221], v3 offset:53248\n" \
"ds_read_b64_tr_b16 a[222:223], v3 offset:53760\n" \
"s_waitcnt vmcnt(12)\n" \
"s_barrier\n" \
"ds_read_b64_tr_b16 a[96:97], v8 offset:0\n" \
"ds_read_b64_tr_b16 a[98:99], v8 offset:512\n" \
"ds_read_b64_tr_b16 a[224:225], v9 offset:0\n" \
"ds_read_b64_tr_b16 a[226:227], v9 offset:512\n" \
"ds_read_b64_tr_b16 a[100:101], v8 offset:4096\n" \
"ds_read_b64_tr_b16 a[102:103], v8 offset:4608\n" \
"ds_read_b64_tr_b16 a[228:229], v9 offset:4096\n" \
"ds_read_b64_tr_b16 a[230:231], v9 offset:4608\n" \
"s_waitcnt vmcnt(8)\n" \
"s_barrier\n" \
"ds_read_b64_tr_b16 a[104:105], v8 offset:16384\n" \
"ds_read_b64_tr_b16 a[106:107], v8 offset:16896\n" \
"ds_read_b64_tr_b16 a[232:233], v9 offset:16384\n" \
"ds_read_b64_tr_b16 a[234:235], v9 offset:16896\n" \
"ds_read_b64_tr_b16 a[108:109], v8 offset:20480\n" \
"ds_read_b64_tr_b16 a[110:111], v8 offset:20992\n" \
"ds_read_b64_tr_b16 a[236:237], v9 offset:20480\n" \
"ds_read_b64_tr_b16 a[238:239], v9 offset:20992\n" \
"s_waitcnt vmcnt(4)\n" \
"s_barrier\n" \
"ds_read_b64_tr_b16 a[112:113], v8 offset:32768\n" \
"ds_read_b64_tr_b16 a[114:115], v8 offset:33280\n" \
"ds_read_b64_tr_b16 a[240:241], v9 offset:32768\n" \
"ds_read_b64_tr_b16 a[242:243], v9 offset:33280\n" \
"ds_read_b64_tr_b16 a[116:117], v8 offset:36864\n" \
"ds_read_b64_tr_b16 a[118:119], v8 offset:37376\n" \
"ds_read_b64_tr_b16 a[244:245], v9 offset:36864\n" \
"ds_read_b64_tr_b16 a[246:247], v9 offset:37376\n" \
"s_waitcnt vmcnt(0)\n" \
"s_barrier\n" \
"ds_read_b64_tr_b16 a[120:121], v8 offset:49152\n" \
"ds_read_b64_tr_b16 a[122:123], v8 offset:49664\n" \
"ds_read_b64_tr_b16 a[248:249], v9 offset:49152\n" \
"ds_read_b64_tr_b16 a[250:251], v9 offset:49664\n" \
"ds_read_b64_tr_b16 a[124:125], v8 offset:53248\n" \
"ds_read_b64_tr_b16 a[126:127], v8 offset:53760\n" \
"ds_read_b64_tr_b16 a[252:253], v9 offset:53248\n" \
"ds_read_b64_tr_b16 a[254:255], v9 offset:53760\n" \
"s_waitcnt lgkmcnt(0)\n" \
"s_barrier\n" \
"v_mov_b32 v14, 4096\n" \
"s_mov_b32 s59, s46\n" \
"s_add_u32 s63, s59, 0\n" \
"s_min_u32 s63, s63, 63\n" \
"s_lshr_b32 s63, s63, 3\n" \
"s_lshl_b32 s63, s63, 7\n" \
"s_branch .Lp1qk_l0_%=\n" \
".Lp1qk_l0k_%=:\n" \
"s_add_u32 s63, s59, 1\n" \
"s_min_u32 s63, s63, 63\n" \
"s_lshr_b32 s63, s63, 3\n" \
"s_lshl_b32 s63, s63, 7\n" \
"s_branch .Lp1qk_l1_%=\n" \
".Lp1qk_l1k_%=:\n" \
"s_lshl_b32 s55, s48, 13\n" \
"s_lshl_b32 s56, s46, 17\n" \
"s_add_u32 s56, s56, s45\n" \
"s_lshl_b32 s60, s47, 17\n" \
"s_sub_u32 s60, s60, 0x20000\n" \
"s_add_u32 s60, s60, s45\n" \
"s_mov_b32 s57, 0\n" \
"s_mov_b32 s58, 0\n" \
"v_mov_b32 v7, v5\n" \
"v_mov_b32 v5, 0x7ffffff0\n" \
"v_mov_b32 v10, v0\n" \
"v_mov_b32 v11, v6\n" \
"v_mov_b32 v6, 0x7ffffff0\n" \
"s_mov_b32 s59, s46\n" \
"s_min_u32 s61, s56, s60\n" \
"s_lshr_b32 s65, s61, 8\n" \
"s_and_b32 s65, s65, 0x2000\n" \
"s_xor_b32 s61, s61, s65\n" \
"s_add_u32 m0, s55, 0\n" \
"s_nop 0\n" \
"buffer_load_dwordx4 v4, s[36:39], s61 offen offset:0 sc1 lds\n" \
"buffer_load_dwordx4 v4, s[36:39], s61 offen offset:1024 sc1 lds\n" \
"buffer_load_dwordx4 v4, s[36:39], s61 offen offset:2048 sc1 lds\n" \
"buffer_load_dwordx4 v4, s[36:39], s61 offen offset:3072 sc1 lds\n" \
"s_add_u32 m0, s55, 4096\n" \
"s_add_u32 s61, s61, 4096\n" \
"buffer_load_dwordx4 v4, s[36:39], s61 offen offset:0 sc1 lds\n" \
"buffer_load_dwordx4 v4, s[36:39], s61 offen offset:1024 sc1 lds\n" \
"buffer_load_dwordx4 v4, s[36:39], s61 offen offset:2048 sc1 lds\n" \
"buffer_load_dwordx4 v4, s[36:39], s61 offen offset:3072 sc1 lds\n" \
"s_add_u32 s61, s61, 28672\n" \
"s_add_u32 s56, s56, 0x20000\n" \
"s_add_u32 m0, s55, 32768\n" \
"s_nop 0\n" \
"buffer_load_dwordx4 v4, s[36:39], s61 offen offset:0 sc1 lds\n" \
"buffer_load_dwordx4 v4, s[36:39], s61 offen offset:1024 sc1 lds\n" \
"buffer_load_dwordx4 v4, s[36:39], s61 offen offset:2048 sc1 lds\n" \
"buffer_load_dwordx4 v4, s[36:39], s61 offen offset:3072 sc1 lds\n" \
"s_add_u32 m0, s55, 36864\n" \
"s_add_u32 s61, s61, 4096\n" \
"buffer_load_dwordx4 v4, s[36:39], s61 offen offset:0 sc1 lds\n" \
"buffer_load_dwordx4 v4, s[36:39], s61 offen offset:1024 sc1 lds\n" \
"buffer_load_dwordx4 v4, s[36:39], s61 offen offset:2048 sc1 lds\n" \
"buffer_load_dwordx4 v4, s[36:39], s61 offen offset:3072 sc1 lds\n" \
"s_mov_b32 s70, 0\n" \
"s_mov_b32 s72, 0\n" \
"s_waitcnt vmcnt(0) lgkmcnt(0)\n" \
"s_barrier\n" \
"v_add_u32 v12, 0x10000, v10\n" \
"ds_read_b128 v[16:19], v10 offset:0\n" \
"ds_read_b128 v[20:23], v10 offset:16384\n" \
"ds_read_b128 v[24:27], v10 offset:1024\n" \
"ds_read_b128 v[28:31], v10 offset:17408\n" \
"ds_read_b128 v[32:35], v10 offset:2048\n" \
"ds_read_b128 v[36:39], v10 offset:18432\n" \
"s_waitcnt lgkmcnt(4)\n" \
"s_nop 7\n" \
"s_nop 7\n" \
".p2align 6\n" \
".Lp1qk_loop_%=:\n" \
"v_mfma_f32_16x16x32_bf16 v[48:51], a[0:3], v[16:19], 0\n" \
"s_min_u32 s61, s56, s60\n" \
"s_lshr_b32 s65, s61, 8\n" \
"s_and_b32 s65, s65, 0x2000\n" \
"s_xor_b32 s61, s61, s65\n" \
"s_add_u32 m0, s55, 65536\n" \
"s_nop 0\n" \
"buffer_load_dwordx4 v4, s[36:39], s61 offen offset:0 sc1 lds\n" \
"v_mov_b32_e64 v13, s59\n" \
"v_mfma_f32_16x16x32_bf16 v[52:55], a[0:3], v[20:23], 0\n" \
"ds_read_b128 v[40:43], v10 offset:3072\n" \
"s_nop 0\n" \
"v_mfma_f32_16x16x32_bf16 v[56:59], a[128:131], v[16:19], 0\n" \
"ds_read_b128 v[44:47], v10 offset:19456\n" \
"s_nop 0\n" \
"v_mfma_f32_16x16x32_bf16 v[60:63], a[128:131], v[20:23], 0\n" \
"s_waitcnt lgkmcnt(4)\n" \
"v_mul_f32 v64, s51, v64\n" \
"v_mul_f32_e64 v65, s51, v65\n" \
"v_mfma_f32_16x16x32_bf16 v[48:51], a[4:7], v[24:27], v[48:51]\n" \
"buffer_load_dwordx4 v4, s[36:39], s61 offen offset:1024 sc1 lds\n" \
"v_mfma_f32_16x16x32_bf16 v[52:55], a[4:7], v[28:31], v[52:55]\n" \
"ds_read_b128 v[16:19], v10 offset:4096\n" \
"v_mul_f32_e64 v66, s51, v66\n" \
"v_mfma_f32_16x16x32_bf16 v[56:59], a[132:135], v[24:27], v[56:59]\n" \
"ds_read_b128 v[20:23], v10 offset:20480\n" \
"v_mul_f32_e64 v67, s51, v67\n" \
"v_mfma_f32_16x16x32_bf16 v[60:63], a[132:135], v[28:31], v[60:63]\n" \
"s_waitcnt lgkmcnt(4)\n" \
"v_mul_f32 v72, s51, v72\n" \
"v_mul_f32_e64 v73, s51, v73\n" \
"v_mfma_f32_16x16x32_bf16 v[48:51], a[8:11], v[32:35], v[48:51]\n" \
"s_add_u32 s63, s59, 2\n" \
"s_min_u32 s63, s63, 63\n" \
"v_mfma_f32_16x16x32_bf16 v[52:55], a[8:11], v[36:39], v[52:55]\n" \
"ds_read_b128 v[24:27], v10 offset:5120\n" \
"s_lshr_b32 s63, s63, 3\n" \
"s_lshl_b32 s63, s63, 7\n" \
"v_mul_f32_e64 v74, s51, v74\n" \
"v_mfma_f32_16x16x32_bf16 v[56:59], a[136:139], v[32:35], v[56:59]\n" \
"ds_read_b128 v[28:31], v10 offset:21504\n" \
"v_mul_f32_e64 v75, s51, v75\n" \
"v_mfma_f32_16x16x32_bf16 v[60:63], a[136:139], v[36:39], v[60:63]\n" \
"s_waitcnt lgkmcnt(4)\n" \
"v_cvt_pk_bf16_f32 v80, v64, v65\n" \
"v_cvt_pk_bf16_f32 v81, v66, v67\n" \
"v_mfma_f32_16x16x32_bf16 v[48:51], a[12:15], v[40:43], v[48:51]\n" \
"buffer_load_dwordx4 v4, s[36:39], s61 offen offset:2048 sc1 lds\n" \
"v_mfma_f32_16x16x32_bf16 v[52:55], a[12:15], v[44:47], v[52:55]\n" \
"ds_read_b128 v[32:35], v10 offset:6144\n" \
"v_cvt_pk_bf16_f32 v82, v72, v73\n" \
"v_mfma_f32_16x16x32_bf16 v[56:59], a[140:143], v[40:43], v[56:59]\n" \
"ds_read_b128 v[36:39], v10 offset:22528\n" \
"v_cvt_pk_bf16_f32 v83, v74, v75\n" \
"v_mfma_f32_16x16x32_bf16 v[60:63], a[140:143], v[44:47], v[60:63]\n" \
"s_waitcnt lgkmcnt(4)\n" \
"buffer_store_dwordx4 v[80:83], v5, s[36:39], s57 offen sc1\n" \
"v_mul_f32_e64 v68, s51, v68\n" \
"v_mfma_f32_16x16x32_bf16 v[48:51], a[16:19], v[16:19], v[48:51]\n" \
"buffer_load_dwordx4 v4, s[36:39], s61 offen offset:3072 sc1 lds\n" \
"v_mfma_f32_16x16x32_bf16 v[52:55], a[16:19], v[20:23], v[52:55]\n" \
"ds_read_b128 v[40:43], v10 offset:7168\n" \
"v_mul_f32_e64 v69, s51, v69\n" \
"v_mfma_f32_16x16x32_bf16 v[56:59], a[144:147], v[16:19], v[56:59]\n" \
"ds_read_b128 v[44:47], v10 offset:23552\n" \
"v_mul_f32_e64 v70, s51, v70\n" \
"v_mfma_f32_16x16x32_bf16 v[60:63], a[144:147], v[20:23], v[60:63]\n" \
"s_waitcnt lgkmcnt(4)\n" \
"v_mul_f32 v71, s51, v71\n" \
"v_mul_f32_e64 v76, s51, v76\n" \
"v_mfma_f32_16x16x32_bf16 v[48:51], a[20:23], v[24:27], v[48:51]\n" \
"buffer_load_dword v15, v14, s[36:39], s63 offen sc1\n" \
"v_mfma_f32_16x16x32_bf16 v[52:55], a[20:23], v[28:31], v[52:55]\n" \
"ds_read_b128 v[16:19], v10 offset:8192\n" \
"v_mul_f32_e64 v77, s51, v77\n" \
"v_mfma_f32_16x16x32_bf16 v[56:59], a[148:151], v[24:27], v[56:59]\n" \
"ds_read_b128 v[20:23], v10 offset:24576\n" \
"v_mul_f32_e64 v78, s51, v78\n" \
"v_mfma_f32_16x16x32_bf16 v[60:63], a[148:151], v[28:31], v[60:63]\n" \
"s_waitcnt lgkmcnt(4)\n" \
"v_mul_f32 v79, s51, v79\n" \
"v_cvt_pk_bf16_f32 v84, v68, v69\n" \
"v_mfma_f32_16x16x32_bf16 v[48:51], a[24:27], v[32:35], v[48:51]\n" \
"s_add_u32 m0, s55, 69632\n" \
"s_add_u32 s61, s61, 4096\n" \
"buffer_load_dwordx4 v4, s[36:39], s61 offen offset:0 sc1 lds\n" \
"v_mfma_f32_16x16x32_bf16 v[52:55], a[24:27], v[36:39], v[52:55]\n" \
"ds_read_b128 v[24:27], v10 offset:9216\n" \
"v_cvt_pk_bf16_f32 v85, v70, v71\n" \
"v_mfma_f32_16x16x32_bf16 v[56:59], a[152:155], v[32:35], v[56:59]\n" \
"ds_read_b128 v[28:31], v10 offset:25600\n" \
"v_cvt_pk_bf16_f32 v86, v76, v77\n" \
"v_mfma_f32_16x16x32_bf16 v[60:63], a[152:155], v[36:39], v[60:63]\n" \
"s_waitcnt lgkmcnt(4)\n" \
"v_cvt_pk_bf16_f32 v87, v78, v79\n" \
"buffer_store_dwordx4 v[84:87], v5, s[36:39], s58 offen sc1\n" \
"v_mfma_f32_16x16x32_bf16 v[48:51], a[28:31], v[40:43], v[48:51]\n" \
"buffer_load_dwordx4 v4, s[36:39], s61 offen offset:1024 sc1 lds\n" \
"v_mfma_f32_16x16x32_bf16 v[52:55], a[28:31], v[44:47], v[52:55]\n" \
"ds_read_b128 v[32:35], v10 offset:10240\n" \
"v_mfma_f32_16x16x32_bf16 v[56:59], a[156:159], v[40:43], v[56:59]\n" \
"ds_read_b128 v[36:39], v10 offset:26624\n" \
"v_mfma_f32_16x16x32_bf16 v[60:63], a[156:159], v[44:47], v[60:63]\n" \
"s_waitcnt lgkmcnt(4)\n" \
"v_mfma_f32_16x16x32_bf16 v[48:51], a[32:35], v[16:19], v[48:51]\n" \
"v_mfma_f32_16x16x32_bf16 v[52:55], a[32:35], v[20:23], v[52:55]\n" \
"ds_read_b128 v[40:43], v10 offset:11264\n" \
"v_mfma_f32_16x16x32_bf16 v[56:59], a[160:163], v[16:19], v[56:59]\n" \
"ds_read_b128 v[44:47], v10 offset:27648\n" \
"v_mfma_f32_16x16x32_bf16 v[60:63], a[160:163], v[20:23], v[60:63]\n" \
"s_waitcnt lgkmcnt(4)\n" \
"v_mfma_f32_16x16x32_bf16 v[48:51], a[36:39], v[24:27], v[48:51]\n" \
"buffer_load_dwordx4 v4, s[36:39], s61 offen offset:2048 sc1 lds\n" \
"v_mfma_f32_16x16x32_bf16 v[52:55], a[36:39], v[28:31], v[52:55]\n" \
"ds_read_b128 v[16:19], v10 offset:12288\n" \
"v_mfma_f32_16x16x32_bf16 v[56:59], a[164:167], v[24:27], v[56:59]\n" \
"ds_read_b128 v[20:23], v10 offset:28672\n" \
"v_mfma_f32_16x16x32_bf16 v[60:63], a[164:167], v[28:31], v[60:63]\n" \
"s_waitcnt lgkmcnt(4)\n" \
"v_mfma_f32_16x16x32_bf16 v[48:51], a[40:43], v[32:35], v[48:51]\n" \
"buffer_load_dwordx4 v4, s[36:39], s61 offen offset:3072 sc1 lds\n" \
"v_mfma_f32_16x16x32_bf16 v[52:55], a[40:43], v[36:39], v[52:55]\n" \
"ds_read_b128 v[24:27], v10 offset:13312\n" \
"v_mfma_f32_16x16x32_bf16 v[56:59], a[168:171], v[32:35], v[56:59]\n" \
"ds_read_b128 v[28:31], v10 offset:29696\n" \
"v_mfma_f32_16x16x32_bf16 v[60:63], a[168:171], v[36:39], v[60:63]\n" \
"s_waitcnt lgkmcnt(4)\n" \
"v_mfma_f32_16x16x32_bf16 v[48:51], a[44:47], v[40:43], v[48:51]\n" \
"v_mfma_f32_16x16x32_bf16 v[52:55], a[44:47], v[44:47], v[52:55]\n" \
"ds_read_b128 v[32:35], v10 offset:14336\n" \
"v_mfma_f32_16x16x32_bf16 v[56:59], a[172:175], v[40:43], v[56:59]\n" \
"ds_read_b128 v[36:39], v10 offset:30720\n" \
"v_mfma_f32_16x16x32_bf16 v[60:63], a[172:175], v[44:47], v[60:63]\n" \
"s_waitcnt lgkmcnt(4)\n" \
"v_mfma_f32_16x16x32_bf16 v[48:51], a[48:51], v[16:19], v[48:51]\n" \
"v_mfma_f32_16x16x32_bf16 v[52:55], a[48:51], v[20:23], v[52:55]\n" \
"ds_read_b128 v[40:43], v10 offset:15360\n" \
"v_mfma_f32_16x16x32_bf16 v[56:59], a[176:179], v[16:19], v[56:59]\n" \
"ds_read_b128 v[44:47], v10 offset:31744\n" \
"v_mfma_f32_16x16x32_bf16 v[60:63], a[176:179], v[20:23], v[60:63]\n" \
"s_waitcnt lgkmcnt(4)\n" \
"v_mfma_f32_16x16x32_bf16 v[48:51], a[52:55], v[24:27], v[48:51]\n" \
"s_waitcnt vmcnt(12)\n" \
"s_barrier\n" \
"v_mfma_f32_16x16x32_bf16 v[52:55], a[52:55], v[28:31], v[52:55]\n" \
"ds_read_b128 v[16:19], v10 offset:32768\n" \
"v_mfma_f32_16x16x32_bf16 v[56:59], a[180:183], v[24:27], v[56:59]\n" \
"ds_read_b128 v[20:23], v10 offset:49152\n" \
"v_mfma_f32_16x16x32_bf16 v[60:63], a[180:183], v[28:31], v[60:63]\n" \
"s_waitcnt lgkmcnt(4)\n" \
"v_mfma_f32_16x16x32_bf16 v[48:51], a[56:59], v[32:35], v[48:51]\n" \
"v_mfma_f32_16x16x32_bf16 v[52:55], a[56:59], v[36:39], v[52:55]\n" \
"ds_read_b128 v[24:27], v10 offset:33792\n" \
"v_mfma_f32_16x16x32_bf16 v[56:59], a[184:187], v[32:35], v[56:59]\n" \
"ds_read_b128 v[28:31], v10 offset:50176\n" \
"v_mfma_f32_16x16x32_bf16 v[60:63], a[184:187], v[36:39], v[60:63]\n" \
"s_waitcnt lgkmcnt(4)\n" \
"v_mfma_f32_16x16x32_bf16 v[48:51], a[60:63], v[40:43], v[48:51]\n" \
"v_mfma_f32_16x16x32_bf16 v[52:55], a[60:63], v[44:47], v[52:55]\n" \
"ds_read_b128 v[32:35], v10 offset:34816\n" \
"v_mfma_f32_16x16x32_bf16 v[56:59], a[188:191], v[40:43], v[56:59]\n" \
"ds_read_b128 v[36:39], v10 offset:51200\n" \
"v_mfma_f32_16x16x32_bf16 v[60:63], a[188:191], v[44:47], v[60:63]\n" \
"s_waitcnt lgkmcnt(4)\n" \
"v_mfma_f32_16x16x32_bf16 v[48:51], a[64:67], v[16:19], v[48:51]\n" \
"s_add_u32 s61, s61, 28672\n" \
"s_add_u32 s56, s56, 0x20000\n" \
"s_add_u32 m0, s55, 98304\n" \
"s_nop 0\n" \
"buffer_load_dwordx4 v4, s[36:39], s61 offen offset:0 sc1 lds\n" \
"v_mfma_f32_16x16x32_bf16 v[52:55], a[64:67], v[20:23], v[52:55]\n" \
"ds_read_b128 v[40:43], v10 offset:35840\n" \
"v_mfma_f32_16x16x32_bf16 v[56:59], a[192:195], v[16:19], v[56:59]\n" \
"ds_read_b128 v[44:47], v10 offset:52224\n" \
"v_mfma_f32_16x16x32_bf16 v[60:63], a[192:195], v[20:23], v[60:63]\n" \
"s_waitcnt lgkmcnt(4)\n" \
"v_mfma_f32_16x16x32_bf16 v[48:51], a[68:71], v[24:27], v[48:51]\n" \
"buffer_load_dwordx4 v4, s[36:39], s61 offen offset:1024 sc1 lds\n" \
"v_mfma_f32_16x16x32_bf16 v[52:55], a[68:71], v[28:31], v[52:55]\n" \
"ds_read_b128 v[16:19], v10 offset:36864\n" \
"v_mfma_f32_16x16x32_bf16 v[56:59], a[196:199], v[24:27], v[56:59]\n" \
"ds_read_b128 v[20:23], v10 offset:53248\n" \
"v_mfma_f32_16x16x32_bf16 v[60:63], a[196:199], v[28:31], v[60:63]\n" \
"s_waitcnt lgkmcnt(4)\n" \
"v_mfma_f32_16x16x32_bf16 v[48:51], a[72:75], v[32:35], v[48:51]\n" \
"v_mfma_f32_16x16x32_bf16 v[52:55], a[72:75], v[36:39], v[52:55]\n" \
"ds_read_b128 v[24:27], v10 offset:37888\n" \
"v_mfma_f32_16x16x32_bf16 v[56:59], a[200:203], v[32:35], v[56:59]\n" \
"ds_read_b128 v[28:31], v10 offset:54272\n" \
"v_mfma_f32_16x16x32_bf16 v[60:63], a[200:203], v[36:39], v[60:63]\n" \
"s_waitcnt lgkmcnt(4)\n" \
"v_mfma_f32_16x16x32_bf16 v[48:51], a[76:79], v[40:43], v[48:51]\n" \
"buffer_load_dwordx4 v4, s[36:39], s61 offen offset:2048 sc1 lds\n" \
"v_mfma_f32_16x16x32_bf16 v[52:55], a[76:79], v[44:47], v[52:55]\n" \
"ds_read_b128 v[32:35], v10 offset:38912\n" \
"v_mfma_f32_16x16x32_bf16 v[56:59], a[204:207], v[40:43], v[56:59]\n" \
"ds_read_b128 v[36:39], v10 offset:55296\n" \
"v_mfma_f32_16x16x32_bf16 v[60:63], a[204:207], v[44:47], v[60:63]\n" \
"s_waitcnt lgkmcnt(4)\n" \
"v_mfma_f32_16x16x32_bf16 v[48:51], a[80:83], v[16:19], v[48:51]\n" \
"buffer_load_dwordx4 v4, s[36:39], s61 offen offset:3072 sc1 lds\n" \
"v_mfma_f32_16x16x32_bf16 v[52:55], a[80:83], v[20:23], v[52:55]\n" \
"ds_read_b128 v[40:43], v10 offset:39936\n" \
"v_mfma_f32_16x16x32_bf16 v[56:59], a[208:211], v[16:19], v[56:59]\n" \
"ds_read_b128 v[44:47], v10 offset:56320\n" \
"v_mfma_f32_16x16x32_bf16 v[60:63], a[208:211], v[20:23], v[60:63]\n" \
"s_waitcnt lgkmcnt(4)\n" \
"v_mfma_f32_16x16x32_bf16 v[48:51], a[84:87], v[24:27], v[48:51]\n" \
"v_mfma_f32_16x16x32_bf16 v[52:55], a[84:87], v[28:31], v[52:55]\n" \
"ds_read_b128 v[16:19], v10 offset:40960\n" \
"v_mfma_f32_16x16x32_bf16 v[56:59], a[212:215], v[24:27], v[56:59]\n" \
"ds_read_b128 v[20:23], v10 offset:57344\n" \
"v_mfma_f32_16x16x32_bf16 v[60:63], a[212:215], v[28:31], v[60:63]\n" \
"s_waitcnt lgkmcnt(4)\n" \
"v_mfma_f32_16x16x32_bf16 v[48:51], a[88:91], v[32:35], v[48:51]\n" \
"s_add_u32 m0, s55, 102400\n" \
"s_add_u32 s61, s61, 4096\n" \
"buffer_load_dwordx4 v4, s[36:39], s61 offen offset:0 sc1 lds\n" \
"v_mfma_f32_16x16x32_bf16 v[52:55], a[88:91], v[36:39], v[52:55]\n" \
"ds_read_b128 v[24:27], v10 offset:41984\n" \
"v_mfma_f32_16x16x32_bf16 v[56:59], a[216:219], v[32:35], v[56:59]\n" \
"ds_read_b128 v[28:31], v10 offset:58368\n" \
"v_mfma_f32_16x16x32_bf16 v[60:63], a[216:219], v[36:39], v[60:63]\n" \
"s_waitcnt lgkmcnt(4)\n" \
"v_mfma_f32_16x16x32_bf16 v[48:51], a[92:95], v[40:43], v[48:51]\n" \
"buffer_load_dwordx4 v4, s[36:39], s61 offen offset:1024 sc1 lds\n" \
"v_mfma_f32_16x16x32_bf16 v[52:55], a[92:95], v[44:47], v[52:55]\n" \
"ds_read_b128 v[32:35], v10 offset:43008\n" \
"v_mfma_f32_16x16x32_bf16 v[56:59], a[220:223], v[40:43], v[56:59]\n" \
"ds_read_b128 v[36:39], v10 offset:59392\n" \
"v_mfma_f32_16x16x32_bf16 v[60:63], a[220:223], v[44:47], v[60:63]\n" \
"s_waitcnt lgkmcnt(4)\n" \
"v_mfma_f32_16x16x32_bf16 v[48:51], a[96:99], v[16:19], v[48:51]\n" \
"v_mfma_f32_16x16x32_bf16 v[52:55], a[96:99], v[20:23], v[52:55]\n" \
"ds_read_b128 v[40:43], v10 offset:44032\n" \
"v_mfma_f32_16x16x32_bf16 v[56:59], a[224:227], v[16:19], v[56:59]\n" \
"ds_read_b128 v[44:47], v10 offset:60416\n" \
"v_mfma_f32_16x16x32_bf16 v[60:63], a[224:227], v[20:23], v[60:63]\n" \
"s_waitcnt lgkmcnt(4)\n" \
"v_mfma_f32_16x16x32_bf16 v[48:51], a[100:103], v[24:27], v[48:51]\n" \
"buffer_load_dwordx4 v4, s[36:39], s61 offen offset:2048 sc1 lds\n" \
"v_mfma_f32_16x16x32_bf16 v[52:55], a[100:103], v[28:31], v[52:55]\n" \
"ds_read_b128 v[16:19], v10 offset:45056\n" \
"v_mfma_f32_16x16x32_bf16 v[56:59], a[228:231], v[24:27], v[56:59]\n" \
"ds_read_b128 v[20:23], v10 offset:61440\n" \
"v_mfma_f32_16x16x32_bf16 v[60:63], a[228:231], v[28:31], v[60:63]\n" \
"s_waitcnt lgkmcnt(4)\n" \
"v_mfma_f32_16x16x32_bf16 v[48:51], a[104:107], v[32:35], v[48:51]\n" \
"buffer_load_dwordx4 v4, s[36:39], s61 offen offset:3072 sc1 lds\n" \
"v_mfma_f32_16x16x32_bf16 v[52:55], a[104:107], v[36:39], v[52:55]\n" \
"ds_read_b128 v[24:27], v10 offset:46080\n" \
"v_mfma_f32_16x16x32_bf16 v[56:59], a[232:235], v[32:35], v[56:59]\n" \
"ds_read_b128 v[28:31], v10 offset:62464\n" \
"v_mfma_f32_16x16x32_bf16 v[60:63], a[232:235], v[36:39], v[60:63]\n" \
"s_waitcnt lgkmcnt(4)\n" \
"v_mfma_f32_16x16x32_bf16 v[48:51], a[108:111], v[40:43], v[48:51]\n" \
"s_lshl_b32 s66, s59, s49\n" \
"s_lshr_b32 s65, s66, 8\n" \
"v_mfma_f32_16x16x32_bf16 v[52:55], a[108:111], v[44:47], v[52:55]\n" \
"ds_read_b128 v[32:35], v10 offset:47104\n" \
"v_mfma_f32_16x16x32_bf16 v[56:59], a[236:239], v[40:43], v[56:59]\n" \
"ds_read_b128 v[36:39], v10 offset:63488\n" \
"v_mfma_f32_16x16x32_bf16 v[60:63], a[236:239], v[44:47], v[60:63]\n" \
"s_waitcnt lgkmcnt(4)\n" \
"s_and_b32 s65, s65, 0x2000\n" \
"s_xor_b32 s66, s66, s65\n" \
"v_mfma_f32_16x16x32_bf16 v[48:51], a[112:115], v[16:19], v[48:51]\n" \
"v_mfma_f32_16x16x32_bf16 v[52:55], a[112:115], v[20:23], v[52:55]\n" \
"ds_read_b128 v[40:43], v10 offset:48128\n" \
"v_mfma_f32_16x16x32_bf16 v[56:59], a[240:243], v[16:19], v[56:59]\n" \
"ds_read_b128 v[44:47], v10 offset:64512\n" \
"v_mfma_f32_16x16x32_bf16 v[60:63], a[240:243], v[20:23], v[60:63]\n" \
"s_waitcnt lgkmcnt(4)\n" \
"v_mfma_f32_16x16x32_bf16 v[48:51], a[116:119], v[24:27], v[48:51]\n" \
"s_waitcnt vmcnt(8)\n" \
"s_barrier\n" \
"v_mfma_f32_16x16x32_bf16 v[52:55], a[116:119], v[28:31], v[52:55]\n" \
"ds_read_b128 v[16:19], v12 offset:0\n" \
"v_mfma_f32_16x16x32_bf16 v[56:59], a[244:247], v[24:27], v[56:59]\n" \
"ds_read_b128 v[20:23], v12 offset:16384\n" \
"v_mfma_f32_16x16x32_bf16 v[60:63], a[244:247], v[28:31], v[60:63]\n" \
"s_waitcnt lgkmcnt(4)\n" \
"buffer_store_dword v13, v6, s[36:39], 0 offen sc1\n" \
"v_mfma_f32_16x16x32_bf16 v[48:51], a[120:123], v[32:35], v[48:51]\n" \
"v_cmp_gt_u32_e64 vcc, 16, v15\n" \
"v_mfma_f32_16x16x32_bf16 v[52:55], a[120:123], v[36:39], v[52:55]\n" \
"ds_read_b128 v[24:27], v12 offset:1024\n" \
"v_mfma_f32_16x16x32_bf16 v[56:59], a[248:251], v[32:35], v[56:59]\n" \
"ds_read_b128 v[28:31], v12 offset:17408\n" \
"v_mfma_f32_16x16x32_bf16 v[60:63], a[248:251], v[36:39], v[60:63]\n" \
"s_waitcnt lgkmcnt(4)\n" \
"v_mfma_f32_16x16x32_bf16 v[48:51], a[124:127], v[40:43], v[48:51]\n" \
"s_cbranch_vccnz .Lp1qk_m0_%=\n" \
".Lp1qk_m0k_%=:\n" \
"v_mfma_f32_16x16x32_bf16 v[52:55], a[124:127], v[44:47], v[52:55]\n" \
"ds_read_b128 v[32:35], v12 offset:2048\n" \
"v_mfma_f32_16x16x32_bf16 v[56:59], a[252:255], v[40:43], v[56:59]\n" \
"ds_read_b128 v[36:39], v12 offset:18432\n" \
"v_mfma_f32_16x16x32_bf16 v[60:63], a[252:255], v[44:47], v[60:63]\n" \
"s_waitcnt lgkmcnt(4)\n" \
"s_xor_b32 s57, s66, s64\n" \
"s_add_u32 s58, s57, s50\n" \
"s_add_u32 s59, s59, 1\n" \
"v_mov_b32_e64 v5, v7\n" \
"v_mov_b32_e64 v6, v11\n" \
"s_cmp_ge_u32 s59, s47\n" \
"s_nop 0\n" \
"s_cbranch_scc1 .Lp1qk_out0_%=\n" \
"v_mfma_f32_16x16x32_bf16 v[64:67], a[0:3], v[16:19], 0\n" \
"s_min_u32 s61, s56, s60\n" \
"s_lshr_b32 s65, s61, 8\n" \
"s_and_b32 s65, s65, 0x2000\n" \
"s_xor_b32 s61, s61, s65\n" \
"s_add_u32 m0, s55, 0\n" \
"s_nop 0\n" \
"buffer_load_dwordx4 v4, s[36:39], s61 offen offset:0 sc1 lds\n" \
"v_mov_b32_e64 v13, s59\n" \
"v_mfma_f32_16x16x32_bf16 v[68:71], a[0:3], v[20:23], 0\n" \
"ds_read_b128 v[40:43], v12 offset:3072\n" \
"s_nop 0\n" \
"v_mfma_f32_16x16x32_bf16 v[72:75], a[128:131], v[16:19], 0\n" \
"ds_read_b128 v[44:47], v12 offset:19456\n" \
"s_nop 0\n" \
"v_mfma_f32_16x16x32_bf16 v[76:79], a[128:131], v[20:23], 0\n" \
"s_waitcnt lgkmcnt(4)\n" \
"v_mul_f32 v48, s51, v48\n" \
"v_mul_f32_e64 v49, s51, v49\n" \
"v_mfma_f32_16x16x32_bf16 v[64:67], a[4:7], v[24:27], v[64:67]\n" \
"buffer_load_dwordx4 v4, s[36:39], s61 offen offset:1024 sc1 lds\n" \
"v_mfma_f32_16x16x32_bf16 v[68:71], a[4:7], v[28:31], v[68:71]\n" \
"ds_read_b128 v[16:19], v12 offset:4096\n" \
"v_mul_f32_e64 v50, s51, v50\n" \
"v_mfma_f32_16x16x32_bf16 v[72:75], a[132:135], v[24:27], v[72:75]\n" \
"ds_read_b128 v[20:23], v12 offset:20480\n" \
"v_mul_f32_e64 v51, s51, v51\n" \
"v_mfma_f32_16x16x32_bf16 v[76:79], a[132:135], v[28:31], v[76:79]\n" \
"s_waitcnt lgkmcnt(4)\n" \
"v_mul_f32 v56, s51, v56\n" \
"v_mul_f32_e64 v57, s51, v57\n" \
"v_mfma_f32_16x16x32_bf16 v[64:67], a[8:11], v[32:35], v[64:67]\n" \
"s_add_u32 s63, s59, 2\n" \
"s_min_u32 s63, s63, 63\n" \
"v_mfma_f32_16x16x32_bf16 v[68:71], a[8:11], v[36:39], v[68:71]\n" \
"ds_read_b128 v[24:27], v12 offset:5120\n" \
"s_lshr_b32 s63, s63, 3\n" \
"s_lshl_b32 s63, s63, 7\n" \
"v_mul_f32_e64 v58, s51, v58\n" \
"v_mfma_f32_16x16x32_bf16 v[72:75], a[136:139], v[32:35], v[72:75]\n" \
"ds_read_b128 v[28:31], v12 offset:21504\n" \
"v_mul_f32_e64 v59, s51, v59\n" \
"v_mfma_f32_16x16x32_bf16 v[76:79], a[136:139], v[36:39], v[76:79]\n" \
"s_waitcnt lgkmcnt(4)\n" \
"v_cvt_pk_bf16_f32 v80, v48, v49\n" \
"v_cvt_pk_bf16_f32 v81, v50, v51\n" \
"v_mfma_f32_16x16x32_bf16 v[64:67], a[12:15], v[40:43], v[64:67]\n" \
"buffer_load_dwordx4 v4, s[36:39], s61 offen offset:2048 sc1 lds\n" \
"v_mfma_f32_16x16x32_bf16 v[68:71], a[12:15], v[44:47], v[68:71]\n" \
"ds_read_b128 v[32:35], v12 offset:6144\n" \
"v_cvt_pk_bf16_f32 v82, v56, v57\n" \
"v_mfma_f32_16x16x32_bf16 v[72:75], a[140:143], v[40:43], v[72:75]\n" \
"ds_read_b128 v[36:39], v12 offset:22528\n" \
"v_cvt_pk_bf16_f32 v83, v58, v59\n" \
"v_mfma_f32_16x16x32_bf16 v[76:79], a[140:143], v[44:47], v[76:79]\n" \
"s_waitcnt lgkmcnt(4)\n" \
"buffer_store_dwordx4 v[80:83], v5, s[36:39], s57 offen sc1\n" \
"v_mul_f32_e64 v52, s51, v52\n" \
"v_mfma_f32_16x16x32_bf16 v[64:67], a[16:19], v[16:19], v[64:67]\n" \
"buffer_load_dwordx4 v4, s[36:39], s61 offen offset:3072 sc1 lds\n" \
"v_mfma_f32_16x16x32_bf16 v[68:71], a[16:19], v[20:23], v[68:71]\n" \
"ds_read_b128 v[40:43], v12 offset:7168\n" \
"v_mul_f32_e64 v53, s51, v53\n" \
"v_mfma_f32_16x16x32_bf16 v[72:75], a[144:147], v[16:19], v[72:75]\n" \
"ds_read_b128 v[44:47], v12 offset:23552\n" \
"v_mul_f32_e64 v54, s51, v54\n" \
"v_mfma_f32_16x16x32_bf16 v[76:79], a[144:147], v[20:23], v[76:79]\n" \
"s_waitcnt lgkmcnt(4)\n" \
"v_mul_f32 v55, s51, v55\n" \
"v_mul_f32_e64 v60, s51, v60\n" \
"v_mfma_f32_16x16x32_bf16 v[64:67], a[20:23], v[24:27], v[64:67]\n" \
"buffer_load_dword v15, v14, s[36:39], s63 offen sc1\n" \
"v_mfma_f32_16x16x32_bf16 v[68:71], a[20:23], v[28:31], v[68:71]\n" \
"ds_read_b128 v[16:19], v12 offset:8192\n" \
"v_mul_f32_e64 v61, s51, v61\n" \
"v_mfma_f32_16x16x32_bf16 v[72:75], a[148:151], v[24:27], v[72:75]\n" \
"ds_read_b128 v[20:23], v12 offset:24576\n" \
"v_mul_f32_e64 v62, s51, v62\n" \
"v_mfma_f32_16x16x32_bf16 v[76:79], a[148:151], v[28:31], v[76:79]\n" \
"s_waitcnt lgkmcnt(4)\n" \
"v_mul_f32 v63, s51, v63\n" \
"v_cvt_pk_bf16_f32 v84, v52, v53\n" \
"v_mfma_f32_16x16x32_bf16 v[64:67], a[24:27], v[32:35], v[64:67]\n" \
"s_add_u32 m0, s55, 4096\n" \
"s_add_u32 s61, s61, 4096\n" \
"buffer_load_dwordx4 v4, s[36:39], s61 offen offset:0 sc1 lds\n" \
"v_mfma_f32_16x16x32_bf16 v[68:71], a[24:27], v[36:39], v[68:71]\n" \
"ds_read_b128 v[24:27], v12 offset:9216\n" \
"v_cvt_pk_bf16_f32 v85, v54, v55\n" \
"v_mfma_f32_16x16x32_bf16 v[72:75], a[152:155], v[32:35], v[72:75]\n" \
"ds_read_b128 v[28:31], v12 offset:25600\n" \
"v_cvt_pk_bf16_f32 v86, v60, v61\n" \
"v_mfma_f32_16x16x32_bf16 v[76:79], a[152:155], v[36:39], v[76:79]\n" \
"s_waitcnt lgkmcnt(4)\n" \
"v_cvt_pk_bf16_f32 v87, v62, v63\n" \
"buffer_store_dwordx4 v[84:87], v5, s[36:39], s58 offen sc1\n" \
"v_mfma_f32_16x16x32_bf16 v[64:67], a[28:31], v[40:43], v[64:67]\n" \
"buffer_load_dwordx4 v4, s[36:39], s61 offen offset:1024 sc1 lds\n" \
"v_mfma_f32_16x16x32_bf16 v[68:71], a[28:31], v[44:47], v[68:71]\n" \
"ds_read_b128 v[32:35], v12 offset:10240\n" \
"v_mfma_f32_16x16x32_bf16 v[72:75], a[156:159], v[40:43], v[72:75]\n" \
"ds_read_b128 v[36:39], v12 offset:26624\n" \
"v_mfma_f32_16x16x32_bf16 v[76:79], a[156:159], v[44:47], v[76:79]\n" \
"s_waitcnt lgkmcnt(4)\n" \
"v_mfma_f32_16x16x32_bf16 v[64:67], a[32:35], v[16:19], v[64:67]\n" \
"v_mfma_f32_16x16x32_bf16 v[68:71], a[32:35], v[20:23], v[68:71]\n" \
"ds_read_b128 v[40:43], v12 offset:11264\n" \
"v_mfma_f32_16x16x32_bf16 v[72:75], a[160:163], v[16:19], v[72:75]\n" \
"ds_read_b128 v[44:47], v12 offset:27648\n" \
"v_mfma_f32_16x16x32_bf16 v[76:79], a[160:163], v[20:23], v[76:79]\n" \
"s_waitcnt lgkmcnt(4)\n" \
"v_mfma_f32_16x16x32_bf16 v[64:67], a[36:39], v[24:27], v[64:67]\n" \
"buffer_load_dwordx4 v4, s[36:39], s61 offen offset:2048 sc1 lds\n" \
"v_mfma_f32_16x16x32_bf16 v[68:71], a[36:39], v[28:31], v[68:71]\n" \
"ds_read_b128 v[16:19], v12 offset:12288\n" \
"v_mfma_f32_16x16x32_bf16 v[72:75], a[164:167], v[24:27], v[72:75]\n" \
"ds_read_b128 v[20:23], v12 offset:28672\n" \
"v_mfma_f32_16x16x32_bf16 v[76:79], a[164:167], v[28:31], v[76:79]\n" \
"s_waitcnt lgkmcnt(4)\n" \
"v_mfma_f32_16x16x32_bf16 v[64:67], a[40:43], v[32:35], v[64:67]\n" \
"buffer_load_dwordx4 v4, s[36:39], s61 offen offset:3072 sc1 lds\n" \
"v_mfma_f32_16x16x32_bf16 v[68:71], a[40:43], v[36:39], v[68:71]\n" \
"ds_read_b128 v[24:27], v12 offset:13312\n" \
"v_mfma_f32_16x16x32_bf16 v[72:75], a[168:171], v[32:35], v[72:75]\n" \
"ds_read_b128 v[28:31], v12 offset:29696\n" \
"v_mfma_f32_16x16x32_bf16 v[76:79], a[168:171], v[36:39], v[76:79]\n" \
"s_waitcnt lgkmcnt(4)\n" \
"v_mfma_f32_16x16x32_bf16 v[64:67], a[44:47], v[40:43], v[64:67]\n" \
"v_mfma_f32_16x16x32_bf16 v[68:71], a[44:47], v[44:47], v[68:71]\n" \
"ds_read_b128 v[32:35], v12 offset:14336\n" \
"v_mfma_f32_16x16x32_bf16 v[72:75], a[172:175], v[40:43], v[72:75]\n" \
"ds_read_b128 v[36:39], v12 offset:30720\n" \
"v_mfma_f32_16x16x32_bf16 v[76:79], a[172:175], v[44:47], v[76:79]\n" \
"s_waitcnt lgkmcnt(4)\n" \
"v_mfma_f32_16x16x32_bf16 v[64:67], a[48:51], v[16:19], v[64:67]\n" \
"v_mfma_f32_16x16x32_bf16 v[68:71], a[48:51], v[20:23], v[68:71]\n" \
"ds_read_b128 v[40:43], v12 offset:15360\n" \
"v_mfma_f32_16x16x32_bf16 v[72:75], a[176:179], v[16:19], v[72:75]\n" \
"ds_read_b128 v[44:47], v12 offset:31744\n" \
"v_mfma_f32_16x16x32_bf16 v[76:79], a[176:179], v[20:23], v[76:79]\n" \
"s_waitcnt lgkmcnt(4)\n" \
"v_mfma_f32_16x16x32_bf16 v[64:67], a[52:55], v[24:27], v[64:67]\n" \
"s_waitcnt vmcnt(12)\n" \
"s_barrier\n" \
"v_mfma_f32_16x16x32_bf16 v[68:71], a[52:55], v[28:31], v[68:71]\n" \
"ds_read_b128 v[16:19], v12 offset:32768\n" \
"v_mfma_f32_16x16x32_bf16 v[72:75], a[180:183], v[24:27], v[72:75]\n" \
"ds_read_b128 v[20:23], v12 offset:49152\n" \
"v_mfma_f32_16x16x32_bf16 v[76:79], a[180:183], v[28:31], v[76:79]\n" \
"s_waitcnt lgkmcnt(4)\n" \
"v_mfma_f32_16x16x32_bf16 v[64:67], a[56:59], v[32:35], v[64:67]\n" \
"v_mfma_f32_16x16x32_bf16 v[68:71], a[56:59], v[36:39], v[68:71]\n" \
"ds_read_b128 v[24:27], v12 offset:33792\n" \
"v_mfma_f32_16x16x32_bf16 v[72:75], a[184:187], v[32:35], v[72:75]\n" \
"ds_read_b128 v[28:31], v12 offset:50176\n" \
"v_mfma_f32_16x16x32_bf16 v[76:79], a[184:187], v[36:39], v[76:79]\n" \
"s_waitcnt lgkmcnt(4)\n" \
"v_mfma_f32_16x16x32_bf16 v[64:67], a[60:63], v[40:43], v[64:67]\n" \
"v_mfma_f32_16x16x32_bf16 v[68:71], a[60:63], v[44:47], v[68:71]\n" \
"ds_read_b128 v[32:35], v12 offset:34816\n" \
"v_mfma_f32_16x16x32_bf16 v[72:75], a[188:191], v[40:43], v[72:75]\n" \
"ds_read_b128 v[36:39], v12 offset:51200\n" \
"v_mfma_f32_16x16x32_bf16 v[76:79], a[188:191], v[44:47], v[76:79]\n" \
"s_waitcnt lgkmcnt(4)\n" \
"v_mfma_f32_16x16x32_bf16 v[64:67], a[64:67], v[16:19], v[64:67]\n" \
"s_add_u32 s61, s61, 28672\n" \
"s_add_u32 s56, s56, 0x20000\n" \
"s_add_u32 m0, s55, 32768\n" \
"s_nop 0\n" \
"buffer_load_dwordx4 v4, s[36:39], s61 offen offset:0 sc1 lds\n" \
"v_mfma_f32_16x16x32_bf16 v[68:71], a[64:67], v[20:23], v[68:71]\n" \
"ds_read_b128 v[40:43], v12 offset:35840\n" \
"v_mfma_f32_16x16x32_bf16 v[72:75], a[192:195], v[16:19], v[72:75]\n" \
"ds_read_b128 v[44:47], v12 offset:52224\n" \
"v_mfma_f32_16x16x32_bf16 v[76:79], a[192:195], v[20:23], v[76:79]\n" \
"s_waitcnt lgkmcnt(4)\n" \
"v_mfma_f32_16x16x32_bf16 v[64:67], a[68:71], v[24:27], v[64:67]\n" \
"buffer_load_dwordx4 v4, s[36:39], s61 offen offset:1024 sc1 lds\n" \
"v_mfma_f32_16x16x32_bf16 v[68:71], a[68:71], v[28:31], v[68:71]\n" \
"ds_read_b128 v[16:19], v12 offset:36864\n" \
"v_mfma_f32_16x16x32_bf16 v[72:75], a[196:199], v[24:27], v[72:75]\n" \
"ds_read_b128 v[20:23], v12 offset:53248\n" \
"v_mfma_f32_16x16x32_bf16 v[76:79], a[196:199], v[28:31], v[76:79]\n" \
"s_waitcnt lgkmcnt(4)\n" \
"v_mfma_f32_16x16x32_bf16 v[64:67], a[72:75], v[32:35], v[64:67]\n" \
"v_mfma_f32_16x16x32_bf16 v[68:71], a[72:75], v[36:39], v[68:71]\n" \
"ds_read_b128 v[24:27], v12 offset:37888\n" \
"v_mfma_f32_16x16x32_bf16 v[72:75], a[200:203], v[32:35], v[72:75]\n" \
"ds_read_b128 v[28:31], v12 offset:54272\n" \
"v_mfma_f32_16x16x32_bf16 v[76:79], a[200:203], v[36:39], v[76:79]\n" \
"s_waitcnt lgkmcnt(4)\n" \
"v_mfma_f32_16x16x32_bf16 v[64:67], a[76:79], v[40:43], v[64:67]\n" \
"buffer_load_dwordx4 v4, s[36:39], s61 offen offset:2048 sc1 lds\n" \
"v_mfma_f32_16x16x32_bf16 v[68:71], a[76:79], v[44:47], v[68:71]\n" \
"ds_read_b128 v[32:35], v12 offset:38912\n" \
"v_mfma_f32_16x16x32_bf16 v[72:75], a[204:207], v[40:43], v[72:75]\n" \
"ds_read_b128 v[36:39], v12 offset:55296\n" \
"v_mfma_f32_16x16x32_bf16 v[76:79], a[204:207], v[44:47], v[76:79]\n" \
"s_waitcnt lgkmcnt(4)\n" \
"v_mfma_f32_16x16x32_bf16 v[64:67], a[80:83], v[16:19], v[64:67]\n" \
"buffer_load_dwordx4 v4, s[36:39], s61 offen offset:3072 sc1 lds\n" \
"v_mfma_f32_16x16x32_bf16 v[68:71], a[80:83], v[20:23], v[68:71]\n" \
"ds_read_b128 v[40:43], v12 offset:39936\n" \
"v_mfma_f32_16x16x32_bf16 v[72:75], a[208:211], v[16:19], v[72:75]\n" \
"ds_read_b128 v[44:47], v12 offset:56320\n" \
"v_mfma_f32_16x16x32_bf16 v[76:79], a[208:211], v[20:23], v[76:79]\n" \
"s_waitcnt lgkmcnt(4)\n" \
"v_mfma_f32_16x16x32_bf16 v[64:67], a[84:87], v[24:27], v[64:67]\n" \
"v_mfma_f32_16x16x32_bf16 v[68:71], a[84:87], v[28:31], v[68:71]\n" \
"ds_read_b128 v[16:19], v12 offset:40960\n" \
"v_mfma_f32_16x16x32_bf16 v[72:75], a[212:215], v[24:27], v[72:75]\n" \
"ds_read_b128 v[20:23], v12 offset:57344\n" \
"v_mfma_f32_16x16x32_bf16 v[76:79], a[212:215], v[28:31], v[76:79]\n" \
"s_waitcnt lgkmcnt(4)\n" \
"v_mfma_f32_16x16x32_bf16 v[64:67], a[88:91], v[32:35], v[64:67]\n" \
"s_add_u32 m0, s55, 36864\n" \
"s_add_u32 s61, s61, 4096\n" \
"buffer_load_dwordx4 v4, s[36:39], s61 offen offset:0 sc1 lds\n" \
"v_mfma_f32_16x16x32_bf16 v[68:71], a[88:91], v[36:39], v[68:71]\n" \
"ds_read_b128 v[24:27], v12 offset:41984\n" \
"v_mfma_f32_16x16x32_bf16 v[72:75], a[216:219], v[32:35], v[72:75]\n" \
"ds_read_b128 v[28:31], v12 offset:58368\n" \
"v_mfma_f32_16x16x32_bf16 v[76:79], a[216:219], v[36:39], v[76:79]\n" \
"s_waitcnt lgkmcnt(4)\n" \
"v_mfma_f32_16x16x32_bf16 v[64:67], a[92:95], v[40:43], v[64:67]\n" \
"buffer_load_dwordx4 v4, s[36:39], s61 offen offset:1024 sc1 lds\n" \
"v_mfma_f32_16x16x32_bf16 v[68:71], a[92:95], v[44:47], v[68:71]\n" \
"ds_read_b128 v[32:35], v12 offset:43008\n" \
"v_mfma_f32_16x16x32_bf16 v[72:75], a[220:223], v[40:43], v[72:75]\n" \
"ds_read_b128 v[36:39], v12 offset:59392\n" \
"v_mfma_f32_16x16x32_bf16 v[76:79], a[220:223], v[44:47], v[76:79]\n" \
"s_waitcnt lgkmcnt(4)\n" \
"v_mfma_f32_16x16x32_bf16 v[64:67], a[96:99], v[16:19], v[64:67]\n" \
"v_mfma_f32_16x16x32_bf16 v[68:71], a[96:99], v[20:23], v[68:71]\n" \
"ds_read_b128 v[40:43], v12 offset:44032\n" \
"v_mfma_f32_16x16x32_bf16 v[72:75], a[224:227], v[16:19], v[72:75]\n" \
"ds_read_b128 v[44:47], v12 offset:60416\n" \
"v_mfma_f32_16x16x32_bf16 v[76:79], a[224:227], v[20:23], v[76:79]\n" \
"s_waitcnt lgkmcnt(4)\n" \
"v_mfma_f32_16x16x32_bf16 v[64:67], a[100:103], v[24:27], v[64:67]\n" \
"buffer_load_dwordx4 v4, s[36:39], s61 offen offset:2048 sc1 lds\n" \
"v_mfma_f32_16x16x32_bf16 v[68:71], a[100:103], v[28:31], v[68:71]\n" \
"ds_read_b128 v[16:19], v12 offset:45056\n" \
"v_mfma_f32_16x16x32_bf16 v[72:75], a[228:231], v[24:27], v[72:75]\n" \
"ds_read_b128 v[20:23], v12 offset:61440\n" \
"v_mfma_f32_16x16x32_bf16 v[76:79], a[228:231], v[28:31], v[76:79]\n" \
"s_waitcnt lgkmcnt(4)\n" \
"v_mfma_f32_16x16x32_bf16 v[64:67], a[104:107], v[32:35], v[64:67]\n" \
"buffer_load_dwordx4 v4, s[36:39], s61 offen offset:3072 sc1 lds\n" \
"v_mfma_f32_16x16x32_bf16 v[68:71], a[104:107], v[36:39], v[68:71]\n" \
"ds_read_b128 v[24:27], v12 offset:46080\n" \
"v_mfma_f32_16x16x32_bf16 v[72:75], a[232:235], v[32:35], v[72:75]\n" \
"ds_read_b128 v[28:31], v12 offset:62464\n" \
"v_mfma_f32_16x16x32_bf16 v[76:79], a[232:235], v[36:39], v[76:79]\n" \
"s_waitcnt lgkmcnt(4)\n" \
"v_mfma_f32_16x16x32_bf16 v[64:67], a[108:111], v[40:43], v[64:67]\n" \
"s_lshl_b32 s66, s59, s49\n" \
"s_lshr_b32 s65, s66, 8\n" \
"v_mfma_f32_16x16x32_bf16 v[68:71], a[108:111], v[44:47], v[68:71]\n" \
"ds_read_b128 v[32:35], v12 offset:47104\n" \
"v_mfma_f32_16x16x32_bf16 v[72:75], a[236:239], v[40:43], v[72:75]\n" \
"ds_read_b128 v[36:39], v12 offset:63488\n" \
"v_mfma_f32_16x16x32_bf16 v[76:79], a[236:239], v[44:47], v[76:79]\n" \
"s_waitcnt lgkmcnt(4)\n" \
"s_and_b32 s65, s65, 0x2000\n" \
"s_xor_b32 s66, s66, s65\n" \
"v_mfma_f32_16x16x32_bf16 v[64:67], a[112:115], v[16:19], v[64:67]\n" \
"v_mfma_f32_16x16x32_bf16 v[68:71], a[112:115], v[20:23], v[68:71]\n" \
"ds_read_b128 v[40:43], v12 offset:48128\n" \
"v_mfma_f32_16x16x32_bf16 v[72:75], a[240:243], v[16:19], v[72:75]\n" \
"ds_read_b128 v[44:47], v12 offset:64512\n" \
"v_mfma_f32_16x16x32_bf16 v[76:79], a[240:243], v[20:23], v[76:79]\n" \
"s_waitcnt lgkmcnt(4)\n" \
"v_mfma_f32_16x16x32_bf16 v[64:67], a[116:119], v[24:27], v[64:67]\n" \
"s_waitcnt vmcnt(8)\n" \
"s_barrier\n" \
"v_mfma_f32_16x16x32_bf16 v[68:71], a[116:119], v[28:31], v[68:71]\n" \
"ds_read_b128 v[16:19], v10 offset:0\n" \
"v_mfma_f32_16x16x32_bf16 v[72:75], a[244:247], v[24:27], v[72:75]\n" \
"ds_read_b128 v[20:23], v10 offset:16384\n" \
"v_mfma_f32_16x16x32_bf16 v[76:79], a[244:247], v[28:31], v[76:79]\n" \
"s_waitcnt lgkmcnt(4)\n" \
"buffer_store_dword v13, v6, s[36:39], 0 offen sc1\n" \
"v_mfma_f32_16x16x32_bf16 v[64:67], a[120:123], v[32:35], v[64:67]\n" \
"v_cmp_gt_u32_e64 vcc, 16, v15\n" \
"v_mfma_f32_16x16x32_bf16 v[68:71], a[120:123], v[36:39], v[68:71]\n" \
"ds_read_b128 v[24:27], v10 offset:1024\n" \
"v_mfma_f32_16x16x32_bf16 v[72:75], a[248:251], v[32:35], v[72:75]\n" \
"ds_read_b128 v[28:31], v10 offset:17408\n" \
"v_mfma_f32_16x16x32_bf16 v[76:79], a[248:251], v[36:39], v[76:79]\n" \
"s_waitcnt lgkmcnt(4)\n" \
"v_mfma_f32_16x16x32_bf16 v[64:67], a[124:127], v[40:43], v[64:67]\n" \
"s_cbranch_vccnz .Lp1qk_m1_%=\n" \
".Lp1qk_m1k_%=:\n" \
"v_mfma_f32_16x16x32_bf16 v[68:71], a[124:127], v[44:47], v[68:71]\n" \
"ds_read_b128 v[32:35], v10 offset:2048\n" \
"v_mfma_f32_16x16x32_bf16 v[72:75], a[252:255], v[40:43], v[72:75]\n" \
"ds_read_b128 v[36:39], v10 offset:18432\n" \
"v_mfma_f32_16x16x32_bf16 v[76:79], a[252:255], v[44:47], v[76:79]\n" \
"s_waitcnt lgkmcnt(4)\n" \
"s_xor_b32 s57, s66, s64\n" \
"s_add_u32 s58, s57, s50\n" \
"s_add_u32 s59, s59, 1\n" \
"v_mov_b32_e64 v5, v7\n" \
"v_mov_b32_e64 v6, v11\n" \
"s_cmp_ge_u32 s59, s47\n" \
"s_nop 0\n" \
"s_cbranch_scc0 .Lp1qk_loop_%=\n" \
".Lp1qk_out1_%=:\n" \
"s_nop 7\n" \
"s_nop 7\n" \
"v_mul_f32 v64, s51, v64\n" \
"v_mul_f32 v65, s51, v65\n" \
"v_mul_f32 v66, s51, v66\n" \
"v_mul_f32 v67, s51, v67\n" \
"v_mul_f32 v72, s51, v72\n" \
"v_mul_f32 v73, s51, v73\n" \
"v_mul_f32 v74, s51, v74\n" \
"v_mul_f32 v75, s51, v75\n" \
"v_cvt_pk_bf16_f32 v80, v64, v65\n" \
"v_cvt_pk_bf16_f32 v81, v66, v67\n" \
"v_cvt_pk_bf16_f32 v82, v72, v73\n" \
"v_cvt_pk_bf16_f32 v83, v74, v75\n" \
"buffer_store_dwordx4 v[80:83], v5, s[36:39], s57 offen sc1\n" \
"v_mul_f32 v68, s51, v68\n" \
"v_mul_f32 v69, s51, v69\n" \
"v_mul_f32 v70, s51, v70\n" \
"v_mul_f32 v71, s51, v71\n" \
"v_mul_f32 v76, s51, v76\n" \
"v_mul_f32 v77, s51, v77\n" \
"v_mul_f32 v78, s51, v78\n" \
"v_mul_f32 v79, s51, v79\n" \
"v_cvt_pk_bf16_f32 v84, v68, v69\n" \
"v_cvt_pk_bf16_f32 v85, v70, v71\n" \
"v_cvt_pk_bf16_f32 v86, v76, v77\n" \
"v_cvt_pk_bf16_f32 v87, v78, v79\n" \
"buffer_store_dwordx4 v[84:87], v5, s[36:39], s58 offen sc1\n" \
"s_branch .Lp1qk_end_%=\n" \
".Lp1qk_out0_%=:\n" \
"s_nop 7\n" \
"s_nop 7\n" \
"v_mul_f32 v48, s51, v48\n" \
"v_mul_f32 v49, s51, v49\n" \
"v_mul_f32 v50, s51, v50\n" \
"v_mul_f32 v51, s51, v51\n" \
"v_mul_f32 v56, s51, v56\n" \
"v_mul_f32 v57, s51, v57\n" \
"v_mul_f32 v58, s51, v58\n" \
"v_mul_f32 v59, s51, v59\n" \
"v_cvt_pk_bf16_f32 v80, v48, v49\n" \
"v_cvt_pk_bf16_f32 v81, v50, v51\n" \
"v_cvt_pk_bf16_f32 v82, v56, v57\n" \
"v_cvt_pk_bf16_f32 v83, v58, v59\n" \
"buffer_store_dwordx4 v[80:83], v5, s[36:39], s57 offen sc1\n" \
"v_mul_f32 v52, s51, v52\n" \
"v_mul_f32 v53, s51, v53\n" \
"v_mul_f32 v54, s51, v54\n" \
"v_mul_f32 v55, s51, v55\n" \
"v_mul_f32 v60, s51, v60\n" \
"v_mul_f32 v61, s51, v61\n" \
"v_mul_f32 v62, s51, v62\n" \
"v_mul_f32 v63, s51, v63\n" \
"v_cvt_pk_bf16_f32 v84, v52, v53\n" \
"v_cvt_pk_bf16_f32 v85, v54, v55\n" \
"v_cvt_pk_bf16_f32 v86, v60, v61\n" \
"v_cvt_pk_bf16_f32 v87, v62, v63\n" \
"buffer_store_dwordx4 v[84:87], v5, s[36:39], s58 offen sc1\n" \
".Lp1qk_end_%=:\n" \
"s_waitcnt vmcnt(0) lgkmcnt(0)\n" \
"s_barrier\n" \
"v_mov_b32 v13, s59\n" \
"buffer_store_dword v13, v6, s[36:39], 0 offen sc1\n" \
"s_waitcnt vmcnt(0) lgkmcnt(0)\n" \
"s_branch .Lp1qk_x_%=\n" \
".Lp1qk_l0_%=:\n" \
"s_nop 7\n" \
"s_nop 7\n" \
"s_waitcnt vmcnt(0) lgkmcnt(0)\n" \
"s_mov_b32 s62, 0\n" \
".Lp1qk_l0a_%=:\n" \
"buffer_load_dword v15, v14, s[36:39], s63 offen sc1\n" \
"s_waitcnt vmcnt(0)\n" \
"v_cmp_gt_u32 vcc, 16, v15\n" \
"s_nop 4\n" \
"s_cbranch_vccz .Lp1qk_l0k_%=\n" \
"s_sleep 4\n" \
"s_add_u32 s62, s62, 1\n" \
"s_cmp_lt_u32 s62, 0x10000\n" \
"s_cbranch_scc1 .Lp1qk_l0a_%=\n" \
"v_mov_b32 v15, 256\n" \
"buffer_store_dword v15, v15, s[36:39], 0 offen sc1\n" \
"s_waitcnt vmcnt(0) lgkmcnt(0)\n" \
"s_branch .Lp1qk_l0k_%=\n" \
".Lp1qk_l1_%=:\n" \
"s_nop 7\n" \
"s_nop 7\n" \
"s_waitcnt vmcnt(0) lgkmcnt(0)\n" \
"s_mov_b32 s62, 0\n" \
".Lp1qk_l1a_%=:\n" \
"buffer_load_dword v15, v14, s[36:39], s63 offen sc1\n" \
"s_waitcnt vmcnt(0)\n" \
"v_cmp_gt_u32 vcc, 16, v15\n" \
"s_nop 4\n" \
"s_cbranch_vccz .Lp1qk_l1k_%=\n" \
"s_sleep 4\n" \
"s_add_u32 s62, s62, 1\n" \
"s_cmp_lt_u32 s62, 0x10000\n" \
"s_cbranch_scc1 .Lp1qk_l1a_%=\n" \
"v_mov_b32 v15, 256\n" \
"buffer_store_dword v15, v15, s[36:39], 0 offen sc1\n" \
"s_waitcnt vmcnt(0) lgkmcnt(0)\n" \
"s_branch .Lp1qk_l1k_%=\n" \
".Lp1qk_m0_%=:\n" \
"s_nop 7\n" \
"s_nop 7\n" \
"s_waitcnt vmcnt(0) lgkmcnt(0)\n" \
"s_mov_b32 s62, 0\n" \
".Lp1qk_m0a_%=:\n" \
"buffer_load_dword v15, v14, s[36:39], s63 offen sc1\n" \
"s_waitcnt vmcnt(0)\n" \
"v_cmp_gt_u32 vcc, 16, v15\n" \
"s_nop 4\n" \
"s_cbranch_vccz .Lp1qk_m0k_%=\n" \
"s_sleep 4\n" \
"s_add_u32 s62, s62, 1\n" \
"s_cmp_lt_u32 s62, 0x10000\n" \
"s_cbranch_scc1 .Lp1qk_m0a_%=\n" \
"v_mov_b32 v15, 256\n" \
"buffer_store_dword v15, v15, s[36:39], 0 offen sc1\n" \
"s_waitcnt vmcnt(0) lgkmcnt(0)\n" \
"s_branch .Lp1qk_m0k_%=\n" \
".Lp1qk_m1_%=:\n" \
"s_nop 7\n" \
"s_nop 7\n" \
"s_waitcnt vmcnt(0) lgkmcnt(0)\n" \
"s_mov_b32 s62, 0\n" \
".Lp1qk_m1a_%=:\n" \
"buffer_load_dword v15, v14, s[36:39], s63 offen sc1\n" \
"s_waitcnt vmcnt(0)\n" \
"v_cmp_gt_u32 vcc, 16, v15\n" \
"s_nop 4\n" \
"s_cbranch_vccz .Lp1qk_m1k_%=\n" \
"s_sleep 4\n" \
"s_add_u32 s62, s62, 1\n" \
"s_cmp_lt_u32 s62, 0x10000\n" \
"s_cbranch_scc1 .Lp1qk_m1a_%=\n" \
"v_mov_b32 v15, 256\n" \
"buffer_store_dword v15, v15, s[36:39], 0 offen sc1\n" \
"s_waitcnt vmcnt(0) lgkmcnt(0)\n" \
"s_branch .Lp1qk_m1k_%=\n" \
".Lp1qk_x_%=:\n"
#define ASM_P1_V \
"s_nop 7\n" \
"s_nop 7\n" \
"s_lshr_b32 s52, s48, 1\n" \
"s_lshl_b32 s52, s52, 13\n" \
"s_and_b32 s53, s48, 1\n" \
"s_lshl_b32 s53, s53, 10\n" \
"s_add_u32 s52, s52, s53\n" \
"s_mov_b32 s54, 0\n" \
"s_add_u32 m0, s52, 0\n" \
"s_nop 0\n" \
"buffer_load_dwordx4 v1, s[40:43], s54 offen nt lds\n" \
"s_add_u32 s54, s54, s44\n" \
"s_add_u32 m0, s52, 2048\n" \
"s_nop 0\n" \
"buffer_load_dwordx4 v1, s[40:43], s54 offen nt lds\n" \
"s_add_u32 s54, s54, s44\n" \
"s_add_u32 m0, s52, 4096\n" \
"s_nop 0\n" \
"buffer_load_dwordx4 v1, s[40:43], s54 offen nt lds\n" \
"s_add_u32 s54, s54, s44\n" \
"s_add_u32 m0, s52, 6144\n" \
"s_nop 0\n" \
"buffer_load_dwordx4 v1, s[40:43], s54 offen nt lds\n" \
"s_add_u32 s54, s54, s44\n" \
"s_add_u32 m0, s52, 16384\n" \
"s_nop 0\n" \
"buffer_load_dwordx4 v1, s[40:43], s54 offen nt lds\n" \
"s_add_u32 s54, s54, s44\n" \
"s_add_u32 m0, s52, 18432\n" \
"s_nop 0\n" \
"buffer_load_dwordx4 v1, s[40:43], s54 offen nt lds\n" \
"s_add_u32 s54, s54, s44\n" \
"s_add_u32 m0, s52, 20480\n" \
"s_nop 0\n" \
"buffer_load_dwordx4 v1, s[40:43], s54 offen nt lds\n" \
"s_add_u32 s54, s54, s44\n" \
"s_add_u32 m0, s52, 22528\n" \
"s_nop 0\n" \
"buffer_load_dwordx4 v1, s[40:43], s54 offen nt lds\n" \
"s_add_u32 s54, s54, s44\n" \
"s_add_u32 m0, s52, 32768\n" \
"s_nop 0\n" \
"buffer_load_dwordx4 v1, s[40:43], s54 offen nt lds\n" \
"s_add_u32 s54, s54, s44\n" \
"s_add_u32 m0, s52, 34816\n" \
"s_nop 0\n" \
"buffer_load_dwordx4 v1, s[40:43], s54 offen nt lds\n" \
"s_add_u32 s54, s54, s44\n" \
"s_add_u32 m0, s52, 36864\n" \
"s_nop 0\n" \
"buffer_load_dwordx4 v1, s[40:43], s54 offen nt lds\n" \
"s_add_u32 s54, s54, s44\n" \
"s_add_u32 m0, s52, 38912\n" \
"s_nop 0\n" \
"buffer_load_dwordx4 v1, s[40:43], s54 offen nt lds\n" \
"s_add_u32 s54, s54, s44\n" \
"s_add_u32 m0, s52, 49152\n" \
"s_nop 0\n" \
"buffer_load_dwordx4 v1, s[40:43], s54 offen nt lds\n" \
"s_add_u32 s54, s54, s44\n" \
"s_add_u32 m0, s52, 51200\n" \
"s_nop 0\n" \
"buffer_load_dwordx4 v1, s[40:43], s54 offen nt lds\n" \
"s_add_u32 s54, s54, s44\n" \
"s_add_u32 m0, s52, 53248\n" \
"s_nop 0\n" \
"buffer_load_dwordx4 v1, s[40:43], s54 offen nt lds\n" \
"s_add_u32 s54, s54, s44\n" \
"s_add_u32 m0, s52, 55296\n" \
"s_nop 0\n" \
"buffer_load_dwordx4 v1, s[40:43], s54 offen nt lds\n" \
"s_add_u32 s54, s54, s44\n" \
"s_add_u32 m0, s52, 65536\n" \
"s_nop 0\n" \
"buffer_load_dwordx4 v1, s[40:43], s54 offen nt lds\n" \
"s_add_u32 s54, s54, s44\n" \
"s_add_u32 m0, s52, 67584\n" \
"s_nop 0\n" \
"buffer_load_dwordx4 v1, s[40:43], s54 offen nt lds\n" \
"s_add_u32 s54, s54, s44\n" \
"s_add_u32 m0, s52, 69632\n" \
"s_nop 0\n" \
"buffer_load_dwordx4 v1, s[40:43], s54 offen nt lds\n" \
"s_add_u32 s54, s54, s44\n" \
"s_add_u32 m0, s52, 71680\n" \
"s_nop 0\n" \
"buffer_load_dwordx4 v1, s[40:43], s54 offen nt lds\n" \
"s_add_u32 s54, s54, s44\n" \
"s_add_u32 m0, s52, 81920\n" \
"s_nop 0\n" \
"buffer_load_dwordx4 v1, s[40:43], s54 offen nt lds\n" \
"s_add_u32 s54, s54, s44\n" \
"s_add_u32 m0, s52, 83968\n" \
"s_nop 0\n" \
"buffer_load_dwordx4 v1, s[40:43], s54 offen nt lds\n" \
"s_add_u32 s54, s54, s44\n" \
"s_add_u32 m0, s52, 86016\n" \
"s_nop 0\n" \
"buffer_load_dwordx4 v1, s[40:43], s54 offen nt lds\n" \
"s_add_u32 s54, s54, s44\n" \
"s_add_u32 m0, s52, 88064\n" \
"s_nop 0\n" \
"buffer_load_dwordx4 v1, s[40:43], s54 offen nt lds\n" \
"s_add_u32 s54, s54, s44\n" \
"s_add_u32 m0, s52, 98304\n" \
"s_nop 0\n" \
"buffer_load_dwordx4 v1, s[40:43], s54 offen nt lds\n" \
"s_add_u32 s54, s54, s44\n" \
"s_add_u32 m0, s52, 100352\n" \
"s_nop 0\n" \
"buffer_load_dwordx4 v1, s[40:43], s54 offen nt lds\n" \
"s_add_u32 s54, s54, s44\n" \
"s_add_u32 m0, s52, 102400\n" \
"s_nop 0\n" \
"buffer_load_dwordx4 v1, s[40:43], s54 offen nt lds\n" \
"s_add_u32 s54, s54, s44\n" \
"s_add_u32 m0, s52, 104448\n" \
"s_nop 0\n" \
"buffer_load_dwordx4 v1, s[40:43], s54 offen nt lds\n" \
"s_add_u32 s54, s54, s44\n" \
"s_add_u32 m0, s52, 114688\n" \
"s_nop 0\n" \
"buffer_load_dwordx4 v1, s[40:43], s54 offen nt lds\n" \
"s_add_u32 s54, s54, s44\n" \
"s_add_u32 m0, s52, 116736\n" \
"s_nop 0\n" \
"buffer_load_dwordx4 v1, s[40:43], s54 offen nt lds\n" \
"s_add_u32 s54, s54, s44\n" \
"s_add_u32 m0, s52, 118784\n" \
"s_nop 0\n" \
"buffer_load_dwordx4 v1, s[40:43], s54 offen nt lds\n" \
"s_add_u32 s54, s54, s44\n" \
"s_add_u32 m0, s52, 120832\n" \
"s_nop 0\n" \
"buffer_load_dwordx4 v1, s[40:43], s54 offen nt lds\n" \
"s_add_u32 s54, s54, s44\n" \
"v_add_u32 v8, 0x10000, v2\n" \
"v_add_u32 v9, 0x10000, v3\n" \
"s_waitcnt vmcnt(28)\n" \
"s_barrier\n" \
"ds_read_b64_tr_b16 a[0:1], v2 offset:0\n" \
"ds_read_b64_tr_b16 a[2:3], v2 offset:512\n" \
"ds_read_b64_tr_b16 a[128:129], v3 offset:0\n" \
"ds_read_b64_tr_b16 a[130:131], v3 offset:512\n" \
"ds_read_b64_tr_b16 a[4:5], v2 offset:4096\n" \
"ds_read_b64_tr_b16 a[6:7], v2 offset:4608\n" \
"ds_read_b64_tr_b16 a[132:133], v3 offset:4096\n" \
"ds_read_b64_tr_b16 a[134:135], v3 offset:4608\n" \
"s_waitcnt lgkmcnt(0)\n" \
"s_barrier\n" \
"s_add_u32 m0, s52, 0\n" \
"s_nop 0\n" \
"buffer_load_dwordx4 v1, s[40:43], s54 offen nt lds\n" \
"s_add_u32 s54, s54, s44\n" \
"s_add_u32 m0, s52, 2048\n" \
"s_nop 0\n" \
"buffer_load_dwordx4 v1, s[40:43], s54 offen nt lds\n" \
"s_add_u32 s54, s54, s44\n" \
"s_add_u32 m0, s52, 4096\n" \
"s_nop 0\n" \
"buffer_load_dwordx4 v1, s[40:43], s54 offen nt lds\n" \
"s_add_u32 s54, s54, s44\n" \
"s_add_u32 m0, s52, 6144\n" \
"s_nop 0\n" \
"buffer_load_dwordx4 v1, s[40:43], s54 offen nt lds\n" \
"s_add_u32 s54, s54, s44\n" \
"s_waitcnt vmcnt(28)\n" \
"s_barrier\n" \
"ds_read_b64_tr_b16 a[8:9], v2 offset:16384\n" \
"ds_read_b64_tr_b16 a[10:11], v2 offset:16896\n" \
"ds_read_b64_tr_b16 a[136:137], v3 offset:16384\n" \
"ds_read_b64_tr_b16 a[138:139], v3 offset:16896\n" \
"ds_read_b64_tr_b16 a[12:13], v2 offset:20480\n" \
"ds_read_b64_tr_b16 a[14:15], v2 offset:20992\n" \
"ds_read_b64_tr_b16 a[140:141], v3 offset:20480\n" \
"ds_read_b64_tr_b16 a[142:143], v3 offset:20992\n" \
"s_waitcnt lgkmcnt(0)\n" \
"s_barrier\n" \
"s_add_u32 m0, s52, 16384\n" \
"s_nop 0\n" \
"buffer_load_dwordx4 v1, s[40:43], s54 offen nt lds\n" \
"s_add_u32 s54, s54, s44\n" \
"s_add_u32 m0, s52, 18432\n" \
"s_nop 0\n" \
"buffer_load_dwordx4 v1, s[40:43], s54 offen nt lds\n" \
"s_add_u32 s54, s54, s44\n" \
"s_add_u32 m0, s52, 20480\n" \
"s_nop 0\n" \
"buffer_load_dwordx4 v1, s[40:43], s54 offen nt lds\n" \
"s_add_u32 s54, s54, s44\n" \
"s_add_u32 m0, s52, 22528\n" \
"s_nop 0\n" \
"buffer_load_dwordx4 v1, s[40:43], s54 offen nt lds\n" \
"s_add_u32 s54, s54, s44\n" \
"s_waitcnt vmcnt(28)\n" \
"s_barrier\n" \
"ds_read_b64_tr_b16 a[16:17], v2 offset:32768\n" \
"ds_read_b64_tr_b16 a[18:19], v2 offset:33280\n" \
"ds_read_b64_tr_b16 a[144:145], v3 offset:32768\n" \
"ds_read_b64_tr_b16 a[146:147], v3 offset:33280\n" \
"ds_read_b64_tr_b16 a[20:21], v2 offset:36864\n" \
"ds_read_b64_tr_b16 a[22:23], v2 offset:37376\n" \
"ds_read_b64_tr_b16 a[148:149], v3 offset:36864\n" \
"ds_read_b64_tr_b16 a[150:151], v3 offset:37376\n" \
"s_waitcnt lgkmcnt(0)\n" \
"s_barrier\n" \
"s_add_u32 m0, s52, 32768\n" \
"s_nop 0\n" \
"buffer_load_dwordx4 v1, s[40:43], s54 offen nt lds\n" \
"s_add_u32 s54, s54, s44\n" \
"s_add_u32 m0, s52, 34816\n" \
"s_nop 0\n" \
"buffer_load_dwordx4 v1, s[40:43], s54 offen nt lds\n" \
"s_add_u32 s54, s54, s44\n" \
"s_add_u32 m0, s52, 36864\n" \
"s_nop 0\n" \
"buffer_load_dwordx4 v1, s[40:43], s54 offen nt lds\n" \
"s_add_u32 s54, s54, s44\n" \
"s_add_u32 m0, s52, 38912\n" \
"s_nop 0\n" \
"buffer_load_dwordx4 v1, s[40:43], s54 offen nt lds\n" \
"s_add_u32 s54, s54, s44\n" \
"s_waitcnt vmcnt(28)\n" \
"s_barrier\n" \
"ds_read_b64_tr_b16 a[24:25], v2 offset:49152\n" \
"ds_read_b64_tr_b16 a[26:27], v2 offset:49664\n" \
"ds_read_b64_tr_b16 a[152:153], v3 offset:49152\n" \
"ds_read_b64_tr_b16 a[154:155], v3 offset:49664\n" \
"ds_read_b64_tr_b16 a[28:29], v2 offset:53248\n" \
"ds_read_b64_tr_b16 a[30:31], v2 offset:53760\n" \
"ds_read_b64_tr_b16 a[156:157], v3 offset:53248\n" \
"ds_read_b64_tr_b16 a[158:159], v3 offset:53760\n" \
"s_waitcnt lgkmcnt(0)\n" \
"s_barrier\n" \
"s_add_u32 m0, s52, 49152\n" \
"s_nop 0\n" \
"buffer_load_dwordx4 v1, s[40:43], s54 offen nt lds\n" \
"s_add_u32 s54, s54, s44\n" \
"s_add_u32 m0, s52, 51200\n" \
"s_nop 0\n" \
"buffer_load_dwordx4 v1, s[40:43], s54 offen nt lds\n" \
"s_add_u32 s54, s54, s44\n" \
"s_add_u32 m0, s52, 53248\n" \
"s_nop 0\n" \
"buffer_load_dwordx4 v1, s[40:43], s54 offen nt lds\n" \
"s_add_u32 s54, s54, s44\n" \
"s_add_u32 m0, s52, 55296\n" \
"s_nop 0\n" \
"buffer_load_dwordx4 v1, s[40:43], s54 offen nt lds\n" \
"s_add_u32 s54, s54, s44\n" \
"s_waitcnt vmcnt(28)\n" \
"s_barrier\n" \
"ds_read_b64_tr_b16 a[32:33], v8 offset:0\n" \
"ds_read_b64_tr_b16 a[34:35], v8 offset:512\n" \
"ds_read_b64_tr_b16 a[160:161], v9 offset:0\n" \
"ds_read_b64_tr_b16 a[162:163], v9 offset:512\n" \
"ds_read_b64_tr_b16 a[36:37], v8 offset:4096\n" \
"ds_read_b64_tr_b16 a[38:39], v8 offset:4608\n" \
"ds_read_b64_tr_b16 a[164:165], v9 offset:4096\n" \
"ds_read_b64_tr_b16 a[166:167], v9 offset:4608\n" \
"s_waitcnt lgkmcnt(0)\n" \
"s_barrier\n" \
"s_add_u32 m0, s52, 65536\n" \
"s_nop 0\n" \
"buffer_load_dwordx4 v1, s[40:43], s54 offen nt lds\n" \
"s_add_u32 s54, s54, s44\n" \
"s_add_u32 m0, s52, 67584\n" \
"s_nop 0\n" \
"buffer_load_dwordx4 v1, s[40:43], s54 offen nt lds\n" \
"s_add_u32 s54, s54, s44\n" \
"s_add_u32 m0, s52, 69632\n" \
"s_nop 0\n" \
"buffer_load_dwordx4 v1, s[40:43], s54 offen nt lds\n" \
"s_add_u32 s54, s54, s44\n" \
"s_add_u32 m0, s52, 71680\n" \
"s_nop 0\n" \
"buffer_load_dwordx4 v1, s[40:43], s54 offen nt lds\n" \
"s_add_u32 s54, s54, s44\n" \
"s_waitcnt vmcnt(28)\n" \
"s_barrier\n" \
"ds_read_b64_tr_b16 a[40:41], v8 offset:16384\n" \
"ds_read_b64_tr_b16 a[42:43], v8 offset:16896\n" \
"ds_read_b64_tr_b16 a[168:169], v9 offset:16384\n" \
"ds_read_b64_tr_b16 a[170:171], v9 offset:16896\n" \
"ds_read_b64_tr_b16 a[44:45], v8 offset:20480\n" \
"ds_read_b64_tr_b16 a[46:47], v8 offset:20992\n" \
"ds_read_b64_tr_b16 a[172:173], v9 offset:20480\n" \
"ds_read_b64_tr_b16 a[174:175], v9 offset:20992\n" \
"s_waitcnt lgkmcnt(0)\n" \
"s_barrier\n" \
"s_add_u32 m0, s52, 81920\n" \
"s_nop 0\n" \
"buffer_load_dwordx4 v1, s[40:43], s54 offen nt lds\n" \
"s_add_u32 s54, s54, s44\n" \
"s_add_u32 m0, s52, 83968\n" \
"s_nop 0\n" \
"buffer_load_dwordx4 v1, s[40:43], s54 offen nt lds\n" \
"s_add_u32 s54, s54, s44\n" \
"s_add_u32 m0, s52, 86016\n" \
"s_nop 0\n" \
"buffer_load_dwordx4 v1, s[40:43], s54 offen nt lds\n" \
"s_add_u32 s54, s54, s44\n" \
"s_add_u32 m0, s52, 88064\n" \
"s_nop 0\n" \
"buffer_load_dwordx4 v1, s[40:43], s54 offen nt lds\n" \
"s_add_u32 s54, s54, s44\n" \
"s_waitcnt vmcnt(28)\n" \
"s_barrier\n" \
"ds_read_b64_tr_b16 a[48:49], v8 offset:32768\n" \
"ds_read_b64_tr_b16 a[50:51], v8 offset:33280\n" \
"ds_read_b64_tr_b16 a[176:177], v9 offset:32768\n" \
"ds_read_b64_tr_b16 a[178:179], v9 offset:33280\n" \
"ds_read_b64_tr_b16 a[52:53], v8 offset:36864\n" \
"ds_read_b64_tr_b16 a[54:55], v8 offset:37376\n" \
"ds_read_b64_tr_b16 a[180:181], v9 offset:36864\n" \
"ds_read_b64_tr_b16 a[182:183], v9 offset:37376\n" \
"s_waitcnt lgkmcnt(0)\n" \
"s_barrier\n" \
"s_add_u32 m0, s52, 98304\n" \
"s_nop 0\n" \
"buffer_load_dwordx4 v1, s[40:43], s54 offen nt lds\n" \
"s_add_u32 s54, s54, s44\n" \
"s_add_u32 m0, s52, 100352\n" \
"s_nop 0\n" \
"buffer_load_dwordx4 v1, s[40:43], s54 offen nt lds\n" \
"s_add_u32 s54, s54, s44\n" \
"s_add_u32 m0, s52, 102400\n" \
"s_nop 0\n" \
"buffer_load_dwordx4 v1, s[40:43], s54 offen nt lds\n" \
"s_add_u32 s54, s54, s44\n" \
"s_add_u32 m0, s52, 104448\n" \
"s_nop 0\n" \
"buffer_load_dwordx4 v1, s[40:43], s54 offen nt lds\n" \
"s_add_u32 s54, s54, s44\n" \
"s_waitcnt vmcnt(28)\n" \
"s_barrier\n" \
"ds_read_b64_tr_b16 a[56:57], v8 offset:49152\n" \
"ds_read_b64_tr_b16 a[58:59], v8 offset:49664\n" \
"ds_read_b64_tr_b16 a[184:185], v9 offset:49152\n" \
"ds_read_b64_tr_b16 a[186:187], v9 offset:49664\n" \
"ds_read_b64_tr_b16 a[60:61], v8 offset:53248\n" \
"ds_read_b64_tr_b16 a[62:63], v8 offset:53760\n" \
"ds_read_b64_tr_b16 a[188:189], v9 offset:53248\n" \
"ds_read_b64_tr_b16 a[190:191], v9 offset:53760\n" \
"s_waitcnt lgkmcnt(0)\n" \
"s_barrier\n" \
"s_add_u32 m0, s52, 114688\n" \
"s_nop 0\n" \
"buffer_load_dwordx4 v1, s[40:43], s54 offen nt lds\n" \
"s_add_u32 s54, s54, s44\n" \
"s_add_u32 m0, s52, 116736\n" \
"s_nop 0\n" \
"buffer_load_dwordx4 v1, s[40:43], s54 offen nt lds\n" \
"s_add_u32 s54, s54, s44\n" \
"s_add_u32 m0, s52, 118784\n" \
"s_nop 0\n" \
"buffer_load_dwordx4 v1, s[40:43], s54 offen nt lds\n" \
"s_add_u32 s54, s54, s44\n" \
"s_add_u32 m0, s52, 120832\n" \
"s_nop 0\n" \
"buffer_load_dwordx4 v1, s[40:43], s54 offen nt lds\n" \
"s_add_u32 s54, s54, s44\n" \
"s_waitcnt vmcnt(28)\n" \
"s_barrier\n" \
"ds_read_b64_tr_b16 a[64:65], v2 offset:0\n" \
"ds_read_b64_tr_b16 a[66:67], v2 offset:512\n" \
"ds_read_b64_tr_b16 a[192:193], v3 offset:0\n" \
"ds_read_b64_tr_b16 a[194:195], v3 offset:512\n" \
"ds_read_b64_tr_b16 a[68:69], v2 offset:4096\n" \
"ds_read_b64_tr_b16 a[70:71], v2 offset:4608\n" \
"ds_read_b64_tr_b16 a[196:197], v3 offset:4096\n" \
"ds_read_b64_tr_b16 a[198:199], v3 offset:4608\n" \
"s_waitcnt vmcnt(24)\n" \
"s_barrier\n" \
"ds_read_b64_tr_b16 a[72:73], v2 offset:16384\n" \
"ds_read_b64_tr_b16 a[74:75], v2 offset:16896\n" \
"ds_read_b64_tr_b16 a[200:201], v3 offset:16384\n" \
"ds_read_b64_tr_b16 a[202:203], v3 offset:16896\n" \
"ds_read_b64_tr_b16 a[76:77], v2 offset:20480\n" \
"ds_read_b64_tr_b16 a[78:79], v2 offset:20992\n" \
"ds_read_b64_tr_b16 a[204:205], v3 offset:20480\n" \
"ds_read_b64_tr_b16 a[206:207], v3 offset:20992\n" \
"s_waitcnt vmcnt(20)\n" \
"s_barrier\n" \
"ds_read_b64_tr_b16 a[80:81], v2 offset:32768\n" \
"ds_read_b64_tr_b16 a[82:83], v2 offset:33280\n" \
"ds_read_b64_tr_b16 a[208:209], v3 offset:32768\n" \
"ds_read_b64_tr_b16 a[210:211], v3 offset:33280\n" \
"ds_read_b64_tr_b16 a[84:85], v2 offset:36864\n" \
"ds_read_b64_tr_b16 a[86:87], v2 offset:37376\n" \
"ds_read_b64_tr_b16 a[212:213], v3 offset:36864\n" \
"ds_read_b64_tr_b16 a[214:215], v3 offset:37376\n" \
"s_waitcnt vmcnt(16)\n" \
"s_barrier\n" \
"ds_read_b64_tr_b16 a[88:89], v2 offset:49152\n" \
"ds_read_b64_tr_b16 a[90:91], v2 offset:49664\n" \
"ds_read_b64_tr_b16 a[216:217], v3 offset:49152\n" \
"ds_read_b64_tr_b16 a[218:219], v3 offset:49664\n" \
"ds_read_b64_tr_b16 a[92:93], v2 offset:53248\n" \
"ds_read_b64_tr_b16 a[94:95], v2 offset:53760\n" \
"ds_read_b64_tr_b16 a[220:221], v3 offset:53248\n" \
"ds_read_b64_tr_b16 a[222:223], v3 offset:53760\n" \
"s_waitcnt vmcnt(12)\n" \
"s_barrier\n" \
"ds_read_b64_tr_b16 a[96:97], v8 offset:0\n" \
"ds_read_b64_tr_b16 a[98:99], v8 offset:512\n" \
"ds_read_b64_tr_b16 a[224:225], v9 offset:0\n" \
"ds_read_b64_tr_b16 a[226:227], v9 offset:512\n" \
"ds_read_b64_tr_b16 a[100:101], v8 offset:4096\n" \
"ds_read_b64_tr_b16 a[102:103], v8 offset:4608\n" \
"ds_read_b64_tr_b16 a[228:229], v9 offset:4096\n" \
"ds_read_b64_tr_b16 a[230:231], v9 offset:4608\n" \
"s_waitcnt vmcnt(8)\n" \
"s_barrier\n" \
"ds_read_b64_tr_b16 a[104:105], v8 offset:16384\n" \
"ds_read_b64_tr_b16 a[106:107], v8 offset:16896\n" \
"ds_read_b64_tr_b16 a[232:233], v9 offset:16384\n" \
"ds_read_b64_tr_b16 a[234:235], v9 offset:16896\n" \
"ds_read_b64_tr_b16 a[108:109], v8 offset:20480\n" \
"ds_read_b64_tr_b16 a[110:111], v8 offset:20992\n" \
"ds_read_b64_tr_b16 a[236:237], v9 offset:20480\n" \
"ds_read_b64_tr_b16 a[238:239], v9 offset:20992\n" \
"s_waitcnt vmcnt(4)\n" \
"s_barrier\n" \
"ds_read_b64_tr_b16 a[112:113], v8 offset:32768\n" \
"ds_read_b64_tr_b16 a[114:115], v8 offset:33280\n" \
"ds_read_b64_tr_b16 a[240:241], v9 offset:32768\n" \
"ds_read_b64_tr_b16 a[242:243], v9 offset:33280\n" \
"ds_read_b64_tr_b16 a[116:117], v8 offset:36864\n" \
"ds_read_b64_tr_b16 a[118:119], v8 offset:37376\n" \
"ds_read_b64_tr_b16 a[244:245], v9 offset:36864\n" \
"ds_read_b64_tr_b16 a[246:247], v9 offset:37376\n" \
"s_waitcnt vmcnt(0)\n" \
"s_barrier\n" \
"ds_read_b64_tr_b16 a[120:121], v8 offset:49152\n" \
"ds_read_b64_tr_b16 a[122:123], v8 offset:49664\n" \
"ds_read_b64_tr_b16 a[248:249], v9 offset:49152\n" \
"ds_read_b64_tr_b16 a[250:251], v9 offset:49664\n" \
"ds_read_b64_tr_b16 a[124:125], v8 offset:53248\n" \
"ds_read_b64_tr_b16 a[126:127], v8 offset:53760\n" \
"ds_read_b64_tr_b16 a[252:253], v9 offset:53248\n" \
"ds_read_b64_tr_b16 a[254:255], v9 offset:53760\n" \
"s_waitcnt lgkmcnt(0)\n" \
"s_barrier\n" \
"v_mov_b32 v14, 4096\n" \
"s_mov_b32 s59, s46\n" \
"s_add_u32 s63, s59, 0\n" \
"s_min_u32 s63, s63, 63\n" \
"s_lshr_b32 s63, s63, 3\n" \
"s_lshl_b32 s63, s63, 7\n" \
"s_branch .Lp1v_l0_%=\n" \
".Lp1v_l0k_%=:\n" \
"s_add_u32 s63, s59, 1\n" \
"s_min_u32 s63, s63, 63\n" \
"s_lshr_b32 s63, s63, 3\n" \
"s_lshl_b32 s63, s63, 7\n" \
"s_branch .Lp1v_l1_%=\n" \
".Lp1v_l1k_%=:\n" \
"s_lshl_b32 s55, s48, 13\n" \
"s_lshl_b32 s56, s46, 17\n" \
"s_add_u32 s56, s56, s45\n" \
"s_lshl_b32 s60, s47, 17\n" \
"s_sub_u32 s60, s60, 0x20000\n" \
"s_add_u32 s60, s60, s45\n" \
"s_mov_b32 s57, 0\n" \
"s_mov_b32 s58, 0\n" \
"v_mov_b32 v7, v5\n" \
"v_mov_b32 v5, 0x7ffffff0\n" \
"v_mov_b32 v10, v0\n" \
"v_mov_b32 v11, v6\n" \
"v_mov_b32 v6, 0x7ffffff0\n" \
"s_mov_b32 s59, s46\n" \
"s_min_u32 s61, s56, s60\n" \
"s_lshr_b32 s65, s61, 8\n" \
"s_and_b32 s65, s65, 0x2000\n" \
"s_xor_b32 s61, s61, s65\n" \
"s_add_u32 m0, s55, 0\n" \
"s_nop 0\n" \
"buffer_load_dwordx4 v4, s[36:39], s61 offen offset:0 sc1 lds\n" \
"buffer_load_dwordx4 v4, s[36:39], s61 offen offset:1024 sc1 lds\n" \
"buffer_load_dwordx4 v4, s[36:39], s61 offen offset:2048 sc1 lds\n" \
"buffer_load_dwordx4 v4, s[36:39], s61 offen offset:3072 sc1 lds\n" \
"s_add_u32 m0, s55, 4096\n" \
"s_add_u32 s61, s61, 4096\n" \
"buffer_load_dwordx4 v4, s[36:39], s61 offen offset:0 sc1 lds\n" \
"buffer_load_dwordx4 v4, s[36:39], s61 offen offset:1024 sc1 lds\n" \
"buffer_load_dwordx4 v4, s[36:39], s61 offen offset:2048 sc1 lds\n" \
"buffer_load_dwordx4 v4, s[36:39], s61 offen offset:3072 sc1 lds\n" \
"s_add_u32 s61, s61, 28672\n" \
"s_add_u32 s56, s56, 0x20000\n" \
"s_add_u32 m0, s55, 32768\n" \
"s_nop 0\n" \
"buffer_load_dwordx4 v4, s[36:39], s61 offen offset:0 sc1 lds\n" \
"buffer_load_dwordx4 v4, s[36:39], s61 offen offset:1024 sc1 lds\n" \
"buffer_load_dwordx4 v4, s[36:39], s61 offen offset:2048 sc1 lds\n" \
"buffer_load_dwordx4 v4, s[36:39], s61 offen offset:3072 sc1 lds\n" \
"s_add_u32 m0, s55, 36864\n" \
"s_add_u32 s61, s61, 4096\n" \
"buffer_load_dwordx4 v4, s[36:39], s61 offen offset:0 sc1 lds\n" \
"buffer_load_dwordx4 v4, s[36:39], s61 offen offset:1024 sc1 lds\n" \
"buffer_load_dwordx4 v4, s[36:39], s61 offen offset:2048 sc1 lds\n" \
"buffer_load_dwordx4 v4, s[36:39], s61 offen offset:3072 sc1 lds\n" \
"s_mov_b32 s70, 0\n" \
"s_mov_b32 s72, 0\n" \
"s_waitcnt vmcnt(0) lgkmcnt(0)\n" \
"s_barrier\n" \
"v_add_u32 v12, 0x10000, v10\n" \
"ds_read_b128 v[16:19], v10 offset:0\n" \
"ds_read_b128 v[20:23], v10 offset:16384\n" \
"ds_read_b128 v[24:27], v10 offset:1024\n" \
"ds_read_b128 v[28:31], v10 offset:17408\n" \
"ds_read_b128 v[32:35], v10 offset:2048\n" \
"ds_read_b128 v[36:39], v10 offset:18432\n" \
"s_waitcnt lgkmcnt(4)\n" \
"s_nop 7\n" \
"s_nop 7\n" \
".p2align 6\n" \
".Lp1v_loop_%=:\n" \
"v_mfma_f32_16x16x32_bf16 v[48:51], v[16:19], a[0:3], 0\n" \
"s_min_u32 s61, s56, s60\n" \
"s_lshr_b32 s65, s61, 8\n" \
"s_and_b32 s65, s65, 0x2000\n" \
"s_xor_b32 s61, s61, s65\n" \
"s_add_u32 m0, s55, 65536\n" \
"s_nop 0\n" \
"buffer_load_dwordx4 v4, s[36:39], s61 offen offset:0 sc1 lds\n" \
"v_mov_b32_e64 v13, s59\n" \
"v_mfma_f32_16x16x32_bf16 v[52:55], v[20:23], a[0:3], 0\n" \
"ds_read_b128 v[40:43], v10 offset:3072\n" \
"s_nop 0\n" \
"v_mfma_f32_16x16x32_bf16 v[56:59], v[16:19], a[128:131], 0\n" \
"ds_read_b128 v[44:47], v10 offset:19456\n" \
"s_nop 0\n" \
"v_mfma_f32_16x16x32_bf16 v[60:63], v[20:23], a[128:131], 0\n" \
"s_waitcnt lgkmcnt(4)\n" \
"v_cvt_pk_bf16_f32 v80, v64, v65\n" \
"v_cvt_pk_bf16_f32 v81, v66, v67\n" \
"v_mfma_f32_16x16x32_bf16 v[48:51], v[24:27], a[4:7], v[48:51]\n" \
"buffer_load_dwordx4 v4, s[36:39], s61 offen offset:1024 sc1 lds\n" \
"v_mfma_f32_16x16x32_bf16 v[52:55], v[28:31], a[4:7], v[52:55]\n" \
"ds_read_b128 v[16:19], v10 offset:4096\n" \
"v_cvt_pk_bf16_f32 v82, v68, v69\n" \
"v_mfma_f32_16x16x32_bf16 v[56:59], v[24:27], a[132:135], v[56:59]\n" \
"ds_read_b128 v[20:23], v10 offset:20480\n" \
"v_cvt_pk_bf16_f32 v83, v70, v71\n" \
"v_mfma_f32_16x16x32_bf16 v[60:63], v[28:31], a[132:135], v[60:63]\n" \
"s_waitcnt lgkmcnt(4)\n" \
"buffer_store_dwordx4 v[80:83], v5, s[36:39], s57 offen sc1\n" \
"v_cvt_pk_bf16_f32 v84, v72, v73\n" \
"v_mfma_f32_16x16x32_bf16 v[48:51], v[32:35], a[8:11], v[48:51]\n" \
"s_add_u32 s63, s59, 2\n" \
"s_min_u32 s63, s63, 63\n" \
"v_mfma_f32_16x16x32_bf16 v[52:55], v[36:39], a[8:11], v[52:55]\n" \
"ds_read_b128 v[24:27], v10 offset:5120\n" \
"s_lshr_b32 s63, s63, 3\n" \
"s_lshl_b32 s63, s63, 7\n" \
"v_cvt_pk_bf16_f32 v85, v74, v75\n" \
"v_mfma_f32_16x16x32_bf16 v[56:59], v[32:35], a[136:139], v[56:59]\n" \
"ds_read_b128 v[28:31], v10 offset:21504\n" \
"v_cvt_pk_bf16_f32 v86, v76, v77\n" \
"v_mfma_f32_16x16x32_bf16 v[60:63], v[36:39], a[136:139], v[60:63]\n" \
"s_waitcnt lgkmcnt(4)\n" \
"v_cvt_pk_bf16_f32 v87, v78, v79\n" \
"buffer_store_dwordx4 v[84:87], v5, s[36:39], s58 offen sc1\n" \
"v_mfma_f32_16x16x32_bf16 v[48:51], v[40:43], a[12:15], v[48:51]\n" \
"buffer_load_dwordx4 v4, s[36:39], s61 offen offset:2048 sc1 lds\n" \
"v_mfma_f32_16x16x32_bf16 v[52:55], v[44:47], a[12:15], v[52:55]\n" \
"ds_read_b128 v[32:35], v10 offset:6144\n" \
"v_mfma_f32_16x16x32_bf16 v[56:59], v[40:43], a[140:143], v[56:59]\n" \
"ds_read_b128 v[36:39], v10 offset:22528\n" \
"v_mfma_f32_16x16x32_bf16 v[60:63], v[44:47], a[140:143], v[60:63]\n" \
"s_waitcnt lgkmcnt(4)\n" \
"v_mfma_f32_16x16x32_bf16 v[48:51], v[16:19], a[16:19], v[48:51]\n" \
"buffer_load_dwordx4 v4, s[36:39], s61 offen offset:3072 sc1 lds\n" \
"v_mfma_f32_16x16x32_bf16 v[52:55], v[20:23], a[16:19], v[52:55]\n" \
"ds_read_b128 v[40:43], v10 offset:7168\n" \
"v_mfma_f32_16x16x32_bf16 v[56:59], v[16:19], a[144:147], v[56:59]\n" \
"ds_read_b128 v[44:47], v10 offset:23552\n" \
"v_mfma_f32_16x16x32_bf16 v[60:63], v[20:23], a[144:147], v[60:63]\n" \
"s_waitcnt lgkmcnt(4)\n" \
"v_mfma_f32_16x16x32_bf16 v[48:51], v[24:27], a[20:23], v[48:51]\n" \
"buffer_load_dword v15, v14, s[36:39], s63 offen sc1\n" \
"v_mfma_f32_16x16x32_bf16 v[52:55], v[28:31], a[20:23], v[52:55]\n" \
"ds_read_b128 v[16:19], v10 offset:8192\n" \
"v_mfma_f32_16x16x32_bf16 v[56:59], v[24:27], a[148:151], v[56:59]\n" \
"ds_read_b128 v[20:23], v10 offset:24576\n" \
"v_mfma_f32_16x16x32_bf16 v[60:63], v[28:31], a[148:151], v[60:63]\n" \
"s_waitcnt lgkmcnt(4)\n" \
"v_mfma_f32_16x16x32_bf16 v[48:51], v[32:35], a[24:27], v[48:51]\n" \
"s_add_u32 m0, s55, 69632\n" \
"s_add_u32 s61, s61, 4096\n" \
"buffer_load_dwordx4 v4, s[36:39], s61 offen offset:0 sc1 lds\n" \
"v_mfma_f32_16x16x32_bf16 v[52:55], v[36:39], a[24:27], v[52:55]\n" \
"ds_read_b128 v[24:27], v10 offset:9216\n" \
"v_mfma_f32_16x16x32_bf16 v[56:59], v[32:35], a[152:155], v[56:59]\n" \
"ds_read_b128 v[28:31], v10 offset:25600\n" \
"v_mfma_f32_16x16x32_bf16 v[60:63], v[36:39], a[152:155], v[60:63]\n" \
"s_waitcnt lgkmcnt(4)\n" \
"v_mfma_f32_16x16x32_bf16 v[48:51], v[40:43], a[28:31], v[48:51]\n" \
"buffer_load_dwordx4 v4, s[36:39], s61 offen offset:1024 sc1 lds\n" \
"v_mfma_f32_16x16x32_bf16 v[52:55], v[44:47], a[28:31], v[52:55]\n" \
"ds_read_b128 v[32:35], v10 offset:10240\n" \
"v_mfma_f32_16x16x32_bf16 v[56:59], v[40:43], a[156:159], v[56:59]\n" \
"ds_read_b128 v[36:39], v10 offset:26624\n" \
"v_mfma_f32_16x16x32_bf16 v[60:63], v[44:47], a[156:159], v[60:63]\n" \
"s_waitcnt lgkmcnt(4)\n" \
"v_mfma_f32_16x16x32_bf16 v[48:51], v[16:19], a[32:35], v[48:51]\n" \
"v_mfma_f32_16x16x32_bf16 v[52:55], v[20:23], a[32:35], v[52:55]\n" \
"ds_read_b128 v[40:43], v10 offset:11264\n" \
"v_mfma_f32_16x16x32_bf16 v[56:59], v[16:19], a[160:163], v[56:59]\n" \
"ds_read_b128 v[44:47], v10 offset:27648\n" \
"v_mfma_f32_16x16x32_bf16 v[60:63], v[20:23], a[160:163], v[60:63]\n" \
"s_waitcnt lgkmcnt(4)\n" \
"v_mfma_f32_16x16x32_bf16 v[48:51], v[24:27], a[36:39], v[48:51]\n" \
"buffer_load_dwordx4 v4, s[36:39], s61 offen offset:2048 sc1 lds\n" \
"v_mfma_f32_16x16x32_bf16 v[52:55], v[28:31], a[36:39], v[52:55]\n" \
"ds_read_b128 v[16:19], v10 offset:12288\n" \
"v_mfma_f32_16x16x32_bf16 v[56:59], v[24:27], a[164:167], v[56:59]\n" \
"ds_read_b128 v[20:23], v10 offset:28672\n" \
"v_mfma_f32_16x16x32_bf16 v[60:63], v[28:31], a[164:167], v[60:63]\n" \
"s_waitcnt lgkmcnt(4)\n" \
"v_mfma_f32_16x16x32_bf16 v[48:51], v[32:35], a[40:43], v[48:51]\n" \
"buffer_load_dwordx4 v4, s[36:39], s61 offen offset:3072 sc1 lds\n" \
"v_mfma_f32_16x16x32_bf16 v[52:55], v[36:39], a[40:43], v[52:55]\n" \
"ds_read_b128 v[24:27], v10 offset:13312\n" \
"v_mfma_f32_16x16x32_bf16 v[56:59], v[32:35], a[168:171], v[56:59]\n" \
"ds_read_b128 v[28:31], v10 offset:29696\n" \
"v_mfma_f32_16x16x32_bf16 v[60:63], v[36:39], a[168:171], v[60:63]\n" \
"s_waitcnt lgkmcnt(4)\n" \
"v_mfma_f32_16x16x32_bf16 v[48:51], v[40:43], a[44:47], v[48:51]\n" \
"v_mfma_f32_16x16x32_bf16 v[52:55], v[44:47], a[44:47], v[52:55]\n" \
"ds_read_b128 v[32:35], v10 offset:14336\n" \
"v_mfma_f32_16x16x32_bf16 v[56:59], v[40:43], a[172:175], v[56:59]\n" \
"ds_read_b128 v[36:39], v10 offset:30720\n" \
"v_mfma_f32_16x16x32_bf16 v[60:63], v[44:47], a[172:175], v[60:63]\n" \
"s_waitcnt lgkmcnt(4)\n" \
"v_mfma_f32_16x16x32_bf16 v[48:51], v[16:19], a[48:51], v[48:51]\n" \
"v_mfma_f32_16x16x32_bf16 v[52:55], v[20:23], a[48:51], v[52:55]\n" \
"ds_read_b128 v[40:43], v10 offset:15360\n" \
"v_mfma_f32_16x16x32_bf16 v[56:59], v[16:19], a[176:179], v[56:59]\n" \
"ds_read_b128 v[44:47], v10 offset:31744\n" \
"v_mfma_f32_16x16x32_bf16 v[60:63], v[20:23], a[176:179], v[60:63]\n" \
"s_waitcnt lgkmcnt(4)\n" \
"v_mfma_f32_16x16x32_bf16 v[48:51], v[24:27], a[52:55], v[48:51]\n" \
"s_waitcnt vmcnt(12)\n" \
"s_barrier\n" \
"v_mfma_f32_16x16x32_bf16 v[52:55], v[28:31], a[52:55], v[52:55]\n" \
"ds_read_b128 v[16:19], v10 offset:32768\n" \
"v_mfma_f32_16x16x32_bf16 v[56:59], v[24:27], a[180:183], v[56:59]\n" \
"ds_read_b128 v[20:23], v10 offset:49152\n" \
"v_mfma_f32_16x16x32_bf16 v[60:63], v[28:31], a[180:183], v[60:63]\n" \
"s_waitcnt lgkmcnt(4)\n" \
"v_mfma_f32_16x16x32_bf16 v[48:51], v[32:35], a[56:59], v[48:51]\n" \
"v_mfma_f32_16x16x32_bf16 v[52:55], v[36:39], a[56:59], v[52:55]\n" \
"ds_read_b128 v[24:27], v10 offset:33792\n" \
"v_mfma_f32_16x16x32_bf16 v[56:59], v[32:35], a[184:187], v[56:59]\n" \
"ds_read_b128 v[28:31], v10 offset:50176\n" \
"v_mfma_f32_16x16x32_bf16 v[60:63], v[36:39], a[184:187], v[60:63]\n" \
"s_waitcnt lgkmcnt(4)\n" \
"v_mfma_f32_16x16x32_bf16 v[48:51], v[40:43], a[60:63], v[48:51]\n" \
"v_mfma_f32_16x16x32_bf16 v[52:55], v[44:47], a[60:63], v[52:55]\n" \
"ds_read_b128 v[32:35], v10 offset:34816\n" \
"v_mfma_f32_16x16x32_bf16 v[56:59], v[40:43], a[188:191], v[56:59]\n" \
"ds_read_b128 v[36:39], v10 offset:51200\n" \
"v_mfma_f32_16x16x32_bf16 v[60:63], v[44:47], a[188:191], v[60:63]\n" \
"s_waitcnt lgkmcnt(4)\n" \
"v_mfma_f32_16x16x32_bf16 v[48:51], v[16:19], a[64:67], v[48:51]\n" \
"s_add_u32 s61, s61, 28672\n" \
"s_add_u32 s56, s56, 0x20000\n" \
"s_add_u32 m0, s55, 98304\n" \
"s_nop 0\n" \
"buffer_load_dwordx4 v4, s[36:39], s61 offen offset:0 sc1 lds\n" \
"v_mfma_f32_16x16x32_bf16 v[52:55], v[20:23], a[64:67], v[52:55]\n" \
"ds_read_b128 v[40:43], v10 offset:35840\n" \
"v_mfma_f32_16x16x32_bf16 v[56:59], v[16:19], a[192:195], v[56:59]\n" \
"ds_read_b128 v[44:47], v10 offset:52224\n" \
"v_mfma_f32_16x16x32_bf16 v[60:63], v[20:23], a[192:195], v[60:63]\n" \
"s_waitcnt lgkmcnt(4)\n" \
"v_mfma_f32_16x16x32_bf16 v[48:51], v[24:27], a[68:71], v[48:51]\n" \
"buffer_load_dwordx4 v4, s[36:39], s61 offen offset:1024 sc1 lds\n" \
"v_mfma_f32_16x16x32_bf16 v[52:55], v[28:31], a[68:71], v[52:55]\n" \
"ds_read_b128 v[16:19], v10 offset:36864\n" \
"v_mfma_f32_16x16x32_bf16 v[56:59], v[24:27], a[196:199], v[56:59]\n" \
"ds_read_b128 v[20:23], v10 offset:53248\n" \
"v_mfma_f32_16x16x32_bf16 v[60:63], v[28:31], a[196:199], v[60:63]\n" \
"s_waitcnt lgkmcnt(4)\n" \
"v_mfma_f32_16x16x32_bf16 v[48:51], v[32:35], a[72:75], v[48:51]\n" \
"v_mfma_f32_16x16x32_bf16 v[52:55], v[36:39], a[72:75], v[52:55]\n" \
"ds_read_b128 v[24:27], v10 offset:37888\n" \
"v_mfma_f32_16x16x32_bf16 v[56:59], v[32:35], a[200:203], v[56:59]\n" \
"ds_read_b128 v[28:31], v10 offset:54272\n" \
"v_mfma_f32_16x16x32_bf16 v[60:63], v[36:39], a[200:203], v[60:63]\n" \
"s_waitcnt lgkmcnt(4)\n" \
"v_mfma_f32_16x16x32_bf16 v[48:51], v[40:43], a[76:79], v[48:51]\n" \
"buffer_load_dwordx4 v4, s[36:39], s61 offen offset:2048 sc1 lds\n" \
"v_mfma_f32_16x16x32_bf16 v[52:55], v[44:47], a[76:79], v[52:55]\n" \
"ds_read_b128 v[32:35], v10 offset:38912\n" \
"v_mfma_f32_16x16x32_bf16 v[56:59], v[40:43], a[204:207], v[56:59]\n" \
"ds_read_b128 v[36:39], v10 offset:55296\n" \
"v_mfma_f32_16x16x32_bf16 v[60:63], v[44:47], a[204:207], v[60:63]\n" \
"s_waitcnt lgkmcnt(4)\n" \
"v_mfma_f32_16x16x32_bf16 v[48:51], v[16:19], a[80:83], v[48:51]\n" \
"buffer_load_dwordx4 v4, s[36:39], s61 offen offset:3072 sc1 lds\n" \
"v_mfma_f32_16x16x32_bf16 v[52:55], v[20:23], a[80:83], v[52:55]\n" \
"ds_read_b128 v[40:43], v10 offset:39936\n" \
"v_mfma_f32_16x16x32_bf16 v[56:59], v[16:19], a[208:211], v[56:59]\n" \
"ds_read_b128 v[44:47], v10 offset:56320\n" \
"v_mfma_f32_16x16x32_bf16 v[60:63], v[20:23], a[208:211], v[60:63]\n" \
"s_waitcnt lgkmcnt(4)\n" \
"v_mfma_f32_16x16x32_bf16 v[48:51], v[24:27], a[84:87], v[48:51]\n" \
"v_mfma_f32_16x16x32_bf16 v[52:55], v[28:31], a[84:87], v[52:55]\n" \
"ds_read_b128 v[16:19], v10 offset:40960\n" \
"v_mfma_f32_16x16x32_bf16 v[56:59], v[24:27], a[212:215], v[56:59]\n" \
"ds_read_b128 v[20:23], v10 offset:57344\n" \
"v_mfma_f32_16x16x32_bf16 v[60:63], v[28:31], a[212:215], v[60:63]\n" \
"s_waitcnt lgkmcnt(4)\n" \
"v_mfma_f32_16x16x32_bf16 v[48:51], v[32:35], a[88:91], v[48:51]\n" \
"s_add_u32 m0, s55, 102400\n" \
"s_add_u32 s61, s61, 4096\n" \
"buffer_load_dwordx4 v4, s[36:39], s61 offen offset:0 sc1 lds\n" \
"v_mfma_f32_16x16x32_bf16 v[52:55], v[36:39], a[88:91], v[52:55]\n" \
"ds_read_b128 v[24:27], v10 offset:41984\n" \
"v_mfma_f32_16x16x32_bf16 v[56:59], v[32:35], a[216:219], v[56:59]\n" \
"ds_read_b128 v[28:31], v10 offset:58368\n" \
"v_mfma_f32_16x16x32_bf16 v[60:63], v[36:39], a[216:219], v[60:63]\n" \
"s_waitcnt lgkmcnt(4)\n" \
"v_mfma_f32_16x16x32_bf16 v[48:51], v[40:43], a[92:95], v[48:51]\n" \
"buffer_load_dwordx4 v4, s[36:39], s61 offen offset:1024 sc1 lds\n" \
"v_mfma_f32_16x16x32_bf16 v[52:55], v[44:47], a[92:95], v[52:55]\n" \
"ds_read_b128 v[32:35], v10 offset:43008\n" \
"v_mfma_f32_16x16x32_bf16 v[56:59], v[40:43], a[220:223], v[56:59]\n" \
"ds_read_b128 v[36:39], v10 offset:59392\n" \
"v_mfma_f32_16x16x32_bf16 v[60:63], v[44:47], a[220:223], v[60:63]\n" \
"s_waitcnt lgkmcnt(4)\n" \
"v_mfma_f32_16x16x32_bf16 v[48:51], v[16:19], a[96:99], v[48:51]\n" \
"v_mfma_f32_16x16x32_bf16 v[52:55], v[20:23], a[96:99], v[52:55]\n" \
"ds_read_b128 v[40:43], v10 offset:44032\n" \
"v_mfma_f32_16x16x32_bf16 v[56:59], v[16:19], a[224:227], v[56:59]\n" \
"ds_read_b128 v[44:47], v10 offset:60416\n" \
"v_mfma_f32_16x16x32_bf16 v[60:63], v[20:23], a[224:227], v[60:63]\n" \
"s_waitcnt lgkmcnt(4)\n" \
"v_mfma_f32_16x16x32_bf16 v[48:51], v[24:27], a[100:103], v[48:51]\n" \
"buffer_load_dwordx4 v4, s[36:39], s61 offen offset:2048 sc1 lds\n" \
"v_mfma_f32_16x16x32_bf16 v[52:55], v[28:31], a[100:103], v[52:55]\n" \
"ds_read_b128 v[16:19], v10 offset:45056\n" \
"v_mfma_f32_16x16x32_bf16 v[56:59], v[24:27], a[228:231], v[56:59]\n" \
"ds_read_b128 v[20:23], v10 offset:61440\n" \
"v_mfma_f32_16x16x32_bf16 v[60:63], v[28:31], a[228:231], v[60:63]\n" \
"s_waitcnt lgkmcnt(4)\n" \
"v_mfma_f32_16x16x32_bf16 v[48:51], v[32:35], a[104:107], v[48:51]\n" \
"buffer_load_dwordx4 v4, s[36:39], s61 offen offset:3072 sc1 lds\n" \
"v_mfma_f32_16x16x32_bf16 v[52:55], v[36:39], a[104:107], v[52:55]\n" \
"ds_read_b128 v[24:27], v10 offset:46080\n" \
"v_mfma_f32_16x16x32_bf16 v[56:59], v[32:35], a[232:235], v[56:59]\n" \
"ds_read_b128 v[28:31], v10 offset:62464\n" \
"v_mfma_f32_16x16x32_bf16 v[60:63], v[36:39], a[232:235], v[60:63]\n" \
"s_waitcnt lgkmcnt(4)\n" \
"v_mfma_f32_16x16x32_bf16 v[48:51], v[40:43], a[108:111], v[48:51]\n" \
"s_lshl_b32 s66, s59, s49\n" \
"s_lshr_b32 s65, s66, 8\n" \
"v_mfma_f32_16x16x32_bf16 v[52:55], v[44:47], a[108:111], v[52:55]\n" \
"ds_read_b128 v[32:35], v10 offset:47104\n" \
"v_mfma_f32_16x16x32_bf16 v[56:59], v[40:43], a[236:239], v[56:59]\n" \
"ds_read_b128 v[36:39], v10 offset:63488\n" \
"v_mfma_f32_16x16x32_bf16 v[60:63], v[44:47], a[236:239], v[60:63]\n" \
"s_waitcnt lgkmcnt(4)\n" \
"s_and_b32 s65, s65, 0x2000\n" \
"s_xor_b32 s66, s66, s65\n" \
"v_mfma_f32_16x16x32_bf16 v[48:51], v[16:19], a[112:115], v[48:51]\n" \
"v_mfma_f32_16x16x32_bf16 v[52:55], v[20:23], a[112:115], v[52:55]\n" \
"ds_read_b128 v[40:43], v10 offset:48128\n" \
"v_mfma_f32_16x16x32_bf16 v[56:59], v[16:19], a[240:243], v[56:59]\n" \
"ds_read_b128 v[44:47], v10 offset:64512\n" \
"v_mfma_f32_16x16x32_bf16 v[60:63], v[20:23], a[240:243], v[60:63]\n" \
"s_waitcnt lgkmcnt(4)\n" \
"v_mfma_f32_16x16x32_bf16 v[48:51], v[24:27], a[116:119], v[48:51]\n" \
"s_waitcnt vmcnt(8)\n" \
"s_barrier\n" \
"v_mfma_f32_16x16x32_bf16 v[52:55], v[28:31], a[116:119], v[52:55]\n" \
"ds_read_b128 v[16:19], v12 offset:0\n" \
"v_mfma_f32_16x16x32_bf16 v[56:59], v[24:27], a[244:247], v[56:59]\n" \
"ds_read_b128 v[20:23], v12 offset:16384\n" \
"v_mfma_f32_16x16x32_bf16 v[60:63], v[28:31], a[244:247], v[60:63]\n" \
"s_waitcnt lgkmcnt(4)\n" \
"buffer_store_dword v13, v6, s[36:39], 0 offen sc1\n" \
"v_mfma_f32_16x16x32_bf16 v[48:51], v[32:35], a[120:123], v[48:51]\n" \
"v_cmp_gt_u32_e64 vcc, 16, v15\n" \
"v_mfma_f32_16x16x32_bf16 v[52:55], v[36:39], a[120:123], v[52:55]\n" \
"ds_read_b128 v[24:27], v12 offset:1024\n" \
"v_mfma_f32_16x16x32_bf16 v[56:59], v[32:35], a[248:251], v[56:59]\n" \
"ds_read_b128 v[28:31], v12 offset:17408\n" \
"v_mfma_f32_16x16x32_bf16 v[60:63], v[36:39], a[248:251], v[60:63]\n" \
"s_waitcnt lgkmcnt(4)\n" \
"v_mfma_f32_16x16x32_bf16 v[48:51], v[40:43], a[124:127], v[48:51]\n" \
"s_nop 0\n" \
"s_cbranch_vccnz .Lp1v_m0_%=\n" \
".Lp1v_m0k_%=:\n" \
"v_mfma_f32_16x16x32_bf16 v[52:55], v[44:47], a[124:127], v[52:55]\n" \
"ds_read_b128 v[32:35], v12 offset:2048\n" \
"v_mfma_f32_16x16x32_bf16 v[56:59], v[40:43], a[252:255], v[56:59]\n" \
"ds_read_b128 v[36:39], v12 offset:18432\n" \
"v_mfma_f32_16x16x32_bf16 v[60:63], v[44:47], a[252:255], v[60:63]\n" \
"s_waitcnt lgkmcnt(4)\n" \
"s_xor_b32 s57, s66, s64\n" \
"s_add_u32 s58, s57, s50\n" \
"s_add_u32 s59, s59, 1\n" \
"v_mov_b32 v5, v7\n" \
"v_mov_b32 v6, v11\n" \
"s_cmp_ge_u32 s59, s47\n" \
"s_cbranch_scc1 .Lp1v_out0_%=\n" \
"v_mfma_f32_16x16x32_bf16 v[64:67], v[16:19], a[0:3], 0\n" \
"s_min_u32 s61, s56, s60\n" \
"s_lshr_b32 s65, s61, 8\n" \
"s_and_b32 s65, s65, 0x2000\n" \
"s_xor_b32 s61, s61, s65\n" \
"s_add_u32 m0, s55, 0\n" \
"s_nop 0\n" \
"buffer_load_dwordx4 v4, s[36:39], s61 offen offset:0 sc1 lds\n" \
"v_mov_b32_e64 v13, s59\n" \
"v_mfma_f32_16x16x32_bf16 v[68:71], v[20:23], a[0:3], 0\n" \
"ds_read_b128 v[40:43], v12 offset:3072\n" \
"s_nop 0\n" \
"v_mfma_f32_16x16x32_bf16 v[72:75], v[16:19], a[128:131], 0\n" \
"ds_read_b128 v[44:47], v12 offset:19456\n" \
"s_nop 0\n" \
"v_mfma_f32_16x16x32_bf16 v[76:79], v[20:23], a[128:131], 0\n" \
"s_waitcnt lgkmcnt(4)\n" \
"v_cvt_pk_bf16_f32 v80, v48, v49\n" \
"v_cvt_pk_bf16_f32 v81, v50, v51\n" \
"v_mfma_f32_16x16x32_bf16 v[64:67], v[24:27], a[4:7], v[64:67]\n" \
"buffer_load_dwordx4 v4, s[36:39], s61 offen offset:1024 sc1 lds\n" \
"v_mfma_f32_16x16x32_bf16 v[68:71], v[28:31], a[4:7], v[68:71]\n" \
"ds_read_b128 v[16:19], v12 offset:4096\n" \
"v_cvt_pk_bf16_f32 v82, v52, v53\n" \
"v_mfma_f32_16x16x32_bf16 v[72:75], v[24:27], a[132:135], v[72:75]\n" \
"ds_read_b128 v[20:23], v12 offset:20480\n" \
"v_cvt_pk_bf16_f32 v83, v54, v55\n" \
"v_mfma_f32_16x16x32_bf16 v[76:79], v[28:31], a[132:135], v[76:79]\n" \
"s_waitcnt lgkmcnt(4)\n" \
"buffer_store_dwordx4 v[80:83], v5, s[36:39], s57 offen sc1\n" \
"v_cvt_pk_bf16_f32 v84, v56, v57\n" \
"v_mfma_f32_16x16x32_bf16 v[64:67], v[32:35], a[8:11], v[64:67]\n" \
"s_add_u32 s63, s59, 2\n" \
"s_min_u32 s63, s63, 63\n" \
"v_mfma_f32_16x16x32_bf16 v[68:71], v[36:39], a[8:11], v[68:71]\n" \
"ds_read_b128 v[24:27], v12 offset:5120\n" \
"s_lshr_b32 s63, s63, 3\n" \
"s_lshl_b32 s63, s63, 7\n" \
"v_cvt_pk_bf16_f32 v85, v58, v59\n" \
"v_mfma_f32_16x16x32_bf16 v[72:75], v[32:35], a[136:139], v[72:75]\n" \
"ds_read_b128 v[28:31], v12 offset:21504\n" \
"v_cvt_pk_bf16_f32 v86, v60, v61\n" \
"v_mfma_f32_16x16x32_bf16 v[76:79], v[36:39], a[136:139], v[76:79]\n" \
"s_waitcnt lgkmcnt(4)\n" \
"v_cvt_pk_bf16_f32 v87, v62, v63\n" \
"buffer_store_dwordx4 v[84:87], v5, s[36:39], s58 offen sc1\n" \
"v_mfma_f32_16x16x32_bf16 v[64:67], v[40:43], a[12:15], v[64:67]\n" \
"buffer_load_dwordx4 v4, s[36:39], s61 offen offset:2048 sc1 lds\n" \
"v_mfma_f32_16x16x32_bf16 v[68:71], v[44:47], a[12:15], v[68:71]\n" \
"ds_read_b128 v[32:35], v12 offset:6144\n" \
"v_mfma_f32_16x16x32_bf16 v[72:75], v[40:43], a[140:143], v[72:75]\n" \
"ds_read_b128 v[36:39], v12 offset:22528\n" \
"v_mfma_f32_16x16x32_bf16 v[76:79], v[44:47], a[140:143], v[76:79]\n" \
"s_waitcnt lgkmcnt(4)\n" \
"v_mfma_f32_16x16x32_bf16 v[64:67], v[16:19], a[16:19], v[64:67]\n" \
"buffer_load_dwordx4 v4, s[36:39], s61 offen offset:3072 sc1 lds\n" \
"v_mfma_f32_16x16x32_bf16 v[68:71], v[20:23], a[16:19], v[68:71]\n" \
"ds_read_b128 v[40:43], v12 offset:7168\n" \
"v_mfma_f32_16x16x32_bf16 v[72:75], v[16:19], a[144:147], v[72:75]\n" \
"ds_read_b128 v[44:47], v12 offset:23552\n" \
"v_mfma_f32_16x16x32_bf16 v[76:79], v[20:23], a[144:147], v[76:79]\n" \
"s_waitcnt lgkmcnt(4)\n" \
"v_mfma_f32_16x16x32_bf16 v[64:67], v[24:27], a[20:23], v[64:67]\n" \
"buffer_load_dword v15, v14, s[36:39], s63 offen sc1\n" \
"v_mfma_f32_16x16x32_bf16 v[68:71], v[28:31], a[20:23], v[68:71]\n" \
"ds_read_b128 v[16:19], v12 offset:8192\n" \
"v_mfma_f32_16x16x32_bf16 v[72:75], v[24:27], a[148:151], v[72:75]\n" \
"ds_read_b128 v[20:23], v12 offset:24576\n" \
"v_mfma_f32_16x16x32_bf16 v[76:79], v[28:31], a[148:151], v[76:79]\n" \
"s_waitcnt lgkmcnt(4)\n" \
"v_mfma_f32_16x16x32_bf16 v[64:67], v[32:35], a[24:27], v[64:67]\n" \
"s_add_u32 m0, s55, 4096\n" \
"s_add_u32 s61, s61, 4096\n" \
"buffer_load_dwordx4 v4, s[36:39], s61 offen offset:0 sc1 lds\n" \
"v_mfma_f32_16x16x32_bf16 v[68:71], v[36:39], a[24:27], v[68:71]\n" \
"ds_read_b128 v[24:27], v12 offset:9216\n" \
"v_mfma_f32_16x16x32_bf16 v[72:75], v[32:35], a[152:155], v[72:75]\n" \
"ds_read_b128 v[28:31], v12 offset:25600\n" \
"v_mfma_f32_16x16x32_bf16 v[76:79], v[36:39], a[152:155], v[76:79]\n" \
"s_waitcnt lgkmcnt(4)\n" \
"v_mfma_f32_16x16x32_bf16 v[64:67], v[40:43], a[28:31], v[64:67]\n" \
"buffer_load_dwordx4 v4, s[36:39], s61 offen offset:1024 sc1 lds\n" \
"v_mfma_f32_16x16x32_bf16 v[68:71], v[44:47], a[28:31], v[68:71]\n" \
"ds_read_b128 v[32:35], v12 offset:10240\n" \
"v_mfma_f32_16x16x32_bf16 v[72:75], v[40:43], a[156:159], v[72:75]\n" \
"ds_read_b128 v[36:39], v12 offset:26624\n" \
"v_mfma_f32_16x16x32_bf16 v[76:79], v[44:47], a[156:159], v[76:79]\n" \
"s_waitcnt lgkmcnt(4)\n" \
"v_mfma_f32_16x16x32_bf16 v[64:67], v[16:19], a[32:35], v[64:67]\n" \
"v_mfma_f32_16x16x32_bf16 v[68:71], v[20:23], a[32:35], v[68:71]\n" \
"ds_read_b128 v[40:43], v12 offset:11264\n" \
"v_mfma_f32_16x16x32_bf16 v[72:75], v[16:19], a[160:163], v[72:75]\n" \
"ds_read_b128 v[44:47], v12 offset:27648\n" \
"v_mfma_f32_16x16x32_bf16 v[76:79], v[20:23], a[160:163], v[76:79]\n" \
"s_waitcnt lgkmcnt(4)\n" \
"v_mfma_f32_16x16x32_bf16 v[64:67], v[24:27], a[36:39], v[64:67]\n" \
"buffer_load_dwordx4 v4, s[36:39], s61 offen offset:2048 sc1 lds\n" \
"v_mfma_f32_16x16x32_bf16 v[68:71], v[28:31], a[36:39], v[68:71]\n" \
"ds_read_b128 v[16:19], v12 offset:12288\n" \
"v_mfma_f32_16x16x32_bf16 v[72:75], v[24:27], a[164:167], v[72:75]\n" \
"ds_read_b128 v[20:23], v12 offset:28672\n" \
"v_mfma_f32_16x16x32_bf16 v[76:79], v[28:31], a[164:167], v[76:79]\n" \
"s_waitcnt lgkmcnt(4)\n" \
"v_mfma_f32_16x16x32_bf16 v[64:67], v[32:35], a[40:43], v[64:67]\n" \
"buffer_load_dwordx4 v4, s[36:39], s61 offen offset:3072 sc1 lds\n" \
"v_mfma_f32_16x16x32_bf16 v[68:71], v[36:39], a[40:43], v[68:71]\n" \
"ds_read_b128 v[24:27], v12 offset:13312\n" \
"v_mfma_f32_16x16x32_bf16 v[72:75], v[32:35], a[168:171], v[72:75]\n" \
"ds_read_b128 v[28:31], v12 offset:29696\n" \
"v_mfma_f32_16x16x32_bf16 v[76:79], v[36:39], a[168:171], v[76:79]\n" \
"s_waitcnt lgkmcnt(4)\n" \
"v_mfma_f32_16x16x32_bf16 v[64:67], v[40:43], a[44:47], v[64:67]\n" \
"v_mfma_f32_16x16x32_bf16 v[68:71], v[44:47], a[44:47], v[68:71]\n" \
"ds_read_b128 v[32:35], v12 offset:14336\n" \
"v_mfma_f32_16x16x32_bf16 v[72:75], v[40:43], a[172:175], v[72:75]\n" \
"ds_read_b128 v[36:39], v12 offset:30720\n" \
"v_mfma_f32_16x16x32_bf16 v[76:79], v[44:47], a[172:175], v[76:79]\n" \
"s_waitcnt lgkmcnt(4)\n" \
"v_mfma_f32_16x16x32_bf16 v[64:67], v[16:19], a[48:51], v[64:67]\n" \
"v_mfma_f32_16x16x32_bf16 v[68:71], v[20:23], a[48:51], v[68:71]\n" \
"ds_read_b128 v[40:43], v12 offset:15360\n" \
"v_mfma_f32_16x16x32_bf16 v[72:75], v[16:19], a[176:179], v[72:75]\n" \
"ds_read_b128 v[44:47], v12 offset:31744\n" \
"v_mfma_f32_16x16x32_bf16 v[76:79], v[20:23], a[176:179], v[76:79]\n" \
"s_waitcnt lgkmcnt(4)\n" \
"v_mfma_f32_16x16x32_bf16 v[64:67], v[24:27], a[52:55], v[64:67]\n" \
"s_waitcnt vmcnt(12)\n" \
"s_barrier\n" \
"v_mfma_f32_16x16x32_bf16 v[68:71], v[28:31], a[52:55], v[68:71]\n" \
"ds_read_b128 v[16:19], v12 offset:32768\n" \
"v_mfma_f32_16x16x32_bf16 v[72:75], v[24:27], a[180:183], v[72:75]\n" \
"ds_read_b128 v[20:23], v12 offset:49152\n" \
"v_mfma_f32_16x16x32_bf16 v[76:79], v[28:31], a[180:183], v[76:79]\n" \
"s_waitcnt lgkmcnt(4)\n" \
"v_mfma_f32_16x16x32_bf16 v[64:67], v[32:35], a[56:59], v[64:67]\n" \
"v_mfma_f32_16x16x32_bf16 v[68:71], v[36:39], a[56:59], v[68:71]\n" \
"ds_read_b128 v[24:27], v12 offset:33792\n" \
"v_mfma_f32_16x16x32_bf16 v[72:75], v[32:35], a[184:187], v[72:75]\n" \
"ds_read_b128 v[28:31], v12 offset:50176\n" \
"v_mfma_f32_16x16x32_bf16 v[76:79], v[36:39], a[184:187], v[76:79]\n" \
"s_waitcnt lgkmcnt(4)\n" \
"v_mfma_f32_16x16x32_bf16 v[64:67], v[40:43], a[60:63], v[64:67]\n" \
"v_mfma_f32_16x16x32_bf16 v[68:71], v[44:47], a[60:63], v[68:71]\n" \
"ds_read_b128 v[32:35], v12 offset:34816\n" \
"v_mfma_f32_16x16x32_bf16 v[72:75], v[40:43], a[188:191], v[72:75]\n" \
"ds_read_b128 v[36:39], v12 offset:51200\n" \
"v_mfma_f32_16x16x32_bf16 v[76:79], v[44:47], a[188:191], v[76:79]\n" \
"s_waitcnt lgkmcnt(4)\n" \
"v_mfma_f32_16x16x32_bf16 v[64:67], v[16:19], a[64:67], v[64:67]\n" \
"s_add_u32 s61, s61, 28672\n" \
"s_add_u32 s56, s56, 0x20000\n" \
"s_add_u32 m0, s55, 32768\n" \
"s_nop 0\n" \
"buffer_load_dwordx4 v4, s[36:39], s61 offen offset:0 sc1 lds\n" \
"v_mfma_f32_16x16x32_bf16 v[68:71], v[20:23], a[64:67], v[68:71]\n" \
"ds_read_b128 v[40:43], v12 offset:35840\n" \
"v_mfma_f32_16x16x32_bf16 v[72:75], v[16:19], a[192:195], v[72:75]\n" \
"ds_read_b128 v[44:47], v12 offset:52224\n" \
"v_mfma_f32_16x16x32_bf16 v[76:79], v[20:23], a[192:195], v[76:79]\n" \
"s_waitcnt lgkmcnt(4)\n" \
"v_mfma_f32_16x16x32_bf16 v[64:67], v[24:27], a[68:71], v[64:67]\n" \
"buffer_load_dwordx4 v4, s[36:39], s61 offen offset:1024 sc1 lds\n" \
"v_mfma_f32_16x16x32_bf16 v[68:71], v[28:31], a[68:71], v[68:71]\n" \
"ds_read_b128 v[16:19], v12 offset:36864\n" \
"v_mfma_f32_16x16x32_bf16 v[72:75], v[24:27], a[196:199], v[72:75]\n" \
"ds_read_b128 v[20:23], v12 offset:53248\n" \
"v_mfma_f32_16x16x32_bf16 v[76:79], v[28:31], a[196:199], v[76:79]\n" \
"s_waitcnt lgkmcnt(4)\n" \
"v_mfma_f32_16x16x32_bf16 v[64:67], v[32:35], a[72:75], v[64:67]\n" \
"v_mfma_f32_16x16x32_bf16 v[68:71], v[36:39], a[72:75], v[68:71]\n" \
"ds_read_b128 v[24:27], v12 offset:37888\n" \
"v_mfma_f32_16x16x32_bf16 v[72:75], v[32:35], a[200:203], v[72:75]\n" \
"ds_read_b128 v[28:31], v12 offset:54272\n" \
"v_mfma_f32_16x16x32_bf16 v[76:79], v[36:39], a[200:203], v[76:79]\n" \
"s_waitcnt lgkmcnt(4)\n" \
"v_mfma_f32_16x16x32_bf16 v[64:67], v[40:43], a[76:79], v[64:67]\n" \
"buffer_load_dwordx4 v4, s[36:39], s61 offen offset:2048 sc1 lds\n" \
"v_mfma_f32_16x16x32_bf16 v[68:71], v[44:47], a[76:79], v[68:71]\n" \
"ds_read_b128 v[32:35], v12 offset:38912\n" \
"v_mfma_f32_16x16x32_bf16 v[72:75], v[40:43], a[204:207], v[72:75]\n" \
"ds_read_b128 v[36:39], v12 offset:55296\n" \
"v_mfma_f32_16x16x32_bf16 v[76:79], v[44:47], a[204:207], v[76:79]\n" \
"s_waitcnt lgkmcnt(4)\n" \
"v_mfma_f32_16x16x32_bf16 v[64:67], v[16:19], a[80:83], v[64:67]\n" \
"buffer_load_dwordx4 v4, s[36:39], s61 offen offset:3072 sc1 lds\n" \
"v_mfma_f32_16x16x32_bf16 v[68:71], v[20:23], a[80:83], v[68:71]\n" \
"ds_read_b128 v[40:43], v12 offset:39936\n" \
"v_mfma_f32_16x16x32_bf16 v[72:75], v[16:19], a[208:211], v[72:75]\n" \
"ds_read_b128 v[44:47], v12 offset:56320\n" \
"v_mfma_f32_16x16x32_bf16 v[76:79], v[20:23], a[208:211], v[76:79]\n" \
"s_waitcnt lgkmcnt(4)\n" \
"v_mfma_f32_16x16x32_bf16 v[64:67], v[24:27], a[84:87], v[64:67]\n" \
"v_mfma_f32_16x16x32_bf16 v[68:71], v[28:31], a[84:87], v[68:71]\n" \
"ds_read_b128 v[16:19], v12 offset:40960\n" \
"v_mfma_f32_16x16x32_bf16 v[72:75], v[24:27], a[212:215], v[72:75]\n" \
"ds_read_b128 v[20:23], v12 offset:57344\n" \
"v_mfma_f32_16x16x32_bf16 v[76:79], v[28:31], a[212:215], v[76:79]\n" \
"s_waitcnt lgkmcnt(4)\n" \
"v_mfma_f32_16x16x32_bf16 v[64:67], v[32:35], a[88:91], v[64:67]\n" \
"s_add_u32 m0, s55, 36864\n" \
"s_add_u32 s61, s61, 4096\n" \
"buffer_load_dwordx4 v4, s[36:39], s61 offen offset:0 sc1 lds\n" \
"v_mfma_f32_16x16x32_bf16 v[68:71], v[36:39], a[88:91], v[68:71]\n" \
"ds_read_b128 v[24:27], v12 offset:41984\n" \
"v_mfma_f32_16x16x32_bf16 v[72:75], v[32:35], a[216:219], v[72:75]\n" \
"ds_read_b128 v[28:31], v12 offset:58368\n" \
"v_mfma_f32_16x16x32_bf16 v[76:79], v[36:39], a[216:219], v[76:79]\n" \
"s_waitcnt lgkmcnt(4)\n" \
"v_mfma_f32_16x16x32_bf16 v[64:67], v[40:43], a[92:95], v[64:67]\n" \
"buffer_load_dwordx4 v4, s[36:39], s61 offen offset:1024 sc1 lds\n" \
"v_mfma_f32_16x16x32_bf16 v[68:71], v[44:47], a[92:95], v[68:71]\n" \
"ds_read_b128 v[32:35], v12 offset:43008\n" \
"v_mfma_f32_16x16x32_bf16 v[72:75], v[40:43], a[220:223], v[72:75]\n" \
"ds_read_b128 v[36:39], v12 offset:59392\n" \
"v_mfma_f32_16x16x32_bf16 v[76:79], v[44:47], a[220:223], v[76:79]\n" \
"s_waitcnt lgkmcnt(4)\n" \
"v_mfma_f32_16x16x32_bf16 v[64:67], v[16:19], a[96:99], v[64:67]\n" \
"v_mfma_f32_16x16x32_bf16 v[68:71], v[20:23], a[96:99], v[68:71]\n" \
"ds_read_b128 v[40:43], v12 offset:44032\n" \
"v_mfma_f32_16x16x32_bf16 v[72:75], v[16:19], a[224:227], v[72:75]\n" \
"ds_read_b128 v[44:47], v12 offset:60416\n" \
"v_mfma_f32_16x16x32_bf16 v[76:79], v[20:23], a[224:227], v[76:79]\n" \
"s_waitcnt lgkmcnt(4)\n" \
"v_mfma_f32_16x16x32_bf16 v[64:67], v[24:27], a[100:103], v[64:67]\n" \
"buffer_load_dwordx4 v4, s[36:39], s61 offen offset:2048 sc1 lds\n" \
"v_mfma_f32_16x16x32_bf16 v[68:71], v[28:31], a[100:103], v[68:71]\n" \
"ds_read_b128 v[16:19], v12 offset:45056\n" \
"v_mfma_f32_16x16x32_bf16 v[72:75], v[24:27], a[228:231], v[72:75]\n" \
"ds_read_b128 v[20:23], v12 offset:61440\n" \
"v_mfma_f32_16x16x32_bf16 v[76:79], v[28:31], a[228:231], v[76:79]\n" \
"s_waitcnt lgkmcnt(4)\n" \
"v_mfma_f32_16x16x32_bf16 v[64:67], v[32:35], a[104:107], v[64:67]\n" \
"buffer_load_dwordx4 v4, s[36:39], s61 offen offset:3072 sc1 lds\n" \
"v_mfma_f32_16x16x32_bf16 v[68:71], v[36:39], a[104:107], v[68:71]\n" \
"ds_read_b128 v[24:27], v12 offset:46080\n" \
"v_mfma_f32_16x16x32_bf16 v[72:75], v[32:35], a[232:235], v[72:75]\n" \
"ds_read_b128 v[28:31], v12 offset:62464\n" \
"v_mfma_f32_16x16x32_bf16 v[76:79], v[36:39], a[232:235], v[76:79]\n" \
"s_waitcnt lgkmcnt(4)\n" \
"v_mfma_f32_16x16x32_bf16 v[64:67], v[40:43], a[108:111], v[64:67]\n" \
"s_lshl_b32 s66, s59, s49\n" \
"s_lshr_b32 s65, s66, 8\n" \
"v_mfma_f32_16x16x32_bf16 v[68:71], v[44:47], a[108:111], v[68:71]\n" \
"ds_read_b128 v[32:35], v12 offset:47104\n" \
"v_mfma_f32_16x16x32_bf16 v[72:75], v[40:43], a[236:239], v[72:75]\n" \
"ds_read_b128 v[36:39], v12 offset:63488\n" \
"v_mfma_f32_16x16x32_bf16 v[76:79], v[44:47], a[236:239], v[76:79]\n" \
"s_waitcnt lgkmcnt(4)\n" \
"s_and_b32 s65, s65, 0x2000\n" \
"s_xor_b32 s66, s66, s65\n" \
"v_mfma_f32_16x16x32_bf16 v[64:67], v[16:19], a[112:115], v[64:67]\n" \
"v_mfma_f32_16x16x32_bf16 v[68:71], v[20:23], a[112:115], v[68:71]\n" \
"ds_read_b128 v[40:43], v12 offset:48128\n" \
"v_mfma_f32_16x16x32_bf16 v[72:75], v[16:19], a[240:243], v[72:75]\n" \
"ds_read_b128 v[44:47], v12 offset:64512\n" \
"v_mfma_f32_16x16x32_bf16 v[76:79], v[20:23], a[240:243], v[76:79]\n" \
"s_waitcnt lgkmcnt(4)\n" \
"v_mfma_f32_16x16x32_bf16 v[64:67], v[24:27], a[116:119], v[64:67]\n" \
"s_waitcnt vmcnt(8)\n" \
"s_barrier\n" \
"v_mfma_f32_16x16x32_bf16 v[68:71], v[28:31], a[116:119], v[68:71]\n" \
"ds_read_b128 v[16:19], v10 offset:0\n" \
"v_mfma_f32_16x16x32_bf16 v[72:75], v[24:27], a[244:247], v[72:75]\n" \
"ds_read_b128 v[20:23], v10 offset:16384\n" \
"v_mfma_f32_16x16x32_bf16 v[76:79], v[28:31], a[244:247], v[76:79]\n" \
"s_waitcnt lgkmcnt(4)\n" \
"buffer_store_dword v13, v6, s[36:39], 0 offen sc1\n" \
"v_mfma_f32_16x16x32_bf16 v[64:67], v[32:35], a[120:123], v[64:67]\n" \
"v_cmp_gt_u32_e64 vcc, 16, v15\n" \
"v_mfma_f32_16x16x32_bf16 v[68:71], v[36:39], a[120:123], v[68:71]\n" \
"ds_read_b128 v[24:27], v10 offset:1024\n" \
"v_mfma_f32_16x16x32_bf16 v[72:75], v[32:35], a[248:251], v[72:75]\n" \
"ds_read_b128 v[28:31], v10 offset:17408\n" \
"v_mfma_f32_16x16x32_bf16 v[76:79], v[36:39], a[248:251], v[76:79]\n" \
"s_waitcnt lgkmcnt(4)\n" \
"v_mfma_f32_16x16x32_bf16 v[64:67], v[40:43], a[124:127], v[64:67]\n" \
"s_cbranch_vccnz .Lp1v_m1_%=\n" \
".Lp1v_m1k_%=:\n" \
"v_mfma_f32_16x16x32_bf16 v[68:71], v[44:47], a[124:127], v[68:71]\n" \
"ds_read_b128 v[32:35], v10 offset:2048\n" \
"v_mfma_f32_16x16x32_bf16 v[72:75], v[40:43], a[252:255], v[72:75]\n" \
"ds_read_b128 v[36:39], v10 offset:18432\n" \
"v_mfma_f32_16x16x32_bf16 v[76:79], v[44:47], a[252:255], v[76:79]\n" \
"s_waitcnt lgkmcnt(4)\n" \
"s_xor_b32 s57, s66, s64\n" \
"s_add_u32 s58, s57, s50\n" \
"s_add_u32 s59, s59, 1\n" \
"v_mov_b32 v5, v7\n" \
"v_mov_b32 v6, v11\n" \
"s_cmp_ge_u32 s59, s47\n" \
"s_cbranch_scc0 .Lp1v_loop_%=\n" \
".Lp1v_out1_%=:\n" \
"s_nop 7\n" \
"s_nop 7\n" \
"v_cvt_pk_bf16_f32 v80, v64, v65\n" \
"v_cvt_pk_bf16_f32 v81, v66, v67\n" \
"v_cvt_pk_bf16_f32 v82, v68, v69\n" \
"v_cvt_pk_bf16_f32 v83, v70, v71\n" \
"buffer_store_dwordx4 v[80:83], v5, s[36:39], s57 offen sc1\n" \
"v_cvt_pk_bf16_f32 v84, v72, v73\n" \
"v_cvt_pk_bf16_f32 v85, v74, v75\n" \
"v_cvt_pk_bf16_f32 v86, v76, v77\n" \
"v_cvt_pk_bf16_f32 v87, v78, v79\n" \
"buffer_store_dwordx4 v[84:87], v5, s[36:39], s58 offen sc1\n" \
"s_branch .Lp1v_end_%=\n" \
".Lp1v_out0_%=:\n" \
"s_nop 7\n" \
"s_nop 7\n" \
"v_cvt_pk_bf16_f32 v80, v48, v49\n" \
"v_cvt_pk_bf16_f32 v81, v50, v51\n" \
"v_cvt_pk_bf16_f32 v82, v52, v53\n" \
"v_cvt_pk_bf16_f32 v83, v54, v55\n" \
"buffer_store_dwordx4 v[80:83], v5, s[36:39], s57 offen sc1\n" \
"v_cvt_pk_bf16_f32 v84, v56, v57\n" \
"v_cvt_pk_bf16_f32 v85, v58, v59\n" \
"v_cvt_pk_bf16_f32 v86, v60, v61\n" \
"v_cvt_pk_bf16_f32 v87, v62, v63\n" \
"buffer_store_dwordx4 v[84:87], v5, s[36:39], s58 offen sc1\n" \
".Lp1v_end_%=:\n" \
"s_waitcnt vmcnt(0) lgkmcnt(0)\n" \
"s_barrier\n" \
"v_mov_b32 v13, s59\n" \
"buffer_store_dword v13, v6, s[36:39], 0 offen sc1\n" \
"s_waitcnt vmcnt(0) lgkmcnt(0)\n" \
"s_branch .Lp1v_x_%=\n" \
".Lp1v_l0_%=:\n" \
"s_nop 7\n" \
"s_nop 7\n" \
"s_waitcnt vmcnt(0) lgkmcnt(0)\n" \
"s_mov_b32 s62, 0\n" \
".Lp1v_l0a_%=:\n" \
"buffer_load_dword v15, v14, s[36:39], s63 offen sc1\n" \
"s_waitcnt vmcnt(0)\n" \
"v_cmp_gt_u32 vcc, 16, v15\n" \
"s_nop 4\n" \
"s_cbranch_vccz .Lp1v_l0k_%=\n" \
"s_sleep 4\n" \
"s_add_u32 s62, s62, 1\n" \
"s_cmp_lt_u32 s62, 0x10000\n" \
"s_cbranch_scc1 .Lp1v_l0a_%=\n" \
"v_mov_b32 v15, 256\n" \
"buffer_store_dword v15, v15, s[36:39], 0 offen sc1\n" \
"s_waitcnt vmcnt(0) lgkmcnt(0)\n" \
"s_branch .Lp1v_l0k_%=\n" \
".Lp1v_l1_%=:\n" \
"s_nop 7\n" \
"s_nop 7\n" \
"s_waitcnt vmcnt(0) lgkmcnt(0)\n" \
"s_mov_b32 s62, 0\n" \
".Lp1v_l1a_%=:\n" \
"buffer_load_dword v15, v14, s[36:39], s63 offen sc1\n" \
"s_waitcnt vmcnt(0)\n" \
"v_cmp_gt_u32 vcc, 16, v15\n" \
"s_nop 4\n" \
"s_cbranch_vccz .Lp1v_l1k_%=\n" \
"s_sleep 4\n" \
"s_add_u32 s62, s62, 1\n" \
"s_cmp_lt_u32 s62, 0x10000\n" \
"s_cbranch_scc1 .Lp1v_l1a_%=\n" \
"v_mov_b32 v15, 256\n" \
"buffer_store_dword v15, v15, s[36:39], 0 offen sc1\n" \
"s_waitcnt vmcnt(0) lgkmcnt(0)\n" \
"s_branch .Lp1v_l1k_%=\n" \
".Lp1v_m0_%=:\n" \
"s_nop 7\n" \
"s_nop 7\n" \
"s_waitcnt vmcnt(0) lgkmcnt(0)\n" \
"s_mov_b32 s62, 0\n" \
".Lp1v_m0a_%=:\n" \
"buffer_load_dword v15, v14, s[36:39], s63 offen sc1\n" \
"s_waitcnt vmcnt(0)\n" \
"v_cmp_gt_u32 vcc, 16, v15\n" \
"s_nop 4\n" \
"s_cbranch_vccz .Lp1v_m0k_%=\n" \
"s_sleep 4\n" \
"s_add_u32 s62, s62, 1\n" \
"s_cmp_lt_u32 s62, 0x10000\n" \
"s_cbranch_scc1 .Lp1v_m0a_%=\n" \
"v_mov_b32 v15, 256\n" \
"buffer_store_dword v15, v15, s[36:39], 0 offen sc1\n" \
"s_waitcnt vmcnt(0) lgkmcnt(0)\n" \
"s_branch .Lp1v_m0k_%=\n" \
".Lp1v_m1_%=:\n" \
"s_nop 7\n" \
"s_nop 7\n" \
"s_waitcnt vmcnt(0) lgkmcnt(0)\n" \
"s_mov_b32 s62, 0\n" \
".Lp1v_m1a_%=:\n" \
"buffer_load_dword v15, v14, s[36:39], s63 offen sc1\n" \
"s_waitcnt vmcnt(0)\n" \
"v_cmp_gt_u32 vcc, 16, v15\n" \
"s_nop 4\n" \
"s_cbranch_vccz .Lp1v_m1k_%=\n" \
"s_sleep 4\n" \
"s_add_u32 s62, s62, 1\n" \
"s_cmp_lt_u32 s62, 0x10000\n" \
"s_cbranch_scc1 .Lp1v_m1a_%=\n" \
"v_mov_b32 v15, 256\n" \
"buffer_store_dword v15, v15, s[36:39], 0 offen sc1\n" \
"s_waitcnt vmcnt(0) lgkmcnt(0)\n" \
"s_branch .Lp1v_m1k_%=\n" \
".Lp1v_x_%=:\n"
#define ASM_P1_G1 \
"s_nop 7\n" \
"s_nop 7\n" \
"s_lshr_b32 s52, s48, 1\n" \
"s_lshl_b32 s52, s52, 13\n" \
"s_and_b32 s53, s48, 1\n" \
"s_lshl_b32 s53, s53, 10\n" \
"s_add_u32 s52, s52, s53\n" \
"s_mov_b32 s54, 0\n" \
"s_add_u32 m0, s52, 0\n" \
"s_nop 0\n" \
"buffer_load_dwordx4 v1, s[40:43], s54 offen nt lds\n" \
"s_add_u32 s54, s54, s44\n" \
"s_add_u32 m0, s52, 2048\n" \
"s_nop 0\n" \
"buffer_load_dwordx4 v1, s[40:43], s54 offen nt lds\n" \
"s_add_u32 s54, s54, s44\n" \
"s_add_u32 m0, s52, 4096\n" \
"s_nop 0\n" \
"buffer_load_dwordx4 v1, s[40:43], s54 offen nt lds\n" \
"s_add_u32 s54, s54, s44\n" \
"s_add_u32 m0, s52, 6144\n" \
"s_nop 0\n" \
"buffer_load_dwordx4 v1, s[40:43], s54 offen nt lds\n" \
"s_add_u32 s54, s54, s44\n" \
"s_add_u32 m0, s52, 16384\n" \
"s_nop 0\n" \
"buffer_load_dwordx4 v1, s[40:43], s54 offen nt lds\n" \
"s_add_u32 s54, s54, s44\n" \
"s_add_u32 m0, s52, 18432\n" \
"s_nop 0\n" \
"buffer_load_dwordx4 v1, s[40:43], s54 offen nt lds\n" \
"s_add_u32 s54, s54, s44\n" \
"s_add_u32 m0, s52, 20480\n" \
"s_nop 0\n" \
"buffer_load_dwordx4 v1, s[40:43], s54 offen nt lds\n" \
"s_add_u32 s54, s54, s44\n" \
"s_add_u32 m0, s52, 22528\n" \
"s_nop 0\n" \
"buffer_load_dwordx4 v1, s[40:43], s54 offen nt lds\n" \
"s_add_u32 s54, s54, s44\n" \
"s_add_u32 m0, s52, 32768\n" \
"s_nop 0\n" \
"buffer_load_dwordx4 v1, s[40:43], s54 offen nt lds\n" \
"s_add_u32 s54, s54, s44\n" \
"s_add_u32 m0, s52, 34816\n" \
"s_nop 0\n" \
"buffer_load_dwordx4 v1, s[40:43], s54 offen nt lds\n" \
"s_add_u32 s54, s54, s44\n" \
"s_add_u32 m0, s52, 36864\n" \
"s_nop 0\n" \
"buffer_load_dwordx4 v1, s[40:43], s54 offen nt lds\n" \
"s_add_u32 s54, s54, s44\n" \
"s_add_u32 m0, s52, 38912\n" \
"s_nop 0\n" \
"buffer_load_dwordx4 v1, s[40:43], s54 offen nt lds\n" \
"s_add_u32 s54, s54, s44\n" \
"s_add_u32 m0, s52, 49152\n" \
"s_nop 0\n" \
"buffer_load_dwordx4 v1, s[40:43], s54 offen nt lds\n" \
"s_add_u32 s54, s54, s44\n" \
"s_add_u32 m0, s52, 51200\n" \
"s_nop 0\n" \
"buffer_load_dwordx4 v1, s[40:43], s54 offen nt lds\n" \
"s_add_u32 s54, s54, s44\n" \
"s_add_u32 m0, s52, 53248\n" \
"s_nop 0\n" \
"buffer_load_dwordx4 v1, s[40:43], s54 offen nt lds\n" \
"s_add_u32 s54, s54, s44\n" \
"s_add_u32 m0, s52, 55296\n" \
"s_nop 0\n" \
"buffer_load_dwordx4 v1, s[40:43], s54 offen nt lds\n" \
"s_add_u32 s54, s54, s44\n" \
"s_add_u32 m0, s52, 65536\n" \
"s_nop 0\n" \
"buffer_load_dwordx4 v1, s[40:43], s54 offen nt lds\n" \
"s_add_u32 s54, s54, s44\n" \
"s_add_u32 m0, s52, 67584\n" \
"s_nop 0\n" \
"buffer_load_dwordx4 v1, s[40:43], s54 offen nt lds\n" \
"s_add_u32 s54, s54, s44\n" \
"s_add_u32 m0, s52, 69632\n" \
"s_nop 0\n" \
"buffer_load_dwordx4 v1, s[40:43], s54 offen nt lds\n" \
"s_add_u32 s54, s54, s44\n" \
"s_add_u32 m0, s52, 71680\n" \
"s_nop 0\n" \
"buffer_load_dwordx4 v1, s[40:43], s54 offen nt lds\n" \
"s_add_u32 s54, s54, s44\n" \
"s_add_u32 m0, s52, 81920\n" \
"s_nop 0\n" \
"buffer_load_dwordx4 v1, s[40:43], s54 offen nt lds\n" \
"s_add_u32 s54, s54, s44\n" \
"s_add_u32 m0, s52, 83968\n" \
"s_nop 0\n" \
"buffer_load_dwordx4 v1, s[40:43], s54 offen nt lds\n" \
"s_add_u32 s54, s54, s44\n" \
"s_add_u32 m0, s52, 86016\n" \
"s_nop 0\n" \
"buffer_load_dwordx4 v1, s[40:43], s54 offen nt lds\n" \
"s_add_u32 s54, s54, s44\n" \
"s_add_u32 m0, s52, 88064\n" \
"s_nop 0\n" \
"buffer_load_dwordx4 v1, s[40:43], s54 offen nt lds\n" \
"s_add_u32 s54, s54, s44\n" \
"s_add_u32 m0, s52, 98304\n" \
"s_nop 0\n" \
"buffer_load_dwordx4 v1, s[40:43], s54 offen nt lds\n" \
"s_add_u32 s54, s54, s44\n" \
"s_add_u32 m0, s52, 100352\n" \
"s_nop 0\n" \
"buffer_load_dwordx4 v1, s[40:43], s54 offen nt lds\n" \
"s_add_u32 s54, s54, s44\n" \
"s_add_u32 m0, s52, 102400\n" \
"s_nop 0\n" \
"buffer_load_dwordx4 v1, s[40:43], s54 offen nt lds\n" \
"s_add_u32 s54, s54, s44\n" \
"s_add_u32 m0, s52, 104448\n" \
"s_nop 0\n" \
"buffer_load_dwordx4 v1, s[40:43], s54 offen nt lds\n" \
"s_add_u32 s54, s54, s44\n" \
"s_add_u32 m0, s52, 114688\n" \
"s_nop 0\n" \
"buffer_load_dwordx4 v1, s[40:43], s54 offen nt lds\n" \
"s_add_u32 s54, s54, s44\n" \
"s_add_u32 m0, s52, 116736\n" \
"s_nop 0\n" \
"buffer_load_dwordx4 v1, s[40:43], s54 offen nt lds\n" \
"s_add_u32 s54, s54, s44\n" \
"s_add_u32 m0, s52, 118784\n" \
"s_nop 0\n" \
"buffer_load_dwordx4 v1, s[40:43], s54 offen nt lds\n" \
"s_add_u32 s54, s54, s44\n" \
"s_add_u32 m0, s52, 120832\n" \
"s_nop 0\n" \
"buffer_load_dwordx4 v1, s[40:43], s54 offen nt lds\n" \
"s_add_u32 s54, s54, s44\n" \
"v_add_u32 v8, 0x10000, v2\n" \
"v_add_u32 v9, 0x10000, v3\n" \
"s_waitcnt vmcnt(28)\n" \
"s_barrier\n" \
"ds_read_b64_tr_b16 a[0:1], v2 offset:0\n" \
"ds_read_b64_tr_b16 a[2:3], v2 offset:512\n" \
"ds_read_b64_tr_b16 a[128:129], v3 offset:0\n" \
"ds_read_b64_tr_b16 a[130:131], v3 offset:512\n" \
"ds_read_b64_tr_b16 a[4:5], v2 offset:4096\n" \
"ds_read_b64_tr_b16 a[6:7], v2 offset:4608\n" \
"ds_read_b64_tr_b16 a[132:133], v3 offset:4096\n" \
"ds_read_b64_tr_b16 a[134:135], v3 offset:4608\n" \
"s_waitcnt lgkmcnt(0)\n" \
"s_barrier\n" \
"s_add_u32 m0, s52, 0\n" \
"s_nop 0\n" \
"buffer_load_dwordx4 v1, s[40:43], s54 offen nt lds\n" \
"s_add_u32 s54, s54, s44\n" \
"s_add_u32 m0, s52, 2048\n" \
"s_nop 0\n" \
"buffer_load_dwordx4 v1, s[40:43], s54 offen nt lds\n" \
"s_add_u32 s54, s54, s44\n" \
"s_add_u32 m0, s52, 4096\n" \
"s_nop 0\n" \
"buffer_load_dwordx4 v1, s[40:43], s54 offen nt lds\n" \
"s_add_u32 s54, s54, s44\n" \
"s_add_u32 m0, s52, 6144\n" \
"s_nop 0\n" \
"buffer_load_dwordx4 v1, s[40:43], s54 offen nt lds\n" \
"s_add_u32 s54, s54, s44\n" \
"s_waitcnt vmcnt(28)\n" \
"s_barrier\n" \
"ds_read_b64_tr_b16 a[8:9], v2 offset:16384\n" \
"ds_read_b64_tr_b16 a[10:11], v2 offset:16896\n" \
"ds_read_b64_tr_b16 a[136:137], v3 offset:16384\n" \
"ds_read_b64_tr_b16 a[138:139], v3 offset:16896\n" \
"ds_read_b64_tr_b16 a[12:13], v2 offset:20480\n" \
"ds_read_b64_tr_b16 a[14:15], v2 offset:20992\n" \
"ds_read_b64_tr_b16 a[140:141], v3 offset:20480\n" \
"ds_read_b64_tr_b16 a[142:143], v3 offset:20992\n" \
"s_waitcnt lgkmcnt(0)\n" \
"s_barrier\n" \
"s_add_u32 m0, s52, 16384\n" \
"s_nop 0\n" \
"buffer_load_dwordx4 v1, s[40:43], s54 offen nt lds\n" \
"s_add_u32 s54, s54, s44\n" \
"s_add_u32 m0, s52, 18432\n" \
"s_nop 0\n" \
"buffer_load_dwordx4 v1, s[40:43], s54 offen nt lds\n" \
"s_add_u32 s54, s54, s44\n" \
"s_add_u32 m0, s52, 20480\n" \
"s_nop 0\n" \
"buffer_load_dwordx4 v1, s[40:43], s54 offen nt lds\n" \
"s_add_u32 s54, s54, s44\n" \
"s_add_u32 m0, s52, 22528\n" \
"s_nop 0\n" \
"buffer_load_dwordx4 v1, s[40:43], s54 offen nt lds\n" \
"s_add_u32 s54, s54, s44\n" \
"s_waitcnt vmcnt(28)\n" \
"s_barrier\n" \
"ds_read_b64_tr_b16 a[16:17], v2 offset:32768\n" \
"ds_read_b64_tr_b16 a[18:19], v2 offset:33280\n" \
"ds_read_b64_tr_b16 a[144:145], v3 offset:32768\n" \
"ds_read_b64_tr_b16 a[146:147], v3 offset:33280\n" \
"ds_read_b64_tr_b16 a[20:21], v2 offset:36864\n" \
"ds_read_b64_tr_b16 a[22:23], v2 offset:37376\n" \
"ds_read_b64_tr_b16 a[148:149], v3 offset:36864\n" \
"ds_read_b64_tr_b16 a[150:151], v3 offset:37376\n" \
"s_waitcnt lgkmcnt(0)\n" \
"s_barrier\n" \
"s_add_u32 m0, s52, 32768\n" \
"s_nop 0\n" \
"buffer_load_dwordx4 v1, s[40:43], s54 offen nt lds\n" \
"s_add_u32 s54, s54, s44\n" \
"s_add_u32 m0, s52, 34816\n" \
"s_nop 0\n" \
"buffer_load_dwordx4 v1, s[40:43], s54 offen nt lds\n" \
"s_add_u32 s54, s54, s44\n" \
"s_add_u32 m0, s52, 36864\n" \
"s_nop 0\n" \
"buffer_load_dwordx4 v1, s[40:43], s54 offen nt lds\n" \
"s_add_u32 s54, s54, s44\n" \
"s_add_u32 m0, s52, 38912\n" \
"s_nop 0\n" \
"buffer_load_dwordx4 v1, s[40:43], s54 offen nt lds\n" \
"s_add_u32 s54, s54, s44\n" \
"s_waitcnt vmcnt(28)\n" \
"s_barrier\n" \
"ds_read_b64_tr_b16 a[24:25], v2 offset:49152\n" \
"ds_read_b64_tr_b16 a[26:27], v2 offset:49664\n" \
"ds_read_b64_tr_b16 a[152:153], v3 offset:49152\n" \
"ds_read_b64_tr_b16 a[154:155], v3 offset:49664\n" \
"ds_read_b64_tr_b16 a[28:29], v2 offset:53248\n" \
"ds_read_b64_tr_b16 a[30:31], v2 offset:53760\n" \
"ds_read_b64_tr_b16 a[156:157], v3 offset:53248\n" \
"ds_read_b64_tr_b16 a[158:159], v3 offset:53760\n" \
"s_waitcnt lgkmcnt(0)\n" \
"s_barrier\n" \
"s_add_u32 m0, s52, 49152\n" \
"s_nop 0\n" \
"buffer_load_dwordx4 v1, s[40:43], s54 offen nt lds\n" \
"s_add_u32 s54, s54, s44\n" \
"s_add_u32 m0, s52, 51200\n" \
"s_nop 0\n" \
"buffer_load_dwordx4 v1, s[40:43], s54 offen nt lds\n" \
"s_add_u32 s54, s54, s44\n" \
"s_add_u32 m0, s52, 53248\n" \
"s_nop 0\n" \
"buffer_load_dwordx4 v1, s[40:43], s54 offen nt lds\n" \
"s_add_u32 s54, s54, s44\n" \
"s_add_u32 m0, s52, 55296\n" \
"s_nop 0\n" \
"buffer_load_dwordx4 v1, s[40:43], s54 offen nt lds\n" \
"s_add_u32 s54, s54, s44\n" \
"s_waitcnt vmcnt(28)\n" \
"s_barrier\n" \
"ds_read_b64_tr_b16 a[32:33], v8 offset:0\n" \
"ds_read_b64_tr_b16 a[34:35], v8 offset:512\n" \
"ds_read_b64_tr_b16 a[160:161], v9 offset:0\n" \
"ds_read_b64_tr_b16 a[162:163], v9 offset:512\n" \
"ds_read_b64_tr_b16 a[36:37], v8 offset:4096\n" \
"ds_read_b64_tr_b16 a[38:39], v8 offset:4608\n" \
"ds_read_b64_tr_b16 a[164:165], v9 offset:4096\n" \
"ds_read_b64_tr_b16 a[166:167], v9 offset:4608\n" \
"s_waitcnt lgkmcnt(0)\n" \
"s_barrier\n" \
"s_add_u32 m0, s52, 65536\n" \
"s_nop 0\n" \
"buffer_load_dwordx4 v1, s[40:43], s54 offen nt lds\n" \
"s_add_u32 s54, s54, s44\n" \
"s_add_u32 m0, s52, 67584\n" \
"s_nop 0\n" \
"buffer_load_dwordx4 v1, s[40:43], s54 offen nt lds\n" \
"s_add_u32 s54, s54, s44\n" \
"s_add_u32 m0, s52, 69632\n" \
"s_nop 0\n" \
"buffer_load_dwordx4 v1, s[40:43], s54 offen nt lds\n" \
"s_add_u32 s54, s54, s44\n" \
"s_add_u32 m0, s52, 71680\n" \
"s_nop 0\n" \
"buffer_load_dwordx4 v1, s[40:43], s54 offen nt lds\n" \
"s_add_u32 s54, s54, s44\n" \
"s_waitcnt vmcnt(28)\n" \
"s_barrier\n" \
"ds_read_b64_tr_b16 a[40:41], v8 offset:16384\n" \
"ds_read_b64_tr_b16 a[42:43], v8 offset:16896\n" \
"ds_read_b64_tr_b16 a[168:169], v9 offset:16384\n" \
"ds_read_b64_tr_b16 a[170:171], v9 offset:16896\n" \
"ds_read_b64_tr_b16 a[44:45], v8 offset:20480\n" \
"ds_read_b64_tr_b16 a[46:47], v8 offset:20992\n" \
"ds_read_b64_tr_b16 a[172:173], v9 offset:20480\n" \
"ds_read_b64_tr_b16 a[174:175], v9 offset:20992\n" \
"s_waitcnt lgkmcnt(0)\n" \
"s_barrier\n" \
"s_add_u32 m0, s52, 81920\n" \
"s_nop 0\n" \
"buffer_load_dwordx4 v1, s[40:43], s54 offen nt lds\n" \
"s_add_u32 s54, s54, s44\n" \
"s_add_u32 m0, s52, 83968\n" \
"s_nop 0\n" \
"buffer_load_dwordx4 v1, s[40:43], s54 offen nt lds\n" \
"s_add_u32 s54, s54, s44\n" \
"s_add_u32 m0, s52, 86016\n" \
"s_nop 0\n" \
"buffer_load_dwordx4 v1, s[40:43], s54 offen nt lds\n" \
"s_add_u32 s54, s54, s44\n" \
"s_add_u32 m0, s52, 88064\n" \
"s_nop 0\n" \
"buffer_load_dwordx4 v1, s[40:43], s54 offen nt lds\n" \
"s_add_u32 s54, s54, s44\n" \
"s_waitcnt vmcnt(28)\n" \
"s_barrier\n" \
"ds_read_b64_tr_b16 a[48:49], v8 offset:32768\n" \
"ds_read_b64_tr_b16 a[50:51], v8 offset:33280\n" \
"ds_read_b64_tr_b16 a[176:177], v9 offset:32768\n" \
"ds_read_b64_tr_b16 a[178:179], v9 offset:33280\n" \
"ds_read_b64_tr_b16 a[52:53], v8 offset:36864\n" \
"ds_read_b64_tr_b16 a[54:55], v8 offset:37376\n" \
"ds_read_b64_tr_b16 a[180:181], v9 offset:36864\n" \
"ds_read_b64_tr_b16 a[182:183], v9 offset:37376\n" \
"s_waitcnt lgkmcnt(0)\n" \
"s_barrier\n" \
"s_add_u32 m0, s52, 98304\n" \
"s_nop 0\n" \
"buffer_load_dwordx4 v1, s[40:43], s54 offen nt lds\n" \
"s_add_u32 s54, s54, s44\n" \
"s_add_u32 m0, s52, 100352\n" \
"s_nop 0\n" \
"buffer_load_dwordx4 v1, s[40:43], s54 offen nt lds\n" \
"s_add_u32 s54, s54, s44\n" \
"s_add_u32 m0, s52, 102400\n" \
"s_nop 0\n" \
"buffer_load_dwordx4 v1, s[40:43], s54 offen nt lds\n" \
"s_add_u32 s54, s54, s44\n" \
"s_add_u32 m0, s52, 104448\n" \
"s_nop 0\n" \
"buffer_load_dwordx4 v1, s[40:43], s54 offen nt lds\n" \
"s_add_u32 s54, s54, s44\n" \
"s_waitcnt vmcnt(28)\n" \
"s_barrier\n" \
"ds_read_b64_tr_b16 a[56:57], v8 offset:49152\n" \
"ds_read_b64_tr_b16 a[58:59], v8 offset:49664\n" \
"ds_read_b64_tr_b16 a[184:185], v9 offset:49152\n" \
"ds_read_b64_tr_b16 a[186:187], v9 offset:49664\n" \
"ds_read_b64_tr_b16 a[60:61], v8 offset:53248\n" \
"ds_read_b64_tr_b16 a[62:63], v8 offset:53760\n" \
"ds_read_b64_tr_b16 a[188:189], v9 offset:53248\n" \
"ds_read_b64_tr_b16 a[190:191], v9 offset:53760\n" \
"s_waitcnt lgkmcnt(0)\n" \
"s_barrier\n" \
"s_add_u32 m0, s52, 114688\n" \
"s_nop 0\n" \
"buffer_load_dwordx4 v1, s[40:43], s54 offen nt lds\n" \
"s_add_u32 s54, s54, s44\n" \
"s_add_u32 m0, s52, 116736\n" \
"s_nop 0\n" \
"buffer_load_dwordx4 v1, s[40:43], s54 offen nt lds\n" \
"s_add_u32 s54, s54, s44\n" \
"s_add_u32 m0, s52, 118784\n" \
"s_nop 0\n" \
"buffer_load_dwordx4 v1, s[40:43], s54 offen nt lds\n" \
"s_add_u32 s54, s54, s44\n" \
"s_add_u32 m0, s52, 120832\n" \
"s_nop 0\n" \
"buffer_load_dwordx4 v1, s[40:43], s54 offen nt lds\n" \
"s_add_u32 s54, s54, s44\n" \
"s_waitcnt vmcnt(28)\n" \
"s_barrier\n" \
"ds_read_b64_tr_b16 a[64:65], v2 offset:0\n" \
"ds_read_b64_tr_b16 a[66:67], v2 offset:512\n" \
"ds_read_b64_tr_b16 a[192:193], v3 offset:0\n" \
"ds_read_b64_tr_b16 a[194:195], v3 offset:512\n" \
"ds_read_b64_tr_b16 a[68:69], v2 offset:4096\n" \
"ds_read_b64_tr_b16 a[70:71], v2 offset:4608\n" \
"ds_read_b64_tr_b16 a[196:197], v3 offset:4096\n" \
"ds_read_b64_tr_b16 a[198:199], v3 offset:4608\n" \
"s_waitcnt vmcnt(24)\n" \
"s_barrier\n" \
"ds_read_b64_tr_b16 a[72:73], v2 offset:16384\n" \
"ds_read_b64_tr_b16 a[74:75], v2 offset:16896\n" \
"ds_read_b64_tr_b16 a[200:201], v3 offset:16384\n" \
"ds_read_b64_tr_b16 a[202:203], v3 offset:16896\n" \
"ds_read_b64_tr_b16 a[76:77], v2 offset:20480\n" \
"ds_read_b64_tr_b16 a[78:79], v2 offset:20992\n" \
"ds_read_b64_tr_b16 a[204:205], v3 offset:20480\n" \
"ds_read_b64_tr_b16 a[206:207], v3 offset:20992\n" \
"s_waitcnt vmcnt(20)\n" \
"s_barrier\n" \
"ds_read_b64_tr_b16 a[80:81], v2 offset:32768\n" \
"ds_read_b64_tr_b16 a[82:83], v2 offset:33280\n" \
"ds_read_b64_tr_b16 a[208:209], v3 offset:32768\n" \
"ds_read_b64_tr_b16 a[210:211], v3 offset:33280\n" \
"ds_read_b64_tr_b16 a[84:85], v2 offset:36864\n" \
"ds_read_b64_tr_b16 a[86:87], v2 offset:37376\n" \
"ds_read_b64_tr_b16 a[212:213], v3 offset:36864\n" \
"ds_read_b64_tr_b16 a[214:215], v3 offset:37376\n" \
"s_waitcnt vmcnt(16)\n" \
"s_barrier\n" \
"ds_read_b64_tr_b16 a[88:89], v2 offset:49152\n" \
"ds_read_b64_tr_b16 a[90:91], v2 offset:49664\n" \
"ds_read_b64_tr_b16 a[216:217], v3 offset:49152\n" \
"ds_read_b64_tr_b16 a[218:219], v3 offset:49664\n" \
"ds_read_b64_tr_b16 a[92:93], v2 offset:53248\n" \
"ds_read_b64_tr_b16 a[94:95], v2 offset:53760\n" \
"ds_read_b64_tr_b16 a[220:221], v3 offset:53248\n" \
"ds_read_b64_tr_b16 a[222:223], v3 offset:53760\n" \
"s_waitcnt vmcnt(12)\n" \
"s_barrier\n" \
"ds_read_b64_tr_b16 a[96:97], v8 offset:0\n" \
"ds_read_b64_tr_b16 a[98:99], v8 offset:512\n" \
"ds_read_b64_tr_b16 a[224:225], v9 offset:0\n" \
"ds_read_b64_tr_b16 a[226:227], v9 offset:512\n" \
"ds_read_b64_tr_b16 a[100:101], v8 offset:4096\n" \
"ds_read_b64_tr_b16 a[102:103], v8 offset:4608\n" \
"ds_read_b64_tr_b16 a[228:229], v9 offset:4096\n" \
"ds_read_b64_tr_b16 a[230:231], v9 offset:4608\n" \
"s_waitcnt vmcnt(8)\n" \
"s_barrier\n" \
"ds_read_b64_tr_b16 a[104:105], v8 offset:16384\n" \
"ds_read_b64_tr_b16 a[106:107], v8 offset:16896\n" \
"ds_read_b64_tr_b16 a[232:233], v9 offset:16384\n" \
"ds_read_b64_tr_b16 a[234:235], v9 offset:16896\n" \
"ds_read_b64_tr_b16 a[108:109], v8 offset:20480\n" \
"ds_read_b64_tr_b16 a[110:111], v8 offset:20992\n" \
"ds_read_b64_tr_b16 a[236:237], v9 offset:20480\n" \
"ds_read_b64_tr_b16 a[238:239], v9 offset:20992\n" \
"s_waitcnt vmcnt(4)\n" \
"s_barrier\n" \
"ds_read_b64_tr_b16 a[112:113], v8 offset:32768\n" \
"ds_read_b64_tr_b16 a[114:115], v8 offset:33280\n" \
"ds_read_b64_tr_b16 a[240:241], v9 offset:32768\n" \
"ds_read_b64_tr_b16 a[242:243], v9 offset:33280\n" \
"ds_read_b64_tr_b16 a[116:117], v8 offset:36864\n" \
"ds_read_b64_tr_b16 a[118:119], v8 offset:37376\n" \
"ds_read_b64_tr_b16 a[244:245], v9 offset:36864\n" \
"ds_read_b64_tr_b16 a[246:247], v9 offset:37376\n" \
"s_waitcnt vmcnt(0)\n" \
"s_barrier\n" \
"ds_read_b64_tr_b16 a[120:121], v8 offset:49152\n" \
"ds_read_b64_tr_b16 a[122:123], v8 offset:49664\n" \
"ds_read_b64_tr_b16 a[248:249], v9 offset:49152\n" \
"ds_read_b64_tr_b16 a[250:251], v9 offset:49664\n" \
"ds_read_b64_tr_b16 a[124:125], v8 offset:53248\n" \
"ds_read_b64_tr_b16 a[126:127], v8 offset:53760\n" \
"ds_read_b64_tr_b16 a[252:253], v9 offset:53248\n" \
"ds_read_b64_tr_b16 a[254:255], v9 offset:53760\n" \
"s_waitcnt lgkmcnt(0)\n" \
"s_barrier\n" \
"v_mov_b32 v14, 4096\n" \
"s_mov_b32 s59, s46\n" \
"s_add_u32 s63, s59, 0\n" \
"s_min_u32 s63, s63, 63\n" \
"s_lshr_b32 s63, s63, 3\n" \
"s_lshl_b32 s63, s63, 7\n" \
"s_branch .Lp1g1_l0_%=\n" \
".Lp1g1_l0k_%=:\n" \
"s_add_u32 s63, s59, 1\n" \
"s_min_u32 s63, s63, 63\n" \
"s_lshr_b32 s63, s63, 3\n" \
"s_lshl_b32 s63, s63, 7\n" \
"s_branch .Lp1g1_l1_%=\n" \
".Lp1g1_l1k_%=:\n" \
"s_lshl_b32 s55, s48, 13\n" \
"s_lshl_b32 s56, s46, 17\n" \
"s_add_u32 s56, s56, s45\n" \
"s_lshl_b32 s60, s47, 17\n" \
"s_sub_u32 s60, s60, 0x20000\n" \
"s_add_u32 s60, s60, s45\n" \
"s_mov_b32 s57, 0\n" \
"s_mov_b32 s58, 0\n" \
"v_mov_b32 v7, v5\n" \
"v_mov_b32 v5, 0x7ffffff0\n" \
"v_mov_b32 v10, v0\n" \
"v_mov_b32 v11, v6\n" \
"v_mov_b32 v6, 0x7ffffff0\n" \
"s_mov_b32 s59, s46\n" \
"s_min_u32 s61, s56, s60\n" \
"s_lshr_b32 s65, s61, 8\n" \
"s_and_b32 s65, s65, 0x2000\n" \
"s_xor_b32 s61, s61, s65\n" \
"s_add_u32 m0, s55, 0\n" \
"s_nop 0\n" \
"buffer_load_dwordx4 v4, s[36:39], s61 offen offset:0 sc1 lds\n" \
"buffer_load_dwordx4 v4, s[36:39], s61 offen offset:1024 sc1 lds\n" \
"buffer_load_dwordx4 v4, s[36:39], s61 offen offset:2048 sc1 lds\n" \
"buffer_load_dwordx4 v4, s[36:39], s61 offen offset:3072 sc1 lds\n" \
"s_add_u32 m0, s55, 4096\n" \
"s_add_u32 s61, s61, 4096\n" \
"buffer_load_dwordx4 v4, s[36:39], s61 offen offset:0 sc1 lds\n" \
"buffer_load_dwordx4 v4, s[36:39], s61 offen offset:1024 sc1 lds\n" \
"buffer_load_dwordx4 v4, s[36:39], s61 offen offset:2048 sc1 lds\n" \
"buffer_load_dwordx4 v4, s[36:39], s61 offen offset:3072 sc1 lds\n" \
"s_add_u32 s61, s61, 28672\n" \
"s_add_u32 s56, s56, 0x20000\n" \
"s_add_u32 m0, s55, 32768\n" \
"s_nop 0\n" \
"buffer_load_dwordx4 v4, s[36:39], s61 offen offset:0 sc1 lds\n" \
"buffer_load_dwordx4 v4, s[36:39], s61 offen offset:1024 sc1 lds\n" \
"buffer_load_dwordx4 v4, s[36:39], s61 offen offset:2048 sc1 lds\n" \
"buffer_load_dwordx4 v4, s[36:39], s61 offen offset:3072 sc1 lds\n" \
"s_add_u32 m0, s55, 36864\n" \
"s_add_u32 s61, s61, 4096\n" \
"buffer_load_dwordx4 v4, s[36:39], s61 offen offset:0 sc1 lds\n" \
"buffer_load_dwordx4 v4, s[36:39], s61 offen offset:1024 sc1 lds\n" \
"buffer_load_dwordx4 v4, s[36:39], s61 offen offset:2048 sc1 lds\n" \
"buffer_load_dwordx4 v4, s[36:39], s61 offen offset:3072 sc1 lds\n" \
"s_mov_b32 s70, 0\n" \
"s_mov_b32 s72, 0\n" \
"s_waitcnt vmcnt(0) lgkmcnt(0)\n" \
"s_barrier\n" \
"v_add_u32 v12, 0x10000, v10\n" \
"ds_read_b128 v[16:19], v10 offset:0\n" \
"ds_read_b128 v[20:23], v10 offset:16384\n" \
"ds_read_b128 v[24:27], v10 offset:1024\n" \
"ds_read_b128 v[28:31], v10 offset:17408\n" \
"ds_read_b128 v[32:35], v10 offset:2048\n" \
"ds_read_b128 v[36:39], v10 offset:18432\n" \
"s_waitcnt lgkmcnt(4)\n" \
"s_nop 7\n" \
"s_nop 7\n" \
".p2align 6\n" \
".Lp1g1_loop_%=:\n" \
"v_mfma_f32_16x16x32_bf16 v[48:51], a[0:3], v[16:19], 0\n" \
"s_min_u32 s61, s56, s60\n" \
"s_lshr_b32 s65, s61, 8\n" \
"s_and_b32 s65, s65, 0x2000\n" \
"s_xor_b32 s61, s61, s65\n" \
"s_add_u32 m0, s55, 65536\n" \
"s_nop 0\n" \
"buffer_load_dwordx4 v4, s[36:39], s61 offen offset:0 sc1 lds\n" \
"v_add_u32_e64 v13, -1, s59\n" \
"v_mfma_f32_16x16x32_bf16 v[52:55], a[0:3], v[20:23], 0\n" \
"ds_read_b128 v[40:43], v10 offset:3072\n" \
"s_nop 0\n" \
"v_mfma_f32_16x16x32_bf16 v[56:59], a[128:131], v[16:19], 0\n" \
"ds_read_b128 v[44:47], v10 offset:19456\n" \
"s_nop 0\n" \
"v_mfma_f32_16x16x32_bf16 v[60:63], a[128:131], v[20:23], 0\n" \
"s_waitcnt lgkmcnt(4)\n" \
"v_mul_f32 v64, 0xbfb8aa3b, v64\n" \
"v_mul_f32 v65, 0xbfb8aa3b, v65\n" \
"v_mfma_f32_16x16x32_bf16 v[48:51], a[4:7], v[24:27], v[48:51]\n" \
"buffer_load_dwordx4 v4, s[36:39], s61 offen offset:1024 sc1 lds\n" \
"v_mfma_f32_16x16x32_bf16 v[52:55], a[4:7], v[28:31], v[52:55]\n" \
"ds_read_b128 v[16:19], v10 offset:4096\n" \
"v_mul_f32 v66, 0xbfb8aa3b, v66\n" \
"v_mfma_f32_16x16x32_bf16 v[56:59], a[132:135], v[24:27], v[56:59]\n" \
"ds_read_b128 v[20:23], v10 offset:20480\n" \
"v_mul_f32 v67, 0xbfb8aa3b, v67\n" \
"v_mfma_f32_16x16x32_bf16 v[60:63], a[132:135], v[28:31], v[60:63]\n" \
"s_waitcnt lgkmcnt(4)\n" \
"v_exp_f32_e64 v64, v64\n" \
"v_mfma_f32_16x16x32_bf16 v[48:51], a[8:11], v[32:35], v[48:51]\n" \
"s_add_u32 s63, s59, 2\n" \
"s_min_u32 s63, s63, 63\n" \
"v_mfma_f32_16x16x32_bf16 v[52:55], a[8:11], v[36:39], v[52:55]\n" \
"ds_read_b128 v[24:27], v10 offset:5120\n" \
"s_lshr_b32 s63, s63, 3\n" \
"s_lshl_b32 s63, s63, 7\n" \
"v_mfma_f32_16x16x32_bf16 v[56:59], a[136:139], v[32:35], v[56:59]\n" \
"ds_read_b128 v[28:31], v10 offset:21504\n" \
"v_mfma_f32_16x16x32_bf16 v[60:63], a[136:139], v[36:39], v[60:63]\n" \
"s_waitcnt lgkmcnt(4)\n" \
"v_exp_f32 v65, v65\n" \
"v_mfma_f32_16x16x32_bf16 v[48:51], a[12:15], v[40:43], v[48:51]\n" \
"buffer_load_dwordx4 v4, s[36:39], s61 offen offset:2048 sc1 lds\n" \
"v_mfma_f32_16x16x32_bf16 v[52:55], a[12:15], v[44:47], v[52:55]\n" \
"ds_read_b128 v[32:35], v10 offset:6144\n" \
"v_mfma_f32_16x16x32_bf16 v[56:59], a[140:143], v[40:43], v[56:59]\n" \
"ds_read_b128 v[36:39], v10 offset:22528\n" \
"v_mfma_f32_16x16x32_bf16 v[60:63], a[140:143], v[44:47], v[60:63]\n" \
"s_waitcnt lgkmcnt(4)\n" \
"v_exp_f32 v66, v66\n" \
"v_mfma_f32_16x16x32_bf16 v[48:51], a[16:19], v[16:19], v[48:51]\n" \
"buffer_load_dwordx4 v4, s[36:39], s61 offen offset:3072 sc1 lds\n" \
"v_mfma_f32_16x16x32_bf16 v[52:55], a[16:19], v[20:23], v[52:55]\n" \
"ds_read_b128 v[40:43], v10 offset:7168\n" \
"v_mfma_f32_16x16x32_bf16 v[56:59], a[144:147], v[16:19], v[56:59]\n" \
"ds_read_b128 v[44:47], v10 offset:23552\n" \
"v_mfma_f32_16x16x32_bf16 v[60:63], a[144:147], v[20:23], v[60:63]\n" \
"s_waitcnt lgkmcnt(4)\n" \
"v_exp_f32 v67, v67\n" \
"v_mfma_f32_16x16x32_bf16 v[48:51], a[20:23], v[24:27], v[48:51]\n" \
"buffer_load_dword v15, v14, s[36:39], s63 offen sc1\n" \
"v_mfma_f32_16x16x32_bf16 v[52:55], a[20:23], v[28:31], v[52:55]\n" \
"ds_read_b128 v[16:19], v10 offset:8192\n" \
"v_add_f32_e64 v64, 1.0, v64\n" \
"v_mfma_f32_16x16x32_bf16 v[56:59], a[148:151], v[24:27], v[56:59]\n" \
"ds_read_b128 v[20:23], v10 offset:24576\n" \
"v_add_f32_e64 v65, 1.0, v65\n" \
"v_mfma_f32_16x16x32_bf16 v[60:63], a[148:151], v[28:31], v[60:63]\n" \
"s_waitcnt lgkmcnt(4)\n" \
"v_add_f32 v66, 1.0, v66\n" \
"v_add_f32_e64 v67, 1.0, v67\n" \
"v_mfma_f32_16x16x32_bf16 v[48:51], a[24:27], v[32:35], v[48:51]\n" \
"s_add_u32 m0, s55, 69632\n" \
"s_add_u32 s61, s61, 4096\n" \
"buffer_load_dwordx4 v4, s[36:39], s61 offen offset:0 sc1 lds\n" \
"v_mfma_f32_16x16x32_bf16 v[52:55], a[24:27], v[36:39], v[52:55]\n" \
"ds_read_b128 v[24:27], v10 offset:9216\n" \
"v_mfma_f32_16x16x32_bf16 v[56:59], a[152:155], v[32:35], v[56:59]\n" \
"ds_read_b128 v[28:31], v10 offset:25600\n" \
"v_mfma_f32_16x16x32_bf16 v[60:63], a[152:155], v[36:39], v[60:63]\n" \
"s_waitcnt lgkmcnt(4)\n" \
"v_rcp_f32 v64, v64\n" \
"v_mfma_f32_16x16x32_bf16 v[48:51], a[28:31], v[40:43], v[48:51]\n" \
"buffer_load_dwordx4 v4, s[36:39], s61 offen offset:1024 sc1 lds\n" \
"v_mfma_f32_16x16x32_bf16 v[52:55], a[28:31], v[44:47], v[52:55]\n" \
"ds_read_b128 v[32:35], v10 offset:10240\n" \
"v_mfma_f32_16x16x32_bf16 v[56:59], a[156:159], v[40:43], v[56:59]\n" \
"ds_read_b128 v[36:39], v10 offset:26624\n" \
"v_mfma_f32_16x16x32_bf16 v[60:63], a[156:159], v[44:47], v[60:63]\n" \
"s_waitcnt lgkmcnt(4)\n" \
"v_rcp_f32 v65, v65\n" \
"v_mfma_f32_16x16x32_bf16 v[48:51], a[32:35], v[16:19], v[48:51]\n" \
"v_rcp_f32_e64 v66, v66\n" \
"v_mfma_f32_16x16x32_bf16 v[52:55], a[32:35], v[20:23], v[52:55]\n" \
"ds_read_b128 v[40:43], v10 offset:11264\n" \
"v_mfma_f32_16x16x32_bf16 v[56:59], a[160:163], v[16:19], v[56:59]\n" \
"ds_read_b128 v[44:47], v10 offset:27648\n" \
"v_mfma_f32_16x16x32_bf16 v[60:63], a[160:163], v[20:23], v[60:63]\n" \
"s_waitcnt lgkmcnt(4)\n" \
"v_rcp_f32 v67, v67\n" \
"v_mfma_f32_16x16x32_bf16 v[48:51], a[36:39], v[24:27], v[48:51]\n" \
"buffer_load_dwordx4 v4, s[36:39], s61 offen offset:2048 sc1 lds\n" \
"v_mfma_f32_16x16x32_bf16 v[52:55], a[36:39], v[28:31], v[52:55]\n" \
"ds_read_b128 v[16:19], v10 offset:12288\n" \
"v_mul_f32_e64 v64, v64, v72\n" \
"v_mfma_f32_16x16x32_bf16 v[56:59], a[164:167], v[24:27], v[56:59]\n" \
"ds_read_b128 v[20:23], v10 offset:28672\n" \
"v_mul_f32_e64 v65, v65, v73\n" \
"v_mfma_f32_16x16x32_bf16 v[60:63], a[164:167], v[28:31], v[60:63]\n" \
"s_waitcnt lgkmcnt(4)\n" \
"v_mul_f32 v66, v66, v74\n" \
"v_mul_f32_e64 v67, v67, v75\n" \
"v_mfma_f32_16x16x32_bf16 v[48:51], a[40:43], v[32:35], v[48:51]\n" \
"buffer_load_dwordx4 v4, s[36:39], s61 offen offset:3072 sc1 lds\n" \
"v_mfma_f32_16x16x32_bf16 v[52:55], a[40:43], v[36:39], v[52:55]\n" \
"ds_read_b128 v[24:27], v10 offset:13312\n" \
"v_cvt_pk_bf16_f32 v80, v64, v65\n" \
"v_mfma_f32_16x16x32_bf16 v[56:59], a[168:171], v[32:35], v[56:59]\n" \
"ds_read_b128 v[28:31], v10 offset:29696\n" \
"v_cvt_pk_bf16_f32 v81, v66, v67\n" \
"v_mfma_f32_16x16x32_bf16 v[60:63], a[168:171], v[36:39], v[60:63]\n" \
"s_waitcnt lgkmcnt(4)\n" \
"v_mul_f32 v68, 0xbfb8aa3b, v68\n" \
"v_mul_f32 v69, 0xbfb8aa3b, v69\n" \
"v_mfma_f32_16x16x32_bf16 v[48:51], a[44:47], v[40:43], v[48:51]\n" \
"v_mul_f32 v70, 0xbfb8aa3b, v70\n" \
"v_mul_f32 v71, 0xbfb8aa3b, v71\n" \
"v_mfma_f32_16x16x32_bf16 v[52:55], a[44:47], v[44:47], v[52:55]\n" \
"ds_read_b128 v[32:35], v10 offset:14336\n" \
"v_mfma_f32_16x16x32_bf16 v[56:59], a[172:175], v[40:43], v[56:59]\n" \
"ds_read_b128 v[36:39], v10 offset:30720\n" \
"v_mfma_f32_16x16x32_bf16 v[60:63], a[172:175], v[44:47], v[60:63]\n" \
"s_waitcnt lgkmcnt(4)\n" \
"v_exp_f32_e64 v68, v68\n" \
"v_mfma_f32_16x16x32_bf16 v[48:51], a[48:51], v[16:19], v[48:51]\n" \
"v_exp_f32_e64 v69, v69\n" \
"v_mfma_f32_16x16x32_bf16 v[52:55], a[48:51], v[20:23], v[52:55]\n" \
"ds_read_b128 v[40:43], v10 offset:15360\n" \
"v_mfma_f32_16x16x32_bf16 v[56:59], a[176:179], v[16:19], v[56:59]\n" \
"ds_read_b128 v[44:47], v10 offset:31744\n" \
"v_mfma_f32_16x16x32_bf16 v[60:63], a[176:179], v[20:23], v[60:63]\n" \
"s_waitcnt lgkmcnt(4)\n" \
"v_exp_f32 v70, v70\n" \
"v_mfma_f32_16x16x32_bf16 v[48:51], a[52:55], v[24:27], v[48:51]\n" \
"s_waitcnt vmcnt(9)\n" \
"s_barrier\n" \
"v_mfma_f32_16x16x32_bf16 v[52:55], a[52:55], v[28:31], v[52:55]\n" \
"ds_read_b128 v[16:19], v10 offset:32768\n" \
"v_mfma_f32_16x16x32_bf16 v[56:59], a[180:183], v[24:27], v[56:59]\n" \
"ds_read_b128 v[20:23], v10 offset:49152\n" \
"v_mfma_f32_16x16x32_bf16 v[60:63], a[180:183], v[28:31], v[60:63]\n" \
"s_waitcnt lgkmcnt(4)\n" \
"buffer_store_dword v13, v6, s[36:39], 0 offen sc1\n" \
"v_exp_f32 v71, v71\n" \
"v_mfma_f32_16x16x32_bf16 v[48:51], a[56:59], v[32:35], v[48:51]\n" \
"v_add_f32 v68, 1.0, v68\n" \
"v_add_f32 v69, 1.0, v69\n" \
"v_mfma_f32_16x16x32_bf16 v[52:55], a[56:59], v[36:39], v[52:55]\n" \
"ds_read_b128 v[24:27], v10 offset:33792\n" \
"v_add_f32_e64 v70, 1.0, v70\n" \
"v_mfma_f32_16x16x32_bf16 v[56:59], a[184:187], v[32:35], v[56:59]\n" \
"ds_read_b128 v[28:31], v10 offset:50176\n" \
"v_add_f32_e64 v71, 1.0, v71\n" \
"v_mfma_f32_16x16x32_bf16 v[60:63], a[184:187], v[36:39], v[60:63]\n" \
"s_waitcnt lgkmcnt(4)\n" \
"v_rcp_f32 v68, v68\n" \
"v_mfma_f32_16x16x32_bf16 v[48:51], a[60:63], v[40:43], v[48:51]\n" \
"v_rcp_f32_e64 v69, v69\n" \
"v_mfma_f32_16x16x32_bf16 v[52:55], a[60:63], v[44:47], v[52:55]\n" \
"ds_read_b128 v[32:35], v10 offset:34816\n" \
"v_mfma_f32_16x16x32_bf16 v[56:59], a[188:191], v[40:43], v[56:59]\n" \
"ds_read_b128 v[36:39], v10 offset:51200\n" \
"v_mfma_f32_16x16x32_bf16 v[60:63], a[188:191], v[44:47], v[60:63]\n" \
"s_waitcnt lgkmcnt(4)\n" \
"v_rcp_f32 v70, v70\n" \
"v_mfma_f32_16x16x32_bf16 v[48:51], a[64:67], v[16:19], v[48:51]\n" \
"s_add_u32 s61, s61, 28672\n" \
"s_add_u32 s56, s56, 0x20000\n" \
"s_add_u32 m0, s55, 98304\n" \
"s_nop 0\n" \
"buffer_load_dwordx4 v4, s[36:39], s61 offen offset:0 sc1 lds\n" \
"v_mfma_f32_16x16x32_bf16 v[52:55], a[64:67], v[20:23], v[52:55]\n" \
"ds_read_b128 v[40:43], v10 offset:35840\n" \
"v_mfma_f32_16x16x32_bf16 v[56:59], a[192:195], v[16:19], v[56:59]\n" \
"ds_read_b128 v[44:47], v10 offset:52224\n" \
"v_mfma_f32_16x16x32_bf16 v[60:63], a[192:195], v[20:23], v[60:63]\n" \
"s_waitcnt lgkmcnt(4)\n" \
"v_rcp_f32_e64 v71, v71\n" \
"v_mfma_f32_16x16x32_bf16 v[48:51], a[68:71], v[24:27], v[48:51]\n" \
"buffer_load_dwordx4 v4, s[36:39], s61 offen offset:1024 sc1 lds\n" \
"v_mfma_f32_16x16x32_bf16 v[52:55], a[68:71], v[28:31], v[52:55]\n" \
"ds_read_b128 v[16:19], v10 offset:36864\n" \
"v_mul_f32_e64 v68, v68, v76\n" \
"v_mfma_f32_16x16x32_bf16 v[56:59], a[196:199], v[24:27], v[56:59]\n" \
"ds_read_b128 v[20:23], v10 offset:53248\n" \
"v_mul_f32_e64 v69, v69, v77\n" \
"v_mfma_f32_16x16x32_bf16 v[60:63], a[196:199], v[28:31], v[60:63]\n" \
"s_waitcnt lgkmcnt(4)\n" \
"v_mul_f32 v70, v70, v78\n" \
"v_mul_f32_e64 v71, v71, v79\n" \
"v_mfma_f32_16x16x32_bf16 v[48:51], a[72:75], v[32:35], v[48:51]\n" \
"v_cvt_pk_bf16_f32 v82, v68, v69\n" \
"v_cvt_pk_bf16_f32 v83, v70, v71\n" \
"v_mfma_f32_16x16x32_bf16 v[52:55], a[72:75], v[36:39], v[52:55]\n" \
"ds_read_b128 v[24:27], v10 offset:37888\n" \
"s_nop 0\n" \
"v_mfma_f32_16x16x32_bf16 v[56:59], a[200:203], v[32:35], v[56:59]\n" \
"ds_read_b128 v[28:31], v10 offset:54272\n" \
"s_nop 0\n" \
"v_mfma_f32_16x16x32_bf16 v[60:63], a[200:203], v[36:39], v[60:63]\n" \
"s_waitcnt lgkmcnt(4)\n" \
"v_permlane16_swap_b32 v80, v82\n" \
"v_permlane16_swap_b32 v81, v83\n" \
"v_mfma_f32_16x16x32_bf16 v[48:51], a[76:79], v[40:43], v[48:51]\n" \
"buffer_load_dwordx4 v4, s[36:39], s61 offen offset:2048 sc1 lds\n" \
"v_mfma_f32_16x16x32_bf16 v[52:55], a[76:79], v[44:47], v[52:55]\n" \
"ds_read_b128 v[32:35], v10 offset:38912\n" \
"buffer_store_dwordx4 v[80:83], v5, s[36:39], s57 offen sc1\n" \
"v_mfma_f32_16x16x32_bf16 v[56:59], a[204:207], v[40:43], v[56:59]\n" \
"ds_read_b128 v[36:39], v10 offset:55296\n" \
"v_mfma_f32_16x16x32_bf16 v[60:63], a[204:207], v[44:47], v[60:63]\n" \
"s_waitcnt lgkmcnt(4)\n" \
"v_mfma_f32_16x16x32_bf16 v[48:51], a[80:83], v[16:19], v[48:51]\n" \
"buffer_load_dwordx4 v4, s[36:39], s61 offen offset:3072 sc1 lds\n" \
"v_mfma_f32_16x16x32_bf16 v[52:55], a[80:83], v[20:23], v[52:55]\n" \
"ds_read_b128 v[40:43], v10 offset:39936\n" \
"v_mfma_f32_16x16x32_bf16 v[56:59], a[208:211], v[16:19], v[56:59]\n" \
"ds_read_b128 v[44:47], v10 offset:56320\n" \
"v_mfma_f32_16x16x32_bf16 v[60:63], a[208:211], v[20:23], v[60:63]\n" \
"s_waitcnt lgkmcnt(4)\n" \
"v_mfma_f32_16x16x32_bf16 v[48:51], a[84:87], v[24:27], v[48:51]\n" \
"v_mfma_f32_16x16x32_bf16 v[52:55], a[84:87], v[28:31], v[52:55]\n" \
"ds_read_b128 v[16:19], v10 offset:40960\n" \
"v_mfma_f32_16x16x32_bf16 v[56:59], a[212:215], v[24:27], v[56:59]\n" \
"ds_read_b128 v[20:23], v10 offset:57344\n" \
"v_mfma_f32_16x16x32_bf16 v[60:63], a[212:215], v[28:31], v[60:63]\n" \
"s_waitcnt lgkmcnt(4)\n" \
"v_mfma_f32_16x16x32_bf16 v[48:51], a[88:91], v[32:35], v[48:51]\n" \
"s_add_u32 m0, s55, 102400\n" \
"s_add_u32 s61, s61, 4096\n" \
"buffer_load_dwordx4 v4, s[36:39], s61 offen offset:0 sc1 lds\n" \
"v_mfma_f32_16x16x32_bf16 v[52:55], a[88:91], v[36:39], v[52:55]\n" \
"ds_read_b128 v[24:27], v10 offset:41984\n" \
"v_mfma_f32_16x16x32_bf16 v[56:59], a[216:219], v[32:35], v[56:59]\n" \
"ds_read_b128 v[28:31], v10 offset:58368\n" \
"v_mfma_f32_16x16x32_bf16 v[60:63], a[216:219], v[36:39], v[60:63]\n" \
"s_waitcnt lgkmcnt(4)\n" \
"v_mfma_f32_16x16x32_bf16 v[48:51], a[92:95], v[40:43], v[48:51]\n" \
"buffer_load_dwordx4 v4, s[36:39], s61 offen offset:1024 sc1 lds\n" \
"v_mfma_f32_16x16x32_bf16 v[52:55], a[92:95], v[44:47], v[52:55]\n" \
"ds_read_b128 v[32:35], v10 offset:43008\n" \
"v_mfma_f32_16x16x32_bf16 v[56:59], a[220:223], v[40:43], v[56:59]\n" \
"ds_read_b128 v[36:39], v10 offset:59392\n" \
"v_mfma_f32_16x16x32_bf16 v[60:63], a[220:223], v[44:47], v[60:63]\n" \
"s_waitcnt lgkmcnt(4)\n" \
"v_mfma_f32_16x16x32_bf16 v[48:51], a[96:99], v[16:19], v[48:51]\n" \
"v_mfma_f32_16x16x32_bf16 v[52:55], a[96:99], v[20:23], v[52:55]\n" \
"ds_read_b128 v[40:43], v10 offset:44032\n" \
"v_mfma_f32_16x16x32_bf16 v[56:59], a[224:227], v[16:19], v[56:59]\n" \
"ds_read_b128 v[44:47], v10 offset:60416\n" \
"v_mfma_f32_16x16x32_bf16 v[60:63], a[224:227], v[20:23], v[60:63]\n" \
"s_waitcnt lgkmcnt(4)\n" \
"v_mfma_f32_16x16x32_bf16 v[48:51], a[100:103], v[24:27], v[48:51]\n" \
"buffer_load_dwordx4 v4, s[36:39], s61 offen offset:2048 sc1 lds\n" \
"v_mfma_f32_16x16x32_bf16 v[52:55], a[100:103], v[28:31], v[52:55]\n" \
"ds_read_b128 v[16:19], v10 offset:45056\n" \
"v_mfma_f32_16x16x32_bf16 v[56:59], a[228:231], v[24:27], v[56:59]\n" \
"ds_read_b128 v[20:23], v10 offset:61440\n" \
"v_mfma_f32_16x16x32_bf16 v[60:63], a[228:231], v[28:31], v[60:63]\n" \
"s_waitcnt lgkmcnt(4)\n" \
"v_mfma_f32_16x16x32_bf16 v[48:51], a[104:107], v[32:35], v[48:51]\n" \
"buffer_load_dwordx4 v4, s[36:39], s61 offen offset:3072 sc1 lds\n" \
"v_mfma_f32_16x16x32_bf16 v[52:55], a[104:107], v[36:39], v[52:55]\n" \
"ds_read_b128 v[24:27], v10 offset:46080\n" \
"v_mfma_f32_16x16x32_bf16 v[56:59], a[232:235], v[32:35], v[56:59]\n" \
"ds_read_b128 v[28:31], v10 offset:62464\n" \
"v_mfma_f32_16x16x32_bf16 v[60:63], a[232:235], v[36:39], v[60:63]\n" \
"s_waitcnt lgkmcnt(4)\n" \
"v_mfma_f32_16x16x32_bf16 v[48:51], a[108:111], v[40:43], v[48:51]\n" \
"s_lshl_b32 s66, s59, s49\n" \
"s_lshr_b32 s65, s66, 8\n" \
"v_mfma_f32_16x16x32_bf16 v[52:55], a[108:111], v[44:47], v[52:55]\n" \
"ds_read_b128 v[32:35], v10 offset:47104\n" \
"v_mfma_f32_16x16x32_bf16 v[56:59], a[236:239], v[40:43], v[56:59]\n" \
"ds_read_b128 v[36:39], v10 offset:63488\n" \
"v_mfma_f32_16x16x32_bf16 v[60:63], a[236:239], v[44:47], v[60:63]\n" \
"s_waitcnt lgkmcnt(4)\n" \
"s_and_b32 s65, s65, 0x2000\n" \
"s_xor_b32 s66, s66, s65\n" \
"v_mfma_f32_16x16x32_bf16 v[48:51], a[112:115], v[16:19], v[48:51]\n" \
"v_mfma_f32_16x16x32_bf16 v[52:55], a[112:115], v[20:23], v[52:55]\n" \
"ds_read_b128 v[40:43], v10 offset:48128\n" \
"v_mfma_f32_16x16x32_bf16 v[56:59], a[240:243], v[16:19], v[56:59]\n" \
"ds_read_b128 v[44:47], v10 offset:64512\n" \
"v_mfma_f32_16x16x32_bf16 v[60:63], a[240:243], v[20:23], v[60:63]\n" \
"s_waitcnt lgkmcnt(4)\n" \
"v_mfma_f32_16x16x32_bf16 v[48:51], a[116:119], v[24:27], v[48:51]\n" \
"s_waitcnt vmcnt(10)\n" \
"s_barrier\n" \
"v_mfma_f32_16x16x32_bf16 v[52:55], a[116:119], v[28:31], v[52:55]\n" \
"ds_read_b128 v[16:19], v12 offset:0\n" \
"v_mfma_f32_16x16x32_bf16 v[56:59], a[244:247], v[24:27], v[56:59]\n" \
"ds_read_b128 v[20:23], v12 offset:16384\n" \
"v_mfma_f32_16x16x32_bf16 v[60:63], a[244:247], v[28:31], v[60:63]\n" \
"s_waitcnt lgkmcnt(4)\n" \
"v_mfma_f32_16x16x32_bf16 v[48:51], a[120:123], v[32:35], v[48:51]\n" \
"v_cmp_gt_u32 vcc, 16, v15\n" \
"v_mfma_f32_16x16x32_bf16 v[52:55], a[120:123], v[36:39], v[52:55]\n" \
"ds_read_b128 v[24:27], v12 offset:1024\n" \
"v_mfma_f32_16x16x32_bf16 v[56:59], a[248:251], v[32:35], v[56:59]\n" \
"ds_read_b128 v[28:31], v12 offset:17408\n" \
"v_mfma_f32_16x16x32_bf16 v[60:63], a[248:251], v[36:39], v[60:63]\n" \
"s_waitcnt lgkmcnt(4)\n" \
"v_mfma_f32_16x16x32_bf16 v[48:51], a[124:127], v[40:43], v[48:51]\n" \
"s_cbranch_vccnz .Lp1g1_m0_%=\n" \
".Lp1g1_m0k_%=:\n" \
"v_mfma_f32_16x16x32_bf16 v[52:55], a[124:127], v[44:47], v[52:55]\n" \
"ds_read_b128 v[32:35], v12 offset:2048\n" \
"v_mfma_f32_16x16x32_bf16 v[56:59], a[252:255], v[40:43], v[56:59]\n" \
"ds_read_b128 v[36:39], v12 offset:18432\n" \
"v_mfma_f32_16x16x32_bf16 v[60:63], a[252:255], v[44:47], v[60:63]\n" \
"s_waitcnt lgkmcnt(4)\n" \
"s_xor_b32 s57, s66, s64\n" \
"s_add_u32 s58, s57, s50\n" \
"s_add_u32 s59, s59, 1\n" \
"v_mov_b32 v5, v7\n" \
"v_mov_b32 v6, v11\n" \
"s_cmp_ge_u32 s59, s47\n" \
"s_cbranch_scc1 .Lp1g1_out0_%=\n" \
"v_mfma_f32_16x16x32_bf16 v[64:67], a[0:3], v[16:19], 0\n" \
"s_min_u32 s61, s56, s60\n" \
"s_lshr_b32 s65, s61, 8\n" \
"s_and_b32 s65, s65, 0x2000\n" \
"s_xor_b32 s61, s61, s65\n" \
"s_add_u32 m0, s55, 0\n" \
"s_nop 0\n" \
"buffer_load_dwordx4 v4, s[36:39], s61 offen offset:0 sc1 lds\n" \
"v_add_u32_e64 v13, -1, s59\n" \
"v_mfma_f32_16x16x32_bf16 v[68:71], a[0:3], v[20:23], 0\n" \
"ds_read_b128 v[40:43], v12 offset:3072\n" \
"s_nop 0\n" \
"v_mfma_f32_16x16x32_bf16 v[72:75], a[128:131], v[16:19], 0\n" \
"ds_read_b128 v[44:47], v12 offset:19456\n" \
"s_nop 0\n" \
"v_mfma_f32_16x16x32_bf16 v[76:79], a[128:131], v[20:23], 0\n" \
"s_waitcnt lgkmcnt(4)\n" \
"v_mul_f32 v48, 0xbfb8aa3b, v48\n" \
"v_mul_f32 v49, 0xbfb8aa3b, v49\n" \
"v_mfma_f32_16x16x32_bf16 v[64:67], a[4:7], v[24:27], v[64:67]\n" \
"buffer_load_dwordx4 v4, s[36:39], s61 offen offset:1024 sc1 lds\n" \
"v_mfma_f32_16x16x32_bf16 v[68:71], a[4:7], v[28:31], v[68:71]\n" \
"ds_read_b128 v[16:19], v12 offset:4096\n" \
"v_mul_f32 v50, 0xbfb8aa3b, v50\n" \
"v_mfma_f32_16x16x32_bf16 v[72:75], a[132:135], v[24:27], v[72:75]\n" \
"ds_read_b128 v[20:23], v12 offset:20480\n" \
"v_mul_f32 v51, 0xbfb8aa3b, v51\n" \
"v_mfma_f32_16x16x32_bf16 v[76:79], a[132:135], v[28:31], v[76:79]\n" \
"s_waitcnt lgkmcnt(4)\n" \
"v_exp_f32 v48, v48\n" \
"v_mfma_f32_16x16x32_bf16 v[64:67], a[8:11], v[32:35], v[64:67]\n" \
"s_add_u32 s63, s59, 2\n" \
"s_min_u32 s63, s63, 63\n" \
"v_mfma_f32_16x16x32_bf16 v[68:71], a[8:11], v[36:39], v[68:71]\n" \
"ds_read_b128 v[24:27], v12 offset:5120\n" \
"s_lshr_b32 s63, s63, 3\n" \
"s_lshl_b32 s63, s63, 7\n" \
"v_mfma_f32_16x16x32_bf16 v[72:75], a[136:139], v[32:35], v[72:75]\n" \
"ds_read_b128 v[28:31], v12 offset:21504\n" \
"v_mfma_f32_16x16x32_bf16 v[76:79], a[136:139], v[36:39], v[76:79]\n" \
"s_waitcnt lgkmcnt(4)\n" \
"v_exp_f32 v49, v49\n" \
"v_mfma_f32_16x16x32_bf16 v[64:67], a[12:15], v[40:43], v[64:67]\n" \
"buffer_load_dwordx4 v4, s[36:39], s61 offen offset:2048 sc1 lds\n" \
"v_mfma_f32_16x16x32_bf16 v[68:71], a[12:15], v[44:47], v[68:71]\n" \
"ds_read_b128 v[32:35], v12 offset:6144\n" \
"v_mfma_f32_16x16x32_bf16 v[72:75], a[140:143], v[40:43], v[72:75]\n" \
"ds_read_b128 v[36:39], v12 offset:22528\n" \
"v_mfma_f32_16x16x32_bf16 v[76:79], a[140:143], v[44:47], v[76:79]\n" \
"s_waitcnt lgkmcnt(4)\n" \
"v_exp_f32 v50, v50\n" \
"v_mfma_f32_16x16x32_bf16 v[64:67], a[16:19], v[16:19], v[64:67]\n" \
"buffer_load_dwordx4 v4, s[36:39], s61 offen offset:3072 sc1 lds\n" \
"v_mfma_f32_16x16x32_bf16 v[68:71], a[16:19], v[20:23], v[68:71]\n" \
"ds_read_b128 v[40:43], v12 offset:7168\n" \
"v_mfma_f32_16x16x32_bf16 v[72:75], a[144:147], v[16:19], v[72:75]\n" \
"ds_read_b128 v[44:47], v12 offset:23552\n" \
"v_mfma_f32_16x16x32_bf16 v[76:79], a[144:147], v[20:23], v[76:79]\n" \
"s_waitcnt lgkmcnt(4)\n" \
"v_exp_f32 v51, v51\n" \
"v_mfma_f32_16x16x32_bf16 v[64:67], a[20:23], v[24:27], v[64:67]\n" \
"buffer_load_dword v15, v14, s[36:39], s63 offen sc1\n" \
"v_mfma_f32_16x16x32_bf16 v[68:71], a[20:23], v[28:31], v[68:71]\n" \
"ds_read_b128 v[16:19], v12 offset:8192\n" \
"v_add_f32_e64 v48, 1.0, v48\n" \
"v_mfma_f32_16x16x32_bf16 v[72:75], a[148:151], v[24:27], v[72:75]\n" \
"ds_read_b128 v[20:23], v12 offset:24576\n" \
"v_add_f32_e64 v49, 1.0, v49\n" \
"v_mfma_f32_16x16x32_bf16 v[76:79], a[148:151], v[28:31], v[76:79]\n" \
"s_waitcnt lgkmcnt(4)\n" \
"v_add_f32 v50, 1.0, v50\n" \
"v_add_f32_e64 v51, 1.0, v51\n" \
"v_mfma_f32_16x16x32_bf16 v[64:67], a[24:27], v[32:35], v[64:67]\n" \
"s_add_u32 m0, s55, 4096\n" \
"s_add_u32 s61, s61, 4096\n" \
"buffer_load_dwordx4 v4, s[36:39], s61 offen offset:0 sc1 lds\n" \
"v_mfma_f32_16x16x32_bf16 v[68:71], a[24:27], v[36:39], v[68:71]\n" \
"ds_read_b128 v[24:27], v12 offset:9216\n" \
"v_mfma_f32_16x16x32_bf16 v[72:75], a[152:155], v[32:35], v[72:75]\n" \
"ds_read_b128 v[28:31], v12 offset:25600\n" \
"v_mfma_f32_16x16x32_bf16 v[76:79], a[152:155], v[36:39], v[76:79]\n" \
"s_waitcnt lgkmcnt(4)\n" \
"v_rcp_f32 v48, v48\n" \
"v_mfma_f32_16x16x32_bf16 v[64:67], a[28:31], v[40:43], v[64:67]\n" \
"buffer_load_dwordx4 v4, s[36:39], s61 offen offset:1024 sc1 lds\n" \
"v_mfma_f32_16x16x32_bf16 v[68:71], a[28:31], v[44:47], v[68:71]\n" \
"ds_read_b128 v[32:35], v12 offset:10240\n" \
"v_mfma_f32_16x16x32_bf16 v[72:75], a[156:159], v[40:43], v[72:75]\n" \
"ds_read_b128 v[36:39], v12 offset:26624\n" \
"v_mfma_f32_16x16x32_bf16 v[76:79], a[156:159], v[44:47], v[76:79]\n" \
"s_waitcnt lgkmcnt(4)\n" \
"v_rcp_f32 v49, v49\n" \
"v_mfma_f32_16x16x32_bf16 v[64:67], a[32:35], v[16:19], v[64:67]\n" \
"v_rcp_f32_e64 v50, v50\n" \
"v_mfma_f32_16x16x32_bf16 v[68:71], a[32:35], v[20:23], v[68:71]\n" \
"ds_read_b128 v[40:43], v12 offset:11264\n" \
"v_mfma_f32_16x16x32_bf16 v[72:75], a[160:163], v[16:19], v[72:75]\n" \
"ds_read_b128 v[44:47], v12 offset:27648\n" \
"v_mfma_f32_16x16x32_bf16 v[76:79], a[160:163], v[20:23], v[76:79]\n" \
"s_waitcnt lgkmcnt(4)\n" \
"v_rcp_f32 v51, v51\n" \
"v_mfma_f32_16x16x32_bf16 v[64:67], a[36:39], v[24:27], v[64:67]\n" \
"buffer_load_dwordx4 v4, s[36:39], s61 offen offset:2048 sc1 lds\n" \
"v_mfma_f32_16x16x32_bf16 v[68:71], a[36:39], v[28:31], v[68:71]\n" \
"ds_read_b128 v[16:19], v12 offset:12288\n" \
"v_mul_f32_e64 v48, v48, v56\n" \
"v_mfma_f32_16x16x32_bf16 v[72:75], a[164:167], v[24:27], v[72:75]\n" \
"ds_read_b128 v[20:23], v12 offset:28672\n" \
"v_mul_f32_e64 v49, v49, v57\n" \
"v_mfma_f32_16x16x32_bf16 v[76:79], a[164:167], v[28:31], v[76:79]\n" \
"s_waitcnt lgkmcnt(4)\n" \
"v_mul_f32 v50, v50, v58\n" \
"v_mul_f32_e64 v51, v51, v59\n" \
"v_mfma_f32_16x16x32_bf16 v[64:67], a[40:43], v[32:35], v[64:67]\n" \
"buffer_load_dwordx4 v4, s[36:39], s61 offen offset:3072 sc1 lds\n" \
"v_mfma_f32_16x16x32_bf16 v[68:71], a[40:43], v[36:39], v[68:71]\n" \
"ds_read_b128 v[24:27], v12 offset:13312\n" \
"v_cvt_pk_bf16_f32 v80, v48, v49\n" \
"v_mfma_f32_16x16x32_bf16 v[72:75], a[168:171], v[32:35], v[72:75]\n" \
"ds_read_b128 v[28:31], v12 offset:29696\n" \
"v_cvt_pk_bf16_f32 v81, v50, v51\n" \
"v_mfma_f32_16x16x32_bf16 v[76:79], a[168:171], v[36:39], v[76:79]\n" \
"s_waitcnt lgkmcnt(4)\n" \
"v_mul_f32 v52, 0xbfb8aa3b, v52\n" \
"v_mul_f32 v53, 0xbfb8aa3b, v53\n" \
"v_mfma_f32_16x16x32_bf16 v[64:67], a[44:47], v[40:43], v[64:67]\n" \
"v_mul_f32 v54, 0xbfb8aa3b, v54\n" \
"v_mul_f32 v55, 0xbfb8aa3b, v55\n" \
"v_mfma_f32_16x16x32_bf16 v[68:71], a[44:47], v[44:47], v[68:71]\n" \
"ds_read_b128 v[32:35], v12 offset:14336\n" \
"v_mfma_f32_16x16x32_bf16 v[72:75], a[172:175], v[40:43], v[72:75]\n" \
"ds_read_b128 v[36:39], v12 offset:30720\n" \
"v_mfma_f32_16x16x32_bf16 v[76:79], a[172:175], v[44:47], v[76:79]\n" \
"s_waitcnt lgkmcnt(4)\n" \
"v_exp_f32_e64 v52, v52\n" \
"v_mfma_f32_16x16x32_bf16 v[64:67], a[48:51], v[16:19], v[64:67]\n" \
"v_exp_f32_e64 v53, v53\n" \
"v_mfma_f32_16x16x32_bf16 v[68:71], a[48:51], v[20:23], v[68:71]\n" \
"ds_read_b128 v[40:43], v12 offset:15360\n" \
"v_mfma_f32_16x16x32_bf16 v[72:75], a[176:179], v[16:19], v[72:75]\n" \
"ds_read_b128 v[44:47], v12 offset:31744\n" \
"v_mfma_f32_16x16x32_bf16 v[76:79], a[176:179], v[20:23], v[76:79]\n" \
"s_waitcnt lgkmcnt(4)\n" \
"v_exp_f32 v54, v54\n" \
"v_mfma_f32_16x16x32_bf16 v[64:67], a[52:55], v[24:27], v[64:67]\n" \
"s_waitcnt vmcnt(9)\n" \
"s_barrier\n" \
"v_mfma_f32_16x16x32_bf16 v[68:71], a[52:55], v[28:31], v[68:71]\n" \
"ds_read_b128 v[16:19], v12 offset:32768\n" \
"v_mfma_f32_16x16x32_bf16 v[72:75], a[180:183], v[24:27], v[72:75]\n" \
"ds_read_b128 v[20:23], v12 offset:49152\n" \
"v_mfma_f32_16x16x32_bf16 v[76:79], a[180:183], v[28:31], v[76:79]\n" \
"s_waitcnt lgkmcnt(4)\n" \
"buffer_store_dword v13, v6, s[36:39], 0 offen sc1\n" \
"v_exp_f32 v55, v55\n" \
"v_mfma_f32_16x16x32_bf16 v[64:67], a[56:59], v[32:35], v[64:67]\n" \
"v_add_f32 v52, 1.0, v52\n" \
"v_add_f32 v53, 1.0, v53\n" \
"v_mfma_f32_16x16x32_bf16 v[68:71], a[56:59], v[36:39], v[68:71]\n" \
"ds_read_b128 v[24:27], v12 offset:33792\n" \
"v_add_f32_e64 v54, 1.0, v54\n" \
"v_mfma_f32_16x16x32_bf16 v[72:75], a[184:187], v[32:35], v[72:75]\n" \
"ds_read_b128 v[28:31], v12 offset:50176\n" \
"v_add_f32_e64 v55, 1.0, v55\n" \
"v_mfma_f32_16x16x32_bf16 v[76:79], a[184:187], v[36:39], v[76:79]\n" \
"s_waitcnt lgkmcnt(4)\n" \
"v_rcp_f32 v52, v52\n" \
"v_mfma_f32_16x16x32_bf16 v[64:67], a[60:63], v[40:43], v[64:67]\n" \
"v_rcp_f32_e64 v53, v53\n" \
"v_mfma_f32_16x16x32_bf16 v[68:71], a[60:63], v[44:47], v[68:71]\n" \
"ds_read_b128 v[32:35], v12 offset:34816\n" \
"v_mfma_f32_16x16x32_bf16 v[72:75], a[188:191], v[40:43], v[72:75]\n" \
"ds_read_b128 v[36:39], v12 offset:51200\n" \
"v_mfma_f32_16x16x32_bf16 v[76:79], a[188:191], v[44:47], v[76:79]\n" \
"s_waitcnt lgkmcnt(4)\n" \
"v_rcp_f32 v54, v54\n" \
"v_mfma_f32_16x16x32_bf16 v[64:67], a[64:67], v[16:19], v[64:67]\n" \
"s_add_u32 s61, s61, 28672\n" \
"s_add_u32 s56, s56, 0x20000\n" \
"s_add_u32 m0, s55, 32768\n" \
"s_nop 0\n" \
"buffer_load_dwordx4 v4, s[36:39], s61 offen offset:0 sc1 lds\n" \
"v_mfma_f32_16x16x32_bf16 v[68:71], a[64:67], v[20:23], v[68:71]\n" \
"ds_read_b128 v[40:43], v12 offset:35840\n" \
"v_mfma_f32_16x16x32_bf16 v[72:75], a[192:195], v[16:19], v[72:75]\n" \
"ds_read_b128 v[44:47], v12 offset:52224\n" \
"v_mfma_f32_16x16x32_bf16 v[76:79], a[192:195], v[20:23], v[76:79]\n" \
"s_waitcnt lgkmcnt(4)\n" \
"v_rcp_f32_e64 v55, v55\n" \
"v_mfma_f32_16x16x32_bf16 v[64:67], a[68:71], v[24:27], v[64:67]\n" \
"buffer_load_dwordx4 v4, s[36:39], s61 offen offset:1024 sc1 lds\n" \
"v_mfma_f32_16x16x32_bf16 v[68:71], a[68:71], v[28:31], v[68:71]\n" \
"ds_read_b128 v[16:19], v12 offset:36864\n" \
"v_mul_f32_e64 v52, v52, v60\n" \
"v_mfma_f32_16x16x32_bf16 v[72:75], a[196:199], v[24:27], v[72:75]\n" \
"ds_read_b128 v[20:23], v12 offset:53248\n" \
"v_mul_f32_e64 v53, v53, v61\n" \
"v_mfma_f32_16x16x32_bf16 v[76:79], a[196:199], v[28:31], v[76:79]\n" \
"s_waitcnt lgkmcnt(4)\n" \
"v_mul_f32 v54, v54, v62\n" \
"v_mul_f32_e64 v55, v55, v63\n" \
"v_mfma_f32_16x16x32_bf16 v[64:67], a[72:75], v[32:35], v[64:67]\n" \
"v_cvt_pk_bf16_f32 v82, v52, v53\n" \
"v_cvt_pk_bf16_f32 v83, v54, v55\n" \
"v_mfma_f32_16x16x32_bf16 v[68:71], a[72:75], v[36:39], v[68:71]\n" \
"ds_read_b128 v[24:27], v12 offset:37888\n" \
"s_nop 0\n" \
"v_mfma_f32_16x16x32_bf16 v[72:75], a[200:203], v[32:35], v[72:75]\n" \
"ds_read_b128 v[28:31], v12 offset:54272\n" \
"s_nop 0\n" \
"v_mfma_f32_16x16x32_bf16 v[76:79], a[200:203], v[36:39], v[76:79]\n" \
"s_waitcnt lgkmcnt(4)\n" \
"v_permlane16_swap_b32 v80, v82\n" \
"v_permlane16_swap_b32 v81, v83\n" \
"v_mfma_f32_16x16x32_bf16 v[64:67], a[76:79], v[40:43], v[64:67]\n" \
"buffer_load_dwordx4 v4, s[36:39], s61 offen offset:2048 sc1 lds\n" \
"v_mfma_f32_16x16x32_bf16 v[68:71], a[76:79], v[44:47], v[68:71]\n" \
"ds_read_b128 v[32:35], v12 offset:38912\n" \
"buffer_store_dwordx4 v[80:83], v5, s[36:39], s57 offen sc1\n" \
"v_mfma_f32_16x16x32_bf16 v[72:75], a[204:207], v[40:43], v[72:75]\n" \
"ds_read_b128 v[36:39], v12 offset:55296\n" \
"v_mfma_f32_16x16x32_bf16 v[76:79], a[204:207], v[44:47], v[76:79]\n" \
"s_waitcnt lgkmcnt(4)\n" \
"v_mfma_f32_16x16x32_bf16 v[64:67], a[80:83], v[16:19], v[64:67]\n" \
"buffer_load_dwordx4 v4, s[36:39], s61 offen offset:3072 sc1 lds\n" \
"v_mfma_f32_16x16x32_bf16 v[68:71], a[80:83], v[20:23], v[68:71]\n" \
"ds_read_b128 v[40:43], v12 offset:39936\n" \
"v_mfma_f32_16x16x32_bf16 v[72:75], a[208:211], v[16:19], v[72:75]\n" \
"ds_read_b128 v[44:47], v12 offset:56320\n" \
"v_mfma_f32_16x16x32_bf16 v[76:79], a[208:211], v[20:23], v[76:79]\n" \
"s_waitcnt lgkmcnt(4)\n" \
"v_mfma_f32_16x16x32_bf16 v[64:67], a[84:87], v[24:27], v[64:67]\n" \
"v_mfma_f32_16x16x32_bf16 v[68:71], a[84:87], v[28:31], v[68:71]\n" \
"ds_read_b128 v[16:19], v12 offset:40960\n" \
"v_mfma_f32_16x16x32_bf16 v[72:75], a[212:215], v[24:27], v[72:75]\n" \
"ds_read_b128 v[20:23], v12 offset:57344\n" \
"v_mfma_f32_16x16x32_bf16 v[76:79], a[212:215], v[28:31], v[76:79]\n" \
"s_waitcnt lgkmcnt(4)\n" \
"v_mfma_f32_16x16x32_bf16 v[64:67], a[88:91], v[32:35], v[64:67]\n" \
"s_add_u32 m0, s55, 36864\n" \
"s_add_u32 s61, s61, 4096\n" \
"buffer_load_dwordx4 v4, s[36:39], s61 offen offset:0 sc1 lds\n" \
"v_mfma_f32_16x16x32_bf16 v[68:71], a[88:91], v[36:39], v[68:71]\n" \
"ds_read_b128 v[24:27], v12 offset:41984\n" \
"v_mfma_f32_16x16x32_bf16 v[72:75], a[216:219], v[32:35], v[72:75]\n" \
"ds_read_b128 v[28:31], v12 offset:58368\n" \
"v_mfma_f32_16x16x32_bf16 v[76:79], a[216:219], v[36:39], v[76:79]\n" \
"s_waitcnt lgkmcnt(4)\n" \
"v_mfma_f32_16x16x32_bf16 v[64:67], a[92:95], v[40:43], v[64:67]\n" \
"buffer_load_dwordx4 v4, s[36:39], s61 offen offset:1024 sc1 lds\n" \
"v_mfma_f32_16x16x32_bf16 v[68:71], a[92:95], v[44:47], v[68:71]\n" \
"ds_read_b128 v[32:35], v12 offset:43008\n" \
"v_mfma_f32_16x16x32_bf16 v[72:75], a[220:223], v[40:43], v[72:75]\n" \
"ds_read_b128 v[36:39], v12 offset:59392\n" \
"v_mfma_f32_16x16x32_bf16 v[76:79], a[220:223], v[44:47], v[76:79]\n" \
"s_waitcnt lgkmcnt(4)\n" \
"v_mfma_f32_16x16x32_bf16 v[64:67], a[96:99], v[16:19], v[64:67]\n" \
"v_mfma_f32_16x16x32_bf16 v[68:71], a[96:99], v[20:23], v[68:71]\n" \
"ds_read_b128 v[40:43], v12 offset:44032\n" \
"v_mfma_f32_16x16x32_bf16 v[72:75], a[224:227], v[16:19], v[72:75]\n" \
"ds_read_b128 v[44:47], v12 offset:60416\n" \
"v_mfma_f32_16x16x32_bf16 v[76:79], a[224:227], v[20:23], v[76:79]\n" \
"s_waitcnt lgkmcnt(4)\n" \
"v_mfma_f32_16x16x32_bf16 v[64:67], a[100:103], v[24:27], v[64:67]\n" \
"buffer_load_dwordx4 v4, s[36:39], s61 offen offset:2048 sc1 lds\n" \
"v_mfma_f32_16x16x32_bf16 v[68:71], a[100:103], v[28:31], v[68:71]\n" \
"ds_read_b128 v[16:19], v12 offset:45056\n" \
"v_mfma_f32_16x16x32_bf16 v[72:75], a[228:231], v[24:27], v[72:75]\n" \
"ds_read_b128 v[20:23], v12 offset:61440\n" \
"v_mfma_f32_16x16x32_bf16 v[76:79], a[228:231], v[28:31], v[76:79]\n" \
"s_waitcnt lgkmcnt(4)\n" \
"v_mfma_f32_16x16x32_bf16 v[64:67], a[104:107], v[32:35], v[64:67]\n" \
"buffer_load_dwordx4 v4, s[36:39], s61 offen offset:3072 sc1 lds\n" \
"v_mfma_f32_16x16x32_bf16 v[68:71], a[104:107], v[36:39], v[68:71]\n" \
"ds_read_b128 v[24:27], v12 offset:46080\n" \
"v_mfma_f32_16x16x32_bf16 v[72:75], a[232:235], v[32:35], v[72:75]\n" \
"ds_read_b128 v[28:31], v12 offset:62464\n" \
"v_mfma_f32_16x16x32_bf16 v[76:79], a[232:235], v[36:39], v[76:79]\n" \
"s_waitcnt lgkmcnt(4)\n" \
"v_mfma_f32_16x16x32_bf16 v[64:67], a[108:111], v[40:43], v[64:67]\n" \
"s_lshl_b32 s66, s59, s49\n" \
"s_lshr_b32 s65, s66, 8\n" \
"v_mfma_f32_16x16x32_bf16 v[68:71], a[108:111], v[44:47], v[68:71]\n" \
"ds_read_b128 v[32:35], v12 offset:47104\n" \
"v_mfma_f32_16x16x32_bf16 v[72:75], a[236:239], v[40:43], v[72:75]\n" \
"ds_read_b128 v[36:39], v12 offset:63488\n" \
"v_mfma_f32_16x16x32_bf16 v[76:79], a[236:239], v[44:47], v[76:79]\n" \
"s_waitcnt lgkmcnt(4)\n" \
"s_and_b32 s65, s65, 0x2000\n" \
"s_xor_b32 s66, s66, s65\n" \
"v_mfma_f32_16x16x32_bf16 v[64:67], a[112:115], v[16:19], v[64:67]\n" \
"v_mfma_f32_16x16x32_bf16 v[68:71], a[112:115], v[20:23], v[68:71]\n" \
"ds_read_b128 v[40:43], v12 offset:48128\n" \
"v_mfma_f32_16x16x32_bf16 v[72:75], a[240:243], v[16:19], v[72:75]\n" \
"ds_read_b128 v[44:47], v12 offset:64512\n" \
"v_mfma_f32_16x16x32_bf16 v[76:79], a[240:243], v[20:23], v[76:79]\n" \
"s_waitcnt lgkmcnt(4)\n" \
"v_mfma_f32_16x16x32_bf16 v[64:67], a[116:119], v[24:27], v[64:67]\n" \
"s_waitcnt vmcnt(10)\n" \
"s_barrier\n" \
"v_mfma_f32_16x16x32_bf16 v[68:71], a[116:119], v[28:31], v[68:71]\n" \
"ds_read_b128 v[16:19], v10 offset:0\n" \
"v_mfma_f32_16x16x32_bf16 v[72:75], a[244:247], v[24:27], v[72:75]\n" \
"ds_read_b128 v[20:23], v10 offset:16384\n" \
"v_mfma_f32_16x16x32_bf16 v[76:79], a[244:247], v[28:31], v[76:79]\n" \
"s_waitcnt lgkmcnt(4)\n" \
"v_mfma_f32_16x16x32_bf16 v[64:67], a[120:123], v[32:35], v[64:67]\n" \
"v_cmp_gt_u32 vcc, 16, v15\n" \
"v_mfma_f32_16x16x32_bf16 v[68:71], a[120:123], v[36:39], v[68:71]\n" \
"ds_read_b128 v[24:27], v10 offset:1024\n" \
"v_mfma_f32_16x16x32_bf16 v[72:75], a[248:251], v[32:35], v[72:75]\n" \
"ds_read_b128 v[28:31], v10 offset:17408\n" \
"v_mfma_f32_16x16x32_bf16 v[76:79], a[248:251], v[36:39], v[76:79]\n" \
"s_waitcnt lgkmcnt(4)\n" \
"v_mfma_f32_16x16x32_bf16 v[64:67], a[124:127], v[40:43], v[64:67]\n" \
"s_cbranch_vccnz .Lp1g1_m1_%=\n" \
".Lp1g1_m1k_%=:\n" \
"v_mfma_f32_16x16x32_bf16 v[68:71], a[124:127], v[44:47], v[68:71]\n" \
"ds_read_b128 v[32:35], v10 offset:2048\n" \
"v_mfma_f32_16x16x32_bf16 v[72:75], a[252:255], v[40:43], v[72:75]\n" \
"ds_read_b128 v[36:39], v10 offset:18432\n" \
"v_mfma_f32_16x16x32_bf16 v[76:79], a[252:255], v[44:47], v[76:79]\n" \
"s_waitcnt lgkmcnt(4)\n" \
"s_xor_b32 s57, s66, s64\n" \
"s_add_u32 s58, s57, s50\n" \
"s_add_u32 s59, s59, 1\n" \
"v_mov_b32_e64 v5, v7\n" \
"v_mov_b32_e64 v6, v11\n" \
"s_cmp_ge_u32 s59, s47\n" \
"s_nop 0\n" \
"s_cbranch_scc0 .Lp1g1_loop_%=\n" \
".Lp1g1_out1_%=:\n" \
"s_nop 7\n" \
"s_nop 7\n" \
"v_mul_f32 v64, 0xbfb8aa3b, v64\n" \
"v_mul_f32 v65, 0xbfb8aa3b, v65\n" \
"v_mul_f32 v66, 0xbfb8aa3b, v66\n" \
"v_mul_f32 v67, 0xbfb8aa3b, v67\n" \
"v_exp_f32 v64, v64\n" \
"v_exp_f32 v65, v65\n" \
"v_exp_f32 v66, v66\n" \
"v_exp_f32 v67, v67\n" \
"v_add_f32 v64, 1.0, v64\n" \
"v_add_f32 v65, 1.0, v65\n" \
"v_add_f32 v66, 1.0, v66\n" \
"v_add_f32 v67, 1.0, v67\n" \
"v_rcp_f32 v64, v64\n" \
"v_rcp_f32 v65, v65\n" \
"v_rcp_f32 v66, v66\n" \
"v_rcp_f32 v67, v67\n" \
"v_mul_f32 v64, v64, v72\n" \
"v_mul_f32 v65, v65, v73\n" \
"v_mul_f32 v66, v66, v74\n" \
"v_mul_f32 v67, v67, v75\n" \
"v_cvt_pk_bf16_f32 v80, v64, v65\n" \
"v_cvt_pk_bf16_f32 v81, v66, v67\n" \
"v_mul_f32 v68, 0xbfb8aa3b, v68\n" \
"v_mul_f32 v69, 0xbfb8aa3b, v69\n" \
"v_mul_f32 v70, 0xbfb8aa3b, v70\n" \
"v_mul_f32 v71, 0xbfb8aa3b, v71\n" \
"v_exp_f32 v68, v68\n" \
"v_exp_f32 v69, v69\n" \
"v_exp_f32 v70, v70\n" \
"v_exp_f32 v71, v71\n" \
"v_add_f32 v68, 1.0, v68\n" \
"v_add_f32 v69, 1.0, v69\n" \
"v_add_f32 v70, 1.0, v70\n" \
"v_add_f32 v71, 1.0, v71\n" \
"v_rcp_f32 v68, v68\n" \
"v_rcp_f32 v69, v69\n" \
"v_rcp_f32 v70, v70\n" \
"v_rcp_f32 v71, v71\n" \
"v_mul_f32 v68, v68, v76\n" \
"v_mul_f32 v69, v69, v77\n" \
"v_mul_f32 v70, v70, v78\n" \
"v_mul_f32 v71, v71, v79\n" \
"v_cvt_pk_bf16_f32 v82, v68, v69\n" \
"v_cvt_pk_bf16_f32 v83, v70, v71\n" \
"s_nop 0\n" \
"s_nop 0\n" \
"v_permlane16_swap_b32 v80, v82\n" \
"v_permlane16_swap_b32 v81, v83\n" \
"buffer_store_dwordx4 v[80:83], v5, s[36:39], s57 offen sc1\n" \
"s_branch .Lp1g1_end_%=\n" \
".Lp1g1_out0_%=:\n" \
"s_nop 7\n" \
"s_nop 7\n" \
"v_mul_f32 v48, 0xbfb8aa3b, v48\n" \
"v_mul_f32 v49, 0xbfb8aa3b, v49\n" \
"v_mul_f32 v50, 0xbfb8aa3b, v50\n" \
"v_mul_f32 v51, 0xbfb8aa3b, v51\n" \
"v_exp_f32 v48, v48\n" \
"v_exp_f32 v49, v49\n" \
"v_exp_f32 v50, v50\n" \
"v_exp_f32 v51, v51\n" \
"v_add_f32 v48, 1.0, v48\n" \
"v_add_f32 v49, 1.0, v49\n" \
"v_add_f32 v50, 1.0, v50\n" \
"v_add_f32 v51, 1.0, v51\n" \
"v_rcp_f32 v48, v48\n" \
"v_rcp_f32 v49, v49\n" \
"v_rcp_f32 v50, v50\n" \
"v_rcp_f32 v51, v51\n" \
"v_mul_f32 v48, v48, v56\n" \
"v_mul_f32 v49, v49, v57\n" \
"v_mul_f32 v50, v50, v58\n" \
"v_mul_f32 v51, v51, v59\n" \
"v_cvt_pk_bf16_f32 v80, v48, v49\n" \
"v_cvt_pk_bf16_f32 v81, v50, v51\n" \
"v_mul_f32 v52, 0xbfb8aa3b, v52\n" \
"v_mul_f32 v53, 0xbfb8aa3b, v53\n" \
"v_mul_f32 v54, 0xbfb8aa3b, v54\n" \
"v_mul_f32 v55, 0xbfb8aa3b, v55\n" \
"v_exp_f32 v52, v52\n" \
"v_exp_f32 v53, v53\n" \
"v_exp_f32 v54, v54\n" \
"v_exp_f32 v55, v55\n" \
"v_add_f32 v52, 1.0, v52\n" \
"v_add_f32 v53, 1.0, v53\n" \
"v_add_f32 v54, 1.0, v54\n" \
"v_add_f32 v55, 1.0, v55\n" \
"v_rcp_f32 v52, v52\n" \
"v_rcp_f32 v53, v53\n" \
"v_rcp_f32 v54, v54\n" \
"v_rcp_f32 v55, v55\n" \
"v_mul_f32 v52, v52, v60\n" \
"v_mul_f32 v53, v53, v61\n" \
"v_mul_f32 v54, v54, v62\n" \
"v_mul_f32 v55, v55, v63\n" \
"v_cvt_pk_bf16_f32 v82, v52, v53\n" \
"v_cvt_pk_bf16_f32 v83, v54, v55\n" \
"s_nop 0\n" \
"s_nop 0\n" \
"v_permlane16_swap_b32 v80, v82\n" \
"v_permlane16_swap_b32 v81, v83\n" \
"buffer_store_dwordx4 v[80:83], v5, s[36:39], s57 offen sc1\n" \
".Lp1g1_end_%=:\n" \
"s_waitcnt vmcnt(0) lgkmcnt(0)\n" \
"s_barrier\n" \
"v_mov_b32 v13, s59\n" \
"buffer_store_dword v13, v6, s[36:39], 0 offen sc1\n" \
"s_waitcnt vmcnt(0) lgkmcnt(0)\n" \
"s_branch .Lp1g1_x_%=\n" \
".Lp1g1_l0_%=:\n" \
"s_nop 7\n" \
"s_nop 7\n" \
"s_waitcnt vmcnt(0) lgkmcnt(0)\n" \
"s_mov_b32 s62, 0\n" \
".Lp1g1_l0a_%=:\n" \
"buffer_load_dword v15, v14, s[36:39], s63 offen sc1\n" \
"s_waitcnt vmcnt(0)\n" \
"v_cmp_gt_u32 vcc, 16, v15\n" \
"s_nop 4\n" \
"s_cbranch_vccz .Lp1g1_l0k_%=\n" \
"s_sleep 4\n" \
"s_add_u32 s62, s62, 1\n" \
"s_cmp_lt_u32 s62, 0x10000\n" \
"s_cbranch_scc1 .Lp1g1_l0a_%=\n" \
"v_mov_b32 v15, 256\n" \
"buffer_store_dword v15, v15, s[36:39], 0 offen sc1\n" \
"s_waitcnt vmcnt(0) lgkmcnt(0)\n" \
"s_branch .Lp1g1_l0k_%=\n" \
".Lp1g1_l1_%=:\n" \
"s_nop 7\n" \
"s_nop 7\n" \
"s_waitcnt vmcnt(0) lgkmcnt(0)\n" \
"s_mov_b32 s62, 0\n" \
".Lp1g1_l1a_%=:\n" \
"buffer_load_dword v15, v14, s[36:39], s63 offen sc1\n" \
"s_waitcnt vmcnt(0)\n" \
"v_cmp_gt_u32 vcc, 16, v15\n" \
"s_nop 4\n" \
"s_cbranch_vccz .Lp1g1_l1k_%=\n" \
"s_sleep 4\n" \
"s_add_u32 s62, s62, 1\n" \
"s_cmp_lt_u32 s62, 0x10000\n" \
"s_cbranch_scc1 .Lp1g1_l1a_%=\n" \
"v_mov_b32 v15, 256\n" \
"buffer_store_dword v15, v15, s[36:39], 0 offen sc1\n" \
"s_waitcnt vmcnt(0) lgkmcnt(0)\n" \
"s_branch .Lp1g1_l1k_%=\n" \
".Lp1g1_m0_%=:\n" \
"s_nop 7\n" \
"s_nop 7\n" \
"s_waitcnt vmcnt(0) lgkmcnt(0)\n" \
"s_mov_b32 s62, 0\n" \
".Lp1g1_m0a_%=:\n" \
"buffer_load_dword v15, v14, s[36:39], s63 offen sc1\n" \
"s_waitcnt vmcnt(0)\n" \
"v_cmp_gt_u32 vcc, 16, v15\n" \
"s_nop 4\n" \
"s_cbranch_vccz .Lp1g1_m0k_%=\n" \
"s_sleep 4\n" \
"s_add_u32 s62, s62, 1\n" \
"s_cmp_lt_u32 s62, 0x10000\n" \
"s_cbranch_scc1 .Lp1g1_m0a_%=\n" \
"v_mov_b32 v15, 256\n" \
"buffer_store_dword v15, v15, s[36:39], 0 offen sc1\n" \
"s_waitcnt vmcnt(0) lgkmcnt(0)\n" \
"s_branch .Lp1g1_m0k_%=\n" \
".Lp1g1_m1_%=:\n" \
"s_nop 7\n" \
"s_nop 7\n" \
"s_waitcnt vmcnt(0) lgkmcnt(0)\n" \
"s_mov_b32 s62, 0\n" \
".Lp1g1_m1a_%=:\n" \
"buffer_load_dword v15, v14, s[36:39], s63 offen sc1\n" \
"s_waitcnt vmcnt(0)\n" \
"v_cmp_gt_u32 vcc, 16, v15\n" \
"s_nop 4\n" \
"s_cbranch_vccz .Lp1g1_m1k_%=\n" \
"s_sleep 4\n" \
"s_add_u32 s62, s62, 1\n" \
"s_cmp_lt_u32 s62, 0x10000\n" \
"s_cbranch_scc1 .Lp1g1_m1a_%=\n" \
"v_mov_b32 v15, 256\n" \
"buffer_store_dword v15, v15, s[36:39], 0 offen sc1\n" \
"s_waitcnt vmcnt(0) lgkmcnt(0)\n" \
"s_branch .Lp1g1_m1k_%=\n" \
".Lp1g1_x_%=:\n"
#define CLOB_P1 \
"a0", "a1", "a2", "a3", "a4", "a5", "a6", "a7", "a8", "a9", "a10", "a11", "a12", "a13", "a14", "a15", "a16", "a17", "a18", "a19", "a20", "a21", "a22", "a23", "a24", "a25", "a26", "a27", "a28", "a29", "a30", "a31", "a32", "a33", "a34", "a35", "a36", "a37", "a38", "a39", "a40", "a41", "a42", "a43", "a44", "a45", "a46", "a47", "a48", "a49", "a50", "a51", "a52", "a53", "a54", "a55", "a56", "a57", "a58", "a59", "a60", "a61", "a62", "a63", "a64", "a65", "a66", "a67", "a68", "a69", "a70", "a71", "a72", "a73", "a74", "a75", "a76", "a77", "a78", "a79", "a80", "a81", "a82", "a83", "a84", "a85", "a86", "a87", "a88", "a89", "a90", "a91", "a92", "a93", "a94", "a95", "a96", "a97", "a98", "a99", "a100", "a101", "a102", "a103", "a104", "a105", "a106", "a107", "a108", "a109", "a110", "a111", "a112", "a113", "a114", "a115", "a116", "a117", "a118", "a119", "a120", "a121", "a122", "a123", "a124", "a125", "a126", "a127", "a128", "a129", "a130", "a131", "a132", "a133", "a134", "a135", "a136", "a137", "a138", "a139", "a140", "a141", "a142", "a143", "a144", "a145", "a146", "a147", "a148", "a149", "a150", "a151", "a152", "a153", "a154", "a155", "a156", "a157", "a158", "a159", "a160", "a161", "a162", "a163", "a164", "a165", "a166", "a167", "a168", "a169", "a170", "a171", "a172", "a173", "a174", "a175", "a176", "a177", "a178", "a179", "a180", "a181", "a182", "a183", "a184", "a185", "a186", "a187", "a188", "a189", "a190", "a191", "a192", "a193", "a194", "a195", "a196", "a197", "a198", "a199", "a200", "a201", "a202", "a203", "a204", "a205", "a206", "a207", "a208", "a209", "a210", "a211", "a212", "a213", "a214", "a215", "a216", "a217", "a218", "a219", "a220", "a221", "a222", "a223", "a224", "a225", "a226", "a227", "a228", "a229", "a230", "a231", "a232", "a233", "a234", "a235", "a236", "a237", "a238", "a239", "a240", "a241", "a242", "a243", "a244", "a245", "a246", "a247", "a248", "a249", "a250", "a251", "a252", "a253", "a254", "a255", "s52", "s53", "s54", "s55", "s56", "s57", "s58", "s59", "s60", "s61", "s62", "s63", "s65", "s66", "v7", "v8", "v9", "v10", "v11", "v12", "v13", "v14", "v15", "v16", "v17", "v18", "v19", "v20", "v21", "v22", "v23", "v24", "v25", "v26", "v27", "v28", "v29", "v30", "v31", "v32", "v33", "v34", "v35", "v36", "v37", "v38", "v39", "v40", "v41", "v42", "v43", "v44", "v45", "v46", "v47", "v48", "v49", "v50", "v51", "v52", "v53", "v54", "v55", "v56", "v57", "v58", "v59", "v60", "v61", "v62", "v63", "v64", "v65", "v66", "v67", "v68", "v69", "v70", "v71", "v72", "v73", "v74", "v75", "v76", "v77", "v78", "v79", "v80", "v81", "v82", "v83", "v84", "v85", "v86", "v87", "vcc", "scc", "memory"
#define ASM_QK \
"s_nop 7\n" \
"s_nop 7\n" \
"v_add_u32 v36, 0x10000, v34\n" \
"v_add_u32 v38, 0x20000, v34\n" \
"v_add_u32 v37, 0x10000, v35\n" \
"v_add_u32 v39, 0x20000, v35\n" \
"s_lshl_b32 s62, s48, 12\n" \
"s_add_u32 m0, s62, 147456\n" \
"s_xor_b32 s54, s15, 524288\n" \
"buffer_load_dwordx4 v32, s[40:43], s54 offen nt lds\n" \
"s_add_u32 m0, s62, 148480\n" \
"s_xor_b32 s54, s15, 524288\n" \
"buffer_load_dwordx4 v33, s[40:43], s54 offen nt lds\n" \
"s_add_u32 m0, s62, 149504\n" \
"s_xor_b32 s54, s15, 655360\n" \
"buffer_load_dwordx4 v32, s[40:43], s54 offen nt lds\n" \
"s_add_u32 m0, s62, 150528\n" \
"s_xor_b32 s54, s15, 655360\n" \
"buffer_load_dwordx4 v33, s[40:43], s54 offen nt lds\n" \
"s_add_u32 m0, s62, 131072\n" \
"s_xor_b32 s54, s15, 786432\n" \
"buffer_load_dwordx4 v32, s[40:43], s54 offen nt lds\n" \
"s_add_u32 m0, s62, 132096\n" \
"s_xor_b32 s54, s15, 786432\n" \
"buffer_load_dwordx4 v33, s[40:43], s54 offen nt lds\n" \
"s_add_u32 m0, s62, 133120\n" \
"s_xor_b32 s54, s15, 917504\n" \
"buffer_load_dwordx4 v32, s[40:43], s54 offen nt lds\n" \
"s_add_u32 m0, s62, 134144\n" \
"s_xor_b32 s54, s15, 917504\n" \
"buffer_load_dwordx4 v33, s[40:43], s54 offen nt lds\n" \
"s_add_u32 m0, s62, 114688\n" \
"s_xor_b32 s54, s15, 1572864\n" \
"buffer_load_dwordx4 v32, s[40:43], s54 offen nt lds\n" \
"s_add_u32 m0, s62, 115712\n" \
"s_xor_b32 s54, s15, 1572864\n" \
"buffer_load_dwordx4 v33, s[40:43], s54 offen nt lds\n" \
"s_add_u32 m0, s62, 116736\n" \
"s_xor_b32 s54, s15, 1703936\n" \
"buffer_load_dwordx4 v32, s[40:43], s54 offen nt lds\n" \
"s_add_u32 m0, s62, 117760\n" \
"s_xor_b32 s54, s15, 1703936\n" \
"buffer_load_dwordx4 v33, s[40:43], s54 offen nt lds\n" \
"s_add_u32 m0, s62, 98304\n" \
"s_xor_b32 s54, s15, 1835008\n" \
"buffer_load_dwordx4 v32, s[40:43], s54 offen nt lds\n" \
"s_add_u32 m0, s62, 99328\n" \
"s_xor_b32 s54, s15, 1835008\n" \
"buffer_load_dwordx4 v33, s[40:43], s54 offen nt lds\n" \
"s_add_u32 m0, s62, 100352\n" \
"s_xor_b32 s54, s15, 1966080\n" \
"buffer_load_dwordx4 v32, s[40:43], s54 offen nt lds\n" \
"s_add_u32 m0, s62, 101376\n" \
"s_xor_b32 s54, s15, 1966080\n" \
"buffer_load_dwordx4 v33, s[40:43], s54 offen nt lds\n" \
"s_add_u32 m0, s62, 81920\n" \
"s_xor_b32 s54, s15, 2621440\n" \
"buffer_load_dwordx4 v32, s[40:43], s54 offen nt lds\n" \
"s_add_u32 m0, s62, 82944\n" \
"s_xor_b32 s54, s15, 2621440\n" \
"buffer_load_dwordx4 v33, s[40:43], s54 offen nt lds\n" \
"s_add_u32 m0, s62, 83968\n" \
"s_xor_b32 s54, s15, 2752512\n" \
"buffer_load_dwordx4 v32, s[40:43], s54 offen nt lds\n" \
"s_add_u32 m0, s62, 84992\n" \
"s_xor_b32 s54, s15, 2752512\n" \
"buffer_load_dwordx4 v33, s[40:43], s54 offen nt lds\n" \
"s_add_u32 m0, s62, 65536\n" \
"s_xor_b32 s54, s15, 2883584\n" \
"buffer_load_dwordx4 v32, s[40:43], s54 offen nt lds\n" \
"s_add_u32 m0, s62, 66560\n" \
"s_xor_b32 s54, s15, 2883584\n" \
"buffer_load_dwordx4 v33, s[40:43], s54 offen nt lds\n" \
"s_add_u32 m0, s62, 67584\n" \
"s_xor_b32 s54, s15, 3014656\n" \
"buffer_load_dwordx4 v32, s[40:43], s54 offen nt lds\n" \
"s_add_u32 m0, s62, 68608\n" \
"s_xor_b32 s54, s15, 3014656\n" \
"buffer_load_dwordx4 v33, s[40:43], s54 offen nt lds\n" \
"s_add_u32 m0, s62, 49152\n" \
"s_xor_b32 s54, s15, 3670016\n" \
"buffer_load_dwordx4 v32, s[40:43], s54 offen nt lds\n" \
"s_add_u32 m0, s62, 50176\n" \
"s_xor_b32 s54, s15, 3670016\n" \
"buffer_load_dwordx4 v33, s[40:43], s54 offen nt lds\n" \
"s_add_u32 m0, s62, 51200\n" \
"s_xor_b32 s54, s15, 3801088\n" \
"buffer_load_dwordx4 v32, s[40:43], s54 offen nt lds\n" \
"s_add_u32 m0, s62, 52224\n" \
"s_xor_b32 s54, s15, 3801088\n" \
"buffer_load_dwordx4 v33, s[40:43], s54 offen nt lds\n" \
"s_add_u32 m0, s62, 32768\n" \
"s_xor_b32 s54, s15, 3932160\n" \
"buffer_load_dwordx4 v32, s[40:43], s54 offen nt lds\n" \
"s_add_u32 m0, s62, 33792\n" \
"s_xor_b32 s54, s15, 3932160\n" \
"buffer_load_dwordx4 v33, s[40:43], s54 offen nt lds\n" \
"s_add_u32 m0, s62, 34816\n" \
"s_xor_b32 s54, s15, 4063232\n" \
"buffer_load_dwordx4 v32, s[40:43], s54 offen nt lds\n" \
"s_add_u32 m0, s62, 35840\n" \
"s_xor_b32 s54, s15, 4063232\n" \
"buffer_load_dwordx4 v33, s[40:43], s54 offen nt lds\n" \
"s_add_u32 m0, s62, 16384\n" \
"s_xor_b32 s54, s15, 4718592\n" \
"buffer_load_dwordx4 v32, s[40:43], s54 offen nt lds\n" \
"s_add_u32 m0, s62, 17408\n" \
"s_xor_b32 s54, s15, 4718592\n" \
"buffer_load_dwordx4 v33, s[40:43], s54 offen nt lds\n" \
"s_add_u32 m0, s62, 18432\n" \
"s_xor_b32 s54, s15, 4849664\n" \
"buffer_load_dwordx4 v32, s[40:43], s54 offen nt lds\n" \
"s_add_u32 m0, s62, 19456\n" \
"s_xor_b32 s54, s15, 4849664\n" \
"buffer_load_dwordx4 v33, s[40:43], s54 offen nt lds\n" \
"s_add_u32 m0, s62, 0\n" \
"s_xor_b32 s54, s15, 4980736\n" \
"buffer_load_dwordx4 v32, s[40:43], s54 offen nt lds\n" \
"s_add_u32 m0, s62, 1024\n" \
"s_xor_b32 s54, s15, 4980736\n" \
"buffer_load_dwordx4 v33, s[40:43], s54 offen nt lds\n" \
"s_add_u32 m0, s62, 2048\n" \
"s_xor_b32 s54, s15, 5111808\n" \
"buffer_load_dwordx4 v32, s[40:43], s54 offen nt lds\n" \
"s_add_u32 m0, s62, 3072\n" \
"s_xor_b32 s54, s15, 5111808\n" \
"buffer_load_dwordx4 v33, s[40:43], s54 offen nt lds\n" \
"s_waitcnt vmcnt(32)\n" \
"s_barrier\n" \
"ds_read_b64_tr_b16 a[16:17], v38 offset:16384\n" \
"ds_read_b64_tr_b16 a[18:19], v38 offset:16896\n" \
"ds_read_b64_tr_b16 a[24:25], v39 offset:16384\n" \
"ds_read_b64_tr_b16 a[26:27], v39 offset:16896\n" \
"ds_read_b64_tr_b16 v[112:113], v38 offset:24576\n" \
"ds_read_b64_tr_b16 v[114:115], v38 offset:25088\n" \
"ds_read_b64_tr_b16 v[120:121], v39 offset:24576\n" \
"ds_read_b64_tr_b16 v[122:123], v39 offset:25088\n" \
"ds_read_b64_tr_b16 a[20:21], v38 offset:0\n" \
"ds_read_b64_tr_b16 a[22:23], v38 offset:512\n" \
"ds_read_b64_tr_b16 a[28:29], v39 offset:0\n" \
"ds_read_b64_tr_b16 a[30:31], v39 offset:512\n" \
"ds_read_b64_tr_b16 v[116:117], v38 offset:8192\n" \
"ds_read_b64_tr_b16 v[118:119], v38 offset:8704\n" \
"ds_read_b64_tr_b16 v[124:125], v39 offset:8192\n" \
"ds_read_b64_tr_b16 v[126:127], v39 offset:8704\n" \
"s_waitcnt lgkmcnt(0)\n" \
"s_barrier\n" \
"s_add_u32 m0, s62, 147456\n" \
"s_xor_b32 s54, s15, 5767168\n" \
"buffer_load_dwordx4 v32, s[40:43], s54 offen nt lds\n" \
"s_add_u32 m0, s62, 148480\n" \
"s_xor_b32 s54, s15, 5767168\n" \
"buffer_load_dwordx4 v33, s[40:43], s54 offen nt lds\n" \
"s_add_u32 m0, s62, 149504\n" \
"s_xor_b32 s54, s15, 5898240\n" \
"buffer_load_dwordx4 v32, s[40:43], s54 offen nt lds\n" \
"s_add_u32 m0, s62, 150528\n" \
"s_xor_b32 s54, s15, 5898240\n" \
"buffer_load_dwordx4 v33, s[40:43], s54 offen nt lds\n" \
"s_add_u32 m0, s62, 131072\n" \
"s_xor_b32 s54, s15, 6029312\n" \
"buffer_load_dwordx4 v32, s[40:43], s54 offen nt lds\n" \
"s_add_u32 m0, s62, 132096\n" \
"s_xor_b32 s54, s15, 6029312\n" \
"buffer_load_dwordx4 v33, s[40:43], s54 offen nt lds\n" \
"s_add_u32 m0, s62, 133120\n" \
"s_xor_b32 s54, s15, 6160384\n" \
"buffer_load_dwordx4 v32, s[40:43], s54 offen nt lds\n" \
"s_add_u32 m0, s62, 134144\n" \
"s_xor_b32 s54, s15, 6160384\n" \
"buffer_load_dwordx4 v33, s[40:43], s54 offen nt lds\n" \
"s_waitcnt vmcnt(32)\n" \
"s_barrier\n" \
"ds_read_b64_tr_b16 a[48:49], v36 offset:49152\n" \
"ds_read_b64_tr_b16 a[50:51], v36 offset:49664\n" \
"ds_read_b64_tr_b16 a[56:57], v37 offset:49152\n" \
"ds_read_b64_tr_b16 a[58:59], v37 offset:49664\n" \
"ds_read_b64_tr_b16 v[128:129], v36 offset:57344\n" \
"ds_read_b64_tr_b16 v[130:131], v36 offset:57856\n" \
"ds_read_b64_tr_b16 v[136:137], v37 offset:57344\n" \
"ds_read_b64_tr_b16 v[138:139], v37 offset:57856\n" \
"ds_read_b64_tr_b16 a[52:53], v36 offset:32768\n" \
"ds_read_b64_tr_b16 a[54:55], v36 offset:33280\n" \
"ds_read_b64_tr_b16 a[60:61], v37 offset:32768\n" \
"ds_read_b64_tr_b16 a[62:63], v37 offset:33280\n" \
"ds_read_b64_tr_b16 v[132:133], v36 offset:40960\n" \
"ds_read_b64_tr_b16 v[134:135], v36 offset:41472\n" \
"ds_read_b64_tr_b16 v[140:141], v37 offset:40960\n" \
"ds_read_b64_tr_b16 v[142:143], v37 offset:41472\n" \
"s_waitcnt lgkmcnt(0)\n" \
"s_barrier\n" \
"s_add_u32 m0, s62, 114688\n" \
"s_xor_b32 s54, s15, 6815744\n" \
"buffer_load_dwordx4 v32, s[40:43], s54 offen nt lds\n" \
"s_add_u32 m0, s62, 115712\n" \
"s_xor_b32 s54, s15, 6815744\n" \
"buffer_load_dwordx4 v33, s[40:43], s54 offen nt lds\n" \
"s_add_u32 m0, s62, 116736\n" \
"s_xor_b32 s54, s15, 6946816\n" \
"buffer_load_dwordx4 v32, s[40:43], s54 offen nt lds\n" \
"s_add_u32 m0, s62, 117760\n" \
"s_xor_b32 s54, s15, 6946816\n" \
"buffer_load_dwordx4 v33, s[40:43], s54 offen nt lds\n" \
"s_add_u32 m0, s62, 98304\n" \
"s_xor_b32 s54, s15, 7077888\n" \
"buffer_load_dwordx4 v32, s[40:43], s54 offen nt lds\n" \
"s_add_u32 m0, s62, 99328\n" \
"s_xor_b32 s54, s15, 7077888\n" \
"buffer_load_dwordx4 v33, s[40:43], s54 offen nt lds\n" \
"s_add_u32 m0, s62, 100352\n" \
"s_xor_b32 s54, s15, 7208960\n" \
"buffer_load_dwordx4 v32, s[40:43], s54 offen nt lds\n" \
"s_add_u32 m0, s62, 101376\n" \
"s_xor_b32 s54, s15, 7208960\n" \
"buffer_load_dwordx4 v33, s[40:43], s54 offen nt lds\n" \
"s_waitcnt vmcnt(32)\n" \
"s_barrier\n" \
"ds_read_b64_tr_b16 a[80:81], v36 offset:16384\n" \
"ds_read_b64_tr_b16 a[82:83], v36 offset:16896\n" \
"ds_read_b64_tr_b16 a[88:89], v37 offset:16384\n" \
"ds_read_b64_tr_b16 a[90:91], v37 offset:16896\n" \
"ds_read_b64_tr_b16 v[144:145], v36 offset:24576\n" \
"ds_read_b64_tr_b16 v[146:147], v36 offset:25088\n" \
"ds_read_b64_tr_b16 v[152:153], v37 offset:24576\n" \
"ds_read_b64_tr_b16 v[154:155], v37 offset:25088\n" \
"ds_read_b64_tr_b16 a[84:85], v36 offset:0\n" \
"ds_read_b64_tr_b16 a[86:87], v36 offset:512\n" \
"ds_read_b64_tr_b16 a[92:93], v37 offset:0\n" \
"ds_read_b64_tr_b16 a[94:95], v37 offset:512\n" \
"ds_read_b64_tr_b16 v[148:149], v36 offset:8192\n" \
"ds_read_b64_tr_b16 v[150:151], v36 offset:8704\n" \
"ds_read_b64_tr_b16 v[156:157], v37 offset:8192\n" \
"ds_read_b64_tr_b16 v[158:159], v37 offset:8704\n" \
"s_waitcnt lgkmcnt(0)\n" \
"s_barrier\n" \
"s_add_u32 m0, s62, 81920\n" \
"s_xor_b32 s54, s15, 7340032\n" \
"buffer_load_dwordx4 v32, s[40:43], s54 offen nt lds\n" \
"s_add_u32 m0, s62, 82944\n" \
"s_xor_b32 s54, s15, 7340032\n" \
"buffer_load_dwordx4 v33, s[40:43], s54 offen nt lds\n" \
"s_add_u32 m0, s62, 83968\n" \
"s_xor_b32 s54, s15, 7471104\n" \
"buffer_load_dwordx4 v32, s[40:43], s54 offen nt lds\n" \
"s_add_u32 m0, s62, 84992\n" \
"s_xor_b32 s54, s15, 7471104\n" \
"buffer_load_dwordx4 v33, s[40:43], s54 offen nt lds\n" \
"s_add_u32 m0, s62, 65536\n" \
"s_xor_b32 s54, s15, 7602176\n" \
"buffer_load_dwordx4 v32, s[40:43], s54 offen nt lds\n" \
"s_add_u32 m0, s62, 66560\n" \
"s_xor_b32 s54, s15, 7602176\n" \
"buffer_load_dwordx4 v33, s[40:43], s54 offen nt lds\n" \
"s_add_u32 m0, s62, 67584\n" \
"s_xor_b32 s54, s15, 7733248\n" \
"buffer_load_dwordx4 v32, s[40:43], s54 offen nt lds\n" \
"s_add_u32 m0, s62, 68608\n" \
"s_xor_b32 s54, s15, 7733248\n" \
"buffer_load_dwordx4 v33, s[40:43], s54 offen nt lds\n" \
"s_waitcnt vmcnt(32)\n" \
"s_barrier\n" \
"ds_read_b64_tr_b16 a[112:113], v34 offset:49152\n" \
"ds_read_b64_tr_b16 a[114:115], v34 offset:49664\n" \
"ds_read_b64_tr_b16 a[120:121], v35 offset:49152\n" \
"ds_read_b64_tr_b16 a[122:123], v35 offset:49664\n" \
"ds_read_b64_tr_b16 v[160:161], v34 offset:57344\n" \
"ds_read_b64_tr_b16 v[162:163], v34 offset:57856\n" \
"ds_read_b64_tr_b16 v[168:169], v35 offset:57344\n" \
"ds_read_b64_tr_b16 v[170:171], v35 offset:57856\n" \
"ds_read_b64_tr_b16 a[116:117], v34 offset:32768\n" \
"ds_read_b64_tr_b16 a[118:119], v34 offset:33280\n" \
"ds_read_b64_tr_b16 a[124:125], v35 offset:32768\n" \
"ds_read_b64_tr_b16 a[126:127], v35 offset:33280\n" \
"ds_read_b64_tr_b16 v[164:165], v34 offset:40960\n" \
"ds_read_b64_tr_b16 v[166:167], v34 offset:41472\n" \
"ds_read_b64_tr_b16 v[172:173], v35 offset:40960\n" \
"ds_read_b64_tr_b16 v[174:175], v35 offset:41472\n" \
"s_waitcnt lgkmcnt(0)\n" \
"s_barrier\n" \
"s_add_u32 m0, s62, 49152\n" \
"s_xor_b32 s54, s15, 7864320\n" \
"buffer_load_dwordx4 v32, s[40:43], s54 offen nt lds\n" \
"s_add_u32 m0, s62, 50176\n" \
"s_xor_b32 s54, s15, 7864320\n" \
"buffer_load_dwordx4 v33, s[40:43], s54 offen nt lds\n" \
"s_add_u32 m0, s62, 51200\n" \
"s_xor_b32 s54, s15, 7995392\n" \
"buffer_load_dwordx4 v32, s[40:43], s54 offen nt lds\n" \
"s_add_u32 m0, s62, 52224\n" \
"s_xor_b32 s54, s15, 7995392\n" \
"buffer_load_dwordx4 v33, s[40:43], s54 offen nt lds\n" \
"s_add_u32 m0, s62, 32768\n" \
"s_xor_b32 s54, s15, 8126464\n" \
"buffer_load_dwordx4 v32, s[40:43], s54 offen nt lds\n" \
"s_add_u32 m0, s62, 33792\n" \
"s_xor_b32 s54, s15, 8126464\n" \
"buffer_load_dwordx4 v33, s[40:43], s54 offen nt lds\n" \
"s_add_u32 m0, s62, 34816\n" \
"s_xor_b32 s54, s15, 8257536\n" \
"buffer_load_dwordx4 v32, s[40:43], s54 offen nt lds\n" \
"s_add_u32 m0, s62, 35840\n" \
"s_xor_b32 s54, s15, 8257536\n" \
"buffer_load_dwordx4 v33, s[40:43], s54 offen nt lds\n" \
"s_waitcnt vmcnt(32)\n" \
"s_barrier\n" \
"ds_read_b64_tr_b16 a[144:145], v34 offset:16384\n" \
"ds_read_b64_tr_b16 a[146:147], v34 offset:16896\n" \
"ds_read_b64_tr_b16 a[152:153], v35 offset:16384\n" \
"ds_read_b64_tr_b16 a[154:155], v35 offset:16896\n" \
"ds_read_b64_tr_b16 v[176:177], v34 offset:24576\n" \
"ds_read_b64_tr_b16 v[178:179], v34 offset:25088\n" \
"ds_read_b64_tr_b16 v[184:185], v35 offset:24576\n" \
"ds_read_b64_tr_b16 v[186:187], v35 offset:25088\n" \
"ds_read_b64_tr_b16 a[148:149], v34 offset:0\n" \
"ds_read_b64_tr_b16 a[150:151], v34 offset:512\n" \
"ds_read_b64_tr_b16 a[156:157], v35 offset:0\n" \
"ds_read_b64_tr_b16 a[158:159], v35 offset:512\n" \
"ds_read_b64_tr_b16 v[180:181], v34 offset:8192\n" \
"ds_read_b64_tr_b16 v[182:183], v34 offset:8704\n" \
"ds_read_b64_tr_b16 v[188:189], v35 offset:8192\n" \
"ds_read_b64_tr_b16 v[190:191], v35 offset:8704\n" \
"s_waitcnt lgkmcnt(0)\n" \
"s_barrier\n" \
"s_add_u32 m0, s62, 0\n" \
"s_xor_b32 s54, s15, 0\n" \
"buffer_load_dwordx4 v32, s[40:43], s54 offen nt lds\n" \
"s_add_u32 m0, s62, 1024\n" \
"s_xor_b32 s54, s15, 0\n" \
"buffer_load_dwordx4 v33, s[40:43], s54 offen nt lds\n" \
"s_add_u32 m0, s62, 2048\n" \
"s_xor_b32 s54, s15, 131072\n" \
"buffer_load_dwordx4 v32, s[40:43], s54 offen nt lds\n" \
"s_add_u32 m0, s62, 3072\n" \
"s_xor_b32 s54, s15, 131072\n" \
"buffer_load_dwordx4 v33, s[40:43], s54 offen nt lds\n" \
"s_add_u32 m0, s62, 16384\n" \
"s_xor_b32 s54, s15, 262144\n" \
"buffer_load_dwordx4 v32, s[40:43], s54 offen nt lds\n" \
"s_add_u32 m0, s62, 17408\n" \
"s_xor_b32 s54, s15, 262144\n" \
"buffer_load_dwordx4 v33, s[40:43], s54 offen nt lds\n" \
"s_add_u32 m0, s62, 18432\n" \
"s_xor_b32 s54, s15, 393216\n" \
"buffer_load_dwordx4 v32, s[40:43], s54 offen nt lds\n" \
"s_add_u32 m0, s62, 19456\n" \
"s_xor_b32 s54, s15, 393216\n" \
"buffer_load_dwordx4 v33, s[40:43], s54 offen nt lds\n" \
"s_waitcnt vmcnt(32)\n" \
"s_barrier\n" \
"ds_read_b64_tr_b16 a[176:177], v38 offset:16384\n" \
"ds_read_b64_tr_b16 a[178:179], v38 offset:16896\n" \
"ds_read_b64_tr_b16 a[184:185], v39 offset:16384\n" \
"ds_read_b64_tr_b16 a[186:187], v39 offset:16896\n" \
"ds_read_b64_tr_b16 v[192:193], v38 offset:24576\n" \
"ds_read_b64_tr_b16 v[194:195], v38 offset:25088\n" \
"ds_read_b64_tr_b16 v[200:201], v39 offset:24576\n" \
"ds_read_b64_tr_b16 v[202:203], v39 offset:25088\n" \
"ds_read_b64_tr_b16 a[180:181], v38 offset:0\n" \
"ds_read_b64_tr_b16 a[182:183], v38 offset:512\n" \
"ds_read_b64_tr_b16 a[188:189], v39 offset:0\n" \
"ds_read_b64_tr_b16 a[190:191], v39 offset:512\n" \
"ds_read_b64_tr_b16 v[196:197], v38 offset:8192\n" \
"ds_read_b64_tr_b16 v[198:199], v38 offset:8704\n" \
"ds_read_b64_tr_b16 v[204:205], v39 offset:8192\n" \
"ds_read_b64_tr_b16 v[206:207], v39 offset:8704\n" \
"s_waitcnt lgkmcnt(0)\n" \
"s_barrier\n" \
"s_waitcnt vmcnt(24)\n" \
"s_barrier\n" \
"ds_read_b64_tr_b16 a[208:209], v36 offset:49152\n" \
"ds_read_b64_tr_b16 a[210:211], v36 offset:49664\n" \
"ds_read_b64_tr_b16 a[216:217], v37 offset:49152\n" \
"ds_read_b64_tr_b16 a[218:219], v37 offset:49664\n" \
"ds_read_b64_tr_b16 v[208:209], v36 offset:57344\n" \
"ds_read_b64_tr_b16 v[210:211], v36 offset:57856\n" \
"ds_read_b64_tr_b16 v[216:217], v37 offset:57344\n" \
"ds_read_b64_tr_b16 v[218:219], v37 offset:57856\n" \
"ds_read_b64_tr_b16 a[212:213], v36 offset:32768\n" \
"ds_read_b64_tr_b16 a[214:215], v36 offset:33280\n" \
"ds_read_b64_tr_b16 a[220:221], v37 offset:32768\n" \
"ds_read_b64_tr_b16 a[222:223], v37 offset:33280\n" \
"ds_read_b64_tr_b16 v[212:213], v36 offset:40960\n" \
"ds_read_b64_tr_b16 v[214:215], v36 offset:41472\n" \
"ds_read_b64_tr_b16 v[220:221], v37 offset:40960\n" \
"ds_read_b64_tr_b16 v[222:223], v37 offset:41472\n" \
"s_waitcnt lgkmcnt(0)\n" \
"s_barrier\n" \
"s_add_u32 m0, s62, 98304\n" \
"s_xor_b32 s54, s15, 1048576\n" \
"buffer_load_dwordx4 v32, s[40:43], s54 offen nt lds\n" \
"s_add_u32 m0, s62, 99328\n" \
"s_xor_b32 s54, s15, 1048576\n" \
"buffer_load_dwordx4 v33, s[40:43], s54 offen nt lds\n" \
"s_add_u32 m0, s62, 100352\n" \
"s_xor_b32 s54, s15, 1179648\n" \
"buffer_load_dwordx4 v32, s[40:43], s54 offen nt lds\n" \
"s_add_u32 m0, s62, 101376\n" \
"s_xor_b32 s54, s15, 1179648\n" \
"buffer_load_dwordx4 v33, s[40:43], s54 offen nt lds\n" \
"s_add_u32 m0, s62, 147456\n" \
"s_xor_b32 s54, s15, 1310720\n" \
"buffer_load_dwordx4 v32, s[40:43], s54 offen nt lds\n" \
"s_add_u32 m0, s62, 148480\n" \
"s_xor_b32 s54, s15, 1310720\n" \
"buffer_load_dwordx4 v33, s[40:43], s54 offen nt lds\n" \
"s_add_u32 m0, s62, 149504\n" \
"s_xor_b32 s54, s15, 1441792\n" \
"buffer_load_dwordx4 v32, s[40:43], s54 offen nt lds\n" \
"s_add_u32 m0, s62, 150528\n" \
"s_xor_b32 s54, s15, 1441792\n" \
"buffer_load_dwordx4 v33, s[40:43], s54 offen nt lds\n" \
"s_waitcnt vmcnt(24)\n" \
"s_barrier\n" \
"ds_read_b64_tr_b16 a[224:225], v36 offset:16384\n" \
"ds_read_b64_tr_b16 a[226:227], v36 offset:16896\n" \
"ds_read_b64_tr_b16 a[232:233], v37 offset:16384\n" \
"ds_read_b64_tr_b16 a[234:235], v37 offset:16896\n" \
"ds_read_b64_tr_b16 v[224:225], v36 offset:24576\n" \
"ds_read_b64_tr_b16 v[226:227], v36 offset:25088\n" \
"ds_read_b64_tr_b16 v[232:233], v37 offset:24576\n" \
"ds_read_b64_tr_b16 v[234:235], v37 offset:25088\n" \
"ds_read_b64_tr_b16 a[228:229], v36 offset:0\n" \
"ds_read_b64_tr_b16 a[230:231], v36 offset:512\n" \
"ds_read_b64_tr_b16 a[236:237], v37 offset:0\n" \
"ds_read_b64_tr_b16 a[238:239], v37 offset:512\n" \
"ds_read_b64_tr_b16 v[228:229], v36 offset:8192\n" \
"ds_read_b64_tr_b16 v[230:231], v36 offset:8704\n" \
"ds_read_b64_tr_b16 v[236:237], v37 offset:8192\n" \
"ds_read_b64_tr_b16 v[238:239], v37 offset:8704\n" \
"s_waitcnt lgkmcnt(0)\n" \
"s_barrier\n" \
"s_add_u32 m0, s62, 65536\n" \
"s_xor_b32 s54, s15, 2097152\n" \
"buffer_load_dwordx4 v32, s[40:43], s54 offen nt lds\n" \
"s_add_u32 m0, s62, 66560\n" \
"s_xor_b32 s54, s15, 2097152\n" \
"buffer_load_dwordx4 v33, s[40:43], s54 offen nt lds\n" \
"s_add_u32 m0, s62, 67584\n" \
"s_xor_b32 s54, s15, 2228224\n" \
"buffer_load_dwordx4 v32, s[40:43], s54 offen nt lds\n" \
"s_add_u32 m0, s62, 68608\n" \
"s_xor_b32 s54, s15, 2228224\n" \
"buffer_load_dwordx4 v33, s[40:43], s54 offen nt lds\n" \
"s_add_u32 m0, s62, 131072\n" \
"s_xor_b32 s54, s15, 2359296\n" \
"buffer_load_dwordx4 v32, s[40:43], s54 offen nt lds\n" \
"s_add_u32 m0, s62, 132096\n" \
"s_xor_b32 s54, s15, 2359296\n" \
"buffer_load_dwordx4 v33, s[40:43], s54 offen nt lds\n" \
"s_add_u32 m0, s62, 133120\n" \
"s_xor_b32 s54, s15, 2490368\n" \
"buffer_load_dwordx4 v32, s[40:43], s54 offen nt lds\n" \
"s_add_u32 m0, s62, 134144\n" \
"s_xor_b32 s54, s15, 2490368\n" \
"buffer_load_dwordx4 v33, s[40:43], s54 offen nt lds\n" \
"s_add_u32 m0, s62, 81920\n" \
"s_xor_b32 s54, s15, 3145728\n" \
"buffer_load_dwordx4 v32, s[40:43], s54 offen nt lds\n" \
"s_add_u32 m0, s62, 82944\n" \
"s_xor_b32 s54, s15, 3145728\n" \
"buffer_load_dwordx4 v33, s[40:43], s54 offen nt lds\n" \
"s_add_u32 m0, s62, 83968\n" \
"s_xor_b32 s54, s15, 3276800\n" \
"buffer_load_dwordx4 v32, s[40:43], s54 offen nt lds\n" \
"s_add_u32 m0, s62, 84992\n" \
"s_xor_b32 s54, s15, 3276800\n" \
"buffer_load_dwordx4 v33, s[40:43], s54 offen nt lds\n" \
"s_add_u32 m0, s62, 114688\n" \
"s_xor_b32 s54, s15, 3407872\n" \
"buffer_load_dwordx4 v32, s[40:43], s54 offen nt lds\n" \
"s_add_u32 m0, s62, 115712\n" \
"s_xor_b32 s54, s15, 3407872\n" \
"buffer_load_dwordx4 v33, s[40:43], s54 offen nt lds\n" \
"s_add_u32 m0, s62, 116736\n" \
"s_xor_b32 s54, s15, 3538944\n" \
"buffer_load_dwordx4 v32, s[40:43], s54 offen nt lds\n" \
"s_add_u32 m0, s62, 117760\n" \
"s_xor_b32 s54, s15, 3538944\n" \
"buffer_load_dwordx4 v33, s[40:43], s54 offen nt lds\n" \
"s_waitcnt vmcnt(32)\n" \
"s_barrier\n" \
"ds_read_b64_tr_b16 a[240:241], v34 offset:49152\n" \
"ds_read_b64_tr_b16 a[242:243], v34 offset:49664\n" \
"ds_read_b64_tr_b16 a[248:249], v35 offset:49152\n" \
"ds_read_b64_tr_b16 a[250:251], v35 offset:49664\n" \
"ds_read_b64_tr_b16 v[240:241], v34 offset:57344\n" \
"ds_read_b64_tr_b16 v[242:243], v34 offset:57856\n" \
"ds_read_b64_tr_b16 v[248:249], v35 offset:57344\n" \
"ds_read_b64_tr_b16 v[250:251], v35 offset:57856\n" \
"ds_read_b64_tr_b16 a[244:245], v34 offset:32768\n" \
"ds_read_b64_tr_b16 a[246:247], v34 offset:33280\n" \
"ds_read_b64_tr_b16 a[252:253], v35 offset:32768\n" \
"ds_read_b64_tr_b16 a[254:255], v35 offset:33280\n" \
"ds_read_b64_tr_b16 v[244:245], v34 offset:40960\n" \
"ds_read_b64_tr_b16 v[246:247], v34 offset:41472\n" \
"ds_read_b64_tr_b16 v[252:253], v35 offset:40960\n" \
"ds_read_b64_tr_b16 v[254:255], v35 offset:41472\n" \
"s_waitcnt lgkmcnt(0)\n" \
"s_barrier\n" \
"s_add_u32 m0, s62, 32768\n" \
"s_xor_b32 s54, s15, 4194304\n" \
"buffer_load_dwordx4 v32, s[40:43], s54 offen nt lds\n" \
"s_add_u32 m0, s62, 33792\n" \
"s_xor_b32 s54, s15, 4194304\n" \
"buffer_load_dwordx4 v33, s[40:43], s54 offen nt lds\n" \
"s_add_u32 m0, s62, 34816\n" \
"s_xor_b32 s54, s15, 4325376\n" \
"buffer_load_dwordx4 v32, s[40:43], s54 offen nt lds\n" \
"s_add_u32 m0, s62, 35840\n" \
"s_xor_b32 s54, s15, 4325376\n" \
"buffer_load_dwordx4 v33, s[40:43], s54 offen nt lds\n" \
"s_add_u32 m0, s62, 49152\n" \
"s_xor_b32 s54, s15, 4456448\n" \
"buffer_load_dwordx4 v32, s[40:43], s54 offen nt lds\n" \
"s_add_u32 m0, s62, 50176\n" \
"s_xor_b32 s54, s15, 4456448\n" \
"buffer_load_dwordx4 v33, s[40:43], s54 offen nt lds\n" \
"s_add_u32 m0, s62, 51200\n" \
"s_xor_b32 s54, s15, 4587520\n" \
"buffer_load_dwordx4 v32, s[40:43], s54 offen nt lds\n" \
"s_add_u32 m0, s62, 52224\n" \
"s_xor_b32 s54, s15, 4587520\n" \
"buffer_load_dwordx4 v33, s[40:43], s54 offen nt lds\n" \
"s_waitcnt vmcnt(32)\n" \
"s_barrier\n" \
"ds_read_b64_tr_b16 a[0:1], v34 offset:0\n" \
"ds_read_b64_tr_b16 a[2:3], v34 offset:512\n" \
"ds_read_b64_tr_b16 a[8:9], v35 offset:0\n" \
"ds_read_b64_tr_b16 a[10:11], v35 offset:512\n" \
"ds_read_b64_tr_b16 v[48:49], v34 offset:8192\n" \
"ds_read_b64_tr_b16 v[50:51], v34 offset:8704\n" \
"ds_read_b64_tr_b16 v[56:57], v35 offset:8192\n" \
"ds_read_b64_tr_b16 v[58:59], v35 offset:8704\n" \
"ds_read_b64_tr_b16 a[4:5], v34 offset:16384\n" \
"ds_read_b64_tr_b16 a[6:7], v34 offset:16896\n" \
"ds_read_b64_tr_b16 a[12:13], v35 offset:16384\n" \
"ds_read_b64_tr_b16 a[14:15], v35 offset:16896\n" \
"ds_read_b64_tr_b16 v[52:53], v34 offset:24576\n" \
"ds_read_b64_tr_b16 v[54:55], v34 offset:25088\n" \
"ds_read_b64_tr_b16 v[60:61], v35 offset:24576\n" \
"ds_read_b64_tr_b16 v[62:63], v35 offset:25088\n" \
"s_waitcnt lgkmcnt(0)\n" \
"s_barrier\n" \
"ds_write_b128 v1, v[48:51] offset:0\n" \
"ds_write_b128 v1, v[52:55] offset:1024\n" \
"ds_write_b128 v1, v[56:59] offset:2048\n" \
"ds_write_b128 v1, v[60:63] offset:3072\n" \
"s_add_u32 m0, s62, 16384\n" \
"s_xor_b32 s54, s15, 5242880\n" \
"buffer_load_dwordx4 v32, s[40:43], s54 offen nt lds\n" \
"s_add_u32 m0, s62, 17408\n" \
"s_xor_b32 s54, s15, 5242880\n" \
"buffer_load_dwordx4 v33, s[40:43], s54 offen nt lds\n" \
"s_add_u32 m0, s62, 18432\n" \
"s_xor_b32 s54, s15, 5373952\n" \
"buffer_load_dwordx4 v32, s[40:43], s54 offen nt lds\n" \
"s_add_u32 m0, s62, 19456\n" \
"s_xor_b32 s54, s15, 5373952\n" \
"buffer_load_dwordx4 v33, s[40:43], s54 offen nt lds\n" \
"s_waitcnt vmcnt(28)\n" \
"s_barrier\n" \
"ds_read_b64_tr_b16 a[32:33], v36 offset:32768\n" \
"ds_read_b64_tr_b16 a[34:35], v36 offset:33280\n" \
"ds_read_b64_tr_b16 a[40:41], v37 offset:32768\n" \
"ds_read_b64_tr_b16 a[42:43], v37 offset:33280\n" \
"ds_read_b64_tr_b16 v[48:49], v36 offset:40960\n" \
"ds_read_b64_tr_b16 v[50:51], v36 offset:41472\n" \
"ds_read_b64_tr_b16 v[56:57], v37 offset:40960\n" \
"ds_read_b64_tr_b16 v[58:59], v37 offset:41472\n" \
"ds_read_b64_tr_b16 a[36:37], v38 offset:16384\n" \
"ds_read_b64_tr_b16 a[38:39], v38 offset:16896\n" \
"ds_read_b64_tr_b16 a[44:45], v39 offset:16384\n" \
"ds_read_b64_tr_b16 a[46:47], v39 offset:16896\n" \
"ds_read_b64_tr_b16 v[52:53], v38 offset:24576\n" \
"ds_read_b64_tr_b16 v[54:55], v38 offset:25088\n" \
"ds_read_b64_tr_b16 v[60:61], v39 offset:24576\n" \
"ds_read_b64_tr_b16 v[62:63], v39 offset:25088\n" \
"s_waitcnt lgkmcnt(0)\n" \
"s_barrier\n" \
"ds_write_b128 v2, v[48:51] offset:32768\n" \
"ds_write_b128 v2, v[52:55] offset:33792\n" \
"ds_write_b128 v2, v[56:59] offset:34816\n" \
"ds_write_b128 v2, v[60:63] offset:35840\n" \
"s_add_u32 m0, s62, 147456\n" \
"s_xor_b32 s54, s15, 5505024\n" \
"buffer_load_dwordx4 v32, s[40:43], s54 offen nt lds\n" \
"s_add_u32 m0, s62, 148480\n" \
"s_xor_b32 s54, s15, 5505024\n" \
"buffer_load_dwordx4 v33, s[40:43], s54 offen nt lds\n" \
"s_add_u32 m0, s62, 149504\n" \
"s_xor_b32 s54, s15, 5636096\n" \
"buffer_load_dwordx4 v32, s[40:43], s54 offen nt lds\n" \
"s_add_u32 m0, s62, 150528\n" \
"s_xor_b32 s54, s15, 5636096\n" \
"buffer_load_dwordx4 v33, s[40:43], s54 offen nt lds\n" \
"s_waitcnt vmcnt(24)\n" \
"s_barrier\n" \
"ds_read_b64_tr_b16 a[64:65], v36 offset:0\n" \
"ds_read_b64_tr_b16 a[66:67], v36 offset:512\n" \
"ds_read_b64_tr_b16 a[72:73], v37 offset:0\n" \
"ds_read_b64_tr_b16 a[74:75], v37 offset:512\n" \
"ds_read_b64_tr_b16 v[48:49], v36 offset:8192\n" \
"ds_read_b64_tr_b16 v[50:51], v36 offset:8704\n" \
"ds_read_b64_tr_b16 v[56:57], v37 offset:8192\n" \
"ds_read_b64_tr_b16 v[58:59], v37 offset:8704\n" \
"ds_read_b64_tr_b16 a[68:69], v38 offset:0\n" \
"ds_read_b64_tr_b16 a[70:71], v38 offset:512\n" \
"ds_read_b64_tr_b16 a[76:77], v39 offset:0\n" \
"ds_read_b64_tr_b16 a[78:79], v39 offset:512\n" \
"ds_read_b64_tr_b16 v[52:53], v38 offset:8192\n" \
"ds_read_b64_tr_b16 v[54:55], v38 offset:8704\n" \
"ds_read_b64_tr_b16 v[60:61], v39 offset:8192\n" \
"ds_read_b64_tr_b16 v[62:63], v39 offset:8704\n" \
"s_waitcnt lgkmcnt(0)\n" \
"s_barrier\n" \
"ds_write_b128 v2, v[48:51] offset:0\n" \
"ds_write_b128 v2, v[52:55] offset:1024\n" \
"ds_write_b128 v2, v[56:59] offset:2048\n" \
"ds_write_b128 v2, v[60:63] offset:3072\n" \
"s_waitcnt vmcnt(16)\n" \
"s_barrier\n" \
"ds_read_b64_tr_b16 a[96:97], v36 offset:16384\n" \
"ds_read_b64_tr_b16 a[98:99], v36 offset:16896\n" \
"ds_read_b64_tr_b16 a[104:105], v37 offset:16384\n" \
"ds_read_b64_tr_b16 a[106:107], v37 offset:16896\n" \
"ds_read_b64_tr_b16 v[48:49], v36 offset:24576\n" \
"ds_read_b64_tr_b16 v[50:51], v36 offset:25088\n" \
"ds_read_b64_tr_b16 v[56:57], v37 offset:24576\n" \
"ds_read_b64_tr_b16 v[58:59], v37 offset:25088\n" \
"ds_read_b64_tr_b16 a[100:101], v36 offset:49152\n" \
"ds_read_b64_tr_b16 a[102:103], v36 offset:49664\n" \
"ds_read_b64_tr_b16 a[108:109], v37 offset:49152\n" \
"ds_read_b64_tr_b16 a[110:111], v37 offset:49664\n" \
"ds_read_b64_tr_b16 v[52:53], v36 offset:57344\n" \
"ds_read_b64_tr_b16 v[54:55], v36 offset:57856\n" \
"ds_read_b64_tr_b16 v[60:61], v37 offset:57344\n" \
"ds_read_b64_tr_b16 v[62:63], v37 offset:57856\n" \
"s_waitcnt lgkmcnt(0)\n" \
"s_barrier\n" \
"ds_write_b128 v2, v[48:51] offset:16384\n" \
"ds_write_b128 v2, v[52:55] offset:17408\n" \
"ds_write_b128 v2, v[56:59] offset:18432\n" \
"ds_write_b128 v2, v[60:63] offset:19456\n" \
"s_waitcnt vmcnt(8)\n" \
"s_barrier\n" \
"ds_read_b64_tr_b16 a[128:129], v34 offset:32768\n" \
"ds_read_b64_tr_b16 a[130:131], v34 offset:33280\n" \
"ds_read_b64_tr_b16 a[136:137], v35 offset:32768\n" \
"ds_read_b64_tr_b16 a[138:139], v35 offset:33280\n" \
"ds_read_b64_tr_b16 v[48:49], v34 offset:40960\n" \
"ds_read_b64_tr_b16 v[50:51], v34 offset:41472\n" \
"ds_read_b64_tr_b16 v[56:57], v35 offset:40960\n" \
"ds_read_b64_tr_b16 v[58:59], v35 offset:41472\n" \
"ds_read_b64_tr_b16 a[132:133], v34 offset:49152\n" \
"ds_read_b64_tr_b16 a[134:135], v34 offset:49664\n" \
"ds_read_b64_tr_b16 a[140:141], v35 offset:49152\n" \
"ds_read_b64_tr_b16 a[142:143], v35 offset:49664\n" \
"ds_read_b64_tr_b16 v[52:53], v34 offset:57344\n" \
"ds_read_b64_tr_b16 v[54:55], v34 offset:57856\n" \
"ds_read_b64_tr_b16 v[60:61], v35 offset:57344\n" \
"ds_read_b64_tr_b16 v[62:63], v35 offset:57856\n" \
"s_waitcnt lgkmcnt(0)\n" \
"s_barrier\n" \
"ds_write_b128 v1, v[48:51] offset:32768\n" \
"ds_write_b128 v1, v[52:55] offset:33792\n" \
"ds_write_b128 v1, v[56:59] offset:34816\n" \
"ds_write_b128 v1, v[60:63] offset:35840\n" \
"s_add_u32 m0, s62, 49152\n" \
"s_xor_b32 s54, s15, 6291456\n" \
"buffer_load_dwordx4 v32, s[40:43], s54 offen nt lds\n" \
"s_add_u32 m0, s62, 50176\n" \
"s_xor_b32 s54, s15, 6291456\n" \
"buffer_load_dwordx4 v33, s[40:43], s54 offen nt lds\n" \
"s_add_u32 m0, s62, 51200\n" \
"s_xor_b32 s54, s15, 6422528\n" \
"buffer_load_dwordx4 v32, s[40:43], s54 offen nt lds\n" \
"s_add_u32 m0, s62, 52224\n" \
"s_xor_b32 s54, s15, 6422528\n" \
"buffer_load_dwordx4 v33, s[40:43], s54 offen nt lds\n" \
"s_add_u32 m0, s62, 131072\n" \
"s_xor_b32 s54, s15, 6553600\n" \
"buffer_load_dwordx4 v32, s[40:43], s54 offen nt lds\n" \
"s_add_u32 m0, s62, 132096\n" \
"s_xor_b32 s54, s15, 6553600\n" \
"buffer_load_dwordx4 v33, s[40:43], s54 offen nt lds\n" \
"s_add_u32 m0, s62, 133120\n" \
"s_xor_b32 s54, s15, 6684672\n" \
"buffer_load_dwordx4 v32, s[40:43], s54 offen nt lds\n" \
"s_add_u32 m0, s62, 134144\n" \
"s_xor_b32 s54, s15, 6684672\n" \
"buffer_load_dwordx4 v33, s[40:43], s54 offen nt lds\n" \
"s_waitcnt vmcnt(8)\n" \
"s_barrier\n" \
"ds_read_b64_tr_b16 a[160:161], v34 offset:16384\n" \
"ds_read_b64_tr_b16 a[162:163], v34 offset:16896\n" \
"ds_read_b64_tr_b16 a[168:169], v35 offset:16384\n" \
"ds_read_b64_tr_b16 a[170:171], v35 offset:16896\n" \
"ds_read_b64_tr_b16 v[48:49], v34 offset:24576\n" \
"ds_read_b64_tr_b16 v[50:51], v34 offset:25088\n" \
"ds_read_b64_tr_b16 v[56:57], v35 offset:24576\n" \
"ds_read_b64_tr_b16 v[58:59], v35 offset:25088\n" \
"ds_read_b64_tr_b16 a[164:165], v38 offset:16384\n" \
"ds_read_b64_tr_b16 a[166:167], v38 offset:16896\n" \
"ds_read_b64_tr_b16 a[172:173], v39 offset:16384\n" \
"ds_read_b64_tr_b16 a[174:175], v39 offset:16896\n" \
"ds_read_b64_tr_b16 v[52:53], v38 offset:24576\n" \
"ds_read_b64_tr_b16 v[54:55], v38 offset:25088\n" \
"ds_read_b64_tr_b16 v[60:61], v39 offset:24576\n" \
"ds_read_b64_tr_b16 v[62:63], v39 offset:25088\n" \
"s_waitcnt lgkmcnt(0)\n" \
"s_barrier\n" \
"ds_write_b128 v1, v[48:51] offset:16384\n" \
"ds_write_b128 v1, v[52:55] offset:17408\n" \
"ds_write_b128 v1, v[56:59] offset:18432\n" \
"ds_write_b128 v1, v[60:63] offset:19456\n" \
"s_waitcnt vmcnt(0)\n" \
"s_barrier\n" \
"ds_read_b64_tr_b16 a[192:193], v34 offset:49152\n" \
"ds_read_b64_tr_b16 a[194:195], v34 offset:49664\n" \
"ds_read_b64_tr_b16 a[200:201], v35 offset:49152\n" \
"ds_read_b64_tr_b16 a[202:203], v35 offset:49664\n" \
"ds_read_b64_tr_b16 v[48:49], v34 offset:57344\n" \
"ds_read_b64_tr_b16 v[50:51], v34 offset:57856\n" \
"ds_read_b64_tr_b16 v[56:57], v35 offset:57344\n" \
"ds_read_b64_tr_b16 v[58:59], v35 offset:57856\n" \
"ds_read_b64_tr_b16 a[196:197], v38 offset:0\n" \
"ds_read_b64_tr_b16 a[198:199], v38 offset:512\n" \
"ds_read_b64_tr_b16 a[204:205], v39 offset:0\n" \
"ds_read_b64_tr_b16 a[206:207], v39 offset:512\n" \
"ds_read_b64_tr_b16 v[52:53], v38 offset:8192\n" \
"ds_read_b64_tr_b16 v[54:55], v38 offset:8704\n" \
"ds_read_b64_tr_b16 v[60:61], v39 offset:8192\n" \
"ds_read_b64_tr_b16 v[62:63], v39 offset:8704\n" \
"s_waitcnt lgkmcnt(0)\n" \
"s_barrier\n" \
"ds_write_b128 v1, v[48:51] offset:49152\n" \
"ds_write_b128 v1, v[52:55] offset:50176\n" \
"ds_write_b128 v1, v[56:59] offset:51200\n" \
"ds_write_b128 v1, v[60:63] offset:52224\n" \
"s_waitcnt vmcnt(0) lgkmcnt(0)\n" \
"s_barrier\n" \
"s_getpc_b64 s[18:19]\n" \
".Lq_pc_%=:\n" \
"v_mbcnt_lo_u32_b32 v40, -1, 0\n" \
"v_mbcnt_hi_u32_b32 v40, -1, v40\n" \
"v_lshl_add_u32 v40, s48, 6, v40\n" \
"v_lshlrev_b32 v40, 6, v40\n" \
"v_min_u32 v40, .Lq_fin_%=-.Lq_pc_%=, v40\n" \
"global_load_dword v41, v40, s[18:19]\n" \
"s_lshl_b32 s62, s48, 11\n" \
"s_mov_b32 s21, s12\n" \
"s_add_u32 s12, s63, s62\n" \
"s_mov_b32 s49, 0\n" \
"s_mov_b32 s52, 0\n" \
"s_mov_b32 s81, 0\n" \
"s_mov_b32 s88, 0\n" \
"s_mov_b32 s95, 0x7ffffff0\n" \
"s_mov_b32 s98, 0x3e0293ee\n" \
"s_mov_b32 s99, 0x3e0293ee\n" \
"v_mov_b32 v20, 0x3f803f80\n" \
"v_mov_b32 v21, 0x3f803f80\n" \
"v_mov_b32 v22, 0x3f803f80\n" \
"v_mov_b32 v23, 0x3f803f80\n" \
"v_mov_b32 v24, 0\n" \
"v_mov_b32 v25, 0\n" \
"v_mov_b32 v26, 0\n" \
"v_mov_b32 v27, 0\n" \
"v_mov_b32 v28, 0\n" \
"v_mov_b32 v29, 0\n" \
"v_mov_b32 v104, 0\n" \
"v_mov_b32 v105, 0\n" \
"v_mov_b32 v106, 0\n" \
"v_mov_b32 v107, 0\n" \
"v_mov_b32 v108, 0\n" \
"v_mov_b32 v109, 0\n" \
"v_mov_b32 v110, 0\n" \
"v_mov_b32 v111, 0\n" \
"v_cmp_le_i32_e64 s[64:65], 0, v14\n" \
"v_cmp_le_i32_e64 s[66:67], 1, v14\n" \
"v_cmp_le_i32_e64 s[68:69], 2, v14\n" \
"v_cmp_le_i32_e64 s[70:71], 3, v14\n" \
"v_cmp_le_i32_e64 s[72:73], 16, v14\n" \
"v_cmp_le_i32_e64 s[74:75], 17, v14\n" \
"v_cmp_le_i32_e64 s[76:77], 18, v14\n" \
"v_cmp_le_i32_e64 s[78:79], 19, v14\n" \
"s_mov_b32 s82, -1\n" \
"s_mov_b32 s83, 0\n" \
"s_mov_b32 s6, 0x33333333\n" \
"s_mov_b32 s7, 0x33333333\n" \
"s_mov_b32 s8, 0x53800000\n" \
"s_mov_b32 s41, s10\n" \
"s_mov_b32 s22, 0xfffffc00\n" \
"s_mov_b32 s23, -1\n" \
"s_mul_i32 s32, s46, 1\n" \
"s_mov_b32 s33, s32\n" \
"s_mov_b32 s34, s46\n" \
"s_add_u32 s35, s17, s13\n" \
"s_mov_b32 s10, s14\n" \
"s_mov_b32 s14, s21\n" \
"s_lshl_b32 s42, s48, 10\n" \
"s_add_u32 s42, s42, 114688\n" \
"s_and_b32 s43, s48, 1\n" \
"s_lshl_b32 s43, s43, 12\n" \
"s_mov_b32 s54, 0\n" \
"buffer_store_dwordx4 v[24:27], v7, s[36:39], s54 offen sc1\n" \
"s_mov_b32 s54, 131072\n" \
"buffer_store_dwordx4 v[24:27], v7, s[36:39], s54 offen sc1\n" \
"s_add_u32 s54, s94, 65536\n" \
"buffer_store_dword v24, v10, s[36:39], s54 offen sc1\n" \
"s_mov_b32 s54, 262144\n" \
"buffer_store_dwordx4 v[24:27], v7, s[36:39], s54 offen sc1\n" \
"s_add_u32 s54, s94, 131072\n" \
"buffer_store_dword v24, v10, s[36:39], s54 offen sc1\n" \
"s_mov_b32 s54, 393216\n" \
"buffer_store_dwordx4 v[24:27], v7, s[36:39], s54 offen sc1\n" \
"s_add_u32 s54, s94, 196608\n" \
"buffer_store_dword v24, v10, s[36:39], s54 offen sc1\n" \
"s_cmp_eq_u32 s48, 0\n" \
"s_cselect_b32 s9, 7, 0\n" \
"buffer_load_dword v30, v15, s[36:39], s80 offen sc1\n" \
"s_waitcnt vmcnt(0)\n" \
"v_cmp_ge_u32_e64 s[96:97], s81, v30\n" \
"s_nop 4\n" \
"s_and_b32 s96, s96, s9\n" \
"s_cbranch_scc1 .Lq_f0_cold_%=\n" \
".Lq_f0_ok_%=:\n" \
"s_barrier\n" \
"s_add_u32 m0, s62, 131072\n" \
"s_sub_u32 s15, s12, 114688\n" \
"buffer_load_dwordx4 v3, s[36:39], s15 offen offset:0 sc1 lds\n" \
"buffer_load_dwordx4 v3, s[36:39], s15 offen offset:1024 sc1 lds\n" \
"s_add_u32 s54, s49, 0\n" \
"s_and_b32 s54, s54, 3\n" \
"s_mul_i32 s58, s54, 131072\n" \
"s_add_u32 s54, s49, 1\n" \
"s_and_b32 s54, s54, 3\n" \
"s_mul_i32 s59, s54, 131072\n" \
"s_add_u32 s54, s49, 2\n" \
"s_and_b32 s54, s54, 3\n" \
"s_mul_i32 s60, s54, 131072\n" \
"s_add_u32 s54, s49, 3\n" \
"s_and_b32 s54, s54, 3\n" \
"s_mul_i32 s61, s54, 131072\n" \
"s_mov_b32 s53, 0\n" \
"s_cmp_lg_u32 s48, 0\n" \
"s_cbranch_scc1 .Lq_go0x_%=\n" \
".Lq_go0_%=:\n" \
"buffer_load_dword v13, v0, s[36:39], 0 offen sc1\n" \
"s_waitcnt vmcnt(0)\n" \
"v_cmp_eq_u32 vcc, 0, v13\n" \
"s_add_u32 s53, s53, 1\n" \
"s_cmp_lt_u32 s53, 32768\n" \
"s_cselect_b64 s[54:55], -1, 0\n" \
"s_and_b64 vcc, vcc, s[54:55]\n" \
"s_cbranch_vccz .Lq_go0x_%=\n" \
"s_sleep 4\n" \
"s_branch .Lq_go0_%=\n" \
".Lq_go0x_%=:\n" \
"s_lshr_b32 s53, s58, 1\n" \
"s_add_u32 s92, s53, s94\n" \
"s_add_u32 s55, s53, s41\n" \
"s_xor_b32 s89, s55, 0x4800\n" \
"s_xor_b32 s90, s55, 0x800\n" \
"s_xor_b32 s91, s55, 0x4000\n" \
"s_lshr_b32 s93, s59, 1\n" \
"s_add_u32 s93, s93, s94\n" \
"s_add_u32 s81, s49, 2\n" \
"s_min_u32 s81, s81, 63\n" \
"ds_read_b128 v[32:35], v1 offset:0\n" \
"ds_read_b128 v[36:39], v1 offset:1024\n" \
"ds_read_b128 v[40:43], v1 offset:2048\n" \
"ds_read_b128 v[44:47], v1 offset:3072\n" \
"ds_read_b128 v[48:51], v2 offset:32768\n" \
"ds_read_b128 v[52:55], v2 offset:33792\n" \
"ds_read_b128 v[56:59], v2 offset:34816\n" \
"ds_read_b128 v[60:63], v2 offset:35840\n" \
"v_mov_b64 v[80:81], 0\n" \
"v_mov_b64 v[82:83], 0\n" \
"v_mov_b64 v[84:85], 0\n" \
"v_mov_b64 v[86:87], 0\n" \
"v_mov_b64 v[88:89], 0\n" \
"v_mov_b64 v[90:91], 0\n" \
"v_mov_b64 v[92:93], 0\n" \
"v_mov_b64 v[94:95], 0\n" \
"v_mov_b32 v13, -1\n" \
"v_add_u32 v14, s62, v3\n" \
"v_xor_b32 v31, 0x400, v11\n" \
"s_max_u32 s53, s49, 1\n" \
"s_sub_u32 s53, s53, 1\n" \
"v_mov_b32 v30, s53\n" \
"s_mov_b32 m0, s42\n" \
"s_memrealtime s[56:57]\n" \
"s_waitcnt lgkmcnt(0)\n" \
"s_sub_u32 s50, s56, 20\n" \
"s_waitcnt vmcnt(0) lgkmcnt(0)\n" \
"s_nop 7\n" \
"s_nop 7\n" \
".p2align 6\n" \
".Lq_loop_%=:\n" \
"s_getpc_b64 s[18:19]\n" \
".Lq_wp_%=:\n" \
"s_add_u32 s18, s18, .Lq_ws_%=-.Lq_wp_%=\n" \
"s_addc_u32 s19, s19, 0\n" \
"s_sub_u32 s21, s50, 5\n" \
"s_nop 0\n" \
".Lq_w_%=:\n" \
"s_memrealtime s[56:57]\n" \
"s_waitcnt lgkmcnt(0)\n" \
"s_sub_u32 s54, s56, s21\n" \
"s_cmp_gt_u32 s54, s22\n" \
"s_cbranch_scc1 .Lq_w_%=\n" \
"s_min_u32 s21, s54, 3\n" \
"s_lshl2_add_u32 s18, s21, s18\n" \
"s_setpc_b64 s[18:19]\n" \
"s_nop 0\n" \
".Lq_ws_%=:\n" \
"s_nop 4\n" \
"s_nop 5\n" \
"s_nop 4\n" \
".Lq_w4_%=:\n" \
"s_barrier\n" \
".Lq_go_%=:\n" \
"buffer_load_dwordx4 v4, s[36:39], s55 offen sc1 lds\n" \
"s_memtime s[0:1]\n" \
"s_cmp_lt_u32 s54, 8\n" \
"s_cselect_b32 s51, s50, s56\n" \
"s_cselect_b32 s23, s23, s49\n" \
"s_cselect_b32 s53, 0, s14\n" \
"s_add_u32 s53, s53, s34\n" \
"v_mov_b32 v17, s35\n" \
"s_nop 7\n" \
"s_nop 7\n" \
"s_add_u32 m0, s42, 4096\n" \
"s_nop 0\n" \
"buffer_load_dwordx4 v4, s[36:39], s89 offen sc1 lds\n" \
"s_nop 7\n" \
"s_nop 7\n" \
"s_nop 7\n" \
"s_add_u32 m0, s42, 8192\n" \
"s_nop 0\n" \
"buffer_load_dwordx4 v4, s[36:39], s90 offen sc1 lds\n" \
"s_add_u32 m0, s42, 12288\n" \
"s_nop 0\n" \
"buffer_load_dwordx4 v4, s[36:39], s91 offen sc1 lds\n" \
"s_add_u32 m0, s62, 147456\n" \
"s_sub_u32 s15, s12, 81920\n" \
"buffer_load_dwordx4 v3, s[36:39], s15 offen offset:0 sc1 lds\n" \
"buffer_load_dwordx4 v3, s[36:39], s15 offen offset:1024 sc1 lds\n" \
"s_add_u32 m0, s62, 139264\n" \
"s_sub_u32 s15, s12, 98304\n" \
"buffer_load_dwordx4 v3, s[36:39], s15 offen offset:0 sc1 lds\n" \
"buffer_load_dwordx4 v3, s[36:39], s15 offen offset:1024 sc1 lds\n" \
"s_waitcnt lgkmcnt(0)\n" \
"s_waitcnt vmcnt(7)\n" \
"s_barrier\n" \
"s_memtime s[2:3]\n" \
"ds_read_b128 v[64:67], v3 offset:0\n" \
"ds_read_b128 v[68:71], v3 offset:1024\n" \
"ds_read_b128 v[72:75], v3 offset:2048\n" \
"ds_read_b128 v[76:79], v3 offset:3072\n" \
"s_waitcnt lgkmcnt(3)\n" \
"v_smfmac_f32_16x16x64_bf16 v[80:83], v[64:67], a[0:7], v12\n" \
"v_min3_u32 v13, v13, v64, v66\n" \
"v_smfmac_f32_16x16x64_bf16 v[84:87], v[64:67], a[8:15], v12\n" \
"s_waitcnt lgkmcnt(2)\n" \
"v_min3_u32 v13, v13, v68, v70\n" \
"v_smfmac_f32_16x16x64_bf16 v[88:91], v[64:67], v[32:39], v12\n" \
"s_waitcnt lgkmcnt(1)\n" \
"v_min3_u32 v13, v13, v72, v74\n" \
"v_smfmac_f32_16x16x64_bf16 v[92:95], v[64:67], v[40:47], v12\n" \
"s_waitcnt lgkmcnt(0)\n" \
"v_min3_u32 v13, v13, v76, v78\n" \
"v_smfmac_f32_16x16x64_bf16 v[80:83], v[68:71], a[16:23], v12\n" \
"s_sub_u32 s2, s2, s0\n" \
"s_mul_i32 s2, s2, 23\n" \
"v_cmp_eq_u32 vcc, 0, v13\n" \
"v_smfmac_f32_16x16x64_bf16 v[84:87], v[68:71], a[24:31], v12\n" \
"ds_read_b128 v[32:35], v2 offset:0\n" \
"v_smfmac_f32_16x16x64_bf16 v[88:91], v[68:71], v[112:119], v12\n" \
"ds_read_b128 v[36:39], v2 offset:1024\n" \
"v_smfmac_f32_16x16x64_bf16 v[92:95], v[68:71], v[120:127], v12\n" \
"s_cbranch_vccnz .Lq_retry_%=\n" \
"s_waitcnt vmcnt(6)\n" \
"s_barrier\n" \
"ds_read_b128 v[64:67], v3 offset:4096\n" \
"v_smfmac_f32_16x16x64_bf16 v[80:83], v[72:75], a[32:39], v12\n" \
"ds_read_b128 v[68:71], v3 offset:5120\n" \
"s_lshr_b32 s2, s2, 9\n" \
"v_smfmac_f32_16x16x64_bf16 v[84:87], v[72:75], a[40:47], v12\n" \
"ds_read_b128 v[40:43], v2 offset:2048\n" \
"s_add_u32 s3, s51, s2\n" \
"v_smfmac_f32_16x16x64_bf16 v[88:91], v[72:75], v[48:55], v12\n" \
"ds_read_b128 v[44:47], v2 offset:3072\n" \
"s_add_u32 s53, s53, s3\n" \
"v_smfmac_f32_16x16x64_bf16 v[92:95], v[72:75], v[56:63], v12\n" \
"ds_read_b128 v[72:75], v3 offset:6144\n" \
"s_max_u32 s53, s53, 1\n" \
"v_mov_b32 v16, s53\n" \
"v_smfmac_f32_16x16x64_bf16 v[80:83], v[76:79], a[48:55], v12\n" \
"buffer_store_dwordx2 v[16:17], v10, s[36:39], s92 offen sc1\n" \
"v_smfmac_f32_16x16x64_bf16 v[84:87], v[76:79], a[56:63], v12\n" \
"ds_read_b128 v[48:51], v2 offset:16384\n" \
"v_smfmac_f32_16x16x64_bf16 v[88:91], v[76:79], v[128:135], v12\n" \
"ds_read_b128 v[52:55], v2 offset:17408\n" \
"v_smfmac_f32_16x16x64_bf16 v[92:95], v[76:79], v[136:143], v12\n" \
"ds_read_b128 v[76:79], v3 offset:7168\n" \
"s_waitcnt lgkmcnt(7)\n" \
"v_smfmac_f32_16x16x64_bf16 v[80:83], v[64:67], a[64:71], v12\n" \
"v_min3_u32 v13, v13, v64, v66\n" \
"v_smfmac_f32_16x16x64_bf16 v[84:87], v[64:67], a[72:79], v12\n" \
"ds_read_b128 v[56:59], v2 offset:18432\n" \
"v_smfmac_f32_16x16x64_bf16 v[88:91], v[64:67], v[32:39], v12\n" \
"ds_read_b128 v[60:63], v2 offset:19456\n" \
"s_waitcnt lgkmcnt(6)\n" \
"v_smfmac_f32_16x16x64_bf16 v[92:95], v[64:67], v[40:47], v12\n" \
"v_smfmac_f32_16x16x64_bf16 v[80:83], v[68:71], a[80:87], v12\n" \
"v_min3_u32 v13, v13, v68, v70\n" \
"v_smfmac_f32_16x16x64_bf16 v[84:87], v[68:71], a[88:95], v12\n" \
"ds_read_b128 v[32:35], v1 offset:32768\n" \
"v_smfmac_f32_16x16x64_bf16 v[88:91], v[68:71], v[144:151], v12\n" \
"ds_read_b128 v[36:39], v1 offset:33792\n" \
"v_smfmac_f32_16x16x64_bf16 v[92:95], v[68:71], v[152:159], v12\n" \
"s_waitcnt lgkmcnt(7)\n" \
"v_smfmac_f32_16x16x64_bf16 v[80:83], v[72:75], a[96:103], v12\n" \
"v_min3_u32 v13, v13, v72, v74\n" \
"v_smfmac_f32_16x16x64_bf16 v[84:87], v[72:75], a[104:111], v12\n" \
"ds_read_b128 v[40:43], v1 offset:34816\n" \
"s_waitcnt lgkmcnt(6)\n" \
"v_smfmac_f32_16x16x64_bf16 v[88:91], v[72:75], v[48:55], v12\n" \
"ds_read_b128 v[44:47], v1 offset:35840\n" \
"s_waitcnt lgkmcnt(4)\n" \
"v_smfmac_f32_16x16x64_bf16 v[92:95], v[72:75], v[56:63], v12\n" \
"s_waitcnt vmcnt(5)\n" \
"s_barrier\n" \
"ds_read_b128 v[64:67], v3 offset:8192\n" \
"v_smfmac_f32_16x16x64_bf16 v[80:83], v[76:79], a[112:119], v12\n" \
"ds_read_b128 v[68:71], v3 offset:9216\n" \
"v_min3_u32 v13, v13, v76, v78\n" \
"v_smfmac_f32_16x16x64_bf16 v[84:87], v[76:79], a[120:127], v12\n" \
"ds_read_b128 v[48:51], v1 offset:16384\n" \
"v_smfmac_f32_16x16x64_bf16 v[88:91], v[76:79], v[160:167], v12\n" \
"ds_read_b128 v[52:55], v1 offset:17408\n" \
"v_smfmac_f32_16x16x64_bf16 v[92:95], v[76:79], v[168:175], v12\n" \
"ds_read_b128 v[72:75], v3 offset:10240\n" \
"ds_read_b128 v[76:79], v3 offset:11264\n" \
"s_waitcnt lgkmcnt(5)\n" \
"v_smfmac_f32_16x16x64_bf16 v[80:83], v[64:67], a[128:135], v12\n" \
"buffer_store_dwordx4 v[96:99], v11, s[36:39], s95 offen sc1\n" \
"v_min3_u32 v13, v13, v64, v66\n" \
"v_smfmac_f32_16x16x64_bf16 v[84:87], v[64:67], a[136:143], v12\n" \
"ds_read_b128 v[56:59], v1 offset:18432\n" \
"v_smfmac_f32_16x16x64_bf16 v[88:91], v[64:67], v[32:39], v12\n" \
"ds_read_b128 v[60:63], v1 offset:19456\n" \
"v_smfmac_f32_16x16x64_bf16 v[92:95], v[64:67], v[40:47], v12\n" \
"ds_read_b128 v[64:67], v3 offset:12288\n" \
"s_waitcnt lgkmcnt(7)\n" \
"v_smfmac_f32_16x16x64_bf16 v[80:83], v[68:71], a[144:151], v12\n" \
"v_min3_u32 v13, v13, v68, v70\n" \
"v_smfmac_f32_16x16x64_bf16 v[84:87], v[68:71], a[152:159], v12\n" \
"ds_read_b128 v[32:35], v1 offset:49152\n" \
"v_smfmac_f32_16x16x64_bf16 v[88:91], v[68:71], v[176:183], v12\n" \
"ds_read_b128 v[36:39], v1 offset:50176\n" \
"v_smfmac_f32_16x16x64_bf16 v[92:95], v[68:71], v[184:191], v12\n" \
"ds_read_b128 v[68:71], v3 offset:13312\n" \
"s_waitcnt lgkmcnt(7)\n" \
"v_smfmac_f32_16x16x64_bf16 v[80:83], v[72:75], a[160:167], v12\n" \
"v_min3_u32 v13, v13, v72, v74\n" \
"v_smfmac_f32_16x16x64_bf16 v[84:87], v[72:75], a[168:175], v12\n" \
"ds_read_b128 v[40:43], v1 offset:51200\n" \
"v_smfmac_f32_16x16x64_bf16 v[88:91], v[72:75], v[48:55], v12\n" \
"ds_read_b128 v[44:47], v1 offset:52224\n" \
"s_waitcnt lgkmcnt(6)\n" \
"v_smfmac_f32_16x16x64_bf16 v[92:95], v[72:75], v[56:63], v12\n" \
"ds_read_b128 v[72:75], v3 offset:14336\n" \
"v_smfmac_f32_16x16x64_bf16 v[80:83], v[76:79], a[176:183], v12\n" \
"buffer_store_dwordx4 v[100:103], v31, s[36:39], s95 offen sc1\n" \
"v_min3_u32 v13, v13, v76, v78\n" \
"v_smfmac_f32_16x16x64_bf16 v[84:87], v[76:79], a[184:191], v12\n" \
"ds_read_b128 v[48:51], v3 offset:20480\n" \
"v_smfmac_f32_16x16x64_bf16 v[88:91], v[76:79], v[192:199], v12\n" \
"ds_read_b128 v[52:55], v3 offset:21504\n" \
"v_smfmac_f32_16x16x64_bf16 v[92:95], v[76:79], v[200:207], v12\n" \
"ds_read_b128 v[76:79], v3 offset:15360\n" \
"s_waitcnt lgkmcnt(9)\n" \
"v_smfmac_f32_16x16x64_bf16 v[80:83], v[64:67], a[192:199], v12\n" \
"s_mov_b32 s95, 0x7ffffff0\n" \
"v_min3_u32 v13, v13, v64, v66\n" \
"v_smfmac_f32_16x16x64_bf16 v[84:87], v[64:67], a[200:207], v12\n" \
"ds_read_b128 v[56:59], v3 offset:22528\n" \
"s_waitcnt lgkmcnt(8)\n" \
"v_smfmac_f32_16x16x64_bf16 v[88:91], v[64:67], v[32:39], v12\n" \
"ds_read_b128 v[60:63], v3 offset:23552\n" \
"s_waitcnt lgkmcnt(6)\n" \
"v_smfmac_f32_16x16x64_bf16 v[92:95], v[64:67], v[40:47], v12\n" \
"v_smfmac_f32_16x16x64_bf16 v[80:83], v[68:71], a[208:215], v12\n" \
"v_min3_u32 v13, v13, v68, v70\n" \
"v_smfmac_f32_16x16x64_bf16 v[84:87], v[68:71], a[216:223], v12\n" \
"ds_read_b128 v[32:35], v3 offset:16384\n" \
"v_smfmac_f32_16x16x64_bf16 v[88:91], v[68:71], v[208:215], v12\n" \
"ds_read_b128 v[36:39], v3 offset:17408\n" \
"v_smfmac_f32_16x16x64_bf16 v[92:95], v[68:71], v[216:223], v12\n" \
"s_waitcnt lgkmcnt(7)\n" \
"v_smfmac_f32_16x16x64_bf16 v[80:83], v[72:75], a[224:231], v12\n" \
"v_min3_u32 v13, v13, v72, v74\n" \
"v_smfmac_f32_16x16x64_bf16 v[84:87], v[72:75], a[232:239], v12\n" \
"ds_read_b128 v[40:43], v3 offset:18432\n" \
"s_waitcnt lgkmcnt(5)\n" \
"v_smfmac_f32_16x16x64_bf16 v[80:83], v[76:79], a[240:247], v12\n" \
"ds_read_b128 v[44:47], v3 offset:19456\n" \
"v_min3_u32 v13, v13, v76, v78\n" \
"v_smfmac_f32_16x16x64_bf16 v[84:87], v[76:79], a[248:255], v12\n" \
"v_cmp_eq_u32_e64 vcc, 0, v13\n" \
"v_smfmac_f32_16x16x64_bf16 v[88:91], v[72:75], v[224:231], v12\n" \
"v_add_f32_e64 v80, v80, v82\n" \
"v_smfmac_f32_16x16x64_bf16 v[92:95], v[72:75], v[232:239], v12\n" \
"v_add_f32_e64 v81, v81, v83\n" \
"v_smfmac_f32_16x16x64_bf16 v[88:91], v[76:79], v[240:247], v12\n" \
"s_cbranch_vccnz .Lq_retry_%=\n" \
"v_add_f32 v84, v84, v86\n" \
"v_smfmac_f32_16x16x64_bf16 v[92:95], v[76:79], v[248:255], v12\n" \
"v_add_f32_e64 v85, v85, v87\n" \
"v_pk_mul_f32 v[80:81], v[80:81], s[98:99]\n" \
"v_pk_mul_f32 v[84:85], v[84:85], s[98:99]\n" \
"v_cvt_pk_bf16_f32 v80, v80, v81\n" \
"v_cvt_pk_bf16_f32 v84, v84, v85\n" \
"v_pk_add_f32 v[88:89], v[88:89], v[90:91]\n" \
"v_cvt_pk_bf16_f32 v81, v88, v89\n" \
"s_nop 0\n" \
"v_pk_add_f32 v[92:93], v[92:93], v[94:95]\n" \
"v_cvt_pk_bf16_f32 v85, v92, v93\n" \
"ds_write2_b64 v5, v[80:81], v[84:85] offset1:64\n" \
"buffer_load_dword v104, v8, s[36:39], s58 offen offset:0 sc1\n" \
"buffer_load_dword v106, v8, s[36:39], s58 offen offset:256 sc1\n" \
"s_cmp_eq_u32 s49, 0\n" \
"s_cbranch_scc1 .Lq_first_%=\n" \
".Lq_first2_%=:\n" \
"s_cmp_eq_u32 s49, 63\n" \
"s_waitcnt vmcnt(5)\n" \
"s_waitcnt lgkmcnt(0)\n" \
"s_barrier\n" \
"ds_read_b64_tr_b16 v[64:65], v6 offset:0\n" \
"ds_read_b64_tr_b16 v[66:67], v6 offset:512\n" \
"ds_read_b64_tr_b16 v[68:69], v6 offset:1024\n" \
"ds_read_b64_tr_b16 v[70:71], v6 offset:1536\n" \
"ds_read_b64_tr_b16 v[72:73], v6 offset:2048\n" \
"ds_read_b64_tr_b16 v[74:75], v6 offset:2560\n" \
"s_cbranch_scc1 .Lq_last_%=\n" \
"s_waitcnt lgkmcnt(4)\n" \
"v_mfma_f32_16x16x32_bf16 v[88:91], v[64:67], v[64:67], 0\n" \
"ds_read_b64_tr_b16 v[76:77], v6 offset:3072\n" \
"v_mfma_f32_16x16x32_bf16 v[80:83], v[32:35], v[64:67], 0\n" \
"ds_read_b64_tr_b16 v[78:79], v6 offset:3584\n" \
"s_waitcnt lgkmcnt(4)\n" \
"v_mfma_f32_16x16x32_bf16 v[88:91], v[68:71], v[68:71], v[88:91]\n" \
"ds_read_b128 v[96:99], v14 offset:32768\n" \
"s_waitcnt lgkmcnt(3)\n" \
"v_mfma_f32_16x16x32_bf16 v[88:91], v[72:75], v[72:75], v[88:91]\n" \
"ds_read_b128 v[100:103], v14 offset:33792\n" \
"v_mfma_f32_16x16x32_bf16 v[80:83], v[36:39], v[68:71], v[80:83]\n" \
"buffer_store_dword v30, v10, s[36:39], s87 offen sc1\n" \
"s_waitcnt lgkmcnt(2)\n" \
"v_mfma_f32_16x16x32_bf16 v[88:91], v[76:79], v[76:79], v[88:91]\n" \
"buffer_load_dword v30, v15, s[36:39], s80 offen sc1\n" \
"v_mfma_f32_16x16x32_bf16 v[80:83], v[40:43], v[72:75], v[80:83]\n" \
"v_mfma_f32_16x16x32_bf16 v[80:83], v[44:47], v[76:79], v[80:83]\n" \
"v_max_f32 v13, v90, v91\n" \
"v_max_f32 v14, v90, v91\n" \
"v_mfma_f32_16x16x32_bf16 v[84:87], v[48:51], v[64:67], 0\n" \
"s_nop 0\n" \
"v_permlane16_swap_b32 v13, v14\n" \
"v_mfma_f32_16x16x32_bf16 v[84:87], v[52:55], v[68:71], v[84:87]\n" \
"v_max_f32 v13, v13, v14\n" \
"v_max_f32 v14, v13, v14\n" \
"v_mfma_f32_16x16x32_bf16 v[84:87], v[56:59], v[72:75], v[84:87]\n" \
"s_nop 0\n" \
"v_permlane32_swap_b32 v13, v14\n" \
"v_mfma_f32_16x16x32_bf16 v[84:87], v[60:63], v[76:79], v[84:87]\n" \
"v_max_f32 v14, v13, v14\n" \
"v_sub_f32 v80, v80, v14\n" \
"v_sub_f32 v81, v81, v14\n" \
"v_exp_f32 v80, v80\n" \
"v_pk_add_f32 v[82:83], v[82:83], v[14:15] op_sel_hi:[1,0] neg_lo:[0,1] neg_hi:[0,1]\n" \
"v_exp_f32 v81, v81\n" \
"v_exp_f32 v82, v82\n" \
"v_exp_f32_e64 v83, v83\n" \
"v_pk_add_f32 v[84:85], v[84:85], v[14:15] op_sel_hi:[1,0] neg_lo:[0,1] neg_hi:[0,1]\n" \
"v_pk_add_f32 v[86:87], v[86:87], v[14:15] op_sel_hi:[1,0] neg_lo:[0,1] neg_hi:[0,1]\n" \
"v_pk_add_f32 v[90:91], v[90:91], v[14:15] op_sel_hi:[1,0] neg_lo:[0,1] neg_hi:[0,1]\n" \
"v_exp_f32 v84, v84\n" \
"v_exp_f32 v85, v85\n" \
"v_exp_f32 v86, v86\n" \
"v_exp_f32 v87, v87\n" \
"v_cvt_pk_bf16_f32 v80, v80, v81\n" \
"v_cvt_pk_bf16_f32 v81, v82, v83\n" \
"v_cvt_pk_bf16_f32 v82, v84, v85\n" \
"v_cvt_pk_bf16_f32 v83, v86, v87\n" \
"v_exp_f32 v90, v90\n" \
"s_waitcnt lgkmcnt(1)\n" \
"v_mfma_f32_16x16x32_bf16 v[84:87], v[96:99], v[80:83], 0\n" \
"v_exp_f32 v91, v91\n" \
"s_waitcnt lgkmcnt(0)\n" \
"v_mfma_f32_16x16x32_bf16 v[92:95], v[100:103], v[80:83], 0\n" \
"v_cvt_pk_bf16_f32 v26, v90, v91\n" \
"v_mfma_f32_16x16x32_bf16 v[16:19], v[20:23], v[80:83], 0\n" \
"s_nop 0\n" \
"v_mfma_f32_16x16x32_bf16 v[16:19], v[20:23], v[26:29], v[16:19]\n" \
"s_waitcnt vmcnt(2)\n" \
"v_mfma_f32_16x16x32_bf16 v[84:87], v[104:107], v[26:29], v[84:87]\n" \
"v_mfma_f32_16x16x32_bf16 v[92:95], v[104:107], v[24:27], v[92:95]\n" \
"v_min_u32 v13, v104, v106\n" \
"v_cmp_eq_u32 vcc, 0, v13\n" \
"v_rcp_f32_e64 v14, v16\n" \
"v_cmp_nlt_f32_e64 s[26:27], v16, s8\n" \
".Lq_vr2_%=:\n" \
"v_pk_mul_f32 v[84:85], v[84:85], v[14:15] op_sel_hi:[1,0]\n" \
"v_pk_mul_f32 v[86:87], v[86:87], v[14:15] op_sel_hi:[1,0]\n" \
"v_cvt_pk_bf16_f32 v88, v84, v85\n" \
"v_cvt_pk_bf16_f32 v89, v86, v87\n" \
"v_max_u32 v88, 1, v88\n" \
"s_and_b64 s[26:27], s[26:27], s[6:7]\n" \
"s_or_b64 vcc, vcc, s[26:27]\n" \
"s_cbranch_vccnz .Lq_vr_%=\n" \
"v_pk_mul_f32 v[92:93], v[92:93], v[14:15] op_sel_hi:[1,0]\n" \
"v_pk_mul_f32 v[94:95], v[94:95], v[14:15] op_sel_hi:[1,0]\n" \
"v_cvt_pk_bf16_f32 v90, v92, v93\n" \
"v_cvt_pk_bf16_f32 v91, v94, v95\n" \
"v_max_u32_e64 v90, 1, v90\n" \
"buffer_store_dwordx4 v[88:91], v7, s[36:39], s58 offen sc1\n" \
".Lq_tok_%=:\n" \
"s_memrealtime s[24:25]\n" \
"v_add_u32_e64 v14, s43, v3\n" \
"ds_read_b128 v[96:99], v14 offset:24576\n" \
"ds_read_b128 v[100:103], v14 offset:25600\n" \
"ds_read_b128 v[92:95], v14 offset:26624\n" \
"s_memrealtime s[26:27]\n" \
"ds_read_b128 v[16:19], v14 offset:27648\n" \
"v_xor_b32 v0, 512, v8\n" \
"s_nop 2\n" \
"s_memrealtime s[28:29]\n" \
"s_nop 4\n" \
"s_memrealtime s[30:31]\n" \
"s_waitcnt lgkmcnt(0)\n" \
"v_mfma_f32_16x16x32_bf16 v[88:91], v[64:67], v[96:99], 0\n" \
"s_sub_u32 s13, s12, s62\n" \
"s_add_u32 s54, s49, 1\n" \
"s_min_u32 s54, s54, 63\n" \
"s_lshl_b32 s54, s54, 16\n" \
"v_mfma_f32_16x16x32_bf16 v[80:83], v[32:35], v[96:99], 0\n" \
"s_lshr_b32 s53, s54, 8\n" \
"s_and_b32 s53, s53, 0x2000\n" \
"s_xor_b32 s54, s54, s53\n" \
"buffer_load_dword v108, v0, s[36:39], s58 offen offset:0 sc1\n" \
"v_mfma_f32_16x16x32_bf16 v[88:91], v[68:71], v[100:103], v[88:91]\n" \
"s_xor_b32 s12, s54, s63\n" \
"s_add_u32 s12, s12, s62\n" \
"buffer_load_dword v110, v0, s[36:39], s58 offen offset:256 sc1\n" \
"v_mfma_f32_16x16x32_bf16 v[88:91], v[72:75], v[92:95], v[88:91]\n" \
"s_add_u32 m0, s62, 131072\n" \
"s_sub_u32 s15, s12, 114688\n" \
"buffer_load_dwordx4 v3, s[36:39], s15 offen offset:0 sc1 lds\n" \
"v_mfma_f32_16x16x32_bf16 v[80:83], v[36:39], v[100:103], v[80:83]\n" \
"buffer_load_dwordx4 v3, s[36:39], s15 offen offset:1024 sc1 lds\n" \
"v_mfma_f32_16x16x32_bf16 v[88:91], v[76:79], v[16:19], v[88:91]\n" \
"s_add_u32 s25, s24, 3\n" \
"s_cmp_eq_u32 s26, s24\n" \
"s_subb_u32 s25, s25, 0\n" \
"v_mfma_f32_16x16x32_bf16 v[80:83], v[40:43], v[92:95], v[80:83]\n" \
"s_cmp_eq_u32 s28, s24\n" \
"s_subb_u32 s25, s25, 0\n" \
"v_mfma_f32_16x16x32_bf16 v[80:83], v[44:47], v[16:19], v[80:83]\n" \
"s_cmp_eq_u32 s30, s24\n" \
"s_subb_u32 s25, s25, 0\n" \
"s_max_u32 s16, s25, 1\n" \
"v_add_u32 v14, s62, v3\n" \
"v_mfma_f32_16x16x32_bf16 v[84:87], v[48:51], v[96:99], 0\n" \
"s_sub_u32 s24, s25, s3\n" \
"s_min_u32 s24, s24, 255\n" \
"v_xor_b32 v0, 0x800, v14\n" \
"v_mfma_f32_16x16x32_bf16 v[84:87], v[52:55], v[100:103], v[84:87]\n" \
"ds_read_b128 v[32:35], v14 offset:32768\n" \
"ds_read_b128 v[36:39], v14 offset:33792\n" \
"s_cmp_eq_u32 s23, s49\n" \
"s_cselect_b32 s24, s34, s24\n" \
"v_mov_b32 v13, 0xceabdc6e\n" \
"v_mfma_f32_16x16x32_bf16 v[84:87], v[56:59], v[92:95], v[84:87]\n" \
"ds_read_b128 v[40:43], v0 offset:32768\n" \
"ds_read_b128 v[44:47], v0 offset:33792\n" \
"s_min_u32 s34, s24, s32\n" \
"s_max_u32 s25, s24, s32\n" \
"v_mfma_f32_16x16x32_bf16 v[84:87], v[60:63], v[16:19], v[84:87]\n" \
"s_min_u32 s25, s25, s33\n" \
"s_max_u32 s34, s34, s25\n" \
"s_mov_b32 s33, s32\n" \
"s_mov_b32 s32, s24\n" \
"v_cndmask_b32_e64 v80, v13, v80, s[64:65]\n" \
"v_cndmask_b32_e64 v81, v13, v81, s[66:67]\n" \
"v_cndmask_b32_e64 v82, v13, v82, s[68:69]\n" \
"v_cndmask_b32_e64 v83, v13, v83, s[70:71]\n" \
"s_waitcnt vmcnt(5)\n" \
"v_cmp_ge_u32_e64 s[96:97], s81, v30\n" \
"v_readlane_b32 s35, v30, 3\n" \
"v_readlane_b32 s53, v30, 4\n" \
"v_cndmask_b32_e64 v84, v13, v84, s[72:73]\n" \
"v_cndmask_b32_e64 v85, v13, v85, s[74:75]\n" \
"v_cndmask_b32_e64 v86, v13, v86, s[76:77]\n" \
"v_cndmask_b32_e64 v87, v13, v87, s[78:79]\n" \
"s_max_u32 s35, s35, s53\n" \
"s_cmp_eq_u32 s35, 0\n" \
"s_cselect_b32 s35, s17, s35\n" \
"s_and_b32 s96, s96, s9\n" \
"s_cbranch_scc1 .Lq_f_cold_%=\n" \
".Lq_f_ok_%=:\n" \
"s_lshr_b32 s11, s58, 1\n" \
"s_mov_b32 s54, s58\n" \
"s_mov_b32 s58, s59\n" \
"s_mov_b32 s59, s60\n" \
"s_mov_b32 s60, s61\n" \
"s_mov_b32 s61, s54\n" \
"buffer_load_dwordx2 v[64:65], v9, s[36:39], s11 offen sc1\n" \
"v_max3_f32 v13, v90, v91, v80\n" \
"v_max3_f32 v14, v81, v82, v83\n" \
"v_max3_f32 v14, v14, v84, v85\n" \
"v_max3_f32 v13, v13, v86, v87\n" \
"v_max_f32 v13, v13, v14\n" \
"v_max_f32 v14, v13, v14\n" \
"s_nop 1\n" \
"v_permlane16_swap_b32 v13, v14\n" \
"v_max_f32 v13, v13, v14\n" \
"v_max_f32 v14, v13, v14\n" \
"s_nop 1\n" \
"v_permlane32_swap_b32 v13, v14\n" \
"v_max_f32 v14, v13, v14\n" \
"v_pk_add_f32 v[80:81], v[80:81], v[14:15] op_sel_hi:[1,0] neg_lo:[0,1] neg_hi:[0,1]\n" \
"v_pk_add_f32 v[82:83], v[82:83], v[14:15] op_sel_hi:[1,0] neg_lo:[0,1] neg_hi:[0,1]\n" \
"v_pk_add_f32 v[84:85], v[84:85], v[14:15] op_sel_hi:[1,0] neg_lo:[0,1] neg_hi:[0,1]\n" \
"v_pk_add_f32 v[86:87], v[86:87], v[14:15] op_sel_hi:[1,0] neg_lo:[0,1] neg_hi:[0,1]\n" \
"v_pk_add_f32 v[90:91], v[90:91], v[14:15] op_sel_hi:[1,0] neg_lo:[0,1] neg_hi:[0,1]\n" \
"v_exp_f32 v80, v80\n" \
"v_exp_f32 v81, v81\n" \
"v_exp_f32 v82, v82\n" \
"v_exp_f32 v83, v83\n" \
"v_exp_f32 v84, v84\n" \
"v_exp_f32 v85, v85\n" \
"v_exp_f32 v86, v86\n" \
"v_exp_f32 v87, v87\n" \
"v_cvt_pk_bf16_f32 v80, v80, v81\n" \
"v_cvt_pk_bf16_f32 v81, v82, v83\n" \
"v_cvt_pk_bf16_f32 v82, v84, v85\n" \
"v_cvt_pk_bf16_f32 v83, v86, v87\n" \
"v_exp_f32 v90, v90\n" \
"s_waitcnt lgkmcnt(3)\n" \
"v_mfma_f32_16x16x32_bf16 v[48:51], v[32:35], v[80:83], 0\n" \
"v_exp_f32 v91, v91\n" \
"s_waitcnt lgkmcnt(2)\n" \
"v_mfma_f32_16x16x32_bf16 v[52:55], v[36:39], v[80:83], 0\n" \
"v_cvt_pk_bf16_f32 v26, v90, v91\n" \
"s_waitcnt lgkmcnt(1)\n" \
"v_mfma_f32_16x16x32_bf16 v[56:59], v[40:43], v[80:83], 0\n" \
"s_waitcnt lgkmcnt(0)\n" \
"v_mfma_f32_16x16x32_bf16 v[60:63], v[44:47], v[80:83], 0\n" \
"v_mfma_f32_16x16x32_bf16 v[92:95], v[20:23], v[80:83], 0\n" \
"s_waitcnt vmcnt(4)\n" \
"v_min3_u32 v13, v104, v106, v108\n" \
"s_waitcnt vmcnt(3)\n" \
"v_min3_u32 v13, v13, v110, v110\n" \
"v_cmp_eq_u32_e64 vcc, 0, v13\n" \
"s_nop 0\n" \
"s_nop 0\n" \
"s_cbranch_vccnz .Lq_v2_cold_%=\n" \
".Lq_v2_ok_%=:\n" \
"v_mfma_f32_16x16x32_bf16 v[92:95], v[20:23], v[26:29], v[92:95]\n" \
"v_mfma_f32_16x16x32_bf16 v[48:51], v[104:107], v[26:29], v[48:51]\n" \
"ds_read_b128 v[32:35], v1 offset:0\n" \
"s_mov_b32 s95, s13\n" \
"s_add_u32 s49, s49, 1\n" \
"v_mfma_f32_16x16x32_bf16 v[52:55], v[104:107], v[24:27], v[52:55]\n" \
"ds_read_b128 v[36:39], v1 offset:1024\n" \
"s_lshr_b32 s53, s58, 1\n" \
"s_add_u32 s92, s53, s94\n" \
"v_mfma_f32_16x16x32_bf16 v[56:59], v[108:111], v[26:29], v[56:59]\n" \
"ds_read_b128 v[40:43], v1 offset:2048\n" \
"s_add_u32 s55, s53, s41\n" \
"s_xor_b32 s89, s55, 0x4800\n" \
"v_mfma_f32_16x16x32_bf16 v[60:63], v[108:111], v[24:27], v[60:63]\n" \
"ds_read_b128 v[44:47], v1 offset:3072\n" \
"s_xor_b32 s90, s55, 0x800\n" \
"s_xor_b32 s91, s55, 0x4000\n" \
"s_lshr_b32 s93, s59, 1\n" \
"s_add_u32 s93, s93, s94\n" \
"s_add_u32 s81, s49, 2\n" \
"s_min_u32 s81, s81, 63\n" \
"s_mov_b32 s14, s10\n" \
"v_rcp_f32 v14, v92\n" \
"s_nop 0\n" \
"v_pk_mul_f32 v[48:49], v[48:49], v[14:15] op_sel_hi:[1,0]\n" \
"v_pk_mul_f32 v[50:51], v[50:51], v[14:15] op_sel_hi:[1,0]\n" \
"v_pk_mul_f32 v[52:53], v[52:53], v[14:15] op_sel_hi:[1,0]\n" \
"v_pk_mul_f32 v[54:55], v[54:55], v[14:15] op_sel_hi:[1,0]\n" \
"v_cvt_pk_bf16_f32 v96, v48, v49\n" \
"v_cvt_pk_bf16_f32 v97, v50, v51\n" \
"v_cvt_pk_bf16_f32 v98, v52, v53\n" \
"v_cvt_pk_bf16_f32 v99, v54, v55\n" \
"ds_read_b128 v[48:51], v2 offset:32768\n" \
"ds_read_b128 v[52:55], v2 offset:33792\n" \
"v_pk_mul_f32 v[56:57], v[56:57], v[14:15] op_sel_hi:[1,0]\n" \
"v_pk_mul_f32 v[58:59], v[58:59], v[14:15] op_sel_hi:[1,0]\n" \
"v_pk_mul_f32 v[60:61], v[60:61], v[14:15] op_sel_hi:[1,0]\n" \
"v_pk_mul_f32 v[62:63], v[62:63], v[14:15] op_sel_hi:[1,0]\n" \
"v_cvt_pk_bf16_f32 v100, v56, v57\n" \
"v_cvt_pk_bf16_f32 v101, v58, v59\n" \
"v_cvt_pk_bf16_f32 v102, v60, v61\n" \
"v_cvt_pk_bf16_f32 v103, v62, v63\n" \
"ds_read_b128 v[56:59], v2 offset:34816\n" \
"ds_read_b128 v[60:63], v2 offset:35840\n" \
"s_cmp_eq_u32 s49, 64\n" \
"s_nop 0\n" \
"s_nop 0\n" \
"s_cbranch_scc1 .Lq_end_%=\n" \
"v_mov_b64 v[80:81], 0\n" \
"v_mov_b64 v[82:83], 0\n" \
"v_mov_b64 v[84:85], 0\n" \
"v_mov_b64 v[86:87], 0\n" \
"v_mov_b64 v[88:89], 0\n" \
"v_mov_b64 v[90:91], 0\n" \
"v_mov_b64 v[92:93], 0\n" \
"v_mov_b64 v[94:95], 0\n" \
"v_mov_b32 v13, -1\n" \
"v_add_u32_e64 v14, s62, v3\n" \
"v_xor_b32 v31, 0x400, v11\n" \
"s_max_u32 s53, s49, 1\n" \
"s_sub_u32 s53, s53, 1\n" \
"v_mov_b32 v30, s53\n" \
"s_mov_b32 m0, s42\n" \
"buffer_store_dwordx4 v[80:83], v7, s[36:39], s59 offen sc1\n" \
"buffer_store_dword v24, v10, s[36:39], s93 offen sc1\n" \
"s_waitcnt lgkmcnt(0)\n" \
"s_mov_b32 s54, 0\n" \
"s_cmp_lg_u32 s48, 0\n" \
"s_cbranch_scc1 .Lq_w4_%=\n" \
"s_waitcnt vmcnt(2)\n" \
"v_cmp_eq_u32_e64 vcc, 0, v64\n" \
"s_nop 0\n" \
"s_and_b64 vcc, vcc, s[82:83]\n" \
"s_nop 0\n" \
"s_cbranch_vccnz .Lq_h_cold_%=\n" \
".Lq_h_ok_%=:\n" \
"v_subrev_u32 v64, s51, v64\n" \
"v_max_i32_dpp v65, v65, v65 row_ror:8 row_mask:0xf bank_mask:0xf\n" \
"s_nop 0\n" \
"v_max_i32_dpp v64, v64, v64 row_ror:8 row_mask:0xf bank_mask:0xf\n" \
"v_max_i32_dpp v65, v65, v65 row_ror:4 row_mask:0xf bank_mask:0xf\n" \
"s_nop 0\n" \
"v_max_i32_dpp v64, v64, v64 row_ror:4 row_mask:0xf bank_mask:0xf\n" \
"v_max_i32_dpp v65, v65, v65 row_ror:2 row_mask:0xf bank_mask:0xf\n" \
"s_nop 0\n" \
"v_max_i32_dpp v64, v64, v64 row_ror:2 row_mask:0xf bank_mask:0xf\n" \
"v_max_i32_dpp v65, v65, v65 row_ror:1 row_mask:0xf bank_mask:0xf\n" \
"s_nop 0\n" \
"v_max_i32_dpp v64, v64, v64 row_ror:1 row_mask:0xf bank_mask:0xf\n" \
"v_readlane_b32 s53, v65, 0\n" \
"v_readlane_b32 s50, v65, 16\n" \
"v_readlane_b32 s54, v64, 0\n" \
"v_readlane_b32 s40, v64, 16\n" \
"s_max_i32 s50, s50, s53\n" \
"s_max_i32 s40, s40, s54\n" \
"s_add_u32 s40, s40, s51\n" \
"s_add_u32 s40, s40, s44\n" \
"s_add_u32 s50, s50, s40\n" \
"s_add_u32 s50, s50, s45\n" \
"s_branch .Lq_loop_%=\n" \
".Lq_f0_cold_%=:\n" \
"s_nop 7\n" \
"s_nop 7\n" \
"s_waitcnt vmcnt(0) lgkmcnt(0)\n" \
"s_mov_b32 s53, 0\n" \
".Lq_f0_again_%=:\n" \
"s_cmp_lg_u32 s52, 0\n" \
"s_cbranch_scc1 .Lq_f0_ok_%=\n" \
"buffer_load_dword v30, v15, s[36:39], s80 offen sc1\n" \
"s_waitcnt vmcnt(0)\n" \
"v_cmp_ge_u32_e64 s[96:97], s81, v30\n" \
"s_nop 4\n" \
"s_and_b32 s96, s96, s9\n" \
"s_cbranch_scc0 .Lq_f0_ok_%=\n" \
"s_sleep 3\n" \
"s_add_u32 s53, s53, 1\n" \
"s_cmp_lt_u32 s53, 32768\n" \
"s_cbranch_scc1 .Lq_f0_again_%=\n" \
"s_mov_b32 s52, 1\n" \
"v_mov_b32 v30, 256\n" \
"v_mov_b32 v31, 1\n" \
"buffer_store_dword v31, v30, s[36:39], 0 offen sc1\n" \
"s_waitcnt vmcnt(0) lgkmcnt(0)\n" \
"s_branch .Lq_f0_ok_%=\n" \
".Lq_f1_cold_%=:\n" \
"s_nop 7\n" \
"s_nop 7\n" \
"s_waitcnt vmcnt(0) lgkmcnt(0)\n" \
"s_mov_b32 s53, 0\n" \
".Lq_f1_again_%=:\n" \
"s_cmp_lg_u32 s52, 0\n" \
"s_cbranch_scc1 .Lq_f1_ok_%=\n" \
"buffer_load_dword v30, v15, s[36:39], s80 offen sc1\n" \
"s_waitcnt vmcnt(0)\n" \
"v_cmp_ge_u32_e64 s[96:97], s81, v30\n" \
"s_nop 4\n" \
"s_and_b32 s96, s96, s9\n" \
"s_cbranch_scc0 .Lq_f1_ok_%=\n" \
"s_sleep 3\n" \
"s_add_u32 s53, s53, 1\n" \
"s_cmp_lt_u32 s53, 32768\n" \
"s_cbranch_scc1 .Lq_f1_again_%=\n" \
"s_mov_b32 s52, 1\n" \
"v_mov_b32 v30, 256\n" \
"v_mov_b32 v31, 1\n" \
"buffer_store_dword v31, v30, s[36:39], 0 offen sc1\n" \
"s_waitcnt vmcnt(0) lgkmcnt(0)\n" \
"s_branch .Lq_f1_ok_%=\n" \
".Lq_first_%=:\n" \
"s_mov_b32 s81, 1\n" \
"s_mov_b32 s96, -1\n" \
"s_and_b32 s96, s96, s9\n" \
"s_cbranch_scc1 .Lq_f1_cold_%=\n" \
".Lq_f1_ok_%=:\n" \
"s_mov_b32 s81, 2\n" \
"v_mov_b32 v30, 0\n" \
"s_branch .Lq_first2_%=\n" \
".Lq_last_%=:\n" \
"ds_read_b64_tr_b16 v[76:77], v6 offset:3072\n" \
"ds_read_b64_tr_b16 v[78:79], v6 offset:3584\n" \
"ds_read_b128 v[96:99], v14 offset:32768\n" \
"ds_read_b128 v[100:103], v14 offset:33792\n" \
"buffer_store_dword v30, v10, s[36:39], s87 offen sc1\n" \
"buffer_load_dword v30, v15, s[36:39], s80 offen sc1\n" \
"s_waitcnt vmcnt(0) lgkmcnt(0)\n" \
"s_barrier\n" \
"v_mov_b32 v13, 63\n" \
"buffer_store_dword v13, v10, s[36:39], s87 offen sc1\n" \
"s_nop 7\n" \
"s_nop 7\n" \
"s_branch .Lq_tok_%=\n" \
".Lq_vr_%=:\n" \
"s_nop 7\n" \
"s_nop 7\n" \
"s_waitcnt vmcnt(0) lgkmcnt(0)\n" \
"v_min3_u32 v13, v104, v106, v106\n" \
"v_cmp_eq_u32 vcc, 0, v13\n" \
"s_nop 0\n" \
"s_cbranch_vccnz .Lq_v_cold_%=\n" \
".Lq_v_ok_%=:\n" \
"s_mov_b32 s23, s49\n" \
"s_cmp_eq_u64 s[26:27], 0\n" \
"s_cbranch_scc1 .Lq_vr3_%=\n" \
"v_mfma_f32_16x16x32_bf16 v[88:91], v[64:67], v[64:67], 0\n" \
"v_mfma_f32_16x16x32_bf16 v[80:83], v[32:35], v[64:67], 0\n" \
"v_mfma_f32_16x16x32_bf16 v[88:91], v[68:71], v[68:71], v[88:91]\n" \
"v_mfma_f32_16x16x32_bf16 v[88:91], v[72:75], v[72:75], v[88:91]\n" \
"v_mfma_f32_16x16x32_bf16 v[80:83], v[36:39], v[68:71], v[80:83]\n" \
"v_mfma_f32_16x16x32_bf16 v[88:91], v[76:79], v[76:79], v[88:91]\n" \
"v_mfma_f32_16x16x32_bf16 v[80:83], v[40:43], v[72:75], v[80:83]\n" \
"v_mfma_f32_16x16x32_bf16 v[80:83], v[44:47], v[76:79], v[80:83]\n" \
"v_mfma_f32_16x16x32_bf16 v[84:87], v[48:51], v[64:67], 0\n" \
"v_mfma_f32_16x16x32_bf16 v[84:87], v[52:55], v[68:71], v[84:87]\n" \
"v_mfma_f32_16x16x32_bf16 v[84:87], v[56:59], v[72:75], v[84:87]\n" \
"v_max3_f32 v13, v90, v91, v80\n" \
"v_mfma_f32_16x16x32_bf16 v[84:87], v[60:63], v[76:79], v[84:87]\n" \
"v_max3_f32 v14, v81, v82, v83\n" \
"s_nop 6\n" \
"v_max3_f32 v14, v14, v84, v85\n" \
"v_max3_f32 v13, v13, v86, v87\n" \
"v_max_f32 v13, v13, v14\n" \
"v_max_f32 v14, v13, v14\n" \
"s_nop 1\n" \
"v_permlane16_swap_b32 v13, v14\n" \
"v_max_f32 v13, v13, v14\n" \
"v_max_f32 v14, v13, v14\n" \
"s_nop 1\n" \
"v_permlane32_swap_b32 v13, v14\n" \
"v_max_f32 v14, v13, v14\n" \
"v_pk_add_f32 v[80:81], v[80:81], v[14:15] op_sel_hi:[1,0] neg_lo:[0,1] neg_hi:[0,1]\n" \
"v_pk_add_f32 v[82:83], v[82:83], v[14:15] op_sel_hi:[1,0] neg_lo:[0,1] neg_hi:[0,1]\n" \
"v_pk_add_f32 v[84:85], v[84:85], v[14:15] op_sel_hi:[1,0] neg_lo:[0,1] neg_hi:[0,1]\n" \
"v_pk_add_f32 v[86:87], v[86:87], v[14:15] op_sel_hi:[1,0] neg_lo:[0,1] neg_hi:[0,1]\n" \
"v_pk_add_f32 v[90:91], v[90:91], v[14:15] op_sel_hi:[1,0] neg_lo:[0,1] neg_hi:[0,1]\n" \
"v_exp_f32 v80, v80\n" \
"v_exp_f32 v81, v81\n" \
"v_exp_f32 v82, v82\n" \
"v_exp_f32 v83, v83\n" \
"v_exp_f32 v84, v84\n" \
"v_exp_f32 v85, v85\n" \
"v_exp_f32 v86, v86\n" \
"v_exp_f32 v87, v87\n" \
"v_cvt_pk_bf16_f32 v80, v80, v81\n" \
"v_cvt_pk_bf16_f32 v81, v82, v83\n" \
"v_cvt_pk_bf16_f32 v82, v84, v85\n" \
"v_cvt_pk_bf16_f32 v83, v86, v87\n" \
"v_exp_f32 v90, v90\n" \
"s_nop 0\n" \
"v_mfma_f32_16x16x32_bf16 v[84:87], v[96:99], v[80:83], 0\n" \
"v_exp_f32 v91, v91\n" \
"v_mfma_f32_16x16x32_bf16 v[92:95], v[100:103], v[80:83], 0\n" \
"v_cvt_pk_bf16_f32 v26, v90, v91\n" \
"v_mfma_f32_16x16x32_bf16 v[16:19], v[20:23], v[80:83], 0\n" \
".Lq_vr3_%=:\n" \
"s_nop 7\n" \
"s_nop 7\n" \
"v_mfma_f32_16x16x32_bf16 v[84:87], v[96:99], v[80:83], 0\n" \
"v_mfma_f32_16x16x32_bf16 v[92:95], v[100:103], v[80:83], 0\n" \
"v_mfma_f32_16x16x32_bf16 v[16:19], v[20:23], v[80:83], 0\n" \
"v_mfma_f32_16x16x32_bf16 v[16:19], v[20:23], v[26:29], v[16:19]\n" \
"v_mfma_f32_16x16x32_bf16 v[84:87], v[104:107], v[26:29], v[84:87]\n" \
"v_mfma_f32_16x16x32_bf16 v[92:95], v[104:107], v[24:27], v[92:95]\n" \
"v_rcp_f32 v14, v16\n" \
"s_mov_b64 vcc, 0\n" \
"s_mov_b64 s[26:27], 0\n" \
"s_nop 7\n" \
"s_nop 7\n" \
"s_branch .Lq_vr2_%=\n" \
".Lq_v_cold_%=:\n" \
"s_nop 7\n" \
"s_nop 7\n" \
"s_waitcnt vmcnt(0) lgkmcnt(0)\n" \
"s_mov_b32 s53, 0\n" \
".Lq_v_again_%=:\n" \
"s_cmp_lg_u32 s52, 0\n" \
"s_cbranch_scc1 .Lq_v_ok_%=\n" \
"buffer_load_dword v104, v8, s[36:39], s58 offen offset:0 sc1\n" \
"buffer_load_dword v106, v8, s[36:39], s58 offen offset:256 sc1\n" \
"s_waitcnt vmcnt(0)\n" \
"v_min3_u32 v13, v104, v106, v106\n" \
"v_cmp_eq_u32 vcc, 0, v13\n" \
"s_nop 4\n" \
"s_cbranch_vccz .Lq_v_ok_%=\n" \
"s_add_u32 s53, s53, 1\n" \
"s_cmp_lt_u32 s53, 32768\n" \
"s_cbranch_scc1 .Lq_v_again_%=\n" \
"s_mov_b32 s52, 1\n" \
"v_mov_b32 v13, 256\n" \
"v_mov_b32 v14, 1\n" \
"buffer_store_dword v14, v13, s[36:39], 0 offen sc1\n" \
"s_waitcnt vmcnt(0) lgkmcnt(0)\n" \
"s_branch .Lq_v_ok_%=\n" \
".Lq_f_cold_%=:\n" \
"s_nop 7\n" \
"s_nop 7\n" \
"s_waitcnt vmcnt(0) lgkmcnt(0)\n" \
"s_mov_b32 s53, 0\n" \
".Lq_f_again_%=:\n" \
"s_cmp_lg_u32 s52, 0\n" \
"s_cbranch_scc1 .Lq_f_ok_%=\n" \
"buffer_load_dword v30, v15, s[36:39], s80 offen sc1\n" \
"s_waitcnt vmcnt(0)\n" \
"v_cmp_ge_u32_e64 s[96:97], s81, v30\n" \
"s_nop 4\n" \
"s_and_b32 s96, s96, s9\n" \
"s_cbranch_scc0 .Lq_f_ok_%=\n" \
"s_sleep 3\n" \
"s_add_u32 s53, s53, 1\n" \
"s_cmp_lt_u32 s53, 32768\n" \
"s_cbranch_scc1 .Lq_f_again_%=\n" \
"s_mov_b32 s52, 1\n" \
"v_mov_b32 v30, 256\n" \
"v_mov_b32 v31, 1\n" \
"buffer_store_dword v31, v30, s[36:39], 0 offen sc1\n" \
"s_waitcnt vmcnt(0) lgkmcnt(0)\n" \
"s_branch .Lq_f_ok_%=\n" \
".Lq_v2_cold_%=:\n" \
"s_nop 7\n" \
"s_nop 7\n" \
"s_waitcnt vmcnt(0) lgkmcnt(0)\n" \
"s_mov_b32 s53, 0\n" \
".Lq_v2_again_%=:\n" \
"s_cmp_lg_u32 s52, 0\n" \
"s_cbranch_scc1 .Lq_v2_ok_%=\n" \
"s_sleep 1\n" \
"v_xor_b32 v31, 512, v8\n" \
"buffer_load_dword v104, v8, s[36:39], s61 offen offset:0 sc1\n" \
"buffer_load_dword v106, v8, s[36:39], s61 offen offset:256 sc1\n" \
"buffer_load_dword v108, v31, s[36:39], s61 offen offset:0 sc1\n" \
"buffer_load_dword v110, v31, s[36:39], s61 offen offset:256 sc1\n" \
"s_waitcnt vmcnt(1)\n" \
"v_min3_u32 v13, v104, v106, v108\n" \
"s_waitcnt vmcnt(0)\n" \
"v_min3_u32 v13, v13, v110, v110\n" \
"v_cmp_eq_u32 vcc, 0, v13\n" \
"s_nop 4\n" \
"s_cbranch_vccz .Lq_v2_ok_%=\n" \
"s_add_u32 s53, s53, 1\n" \
"s_cmp_lt_u32 s53, 32768\n" \
"s_cbranch_scc1 .Lq_v2_again_%=\n" \
"s_mov_b32 s52, 1\n" \
"v_mov_b32 v13, 256\n" \
"v_mov_b32 v14, 1\n" \
"buffer_store_dword v14, v13, s[36:39], 0 offen sc1\n" \
"s_waitcnt vmcnt(0) lgkmcnt(0)\n" \
"s_branch .Lq_v2_ok_%=\n" \
".Lq_h_cold_%=:\n" \
"s_nop 7\n" \
"s_nop 7\n" \
"s_waitcnt vmcnt(0) lgkmcnt(0)\n" \
"s_mov_b32 s53, 0\n" \
".Lq_h_again_%=:\n" \
"s_cmp_lg_u32 s52, 0\n" \
"s_cbranch_scc1 .Lq_h_ok_%=\n" \
"s_sleep 1\n" \
"buffer_load_dwordx2 v[64:65], v9, s[36:39], s11 offen sc1\n" \
"s_waitcnt vmcnt(0)\n" \
"v_cmp_eq_u32 vcc, 0, v64\n" \
"s_nop 0\n" \
"s_and_b64 vcc, vcc, s[82:83]\n" \
"s_nop 4\n" \
"s_cbranch_vccz .Lq_h_ok_%=\n" \
"s_add_u32 s53, s53, 1\n" \
"s_cmp_lt_u32 s53, 32768\n" \
"s_cbranch_scc1 .Lq_h_again_%=\n" \
"s_mov_b32 s52, 1\n" \
"v_mov_b32 v18, 256\n" \
"v_mov_b32 v19, 1\n" \
"buffer_store_dword v19, v18, s[36:39], 0 offen sc1\n" \
"s_waitcnt vmcnt(0) lgkmcnt(0)\n" \
"s_branch .Lq_h_ok_%=\n" \
".Lq_retry_%=:\n" \
"s_nop 7\n" \
"s_nop 7\n" \
"s_waitcnt vmcnt(0) lgkmcnt(0)\n" \
"ds_read_b128 v[32:35], v1 offset:0\n" \
"ds_read_b128 v[36:39], v1 offset:1024\n" \
"ds_read_b128 v[40:43], v1 offset:2048\n" \
"ds_read_b128 v[44:47], v1 offset:3072\n" \
"ds_read_b128 v[48:51], v2 offset:32768\n" \
"ds_read_b128 v[52:55], v2 offset:33792\n" \
"ds_read_b128 v[56:59], v2 offset:34816\n" \
"ds_read_b128 v[60:63], v2 offset:35840\n" \
"v_mov_b64 v[80:81], 0\n" \
"v_mov_b64 v[82:83], 0\n" \
"v_mov_b64 v[84:85], 0\n" \
"v_mov_b64 v[86:87], 0\n" \
"v_mov_b64 v[88:89], 0\n" \
"v_mov_b64 v[90:91], 0\n" \
"v_mov_b64 v[92:93], 0\n" \
"v_mov_b64 v[94:95], 0\n" \
"v_mov_b32 v13, -1\n" \
"v_add_u32 v14, s62, v3\n" \
"v_xor_b32 v31, 0x400, v11\n" \
"s_max_u32 s53, s49, 1\n" \
"s_sub_u32 s53, s53, 1\n" \
"v_mov_b32 v30, s53\n" \
"s_mov_b32 m0, s42\n" \
"s_memrealtime s[56:57]\n" \
"s_mov_b32 s54, 100\n" \
"s_waitcnt vmcnt(0) lgkmcnt(0)\n" \
"s_barrier\n" \
"s_add_u32 s88, s88, 1\n" \
"s_cmp_lt_u32 s88, 32768\n" \
"s_cbranch_scc1 .Lq_r1_%=\n" \
"s_mov_b32 s52, 1\n" \
"v_mov_b32 v13, 256\n" \
"v_mov_b32 v14, 1\n" \
"buffer_store_dword v14, v13, s[36:39], 0 offen sc1\n" \
"s_waitcnt vmcnt(0) lgkmcnt(0)\n" \
"s_endpgm\n" \
".Lq_r1_%=:\n" \
"s_cmp_lg_u32 s49, 0\n" \
"s_cbranch_scc1 .Lq_r2_%=\n" \
"s_sleep 8\n" \
".Lq_r2_%=:\n" \
"s_branch .Lq_go_%=\n" \
".Lq_end_%=:\n" \
"s_nop 7\n" \
"s_nop 7\n" \
"v_xor_b32 v31, 0x400, v11\n" \
"buffer_store_dwordx4 v[96:99], v11, s[36:39], s95 offen sc1\n" \
"buffer_store_dwordx4 v[100:103], v31, s[36:39], s95 offen sc1\n" \
"s_waitcnt vmcnt(0) lgkmcnt(0)\n" \
"s_barrier\n" \
"v_mov_b32 v31, 64\n" \
"buffer_store_dword v31, v10, s[36:39], s87 offen sc1\n" \
"s_waitcnt vmcnt(0) lgkmcnt(0)\n" \
".Lq_fin_%=:\n" \
"s_endpgm\n"
#define ASM_VO \
"s_nop 7\n" \
"s_nop 7\n" \
"buffer_load_dword v20, v13, s[68:71], 0 offen offset:0\n" \
"buffer_load_dword v21, v13, s[68:71], 0 offen offset:8\n" \
"buffer_load_dword v22, v13, s[68:71], 0 offen offset:16\n" \
"buffer_load_dword v23, v13, s[68:71], 0 offen offset:24\n" \
"v_mov_b32 v16, 0\n" \
"v_mov_b32 v17, 0\n" \
"v_mov_b32 v18, 0\n" \
"v_mov_b32 v19, 0\n" \
"s_mov_b32 s54, 0\n" \
"buffer_store_dword v16, v7, s[36:39], s54 offen sc1\n" \
"s_mov_b32 s54, 131072\n" \
"buffer_store_dword v16, v7, s[36:39], s54 offen sc1\n" \
"s_mov_b32 s54, 65536\n" \
"buffer_store_dwordx4 v[16:19], v8, s[36:39], s54 offen sc1\n" \
"s_mov_b32 s54, 262144\n" \
"buffer_store_dword v16, v7, s[36:39], s54 offen sc1\n" \
"s_mov_b32 s54, 131072\n" \
"buffer_store_dwordx4 v[16:19], v8, s[36:39], s54 offen sc1\n" \
"s_mov_b32 s54, 393216\n" \
"buffer_store_dword v16, v7, s[36:39], s54 offen sc1\n" \
"s_mov_b32 s54, 196608\n" \
"buffer_store_dwordx4 v[16:19], v8, s[36:39], s54 offen sc1\n" \
"s_mul_i32 s62, s48, 40960\n" \
"v_add_u32 v32, 40960, v3\n" \
"v_add_u32 v33, 81920, v3\n" \
"v_add_u32 v34, 122880, v3\n" \
"s_add_u32 m0, s62, 0\n" \
"s_mov_b32 s54, 0\n" \
"buffer_load_dwordx4 v1, s[40:43], s54 offen nt lds\n" \
"s_add_u32 m0, s62, 1024\n" \
"s_mov_b32 s54, 65536\n" \
"buffer_load_dwordx4 v1, s[40:43], s54 offen nt lds\n" \
"s_add_u32 m0, s62, 2048\n" \
"s_mov_b32 s54, 131072\n" \
"buffer_load_dwordx4 v1, s[40:43], s54 offen nt lds\n" \
"s_add_u32 m0, s62, 3072\n" \
"s_mov_b32 s54, 196608\n" \
"buffer_load_dwordx4 v1, s[40:43], s54 offen nt lds\n" \
"s_add_u32 m0, s62, 4096\n" \
"s_mov_b32 s54, 262144\n" \
"buffer_load_dwordx4 v1, s[40:43], s54 offen nt lds\n" \
"s_add_u32 m0, s62, 5120\n" \
"s_mov_b32 s54, 327680\n" \
"buffer_load_dwordx4 v1, s[40:43], s54 offen nt lds\n" \
"s_add_u32 m0, s62, 6144\n" \
"s_mov_b32 s54, 393216\n" \
"buffer_load_dwordx4 v1, s[40:43], s54 offen nt lds\n" \
"s_add_u32 m0, s62, 7168\n" \
"s_mov_b32 s54, 458752\n" \
"buffer_load_dwordx4 v1, s[40:43], s54 offen nt lds\n" \
"s_add_u32 m0, s62, 8192\n" \
"s_mov_b32 s54, 524288\n" \
"buffer_load_dwordx4 v1, s[40:43], s54 offen nt lds\n" \
"s_add_u32 m0, s62, 9216\n" \
"s_mov_b32 s54, 589824\n" \
"buffer_load_dwordx4 v1, s[40:43], s54 offen nt lds\n" \
"s_add_u32 m0, s62, 10240\n" \
"s_mov_b32 s54, 655360\n" \
"buffer_load_dwordx4 v1, s[40:43], s54 offen nt lds\n" \
"s_add_u32 m0, s62, 11264\n" \
"s_mov_b32 s54, 720896\n" \
"buffer_load_dwordx4 v1, s[40:43], s54 offen nt lds\n" \
"s_add_u32 m0, s62, 12288\n" \
"s_mov_b32 s54, 786432\n" \
"buffer_load_dwordx4 v1, s[40:43], s54 offen nt lds\n" \
"s_add_u32 m0, s62, 13312\n" \
"s_mov_b32 s54, 851968\n" \
"buffer_load_dwordx4 v1, s[40:43], s54 offen nt lds\n" \
"s_add_u32 m0, s62, 14336\n" \
"s_mov_b32 s54, 917504\n" \
"buffer_load_dwordx4 v1, s[40:43], s54 offen nt lds\n" \
"s_add_u32 m0, s62, 15360\n" \
"s_mov_b32 s54, 983040\n" \
"buffer_load_dwordx4 v1, s[40:43], s54 offen nt lds\n" \
"s_add_u32 m0, s62, 16384\n" \
"s_mov_b32 s54, 1048576\n" \
"buffer_load_dwordx4 v1, s[40:43], s54 offen nt lds\n" \
"s_add_u32 m0, s62, 17408\n" \
"s_mov_b32 s54, 1114112\n" \
"buffer_load_dwordx4 v1, s[40:43], s54 offen nt lds\n" \
"s_add_u32 m0, s62, 18432\n" \
"s_mov_b32 s54, 1179648\n" \
"buffer_load_dwordx4 v1, s[40:43], s54 offen nt lds\n" \
"s_add_u32 m0, s62, 19456\n" \
"s_mov_b32 s54, 1245184\n" \
"buffer_load_dwordx4 v1, s[40:43], s54 offen nt lds\n" \
"s_add_u32 m0, s62, 20480\n" \
"s_mov_b32 s54, 1310720\n" \
"buffer_load_dwordx4 v1, s[40:43], s54 offen nt lds\n" \
"s_add_u32 m0, s62, 21504\n" \
"s_mov_b32 s54, 1376256\n" \
"buffer_load_dwordx4 v1, s[40:43], s54 offen nt lds\n" \
"s_add_u32 m0, s62, 22528\n" \
"s_mov_b32 s54, 1441792\n" \
"buffer_load_dwordx4 v1, s[40:43], s54 offen nt lds\n" \
"s_add_u32 m0, s62, 23552\n" \
"s_mov_b32 s54, 1507328\n" \
"buffer_load_dwordx4 v1, s[40:43], s54 offen nt lds\n" \
"s_add_u32 m0, s62, 24576\n" \
"s_mov_b32 s54, 1572864\n" \
"buffer_load_dwordx4 v1, s[40:43], s54 offen nt lds\n" \
"s_add_u32 m0, s62, 25600\n" \
"s_mov_b32 s54, 1638400\n" \
"buffer_load_dwordx4 v1, s[40:43], s54 offen nt lds\n" \
"s_add_u32 m0, s62, 26624\n" \
"s_mov_b32 s54, 1703936\n" \
"buffer_load_dwordx4 v1, s[40:43], s54 offen nt lds\n" \
"s_add_u32 m0, s62, 27648\n" \
"s_mov_b32 s54, 1769472\n" \
"buffer_load_dwordx4 v1, s[40:43], s54 offen nt lds\n" \
"s_add_u32 m0, s62, 28672\n" \
"s_mov_b32 s54, 1835008\n" \
"buffer_load_dwordx4 v1, s[40:43], s54 offen nt lds\n" \
"s_add_u32 m0, s62, 29696\n" \
"s_mov_b32 s54, 1900544\n" \
"buffer_load_dwordx4 v1, s[40:43], s54 offen nt lds\n" \
"s_add_u32 m0, s62, 30720\n" \
"s_mov_b32 s54, 1966080\n" \
"buffer_load_dwordx4 v1, s[40:43], s54 offen nt lds\n" \
"s_add_u32 m0, s62, 31744\n" \
"s_mov_b32 s54, 2031616\n" \
"buffer_load_dwordx4 v1, s[40:43], s54 offen nt lds\n" \
"s_add_u32 m0, s62, 32768\n" \
"s_mov_b32 s54, 0\n" \
"buffer_load_dwordx4 v2, s[64:67], s54 offen lds\n" \
"s_add_u32 m0, s62, 33792\n" \
"s_mov_b32 s54, 32768\n" \
"buffer_load_dwordx4 v2, s[64:67], s54 offen lds\n" \
"s_add_u32 m0, s62, 34816\n" \
"s_mov_b32 s54, 65536\n" \
"buffer_load_dwordx4 v2, s[64:67], s54 offen lds\n" \
"s_add_u32 m0, s62, 35840\n" \
"s_mov_b32 s54, 98304\n" \
"buffer_load_dwordx4 v2, s[64:67], s54 offen lds\n" \
"s_add_u32 m0, s62, 36864\n" \
"s_mov_b32 s54, 131072\n" \
"buffer_load_dwordx4 v2, s[64:67], s54 offen lds\n" \
"s_add_u32 m0, s62, 37888\n" \
"s_mov_b32 s54, 163840\n" \
"buffer_load_dwordx4 v2, s[64:67], s54 offen lds\n" \
"s_add_u32 m0, s62, 38912\n" \
"s_mov_b32 s54, 196608\n" \
"buffer_load_dwordx4 v2, s[64:67], s54 offen lds\n" \
"s_add_u32 m0, s62, 39936\n" \
"s_mov_b32 s54, 229376\n" \
"buffer_load_dwordx4 v2, s[64:67], s54 offen lds\n" \
"s_waitcnt vmcnt(32)\n" \
"s_barrier\n" \
"ds_read_b64_tr_b16 v[128:129], v3 offset:0\n" \
"ds_read_b64_tr_b16 v[130:131], v3 offset:512\n" \
"ds_read_b64_tr_b16 v[132:133], v3 offset:4096\n" \
"ds_read_b64_tr_b16 v[134:135], v3 offset:4608\n" \
"ds_read_b64_tr_b16 v[160:161], v32 offset:0\n" \
"ds_read_b64_tr_b16 v[162:163], v32 offset:512\n" \
"ds_read_b64_tr_b16 v[164:165], v32 offset:4096\n" \
"ds_read_b64_tr_b16 v[166:167], v32 offset:4608\n" \
"ds_read_b64_tr_b16 v[192:193], v33 offset:0\n" \
"ds_read_b64_tr_b16 v[194:195], v33 offset:512\n" \
"ds_read_b64_tr_b16 v[196:197], v33 offset:4096\n" \
"ds_read_b64_tr_b16 v[198:199], v33 offset:4608\n" \
"ds_read_b64_tr_b16 v[224:225], v34 offset:0\n" \
"ds_read_b64_tr_b16 v[226:227], v34 offset:512\n" \
"ds_read_b64_tr_b16 v[228:229], v34 offset:4096\n" \
"ds_read_b64_tr_b16 v[230:231], v34 offset:4608\n" \
"s_waitcnt lgkmcnt(0)\n" \
"s_barrier\n" \
"s_add_u32 m0, s62, 0\n" \
"s_mov_b32 s54, 262144\n" \
"buffer_load_dwordx4 v2, s[64:67], s54 offen lds\n" \
"s_add_u32 m0, s62, 1024\n" \
"s_mov_b32 s54, 294912\n" \
"buffer_load_dwordx4 v2, s[64:67], s54 offen lds\n" \
"s_add_u32 m0, s62, 2048\n" \
"s_mov_b32 s54, 327680\n" \
"buffer_load_dwordx4 v2, s[64:67], s54 offen lds\n" \
"s_add_u32 m0, s62, 3072\n" \
"s_mov_b32 s54, 360448\n" \
"buffer_load_dwordx4 v2, s[64:67], s54 offen lds\n" \
"s_add_u32 m0, s62, 4096\n" \
"s_mov_b32 s54, 393216\n" \
"buffer_load_dwordx4 v2, s[64:67], s54 offen lds\n" \
"s_add_u32 m0, s62, 5120\n" \
"s_mov_b32 s54, 425984\n" \
"buffer_load_dwordx4 v2, s[64:67], s54 offen lds\n" \
"s_add_u32 m0, s62, 6144\n" \
"s_mov_b32 s54, 458752\n" \
"buffer_load_dwordx4 v2, s[64:67], s54 offen lds\n" \
"s_add_u32 m0, s62, 7168\n" \
"s_mov_b32 s54, 491520\n" \
"buffer_load_dwordx4 v2, s[64:67], s54 offen lds\n" \
"s_waitcnt vmcnt(32)\n" \
"s_barrier\n" \
"ds_read_b64_tr_b16 v[136:137], v3 offset:8192\n" \
"ds_read_b64_tr_b16 v[138:139], v3 offset:8704\n" \
"ds_read_b64_tr_b16 v[140:141], v3 offset:12288\n" \
"ds_read_b64_tr_b16 v[142:143], v3 offset:12800\n" \
"ds_read_b64_tr_b16 v[168:169], v32 offset:8192\n" \
"ds_read_b64_tr_b16 v[170:171], v32 offset:8704\n" \
"ds_read_b64_tr_b16 v[172:173], v32 offset:12288\n" \
"ds_read_b64_tr_b16 v[174:175], v32 offset:12800\n" \
"ds_read_b64_tr_b16 v[200:201], v33 offset:8192\n" \
"ds_read_b64_tr_b16 v[202:203], v33 offset:8704\n" \
"ds_read_b64_tr_b16 v[204:205], v33 offset:12288\n" \
"ds_read_b64_tr_b16 v[206:207], v33 offset:12800\n" \
"ds_read_b64_tr_b16 v[232:233], v34 offset:8192\n" \
"ds_read_b64_tr_b16 v[234:235], v34 offset:8704\n" \
"ds_read_b64_tr_b16 v[236:237], v34 offset:12288\n" \
"ds_read_b64_tr_b16 v[238:239], v34 offset:12800\n" \
"s_waitcnt lgkmcnt(0)\n" \
"s_barrier\n" \
"s_add_u32 m0, s62, 8192\n" \
"s_mov_b32 s54, 524288\n" \
"buffer_load_dwordx4 v2, s[64:67], s54 offen lds\n" \
"s_add_u32 m0, s62, 9216\n" \
"s_mov_b32 s54, 557056\n" \
"buffer_load_dwordx4 v2, s[64:67], s54 offen lds\n" \
"s_add_u32 m0, s62, 10240\n" \
"s_mov_b32 s54, 589824\n" \
"buffer_load_dwordx4 v2, s[64:67], s54 offen lds\n" \
"s_add_u32 m0, s62, 11264\n" \
"s_mov_b32 s54, 622592\n" \
"buffer_load_dwordx4 v2, s[64:67], s54 offen lds\n" \
"s_add_u32 m0, s62, 12288\n" \
"s_mov_b32 s54, 655360\n" \
"buffer_load_dwordx4 v2, s[64:67], s54 offen lds\n" \
"s_add_u32 m0, s62, 13312\n" \
"s_mov_b32 s54, 688128\n" \
"buffer_load_dwordx4 v2, s[64:67], s54 offen lds\n" \
"s_add_u32 m0, s62, 14336\n" \
"s_mov_b32 s54, 720896\n" \
"buffer_load_dwordx4 v2, s[64:67], s54 offen lds\n" \
"s_add_u32 m0, s62, 15360\n" \
"s_mov_b32 s54, 753664\n" \
"buffer_load_dwordx4 v2, s[64:67], s54 offen lds\n" \
"s_waitcnt vmcnt(32)\n" \
"s_barrier\n" \
"ds_read_b64_tr_b16 v[144:145], v3 offset:16384\n" \
"ds_read_b64_tr_b16 v[146:147], v3 offset:16896\n" \
"ds_read_b64_tr_b16 v[148:149], v3 offset:20480\n" \
"ds_read_b64_tr_b16 v[150:151], v3 offset:20992\n" \
"ds_read_b64_tr_b16 v[176:177], v32 offset:16384\n" \
"ds_read_b64_tr_b16 v[178:179], v32 offset:16896\n" \
"ds_read_b64_tr_b16 v[180:181], v32 offset:20480\n" \
"ds_read_b64_tr_b16 v[182:183], v32 offset:20992\n" \
"ds_read_b64_tr_b16 v[208:209], v33 offset:16384\n" \
"ds_read_b64_tr_b16 v[210:211], v33 offset:16896\n" \
"ds_read_b64_tr_b16 v[212:213], v33 offset:20480\n" \
"ds_read_b64_tr_b16 v[214:215], v33 offset:20992\n" \
"ds_read_b64_tr_b16 v[240:241], v34 offset:16384\n" \
"ds_read_b64_tr_b16 v[242:243], v34 offset:16896\n" \
"ds_read_b64_tr_b16 v[244:245], v34 offset:20480\n" \
"ds_read_b64_tr_b16 v[246:247], v34 offset:20992\n" \
"s_waitcnt lgkmcnt(0)\n" \
"s_barrier\n" \
"s_add_u32 m0, s62, 16384\n" \
"s_mov_b32 s54, 786432\n" \
"buffer_load_dwordx4 v2, s[64:67], s54 offen lds\n" \
"s_add_u32 m0, s62, 17408\n" \
"s_mov_b32 s54, 819200\n" \
"buffer_load_dwordx4 v2, s[64:67], s54 offen lds\n" \
"s_add_u32 m0, s62, 18432\n" \
"s_mov_b32 s54, 851968\n" \
"buffer_load_dwordx4 v2, s[64:67], s54 offen lds\n" \
"s_add_u32 m0, s62, 19456\n" \
"s_mov_b32 s54, 884736\n" \
"buffer_load_dwordx4 v2, s[64:67], s54 offen lds\n" \
"s_add_u32 m0, s62, 20480\n" \
"s_mov_b32 s54, 917504\n" \
"buffer_load_dwordx4 v2, s[64:67], s54 offen lds\n" \
"s_add_u32 m0, s62, 21504\n" \
"s_mov_b32 s54, 950272\n" \
"buffer_load_dwordx4 v2, s[64:67], s54 offen lds\n" \
"s_add_u32 m0, s62, 22528\n" \
"s_mov_b32 s54, 983040\n" \
"buffer_load_dwordx4 v2, s[64:67], s54 offen lds\n" \
"s_add_u32 m0, s62, 23552\n" \
"s_mov_b32 s54, 1015808\n" \
"buffer_load_dwordx4 v2, s[64:67], s54 offen lds\n" \
"s_waitcnt vmcnt(32)\n" \
"s_barrier\n" \
"ds_read_b64_tr_b16 v[152:153], v3 offset:24576\n" \
"ds_read_b64_tr_b16 v[154:155], v3 offset:25088\n" \
"ds_read_b64_tr_b16 v[156:157], v3 offset:28672\n" \
"ds_read_b64_tr_b16 v[158:159], v3 offset:29184\n" \
"ds_read_b64_tr_b16 v[184:185], v32 offset:24576\n" \
"ds_read_b64_tr_b16 v[186:187], v32 offset:25088\n" \
"ds_read_b64_tr_b16 v[188:189], v32 offset:28672\n" \
"ds_read_b64_tr_b16 v[190:191], v32 offset:29184\n" \
"ds_read_b64_tr_b16 v[216:217], v33 offset:24576\n" \
"ds_read_b64_tr_b16 v[218:219], v33 offset:25088\n" \
"ds_read_b64_tr_b16 v[220:221], v33 offset:28672\n" \
"ds_read_b64_tr_b16 v[222:223], v33 offset:29184\n" \
"ds_read_b64_tr_b16 v[248:249], v34 offset:24576\n" \
"ds_read_b64_tr_b16 v[250:251], v34 offset:25088\n" \
"ds_read_b64_tr_b16 v[252:253], v34 offset:28672\n" \
"ds_read_b64_tr_b16 v[254:255], v34 offset:29184\n" \
"s_waitcnt lgkmcnt(0)\n" \
"s_barrier\n" \
"s_add_u32 m0, s62, 24576\n" \
"s_mov_b32 s54, 1048576\n" \
"buffer_load_dwordx4 v2, s[64:67], s54 offen lds\n" \
"s_add_u32 m0, s62, 25600\n" \
"s_mov_b32 s54, 1081344\n" \
"buffer_load_dwordx4 v2, s[64:67], s54 offen lds\n" \
"s_add_u32 m0, s62, 26624\n" \
"s_mov_b32 s54, 1114112\n" \
"buffer_load_dwordx4 v2, s[64:67], s54 offen lds\n" \
"s_add_u32 m0, s62, 27648\n" \
"s_mov_b32 s54, 1146880\n" \
"buffer_load_dwordx4 v2, s[64:67], s54 offen lds\n" \
"s_add_u32 m0, s62, 28672\n" \
"s_mov_b32 s54, 1179648\n" \
"buffer_load_dwordx4 v2, s[64:67], s54 offen lds\n" \
"s_add_u32 m0, s62, 29696\n" \
"s_mov_b32 s54, 1212416\n" \
"buffer_load_dwordx4 v2, s[64:67], s54 offen lds\n" \
"s_add_u32 m0, s62, 30720\n" \
"s_mov_b32 s54, 1245184\n" \
"buffer_load_dwordx4 v2, s[64:67], s54 offen lds\n" \
"s_add_u32 m0, s62, 31744\n" \
"s_mov_b32 s54, 1277952\n" \
"buffer_load_dwordx4 v2, s[64:67], s54 offen lds\n" \
"s_waitcnt vmcnt(36)\n" \
"ds_read_b64_tr_b16 a[0:1], v4 offset:32768\n" \
"ds_read_b64_tr_b16 a[2:3], v4 offset:32896\n" \
"ds_read_b64_tr_b16 a[4:5], v4 offset:34816\n" \
"ds_read_b64_tr_b16 a[6:7], v4 offset:34944\n" \
"ds_read_b64_tr_b16 a[8:9], v4 offset:32800\n" \
"ds_read_b64_tr_b16 a[10:11], v4 offset:32928\n" \
"ds_read_b64_tr_b16 a[12:13], v4 offset:34848\n" \
"ds_read_b64_tr_b16 a[14:15], v4 offset:34976\n" \
"s_waitcnt lgkmcnt(0)\n" \
"s_add_u32 m0, s62, 32768\n" \
"s_mov_b32 s54, 1310720\n" \
"buffer_load_dwordx4 v2, s[64:67], s54 offen lds\n" \
"s_add_u32 m0, s62, 33792\n" \
"s_mov_b32 s54, 1343488\n" \
"buffer_load_dwordx4 v2, s[64:67], s54 offen lds\n" \
"s_add_u32 m0, s62, 34816\n" \
"s_mov_b32 s54, 1376256\n" \
"buffer_load_dwordx4 v2, s[64:67], s54 offen lds\n" \
"s_add_u32 m0, s62, 35840\n" \
"s_mov_b32 s54, 1409024\n" \
"buffer_load_dwordx4 v2, s[64:67], s54 offen lds\n" \
"s_waitcnt vmcnt(36)\n" \
"ds_read_b64_tr_b16 a[16:17], v4 offset:36864\n" \
"ds_read_b64_tr_b16 a[18:19], v4 offset:36992\n" \
"ds_read_b64_tr_b16 a[20:21], v4 offset:38912\n" \
"ds_read_b64_tr_b16 a[22:23], v4 offset:39040\n" \
"ds_read_b64_tr_b16 a[24:25], v4 offset:36896\n" \
"ds_read_b64_tr_b16 a[26:27], v4 offset:37024\n" \
"ds_read_b64_tr_b16 a[28:29], v4 offset:38944\n" \
"ds_read_b64_tr_b16 a[30:31], v4 offset:39072\n" \
"s_waitcnt lgkmcnt(0)\n" \
"s_add_u32 m0, s62, 36864\n" \
"s_mov_b32 s54, 1441792\n" \
"buffer_load_dwordx4 v2, s[64:67], s54 offen lds\n" \
"s_add_u32 m0, s62, 37888\n" \
"s_mov_b32 s54, 1474560\n" \
"buffer_load_dwordx4 v2, s[64:67], s54 offen lds\n" \
"s_add_u32 m0, s62, 38912\n" \
"s_mov_b32 s54, 1507328\n" \
"buffer_load_dwordx4 v2, s[64:67], s54 offen lds\n" \
"s_add_u32 m0, s62, 39936\n" \
"s_mov_b32 s54, 1540096\n" \
"buffer_load_dwordx4 v2, s[64:67], s54 offen lds\n" \
"s_waitcnt vmcnt(36)\n" \
"ds_read_b64_tr_b16 a[32:33], v4 offset:0\n" \
"ds_read_b64_tr_b16 a[34:35], v4 offset:128\n" \
"ds_read_b64_tr_b16 a[36:37], v4 offset:2048\n" \
"ds_read_b64_tr_b16 a[38:39], v4 offset:2176\n" \
"ds_read_b64_tr_b16 a[40:41], v4 offset:32\n" \
"ds_read_b64_tr_b16 a[42:43], v4 offset:160\n" \
"ds_read_b64_tr_b16 a[44:45], v4 offset:2080\n" \
"ds_read_b64_tr_b16 a[46:47], v4 offset:2208\n" \
"s_waitcnt lgkmcnt(0)\n" \
"s_add_u32 m0, s62, 0\n" \
"s_mov_b32 s54, 1572864\n" \
"buffer_load_dwordx4 v2, s[64:67], s54 offen lds\n" \
"s_add_u32 m0, s62, 1024\n" \
"s_mov_b32 s54, 1605632\n" \
"buffer_load_dwordx4 v2, s[64:67], s54 offen lds\n" \
"s_add_u32 m0, s62, 2048\n" \
"s_mov_b32 s54, 1638400\n" \
"buffer_load_dwordx4 v2, s[64:67], s54 offen lds\n" \
"s_add_u32 m0, s62, 3072\n" \
"s_mov_b32 s54, 1671168\n" \
"buffer_load_dwordx4 v2, s[64:67], s54 offen lds\n" \
"s_waitcnt vmcnt(36)\n" \
"ds_read_b64_tr_b16 a[48:49], v4 offset:4096\n" \
"ds_read_b64_tr_b16 a[50:51], v4 offset:4224\n" \
"ds_read_b64_tr_b16 a[52:53], v4 offset:6144\n" \
"ds_read_b64_tr_b16 a[54:55], v4 offset:6272\n" \
"ds_read_b64_tr_b16 a[56:57], v4 offset:4128\n" \
"ds_read_b64_tr_b16 a[58:59], v4 offset:4256\n" \
"ds_read_b64_tr_b16 a[60:61], v4 offset:6176\n" \
"ds_read_b64_tr_b16 a[62:63], v4 offset:6304\n" \
"s_waitcnt lgkmcnt(0)\n" \
"s_add_u32 m0, s62, 4096\n" \
"s_mov_b32 s54, 1703936\n" \
"buffer_load_dwordx4 v2, s[64:67], s54 offen lds\n" \
"s_add_u32 m0, s62, 5120\n" \
"s_mov_b32 s54, 1736704\n" \
"buffer_load_dwordx4 v2, s[64:67], s54 offen lds\n" \
"s_add_u32 m0, s62, 6144\n" \
"s_mov_b32 s54, 1769472\n" \
"buffer_load_dwordx4 v2, s[64:67], s54 offen lds\n" \
"s_add_u32 m0, s62, 7168\n" \
"s_mov_b32 s54, 1802240\n" \
"buffer_load_dwordx4 v2, s[64:67], s54 offen lds\n" \
"s_waitcnt vmcnt(36)\n" \
"ds_read_b64_tr_b16 a[64:65], v4 offset:8192\n" \
"ds_read_b64_tr_b16 a[66:67], v4 offset:8320\n" \
"ds_read_b64_tr_b16 a[68:69], v4 offset:10240\n" \
"ds_read_b64_tr_b16 a[70:71], v4 offset:10368\n" \
"ds_read_b64_tr_b16 a[72:73], v4 offset:8224\n" \
"ds_read_b64_tr_b16 a[74:75], v4 offset:8352\n" \
"ds_read_b64_tr_b16 a[76:77], v4 offset:10272\n" \
"ds_read_b64_tr_b16 a[78:79], v4 offset:10400\n" \
"s_waitcnt lgkmcnt(0)\n" \
"s_add_u32 m0, s62, 8192\n" \
"s_mov_b32 s54, 1835008\n" \
"buffer_load_dwordx4 v2, s[64:67], s54 offen lds\n" \
"s_add_u32 m0, s62, 9216\n" \
"s_mov_b32 s54, 1867776\n" \
"buffer_load_dwordx4 v2, s[64:67], s54 offen lds\n" \
"s_add_u32 m0, s62, 10240\n" \
"s_mov_b32 s54, 1900544\n" \
"buffer_load_dwordx4 v2, s[64:67], s54 offen lds\n" \
"s_add_u32 m0, s62, 11264\n" \
"s_mov_b32 s54, 1933312\n" \
"buffer_load_dwordx4 v2, s[64:67], s54 offen lds\n" \
"s_waitcnt vmcnt(36)\n" \
"ds_read_b64_tr_b16 a[80:81], v4 offset:12288\n" \
"ds_read_b64_tr_b16 a[82:83], v4 offset:12416\n" \
"ds_read_b64_tr_b16 a[84:85], v4 offset:14336\n" \
"ds_read_b64_tr_b16 a[86:87], v4 offset:14464\n" \
"ds_read_b64_tr_b16 a[88:89], v4 offset:12320\n" \
"ds_read_b64_tr_b16 a[90:91], v4 offset:12448\n" \
"ds_read_b64_tr_b16 a[92:93], v4 offset:14368\n" \
"ds_read_b64_tr_b16 a[94:95], v4 offset:14496\n" \
"s_waitcnt lgkmcnt(0)\n" \
"s_add_u32 m0, s62, 12288\n" \
"s_mov_b32 s54, 1966080\n" \
"buffer_load_dwordx4 v2, s[64:67], s54 offen lds\n" \
"s_add_u32 m0, s62, 13312\n" \
"s_mov_b32 s54, 1998848\n" \
"buffer_load_dwordx4 v2, s[64:67], s54 offen lds\n" \
"s_add_u32 m0, s62, 14336\n" \
"s_mov_b32 s54, 2031616\n" \
"buffer_load_dwordx4 v2, s[64:67], s54 offen lds\n" \
"s_add_u32 m0, s62, 15360\n" \
"s_mov_b32 s54, 2064384\n" \
"buffer_load_dwordx4 v2, s[64:67], s54 offen lds\n" \
"s_waitcnt vmcnt(36)\n" \
"ds_read_b64_tr_b16 a[96:97], v4 offset:16384\n" \
"ds_read_b64_tr_b16 a[98:99], v4 offset:16512\n" \
"ds_read_b64_tr_b16 a[100:101], v4 offset:18432\n" \
"ds_read_b64_tr_b16 a[102:103], v4 offset:18560\n" \
"ds_read_b64_tr_b16 a[104:105], v4 offset:16416\n" \
"ds_read_b64_tr_b16 a[106:107], v4 offset:16544\n" \
"ds_read_b64_tr_b16 a[108:109], v4 offset:18464\n" \
"ds_read_b64_tr_b16 a[110:111], v4 offset:18592\n" \
"s_waitcnt vmcnt(32)\n" \
"ds_read_b64_tr_b16 a[112:113], v4 offset:20480\n" \
"ds_read_b64_tr_b16 a[114:115], v4 offset:20608\n" \
"ds_read_b64_tr_b16 a[116:117], v4 offset:22528\n" \
"ds_read_b64_tr_b16 a[118:119], v4 offset:22656\n" \
"ds_read_b64_tr_b16 a[120:121], v4 offset:20512\n" \
"ds_read_b64_tr_b16 a[122:123], v4 offset:20640\n" \
"ds_read_b64_tr_b16 a[124:125], v4 offset:22560\n" \
"ds_read_b64_tr_b16 a[126:127], v4 offset:22688\n" \
"s_waitcnt vmcnt(28)\n" \
"ds_read_b64_tr_b16 a[128:129], v4 offset:24576\n" \
"ds_read_b64_tr_b16 a[130:131], v4 offset:24704\n" \
"ds_read_b64_tr_b16 a[132:133], v4 offset:26624\n" \
"ds_read_b64_tr_b16 a[134:135], v4 offset:26752\n" \
"ds_read_b64_tr_b16 a[136:137], v4 offset:24608\n" \
"ds_read_b64_tr_b16 a[138:139], v4 offset:24736\n" \
"ds_read_b64_tr_b16 a[140:141], v4 offset:26656\n" \
"ds_read_b64_tr_b16 a[142:143], v4 offset:26784\n" \
"s_waitcnt vmcnt(24)\n" \
"ds_read_b64_tr_b16 a[144:145], v4 offset:28672\n" \
"ds_read_b64_tr_b16 a[146:147], v4 offset:28800\n" \
"ds_read_b64_tr_b16 a[148:149], v4 offset:30720\n" \
"ds_read_b64_tr_b16 a[150:151], v4 offset:30848\n" \
"ds_read_b64_tr_b16 a[152:153], v4 offset:28704\n" \
"ds_read_b64_tr_b16 a[154:155], v4 offset:28832\n" \
"ds_read_b64_tr_b16 a[156:157], v4 offset:30752\n" \
"ds_read_b64_tr_b16 a[158:159], v4 offset:30880\n" \
"s_waitcnt vmcnt(20)\n" \
"ds_read_b64_tr_b16 a[160:161], v4 offset:32768\n" \
"ds_read_b64_tr_b16 a[162:163], v4 offset:32896\n" \
"ds_read_b64_tr_b16 a[164:165], v4 offset:34816\n" \
"ds_read_b64_tr_b16 a[166:167], v4 offset:34944\n" \
"ds_read_b64_tr_b16 a[168:169], v4 offset:32800\n" \
"ds_read_b64_tr_b16 a[170:171], v4 offset:32928\n" \
"ds_read_b64_tr_b16 a[172:173], v4 offset:34848\n" \
"ds_read_b64_tr_b16 a[174:175], v4 offset:34976\n" \
"s_waitcnt vmcnt(16)\n" \
"ds_read_b64_tr_b16 a[176:177], v4 offset:36864\n" \
"ds_read_b64_tr_b16 a[178:179], v4 offset:36992\n" \
"ds_read_b64_tr_b16 a[180:181], v4 offset:38912\n" \
"ds_read_b64_tr_b16 a[182:183], v4 offset:39040\n" \
"ds_read_b64_tr_b16 a[184:185], v4 offset:36896\n" \
"ds_read_b64_tr_b16 a[186:187], v4 offset:37024\n" \
"ds_read_b64_tr_b16 a[188:189], v4 offset:38944\n" \
"ds_read_b64_tr_b16 a[190:191], v4 offset:39072\n" \
"s_waitcnt vmcnt(12)\n" \
"ds_read_b64_tr_b16 a[192:193], v4 offset:0\n" \
"ds_read_b64_tr_b16 a[194:195], v4 offset:128\n" \
"ds_read_b64_tr_b16 a[196:197], v4 offset:2048\n" \
"ds_read_b64_tr_b16 a[198:199], v4 offset:2176\n" \
"ds_read_b64_tr_b16 a[200:201], v4 offset:32\n" \
"ds_read_b64_tr_b16 a[202:203], v4 offset:160\n" \
"ds_read_b64_tr_b16 a[204:205], v4 offset:2080\n" \
"ds_read_b64_tr_b16 a[206:207], v4 offset:2208\n" \
"s_waitcnt vmcnt(8)\n" \
"ds_read_b64_tr_b16 a[208:209], v4 offset:4096\n" \
"ds_read_b64_tr_b16 a[210:211], v4 offset:4224\n" \
"ds_read_b64_tr_b16 a[212:213], v4 offset:6144\n" \
"ds_read_b64_tr_b16 a[214:215], v4 offset:6272\n" \
"ds_read_b64_tr_b16 a[216:217], v4 offset:4128\n" \
"ds_read_b64_tr_b16 a[218:219], v4 offset:4256\n" \
"ds_read_b64_tr_b16 a[220:221], v4 offset:6176\n" \
"ds_read_b64_tr_b16 a[222:223], v4 offset:6304\n" \
"s_waitcnt vmcnt(4)\n" \
"ds_read_b64_tr_b16 a[224:225], v4 offset:8192\n" \
"ds_read_b64_tr_b16 a[226:227], v4 offset:8320\n" \
"ds_read_b64_tr_b16 a[228:229], v4 offset:10240\n" \
"ds_read_b64_tr_b16 a[230:231], v4 offset:10368\n" \
"ds_read_b64_tr_b16 a[232:233], v4 offset:8224\n" \
"ds_read_b64_tr_b16 a[234:235], v4 offset:8352\n" \
"ds_read_b64_tr_b16 a[236:237], v4 offset:10272\n" \
"ds_read_b64_tr_b16 a[238:239], v4 offset:10400\n" \
"s_waitcnt vmcnt(0)\n" \
"ds_read_b64_tr_b16 a[240:241], v4 offset:12288\n" \
"ds_read_b64_tr_b16 a[242:243], v4 offset:12416\n" \
"ds_read_b64_tr_b16 a[244:245], v4 offset:14336\n" \
"ds_read_b64_tr_b16 a[246:247], v4 offset:14464\n" \
"ds_read_b64_tr_b16 a[248:249], v4 offset:12320\n" \
"ds_read_b64_tr_b16 a[250:251], v4 offset:12448\n" \
"ds_read_b64_tr_b16 a[252:253], v4 offset:14368\n" \
"ds_read_b64_tr_b16 a[254:255], v4 offset:14496\n" \
"s_waitcnt vmcnt(0) lgkmcnt(0)\n" \
"s_barrier\n" \
"s_getpc_b64 s[18:19]\n" \
".Lv_pc_%=:\n" \
"v_mbcnt_lo_u32_b32 v40, -1, 0\n" \
"v_mbcnt_hi_u32_b32 v40, -1, v40\n" \
"v_lshl_add_u32 v40, s48, 6, v40\n" \
"v_lshlrev_b32 v40, 6, v40\n" \
"v_min_u32 v40, .Lv_fin_%=-.Lv_pc_%=, v40\n" \
"global_load_dword v41, v40, s[18:19]\n" \
"s_mov_b32 s49, 0\n" \
"s_mov_b32 s52, 0\n" \
"s_mov_b32 s88, 0\n" \
"s_mov_b32 s76, 0\n" \
"s_mov_b32 s22, 0xfffffc00\n" \
"s_mov_b32 s23, -1\n" \
"s_mul_i32 s32, s17, 1\n" \
"s_mov_b32 s33, s32\n" \
"s_mov_b32 s34, s17\n" \
"v_max_u32 v20, 1, v20\n" \
"v_max_u32 v21, 1, v21\n" \
"v_max_u32 v22, 1, v22\n" \
"v_max_u32 v23, 1, v23\n" \
"buffer_store_dwordx4 v[20:23], v8, s[36:39], 0 offen sc1\n" \
"s_mov_b32 s72, 0\n" \
"s_mov_b32 s73, -1\n" \
"v_lshlrev_b32 v31, 1, v0\n" \
"v_add_u32 v31, 16384, v31\n" \
"s_lshl_b32 s79, s48, 10\n" \
"s_add_u32 s79, s79, 0\n" \
"v_xor_b32 v30, 0x1000, v6\n" \
"s_mov_b32 s53, 0\n" \
"s_cmp_lg_u32 s48, 0\n" \
"s_cbranch_scc1 .Lv_go0x_%=\n" \
".Lv_go0_%=:\n" \
"buffer_load_dword v13, v10, s[36:39], 0 offen sc1\n" \
"s_waitcnt vmcnt(0)\n" \
"v_cmp_eq_u32 vcc, 0, v13\n" \
"s_add_u32 s53, s53, 1\n" \
"s_cmp_lt_u32 s53, 32768\n" \
"s_cselect_b64 s[54:55], -1, 0\n" \
"s_and_b64 vcc, vcc, s[54:55]\n" \
"s_cbranch_vccz .Lv_go0x_%=\n" \
"s_sleep 4\n" \
"s_branch .Lv_go0_%=\n" \
".Lv_go0x_%=:\n" \
"s_memrealtime s[56:57]\n" \
"s_waitcnt lgkmcnt(0)\n" \
"s_sub_u32 s50, s56, 20\n" \
"s_waitcnt vmcnt(0) lgkmcnt(0)\n" \
"s_add_u32 s54, s49, 0\n" \
"s_and_b32 s54, s54, 3\n" \
"s_mul_i32 s58, s54, 131072\n" \
"s_add_u32 s54, s49, 1\n" \
"s_and_b32 s54, s54, 3\n" \
"s_mul_i32 s59, s54, 131072\n" \
"s_add_u32 s54, s49, 2\n" \
"s_and_b32 s54, s54, 3\n" \
"s_mul_i32 s60, s54, 131072\n" \
"s_add_u32 s54, s49, 3\n" \
"s_and_b32 s54, s54, 3\n" \
"s_mul_i32 s61, s54, 131072\n" \
"s_mov_b32 s77, 0\n" \
"s_add_u32 s78, s77, s63\n" \
"s_xor_b32 s89, s78, 0x4800\n" \
"s_xor_b32 s90, s78, 0x800\n" \
"s_xor_b32 s91, s78, 0x4000\n" \
"s_mov_b32 m0, s79\n" \
"s_add_u32 s51, s50, s46\n" \
"v_mov_b64 v[80:81], 0\n" \
"v_mov_b64 v[82:83], 0\n" \
"v_mov_b32 v13, -1\n" \
"s_mov_b32 s23, -1\n" \
"s_cmp_eq_u32 s48, 0\n" \
"s_cselect_b32 s80, -1, 0\n" \
"s_mov_b32 s81, 0\n" \
"s_nop 7\n" \
"s_nop 7\n" \
".p2align 6\n" \
".Lv_loop_%=:\n" \
"s_getpc_b64 s[18:19]\n" \
".Lv_wp_%=:\n" \
"s_add_u32 s18, s18, .Lv_ws_%=-.Lv_wp_%=\n" \
"s_addc_u32 s19, s19, 0\n" \
"s_sub_u32 s21, s51, 5\n" \
"s_nop 0\n" \
".Lv_w_%=:\n" \
"s_memrealtime s[56:57]\n" \
"s_waitcnt lgkmcnt(0)\n" \
"s_sub_u32 s54, s56, s21\n" \
"s_cmp_gt_u32 s54, s22\n" \
"s_cbranch_scc1 .Lv_w_%=\n" \
"s_min_u32 s21, s54, 3\n" \
"s_lshl2_add_u32 s18, s21, s18\n" \
"s_setpc_b64 s[18:19]\n" \
"s_nop 0\n" \
".Lv_ws_%=:\n" \
"s_nop 4\n" \
"s_nop 5\n" \
"s_nop 4\n" \
".Lv_w4_%=:\n" \
"s_barrier\n" \
".Lv_go_%=:\n" \
"buffer_load_dwordx4 v5, s[36:39], s78 offen sc1 lds\n" \
"s_nop 7\n" \
"s_add_u32 m0, s79, 4096\n" \
"s_nop 0\n" \
"buffer_load_dwordx4 v5, s[36:39], s89 offen sc1 lds\n" \
"s_nop 7\n" \
"s_add_u32 m0, s79, 8192\n" \
"s_nop 0\n" \
"buffer_load_dwordx4 v5, s[36:39], s90 offen sc1 lds\n" \
"s_nop 7\n" \
"s_add_u32 m0, s79, 12288\n" \
"s_nop 0\n" \
"buffer_load_dwordx4 v5, s[36:39], s91 offen sc1 lds\n" \
".Lv_x_%=:\n" \
"s_waitcnt vmcnt(2)\n" \
"s_barrier\n" \
"ds_read_b128 v[32:35], v0 offset:0\n" \
"ds_read_b128 v[36:39], v0 offset:1024\n" \
"ds_read_b128 v[40:43], v0 offset:2048\n" \
"ds_read_b128 v[44:47], v0 offset:3072\n" \
"ds_read_b128 v[48:51], v0 offset:4096\n" \
"s_waitcnt lgkmcnt(4)\n" \
"v_smfmac_f32_16x16x64_bf16 v[80:83], v[32:35], v[128:135], v12\n" \
"ds_read_b128 v[52:55], v0 offset:5120\n" \
"s_waitcnt lgkmcnt(4)\n" \
"v_min3_u32 v13, v13, v32, v34\n" \
"v_smfmac_f32_16x16x64_bf16 v[80:83], v[36:39], v[136:143], v12\n" \
"ds_read_b128 v[56:59], v0 offset:6144\n" \
"s_waitcnt lgkmcnt(4)\n" \
"v_min3_u32 v13, v13, v36, v38\n" \
"v_smfmac_f32_16x16x64_bf16 v[80:83], v[40:43], v[144:151], v12\n" \
"ds_read_b128 v[60:63], v0 offset:7168\n" \
"s_waitcnt lgkmcnt(4)\n" \
"v_min3_u32 v13, v13, v40, v42\n" \
"v_smfmac_f32_16x16x64_bf16 v[80:83], v[44:47], v[152:159], v12\n" \
"s_waitcnt vmcnt(0)\n" \
"s_barrier\n" \
"ds_read_b128 v[32:35], v0 offset:8192\n" \
"s_waitcnt lgkmcnt(4)\n" \
"v_min3_u32 v13, v13, v44, v46\n" \
"v_smfmac_f32_16x16x64_bf16 v[80:83], v[48:51], v[160:167], v12\n" \
"ds_read_b128 v[36:39], v0 offset:9216\n" \
"s_waitcnt lgkmcnt(4)\n" \
"v_min3_u32 v13, v13, v48, v50\n" \
"v_cmp_eq_u32_e64 vcc, 0, v13\n" \
"v_smfmac_f32_16x16x64_bf16 v[80:83], v[52:55], v[168:175], v12\n" \
"ds_read_b128 v[40:43], v0 offset:10240\n" \
"s_waitcnt lgkmcnt(4)\n" \
"v_min3_u32 v13, v13, v52, v54\n" \
"v_smfmac_f32_16x16x64_bf16 v[80:83], v[56:59], v[176:183], v12\n" \
"ds_read_b128 v[44:47], v0 offset:11264\n" \
"s_waitcnt lgkmcnt(4)\n" \
"v_min3_u32 v13, v13, v56, v58\n" \
"v_smfmac_f32_16x16x64_bf16 v[80:83], v[60:63], v[184:191], v12\n" \
"ds_read_b128 v[48:51], v0 offset:12288\n" \
"s_waitcnt lgkmcnt(4)\n" \
"s_nop 0\n" \
"s_cbranch_vccnz .Lv_mretry_%=\n" \
"v_min3_u32 v13, v13, v60, v62\n" \
"v_smfmac_f32_16x16x64_bf16 v[80:83], v[32:35], v[192:199], v12\n" \
"ds_read_b128 v[52:55], v0 offset:13312\n" \
"s_waitcnt lgkmcnt(4)\n" \
"v_min3_u32 v13, v13, v32, v34\n" \
"v_smfmac_f32_16x16x64_bf16 v[80:83], v[36:39], v[200:207], v12\n" \
"ds_read_b128 v[56:59], v0 offset:14336\n" \
"s_waitcnt lgkmcnt(4)\n" \
"v_min3_u32 v13, v13, v36, v38\n" \
"v_smfmac_f32_16x16x64_bf16 v[80:83], v[40:43], v[208:215], v12\n" \
"ds_read_b128 v[60:63], v0 offset:15360\n" \
"s_waitcnt lgkmcnt(4)\n" \
"v_min3_u32 v13, v13, v40, v42\n" \
"v_smfmac_f32_16x16x64_bf16 v[80:83], v[44:47], v[216:223], v12\n" \
"s_waitcnt lgkmcnt(3)\n" \
"v_min3_u32 v13, v13, v44, v46\n" \
"v_smfmac_f32_16x16x64_bf16 v[80:83], v[48:51], v[224:231], v12\n" \
"s_waitcnt lgkmcnt(2)\n" \
"v_min3_u32 v13, v13, v48, v50\n" \
"v_smfmac_f32_16x16x64_bf16 v[80:83], v[52:55], v[232:239], v12\n" \
"s_waitcnt lgkmcnt(1)\n" \
"v_min3_u32 v13, v13, v52, v54\n" \
"v_smfmac_f32_16x16x64_bf16 v[80:83], v[56:59], v[240:247], v12\n" \
"s_waitcnt lgkmcnt(0)\n" \
"v_min3_u32 v13, v13, v56, v58\n" \
"v_smfmac_f32_16x16x64_bf16 v[80:83], v[60:63], v[248:255], v12\n" \
"v_min3_u32 v13, v13, v60, v62\n" \
"v_cmp_eq_u32 vcc, 0, v13\n" \
"s_nop 5\n" \
"v_pk_add_f32 v[80:81], v[80:81], v[82:83]\n" \
"s_cbranch_vccnz .Lv_mretry_%=\n" \
"v_cvt_pk_bf16_f32 v84, v80, v81\n" \
"v_max_u32 v84, 1, v84\n" \
"buffer_store_dword v84, v7, s[36:39], s58 offen sc1\n" \
"s_mov_b32 s75, s77\n" \
"s_lshr_b32 s77, s59, 1\n" \
"buffer_load_dwordx2 v[64:65], v9, s[36:39], s75 offen sc1\n" \
"s_sub_u32 s53, s49, 1\n" \
"s_add_u32 s25, s24, 3\n" \
"s_cmp_eq_u32 s26, s24\n" \
"s_subb_u32 s25, s25, 0\n" \
"s_cmp_eq_u32 s28, s24\n" \
"s_subb_u32 s25, s25, 0\n" \
"s_cmp_eq_u32 s30, s24\n" \
"s_subb_u32 s25, s25, 0\n" \
"s_max_u32 s16, s25, 1\n" \
"s_sub_u32 s24, s25, s55\n" \
"s_min_u32 s24, s24, 255\n" \
"s_cmp_eq_u32 s23, s53\n" \
"s_cselect_b32 s24, s34, s24\n" \
"s_min_u32 s34, s24, s32\n" \
"s_max_u32 s25, s24, s32\n" \
"s_min_u32 s25, s25, s33\n" \
"s_max_u32 s34, s34, s25\n" \
"s_mov_b32 s33, s32\n" \
"s_mov_b32 s32, s24\n" \
"s_cmp_eq_u32 s49, 63\n" \
"s_cbranch_scc1 .Lv_end_%=\n" \
"v_mov_b64 v[80:81], 0\n" \
"v_mov_b64 v[82:83], 0\n" \
"v_mov_b64 v[84:85], 0\n" \
"v_mov_b64 v[86:87], 0\n" \
"v_mov_b64 v[88:89], 0\n" \
"v_mov_b64 v[90:91], 0\n" \
"v_mov_b64 v[92:93], 0\n" \
"v_mov_b64 v[94:95], 0\n" \
"buffer_store_dword v16, v7, s[36:39], s60 offen sc1\n" \
"s_lshr_b32 s54, s61, 1\n" \
"buffer_store_dwordx4 v[16:19], v8, s[36:39], s54 offen sc1\n" \
"s_sub_u32 s54, s34, s74\n" \
"v_mov_b32_e64 v13, s54\n" \
"buffer_store_dword v13, v28, s[36:39], 0 offen sc1\n" \
"v_mov_b32_e64 v13, -1\n" \
"s_mov_b32 s54, 0\n" \
"s_cmp_lg_u32 s48, 0\n" \
"s_cbranch_scc1 .Lv_wo4_%=\n" \
"s_waitcnt vmcnt(3)\n" \
"v_cmp_eq_u32_e64 vcc, 0, v64\n" \
"s_nop 0\n" \
"s_and_b64 vcc, vcc, s[80:81]\n" \
"s_cbranch_vccnz .Lv_h_cold_%=\n" \
".Lv_h_ok_%=:\n" \
"v_subrev_u32 v64, s51, v64\n" \
"v_max_i32_dpp v65, v65, v65 row_ror:8 row_mask:0xf bank_mask:0xf\n" \
"s_nop 0\n" \
"v_max_i32_dpp v64, v64, v64 row_ror:8 row_mask:0xf bank_mask:0xf\n" \
"v_max_i32_dpp v65, v65, v65 row_ror:4 row_mask:0xf bank_mask:0xf\n" \
"s_nop 0\n" \
"v_max_i32_dpp v64, v64, v64 row_ror:4 row_mask:0xf bank_mask:0xf\n" \
"v_max_i32_dpp v65, v65, v65 row_ror:2 row_mask:0xf bank_mask:0xf\n" \
"s_nop 0\n" \
"v_max_i32_dpp v64, v64, v64 row_ror:2 row_mask:0xf bank_mask:0xf\n" \
"v_max_i32_dpp v65, v65, v65 row_ror:1 row_mask:0xf bank_mask:0xf\n" \
"s_nop 0\n" \
"v_max_i32_dpp v64, v64, v64 row_ror:1 row_mask:0xf bank_mask:0xf\n" \
"v_readlane_b32 s53, v65, 0\n" \
"v_readlane_b32 s50, v65, 16\n" \
"v_readlane_b32 s54, v64, 0\n" \
"v_readlane_b32 s55, v64, 16\n" \
"s_max_i32 s50, s50, s53\n" \
"s_max_i32 s55, s55, s54\n" \
"s_add_u32 s55, s55, s51\n" \
"s_add_u32 s55, s55, s44\n" \
"s_add_u32 s50, s50, s55\n" \
"s_add_u32 s50, s50, s45\n" \
"s_getpc_b64 s[18:19]\n" \
".Lv_wop_%=:\n" \
"s_add_u32 s18, s18, .Lv_wos_%=-.Lv_wop_%=\n" \
"s_addc_u32 s19, s19, 0\n" \
"s_sub_u32 s21, s55, 5\n" \
"s_nop 0\n" \
"s_nop 0\n" \
"s_nop 0\n" \
"s_nop 0\n" \
"s_nop 0\n" \
".Lv_wo_%=:\n" \
"s_memrealtime s[56:57]\n" \
"s_waitcnt lgkmcnt(0)\n" \
"s_sub_u32 s54, s56, s21\n" \
"s_cmp_gt_u32 s54, s22\n" \
"s_cbranch_scc1 .Lv_wo_%=\n" \
"s_min_u32 s21, s54, 3\n" \
"s_lshl2_add_u32 s18, s21, s18\n" \
"s_setpc_b64 s[18:19]\n" \
"s_nop 0\n" \
".Lv_wos_%=:\n" \
"s_nop 4\n" \
"s_nop 5\n" \
"s_nop 4\n" \
".Lv_wo4_%=:\n" \
"s_barrier\n" \
".Lv_o_%=:\n" \
"buffer_load_dwordx4 v[32:35], v6, s[36:39], s58 offen offset:0 sc1\n" \
"s_cmp_lt_u32 s54, 8\n" \
"s_cselect_b32 s23, s23, s49\n" \
"s_nop 8\n" \
"buffer_load_dwordx4 v[36:39], v6, s[36:39], s58 offen offset:1024 sc1\n" \
"s_nop 10\n" \
"buffer_load_dwordx4 v[40:43], v6, s[36:39], s58 offen offset:2048 sc1\n" \
"s_nop 10\n" \
"buffer_load_dwordx4 v[44:47], v6, s[36:39], s58 offen offset:3072 sc1\n" \
"s_nop 10\n" \
"buffer_load_dwordx4 v[48:51], v30, s[36:39], s58 offen offset:0 sc1\n" \
"s_nop 10\n" \
"buffer_load_dwordx4 v[52:55], v30, s[36:39], s58 offen offset:1024 sc1\n" \
"s_nop 10\n" \
"buffer_load_dwordx4 v[56:59], v30, s[36:39], s58 offen offset:2048 sc1\n" \
"s_nop 10\n" \
"buffer_load_dwordx4 v[60:63], v30, s[36:39], s58 offen offset:3072 sc1\n" \
".Lv_y_%=:\n" \
"s_waitcnt vmcnt(7)\n" \
"v_smfmac_f32_16x16x64_bf16 v[80:83], v[32:35], a[0:7], v12\n" \
"v_min3_u32 v13, v13, v32, v34\n" \
"v_smfmac_f32_16x16x64_bf16 v[84:87], v[32:35], a[8:15], v12\n" \
"v_smfmac_f32_16x16x64_bf16 v[88:91], v[32:35], a[16:23], v12\n" \
"v_smfmac_f32_16x16x64_bf16 v[92:95], v[32:35], a[24:31], v12\n" \
"s_waitcnt vmcnt(6)\n" \
"v_smfmac_f32_16x16x64_bf16 v[80:83], v[36:39], a[32:39], v12\n" \
"v_min3_u32 v13, v13, v36, v38\n" \
"v_smfmac_f32_16x16x64_bf16 v[84:87], v[36:39], a[40:47], v12\n" \
"v_cmp_eq_u32 vcc, 0, v13\n" \
"v_smfmac_f32_16x16x64_bf16 v[88:91], v[36:39], a[48:55], v12\n" \
"v_smfmac_f32_16x16x64_bf16 v[92:95], v[36:39], a[56:63], v12\n" \
"s_cbranch_vccnz .Lv_retry_%=\n" \
"s_waitcnt vmcnt(5)\n" \
"v_smfmac_f32_16x16x64_bf16 v[80:83], v[40:43], a[64:71], v12\n" \
"v_min3_u32 v13, v13, v40, v42\n" \
"v_smfmac_f32_16x16x64_bf16 v[84:87], v[40:43], a[72:79], v12\n" \
"v_smfmac_f32_16x16x64_bf16 v[88:91], v[40:43], a[80:87], v12\n" \
"v_smfmac_f32_16x16x64_bf16 v[92:95], v[40:43], a[88:95], v12\n" \
"s_waitcnt vmcnt(4)\n" \
"v_smfmac_f32_16x16x64_bf16 v[80:83], v[44:47], a[96:103], v12\n" \
"v_min3_u32 v13, v13, v44, v46\n" \
"v_smfmac_f32_16x16x64_bf16 v[84:87], v[44:47], a[104:111], v12\n" \
"v_smfmac_f32_16x16x64_bf16 v[88:91], v[44:47], a[112:119], v12\n" \
"v_smfmac_f32_16x16x64_bf16 v[92:95], v[44:47], a[120:127], v12\n" \
"s_waitcnt vmcnt(3)\n" \
"v_smfmac_f32_16x16x64_bf16 v[80:83], v[48:51], a[128:135], v12\n" \
"v_min3_u32 v13, v13, v48, v50\n" \
"v_smfmac_f32_16x16x64_bf16 v[84:87], v[48:51], a[136:143], v12\n" \
"v_smfmac_f32_16x16x64_bf16 v[88:91], v[48:51], a[144:151], v12\n" \
"v_smfmac_f32_16x16x64_bf16 v[92:95], v[48:51], a[152:159], v12\n" \
"s_waitcnt vmcnt(2)\n" \
"v_smfmac_f32_16x16x64_bf16 v[80:83], v[52:55], a[160:167], v12\n" \
"v_min3_u32 v13, v13, v52, v54\n" \
"v_smfmac_f32_16x16x64_bf16 v[84:87], v[52:55], a[168:175], v12\n" \
"v_smfmac_f32_16x16x64_bf16 v[88:91], v[52:55], a[176:183], v12\n" \
"v_smfmac_f32_16x16x64_bf16 v[92:95], v[52:55], a[184:191], v12\n" \
"s_waitcnt vmcnt(1)\n" \
"v_smfmac_f32_16x16x64_bf16 v[80:83], v[56:59], a[192:199], v12\n" \
"v_min3_u32 v13, v13, v56, v58\n" \
"v_smfmac_f32_16x16x64_bf16 v[84:87], v[56:59], a[200:207], v12\n" \
"v_smfmac_f32_16x16x64_bf16 v[88:91], v[56:59], a[208:215], v12\n" \
"v_smfmac_f32_16x16x64_bf16 v[92:95], v[56:59], a[216:223], v12\n" \
"s_waitcnt vmcnt(0)\n" \
"v_smfmac_f32_16x16x64_bf16 v[80:83], v[60:63], a[224:231], v12\n" \
"v_min3_u32 v13, v13, v60, v62\n" \
"v_smfmac_f32_16x16x64_bf16 v[84:87], v[60:63], a[232:239], v12\n" \
"v_cmp_eq_u32 vcc, 0, v13\n" \
"v_smfmac_f32_16x16x64_bf16 v[88:91], v[60:63], a[240:247], v12\n" \
"v_smfmac_f32_16x16x64_bf16 v[92:95], v[60:63], a[248:255], v12\n" \
"s_cbranch_vccnz .Lv_retry_%=\n" \
".Lv_y2_%=:\n" \
"v_pk_add_f32 v[80:81], v[80:81], v[82:83]\n" \
"v_pk_add_f32 v[84:85], v[84:85], v[86:87]\n" \
"s_nop 0\n" \
"v_pk_add_f32 v[88:89], v[88:89], v[90:91]\n" \
"s_nop 2\n" \
"v_pk_add_f32 v[92:93], v[92:93], v[94:95]\n" \
"v_cndmask_b32_e64 v80, v80, v88, s[72:73]\n" \
"v_cndmask_b32_e64 v81, v81, v89, s[72:73]\n" \
"v_cndmask_b32_e64 v84, v84, v92, s[72:73]\n" \
"v_cndmask_b32_e64 v85, v85, v93, s[72:73]\n" \
"s_nop 0\n" \
"v_permlane32_swap_b32 v80, v84\n" \
"v_permlane32_swap_b32 v81, v85\n" \
"v_pk_add_f32 v[80:81], v[80:81], v[84:85]\n" \
"ds_write2_b32 v15, v80, v81 offset1:32\n" \
"s_cmp_lg_u32 s48, 0\n" \
"s_waitcnt lgkmcnt(0)\n" \
"s_barrier\n" \
"s_nop 0\n" \
"s_nop 0\n" \
"s_nop 0\n" \
"s_cbranch_scc1 .Lv_skip_%=\n" \
"ds_read_b128 v[64:67], v31\n" \
"ds_read_b128 v[68:71], v31 offset:16\n" \
"s_waitcnt lgkmcnt(1)\n" \
"v_pk_add_f32 v[64:65], v[64:65], v[66:67]\n" \
"s_waitcnt lgkmcnt(0)\n" \
"v_pk_add_f32 v[68:69], v[68:69], v[70:71]\n" \
"v_pk_add_f32 v[64:65], v[64:65], v[68:69]\n" \
"v_cvt_pk_bf16_f32 v72, v64, v65\n" \
"v_max_u32 v72, 1, v72\n" \
"s_nop 1\n" \
"v_mov_b32_dpp v73, v72 quad_perm:[1,1,1,1] row_mask:0xf bank_mask:0xf\n" \
"v_mov_b32_dpp v74, v72 quad_perm:[2,2,2,2] row_mask:0xf bank_mask:0xf\n" \
"v_mov_b32_dpp v75, v72 quad_perm:[3,3,3,3] row_mask:0xf bank_mask:0xf\n" \
".Lv_skip_%=:\n" \
"buffer_store_dwordx4 v[72:75], v8, s[36:39], s77 offen sc1\n" \
"s_memrealtime s[24:25]\n" \
"s_mov_b32 s54, s58\n" \
"s_mov_b32 s58, s59\n" \
"s_mov_b32 s59, s60\n" \
"s_mov_b32 s60, s61\n" \
"s_mov_b32 s61, s54\n" \
"s_memrealtime s[26:27]\n" \
"s_add_u32 s78, s77, s63\n" \
"s_xor_b32 s89, s78, 0x4800\n" \
"s_xor_b32 s90, s78, 0x800\n" \
"s_xor_b32 s91, s78, 0x4000\n" \
"s_mov_b32 m0, s79\n" \
"s_memrealtime s[28:29]\n" \
"s_add_u32 s51, s50, s46\n" \
"v_mov_b64 v[80:81], 0\n" \
"v_mov_b64 v[82:83], 0\n" \
"v_mov_b32 v13, -1\n" \
"s_memrealtime s[30:31]\n" \
"s_add_u32 s49, s49, 1\n" \
"s_waitcnt lgkmcnt(0)\n" \
"s_cmp_lg_u32 s48, 0\n" \
"s_nop 0\n" \
"s_nop 0\n" \
"s_nop 0\n" \
"s_cbranch_scc1 .Lv_w4_%=\n" \
"s_branch .Lv_loop_%=\n" \
".Lv_h_cold_%=:\n" \
"s_nop 7\n" \
"s_nop 7\n" \
"s_waitcnt vmcnt(0) lgkmcnt(0)\n" \
"s_mov_b32 s53, 0\n" \
".Lv_h_again_%=:\n" \
"s_cmp_lg_u32 s52, 0\n" \
"s_cbranch_scc1 .Lv_h_ok_%=\n" \
"s_sleep 1\n" \
"buffer_load_dwordx2 v[64:65], v9, s[36:39], s75 offen sc1\n" \
"s_waitcnt vmcnt(0)\n" \
"v_cmp_eq_u32 vcc, 0, v64\n" \
"s_nop 0\n" \
"s_and_b64 vcc, vcc, s[80:81]\n" \
"s_nop 4\n" \
"s_cbranch_vccz .Lv_h_ok_%=\n" \
"s_add_u32 s53, s53, 1\n" \
"s_cmp_lt_u32 s53, 32768\n" \
"s_cbranch_scc1 .Lv_h_again_%=\n" \
"s_mov_b32 s52, 1\n" \
"v_mov_b32 v10, 256\n" \
"v_mov_b32 v11, 1\n" \
"buffer_store_dword v11, v10, s[36:39], 0 offen sc1\n" \
"s_waitcnt vmcnt(0) lgkmcnt(0)\n" \
"s_branch .Lv_h_ok_%=\n" \
".Lv_retry_%=:\n" \
"s_nop 7\n" \
"s_nop 7\n" \
"s_cmp_lg_u32 s52, 0\n" \
"s_cbranch_scc1 .Lv_dead_%=\n" \
"v_mov_b64 v[80:81], 0\n" \
"v_mov_b64 v[82:83], 0\n" \
"v_mov_b64 v[84:85], 0\n" \
"v_mov_b64 v[86:87], 0\n" \
"v_mov_b64 v[88:89], 0\n" \
"v_mov_b64 v[90:91], 0\n" \
"v_mov_b64 v[92:93], 0\n" \
"v_mov_b64 v[94:95], 0\n" \
"v_mov_b32 v13, -1\n" \
"s_mov_b32 s54, 100\n" \
"s_add_u32 s76, s76, 1\n" \
"s_cmp_lt_u32 s76, 32768\n" \
"s_cbranch_scc1 .Lv_o_%=\n" \
"s_mov_b32 s52, 1\n" \
"v_mov_b32 v13, 256\n" \
"v_mov_b32 v14, 1\n" \
"buffer_store_dword v14, v13, s[36:39], 0 offen sc1\n" \
".Lv_dead_%=:\n" \
"s_waitcnt vmcnt(0) lgkmcnt(0)\n" \
"s_branch .Lv_y2_%=\n" \
".Lv_mretry_%=:\n" \
"s_nop 7\n" \
"s_nop 7\n" \
"v_mov_b64 v[80:81], 0\n" \
"v_mov_b64 v[82:83], 0\n" \
"v_mov_b32 v13, -1\n" \
"s_mov_b32 m0, s79\n" \
"s_waitcnt vmcnt(0) lgkmcnt(0)\n" \
"s_barrier\n" \
"s_add_u32 s88, s88, 1\n" \
"s_cmp_lt_u32 s88, 32768\n" \
"s_cbranch_scc1 .Lv_r1_%=\n" \
"s_mov_b32 s52, 1\n" \
"v_mov_b32 v13, 256\n" \
"v_mov_b32 v14, 1\n" \
"buffer_store_dword v14, v13, s[36:39], 0 offen sc1\n" \
"s_waitcnt vmcnt(0) lgkmcnt(0)\n" \
"s_endpgm\n" \
".Lv_r1_%=:\n" \
"s_cmp_lg_u32 s49, 0\n" \
"s_cbranch_scc1 .Lv_r2_%=\n" \
"s_sleep 8\n" \
".Lv_r2_%=:\n" \
"s_branch .Lv_go_%=\n" \
".Lv_end_%=:\n" \
"s_nop 7\n" \
"s_nop 7\n" \
"s_waitcnt vmcnt(0) lgkmcnt(0)\n" \
"s_waitcnt vmcnt(0) lgkmcnt(0)\n" \
".Lv_fin_%=:\n" \
"s_endpgm\n"
#define ASM_S64_WO \
"s_nop 7\n" \
"s_nop 7\n" \
"s_lshl_b32 s52, s48, 15\n" \
"s_lshl_b32 s53, s48, 13\n" \
"s_add_u32 s53, s53, 98304\n" \
"s_sub_u32 s54, s53, s52\n" \
"v_add_u32 v9, s54, v1\n" \
"s_add_u32 m0, s52, 0\n" \
"s_mov_b32 s54, 0\n" \
"buffer_load_dwordx4 v0, s[40:43], s54 offen sc1 lds\n" \
"s_add_u32 m0, s52, 1024\n" \
"s_mov_b32 s54, 16384\n" \
"buffer_load_dwordx4 v0, s[40:43], s54 offen sc1 lds\n" \
"s_add_u32 m0, s52, 2048\n" \
"s_mov_b32 s54, 32768\n" \
"buffer_load_dwordx4 v0, s[40:43], s54 offen sc1 lds\n" \
"s_add_u32 m0, s52, 3072\n" \
"s_mov_b32 s54, 49152\n" \
"buffer_load_dwordx4 v0, s[40:43], s54 offen sc1 lds\n" \
"s_mov_b32 s54, s99\n" \
".Lswo_z0_%=:\n" \
"s_sleep 1\n" \
"s_sub_u32 s54, s54, 1\n" \
"s_cbranch_scc0 .Lswo_z0_%=\n" \
"s_add_u32 m0, s52, 4096\n" \
"s_mov_b32 s54, 65536\n" \
"buffer_load_dwordx4 v0, s[40:43], s54 offen sc1 lds\n" \
"s_add_u32 m0, s52, 5120\n" \
"s_mov_b32 s54, 81920\n" \
"buffer_load_dwordx4 v0, s[40:43], s54 offen sc1 lds\n" \
"s_add_u32 m0, s52, 6144\n" \
"s_mov_b32 s54, 98304\n" \
"buffer_load_dwordx4 v0, s[40:43], s54 offen sc1 lds\n" \
"s_add_u32 m0, s52, 7168\n" \
"s_mov_b32 s54, 114688\n" \
"buffer_load_dwordx4 v0, s[40:43], s54 offen sc1 lds\n" \
"s_mov_b32 s54, s99\n" \
".Lswo_z1_%=:\n" \
"s_sleep 1\n" \
"s_sub_u32 s54, s54, 1\n" \
"s_cbranch_scc0 .Lswo_z1_%=\n" \
"s_add_u32 m0, s52, 8192\n" \
"s_mov_b32 s54, 131072\n" \
"buffer_load_dwordx4 v0, s[40:43], s54 offen sc1 lds\n" \
"s_add_u32 m0, s52, 9216\n" \
"s_mov_b32 s54, 147456\n" \
"buffer_load_dwordx4 v0, s[40:43], s54 offen sc1 lds\n" \
"s_add_u32 m0, s52, 10240\n" \
"s_mov_b32 s54, 163840\n" \
"buffer_load_dwordx4 v0, s[40:43], s54 offen sc1 lds\n" \
"s_add_u32 m0, s52, 11264\n" \
"s_mov_b32 s54, 180224\n" \
"buffer_load_dwordx4 v0, s[40:43], s54 offen sc1 lds\n" \
"s_mov_b32 s54, s99\n" \
".Lswo_z2_%=:\n" \
"s_sleep 1\n" \
"s_sub_u32 s54, s54, 1\n" \
"s_cbranch_scc0 .Lswo_z2_%=\n" \
"s_add_u32 m0, s52, 12288\n" \
"s_mov_b32 s54, 196608\n" \
"buffer_load_dwordx4 v0, s[40:43], s54 offen sc1 lds\n" \
"s_add_u32 m0, s52, 13312\n" \
"s_mov_b32 s54, 212992\n" \
"buffer_load_dwordx4 v0, s[40:43], s54 offen sc1 lds\n" \
"s_add_u32 m0, s52, 14336\n" \
"s_mov_b32 s54, 229376\n" \
"buffer_load_dwordx4 v0, s[40:43], s54 offen sc1 lds\n" \
"s_add_u32 m0, s52, 15360\n" \
"s_mov_b32 s54, 245760\n" \
"buffer_load_dwordx4 v0, s[40:43], s54 offen sc1 lds\n" \
"s_mov_b32 s54, s99\n" \
".Lswo_z3_%=:\n" \
"s_sleep 1\n" \
"s_sub_u32 s54, s54, 1\n" \
"s_cbranch_scc0 .Lswo_z3_%=\n" \
"s_add_u32 m0, s52, 16384\n" \
"s_mov_b32 s54, 262144\n" \
"buffer_load_dwordx4 v0, s[40:43], s54 offen sc1 lds\n" \
"s_add_u32 m0, s52, 17408\n" \
"s_mov_b32 s54, 278528\n" \
"buffer_load_dwordx4 v0, s[40:43], s54 offen sc1 lds\n" \
"s_add_u32 m0, s52, 18432\n" \
"s_mov_b32 s54, 294912\n" \
"buffer_load_dwordx4 v0, s[40:43], s54 offen sc1 lds\n" \
"s_add_u32 m0, s52, 19456\n" \
"s_mov_b32 s54, 311296\n" \
"buffer_load_dwordx4 v0, s[40:43], s54 offen sc1 lds\n" \
"s_mov_b32 s54, s99\n" \
".Lswo_z4_%=:\n" \
"s_sleep 1\n" \
"s_sub_u32 s54, s54, 1\n" \
"s_cbranch_scc0 .Lswo_z4_%=\n" \
"s_add_u32 m0, s52, 20480\n" \
"s_mov_b32 s54, 327680\n" \
"buffer_load_dwordx4 v0, s[40:43], s54 offen sc1 lds\n" \
"s_add_u32 m0, s52, 21504\n" \
"s_mov_b32 s54, 344064\n" \
"buffer_load_dwordx4 v0, s[40:43], s54 offen sc1 lds\n" \
"s_add_u32 m0, s52, 22528\n" \
"s_mov_b32 s54, 360448\n" \
"buffer_load_dwordx4 v0, s[40:43], s54 offen sc1 lds\n" \
"s_add_u32 m0, s52, 23552\n" \
"s_mov_b32 s54, 376832\n" \
"buffer_load_dwordx4 v0, s[40:43], s54 offen sc1 lds\n" \
"s_mov_b32 s54, s99\n" \
".Lswo_z5_%=:\n" \
"s_sleep 1\n" \
"s_sub_u32 s54, s54, 1\n" \
"s_cbranch_scc0 .Lswo_z5_%=\n" \
"s_add_u32 m0, s52, 24576\n" \
"s_mov_b32 s54, 393216\n" \
"buffer_load_dwordx4 v0, s[40:43], s54 offen sc1 lds\n" \
"s_add_u32 m0, s52, 25600\n" \
"s_mov_b32 s54, 409600\n" \
"buffer_load_dwordx4 v0, s[40:43], s54 offen sc1 lds\n" \
"s_add_u32 m0, s52, 26624\n" \
"s_mov_b32 s54, 425984\n" \
"buffer_load_dwordx4 v0, s[40:43], s54 offen sc1 lds\n" \
"s_add_u32 m0, s52, 27648\n" \
"s_mov_b32 s54, 442368\n" \
"buffer_load_dwordx4 v0, s[40:43], s54 offen sc1 lds\n" \
"s_mov_b32 s54, s99\n" \
".Lswo_z6_%=:\n" \
"s_sleep 1\n" \
"s_sub_u32 s54, s54, 1\n" \
"s_cbranch_scc0 .Lswo_z6_%=\n" \
"s_add_u32 m0, s52, 28672\n" \
"s_mov_b32 s54, 458752\n" \
"buffer_load_dwordx4 v0, s[40:43], s54 offen sc1 lds\n" \
"s_add_u32 m0, s52, 29696\n" \
"s_mov_b32 s54, 475136\n" \
"buffer_load_dwordx4 v0, s[40:43], s54 offen sc1 lds\n" \
"s_add_u32 m0, s52, 30720\n" \
"s_mov_b32 s54, 491520\n" \
"buffer_load_dwordx4 v0, s[40:43], s54 offen sc1 lds\n" \
"s_add_u32 m0, s52, 31744\n" \
"s_mov_b32 s54, 507904\n" \
"buffer_load_dwordx4 v0, s[40:43], s54 offen sc1 lds\n" \
"s_mov_b32 s54, s99\n" \
".Lswo_z7_%=:\n" \
"s_sleep 1\n" \
"s_sub_u32 s54, s54, 1\n" \
"s_cbranch_scc0 .Lswo_z7_%=\n" \
"s_add_u32 m0, s53, 32768\n" \
"s_mov_b32 s54, 524288\n" \
"buffer_load_dwordx4 v0, s[40:43], s54 offen sc1 lds\n" \
"s_add_u32 m0, s53, 33792\n" \
"s_mov_b32 s54, 540672\n" \
"buffer_load_dwordx4 v0, s[40:43], s54 offen sc1 lds\n" \
"s_add_u32 m0, s53, 34816\n" \
"s_mov_b32 s54, 557056\n" \
"buffer_load_dwordx4 v0, s[40:43], s54 offen sc1 lds\n" \
"s_add_u32 m0, s53, 35840\n" \
"s_mov_b32 s54, 573440\n" \
"buffer_load_dwordx4 v0, s[40:43], s54 offen sc1 lds\n" \
"s_mov_b32 s54, s99\n" \
".Lswo_z8_%=:\n" \
"s_sleep 1\n" \
"s_sub_u32 s54, s54, 1\n" \
"s_cbranch_scc0 .Lswo_z8_%=\n" \
"s_add_u32 m0, s53, 36864\n" \
"s_mov_b32 s54, 589824\n" \
"buffer_load_dwordx4 v0, s[40:43], s54 offen sc1 lds\n" \
"s_add_u32 m0, s53, 37888\n" \
"s_mov_b32 s54, 606208\n" \
"buffer_load_dwordx4 v0, s[40:43], s54 offen sc1 lds\n" \
"s_add_u32 m0, s53, 38912\n" \
"s_mov_b32 s54, 622592\n" \
"buffer_load_dwordx4 v0, s[40:43], s54 offen sc1 lds\n" \
"s_add_u32 m0, s53, 39936\n" \
"s_mov_b32 s54, 638976\n" \
"buffer_load_dwordx4 v0, s[40:43], s54 offen sc1 lds\n" \
"s_mov_b32 s54, s99\n" \
".Lswo_z9_%=:\n" \
"s_sleep 1\n" \
"s_sub_u32 s54, s54, 1\n" \
"s_cbranch_scc0 .Lswo_z9_%=\n" \
"s_waitcnt vmcnt(36)\n" \
"ds_read_b64_tr_b16 a[0:1], v1 offset:0\n" \
"ds_read_b64_tr_b16 a[2:3], v1 offset:2048\n" \
"ds_read_b64_tr_b16 a[128:129], v1 offset:32\n" \
"ds_read_b64_tr_b16 a[130:131], v1 offset:2080\n" \
"ds_read_b64_tr_b16 v[128:129], v1 offset:64\n" \
"ds_read_b64_tr_b16 v[130:131], v1 offset:2112\n" \
"ds_read_b64_tr_b16 v[16:17], v1 offset:96\n" \
"ds_read_b64_tr_b16 v[18:19], v1 offset:2144\n" \
"s_waitcnt lgkmcnt(0)\n" \
"s_add_u32 m0, s52, 0\n" \
"s_mov_b32 s54, 655360\n" \
"buffer_load_dwordx4 v0, s[40:43], s54 offen sc1 lds\n" \
"s_add_u32 m0, s52, 1024\n" \
"s_mov_b32 s54, 671744\n" \
"buffer_load_dwordx4 v0, s[40:43], s54 offen sc1 lds\n" \
"s_add_u32 m0, s52, 2048\n" \
"s_mov_b32 s54, 688128\n" \
"buffer_load_dwordx4 v0, s[40:43], s54 offen sc1 lds\n" \
"s_add_u32 m0, s52, 3072\n" \
"s_mov_b32 s54, 704512\n" \
"buffer_load_dwordx4 v0, s[40:43], s54 offen sc1 lds\n" \
"s_mov_b32 s54, s99\n" \
".Lswo_z10_%=:\n" \
"s_sleep 1\n" \
"s_sub_u32 s54, s54, 1\n" \
"s_cbranch_scc0 .Lswo_z10_%=\n" \
"s_waitcnt vmcnt(36)\n" \
"ds_read_b64_tr_b16 a[4:5], v1 offset:4096\n" \
"ds_read_b64_tr_b16 a[6:7], v1 offset:6144\n" \
"ds_read_b64_tr_b16 a[132:133], v1 offset:4128\n" \
"ds_read_b64_tr_b16 a[134:135], v1 offset:6176\n" \
"ds_read_b64_tr_b16 v[132:133], v1 offset:4160\n" \
"ds_read_b64_tr_b16 v[134:135], v1 offset:6208\n" \
"ds_read_b64_tr_b16 v[20:21], v1 offset:4192\n" \
"ds_read_b64_tr_b16 v[22:23], v1 offset:6240\n" \
"s_waitcnt lgkmcnt(0)\n" \
"s_add_u32 m0, s52, 4096\n" \
"s_mov_b32 s54, 720896\n" \
"buffer_load_dwordx4 v0, s[40:43], s54 offen sc1 lds\n" \
"s_add_u32 m0, s52, 5120\n" \
"s_mov_b32 s54, 737280\n" \
"buffer_load_dwordx4 v0, s[40:43], s54 offen sc1 lds\n" \
"s_add_u32 m0, s52, 6144\n" \
"s_mov_b32 s54, 753664\n" \
"buffer_load_dwordx4 v0, s[40:43], s54 offen sc1 lds\n" \
"s_add_u32 m0, s52, 7168\n" \
"s_mov_b32 s54, 770048\n" \
"buffer_load_dwordx4 v0, s[40:43], s54 offen sc1 lds\n" \
"s_mov_b32 s54, s99\n" \
".Lswo_z11_%=:\n" \
"s_sleep 1\n" \
"s_sub_u32 s54, s54, 1\n" \
"s_cbranch_scc0 .Lswo_z11_%=\n" \
"s_waitcnt vmcnt(36)\n" \
"ds_read_b64_tr_b16 a[8:9], v1 offset:8192\n" \
"ds_read_b64_tr_b16 a[10:11], v1 offset:10240\n" \
"ds_read_b64_tr_b16 a[136:137], v1 offset:8224\n" \
"ds_read_b64_tr_b16 a[138:139], v1 offset:10272\n" \
"ds_read_b64_tr_b16 v[136:137], v1 offset:8256\n" \
"ds_read_b64_tr_b16 v[138:139], v1 offset:10304\n" \
"ds_read_b64_tr_b16 v[24:25], v1 offset:8288\n" \
"ds_read_b64_tr_b16 v[26:27], v1 offset:10336\n" \
"s_waitcnt lgkmcnt(0)\n" \
"s_add_u32 m0, s52, 8192\n" \
"s_mov_b32 s54, 786432\n" \
"buffer_load_dwordx4 v0, s[40:43], s54 offen sc1 lds\n" \
"s_add_u32 m0, s52, 9216\n" \
"s_mov_b32 s54, 802816\n" \
"buffer_load_dwordx4 v0, s[40:43], s54 offen sc1 lds\n" \
"s_add_u32 m0, s52, 10240\n" \
"s_mov_b32 s54, 819200\n" \
"buffer_load_dwordx4 v0, s[40:43], s54 offen sc1 lds\n" \
"s_add_u32 m0, s52, 11264\n" \
"s_mov_b32 s54, 835584\n" \
"buffer_load_dwordx4 v0, s[40:43], s54 offen sc1 lds\n" \
"s_mov_b32 s54, s99\n" \
".Lswo_z12_%=:\n" \
"s_sleep 1\n" \
"s_sub_u32 s54, s54, 1\n" \
"s_cbranch_scc0 .Lswo_z12_%=\n" \
"s_waitcnt vmcnt(36)\n" \
"ds_read_b64_tr_b16 a[12:13], v1 offset:12288\n" \
"ds_read_b64_tr_b16 a[14:15], v1 offset:14336\n" \
"ds_read_b64_tr_b16 a[140:141], v1 offset:12320\n" \
"ds_read_b64_tr_b16 a[142:143], v1 offset:14368\n" \
"ds_read_b64_tr_b16 v[140:141], v1 offset:12352\n" \
"ds_read_b64_tr_b16 v[142:143], v1 offset:14400\n" \
"ds_read_b64_tr_b16 v[28:29], v1 offset:12384\n" \
"ds_read_b64_tr_b16 v[30:31], v1 offset:14432\n" \
"s_waitcnt lgkmcnt(0)\n" \
"s_add_u32 m0, s52, 12288\n" \
"s_mov_b32 s54, 851968\n" \
"buffer_load_dwordx4 v0, s[40:43], s54 offen sc1 lds\n" \
"s_add_u32 m0, s52, 13312\n" \
"s_mov_b32 s54, 868352\n" \
"buffer_load_dwordx4 v0, s[40:43], s54 offen sc1 lds\n" \
"s_add_u32 m0, s52, 14336\n" \
"s_mov_b32 s54, 884736\n" \
"buffer_load_dwordx4 v0, s[40:43], s54 offen sc1 lds\n" \
"s_add_u32 m0, s52, 15360\n" \
"s_mov_b32 s54, 901120\n" \
"buffer_load_dwordx4 v0, s[40:43], s54 offen sc1 lds\n" \
"s_mov_b32 s54, s99\n" \
".Lswo_z13_%=:\n" \
"s_sleep 1\n" \
"s_sub_u32 s54, s54, 1\n" \
"s_cbranch_scc0 .Lswo_z13_%=\n" \
"s_waitcnt vmcnt(36)\n" \
"ds_read_b64_tr_b16 a[16:17], v1 offset:16384\n" \
"ds_read_b64_tr_b16 a[18:19], v1 offset:18432\n" \
"ds_read_b64_tr_b16 a[144:145], v1 offset:16416\n" \
"ds_read_b64_tr_b16 a[146:147], v1 offset:18464\n" \
"ds_read_b64_tr_b16 v[144:145], v1 offset:16448\n" \
"ds_read_b64_tr_b16 v[146:147], v1 offset:18496\n" \
"ds_read_b64_tr_b16 v[32:33], v1 offset:16480\n" \
"ds_read_b64_tr_b16 v[34:35], v1 offset:18528\n" \
"s_waitcnt lgkmcnt(0)\n" \
"s_add_u32 m0, s52, 16384\n" \
"s_mov_b32 s54, 917504\n" \
"buffer_load_dwordx4 v0, s[40:43], s54 offen sc1 lds\n" \
"s_add_u32 m0, s52, 17408\n" \
"s_mov_b32 s54, 933888\n" \
"buffer_load_dwordx4 v0, s[40:43], s54 offen sc1 lds\n" \
"s_add_u32 m0, s52, 18432\n" \
"s_mov_b32 s54, 950272\n" \
"buffer_load_dwordx4 v0, s[40:43], s54 offen sc1 lds\n" \
"s_add_u32 m0, s52, 19456\n" \
"s_mov_b32 s54, 966656\n" \
"buffer_load_dwordx4 v0, s[40:43], s54 offen sc1 lds\n" \
"s_mov_b32 s54, s99\n" \
".Lswo_z14_%=:\n" \
"s_sleep 1\n" \
"s_sub_u32 s54, s54, 1\n" \
"s_cbranch_scc0 .Lswo_z14_%=\n" \
"s_waitcnt vmcnt(36)\n" \
"ds_read_b64_tr_b16 a[20:21], v1 offset:20480\n" \
"ds_read_b64_tr_b16 a[22:23], v1 offset:22528\n" \
"ds_read_b64_tr_b16 a[148:149], v1 offset:20512\n" \
"ds_read_b64_tr_b16 a[150:151], v1 offset:22560\n" \
"ds_read_b64_tr_b16 v[148:149], v1 offset:20544\n" \
"ds_read_b64_tr_b16 v[150:151], v1 offset:22592\n" \
"ds_read_b64_tr_b16 v[36:37], v1 offset:20576\n" \
"ds_read_b64_tr_b16 v[38:39], v1 offset:22624\n" \
"s_waitcnt lgkmcnt(0)\n" \
"s_add_u32 m0, s52, 20480\n" \
"s_mov_b32 s54, 983040\n" \
"buffer_load_dwordx4 v0, s[40:43], s54 offen sc1 lds\n" \
"s_add_u32 m0, s52, 21504\n" \
"s_mov_b32 s54, 999424\n" \
"buffer_load_dwordx4 v0, s[40:43], s54 offen sc1 lds\n" \
"s_add_u32 m0, s52, 22528\n" \
"s_mov_b32 s54, 1015808\n" \
"buffer_load_dwordx4 v0, s[40:43], s54 offen sc1 lds\n" \
"s_add_u32 m0, s52, 23552\n" \
"s_mov_b32 s54, 1032192\n" \
"buffer_load_dwordx4 v0, s[40:43], s54 offen sc1 lds\n" \
"s_mov_b32 s54, s99\n" \
".Lswo_z15_%=:\n" \
"s_sleep 1\n" \
"s_sub_u32 s54, s54, 1\n" \
"s_cbranch_scc0 .Lswo_z15_%=\n" \
"s_waitcnt vmcnt(36)\n" \
"ds_read_b64_tr_b16 a[24:25], v1 offset:24576\n" \
"ds_read_b64_tr_b16 a[26:27], v1 offset:26624\n" \
"ds_read_b64_tr_b16 a[152:153], v1 offset:24608\n" \
"ds_read_b64_tr_b16 a[154:155], v1 offset:26656\n" \
"ds_read_b64_tr_b16 v[152:153], v1 offset:24640\n" \
"ds_read_b64_tr_b16 v[154:155], v1 offset:26688\n" \
"ds_read_b64_tr_b16 v[40:41], v1 offset:24672\n" \
"ds_read_b64_tr_b16 v[42:43], v1 offset:26720\n" \
"s_waitcnt lgkmcnt(0)\n" \
"s_add_u32 m0, s52, 24576\n" \
"s_mov_b32 s54, 1048576\n" \
"buffer_load_dwordx4 v0, s[40:43], s54 offen sc1 lds\n" \
"s_add_u32 m0, s52, 25600\n" \
"s_mov_b32 s54, 1064960\n" \
"buffer_load_dwordx4 v0, s[40:43], s54 offen sc1 lds\n" \
"s_add_u32 m0, s52, 26624\n" \
"s_mov_b32 s54, 1081344\n" \
"buffer_load_dwordx4 v0, s[40:43], s54 offen sc1 lds\n" \
"s_add_u32 m0, s52, 27648\n" \
"s_mov_b32 s54, 1097728\n" \
"buffer_load_dwordx4 v0, s[40:43], s54 offen sc1 lds\n" \
"s_mov_b32 s54, s99\n" \
".Lswo_z16_%=:\n" \
"s_sleep 1\n" \
"s_sub_u32 s54, s54, 1\n" \
"s_cbranch_scc0 .Lswo_z16_%=\n" \
"s_waitcnt vmcnt(36)\n" \
"ds_read_b64_tr_b16 a[28:29], v1 offset:28672\n" \
"ds_read_b64_tr_b16 a[30:31], v1 offset:30720\n" \
"ds_read_b64_tr_b16 a[156:157], v1 offset:28704\n" \
"ds_read_b64_tr_b16 a[158:159], v1 offset:30752\n" \
"ds_read_b64_tr_b16 v[156:157], v1 offset:28736\n" \
"ds_read_b64_tr_b16 v[158:159], v1 offset:30784\n" \
"ds_read_b64_tr_b16 v[44:45], v1 offset:28768\n" \
"ds_read_b64_tr_b16 v[46:47], v1 offset:30816\n" \
"s_waitcnt lgkmcnt(0)\n" \
"s_add_u32 m0, s52, 28672\n" \
"s_mov_b32 s54, 1114112\n" \
"buffer_load_dwordx4 v0, s[40:43], s54 offen sc1 lds\n" \
"s_add_u32 m0, s52, 29696\n" \
"s_mov_b32 s54, 1130496\n" \
"buffer_load_dwordx4 v0, s[40:43], s54 offen sc1 lds\n" \
"s_add_u32 m0, s52, 30720\n" \
"s_mov_b32 s54, 1146880\n" \
"buffer_load_dwordx4 v0, s[40:43], s54 offen sc1 lds\n" \
"s_add_u32 m0, s52, 31744\n" \
"s_mov_b32 s54, 1163264\n" \
"buffer_load_dwordx4 v0, s[40:43], s54 offen sc1 lds\n" \
"s_mov_b32 s54, s99\n" \
".Lswo_z17_%=:\n" \
"s_sleep 1\n" \
"s_sub_u32 s54, s54, 1\n" \
"s_cbranch_scc0 .Lswo_z17_%=\n" \
"s_waitcnt vmcnt(36)\n" \
"ds_read_b64_tr_b16 a[32:33], v9 offset:32768\n" \
"ds_read_b64_tr_b16 a[34:35], v9 offset:34816\n" \
"ds_read_b64_tr_b16 a[160:161], v9 offset:32800\n" \
"ds_read_b64_tr_b16 a[162:163], v9 offset:34848\n" \
"ds_read_b64_tr_b16 v[160:161], v9 offset:32832\n" \
"ds_read_b64_tr_b16 v[162:163], v9 offset:34880\n" \
"ds_read_b64_tr_b16 v[48:49], v9 offset:32864\n" \
"ds_read_b64_tr_b16 v[50:51], v9 offset:34912\n" \
"s_waitcnt lgkmcnt(0)\n" \
"s_add_u32 m0, s53, 32768\n" \
"s_mov_b32 s54, 1179648\n" \
"buffer_load_dwordx4 v0, s[40:43], s54 offen sc1 lds\n" \
"s_add_u32 m0, s53, 33792\n" \
"s_mov_b32 s54, 1196032\n" \
"buffer_load_dwordx4 v0, s[40:43], s54 offen sc1 lds\n" \
"s_add_u32 m0, s53, 34816\n" \
"s_mov_b32 s54, 1212416\n" \
"buffer_load_dwordx4 v0, s[40:43], s54 offen sc1 lds\n" \
"s_add_u32 m0, s53, 35840\n" \
"s_mov_b32 s54, 1228800\n" \
"buffer_load_dwordx4 v0, s[40:43], s54 offen sc1 lds\n" \
"s_mov_b32 s54, s99\n" \
".Lswo_z18_%=:\n" \
"s_sleep 1\n" \
"s_sub_u32 s54, s54, 1\n" \
"s_cbranch_scc0 .Lswo_z18_%=\n" \
"s_waitcnt vmcnt(36)\n" \
"ds_read_b64_tr_b16 a[36:37], v9 offset:36864\n" \
"ds_read_b64_tr_b16 a[38:39], v9 offset:38912\n" \
"ds_read_b64_tr_b16 a[164:165], v9 offset:36896\n" \
"ds_read_b64_tr_b16 a[166:167], v9 offset:38944\n" \
"ds_read_b64_tr_b16 v[164:165], v9 offset:36928\n" \
"ds_read_b64_tr_b16 v[166:167], v9 offset:38976\n" \
"ds_read_b64_tr_b16 v[52:53], v9 offset:36960\n" \
"ds_read_b64_tr_b16 v[54:55], v9 offset:39008\n" \
"s_waitcnt lgkmcnt(0)\n" \
"s_add_u32 m0, s53, 36864\n" \
"s_mov_b32 s54, 1245184\n" \
"buffer_load_dwordx4 v0, s[40:43], s54 offen sc1 lds\n" \
"s_add_u32 m0, s53, 37888\n" \
"s_mov_b32 s54, 1261568\n" \
"buffer_load_dwordx4 v0, s[40:43], s54 offen sc1 lds\n" \
"s_add_u32 m0, s53, 38912\n" \
"s_mov_b32 s54, 1277952\n" \
"buffer_load_dwordx4 v0, s[40:43], s54 offen sc1 lds\n" \
"s_add_u32 m0, s53, 39936\n" \
"s_mov_b32 s54, 1294336\n" \
"buffer_load_dwordx4 v0, s[40:43], s54 offen sc1 lds\n" \
"s_mov_b32 s54, s99\n" \
".Lswo_z19_%=:\n" \
"s_sleep 1\n" \
"s_sub_u32 s54, s54, 1\n" \
"s_cbranch_scc0 .Lswo_z19_%=\n" \
"s_waitcnt vmcnt(36)\n" \
"ds_read_b64_tr_b16 a[40:41], v1 offset:0\n" \
"ds_read_b64_tr_b16 a[42:43], v1 offset:2048\n" \
"ds_read_b64_tr_b16 a[168:169], v1 offset:32\n" \
"ds_read_b64_tr_b16 a[170:171], v1 offset:2080\n" \
"ds_read_b64_tr_b16 v[168:169], v1 offset:64\n" \
"ds_read_b64_tr_b16 v[170:171], v1 offset:2112\n" \
"ds_read_b64_tr_b16 v[56:57], v1 offset:96\n" \
"ds_read_b64_tr_b16 v[58:59], v1 offset:2144\n" \
"s_waitcnt lgkmcnt(0)\n" \
"s_add_u32 m0, s52, 0\n" \
"s_mov_b32 s54, 1310720\n" \
"buffer_load_dwordx4 v0, s[40:43], s54 offen sc1 lds\n" \
"s_add_u32 m0, s52, 1024\n" \
"s_mov_b32 s54, 1327104\n" \
"buffer_load_dwordx4 v0, s[40:43], s54 offen sc1 lds\n" \
"s_add_u32 m0, s52, 2048\n" \
"s_mov_b32 s54, 1343488\n" \
"buffer_load_dwordx4 v0, s[40:43], s54 offen sc1 lds\n" \
"s_add_u32 m0, s52, 3072\n" \
"s_mov_b32 s54, 1359872\n" \
"buffer_load_dwordx4 v0, s[40:43], s54 offen sc1 lds\n" \
"s_mov_b32 s54, s99\n" \
".Lswo_z20_%=:\n" \
"s_sleep 1\n" \
"s_sub_u32 s54, s54, 1\n" \
"s_cbranch_scc0 .Lswo_z20_%=\n" \
"s_waitcnt vmcnt(36)\n" \
"ds_read_b64_tr_b16 a[44:45], v1 offset:4096\n" \
"ds_read_b64_tr_b16 a[46:47], v1 offset:6144\n" \
"ds_read_b64_tr_b16 a[172:173], v1 offset:4128\n" \
"ds_read_b64_tr_b16 a[174:175], v1 offset:6176\n" \
"ds_read_b64_tr_b16 v[172:173], v1 offset:4160\n" \
"ds_read_b64_tr_b16 v[174:175], v1 offset:6208\n" \
"ds_read_b64_tr_b16 v[60:61], v1 offset:4192\n" \
"ds_read_b64_tr_b16 v[62:63], v1 offset:6240\n" \
"s_waitcnt lgkmcnt(0)\n" \
"s_add_u32 m0, s52, 4096\n" \
"s_mov_b32 s54, 1376256\n" \
"buffer_load_dwordx4 v0, s[40:43], s54 offen sc1 lds\n" \
"s_add_u32 m0, s52, 5120\n" \
"s_mov_b32 s54, 1392640\n" \
"buffer_load_dwordx4 v0, s[40:43], s54 offen sc1 lds\n" \
"s_add_u32 m0, s52, 6144\n" \
"s_mov_b32 s54, 1409024\n" \
"buffer_load_dwordx4 v0, s[40:43], s54 offen sc1 lds\n" \
"s_add_u32 m0, s52, 7168\n" \
"s_mov_b32 s54, 1425408\n" \
"buffer_load_dwordx4 v0, s[40:43], s54 offen sc1 lds\n" \
"s_mov_b32 s54, s99\n" \
".Lswo_z21_%=:\n" \
"s_sleep 1\n" \
"s_sub_u32 s54, s54, 1\n" \
"s_cbranch_scc0 .Lswo_z21_%=\n" \
"s_waitcnt vmcnt(36)\n" \
"ds_read_b64_tr_b16 a[48:49], v1 offset:8192\n" \
"ds_read_b64_tr_b16 a[50:51], v1 offset:10240\n" \
"ds_read_b64_tr_b16 a[176:177], v1 offset:8224\n" \
"ds_read_b64_tr_b16 a[178:179], v1 offset:10272\n" \
"ds_read_b64_tr_b16 v[176:177], v1 offset:8256\n" \
"ds_read_b64_tr_b16 v[178:179], v1 offset:10304\n" \
"ds_read_b64_tr_b16 v[64:65], v1 offset:8288\n" \
"ds_read_b64_tr_b16 v[66:67], v1 offset:10336\n" \
"s_waitcnt lgkmcnt(0)\n" \
"s_add_u32 m0, s52, 8192\n" \
"s_mov_b32 s54, 1441792\n" \
"buffer_load_dwordx4 v0, s[40:43], s54 offen sc1 lds\n" \
"s_add_u32 m0, s52, 9216\n" \
"s_mov_b32 s54, 1458176\n" \
"buffer_load_dwordx4 v0, s[40:43], s54 offen sc1 lds\n" \
"s_add_u32 m0, s52, 10240\n" \
"s_mov_b32 s54, 1474560\n" \
"buffer_load_dwordx4 v0, s[40:43], s54 offen sc1 lds\n" \
"s_add_u32 m0, s52, 11264\n" \
"s_mov_b32 s54, 1490944\n" \
"buffer_load_dwordx4 v0, s[40:43], s54 offen sc1 lds\n" \
"s_mov_b32 s54, s99\n" \
".Lswo_z22_%=:\n" \
"s_sleep 1\n" \
"s_sub_u32 s54, s54, 1\n" \
"s_cbranch_scc0 .Lswo_z22_%=\n" \
"s_waitcnt vmcnt(36)\n" \
"ds_read_b64_tr_b16 a[52:53], v1 offset:12288\n" \
"ds_read_b64_tr_b16 a[54:55], v1 offset:14336\n" \
"ds_read_b64_tr_b16 a[180:181], v1 offset:12320\n" \
"ds_read_b64_tr_b16 a[182:183], v1 offset:14368\n" \
"ds_read_b64_tr_b16 v[180:181], v1 offset:12352\n" \
"ds_read_b64_tr_b16 v[182:183], v1 offset:14400\n" \
"ds_read_b64_tr_b16 v[68:69], v1 offset:12384\n" \
"ds_read_b64_tr_b16 v[70:71], v1 offset:14432\n" \
"s_waitcnt lgkmcnt(0)\n" \
"s_add_u32 m0, s52, 12288\n" \
"s_mov_b32 s54, 1507328\n" \
"buffer_load_dwordx4 v0, s[40:43], s54 offen sc1 lds\n" \
"s_add_u32 m0, s52, 13312\n" \
"s_mov_b32 s54, 1523712\n" \
"buffer_load_dwordx4 v0, s[40:43], s54 offen sc1 lds\n" \
"s_add_u32 m0, s52, 14336\n" \
"s_mov_b32 s54, 1540096\n" \
"buffer_load_dwordx4 v0, s[40:43], s54 offen sc1 lds\n" \
"s_add_u32 m0, s52, 15360\n" \
"s_mov_b32 s54, 1556480\n" \
"buffer_load_dwordx4 v0, s[40:43], s54 offen sc1 lds\n" \
"s_mov_b32 s54, s99\n" \
".Lswo_z23_%=:\n" \
"s_sleep 1\n" \
"s_sub_u32 s54, s54, 1\n" \
"s_cbranch_scc0 .Lswo_z23_%=\n" \
"s_waitcnt vmcnt(36)\n" \
"ds_read_b64_tr_b16 a[56:57], v1 offset:16384\n" \
"ds_read_b64_tr_b16 a[58:59], v1 offset:18432\n" \
"ds_read_b64_tr_b16 a[184:185], v1 offset:16416\n" \
"ds_read_b64_tr_b16 a[186:187], v1 offset:18464\n" \
"ds_read_b64_tr_b16 v[184:185], v1 offset:16448\n" \
"ds_read_b64_tr_b16 v[186:187], v1 offset:18496\n" \
"ds_read_b64_tr_b16 v[72:73], v1 offset:16480\n" \
"ds_read_b64_tr_b16 v[74:75], v1 offset:18528\n" \
"s_waitcnt lgkmcnt(0)\n" \
"s_add_u32 m0, s52, 16384\n" \
"s_mov_b32 s54, 1572864\n" \
"buffer_load_dwordx4 v0, s[40:43], s54 offen sc1 lds\n" \
"s_add_u32 m0, s52, 17408\n" \
"s_mov_b32 s54, 1589248\n" \
"buffer_load_dwordx4 v0, s[40:43], s54 offen sc1 lds\n" \
"s_add_u32 m0, s52, 18432\n" \
"s_mov_b32 s54, 1605632\n" \
"buffer_load_dwordx4 v0, s[40:43], s54 offen sc1 lds\n" \
"s_add_u32 m0, s52, 19456\n" \
"s_mov_b32 s54, 1622016\n" \
"buffer_load_dwordx4 v0, s[40:43], s54 offen sc1 lds\n" \
"s_mov_b32 s54, s99\n" \
".Lswo_z24_%=:\n" \
"s_sleep 1\n" \
"s_sub_u32 s54, s54, 1\n" \
"s_cbranch_scc0 .Lswo_z24_%=\n" \
"s_waitcnt vmcnt(36)\n" \
"ds_read_b64_tr_b16 a[60:61], v1 offset:20480\n" \
"ds_read_b64_tr_b16 a[62:63], v1 offset:22528\n" \
"ds_read_b64_tr_b16 a[188:189], v1 offset:20512\n" \
"ds_read_b64_tr_b16 a[190:191], v1 offset:22560\n" \
"ds_read_b64_tr_b16 v[188:189], v1 offset:20544\n" \
"ds_read_b64_tr_b16 v[190:191], v1 offset:22592\n" \
"ds_read_b64_tr_b16 v[76:77], v1 offset:20576\n" \
"ds_read_b64_tr_b16 v[78:79], v1 offset:22624\n" \
"s_waitcnt lgkmcnt(0)\n" \
"s_add_u32 m0, s52, 20480\n" \
"s_mov_b32 s54, 1638400\n" \
"buffer_load_dwordx4 v0, s[40:43], s54 offen sc1 lds\n" \
"s_add_u32 m0, s52, 21504\n" \
"s_mov_b32 s54, 1654784\n" \
"buffer_load_dwordx4 v0, s[40:43], s54 offen sc1 lds\n" \
"s_add_u32 m0, s52, 22528\n" \
"s_mov_b32 s54, 1671168\n" \
"buffer_load_dwordx4 v0, s[40:43], s54 offen sc1 lds\n" \
"s_add_u32 m0, s52, 23552\n" \
"s_mov_b32 s54, 1687552\n" \
"buffer_load_dwordx4 v0, s[40:43], s54 offen sc1 lds\n" \
"s_mov_b32 s54, s99\n" \
".Lswo_z25_%=:\n" \
"s_sleep 1\n" \
"s_sub_u32 s54, s54, 1\n" \
"s_cbranch_scc0 .Lswo_z25_%=\n" \
"s_waitcnt vmcnt(36)\n" \
"ds_read_b64_tr_b16 a[64:65], v1 offset:24576\n" \
"ds_read_b64_tr_b16 a[66:67], v1 offset:26624\n" \
"ds_read_b64_tr_b16 a[192:193], v1 offset:24608\n" \
"ds_read_b64_tr_b16 a[194:195], v1 offset:26656\n" \
"ds_read_b64_tr_b16 v[192:193], v1 offset:24640\n" \
"ds_read_b64_tr_b16 v[194:195], v1 offset:26688\n" \
"ds_read_b64_tr_b16 v[80:81], v1 offset:24672\n" \
"ds_read_b64_tr_b16 v[82:83], v1 offset:26720\n" \
"s_waitcnt lgkmcnt(0)\n" \
"s_add_u32 m0, s52, 24576\n" \
"s_mov_b32 s54, 1703936\n" \
"buffer_load_dwordx4 v0, s[40:43], s54 offen sc1 lds\n" \
"s_add_u32 m0, s52, 25600\n" \
"s_mov_b32 s54, 1720320\n" \
"buffer_load_dwordx4 v0, s[40:43], s54 offen sc1 lds\n" \
"s_add_u32 m0, s52, 26624\n" \
"s_mov_b32 s54, 1736704\n" \
"buffer_load_dwordx4 v0, s[40:43], s54 offen sc1 lds\n" \
"s_add_u32 m0, s52, 27648\n" \
"s_mov_b32 s54, 1753088\n" \
"buffer_load_dwordx4 v0, s[40:43], s54 offen sc1 lds\n" \
"s_mov_b32 s54, s99\n" \
".Lswo_z26_%=:\n" \
"s_sleep 1\n" \
"s_sub_u32 s54, s54, 1\n" \
"s_cbranch_scc0 .Lswo_z26_%=\n" \
"s_waitcnt vmcnt(36)\n" \
"ds_read_b64_tr_b16 a[68:69], v1 offset:28672\n" \
"ds_read_b64_tr_b16 a[70:71], v1 offset:30720\n" \
"ds_read_b64_tr_b16 a[196:197], v1 offset:28704\n" \
"ds_read_b64_tr_b16 a[198:199], v1 offset:30752\n" \
"ds_read_b64_tr_b16 v[196:197], v1 offset:28736\n" \
"ds_read_b64_tr_b16 v[198:199], v1 offset:30784\n" \
"ds_read_b64_tr_b16 v[84:85], v1 offset:28768\n" \
"ds_read_b64_tr_b16 v[86:87], v1 offset:30816\n" \
"s_waitcnt lgkmcnt(0)\n" \
"s_add_u32 m0, s52, 28672\n" \
"s_mov_b32 s54, 1769472\n" \
"buffer_load_dwordx4 v0, s[40:43], s54 offen sc1 lds\n" \
"s_add_u32 m0, s52, 29696\n" \
"s_mov_b32 s54, 1785856\n" \
"buffer_load_dwordx4 v0, s[40:43], s54 offen sc1 lds\n" \
"s_add_u32 m0, s52, 30720\n" \
"s_mov_b32 s54, 1802240\n" \
"buffer_load_dwordx4 v0, s[40:43], s54 offen sc1 lds\n" \
"s_add_u32 m0, s52, 31744\n" \
"s_mov_b32 s54, 1818624\n" \
"buffer_load_dwordx4 v0, s[40:43], s54 offen sc1 lds\n" \
"s_mov_b32 s54, s99\n" \
".Lswo_z27_%=:\n" \
"s_sleep 1\n" \
"s_sub_u32 s54, s54, 1\n" \
"s_cbranch_scc0 .Lswo_z27_%=\n" \
"s_waitcnt vmcnt(36)\n" \
"ds_read_b64_tr_b16 a[72:73], v9 offset:32768\n" \
"ds_read_b64_tr_b16 a[74:75], v9 offset:34816\n" \
"ds_read_b64_tr_b16 a[200:201], v9 offset:32800\n" \
"ds_read_b64_tr_b16 a[202:203], v9 offset:34848\n" \
"ds_read_b64_tr_b16 v[200:201], v9 offset:32832\n" \
"ds_read_b64_tr_b16 v[202:203], v9 offset:34880\n" \
"ds_read_b64_tr_b16 v[88:89], v9 offset:32864\n" \
"ds_read_b64_tr_b16 v[90:91], v9 offset:34912\n" \
"s_waitcnt lgkmcnt(0)\n" \
"s_add_u32 m0, s53, 32768\n" \
"s_mov_b32 s54, 1835008\n" \
"buffer_load_dwordx4 v0, s[40:43], s54 offen sc1 lds\n" \
"s_add_u32 m0, s53, 33792\n" \
"s_mov_b32 s54, 1851392\n" \
"buffer_load_dwordx4 v0, s[40:43], s54 offen sc1 lds\n" \
"s_add_u32 m0, s53, 34816\n" \
"s_mov_b32 s54, 1867776\n" \
"buffer_load_dwordx4 v0, s[40:43], s54 offen sc1 lds\n" \
"s_add_u32 m0, s53, 35840\n" \
"s_mov_b32 s54, 1884160\n" \
"buffer_load_dwordx4 v0, s[40:43], s54 offen sc1 lds\n" \
"s_mov_b32 s54, s99\n" \
".Lswo_z28_%=:\n" \
"s_sleep 1\n" \
"s_sub_u32 s54, s54, 1\n" \
"s_cbranch_scc0 .Lswo_z28_%=\n" \
"s_waitcnt vmcnt(36)\n" \
"ds_read_b64_tr_b16 a[76:77], v9 offset:36864\n" \
"ds_read_b64_tr_b16 a[78:79], v9 offset:38912\n" \
"ds_read_b64_tr_b16 a[204:205], v9 offset:36896\n" \
"ds_read_b64_tr_b16 a[206:207], v9 offset:38944\n" \
"ds_read_b64_tr_b16 v[204:205], v9 offset:36928\n" \
"ds_read_b64_tr_b16 v[206:207], v9 offset:38976\n" \
"ds_read_b64_tr_b16 v[92:93], v9 offset:36960\n" \
"ds_read_b64_tr_b16 v[94:95], v9 offset:39008\n" \
"s_waitcnt lgkmcnt(0)\n" \
"s_add_u32 m0, s53, 36864\n" \
"s_mov_b32 s54, 1900544\n" \
"buffer_load_dwordx4 v0, s[40:43], s54 offen sc1 lds\n" \
"s_add_u32 m0, s53, 37888\n" \
"s_mov_b32 s54, 1916928\n" \
"buffer_load_dwordx4 v0, s[40:43], s54 offen sc1 lds\n" \
"s_add_u32 m0, s53, 38912\n" \
"s_mov_b32 s54, 1933312\n" \
"buffer_load_dwordx4 v0, s[40:43], s54 offen sc1 lds\n" \
"s_add_u32 m0, s53, 39936\n" \
"s_mov_b32 s54, 1949696\n" \
"buffer_load_dwordx4 v0, s[40:43], s54 offen sc1 lds\n" \
"s_mov_b32 s54, s99\n" \
".Lswo_z29_%=:\n" \
"s_sleep 1\n" \
"s_sub_u32 s54, s54, 1\n" \
"s_cbranch_scc0 .Lswo_z29_%=\n" \
"s_waitcnt vmcnt(36)\n" \
"ds_read_b64_tr_b16 a[80:81], v1 offset:0\n" \
"ds_read_b64_tr_b16 a[82:83], v1 offset:2048\n" \
"ds_read_b64_tr_b16 a[208:209], v1 offset:32\n" \
"ds_read_b64_tr_b16 a[210:211], v1 offset:2080\n" \
"ds_read_b64_tr_b16 v[208:209], v1 offset:64\n" \
"ds_read_b64_tr_b16 v[210:211], v1 offset:2112\n" \
"ds_read_b64_tr_b16 v[96:97], v1 offset:96\n" \
"ds_read_b64_tr_b16 v[98:99], v1 offset:2144\n" \
"s_waitcnt lgkmcnt(0)\n" \
"s_add_u32 m0, s52, 0\n" \
"s_mov_b32 s54, 1966080\n" \
"buffer_load_dwordx4 v0, s[40:43], s54 offen sc1 lds\n" \
"s_add_u32 m0, s52, 1024\n" \
"s_mov_b32 s54, 1982464\n" \
"buffer_load_dwordx4 v0, s[40:43], s54 offen sc1 lds\n" \
"s_add_u32 m0, s52, 2048\n" \
"s_mov_b32 s54, 1998848\n" \
"buffer_load_dwordx4 v0, s[40:43], s54 offen sc1 lds\n" \
"s_add_u32 m0, s52, 3072\n" \
"s_mov_b32 s54, 2015232\n" \
"buffer_load_dwordx4 v0, s[40:43], s54 offen sc1 lds\n" \
"s_mov_b32 s54, s99\n" \
".Lswo_z30_%=:\n" \
"s_sleep 1\n" \
"s_sub_u32 s54, s54, 1\n" \
"s_cbranch_scc0 .Lswo_z30_%=\n" \
"s_waitcnt vmcnt(36)\n" \
"ds_read_b64_tr_b16 a[84:85], v1 offset:4096\n" \
"ds_read_b64_tr_b16 a[86:87], v1 offset:6144\n" \
"ds_read_b64_tr_b16 a[212:213], v1 offset:4128\n" \
"ds_read_b64_tr_b16 a[214:215], v1 offset:6176\n" \
"ds_read_b64_tr_b16 v[212:213], v1 offset:4160\n" \
"ds_read_b64_tr_b16 v[214:215], v1 offset:6208\n" \
"ds_read_b64_tr_b16 v[100:101], v1 offset:4192\n" \
"ds_read_b64_tr_b16 v[102:103], v1 offset:6240\n" \
"s_waitcnt lgkmcnt(0)\n" \
"s_add_u32 m0, s52, 4096\n" \
"s_mov_b32 s54, 2031616\n" \
"buffer_load_dwordx4 v0, s[40:43], s54 offen sc1 lds\n" \
"s_add_u32 m0, s52, 5120\n" \
"s_mov_b32 s54, 2048000\n" \
"buffer_load_dwordx4 v0, s[40:43], s54 offen sc1 lds\n" \
"s_add_u32 m0, s52, 6144\n" \
"s_mov_b32 s54, 2064384\n" \
"buffer_load_dwordx4 v0, s[40:43], s54 offen sc1 lds\n" \
"s_add_u32 m0, s52, 7168\n" \
"s_mov_b32 s54, 2080768\n" \
"buffer_load_dwordx4 v0, s[40:43], s54 offen sc1 lds\n" \
"s_mov_b32 s54, s99\n" \
".Lswo_z31_%=:\n" \
"s_sleep 1\n" \
"s_sub_u32 s54, s54, 1\n" \
"s_cbranch_scc0 .Lswo_z31_%=\n" \
"s_waitcnt vmcnt(36)\n" \
"ds_read_b64_tr_b16 a[88:89], v1 offset:8192\n" \
"ds_read_b64_tr_b16 a[90:91], v1 offset:10240\n" \
"ds_read_b64_tr_b16 a[216:217], v1 offset:8224\n" \
"ds_read_b64_tr_b16 a[218:219], v1 offset:10272\n" \
"ds_read_b64_tr_b16 v[216:217], v1 offset:8256\n" \
"ds_read_b64_tr_b16 v[218:219], v1 offset:10304\n" \
"ds_read_b64_tr_b16 v[104:105], v1 offset:8288\n" \
"ds_read_b64_tr_b16 v[106:107], v1 offset:10336\n" \
"s_waitcnt lgkmcnt(0)\n" \
"ds_write_b128 v2, v[16:19] offset:8192\n" \
"ds_write_b128 v2, v[20:23] offset:9216\n" \
"ds_write_b128 v2, v[24:27] offset:10240\n" \
"ds_write_b128 v2, v[28:31] offset:11264\n" \
"s_waitcnt vmcnt(32)\n" \
"ds_read_b64_tr_b16 a[92:93], v1 offset:12288\n" \
"ds_read_b64_tr_b16 a[94:95], v1 offset:14336\n" \
"ds_read_b64_tr_b16 a[220:221], v1 offset:12320\n" \
"ds_read_b64_tr_b16 a[222:223], v1 offset:14368\n" \
"ds_read_b64_tr_b16 v[220:221], v1 offset:12352\n" \
"ds_read_b64_tr_b16 v[222:223], v1 offset:14400\n" \
"ds_read_b64_tr_b16 v[108:109], v1 offset:12384\n" \
"ds_read_b64_tr_b16 v[110:111], v1 offset:14432\n" \
"s_waitcnt lgkmcnt(0)\n" \
"ds_write_b128 v2, v[32:35] offset:12288\n" \
"ds_write_b128 v2, v[36:39] offset:13312\n" \
"ds_write_b128 v2, v[40:43] offset:14336\n" \
"ds_write_b128 v2, v[44:47] offset:15360\n" \
"s_waitcnt vmcnt(28)\n" \
"ds_read_b64_tr_b16 a[96:97], v1 offset:16384\n" \
"ds_read_b64_tr_b16 a[98:99], v1 offset:18432\n" \
"ds_read_b64_tr_b16 a[224:225], v1 offset:16416\n" \
"ds_read_b64_tr_b16 a[226:227], v1 offset:18464\n" \
"ds_read_b64_tr_b16 v[224:225], v1 offset:16448\n" \
"ds_read_b64_tr_b16 v[226:227], v1 offset:18496\n" \
"ds_read_b64_tr_b16 v[16:17], v1 offset:16480\n" \
"ds_read_b64_tr_b16 v[18:19], v1 offset:18528\n" \
"s_waitcnt lgkmcnt(0)\n" \
"ds_write_b128 v2, v[48:51] offset:16384\n" \
"ds_write_b128 v2, v[52:55] offset:17408\n" \
"ds_write_b128 v2, v[56:59] offset:18432\n" \
"ds_write_b128 v2, v[60:63] offset:19456\n" \
"s_waitcnt vmcnt(24)\n" \
"ds_read_b64_tr_b16 a[100:101], v1 offset:20480\n" \
"ds_read_b64_tr_b16 a[102:103], v1 offset:22528\n" \
"ds_read_b64_tr_b16 a[228:229], v1 offset:20512\n" \
"ds_read_b64_tr_b16 a[230:231], v1 offset:22560\n" \
"ds_read_b64_tr_b16 v[228:229], v1 offset:20544\n" \
"ds_read_b64_tr_b16 v[230:231], v1 offset:22592\n" \
"ds_read_b64_tr_b16 v[20:21], v1 offset:20576\n" \
"ds_read_b64_tr_b16 v[22:23], v1 offset:22624\n" \
"s_waitcnt lgkmcnt(0)\n" \
"ds_write_b128 v2, v[64:67] offset:20480\n" \
"ds_write_b128 v2, v[68:71] offset:21504\n" \
"ds_write_b128 v2, v[72:75] offset:22528\n" \
"ds_write_b128 v2, v[76:79] offset:23552\n" \
"s_waitcnt vmcnt(20)\n" \
"ds_read_b64_tr_b16 a[104:105], v1 offset:24576\n" \
"ds_read_b64_tr_b16 a[106:107], v1 offset:26624\n" \
"ds_read_b64_tr_b16 a[232:233], v1 offset:24608\n" \
"ds_read_b64_tr_b16 a[234:235], v1 offset:26656\n" \
"ds_read_b64_tr_b16 v[232:233], v1 offset:24640\n" \
"ds_read_b64_tr_b16 v[234:235], v1 offset:26688\n" \
"ds_read_b64_tr_b16 v[24:25], v1 offset:24672\n" \
"ds_read_b64_tr_b16 v[26:27], v1 offset:26720\n" \
"s_waitcnt lgkmcnt(0)\n" \
"ds_write_b128 v2, v[80:83] offset:24576\n" \
"ds_write_b128 v2, v[84:87] offset:25600\n" \
"ds_write_b128 v2, v[88:91] offset:26624\n" \
"ds_write_b128 v2, v[92:95] offset:27648\n" \
"s_waitcnt vmcnt(16)\n" \
"ds_read_b64_tr_b16 a[108:109], v1 offset:28672\n" \
"ds_read_b64_tr_b16 a[110:111], v1 offset:30720\n" \
"ds_read_b64_tr_b16 a[236:237], v1 offset:28704\n" \
"ds_read_b64_tr_b16 a[238:239], v1 offset:30752\n" \
"ds_read_b64_tr_b16 v[236:237], v1 offset:28736\n" \
"ds_read_b64_tr_b16 v[238:239], v1 offset:30784\n" \
"ds_read_b64_tr_b16 v[28:29], v1 offset:28768\n" \
"ds_read_b64_tr_b16 v[30:31], v1 offset:30816\n" \
"s_waitcnt lgkmcnt(0)\n" \
"ds_write_b128 v2, v[96:99] offset:28672\n" \
"ds_write_b128 v2, v[100:103] offset:29696\n" \
"ds_write_b128 v2, v[104:107] offset:30720\n" \
"ds_write_b128 v2, v[108:111] offset:31744\n" \
"s_waitcnt vmcnt(12)\n" \
"ds_read_b64_tr_b16 a[112:113], v9 offset:32768\n" \
"ds_read_b64_tr_b16 a[114:115], v9 offset:34816\n" \
"ds_read_b64_tr_b16 a[240:241], v9 offset:32800\n" \
"ds_read_b64_tr_b16 a[242:243], v9 offset:34848\n" \
"ds_read_b64_tr_b16 v[240:241], v9 offset:32832\n" \
"ds_read_b64_tr_b16 v[242:243], v9 offset:34880\n" \
"ds_read_b64_tr_b16 v[32:33], v9 offset:32864\n" \
"ds_read_b64_tr_b16 v[34:35], v9 offset:34912\n" \
"s_waitcnt lgkmcnt(0)\n" \
"s_waitcnt vmcnt(8)\n" \
"ds_read_b64_tr_b16 a[116:117], v9 offset:36864\n" \
"ds_read_b64_tr_b16 a[118:119], v9 offset:38912\n" \
"ds_read_b64_tr_b16 a[244:245], v9 offset:36896\n" \
"ds_read_b64_tr_b16 a[246:247], v9 offset:38944\n" \
"ds_read_b64_tr_b16 v[244:245], v9 offset:36928\n" \
"ds_read_b64_tr_b16 v[246:247], v9 offset:38976\n" \
"ds_read_b64_tr_b16 v[36:37], v9 offset:36960\n" \
"ds_read_b64_tr_b16 v[38:39], v9 offset:39008\n" \
"s_waitcnt lgkmcnt(0)\n" \
"s_waitcnt vmcnt(4)\n" \
"ds_read_b64_tr_b16 a[120:121], v1 offset:0\n" \
"ds_read_b64_tr_b16 a[122:123], v1 offset:2048\n" \
"ds_read_b64_tr_b16 a[248:249], v1 offset:32\n" \
"ds_read_b64_tr_b16 a[250:251], v1 offset:2080\n" \
"ds_read_b64_tr_b16 v[248:249], v1 offset:64\n" \
"ds_read_b64_tr_b16 v[250:251], v1 offset:2112\n" \
"ds_read_b64_tr_b16 v[40:41], v1 offset:96\n" \
"ds_read_b64_tr_b16 v[42:43], v1 offset:2144\n" \
"s_waitcnt lgkmcnt(0)\n" \
"ds_write_b128 v2, v[16:19] offset:0\n" \
"ds_write_b128 v2, v[20:23] offset:1024\n" \
"ds_write_b128 v2, v[24:27] offset:2048\n" \
"ds_write_b128 v2, v[28:31] offset:3072\n" \
"s_waitcnt vmcnt(0)\n" \
"ds_read_b64_tr_b16 a[124:125], v1 offset:4096\n" \
"ds_read_b64_tr_b16 a[126:127], v1 offset:6144\n" \
"ds_read_b64_tr_b16 a[252:253], v1 offset:4128\n" \
"ds_read_b64_tr_b16 a[254:255], v1 offset:6176\n" \
"ds_read_b64_tr_b16 v[252:253], v1 offset:4160\n" \
"ds_read_b64_tr_b16 v[254:255], v1 offset:6208\n" \
"ds_read_b64_tr_b16 v[44:45], v1 offset:4192\n" \
"ds_read_b64_tr_b16 v[46:47], v1 offset:6240\n" \
"s_waitcnt lgkmcnt(0)\n" \
"ds_write_b128 v2, v[32:35] offset:4096\n" \
"ds_write_b128 v2, v[36:39] offset:5120\n" \
"ds_write_b128 v2, v[40:43] offset:6144\n" \
"ds_write_b128 v2, v[44:47] offset:7168\n" \
"s_waitcnt vmcnt(0) lgkmcnt(0)\n" \
"s_barrier\n" \
"s_add_u32 s86, s85, 0\n" \
"s_add_u32 s87, s85, 4259840\n" \
"s_add_u32 s88, s85, 8519680\n" \
"s_add_u32 s89, s85, 12779520\n" \
"s_add_u32 s90, s85, 17039360\n" \
"s_add_u32 s91, s85, 21299200\n" \
"s_add_u32 s92, s85, 25559040\n" \
"s_add_u32 s93, s85, 29818880\n" \
"s_mov_b32 s55, 0\n" \
"s_mov_b32 s60, 0\n" \
"s_mov_b32 s79, s95\n" \
"s_mov_b32 s98, 0x70000000\n" \
".Lswo_prime_%=:\n" \
"s_lshr_b32 s59, s44, 1\n" \
"s_add_u32 s59, s59, 1\n" \
"s_lshr_b32 s56, s44, 1\n" \
"s_lshl_b32 s56, s56, 16\n" \
"s_lshr_b32 s58, s56, 8\n" \
"s_and_b32 s58, s58, 0x2000\n" \
"s_xor_b32 s56, s56, s58\n" \
"s_and_b32 s58, s44, 1\n" \
"s_lshl_b32 s58, s58, 12\n" \
"s_or_b32 s56, s56, s58\n" \
"s_lshl_b32 s54, s44, 16\n" \
"s_add_u32 s54, s54, s78\n" \
"s_lshr_b32 s58, s54, 8\n" \
"s_and_b32 s58, s58, 0x2000\n" \
"s_xor_b32 s54, s54, s58\n" \
"s_add_u32 s54, s54, 8388608\n" \
"s_lshl_b32 s77, s44, 15\n" \
"s_or_b32 s77, s77, s98\n" \
"s_or_b32 s54, s54, s98\n" \
"buffer_load_dwordx2 v[14:15], v6, s[36:39], s54 offen sc1\n" \
"buffer_load_dwordx2 v[12:13], v5, s[64:67], s77 offen\n" \
"buffer_load_dword v11, v4, s[36:39], 0 offen sc1\n" \
"s_waitcnt vmcnt(0)\n" \
"v_cmp_gt_u32_e64 s[62:63], s59, v11\n" \
"s_nop 4\n" \
"s_cmp_lg_u64 s[62:63], 0\n" \
"s_cbranch_scc1 .Lswo_w_%=\n" \
".Lswo_wk_%=:\n" \
"s_mov_b32 s76, 0x7ffffff0\n" \
"s_mov_b32 s81, 1\n" \
"s_mov_b32 s84, 0\n" \
"s_waitcnt vmcnt(0) lgkmcnt(0)\n" \
"s_xor_b32 s57, s56, s86\n" \
"buffer_load_dwordx4 v[16:19], v3, s[36:39], s57 offen offset:0 sc1\n" \
"buffer_load_dwordx4 v[20:23], v3, s[36:39], s57 offen offset:1024 sc1\n" \
"buffer_load_dwordx4 v[24:27], v3, s[36:39], s57 offen offset:2048 sc1\n" \
"buffer_load_dwordx4 v[28:31], v3, s[36:39], s57 offen offset:3072 sc1\n" \
"s_xor_b32 s57, s56, s87\n" \
"buffer_load_dwordx4 v[32:35], v3, s[36:39], s57 offen offset:0 sc1\n" \
"buffer_load_dwordx4 v[36:39], v3, s[36:39], s57 offen offset:1024 sc1\n" \
"buffer_load_dwordx4 v[40:43], v3, s[36:39], s57 offen offset:2048 sc1\n" \
"buffer_load_dwordx4 v[44:47], v3, s[36:39], s57 offen offset:3072 sc1\n" \
"s_xor_b32 s57, s56, s88\n" \
"buffer_load_dwordx4 v[48:51], v3, s[36:39], s57 offen offset:0 sc1\n" \
"buffer_load_dwordx4 v[52:55], v3, s[36:39], s57 offen offset:1024 sc1\n" \
"buffer_load_dwordx4 v[56:59], v3, s[36:39], s57 offen offset:2048 sc1\n" \
"buffer_load_dwordx4 v[60:63], v3, s[36:39], s57 offen offset:3072 sc1\n" \
"s_xor_b32 s57, s56, s89\n" \
"buffer_load_dwordx4 v[64:67], v3, s[36:39], s57 offen offset:0 sc1\n" \
"buffer_load_dwordx4 v[68:71], v3, s[36:39], s57 offen offset:1024 sc1\n" \
"buffer_load_dwordx4 v[72:75], v3, s[36:39], s57 offen offset:2048 sc1\n" \
"buffer_load_dwordx4 v[76:79], v3, s[36:39], s57 offen offset:3072 sc1\n" \
"ds_read_b128 v[80:83], v2 offset:8192\n" \
"ds_read_b128 v[84:87], v2 offset:9216\n" \
"ds_read_b128 v[88:91], v2 offset:10240\n" \
"s_waitcnt lgkmcnt(0)\n" \
"v_mov_b32 v9, 0x7ffffff0\n" \
"buffer_store_dword v9, v9, s[36:39], 0 offen\n" \
"s_waitcnt vmcnt(16)\n" \
"s_nop 7\n" \
"s_nop 7\n" \
".p2align 6\n" \
".Lswo_loop_%=:\n" \
"v_mfma_f32_16x16x32_bf16 v[96:99], a[0:3], v[16:19], 0\n" \
"ds_write_b128 v7, v[104:107] offset:2048\n" \
"v_xor_b32_e64 v7, 64, v7\n" \
"v_mfma_f32_16x16x32_bf16 v[100:103], a[128:131], v[16:19], 0\n" \
"ds_write_b128 v7, v[108:111] offset:3072\n" \
"ds_read_b128 v[92:95], v2 offset:11264\n" \
"v_xor_b32 v7, 192, v7\n" \
"v_mfma_f32_16x16x32_bf16 v[104:107], v[128:131], v[16:19], 0\n" \
"s_waitcnt vmcnt(15) lgkmcnt(7)\n" \
"v_mfma_f32_16x16x32_bf16 v[108:111], v[80:83], v[16:19], 0\n" \
"s_xor_b32 s57, s56, s90\n" \
"buffer_load_dwordx4 v[16:19], v3, s[36:39], s57 offen offset:0 sc1\n" \
"v_mfma_f32_16x16x32_bf16 v[96:99], a[4:7], v[20:23], v[96:99]\n" \
"s_cmp_lt_i32 s44, s50\n" \
"s_cselect_b32 s82, s45, s47\n" \
"v_mfma_f32_16x16x32_bf16 v[100:103], a[132:135], v[20:23], v[100:103]\n" \
"ds_read_b128 v[80:83], v2 offset:12288\n" \
"v_mfma_f32_16x16x32_bf16 v[104:107], v[132:135], v[20:23], v[104:107]\n" \
"s_waitcnt vmcnt(15) lgkmcnt(7)\n" \
"v_mfma_f32_16x16x32_bf16 v[108:111], v[84:87], v[20:23], v[108:111]\n" \
"buffer_load_dwordx4 v[20:23], v3, s[36:39], s57 offen offset:1024 sc1\n" \
"v_mfma_f32_16x16x32_bf16 v[96:99], a[8:11], v[24:27], v[96:99]\n" \
"s_add_u32 s82, s82, s44\n" \
"s_cmp_lt_u32 s82, 128\n" \
"v_mfma_f32_16x16x32_bf16 v[100:103], a[136:139], v[24:27], v[100:103]\n" \
"ds_read_b128 v[84:87], v2 offset:13312\n" \
"s_cselect_b32 s82, s82, s94\n" \
"v_mfma_f32_16x16x32_bf16 v[104:107], v[136:139], v[24:27], v[104:107]\n" \
"s_waitcnt vmcnt(15) lgkmcnt(7)\n" \
"v_mfma_f32_16x16x32_bf16 v[108:111], v[88:91], v[24:27], v[108:111]\n" \
"buffer_load_dwordx4 v[24:27], v3, s[36:39], s57 offen offset:2048 sc1\n" \
"v_mfma_f32_16x16x32_bf16 v[96:99], a[12:15], v[28:31], v[96:99]\n" \
"s_waitcnt lgkmcnt(3)\n" \
"s_cmp_lg_u32 s81, 0\n" \
"s_cbranch_scc1 .Lswo_nb_%=\n" \
"s_barrier\n" \
".Lswo_nb_%=:\n" \
"v_mfma_f32_16x16x32_bf16 v[100:103], a[140:143], v[28:31], v[100:103]\n" \
"ds_read_b128 v[88:91], v2 offset:14336\n" \
"v_xor_b32_e64 v7, s84, v7\n" \
"v_mfma_f32_16x16x32_bf16 v[104:107], v[140:143], v[28:31], v[104:107]\n" \
"s_waitcnt vmcnt(15) lgkmcnt(3)\n" \
"s_lshr_b32 s83, s82, 1\n" \
"v_mfma_f32_16x16x32_bf16 v[108:111], v[92:95], v[28:31], v[108:111]\n" \
"buffer_load_dwordx4 v[28:31], v3, s[36:39], s57 offen offset:3072 sc1\n" \
"v_mfma_f32_16x16x32_bf16 v[96:99], a[16:19], v[32:35], v[96:99]\n" \
"ds_read_b128 v[112:115], v8 offset:0\n" \
"s_lshl_b32 s83, s83, 16\n" \
"v_mfma_f32_16x16x32_bf16 v[100:103], a[144:147], v[32:35], v[100:103]\n" \
"ds_read_b128 v[92:95], v2 offset:15360\n" \
"v_mfma_f32_16x16x32_bf16 v[104:107], v[144:147], v[32:35], v[104:107]\n" \
"s_waitcnt vmcnt(15) lgkmcnt(4)\n" \
"s_lshr_b32 s58, s83, 8\n" \
"v_mfma_f32_16x16x32_bf16 v[108:111], v[80:83], v[32:35], v[108:111]\n" \
"s_xor_b32 s57, s56, s91\n" \
"buffer_load_dwordx4 v[32:35], v3, s[36:39], s57 offen offset:0 sc1\n" \
"v_mfma_f32_16x16x32_bf16 v[96:99], a[20:23], v[36:39], v[96:99]\n" \
"ds_read_b128 v[116:119], v8 offset:4096\n" \
"s_and_b32 s58, s58, 0x2000\n" \
"v_mfma_f32_16x16x32_bf16 v[100:103], a[148:151], v[36:39], v[100:103]\n" \
"ds_read_b128 v[80:83], v2 offset:16384\n" \
"v_mfma_f32_16x16x32_bf16 v[104:107], v[148:151], v[36:39], v[104:107]\n" \
"s_waitcnt vmcnt(15) lgkmcnt(5)\n" \
"s_xor_b32 s83, s83, s58\n" \
"v_mfma_f32_16x16x32_bf16 v[108:111], v[84:87], v[36:39], v[108:111]\n" \
"buffer_load_dwordx4 v[36:39], v3, s[36:39], s57 offen offset:1024 sc1\n" \
"v_mfma_f32_16x16x32_bf16 v[96:99], a[24:27], v[40:43], v[96:99]\n" \
"ds_read_b128 v[120:123], v8 offset:8192\n" \
"s_and_b32 s58, s82, 1\n" \
"v_mfma_f32_16x16x32_bf16 v[100:103], a[152:155], v[40:43], v[100:103]\n" \
"ds_read_b128 v[84:87], v2 offset:17408\n" \
"v_mfma_f32_16x16x32_bf16 v[104:107], v[152:155], v[40:43], v[104:107]\n" \
"s_waitcnt vmcnt(15) lgkmcnt(6)\n" \
"s_lshl_b32 s58, s58, 12\n" \
"v_mfma_f32_16x16x32_bf16 v[108:111], v[88:91], v[40:43], v[108:111]\n" \
"buffer_load_dwordx4 v[40:43], v3, s[36:39], s57 offen offset:2048 sc1\n" \
"v_mfma_f32_16x16x32_bf16 v[96:99], a[28:31], v[44:47], v[96:99]\n" \
"ds_read_b128 v[124:127], v8 offset:12288\n" \
"s_or_b32 s83, s83, s58\n" \
"v_mfma_f32_16x16x32_bf16 v[100:103], a[156:159], v[44:47], v[100:103]\n" \
"ds_read_b128 v[88:91], v2 offset:18432\n" \
"v_mfma_f32_16x16x32_bf16 v[104:107], v[156:159], v[44:47], v[104:107]\n" \
"s_waitcnt vmcnt(15) lgkmcnt(6)\n" \
"v_mfma_f32_16x16x32_bf16 v[108:111], v[92:95], v[44:47], v[108:111]\n" \
"buffer_load_dwordx4 v[44:47], v3, s[36:39], s57 offen offset:3072 sc1\n" \
"v_mfma_f32_16x16x32_bf16 v[96:99], a[32:35], v[48:51], v[96:99]\n" \
"s_lshr_b32 s59, s82, 1\n" \
"s_add_u32 s59, s59, 1\n" \
"v_mfma_f32_16x16x32_bf16 v[100:103], a[160:163], v[48:51], v[100:103]\n" \
"ds_read_b128 v[92:95], v2 offset:19456\n" \
"v_mfma_f32_16x16x32_bf16 v[104:107], v[160:163], v[48:51], v[104:107]\n" \
"s_waitcnt vmcnt(15) lgkmcnt(5)\n" \
"v_mfma_f32_16x16x32_bf16 v[108:111], v[80:83], v[48:51], v[108:111]\n" \
"s_xor_b32 s57, s56, s92\n" \
"buffer_load_dwordx4 v[48:51], v3, s[36:39], s57 offen offset:0 sc1\n" \
"v_mfma_f32_16x16x32_bf16 v[96:99], a[36:39], v[52:55], v[96:99]\n" \
"v_add_f32 v112, v112, v116\n" \
"v_mfma_f32_16x16x32_bf16 v[100:103], a[164:167], v[52:55], v[100:103]\n" \
"ds_read_b128 v[80:83], v2 offset:20480\n" \
"v_add_f32_e64 v113, v113, v117\n" \
"v_mfma_f32_16x16x32_bf16 v[104:107], v[164:167], v[52:55], v[104:107]\n" \
"s_waitcnt vmcnt(15) lgkmcnt(4)\n" \
"v_add_f32 v114, v114, v118\n" \
"v_mfma_f32_16x16x32_bf16 v[108:111], v[84:87], v[52:55], v[108:111]\n" \
"buffer_load_dwordx4 v[52:55], v3, s[36:39], s57 offen offset:1024 sc1\n" \
"v_add_f32_e64 v115, v115, v119\n" \
"v_mfma_f32_16x16x32_bf16 v[96:99], a[40:43], v[56:59], v[96:99]\n" \
"s_waitcnt lgkmcnt(3)\n" \
"v_add_f32 v120, v120, v124\n" \
"v_mfma_f32_16x16x32_bf16 v[100:103], a[168:171], v[56:59], v[100:103]\n" \
"ds_read_b128 v[84:87], v2 offset:21504\n" \
"v_add_f32_e64 v121, v121, v125\n" \
"v_mfma_f32_16x16x32_bf16 v[104:107], v[168:171], v[56:59], v[104:107]\n" \
"s_waitcnt vmcnt(15) lgkmcnt(3)\n" \
"v_add_f32 v122, v122, v126\n" \
"v_mfma_f32_16x16x32_bf16 v[108:111], v[88:91], v[56:59], v[108:111]\n" \
"buffer_load_dwordx4 v[56:59], v3, s[36:39], s57 offen offset:2048 sc1\n" \
"v_add_f32_e64 v123, v123, v127\n" \
"v_mfma_f32_16x16x32_bf16 v[96:99], a[44:47], v[60:63], v[96:99]\n" \
"v_add_f32_e64 v112, v112, v120\n" \
"v_mfma_f32_16x16x32_bf16 v[100:103], a[172:175], v[60:63], v[100:103]\n" \
"ds_read_b128 v[88:91], v2 offset:22528\n" \
"v_add_f32_e64 v113, v113, v121\n" \
"v_mfma_f32_16x16x32_bf16 v[104:107], v[172:175], v[60:63], v[104:107]\n" \
"s_waitcnt vmcnt(15) lgkmcnt(3)\n" \
"v_add_f32 v114, v114, v122\n" \
"v_mfma_f32_16x16x32_bf16 v[108:111], v[92:95], v[60:63], v[108:111]\n" \
"buffer_load_dwordx4 v[60:63], v3, s[36:39], s57 offen offset:3072 sc1\n" \
"v_add_f32_e64 v115, v115, v123\n" \
"v_mfma_f32_16x16x32_bf16 v[96:99], a[48:51], v[64:67], v[96:99]\n" \
"v_xor_b32_e64 v8, s84, v8\n" \
"v_mfma_f32_16x16x32_bf16 v[100:103], a[176:179], v[64:67], v[100:103]\n" \
"ds_read_b128 v[92:95], v2 offset:23552\n" \
"v_cvt_pk_bf16_f32 v0, v112, v113\n" \
"v_mfma_f32_16x16x32_bf16 v[104:107], v[176:179], v[64:67], v[104:107]\n" \
"s_waitcnt vmcnt(14) lgkmcnt(3)\n" \
"v_cvt_pk_bf16_f32 v1, v114, v115\n" \
"v_mfma_f32_16x16x32_bf16 v[108:111], v[80:83], v[64:67], v[108:111]\n" \
"s_xor_b32 s57, s56, s93\n" \
"buffer_load_dwordx4 v[64:67], v3, s[36:39], s57 offen offset:0 sc1\n" \
"v_mfma_f32_16x16x32_bf16 v[96:99], a[52:55], v[68:71], v[96:99]\n" \
"buffer_store_dwordx2 v[0:1], v6, s[36:39], s76 offen sc1\n" \
"v_cmp_gt_u32_e64 s[62:63], s59, v11\n" \
"v_mfma_f32_16x16x32_bf16 v[100:103], a[180:183], v[68:71], v[100:103]\n" \
"ds_read_b128 v[80:83], v2 offset:24576\n" \
"v_mfma_f32_16x16x32_bf16 v[104:107], v[180:183], v[68:71], v[104:107]\n" \
"s_waitcnt vmcnt(15) lgkmcnt(3)\n" \
"v_mfma_f32_16x16x32_bf16 v[108:111], v[84:87], v[68:71], v[108:111]\n" \
"buffer_load_dwordx4 v[68:71], v3, s[36:39], s57 offen offset:1024 sc1\n" \
"v_mfma_f32_16x16x32_bf16 v[96:99], a[56:59], v[72:75], v[96:99]\n" \
"s_add_u32 s54, s55, 1\n" \
"s_cmp_lt_u32 s54, s79\n" \
"v_mfma_f32_16x16x32_bf16 v[100:103], a[184:187], v[72:75], v[100:103]\n" \
"ds_read_b128 v[84:87], v2 offset:25600\n" \
"s_cselect_b64 s[62:63], s[62:63], -1\n" \
"v_mfma_f32_16x16x32_bf16 v[104:107], v[184:187], v[72:75], v[104:107]\n" \
"s_waitcnt vmcnt(15) lgkmcnt(3)\n" \
"v_mfma_f32_16x16x32_bf16 v[108:111], v[88:91], v[72:75], v[108:111]\n" \
"buffer_load_dwordx4 v[72:75], v3, s[36:39], s57 offen offset:2048 sc1\n" \
"v_mfma_f32_16x16x32_bf16 v[96:99], a[60:63], v[76:79], v[96:99]\n" \
"s_cmp_lg_u64 s[62:63], 0\n" \
"s_cselect_b32 s81, 1, 0\n" \
"v_mfma_f32_16x16x32_bf16 v[100:103], a[188:191], v[76:79], v[100:103]\n" \
"ds_read_b128 v[88:91], v2 offset:26624\n" \
"s_cselect_b32 s83, 0x70000000, s83\n" \
"v_mfma_f32_16x16x32_bf16 v[104:107], v[188:191], v[76:79], v[104:107]\n" \
"s_waitcnt vmcnt(15) lgkmcnt(3)\n" \
"v_mfma_f32_16x16x32_bf16 v[108:111], v[92:95], v[76:79], v[108:111]\n" \
"buffer_load_dwordx4 v[76:79], v3, s[36:39], s57 offen offset:3072 sc1\n" \
"v_mfma_f32_16x16x32_bf16 v[96:99], a[64:67], v[16:19], v[96:99]\n" \
"v_mfma_f32_16x16x32_bf16 v[100:103], a[192:195], v[16:19], v[100:103]\n" \
"ds_read_b128 v[92:95], v2 offset:27648\n" \
"v_mfma_f32_16x16x32_bf16 v[104:107], v[192:195], v[16:19], v[104:107]\n" \
"s_waitcnt vmcnt(15) lgkmcnt(3)\n" \
"s_mov_b32 s56, s83\n" \
"v_mfma_f32_16x16x32_bf16 v[108:111], v[80:83], v[16:19], v[108:111]\n" \
"s_xor_b32 s57, s56, s86\n" \
"buffer_load_dwordx4 v[16:19], v3, s[36:39], s57 offen offset:0 sc1\n" \
"v_mfma_f32_16x16x32_bf16 v[96:99], a[68:71], v[20:23], v[96:99]\n" \
"v_mfma_f32_16x16x32_bf16 v[100:103], a[196:199], v[20:23], v[100:103]\n" \
"ds_read_b128 v[80:83], v2 offset:28672\n" \
"v_mfma_f32_16x16x32_bf16 v[104:107], v[196:199], v[20:23], v[104:107]\n" \
"s_waitcnt vmcnt(15) lgkmcnt(3)\n" \
"v_mfma_f32_16x16x32_bf16 v[108:111], v[84:87], v[20:23], v[108:111]\n" \
"buffer_load_dwordx4 v[20:23], v3, s[36:39], s57 offen offset:1024 sc1\n" \
"v_mfma_f32_16x16x32_bf16 v[96:99], a[72:75], v[24:27], v[96:99]\n" \
"v_mfma_f32_16x16x32_bf16 v[100:103], a[200:203], v[24:27], v[100:103]\n" \
"ds_read_b128 v[84:87], v2 offset:29696\n" \
"v_mfma_f32_16x16x32_bf16 v[104:107], v[200:203], v[24:27], v[104:107]\n" \
"s_waitcnt vmcnt(15) lgkmcnt(3)\n" \
"v_mfma_f32_16x16x32_bf16 v[108:111], v[88:91], v[24:27], v[108:111]\n" \
"buffer_load_dwordx4 v[24:27], v3, s[36:39], s57 offen offset:2048 sc1\n" \
"v_mfma_f32_16x16x32_bf16 v[96:99], a[76:79], v[28:31], v[96:99]\n" \
"v_mfma_f32_16x16x32_bf16 v[100:103], a[204:207], v[28:31], v[100:103]\n" \
"ds_read_b128 v[88:91], v2 offset:30720\n" \
"v_mfma_f32_16x16x32_bf16 v[104:107], v[204:207], v[28:31], v[104:107]\n" \
"s_waitcnt vmcnt(15) lgkmcnt(3)\n" \
"v_mfma_f32_16x16x32_bf16 v[108:111], v[92:95], v[28:31], v[108:111]\n" \
"buffer_load_dwordx4 v[28:31], v3, s[36:39], s57 offen offset:3072 sc1\n" \
"v_mfma_f32_16x16x32_bf16 v[96:99], a[80:83], v[32:35], v[96:99]\n" \
"v_mfma_f32_16x16x32_bf16 v[100:103], a[208:211], v[32:35], v[100:103]\n" \
"ds_read_b128 v[92:95], v2 offset:31744\n" \
"v_mfma_f32_16x16x32_bf16 v[104:107], v[208:211], v[32:35], v[104:107]\n" \
"s_waitcnt vmcnt(15) lgkmcnt(3)\n" \
"v_mfma_f32_16x16x32_bf16 v[108:111], v[80:83], v[32:35], v[108:111]\n" \
"s_xor_b32 s57, s56, s87\n" \
"buffer_load_dwordx4 v[32:35], v3, s[36:39], s57 offen offset:0 sc1\n" \
"v_mfma_f32_16x16x32_bf16 v[96:99], a[84:87], v[36:39], v[96:99]\n" \
"v_mfma_f32_16x16x32_bf16 v[100:103], a[212:215], v[36:39], v[100:103]\n" \
"ds_read_b128 v[80:83], v2 offset:0\n" \
"v_mfma_f32_16x16x32_bf16 v[104:107], v[212:215], v[36:39], v[104:107]\n" \
"s_waitcnt vmcnt(15) lgkmcnt(3)\n" \
"v_mfma_f32_16x16x32_bf16 v[108:111], v[84:87], v[36:39], v[108:111]\n" \
"buffer_load_dwordx4 v[36:39], v3, s[36:39], s57 offen offset:1024 sc1\n" \
"v_mfma_f32_16x16x32_bf16 v[96:99], a[88:91], v[40:43], v[96:99]\n" \
"v_mfma_f32_16x16x32_bf16 v[100:103], a[216:219], v[40:43], v[100:103]\n" \
"ds_read_b128 v[84:87], v2 offset:1024\n" \
"v_mfma_f32_16x16x32_bf16 v[104:107], v[216:219], v[40:43], v[104:107]\n" \
"s_waitcnt vmcnt(15) lgkmcnt(3)\n" \
"v_mfma_f32_16x16x32_bf16 v[108:111], v[88:91], v[40:43], v[108:111]\n" \
"buffer_load_dwordx4 v[40:43], v3, s[36:39], s57 offen offset:2048 sc1\n" \
"v_mfma_f32_16x16x32_bf16 v[96:99], a[92:95], v[44:47], v[96:99]\n" \
"v_mfma_f32_16x16x32_bf16 v[100:103], a[220:223], v[44:47], v[100:103]\n" \
"ds_read_b128 v[88:91], v2 offset:2048\n" \
"v_mfma_f32_16x16x32_bf16 v[104:107], v[220:223], v[44:47], v[104:107]\n" \
"s_waitcnt vmcnt(15) lgkmcnt(3)\n" \
"v_mfma_f32_16x16x32_bf16 v[108:111], v[92:95], v[44:47], v[108:111]\n" \
"buffer_load_dwordx4 v[44:47], v3, s[36:39], s57 offen offset:3072 sc1\n" \
"v_mfma_f32_16x16x32_bf16 v[96:99], a[96:99], v[48:51], v[96:99]\n" \
"v_mfma_f32_16x16x32_bf16 v[100:103], a[224:227], v[48:51], v[100:103]\n" \
"ds_read_b128 v[92:95], v2 offset:3072\n" \
"v_mfma_f32_16x16x32_bf16 v[104:107], v[224:227], v[48:51], v[104:107]\n" \
"s_waitcnt vmcnt(15) lgkmcnt(3)\n" \
"v_mfma_f32_16x16x32_bf16 v[108:111], v[80:83], v[48:51], v[108:111]\n" \
"s_xor_b32 s57, s56, s88\n" \
"buffer_load_dwordx4 v[48:51], v3, s[36:39], s57 offen offset:0 sc1\n" \
"v_mfma_f32_16x16x32_bf16 v[96:99], a[100:103], v[52:55], v[96:99]\n" \
"s_lshl_b32 s80, s44, 16\n" \
"v_mfma_f32_16x16x32_bf16 v[100:103], a[228:231], v[52:55], v[100:103]\n" \
"ds_read_b128 v[80:83], v2 offset:4096\n" \
"v_mfma_f32_16x16x32_bf16 v[104:107], v[228:231], v[52:55], v[104:107]\n" \
"s_waitcnt vmcnt(15) lgkmcnt(3)\n" \
"s_add_u32 s80, s80, s78\n" \
"v_mfma_f32_16x16x32_bf16 v[108:111], v[84:87], v[52:55], v[108:111]\n" \
"buffer_load_dwordx4 v[52:55], v3, s[36:39], s57 offen offset:1024 sc1\n" \
"v_mfma_f32_16x16x32_bf16 v[96:99], a[104:107], v[56:59], v[96:99]\n" \
"s_lshr_b32 s58, s80, 8\n" \
"v_mfma_f32_16x16x32_bf16 v[100:103], a[232:235], v[56:59], v[100:103]\n" \
"ds_read_b128 v[84:87], v2 offset:5120\n" \
"v_mfma_f32_16x16x32_bf16 v[104:107], v[232:235], v[56:59], v[104:107]\n" \
"s_waitcnt vmcnt(15) lgkmcnt(3)\n" \
"s_and_b32 s58, s58, 0x2000\n" \
"v_mfma_f32_16x16x32_bf16 v[108:111], v[88:91], v[56:59], v[108:111]\n" \
"buffer_load_dwordx4 v[56:59], v3, s[36:39], s57 offen offset:2048 sc1\n" \
"v_mfma_f32_16x16x32_bf16 v[96:99], a[108:111], v[60:63], v[96:99]\n" \
"s_xor_b32 s80, s80, s58\n" \
"v_mfma_f32_16x16x32_bf16 v[100:103], a[236:239], v[60:63], v[100:103]\n" \
"ds_read_b128 v[88:91], v2 offset:6144\n" \
"v_mfma_f32_16x16x32_bf16 v[104:107], v[236:239], v[60:63], v[104:107]\n" \
"s_waitcnt vmcnt(15) lgkmcnt(3)\n" \
"v_mfma_f32_16x16x32_bf16 v[108:111], v[92:95], v[60:63], v[108:111]\n" \
"buffer_load_dwordx4 v[60:63], v3, s[36:39], s57 offen offset:3072 sc1\n" \
"v_mfma_f32_16x16x32_bf16 v[96:99], a[112:115], v[64:67], v[96:99]\n" \
"v_mfma_f32_16x16x32_bf16 v[100:103], a[240:243], v[64:67], v[100:103]\n" \
"ds_read_b128 v[92:95], v2 offset:7168\n" \
"v_mfma_f32_16x16x32_bf16 v[104:107], v[240:243], v[64:67], v[104:107]\n" \
"s_waitcnt vmcnt(14) lgkmcnt(3)\n" \
"v_mfma_f32_16x16x32_bf16 v[108:111], v[80:83], v[64:67], v[108:111]\n" \
"s_xor_b32 s57, s56, s89\n" \
"buffer_load_dwordx4 v[64:67], v3, s[36:39], s57 offen offset:0 sc1\n" \
"v_mfma_f32_16x16x32_bf16 v[96:99], a[116:119], v[68:71], v[96:99]\n" \
"buffer_load_dword v11, v4, s[36:39], 0 offen sc1\n" \
"v_mfma_f32_16x16x32_bf16 v[100:103], a[244:247], v[68:71], v[100:103]\n" \
"ds_read_b128 v[80:83], v2 offset:8192\n" \
"v_mfma_f32_16x16x32_bf16 v[104:107], v[244:247], v[68:71], v[104:107]\n" \
"s_waitcnt vmcnt(15) lgkmcnt(3)\n" \
"v_mfma_f32_16x16x32_bf16 v[108:111], v[84:87], v[68:71], v[108:111]\n" \
"buffer_load_dwordx4 v[68:71], v3, s[36:39], s57 offen offset:1024 sc1\n" \
"v_mfma_f32_16x16x32_bf16 v[96:99], a[120:123], v[72:75], v[96:99]\n" \
"s_add_u32 s55, s55, 1\n" \
"s_mov_b32 s84, 0x4000\n" \
"v_mfma_f32_16x16x32_bf16 v[100:103], a[248:251], v[72:75], v[100:103]\n" \
"ds_read_b128 v[84:87], v2 offset:9216\n" \
"v_mfma_f32_16x16x32_bf16 v[104:107], v[248:251], v[72:75], v[104:107]\n" \
"s_waitcnt vmcnt(15) lgkmcnt(3)\n" \
"v_mfma_f32_16x16x32_bf16 v[108:111], v[88:91], v[72:75], v[108:111]\n" \
"buffer_load_dwordx4 v[72:75], v3, s[36:39], s57 offen offset:2048 sc1\n" \
"v_mfma_f32_16x16x32_bf16 v[96:99], a[124:127], v[76:79], v[96:99]\n" \
"s_mov_b32 s76, s80\n" \
"v_mfma_f32_16x16x32_bf16 v[100:103], a[252:255], v[76:79], v[100:103]\n" \
"ds_read_b128 v[88:91], v2 offset:10240\n" \
"s_mov_b32 s44, s82\n" \
"v_mfma_f32_16x16x32_bf16 v[104:107], v[252:255], v[76:79], v[104:107]\n" \
"s_waitcnt vmcnt(15) lgkmcnt(3)\n" \
"ds_write_b128 v7, v[96:99] offset:0\n" \
"v_xor_b32_e64 v7, 64, v7\n" \
"v_mfma_f32_16x16x32_bf16 v[108:111], v[92:95], v[76:79], v[108:111]\n" \
"buffer_load_dwordx4 v[76:79], v3, s[36:39], s57 offen offset:3072 sc1\n" \
"ds_write_b128 v7, v[100:103] offset:1024\n" \
"v_xor_b32 v7, 192, v7\n" \
"s_cmp_eq_u32 s81, 0\n" \
"s_cbranch_scc1 .Lswo_loop_%=\n" \
"s_nop 7\n" \
"s_nop 7\n" \
"s_waitcnt vmcnt(0) lgkmcnt(0)\n" \
"ds_write_b128 v7, v[104:107] offset:2048\n" \
"v_xor_b32 v7, 64, v7\n" \
"ds_write_b128 v7, v[108:111] offset:3072\n" \
"v_xor_b32 v7, 64, v7\n" \
"s_waitcnt lgkmcnt(0)\n" \
"s_barrier\n" \
"ds_read_b128 v[112:115], v8 offset:0\n" \
"ds_read_b128 v[116:119], v8 offset:4096\n" \
"ds_read_b128 v[120:123], v8 offset:8192\n" \
"ds_read_b128 v[124:127], v8 offset:12288\n" \
"s_waitcnt lgkmcnt(2)\n" \
"v_add_f32 v112, v112, v116\n" \
"v_add_f32 v113, v113, v117\n" \
"v_add_f32 v114, v114, v118\n" \
"v_add_f32 v115, v115, v119\n" \
"s_waitcnt lgkmcnt(0)\n" \
"v_add_f32 v120, v120, v124\n" \
"v_add_f32 v121, v121, v125\n" \
"v_add_f32 v122, v122, v126\n" \
"v_add_f32 v123, v123, v127\n" \
"v_add_f32 v112, v112, v120\n" \
"v_add_f32 v113, v113, v121\n" \
"v_add_f32 v114, v114, v122\n" \
"v_add_f32 v115, v115, v123\n" \
"v_xor_b32 v8, s84, v8\n" \
"v_cvt_pk_bf16_f32 v0, v112, v113\n" \
"v_cvt_pk_bf16_f32 v1, v114, v115\n" \
"v_xor_b32 v7, 0x4000, v7\n" \
"s_cmp_eq_u32 s98, 0\n" \
"s_cbranch_scc0 .Lswo_f_%=\n" \
"v_lshlrev_b32 v112, 16, v0\n" \
"v_and_b32 v113, 0xffff0000, v0\n" \
"v_lshlrev_b32 v114, 16, v1\n" \
"v_and_b32 v115, 0xffff0000, v1\n" \
"v_lshlrev_b32 v116, 16, v14\n" \
"v_and_b32 v117, 0xffff0000, v14\n" \
"v_lshlrev_b32 v118, 16, v15\n" \
"v_and_b32 v119, 0xffff0000, v15\n" \
"v_lshlrev_b32 v120, 16, v12\n" \
"v_and_b32 v121, 0xffff0000, v12\n" \
"v_lshlrev_b32 v122, 16, v13\n" \
"v_and_b32 v123, 0xffff0000, v13\n" \
"v_fmac_f32 v120, v112, v116\n" \
"v_fmac_f32 v121, v113, v117\n" \
"v_fmac_f32 v122, v114, v118\n" \
"v_fmac_f32 v123, v115, v119\n" \
"v_cvt_pk_bf16_f32 v12, v120, v121\n" \
"v_cvt_pk_bf16_f32 v13, v122, v123\n" \
"buffer_store_dwordx2 v[12:13], v5, s[68:71], s77 offen\n" \
"s_branch .Lswo_g_%=\n" \
".Lswo_f_%=:\n" \
"buffer_store_dwordx2 v[0:1], v6, s[36:39], s76 offen sc1\n" \
"s_cmp_lg_u32 s55, s95\n" \
"s_cbranch_scc1 .Lswo_g_%=\n" \
"s_waitcnt vmcnt(0) lgkmcnt(0)\n" \
"v_mov_b32 v9, 1\n" \
"s_mov_b64 exec, 1\n" \
"buffer_atomic_add v9, off, s[36:39], s49 offset:0\n" \
"s_mov_b64 exec, -1\n" \
"s_mov_b32 s79, 0\n" \
"s_mov_b32 s98, 0\n" \
"s_branch .Lswo_o_%=\n" \
".Lswo_ok_%=:\n" \
".Lswo_g_%=:\n" \
"s_cmp_lt_u32 s55, s46\n" \
"s_cbranch_scc1 .Lswo_prime_%=\n" \
"s_branch .Lswo_end_%=\n" \
".Lswo_w_%=:\n" \
"s_nop 7\n" \
"s_nop 7\n" \
"s_waitcnt vmcnt(0) lgkmcnt(0)\n" \
"s_mov_b32 s61, 0\n" \
".Lswo_wa_%=:\n" \
"s_cmp_lg_u32 s60, 0\n" \
"s_cbranch_scc1 .Lswo_wk_%=\n" \
"buffer_load_dword v11, v4, s[36:39], 0 offen sc1\n" \
"s_waitcnt vmcnt(0)\n" \
"v_cmp_gt_u32_e64 s[62:63], s59, v11\n" \
"s_nop 4\n" \
"s_cmp_eq_u64 s[62:63], 0\n" \
"s_cbranch_scc1 .Lswo_wk_%=\n" \
"s_sleep 1\n" \
"s_add_u32 s61, s61, 1\n" \
"s_cmp_lt_u32 s61, 65536\n" \
"s_cbranch_scc1 .Lswo_wa_%=\n" \
"s_mov_b32 s60, 1\n" \
"v_mov_b32 v11, 256\n" \
"v_mov_b32 v9, 1\n" \
"buffer_store_dword v9, v11, s[36:39], 0 offen sc1\n" \
"s_waitcnt vmcnt(0) lgkmcnt(0)\n" \
"s_branch .Lswo_wk_%=\n" \
".Lswo_o_%=:\n" \
"s_nop 7\n" \
"s_nop 7\n" \
"s_waitcnt vmcnt(0) lgkmcnt(0)\n" \
"s_mov_b32 s61, 0\n" \
".Lswo_oa_%=:\n" \
"s_cmp_lg_u32 s60, 0\n" \
"s_cbranch_scc1 .Lswo_ok_%=\n" \
"buffer_load_dword v9, off, s[36:39], s49 offset:64 sc1\n" \
"s_sub_u32 s54, 10, s47\n" \
"s_waitcnt vmcnt(0)\n" \
"v_cmp_gt_u32 vcc, s54, v9\n" \
"s_nop 4\n" \
"s_cbranch_vccz .Lswo_ok_%=\n" \
"s_sleep 1\n" \
"s_add_u32 s61, s61, 1\n" \
"s_cmp_lt_u32 s61, 65536\n" \
"s_cbranch_scc1 .Lswo_oa_%=\n" \
"s_mov_b32 s60, 1\n" \
"v_mov_b32 v11, 256\n" \
"v_mov_b32 v9, 1\n" \
"buffer_store_dword v9, v11, s[36:39], 0 offen sc1\n" \
"s_waitcnt vmcnt(0) lgkmcnt(0)\n" \
"s_branch .Lswo_ok_%=\n" \
".Lswo_end_%=:\n" \
"s_endpgm\n"
#define ASM_S64_G2 \
"s_nop 7\n" \
"s_nop 7\n" \
"s_lshl_b32 s52, s48, 15\n" \
"s_lshl_b32 s53, s48, 13\n" \
"s_add_u32 s53, s53, 98304\n" \
"s_sub_u32 s54, s53, s52\n" \
"v_add_u32 v9, s54, v1\n" \
"s_add_u32 m0, s52, 0\n" \
"s_add_u32 s54, s97, 0\n" \
"s_lshr_b32 s55, s54, 8\n" \
"s_and_b32 s55, s55, 0x2000\n" \
"s_xor_b32 s54, s54, s55\n" \
"buffer_load_dwordx4 v0, s[40:43], s54 offen sc1 lds\n" \
"s_add_u32 m0, s52, 1024\n" \
"s_add_u32 s54, s97, 32768\n" \
"s_lshr_b32 s55, s54, 8\n" \
"s_and_b32 s55, s55, 0x2000\n" \
"s_xor_b32 s54, s54, s55\n" \
"buffer_load_dwordx4 v0, s[40:43], s54 offen sc1 lds\n" \
"s_add_u32 m0, s52, 2048\n" \
"s_add_u32 s54, s97, 65536\n" \
"s_lshr_b32 s55, s54, 8\n" \
"s_and_b32 s55, s55, 0x2000\n" \
"s_xor_b32 s54, s54, s55\n" \
"buffer_load_dwordx4 v0, s[40:43], s54 offen sc1 lds\n" \
"s_add_u32 m0, s52, 3072\n" \
"s_add_u32 s54, s97, 98304\n" \
"s_lshr_b32 s55, s54, 8\n" \
"s_and_b32 s55, s55, 0x2000\n" \
"s_xor_b32 s54, s54, s55\n" \
"buffer_load_dwordx4 v0, s[40:43], s54 offen sc1 lds\n" \
"s_mov_b32 s54, s99\n" \
".Lsg2_z0_%=:\n" \
"s_sleep 1\n" \
"s_sub_u32 s54, s54, 1\n" \
"s_cbranch_scc0 .Lsg2_z0_%=\n" \
"s_add_u32 m0, s52, 4096\n" \
"s_add_u32 s54, s97, 131072\n" \
"s_lshr_b32 s55, s54, 8\n" \
"s_and_b32 s55, s55, 0x2000\n" \
"s_xor_b32 s54, s54, s55\n" \
"buffer_load_dwordx4 v0, s[40:43], s54 offen sc1 lds\n" \
"s_add_u32 m0, s52, 5120\n" \
"s_add_u32 s54, s97, 163840\n" \
"s_lshr_b32 s55, s54, 8\n" \
"s_and_b32 s55, s55, 0x2000\n" \
"s_xor_b32 s54, s54, s55\n" \
"buffer_load_dwordx4 v0, s[40:43], s54 offen sc1 lds\n" \
"s_add_u32 m0, s52, 6144\n" \
"s_add_u32 s54, s97, 196608\n" \
"s_lshr_b32 s55, s54, 8\n" \
"s_and_b32 s55, s55, 0x2000\n" \
"s_xor_b32 s54, s54, s55\n" \
"buffer_load_dwordx4 v0, s[40:43], s54 offen sc1 lds\n" \
"s_add_u32 m0, s52, 7168\n" \
"s_add_u32 s54, s97, 229376\n" \
"s_lshr_b32 s55, s54, 8\n" \
"s_and_b32 s55, s55, 0x2000\n" \
"s_xor_b32 s54, s54, s55\n" \
"buffer_load_dwordx4 v0, s[40:43], s54 offen sc1 lds\n" \
"s_mov_b32 s54, s99\n" \
".Lsg2_z1_%=:\n" \
"s_sleep 1\n" \
"s_sub_u32 s54, s54, 1\n" \
"s_cbranch_scc0 .Lsg2_z1_%=\n" \
"s_add_u32 m0, s52, 8192\n" \
"s_add_u32 s54, s97, 262144\n" \
"s_lshr_b32 s55, s54, 8\n" \
"s_and_b32 s55, s55, 0x2000\n" \
"s_xor_b32 s54, s54, s55\n" \
"buffer_load_dwordx4 v0, s[40:43], s54 offen sc1 lds\n" \
"s_add_u32 m0, s52, 9216\n" \
"s_add_u32 s54, s97, 294912\n" \
"s_lshr_b32 s55, s54, 8\n" \
"s_and_b32 s55, s55, 0x2000\n" \
"s_xor_b32 s54, s54, s55\n" \
"buffer_load_dwordx4 v0, s[40:43], s54 offen sc1 lds\n" \
"s_add_u32 m0, s52, 10240\n" \
"s_add_u32 s54, s97, 327680\n" \
"s_lshr_b32 s55, s54, 8\n" \
"s_and_b32 s55, s55, 0x2000\n" \
"s_xor_b32 s54, s54, s55\n" \
"buffer_load_dwordx4 v0, s[40:43], s54 offen sc1 lds\n" \
"s_add_u32 m0, s52, 11264\n" \
"s_add_u32 s54, s97, 360448\n" \
"s_lshr_b32 s55, s54, 8\n" \
"s_and_b32 s55, s55, 0x2000\n" \
"s_xor_b32 s54, s54, s55\n" \
"buffer_load_dwordx4 v0, s[40:43], s54 offen sc1 lds\n" \
"s_mov_b32 s54, s99\n" \
".Lsg2_z2_%=:\n" \
"s_sleep 1\n" \
"s_sub_u32 s54, s54, 1\n" \
"s_cbranch_scc0 .Lsg2_z2_%=\n" \
"s_add_u32 m0, s52, 12288\n" \
"s_add_u32 s54, s97, 393216\n" \
"s_lshr_b32 s55, s54, 8\n" \
"s_and_b32 s55, s55, 0x2000\n" \
"s_xor_b32 s54, s54, s55\n" \
"buffer_load_dwordx4 v0, s[40:43], s54 offen sc1 lds\n" \
"s_add_u32 m0, s52, 13312\n" \
"s_add_u32 s54, s97, 425984\n" \
"s_lshr_b32 s55, s54, 8\n" \
"s_and_b32 s55, s55, 0x2000\n" \
"s_xor_b32 s54, s54, s55\n" \
"buffer_load_dwordx4 v0, s[40:43], s54 offen sc1 lds\n" \
"s_add_u32 m0, s52, 14336\n" \
"s_add_u32 s54, s97, 458752\n" \
"s_lshr_b32 s55, s54, 8\n" \
"s_and_b32 s55, s55, 0x2000\n" \
"s_xor_b32 s54, s54, s55\n" \
"buffer_load_dwordx4 v0, s[40:43], s54 offen sc1 lds\n" \
"s_add_u32 m0, s52, 15360\n" \
"s_add_u32 s54, s97, 491520\n" \
"s_lshr_b32 s55, s54, 8\n" \
"s_and_b32 s55, s55, 0x2000\n" \
"s_xor_b32 s54, s54, s55\n" \
"buffer_load_dwordx4 v0, s[40:43], s54 offen sc1 lds\n" \
"s_mov_b32 s54, s99\n" \
".Lsg2_z3_%=:\n" \
"s_sleep 1\n" \
"s_sub_u32 s54, s54, 1\n" \
"s_cbranch_scc0 .Lsg2_z3_%=\n" \
"s_add_u32 m0, s52, 16384\n" \
"s_add_u32 s54, s97, 524288\n" \
"s_lshr_b32 s55, s54, 8\n" \
"s_and_b32 s55, s55, 0x2000\n" \
"s_xor_b32 s54, s54, s55\n" \
"buffer_load_dwordx4 v0, s[40:43], s54 offen sc1 lds\n" \
"s_add_u32 m0, s52, 17408\n" \
"s_add_u32 s54, s97, 557056\n" \
"s_lshr_b32 s55, s54, 8\n" \
"s_and_b32 s55, s55, 0x2000\n" \
"s_xor_b32 s54, s54, s55\n" \
"buffer_load_dwordx4 v0, s[40:43], s54 offen sc1 lds\n" \
"s_add_u32 m0, s52, 18432\n" \
"s_add_u32 s54, s97, 589824\n" \
"s_lshr_b32 s55, s54, 8\n" \
"s_and_b32 s55, s55, 0x2000\n" \
"s_xor_b32 s54, s54, s55\n" \
"buffer_load_dwordx4 v0, s[40:43], s54 offen sc1 lds\n" \
"s_add_u32 m0, s52, 19456\n" \
"s_add_u32 s54, s97, 622592\n" \
"s_lshr_b32 s55, s54, 8\n" \
"s_and_b32 s55, s55, 0x2000\n" \
"s_xor_b32 s54, s54, s55\n" \
"buffer_load_dwordx4 v0, s[40:43], s54 offen sc1 lds\n" \
"s_mov_b32 s54, s99\n" \
".Lsg2_z4_%=:\n" \
"s_sleep 1\n" \
"s_sub_u32 s54, s54, 1\n" \
"s_cbranch_scc0 .Lsg2_z4_%=\n" \
"s_add_u32 m0, s52, 20480\n" \
"s_add_u32 s54, s97, 655360\n" \
"s_lshr_b32 s55, s54, 8\n" \
"s_and_b32 s55, s55, 0x2000\n" \
"s_xor_b32 s54, s54, s55\n" \
"buffer_load_dwordx4 v0, s[40:43], s54 offen sc1 lds\n" \
"s_add_u32 m0, s52, 21504\n" \
"s_add_u32 s54, s97, 688128\n" \
"s_lshr_b32 s55, s54, 8\n" \
"s_and_b32 s55, s55, 0x2000\n" \
"s_xor_b32 s54, s54, s55\n" \
"buffer_load_dwordx4 v0, s[40:43], s54 offen sc1 lds\n" \
"s_add_u32 m0, s52, 22528\n" \
"s_add_u32 s54, s97, 720896\n" \
"s_lshr_b32 s55, s54, 8\n" \
"s_and_b32 s55, s55, 0x2000\n" \
"s_xor_b32 s54, s54, s55\n" \
"buffer_load_dwordx4 v0, s[40:43], s54 offen sc1 lds\n" \
"s_add_u32 m0, s52, 23552\n" \
"s_add_u32 s54, s97, 753664\n" \
"s_lshr_b32 s55, s54, 8\n" \
"s_and_b32 s55, s55, 0x2000\n" \
"s_xor_b32 s54, s54, s55\n" \
"buffer_load_dwordx4 v0, s[40:43], s54 offen sc1 lds\n" \
"s_mov_b32 s54, s99\n" \
".Lsg2_z5_%=:\n" \
"s_sleep 1\n" \
"s_sub_u32 s54, s54, 1\n" \
"s_cbranch_scc0 .Lsg2_z5_%=\n" \
"s_add_u32 m0, s52, 24576\n" \
"s_add_u32 s54, s97, 786432\n" \
"s_lshr_b32 s55, s54, 8\n" \
"s_and_b32 s55, s55, 0x2000\n" \
"s_xor_b32 s54, s54, s55\n" \
"buffer_load_dwordx4 v0, s[40:43], s54 offen sc1 lds\n" \
"s_add_u32 m0, s52, 25600\n" \
"s_add_u32 s54, s97, 819200\n" \
"s_lshr_b32 s55, s54, 8\n" \
"s_and_b32 s55, s55, 0x2000\n" \
"s_xor_b32 s54, s54, s55\n" \
"buffer_load_dwordx4 v0, s[40:43], s54 offen sc1 lds\n" \
"s_add_u32 m0, s52, 26624\n" \
"s_add_u32 s54, s97, 851968\n" \
"s_lshr_b32 s55, s54, 8\n" \
"s_and_b32 s55, s55, 0x2000\n" \
"s_xor_b32 s54, s54, s55\n" \
"buffer_load_dwordx4 v0, s[40:43], s54 offen sc1 lds\n" \
"s_add_u32 m0, s52, 27648\n" \
"s_add_u32 s54, s97, 884736\n" \
"s_lshr_b32 s55, s54, 8\n" \
"s_and_b32 s55, s55, 0x2000\n" \
"s_xor_b32 s54, s54, s55\n" \
"buffer_load_dwordx4 v0, s[40:43], s54 offen sc1 lds\n" \
"s_mov_b32 s54, s99\n" \
".Lsg2_z6_%=:\n" \
"s_sleep 1\n" \
"s_sub_u32 s54, s54, 1\n" \
"s_cbranch_scc0 .Lsg2_z6_%=\n" \
"s_add_u32 m0, s52, 28672\n" \
"s_add_u32 s54, s97, 917504\n" \
"s_lshr_b32 s55, s54, 8\n" \
"s_and_b32 s55, s55, 0x2000\n" \
"s_xor_b32 s54, s54, s55\n" \
"buffer_load_dwordx4 v0, s[40:43], s54 offen sc1 lds\n" \
"s_add_u32 m0, s52, 29696\n" \
"s_add_u32 s54, s97, 950272\n" \
"s_lshr_b32 s55, s54, 8\n" \
"s_and_b32 s55, s55, 0x2000\n" \
"s_xor_b32 s54, s54, s55\n" \
"buffer_load_dwordx4 v0, s[40:43], s54 offen sc1 lds\n" \
"s_add_u32 m0, s52, 30720\n" \
"s_add_u32 s54, s97, 983040\n" \
"s_lshr_b32 s55, s54, 8\n" \
"s_and_b32 s55, s55, 0x2000\n" \
"s_xor_b32 s54, s54, s55\n" \
"buffer_load_dwordx4 v0, s[40:43], s54 offen sc1 lds\n" \
"s_add_u32 m0, s52, 31744\n" \
"s_add_u32 s54, s97, 1015808\n" \
"s_lshr_b32 s55, s54, 8\n" \
"s_and_b32 s55, s55, 0x2000\n" \
"s_xor_b32 s54, s54, s55\n" \
"buffer_load_dwordx4 v0, s[40:43], s54 offen sc1 lds\n" \
"s_mov_b32 s54, s99\n" \
".Lsg2_z7_%=:\n" \
"s_sleep 1\n" \
"s_sub_u32 s54, s54, 1\n" \
"s_cbranch_scc0 .Lsg2_z7_%=\n" \
"s_add_u32 m0, s53, 32768\n" \
"s_add_u32 s54, s97, 1048576\n" \
"s_lshr_b32 s55, s54, 8\n" \
"s_and_b32 s55, s55, 0x2000\n" \
"s_xor_b32 s54, s54, s55\n" \
"buffer_load_dwordx4 v0, s[40:43], s54 offen sc1 lds\n" \
"s_add_u32 m0, s53, 33792\n" \
"s_add_u32 s54, s97, 1081344\n" \
"s_lshr_b32 s55, s54, 8\n" \
"s_and_b32 s55, s55, 0x2000\n" \
"s_xor_b32 s54, s54, s55\n" \
"buffer_load_dwordx4 v0, s[40:43], s54 offen sc1 lds\n" \
"s_add_u32 m0, s53, 34816\n" \
"s_add_u32 s54, s97, 1114112\n" \
"s_lshr_b32 s55, s54, 8\n" \
"s_and_b32 s55, s55, 0x2000\n" \
"s_xor_b32 s54, s54, s55\n" \
"buffer_load_dwordx4 v0, s[40:43], s54 offen sc1 lds\n" \
"s_add_u32 m0, s53, 35840\n" \
"s_add_u32 s54, s97, 1146880\n" \
"s_lshr_b32 s55, s54, 8\n" \
"s_and_b32 s55, s55, 0x2000\n" \
"s_xor_b32 s54, s54, s55\n" \
"buffer_load_dwordx4 v0, s[40:43], s54 offen sc1 lds\n" \
"s_mov_b32 s54, s99\n" \
".Lsg2_z8_%=:\n" \
"s_sleep 1\n" \
"s_sub_u32 s54, s54, 1\n" \
"s_cbranch_scc0 .Lsg2_z8_%=\n" \
"s_add_u32 m0, s53, 36864\n" \
"s_add_u32 s54, s97, 1179648\n" \
"s_lshr_b32 s55, s54, 8\n" \
"s_and_b32 s55, s55, 0x2000\n" \
"s_xor_b32 s54, s54, s55\n" \
"buffer_load_dwordx4 v0, s[40:43], s54 offen sc1 lds\n" \
"s_add_u32 m0, s53, 37888\n" \
"s_add_u32 s54, s97, 1212416\n" \
"s_lshr_b32 s55, s54, 8\n" \
"s_and_b32 s55, s55, 0x2000\n" \
"s_xor_b32 s54, s54, s55\n" \
"buffer_load_dwordx4 v0, s[40:43], s54 offen sc1 lds\n" \
"s_add_u32 m0, s53, 38912\n" \
"s_add_u32 s54, s97, 1245184\n" \
"s_lshr_b32 s55, s54, 8\n" \
"s_and_b32 s55, s55, 0x2000\n" \
"s_xor_b32 s54, s54, s55\n" \
"buffer_load_dwordx4 v0, s[40:43], s54 offen sc1 lds\n" \
"s_add_u32 m0, s53, 39936\n" \
"s_add_u32 s54, s97, 1277952\n" \
"s_lshr_b32 s55, s54, 8\n" \
"s_and_b32 s55, s55, 0x2000\n" \
"s_xor_b32 s54, s54, s55\n" \
"buffer_load_dwordx4 v0, s[40:43], s54 offen sc1 lds\n" \
"s_mov_b32 s54, s99\n" \
".Lsg2_z9_%=:\n" \
"s_sleep 1\n" \
"s_sub_u32 s54, s54, 1\n" \
"s_cbranch_scc0 .Lsg2_z9_%=\n" \
"s_waitcnt vmcnt(36)\n" \
"ds_read_b64_tr_b16 a[0:1], v1 offset:0\n" \
"ds_read_b64_tr_b16 a[2:3], v1 offset:512\n" \
"ds_read_b64_tr_b16 a[128:129], v1 offset:32\n" \
"ds_read_b64_tr_b16 a[130:131], v1 offset:544\n" \
"ds_read_b64_tr_b16 v[128:129], v1 offset:64\n" \
"ds_read_b64_tr_b16 v[130:131], v1 offset:576\n" \
"ds_read_b64_tr_b16 v[16:17], v1 offset:96\n" \
"ds_read_b64_tr_b16 v[18:19], v1 offset:608\n" \
"s_waitcnt lgkmcnt(0)\n" \
"s_add_u32 m0, s52, 0\n" \
"s_add_u32 s54, s97, 1310720\n" \
"s_lshr_b32 s55, s54, 8\n" \
"s_and_b32 s55, s55, 0x2000\n" \
"s_xor_b32 s54, s54, s55\n" \
"buffer_load_dwordx4 v0, s[40:43], s54 offen sc1 lds\n" \
"s_add_u32 m0, s52, 1024\n" \
"s_add_u32 s54, s97, 1343488\n" \
"s_lshr_b32 s55, s54, 8\n" \
"s_and_b32 s55, s55, 0x2000\n" \
"s_xor_b32 s54, s54, s55\n" \
"buffer_load_dwordx4 v0, s[40:43], s54 offen sc1 lds\n" \
"s_add_u32 m0, s52, 2048\n" \
"s_add_u32 s54, s97, 1376256\n" \
"s_lshr_b32 s55, s54, 8\n" \
"s_and_b32 s55, s55, 0x2000\n" \
"s_xor_b32 s54, s54, s55\n" \
"buffer_load_dwordx4 v0, s[40:43], s54 offen sc1 lds\n" \
"s_add_u32 m0, s52, 3072\n" \
"s_add_u32 s54, s97, 1409024\n" \
"s_lshr_b32 s55, s54, 8\n" \
"s_and_b32 s55, s55, 0x2000\n" \
"s_xor_b32 s54, s54, s55\n" \
"buffer_load_dwordx4 v0, s[40:43], s54 offen sc1 lds\n" \
"s_mov_b32 s54, s99\n" \
".Lsg2_z10_%=:\n" \
"s_sleep 1\n" \
"s_sub_u32 s54, s54, 1\n" \
"s_cbranch_scc0 .Lsg2_z10_%=\n" \
"s_waitcnt vmcnt(36)\n" \
"ds_read_b64_tr_b16 a[4:5], v1 offset:4096\n" \
"ds_read_b64_tr_b16 a[6:7], v1 offset:4608\n" \
"ds_read_b64_tr_b16 a[132:133], v1 offset:4128\n" \
"ds_read_b64_tr_b16 a[134:135], v1 offset:4640\n" \
"ds_read_b64_tr_b16 v[132:133], v1 offset:4160\n" \
"ds_read_b64_tr_b16 v[134:135], v1 offset:4672\n" \
"ds_read_b64_tr_b16 v[20:21], v1 offset:4192\n" \
"ds_read_b64_tr_b16 v[22:23], v1 offset:4704\n" \
"s_waitcnt lgkmcnt(0)\n" \
"s_add_u32 m0, s52, 4096\n" \
"s_add_u32 s54, s97, 1441792\n" \
"s_lshr_b32 s55, s54, 8\n" \
"s_and_b32 s55, s55, 0x2000\n" \
"s_xor_b32 s54, s54, s55\n" \
"buffer_load_dwordx4 v0, s[40:43], s54 offen sc1 lds\n" \
"s_add_u32 m0, s52, 5120\n" \
"s_add_u32 s54, s97, 1474560\n" \
"s_lshr_b32 s55, s54, 8\n" \
"s_and_b32 s55, s55, 0x2000\n" \
"s_xor_b32 s54, s54, s55\n" \
"buffer_load_dwordx4 v0, s[40:43], s54 offen sc1 lds\n" \
"s_add_u32 m0, s52, 6144\n" \
"s_add_u32 s54, s97, 1507328\n" \
"s_lshr_b32 s55, s54, 8\n" \
"s_and_b32 s55, s55, 0x2000\n" \
"s_xor_b32 s54, s54, s55\n" \
"buffer_load_dwordx4 v0, s[40:43], s54 offen sc1 lds\n" \
"s_add_u32 m0, s52, 7168\n" \
"s_add_u32 s54, s97, 1540096\n" \
"s_lshr_b32 s55, s54, 8\n" \
"s_and_b32 s55, s55, 0x2000\n" \
"s_xor_b32 s54, s54, s55\n" \
"buffer_load_dwordx4 v0, s[40:43], s54 offen sc1 lds\n" \
"s_mov_b32 s54, s99\n" \
".Lsg2_z11_%=:\n" \
"s_sleep 1\n" \
"s_sub_u32 s54, s54, 1\n" \
"s_cbranch_scc0 .Lsg2_z11_%=\n" \
"s_waitcnt vmcnt(36)\n" \
"ds_read_b64_tr_b16 a[8:9], v1 offset:8192\n" \
"ds_read_b64_tr_b16 a[10:11], v1 offset:8704\n" \
"ds_read_b64_tr_b16 a[136:137], v1 offset:8224\n" \
"ds_read_b64_tr_b16 a[138:139], v1 offset:8736\n" \
"ds_read_b64_tr_b16 v[136:137], v1 offset:8256\n" \
"ds_read_b64_tr_b16 v[138:139], v1 offset:8768\n" \
"ds_read_b64_tr_b16 v[24:25], v1 offset:8288\n" \
"ds_read_b64_tr_b16 v[26:27], v1 offset:8800\n" \
"s_waitcnt lgkmcnt(0)\n" \
"s_add_u32 m0, s52, 8192\n" \
"s_add_u32 s54, s97, 1572864\n" \
"s_lshr_b32 s55, s54, 8\n" \
"s_and_b32 s55, s55, 0x2000\n" \
"s_xor_b32 s54, s54, s55\n" \
"buffer_load_dwordx4 v0, s[40:43], s54 offen sc1 lds\n" \
"s_add_u32 m0, s52, 9216\n" \
"s_add_u32 s54, s97, 1605632\n" \
"s_lshr_b32 s55, s54, 8\n" \
"s_and_b32 s55, s55, 0x2000\n" \
"s_xor_b32 s54, s54, s55\n" \
"buffer_load_dwordx4 v0, s[40:43], s54 offen sc1 lds\n" \
"s_add_u32 m0, s52, 10240\n" \
"s_add_u32 s54, s97, 1638400\n" \
"s_lshr_b32 s55, s54, 8\n" \
"s_and_b32 s55, s55, 0x2000\n" \
"s_xor_b32 s54, s54, s55\n" \
"buffer_load_dwordx4 v0, s[40:43], s54 offen sc1 lds\n" \
"s_add_u32 m0, s52, 11264\n" \
"s_add_u32 s54, s97, 1671168\n" \
"s_lshr_b32 s55, s54, 8\n" \
"s_and_b32 s55, s55, 0x2000\n" \
"s_xor_b32 s54, s54, s55\n" \
"buffer_load_dwordx4 v0, s[40:43], s54 offen sc1 lds\n" \
"s_mov_b32 s54, s99\n" \
".Lsg2_z12_%=:\n" \
"s_sleep 1\n" \
"s_sub_u32 s54, s54, 1\n" \
"s_cbranch_scc0 .Lsg2_z12_%=\n" \
"s_waitcnt vmcnt(36)\n" \
"ds_read_b64_tr_b16 a[12:13], v1 offset:12288\n" \
"ds_read_b64_tr_b16 a[14:15], v1 offset:12800\n" \
"ds_read_b64_tr_b16 a[140:141], v1 offset:12320\n" \
"ds_read_b64_tr_b16 a[142:143], v1 offset:12832\n" \
"ds_read_b64_tr_b16 v[140:141], v1 offset:12352\n" \
"ds_read_b64_tr_b16 v[142:143], v1 offset:12864\n" \
"ds_read_b64_tr_b16 v[28:29], v1 offset:12384\n" \
"ds_read_b64_tr_b16 v[30:31], v1 offset:12896\n" \
"s_waitcnt lgkmcnt(0)\n" \
"s_add_u32 m0, s52, 12288\n" \
"s_add_u32 s54, s97, 1703936\n" \
"s_lshr_b32 s55, s54, 8\n" \
"s_and_b32 s55, s55, 0x2000\n" \
"s_xor_b32 s54, s54, s55\n" \
"buffer_load_dwordx4 v0, s[40:43], s54 offen sc1 lds\n" \
"s_add_u32 m0, s52, 13312\n" \
"s_add_u32 s54, s97, 1736704\n" \
"s_lshr_b32 s55, s54, 8\n" \
"s_and_b32 s55, s55, 0x2000\n" \
"s_xor_b32 s54, s54, s55\n" \
"buffer_load_dwordx4 v0, s[40:43], s54 offen sc1 lds\n" \
"s_add_u32 m0, s52, 14336\n" \
"s_add_u32 s54, s97, 1769472\n" \
"s_lshr_b32 s55, s54, 8\n" \
"s_and_b32 s55, s55, 0x2000\n" \
"s_xor_b32 s54, s54, s55\n" \
"buffer_load_dwordx4 v0, s[40:43], s54 offen sc1 lds\n" \
"s_add_u32 m0, s52, 15360\n" \
"s_add_u32 s54, s97, 1802240\n" \
"s_lshr_b32 s55, s54, 8\n" \
"s_and_b32 s55, s55, 0x2000\n" \
"s_xor_b32 s54, s54, s55\n" \
"buffer_load_dwordx4 v0, s[40:43], s54 offen sc1 lds\n" \
"s_mov_b32 s54, s99\n" \
".Lsg2_z13_%=:\n" \
"s_sleep 1\n" \
"s_sub_u32 s54, s54, 1\n" \
"s_cbranch_scc0 .Lsg2_z13_%=\n" \
"s_waitcnt vmcnt(36)\n" \
"ds_read_b64_tr_b16 a[16:17], v1 offset:16384\n" \
"ds_read_b64_tr_b16 a[18:19], v1 offset:16896\n" \
"ds_read_b64_tr_b16 a[144:145], v1 offset:16416\n" \
"ds_read_b64_tr_b16 a[146:147], v1 offset:16928\n" \
"ds_read_b64_tr_b16 v[144:145], v1 offset:16448\n" \
"ds_read_b64_tr_b16 v[146:147], v1 offset:16960\n" \
"ds_read_b64_tr_b16 v[32:33], v1 offset:16480\n" \
"ds_read_b64_tr_b16 v[34:35], v1 offset:16992\n" \
"s_waitcnt lgkmcnt(0)\n" \
"s_add_u32 m0, s52, 16384\n" \
"s_add_u32 s54, s97, 1835008\n" \
"s_lshr_b32 s55, s54, 8\n" \
"s_and_b32 s55, s55, 0x2000\n" \
"s_xor_b32 s54, s54, s55\n" \
"buffer_load_dwordx4 v0, s[40:43], s54 offen sc1 lds\n" \
"s_add_u32 m0, s52, 17408\n" \
"s_add_u32 s54, s97, 1867776\n" \
"s_lshr_b32 s55, s54, 8\n" \
"s_and_b32 s55, s55, 0x2000\n" \
"s_xor_b32 s54, s54, s55\n" \
"buffer_load_dwordx4 v0, s[40:43], s54 offen sc1 lds\n" \
"s_add_u32 m0, s52, 18432\n" \
"s_add_u32 s54, s97, 1900544\n" \
"s_lshr_b32 s55, s54, 8\n" \
"s_and_b32 s55, s55, 0x2000\n" \
"s_xor_b32 s54, s54, s55\n" \
"buffer_load_dwordx4 v0, s[40:43], s54 offen sc1 lds\n" \
"s_add_u32 m0, s52, 19456\n" \
"s_add_u32 s54, s97, 1933312\n" \
"s_lshr_b32 s55, s54, 8\n" \
"s_and_b32 s55, s55, 0x2000\n" \
"s_xor_b32 s54, s54, s55\n" \
"buffer_load_dwordx4 v0, s[40:43], s54 offen sc1 lds\n" \
"s_mov_b32 s54, s99\n" \
".Lsg2_z14_%=:\n" \
"s_sleep 1\n" \
"s_sub_u32 s54, s54, 1\n" \
"s_cbranch_scc0 .Lsg2_z14_%=\n" \
"s_waitcnt vmcnt(36)\n" \
"ds_read_b64_tr_b16 a[20:21], v1 offset:20480\n" \
"ds_read_b64_tr_b16 a[22:23], v1 offset:20992\n" \
"ds_read_b64_tr_b16 a[148:149], v1 offset:20512\n" \
"ds_read_b64_tr_b16 a[150:151], v1 offset:21024\n" \
"ds_read_b64_tr_b16 v[148:149], v1 offset:20544\n" \
"ds_read_b64_tr_b16 v[150:151], v1 offset:21056\n" \
"ds_read_b64_tr_b16 v[36:37], v1 offset:20576\n" \
"ds_read_b64_tr_b16 v[38:39], v1 offset:21088\n" \
"s_waitcnt lgkmcnt(0)\n" \
"s_add_u32 m0, s52, 20480\n" \
"s_add_u32 s54, s97, 1966080\n" \
"s_lshr_b32 s55, s54, 8\n" \
"s_and_b32 s55, s55, 0x2000\n" \
"s_xor_b32 s54, s54, s55\n" \
"buffer_load_dwordx4 v0, s[40:43], s54 offen sc1 lds\n" \
"s_add_u32 m0, s52, 21504\n" \
"s_add_u32 s54, s97, 1998848\n" \
"s_lshr_b32 s55, s54, 8\n" \
"s_and_b32 s55, s55, 0x2000\n" \
"s_xor_b32 s54, s54, s55\n" \
"buffer_load_dwordx4 v0, s[40:43], s54 offen sc1 lds\n" \
"s_add_u32 m0, s52, 22528\n" \
"s_add_u32 s54, s97, 2031616\n" \
"s_lshr_b32 s55, s54, 8\n" \
"s_and_b32 s55, s55, 0x2000\n" \
"s_xor_b32 s54, s54, s55\n" \
"buffer_load_dwordx4 v0, s[40:43], s54 offen sc1 lds\n" \
"s_add_u32 m0, s52, 23552\n" \
"s_add_u32 s54, s97, 2064384\n" \
"s_lshr_b32 s55, s54, 8\n" \
"s_and_b32 s55, s55, 0x2000\n" \
"s_xor_b32 s54, s54, s55\n" \
"buffer_load_dwordx4 v0, s[40:43], s54 offen sc1 lds\n" \
"s_mov_b32 s54, s99\n" \
".Lsg2_z15_%=:\n" \
"s_sleep 1\n" \
"s_sub_u32 s54, s54, 1\n" \
"s_cbranch_scc0 .Lsg2_z15_%=\n" \
"s_waitcnt vmcnt(36)\n" \
"ds_read_b64_tr_b16 a[24:25], v1 offset:24576\n" \
"ds_read_b64_tr_b16 a[26:27], v1 offset:25088\n" \
"ds_read_b64_tr_b16 a[152:153], v1 offset:24608\n" \
"ds_read_b64_tr_b16 a[154:155], v1 offset:25120\n" \
"ds_read_b64_tr_b16 v[152:153], v1 offset:24640\n" \
"ds_read_b64_tr_b16 v[154:155], v1 offset:25152\n" \
"ds_read_b64_tr_b16 v[40:41], v1 offset:24672\n" \
"ds_read_b64_tr_b16 v[42:43], v1 offset:25184\n" \
"s_waitcnt lgkmcnt(0)\n" \
"s_add_u32 m0, s52, 24576\n" \
"s_add_u32 s54, s97, 2097152\n" \
"s_lshr_b32 s55, s54, 8\n" \
"s_and_b32 s55, s55, 0x2000\n" \
"s_xor_b32 s54, s54, s55\n" \
"buffer_load_dwordx4 v0, s[40:43], s54 offen sc1 lds\n" \
"s_add_u32 m0, s52, 25600\n" \
"s_add_u32 s54, s97, 2129920\n" \
"s_lshr_b32 s55, s54, 8\n" \
"s_and_b32 s55, s55, 0x2000\n" \
"s_xor_b32 s54, s54, s55\n" \
"buffer_load_dwordx4 v0, s[40:43], s54 offen sc1 lds\n" \
"s_add_u32 m0, s52, 26624\n" \
"s_add_u32 s54, s97, 2162688\n" \
"s_lshr_b32 s55, s54, 8\n" \
"s_and_b32 s55, s55, 0x2000\n" \
"s_xor_b32 s54, s54, s55\n" \
"buffer_load_dwordx4 v0, s[40:43], s54 offen sc1 lds\n" \
"s_add_u32 m0, s52, 27648\n" \
"s_add_u32 s54, s97, 2195456\n" \
"s_lshr_b32 s55, s54, 8\n" \
"s_and_b32 s55, s55, 0x2000\n" \
"s_xor_b32 s54, s54, s55\n" \
"buffer_load_dwordx4 v0, s[40:43], s54 offen sc1 lds\n" \
"s_mov_b32 s54, s99\n" \
".Lsg2_z16_%=:\n" \
"s_sleep 1\n" \
"s_sub_u32 s54, s54, 1\n" \
"s_cbranch_scc0 .Lsg2_z16_%=\n" \
"s_waitcnt vmcnt(36)\n" \
"ds_read_b64_tr_b16 a[28:29], v1 offset:28672\n" \
"ds_read_b64_tr_b16 a[30:31], v1 offset:29184\n" \
"ds_read_b64_tr_b16 a[156:157], v1 offset:28704\n" \
"ds_read_b64_tr_b16 a[158:159], v1 offset:29216\n" \
"ds_read_b64_tr_b16 v[156:157], v1 offset:28736\n" \
"ds_read_b64_tr_b16 v[158:159], v1 offset:29248\n" \
"ds_read_b64_tr_b16 v[44:45], v1 offset:28768\n" \
"ds_read_b64_tr_b16 v[46:47], v1 offset:29280\n" \
"s_waitcnt lgkmcnt(0)\n" \
"s_add_u32 m0, s52, 28672\n" \
"s_add_u32 s54, s97, 2228224\n" \
"s_lshr_b32 s55, s54, 8\n" \
"s_and_b32 s55, s55, 0x2000\n" \
"s_xor_b32 s54, s54, s55\n" \
"buffer_load_dwordx4 v0, s[40:43], s54 offen sc1 lds\n" \
"s_add_u32 m0, s52, 29696\n" \
"s_add_u32 s54, s97, 2260992\n" \
"s_lshr_b32 s55, s54, 8\n" \
"s_and_b32 s55, s55, 0x2000\n" \
"s_xor_b32 s54, s54, s55\n" \
"buffer_load_dwordx4 v0, s[40:43], s54 offen sc1 lds\n" \
"s_add_u32 m0, s52, 30720\n" \
"s_add_u32 s54, s97, 2293760\n" \
"s_lshr_b32 s55, s54, 8\n" \
"s_and_b32 s55, s55, 0x2000\n" \
"s_xor_b32 s54, s54, s55\n" \
"buffer_load_dwordx4 v0, s[40:43], s54 offen sc1 lds\n" \
"s_add_u32 m0, s52, 31744\n" \
"s_add_u32 s54, s97, 2326528\n" \
"s_lshr_b32 s55, s54, 8\n" \
"s_and_b32 s55, s55, 0x2000\n" \
"s_xor_b32 s54, s54, s55\n" \
"buffer_load_dwordx4 v0, s[40:43], s54 offen sc1 lds\n" \
"s_mov_b32 s54, s99\n" \
".Lsg2_z17_%=:\n" \
"s_sleep 1\n" \
"s_sub_u32 s54, s54, 1\n" \
"s_cbranch_scc0 .Lsg2_z17_%=\n" \
"s_waitcnt vmcnt(36)\n" \
"ds_read_b64_tr_b16 a[32:33], v9 offset:32768\n" \
"ds_read_b64_tr_b16 a[34:35], v9 offset:33280\n" \
"ds_read_b64_tr_b16 a[160:161], v9 offset:32800\n" \
"ds_read_b64_tr_b16 a[162:163], v9 offset:33312\n" \
"ds_read_b64_tr_b16 v[160:161], v9 offset:32832\n" \
"ds_read_b64_tr_b16 v[162:163], v9 offset:33344\n" \
"ds_read_b64_tr_b16 v[48:49], v9 offset:32864\n" \
"ds_read_b64_tr_b16 v[50:51], v9 offset:33376\n" \
"s_waitcnt lgkmcnt(0)\n" \
"s_add_u32 m0, s53, 32768\n" \
"s_add_u32 s54, s97, 2359296\n" \
"s_lshr_b32 s55, s54, 8\n" \
"s_and_b32 s55, s55, 0x2000\n" \
"s_xor_b32 s54, s54, s55\n" \
"buffer_load_dwordx4 v0, s[40:43], s54 offen sc1 lds\n" \
"s_add_u32 m0, s53, 33792\n" \
"s_add_u32 s54, s97, 2392064\n" \
"s_lshr_b32 s55, s54, 8\n" \
"s_and_b32 s55, s55, 0x2000\n" \
"s_xor_b32 s54, s54, s55\n" \
"buffer_load_dwordx4 v0, s[40:43], s54 offen sc1 lds\n" \
"s_add_u32 m0, s53, 34816\n" \
"s_add_u32 s54, s97, 2424832\n" \
"s_lshr_b32 s55, s54, 8\n" \
"s_and_b32 s55, s55, 0x2000\n" \
"s_xor_b32 s54, s54, s55\n" \
"buffer_load_dwordx4 v0, s[40:43], s54 offen sc1 lds\n" \
"s_add_u32 m0, s53, 35840\n" \
"s_add_u32 s54, s97, 2457600\n" \
"s_lshr_b32 s55, s54, 8\n" \
"s_and_b32 s55, s55, 0x2000\n" \
"s_xor_b32 s54, s54, s55\n" \
"buffer_load_dwordx4 v0, s[40:43], s54 offen sc1 lds\n" \
"s_mov_b32 s54, s99\n" \
".Lsg2_z18_%=:\n" \
"s_sleep 1\n" \
"s_sub_u32 s54, s54, 1\n" \
"s_cbranch_scc0 .Lsg2_z18_%=\n" \
"s_waitcnt vmcnt(36)\n" \
"ds_read_b64_tr_b16 a[36:37], v9 offset:36864\n" \
"ds_read_b64_tr_b16 a[38:39], v9 offset:37376\n" \
"ds_read_b64_tr_b16 a[164:165], v9 offset:36896\n" \
"ds_read_b64_tr_b16 a[166:167], v9 offset:37408\n" \
"ds_read_b64_tr_b16 v[164:165], v9 offset:36928\n" \
"ds_read_b64_tr_b16 v[166:167], v9 offset:37440\n" \
"ds_read_b64_tr_b16 v[52:53], v9 offset:36960\n" \
"ds_read_b64_tr_b16 v[54:55], v9 offset:37472\n" \
"s_waitcnt lgkmcnt(0)\n" \
"s_add_u32 m0, s53, 36864\n" \
"s_add_u32 s54, s97, 2490368\n" \
"s_lshr_b32 s55, s54, 8\n" \
"s_and_b32 s55, s55, 0x2000\n" \
"s_xor_b32 s54, s54, s55\n" \
"buffer_load_dwordx4 v0, s[40:43], s54 offen sc1 lds\n" \
"s_add_u32 m0, s53, 37888\n" \
"s_add_u32 s54, s97, 2523136\n" \
"s_lshr_b32 s55, s54, 8\n" \
"s_and_b32 s55, s55, 0x2000\n" \
"s_xor_b32 s54, s54, s55\n" \
"buffer_load_dwordx4 v0, s[40:43], s54 offen sc1 lds\n" \
"s_add_u32 m0, s53, 38912\n" \
"s_add_u32 s54, s97, 2555904\n" \
"s_lshr_b32 s55, s54, 8\n" \
"s_and_b32 s55, s55, 0x2000\n" \
"s_xor_b32 s54, s54, s55\n" \
"buffer_load_dwordx4 v0, s[40:43], s54 offen sc1 lds\n" \
"s_add_u32 m0, s53, 39936\n" \
"s_add_u32 s54, s97, 2588672\n" \
"s_lshr_b32 s55, s54, 8\n" \
"s_and_b32 s55, s55, 0x2000\n" \
"s_xor_b32 s54, s54, s55\n" \
"buffer_load_dwordx4 v0, s[40:43], s54 offen sc1 lds\n" \
"s_mov_b32 s54, s99\n" \
".Lsg2_z19_%=:\n" \
"s_sleep 1\n" \
"s_sub_u32 s54, s54, 1\n" \
"s_cbranch_scc0 .Lsg2_z19_%=\n" \
"s_waitcnt vmcnt(36)\n" \
"ds_read_b64_tr_b16 a[40:41], v1 offset:0\n" \
"ds_read_b64_tr_b16 a[42:43], v1 offset:512\n" \
"ds_read_b64_tr_b16 a[168:169], v1 offset:32\n" \
"ds_read_b64_tr_b16 a[170:171], v1 offset:544\n" \
"ds_read_b64_tr_b16 v[168:169], v1 offset:64\n" \
"ds_read_b64_tr_b16 v[170:171], v1 offset:576\n" \
"ds_read_b64_tr_b16 v[56:57], v1 offset:96\n" \
"ds_read_b64_tr_b16 v[58:59], v1 offset:608\n" \
"s_waitcnt lgkmcnt(0)\n" \
"s_add_u32 m0, s52, 0\n" \
"s_add_u32 s54, s97, 2621440\n" \
"s_lshr_b32 s55, s54, 8\n" \
"s_and_b32 s55, s55, 0x2000\n" \
"s_xor_b32 s54, s54, s55\n" \
"buffer_load_dwordx4 v0, s[40:43], s54 offen sc1 lds\n" \
"s_add_u32 m0, s52, 1024\n" \
"s_add_u32 s54, s97, 2654208\n" \
"s_lshr_b32 s55, s54, 8\n" \
"s_and_b32 s55, s55, 0x2000\n" \
"s_xor_b32 s54, s54, s55\n" \
"buffer_load_dwordx4 v0, s[40:43], s54 offen sc1 lds\n" \
"s_add_u32 m0, s52, 2048\n" \
"s_add_u32 s54, s97, 2686976\n" \
"s_lshr_b32 s55, s54, 8\n" \
"s_and_b32 s55, s55, 0x2000\n" \
"s_xor_b32 s54, s54, s55\n" \
"buffer_load_dwordx4 v0, s[40:43], s54 offen sc1 lds\n" \
"s_add_u32 m0, s52, 3072\n" \
"s_add_u32 s54, s97, 2719744\n" \
"s_lshr_b32 s55, s54, 8\n" \
"s_and_b32 s55, s55, 0x2000\n" \
"s_xor_b32 s54, s54, s55\n" \
"buffer_load_dwordx4 v0, s[40:43], s54 offen sc1 lds\n" \
"s_mov_b32 s54, s99\n" \
".Lsg2_z20_%=:\n" \
"s_sleep 1\n" \
"s_sub_u32 s54, s54, 1\n" \
"s_cbranch_scc0 .Lsg2_z20_%=\n" \
"s_waitcnt vmcnt(36)\n" \
"ds_read_b64_tr_b16 a[44:45], v1 offset:4096\n" \
"ds_read_b64_tr_b16 a[46:47], v1 offset:4608\n" \
"ds_read_b64_tr_b16 a[172:173], v1 offset:4128\n" \
"ds_read_b64_tr_b16 a[174:175], v1 offset:4640\n" \
"ds_read_b64_tr_b16 v[172:173], v1 offset:4160\n" \
"ds_read_b64_tr_b16 v[174:175], v1 offset:4672\n" \
"ds_read_b64_tr_b16 v[60:61], v1 offset:4192\n" \
"ds_read_b64_tr_b16 v[62:63], v1 offset:4704\n" \
"s_waitcnt lgkmcnt(0)\n" \
"s_add_u32 m0, s52, 4096\n" \
"s_add_u32 s54, s97, 2752512\n" \
"s_lshr_b32 s55, s54, 8\n" \
"s_and_b32 s55, s55, 0x2000\n" \
"s_xor_b32 s54, s54, s55\n" \
"buffer_load_dwordx4 v0, s[40:43], s54 offen sc1 lds\n" \
"s_add_u32 m0, s52, 5120\n" \
"s_add_u32 s54, s97, 2785280\n" \
"s_lshr_b32 s55, s54, 8\n" \
"s_and_b32 s55, s55, 0x2000\n" \
"s_xor_b32 s54, s54, s55\n" \
"buffer_load_dwordx4 v0, s[40:43], s54 offen sc1 lds\n" \
"s_add_u32 m0, s52, 6144\n" \
"s_add_u32 s54, s97, 2818048\n" \
"s_lshr_b32 s55, s54, 8\n" \
"s_and_b32 s55, s55, 0x2000\n" \
"s_xor_b32 s54, s54, s55\n" \
"buffer_load_dwordx4 v0, s[40:43], s54 offen sc1 lds\n" \
"s_add_u32 m0, s52, 7168\n" \
"s_add_u32 s54, s97, 2850816\n" \
"s_lshr_b32 s55, s54, 8\n" \
"s_and_b32 s55, s55, 0x2000\n" \
"s_xor_b32 s54, s54, s55\n" \
"buffer_load_dwordx4 v0, s[40:43], s54 offen sc1 lds\n" \
"s_mov_b32 s54, s99\n" \
".Lsg2_z21_%=:\n" \
"s_sleep 1\n" \
"s_sub_u32 s54, s54, 1\n" \
"s_cbranch_scc0 .Lsg2_z21_%=\n" \
"s_waitcnt vmcnt(36)\n" \
"ds_read_b64_tr_b16 a[48:49], v1 offset:8192\n" \
"ds_read_b64_tr_b16 a[50:51], v1 offset:8704\n" \
"ds_read_b64_tr_b16 a[176:177], v1 offset:8224\n" \
"ds_read_b64_tr_b16 a[178:179], v1 offset:8736\n" \
"ds_read_b64_tr_b16 v[176:177], v1 offset:8256\n" \
"ds_read_b64_tr_b16 v[178:179], v1 offset:8768\n" \
"ds_read_b64_tr_b16 v[64:65], v1 offset:8288\n" \
"ds_read_b64_tr_b16 v[66:67], v1 offset:8800\n" \
"s_waitcnt lgkmcnt(0)\n" \
"s_add_u32 m0, s52, 8192\n" \
"s_add_u32 s54, s97, 2883584\n" \
"s_lshr_b32 s55, s54, 8\n" \
"s_and_b32 s55, s55, 0x2000\n" \
"s_xor_b32 s54, s54, s55\n" \
"buffer_load_dwordx4 v0, s[40:43], s54 offen sc1 lds\n" \
"s_add_u32 m0, s52, 9216\n" \
"s_add_u32 s54, s97, 2916352\n" \
"s_lshr_b32 s55, s54, 8\n" \
"s_and_b32 s55, s55, 0x2000\n" \
"s_xor_b32 s54, s54, s55\n" \
"buffer_load_dwordx4 v0, s[40:43], s54 offen sc1 lds\n" \
"s_add_u32 m0, s52, 10240\n" \
"s_add_u32 s54, s97, 2949120\n" \
"s_lshr_b32 s55, s54, 8\n" \
"s_and_b32 s55, s55, 0x2000\n" \
"s_xor_b32 s54, s54, s55\n" \
"buffer_load_dwordx4 v0, s[40:43], s54 offen sc1 lds\n" \
"s_add_u32 m0, s52, 11264\n" \
"s_add_u32 s54, s97, 2981888\n" \
"s_lshr_b32 s55, s54, 8\n" \
"s_and_b32 s55, s55, 0x2000\n" \
"s_xor_b32 s54, s54, s55\n" \
"buffer_load_dwordx4 v0, s[40:43], s54 offen sc1 lds\n" \
"s_mov_b32 s54, s99\n" \
".Lsg2_z22_%=:\n" \
"s_sleep 1\n" \
"s_sub_u32 s54, s54, 1\n" \
"s_cbranch_scc0 .Lsg2_z22_%=\n" \
"s_waitcnt vmcnt(36)\n" \
"ds_read_b64_tr_b16 a[52:53], v1 offset:12288\n" \
"ds_read_b64_tr_b16 a[54:55], v1 offset:12800\n" \
"ds_read_b64_tr_b16 a[180:181], v1 offset:12320\n" \
"ds_read_b64_tr_b16 a[182:183], v1 offset:12832\n" \
"ds_read_b64_tr_b16 v[180:181], v1 offset:12352\n" \
"ds_read_b64_tr_b16 v[182:183], v1 offset:12864\n" \
"ds_read_b64_tr_b16 v[68:69], v1 offset:12384\n" \
"ds_read_b64_tr_b16 v[70:71], v1 offset:12896\n" \
"s_waitcnt lgkmcnt(0)\n" \
"s_add_u32 m0, s52, 12288\n" \
"s_add_u32 s54, s97, 3014656\n" \
"s_lshr_b32 s55, s54, 8\n" \
"s_and_b32 s55, s55, 0x2000\n" \
"s_xor_b32 s54, s54, s55\n" \
"buffer_load_dwordx4 v0, s[40:43], s54 offen sc1 lds\n" \
"s_add_u32 m0, s52, 13312\n" \
"s_add_u32 s54, s97, 3047424\n" \
"s_lshr_b32 s55, s54, 8\n" \
"s_and_b32 s55, s55, 0x2000\n" \
"s_xor_b32 s54, s54, s55\n" \
"buffer_load_dwordx4 v0, s[40:43], s54 offen sc1 lds\n" \
"s_add_u32 m0, s52, 14336\n" \
"s_add_u32 s54, s97, 3080192\n" \
"s_lshr_b32 s55, s54, 8\n" \
"s_and_b32 s55, s55, 0x2000\n" \
"s_xor_b32 s54, s54, s55\n" \
"buffer_load_dwordx4 v0, s[40:43], s54 offen sc1 lds\n" \
"s_add_u32 m0, s52, 15360\n" \
"s_add_u32 s54, s97, 3112960\n" \
"s_lshr_b32 s55, s54, 8\n" \
"s_and_b32 s55, s55, 0x2000\n" \
"s_xor_b32 s54, s54, s55\n" \
"buffer_load_dwordx4 v0, s[40:43], s54 offen sc1 lds\n" \
"s_mov_b32 s54, s99\n" \
".Lsg2_z23_%=:\n" \
"s_sleep 1\n" \
"s_sub_u32 s54, s54, 1\n" \
"s_cbranch_scc0 .Lsg2_z23_%=\n" \
"s_waitcnt vmcnt(36)\n" \
"ds_read_b64_tr_b16 a[56:57], v1 offset:16384\n" \
"ds_read_b64_tr_b16 a[58:59], v1 offset:16896\n" \
"ds_read_b64_tr_b16 a[184:185], v1 offset:16416\n" \
"ds_read_b64_tr_b16 a[186:187], v1 offset:16928\n" \
"ds_read_b64_tr_b16 v[184:185], v1 offset:16448\n" \
"ds_read_b64_tr_b16 v[186:187], v1 offset:16960\n" \
"ds_read_b64_tr_b16 v[72:73], v1 offset:16480\n" \
"ds_read_b64_tr_b16 v[74:75], v1 offset:16992\n" \
"s_waitcnt lgkmcnt(0)\n" \
"s_add_u32 m0, s52, 16384\n" \
"s_add_u32 s54, s97, 3145728\n" \
"s_lshr_b32 s55, s54, 8\n" \
"s_and_b32 s55, s55, 0x2000\n" \
"s_xor_b32 s54, s54, s55\n" \
"buffer_load_dwordx4 v0, s[40:43], s54 offen sc1 lds\n" \
"s_add_u32 m0, s52, 17408\n" \
"s_add_u32 s54, s97, 3178496\n" \
"s_lshr_b32 s55, s54, 8\n" \
"s_and_b32 s55, s55, 0x2000\n" \
"s_xor_b32 s54, s54, s55\n" \
"buffer_load_dwordx4 v0, s[40:43], s54 offen sc1 lds\n" \
"s_add_u32 m0, s52, 18432\n" \
"s_add_u32 s54, s97, 3211264\n" \
"s_lshr_b32 s55, s54, 8\n" \
"s_and_b32 s55, s55, 0x2000\n" \
"s_xor_b32 s54, s54, s55\n" \
"buffer_load_dwordx4 v0, s[40:43], s54 offen sc1 lds\n" \
"s_add_u32 m0, s52, 19456\n" \
"s_add_u32 s54, s97, 3244032\n" \
"s_lshr_b32 s55, s54, 8\n" \
"s_and_b32 s55, s55, 0x2000\n" \
"s_xor_b32 s54, s54, s55\n" \
"buffer_load_dwordx4 v0, s[40:43], s54 offen sc1 lds\n" \
"s_mov_b32 s54, s99\n" \
".Lsg2_z24_%=:\n" \
"s_sleep 1\n" \
"s_sub_u32 s54, s54, 1\n" \
"s_cbranch_scc0 .Lsg2_z24_%=\n" \
"s_waitcnt vmcnt(36)\n" \
"ds_read_b64_tr_b16 a[60:61], v1 offset:20480\n" \
"ds_read_b64_tr_b16 a[62:63], v1 offset:20992\n" \
"ds_read_b64_tr_b16 a[188:189], v1 offset:20512\n" \
"ds_read_b64_tr_b16 a[190:191], v1 offset:21024\n" \
"ds_read_b64_tr_b16 v[188:189], v1 offset:20544\n" \
"ds_read_b64_tr_b16 v[190:191], v1 offset:21056\n" \
"ds_read_b64_tr_b16 v[76:77], v1 offset:20576\n" \
"ds_read_b64_tr_b16 v[78:79], v1 offset:21088\n" \
"s_waitcnt lgkmcnt(0)\n" \
"s_add_u32 m0, s52, 20480\n" \
"s_add_u32 s54, s97, 3276800\n" \
"s_lshr_b32 s55, s54, 8\n" \
"s_and_b32 s55, s55, 0x2000\n" \
"s_xor_b32 s54, s54, s55\n" \
"buffer_load_dwordx4 v0, s[40:43], s54 offen sc1 lds\n" \
"s_add_u32 m0, s52, 21504\n" \
"s_add_u32 s54, s97, 3309568\n" \
"s_lshr_b32 s55, s54, 8\n" \
"s_and_b32 s55, s55, 0x2000\n" \
"s_xor_b32 s54, s54, s55\n" \
"buffer_load_dwordx4 v0, s[40:43], s54 offen sc1 lds\n" \
"s_add_u32 m0, s52, 22528\n" \
"s_add_u32 s54, s97, 3342336\n" \
"s_lshr_b32 s55, s54, 8\n" \
"s_and_b32 s55, s55, 0x2000\n" \
"s_xor_b32 s54, s54, s55\n" \
"buffer_load_dwordx4 v0, s[40:43], s54 offen sc1 lds\n" \
"s_add_u32 m0, s52, 23552\n" \
"s_add_u32 s54, s97, 3375104\n" \
"s_lshr_b32 s55, s54, 8\n" \
"s_and_b32 s55, s55, 0x2000\n" \
"s_xor_b32 s54, s54, s55\n" \
"buffer_load_dwordx4 v0, s[40:43], s54 offen sc1 lds\n" \
"s_mov_b32 s54, s99\n" \
".Lsg2_z25_%=:\n" \
"s_sleep 1\n" \
"s_sub_u32 s54, s54, 1\n" \
"s_cbranch_scc0 .Lsg2_z25_%=\n" \
"s_waitcnt vmcnt(36)\n" \
"ds_read_b64_tr_b16 a[64:65], v1 offset:24576\n" \
"ds_read_b64_tr_b16 a[66:67], v1 offset:25088\n" \
"ds_read_b64_tr_b16 a[192:193], v1 offset:24608\n" \
"ds_read_b64_tr_b16 a[194:195], v1 offset:25120\n" \
"ds_read_b64_tr_b16 v[192:193], v1 offset:24640\n" \
"ds_read_b64_tr_b16 v[194:195], v1 offset:25152\n" \
"ds_read_b64_tr_b16 v[80:81], v1 offset:24672\n" \
"ds_read_b64_tr_b16 v[82:83], v1 offset:25184\n" \
"s_waitcnt lgkmcnt(0)\n" \
"s_add_u32 m0, s52, 24576\n" \
"s_add_u32 s54, s97, 3407872\n" \
"s_lshr_b32 s55, s54, 8\n" \
"s_and_b32 s55, s55, 0x2000\n" \
"s_xor_b32 s54, s54, s55\n" \
"buffer_load_dwordx4 v0, s[40:43], s54 offen sc1 lds\n" \
"s_add_u32 m0, s52, 25600\n" \
"s_add_u32 s54, s97, 3440640\n" \
"s_lshr_b32 s55, s54, 8\n" \
"s_and_b32 s55, s55, 0x2000\n" \
"s_xor_b32 s54, s54, s55\n" \
"buffer_load_dwordx4 v0, s[40:43], s54 offen sc1 lds\n" \
"s_add_u32 m0, s52, 26624\n" \
"s_add_u32 s54, s97, 3473408\n" \
"s_lshr_b32 s55, s54, 8\n" \
"s_and_b32 s55, s55, 0x2000\n" \
"s_xor_b32 s54, s54, s55\n" \
"buffer_load_dwordx4 v0, s[40:43], s54 offen sc1 lds\n" \
"s_add_u32 m0, s52, 27648\n" \
"s_add_u32 s54, s97, 3506176\n" \
"s_lshr_b32 s55, s54, 8\n" \
"s_and_b32 s55, s55, 0x2000\n" \
"s_xor_b32 s54, s54, s55\n" \
"buffer_load_dwordx4 v0, s[40:43], s54 offen sc1 lds\n" \
"s_mov_b32 s54, s99\n" \
".Lsg2_z26_%=:\n" \
"s_sleep 1\n" \
"s_sub_u32 s54, s54, 1\n" \
"s_cbranch_scc0 .Lsg2_z26_%=\n" \
"s_waitcnt vmcnt(36)\n" \
"ds_read_b64_tr_b16 a[68:69], v1 offset:28672\n" \
"ds_read_b64_tr_b16 a[70:71], v1 offset:29184\n" \
"ds_read_b64_tr_b16 a[196:197], v1 offset:28704\n" \
"ds_read_b64_tr_b16 a[198:199], v1 offset:29216\n" \
"ds_read_b64_tr_b16 v[196:197], v1 offset:28736\n" \
"ds_read_b64_tr_b16 v[198:199], v1 offset:29248\n" \
"ds_read_b64_tr_b16 v[84:85], v1 offset:28768\n" \
"ds_read_b64_tr_b16 v[86:87], v1 offset:29280\n" \
"s_waitcnt lgkmcnt(0)\n" \
"s_add_u32 m0, s52, 28672\n" \
"s_add_u32 s54, s97, 3538944\n" \
"s_lshr_b32 s55, s54, 8\n" \
"s_and_b32 s55, s55, 0x2000\n" \
"s_xor_b32 s54, s54, s55\n" \
"buffer_load_dwordx4 v0, s[40:43], s54 offen sc1 lds\n" \
"s_add_u32 m0, s52, 29696\n" \
"s_add_u32 s54, s97, 3571712\n" \
"s_lshr_b32 s55, s54, 8\n" \
"s_and_b32 s55, s55, 0x2000\n" \
"s_xor_b32 s54, s54, s55\n" \
"buffer_load_dwordx4 v0, s[40:43], s54 offen sc1 lds\n" \
"s_add_u32 m0, s52, 30720\n" \
"s_add_u32 s54, s97, 3604480\n" \
"s_lshr_b32 s55, s54, 8\n" \
"s_and_b32 s55, s55, 0x2000\n" \
"s_xor_b32 s54, s54, s55\n" \
"buffer_load_dwordx4 v0, s[40:43], s54 offen sc1 lds\n" \
"s_add_u32 m0, s52, 31744\n" \
"s_add_u32 s54, s97, 3637248\n" \
"s_lshr_b32 s55, s54, 8\n" \
"s_and_b32 s55, s55, 0x2000\n" \
"s_xor_b32 s54, s54, s55\n" \
"buffer_load_dwordx4 v0, s[40:43], s54 offen sc1 lds\n" \
"s_mov_b32 s54, s99\n" \
".Lsg2_z27_%=:\n" \
"s_sleep 1\n" \
"s_sub_u32 s54, s54, 1\n" \
"s_cbranch_scc0 .Lsg2_z27_%=\n" \
"s_waitcnt vmcnt(36)\n" \
"ds_read_b64_tr_b16 a[72:73], v9 offset:32768\n" \
"ds_read_b64_tr_b16 a[74:75], v9 offset:33280\n" \
"ds_read_b64_tr_b16 a[200:201], v9 offset:32800\n" \
"ds_read_b64_tr_b16 a[202:203], v9 offset:33312\n" \
"ds_read_b64_tr_b16 v[200:201], v9 offset:32832\n" \
"ds_read_b64_tr_b16 v[202:203], v9 offset:33344\n" \
"ds_read_b64_tr_b16 v[88:89], v9 offset:32864\n" \
"ds_read_b64_tr_b16 v[90:91], v9 offset:33376\n" \
"s_waitcnt lgkmcnt(0)\n" \
"s_waitcnt vmcnt(32)\n" \
"ds_read_b64_tr_b16 a[76:77], v9 offset:36864\n" \
"ds_read_b64_tr_b16 a[78:79], v9 offset:37376\n" \
"ds_read_b64_tr_b16 a[204:205], v9 offset:36896\n" \
"ds_read_b64_tr_b16 a[206:207], v9 offset:37408\n" \
"ds_read_b64_tr_b16 v[204:205], v9 offset:36928\n" \
"ds_read_b64_tr_b16 v[206:207], v9 offset:37440\n" \
"ds_read_b64_tr_b16 v[92:93], v9 offset:36960\n" \
"ds_read_b64_tr_b16 v[94:95], v9 offset:37472\n" \
"s_waitcnt lgkmcnt(0)\n" \
"s_waitcnt vmcnt(28)\n" \
"ds_read_b64_tr_b16 a[80:81], v1 offset:0\n" \
"ds_read_b64_tr_b16 a[82:83], v1 offset:512\n" \
"ds_read_b64_tr_b16 a[208:209], v1 offset:32\n" \
"ds_read_b64_tr_b16 a[210:211], v1 offset:544\n" \
"ds_read_b64_tr_b16 v[208:209], v1 offset:64\n" \
"ds_read_b64_tr_b16 v[210:211], v1 offset:576\n" \
"ds_read_b64_tr_b16 v[96:97], v1 offset:96\n" \
"ds_read_b64_tr_b16 v[98:99], v1 offset:608\n" \
"s_waitcnt lgkmcnt(0)\n" \
"ds_write_b128 v2, v[16:19] offset:0\n" \
"ds_write_b128 v2, v[20:23] offset:1024\n" \
"ds_write_b128 v2, v[24:27] offset:2048\n" \
"ds_write_b128 v2, v[28:31] offset:3072\n" \
"s_waitcnt vmcnt(24)\n" \
"ds_read_b64_tr_b16 a[84:85], v1 offset:4096\n" \
"ds_read_b64_tr_b16 a[86:87], v1 offset:4608\n" \
"ds_read_b64_tr_b16 a[212:213], v1 offset:4128\n" \
"ds_read_b64_tr_b16 a[214:215], v1 offset:4640\n" \
"ds_read_b64_tr_b16 v[212:213], v1 offset:4160\n" \
"ds_read_b64_tr_b16 v[214:215], v1 offset:4672\n" \
"ds_read_b64_tr_b16 v[100:101], v1 offset:4192\n" \
"ds_read_b64_tr_b16 v[102:103], v1 offset:4704\n" \
"s_waitcnt lgkmcnt(0)\n" \
"ds_write_b128 v2, v[32:35] offset:4096\n" \
"ds_write_b128 v2, v[36:39] offset:5120\n" \
"ds_write_b128 v2, v[40:43] offset:6144\n" \
"ds_write_b128 v2, v[44:47] offset:7168\n" \
"s_waitcnt vmcnt(20)\n" \
"ds_read_b64_tr_b16 a[88:89], v1 offset:8192\n" \
"ds_read_b64_tr_b16 a[90:91], v1 offset:8704\n" \
"ds_read_b64_tr_b16 a[216:217], v1 offset:8224\n" \
"ds_read_b64_tr_b16 a[218:219], v1 offset:8736\n" \
"ds_read_b64_tr_b16 v[216:217], v1 offset:8256\n" \
"ds_read_b64_tr_b16 v[218:219], v1 offset:8768\n" \
"ds_read_b64_tr_b16 v[104:105], v1 offset:8288\n" \
"ds_read_b64_tr_b16 v[106:107], v1 offset:8800\n" \
"s_waitcnt lgkmcnt(0)\n" \
"ds_write_b128 v2, v[48:51] offset:8192\n" \
"ds_write_b128 v2, v[52:55] offset:9216\n" \
"ds_write_b128 v2, v[56:59] offset:10240\n" \
"ds_write_b128 v2, v[60:63] offset:11264\n" \
"s_waitcnt vmcnt(16)\n" \
"ds_read_b64_tr_b16 a[92:93], v1 offset:12288\n" \
"ds_read_b64_tr_b16 a[94:95], v1 offset:12800\n" \
"ds_read_b64_tr_b16 a[220:221], v1 offset:12320\n" \
"ds_read_b64_tr_b16 a[222:223], v1 offset:12832\n" \
"ds_read_b64_tr_b16 v[220:221], v1 offset:12352\n" \
"ds_read_b64_tr_b16 v[222:223], v1 offset:12864\n" \
"ds_read_b64_tr_b16 v[108:109], v1 offset:12384\n" \
"ds_read_b64_tr_b16 v[110:111], v1 offset:12896\n" \
"s_waitcnt lgkmcnt(0)\n" \
"ds_write_b128 v2, v[64:67] offset:12288\n" \
"ds_write_b128 v2, v[68:71] offset:13312\n" \
"ds_write_b128 v2, v[72:75] offset:14336\n" \
"ds_write_b128 v2, v[76:79] offset:15360\n" \
"s_waitcnt vmcnt(12)\n" \
"ds_read_b64_tr_b16 a[96:97], v1 offset:16384\n" \
"ds_read_b64_tr_b16 a[98:99], v1 offset:16896\n" \
"ds_read_b64_tr_b16 a[224:225], v1 offset:16416\n" \
"ds_read_b64_tr_b16 a[226:227], v1 offset:16928\n" \
"ds_read_b64_tr_b16 v[224:225], v1 offset:16448\n" \
"ds_read_b64_tr_b16 v[226:227], v1 offset:16960\n" \
"ds_read_b64_tr_b16 v[16:17], v1 offset:16480\n" \
"ds_read_b64_tr_b16 v[18:19], v1 offset:16992\n" \
"s_waitcnt lgkmcnt(0)\n" \
"ds_write_b128 v2, v[80:83] offset:16384\n" \
"ds_write_b128 v2, v[84:87] offset:17408\n" \
"ds_write_b128 v2, v[88:91] offset:18432\n" \
"ds_write_b128 v2, v[92:95] offset:19456\n" \
"s_waitcnt vmcnt(8)\n" \
"ds_read_b64_tr_b16 a[100:101], v1 offset:20480\n" \
"ds_read_b64_tr_b16 a[102:103], v1 offset:20992\n" \
"ds_read_b64_tr_b16 a[228:229], v1 offset:20512\n" \
"ds_read_b64_tr_b16 a[230:231], v1 offset:21024\n" \
"ds_read_b64_tr_b16 v[228:229], v1 offset:20544\n" \
"ds_read_b64_tr_b16 v[230:231], v1 offset:21056\n" \
"ds_read_b64_tr_b16 v[20:21], v1 offset:20576\n" \
"ds_read_b64_tr_b16 v[22:23], v1 offset:21088\n" \
"s_waitcnt lgkmcnt(0)\n" \
"ds_write_b128 v2, v[96:99] offset:20480\n" \
"ds_write_b128 v2, v[100:103] offset:21504\n" \
"ds_write_b128 v2, v[104:107] offset:22528\n" \
"ds_write_b128 v2, v[108:111] offset:23552\n" \
"s_waitcnt vmcnt(4)\n" \
"ds_read_b64_tr_b16 a[104:105], v1 offset:24576\n" \
"ds_read_b64_tr_b16 a[106:107], v1 offset:25088\n" \
"ds_read_b64_tr_b16 a[232:233], v1 offset:24608\n" \
"ds_read_b64_tr_b16 a[234:235], v1 offset:25120\n" \
"ds_read_b64_tr_b16 v[232:233], v1 offset:24640\n" \
"ds_read_b64_tr_b16 v[234:235], v1 offset:25152\n" \
"ds_read_b64_tr_b16 v[24:25], v1 offset:24672\n" \
"ds_read_b64_tr_b16 v[26:27], v1 offset:25184\n" \
"s_waitcnt lgkmcnt(0)\n" \
"ds_write_b128 v2, v[16:19] offset:24576\n" \
"ds_write_b128 v2, v[20:23] offset:25600\n" \
"ds_write_b128 v2, v[24:27] offset:26624\n" \
"s_waitcnt vmcnt(0)\n" \
"ds_read_b64_tr_b16 a[108:109], v1 offset:28672\n" \
"ds_read_b64_tr_b16 a[110:111], v1 offset:29184\n" \
"ds_read_b64_tr_b16 a[236:237], v1 offset:28704\n" \
"ds_read_b64_tr_b16 a[238:239], v1 offset:29216\n" \
"ds_read_b64_tr_b16 v[236:237], v1 offset:28736\n" \
"ds_read_b64_tr_b16 v[238:239], v1 offset:29248\n" \
"ds_read_b64_tr_b16 v[28:29], v1 offset:28768\n" \
"ds_read_b64_tr_b16 v[30:31], v1 offset:29280\n" \
"s_waitcnt lgkmcnt(0)\n" \
"ds_write_b128 v2, v[28:31] offset:28672\n" \
"s_waitcnt vmcnt(0) lgkmcnt(0)\n" \
"s_barrier\n" \
"s_mul_i32 s54, s48, 7\n" \
"s_add_u32 s54, s54, 0\n" \
"s_lshr_b32 s55, s54, 1\n" \
"s_lshl_b32 s55, s55, 14\n" \
"s_and_b32 s54, s54, 1\n" \
"s_lshl_b32 s54, s54, 12\n" \
"s_add_u32 s54, s54, s55\n" \
"s_add_u32 s86, s54, s85\n" \
"s_mul_i32 s54, s48, 7\n" \
"s_add_u32 s54, s54, 1\n" \
"s_lshr_b32 s55, s54, 1\n" \
"s_lshl_b32 s55, s55, 14\n" \
"s_and_b32 s54, s54, 1\n" \
"s_lshl_b32 s54, s54, 12\n" \
"s_add_u32 s54, s54, s55\n" \
"s_add_u32 s87, s54, s85\n" \
"s_mul_i32 s54, s48, 7\n" \
"s_add_u32 s54, s54, 2\n" \
"s_lshr_b32 s55, s54, 1\n" \
"s_lshl_b32 s55, s55, 14\n" \
"s_and_b32 s54, s54, 1\n" \
"s_lshl_b32 s54, s54, 12\n" \
"s_add_u32 s54, s54, s55\n" \
"s_add_u32 s88, s54, s85\n" \
"s_mul_i32 s54, s48, 7\n" \
"s_add_u32 s54, s54, 3\n" \
"s_lshr_b32 s55, s54, 1\n" \
"s_lshl_b32 s55, s55, 14\n" \
"s_and_b32 s54, s54, 1\n" \
"s_lshl_b32 s54, s54, 12\n" \
"s_add_u32 s54, s54, s55\n" \
"s_add_u32 s89, s54, s85\n" \
"s_mul_i32 s54, s48, 7\n" \
"s_add_u32 s54, s54, 4\n" \
"s_lshr_b32 s55, s54, 1\n" \
"s_lshl_b32 s55, s55, 14\n" \
"s_and_b32 s54, s54, 1\n" \
"s_lshl_b32 s54, s54, 12\n" \
"s_add_u32 s54, s54, s55\n" \
"s_add_u32 s90, s54, s85\n" \
"s_mul_i32 s54, s48, 7\n" \
"s_add_u32 s54, s54, 5\n" \
"s_lshr_b32 s55, s54, 1\n" \
"s_lshl_b32 s55, s55, 14\n" \
"s_and_b32 s54, s54, 1\n" \
"s_lshl_b32 s54, s54, 12\n" \
"s_add_u32 s54, s54, s55\n" \
"s_add_u32 s91, s54, s85\n" \
"s_mul_i32 s54, s48, 7\n" \
"s_add_u32 s54, s54, 6\n" \
"s_lshr_b32 s55, s54, 1\n" \
"s_lshl_b32 s55, s55, 14\n" \
"s_and_b32 s54, s54, 1\n" \
"s_lshl_b32 s54, s54, 12\n" \
"s_add_u32 s54, s54, s55\n" \
"s_add_u32 s92, s54, s85\n" \
"s_mov_b32 s55, 0\n" \
"s_mov_b32 s60, 0\n" \
"s_mov_b32 s79, s95\n" \
"s_mov_b32 s98, 0x70000000\n" \
".Lsg2_prime_%=:\n" \
"s_lshr_b32 s59, s44, 1\n" \
"s_add_u32 s59, s59, 1\n" \
"s_lshl_b32 s56, s44, 18\n" \
"s_lshr_b32 s58, s56, 8\n" \
"s_and_b32 s58, s58, 0x2000\n" \
"s_xor_b32 s56, s56, s58\n" \
"buffer_load_dword v11, v4, s[36:39], 0 offen sc1\n" \
"s_waitcnt vmcnt(0)\n" \
"v_cmp_gt_u32_e64 s[62:63], s59, v11\n" \
"s_nop 4\n" \
"s_cmp_lg_u64 s[62:63], 0\n" \
"s_cbranch_scc1 .Lsg2_w_%=\n" \
".Lsg2_wk_%=:\n" \
"s_mov_b32 s76, 0x7ffffff0\n" \
"s_mov_b32 s81, 1\n" \
"s_mov_b32 s84, 0\n" \
"s_waitcnt vmcnt(0) lgkmcnt(0)\n" \
"s_xor_b32 s57, s56, s86\n" \
"buffer_load_dwordx4 v[16:19], v3, s[36:39], s57 offen offset:0 sc1\n" \
"buffer_load_dwordx4 v[20:23], v3, s[36:39], s57 offen offset:1024 sc1\n" \
"buffer_load_dwordx4 v[24:27], v3, s[36:39], s57 offen offset:2048 sc1\n" \
"buffer_load_dwordx4 v[28:31], v3, s[36:39], s57 offen offset:3072 sc1\n" \
"s_xor_b32 s57, s56, s87\n" \
"buffer_load_dwordx4 v[32:35], v3, s[36:39], s57 offen offset:0 sc1\n" \
"buffer_load_dwordx4 v[36:39], v3, s[36:39], s57 offen offset:1024 sc1\n" \
"buffer_load_dwordx4 v[40:43], v3, s[36:39], s57 offen offset:2048 sc1\n" \
"buffer_load_dwordx4 v[44:47], v3, s[36:39], s57 offen offset:3072 sc1\n" \
"s_xor_b32 s57, s56, s88\n" \
"buffer_load_dwordx4 v[48:51], v3, s[36:39], s57 offen offset:0 sc1\n" \
"buffer_load_dwordx4 v[52:55], v3, s[36:39], s57 offen offset:1024 sc1\n" \
"buffer_load_dwordx4 v[56:59], v3, s[36:39], s57 offen offset:2048 sc1\n" \
"buffer_load_dwordx4 v[60:63], v3, s[36:39], s57 offen offset:3072 sc1\n" \
"s_xor_b32 s57, s56, s89\n" \
"buffer_load_dwordx4 v[64:67], v3, s[36:39], s57 offen offset:0 sc1\n" \
"buffer_load_dwordx4 v[68:71], v3, s[36:39], s57 offen offset:1024 sc1\n" \
"ds_read_b128 v[80:83], v2 offset:0\n" \
"ds_read_b128 v[84:87], v2 offset:1024\n" \
"ds_read_b128 v[88:91], v2 offset:2048\n" \
"s_waitcnt lgkmcnt(0)\n" \
"v_mov_b32 v9, 0x7ffffff0\n" \
"buffer_store_dword v9, v9, s[36:39], 0 offen\n" \
"buffer_store_dword v9, v9, s[36:39], 0 offen\n" \
"s_waitcnt vmcnt(15)\n" \
"s_nop 7\n" \
"s_nop 7\n" \
".p2align 6\n" \
".Lsg2_loop_%=:\n" \
"v_mfma_f32_16x16x32_bf16 v[96:99], a[0:3], v[16:19], 0\n" \
"ds_write_b128 v7, v[104:107] offset:2048\n" \
"v_xor_b32_e64 v7, 64, v7\n" \
"v_mfma_f32_16x16x32_bf16 v[100:103], a[128:131], v[16:19], 0\n" \
"ds_write_b128 v7, v[108:111] offset:3072\n" \
"ds_read_b128 v[92:95], v2 offset:3072\n" \
"v_xor_b32 v7, 192, v7\n" \
"v_mfma_f32_16x16x32_bf16 v[104:107], v[128:131], v[16:19], 0\n" \
"s_waitcnt vmcnt(14) lgkmcnt(7)\n" \
"v_mfma_f32_16x16x32_bf16 v[108:111], v[80:83], v[16:19], 0\n" \
"buffer_load_dwordx4 v[16:19], v3, s[36:39], s57 offen offset:2048 sc1\n" \
"v_mfma_f32_16x16x32_bf16 v[96:99], a[4:7], v[20:23], v[96:99]\n" \
"s_cmp_lt_i32 s44, s50\n" \
"s_cselect_b32 s82, s45, s47\n" \
"v_mfma_f32_16x16x32_bf16 v[100:103], a[132:135], v[20:23], v[100:103]\n" \
"ds_read_b128 v[80:83], v2 offset:4096\n" \
"v_mfma_f32_16x16x32_bf16 v[104:107], v[132:135], v[20:23], v[104:107]\n" \
"s_waitcnt vmcnt(14) lgkmcnt(7)\n" \
"v_mfma_f32_16x16x32_bf16 v[108:111], v[84:87], v[20:23], v[108:111]\n" \
"buffer_load_dwordx4 v[20:23], v3, s[36:39], s57 offen offset:3072 sc1\n" \
"v_mfma_f32_16x16x32_bf16 v[96:99], a[8:11], v[24:27], v[96:99]\n" \
"s_add_u32 s82, s82, s44\n" \
"s_cmp_lt_u32 s82, 128\n" \
"v_mfma_f32_16x16x32_bf16 v[100:103], a[136:139], v[24:27], v[100:103]\n" \
"ds_read_b128 v[84:87], v2 offset:5120\n" \
"s_cselect_b32 s82, s82, s94\n" \
"v_mfma_f32_16x16x32_bf16 v[104:107], v[136:139], v[24:27], v[104:107]\n" \
"s_waitcnt vmcnt(14) lgkmcnt(7)\n" \
"v_mfma_f32_16x16x32_bf16 v[108:111], v[88:91], v[24:27], v[108:111]\n" \
"s_xor_b32 s57, s56, s90\n" \
"buffer_load_dwordx4 v[24:27], v3, s[36:39], s57 offen offset:0 sc1\n" \
"v_mfma_f32_16x16x32_bf16 v[96:99], a[12:15], v[28:31], v[96:99]\n" \
"s_waitcnt lgkmcnt(3)\n" \
"s_cmp_lg_u32 s81, 0\n" \
"s_cbranch_scc1 .Lsg2_nb_%=\n" \
"s_barrier\n" \
".Lsg2_nb_%=:\n" \
"v_mfma_f32_16x16x32_bf16 v[100:103], a[140:143], v[28:31], v[100:103]\n" \
"ds_read_b128 v[88:91], v2 offset:6144\n" \
"v_xor_b32_e64 v7, s84, v7\n" \
"v_mfma_f32_16x16x32_bf16 v[104:107], v[140:143], v[28:31], v[104:107]\n" \
"s_waitcnt vmcnt(14) lgkmcnt(3)\n" \
"s_lshl_b32 s83, s82, 18\n" \
"v_mfma_f32_16x16x32_bf16 v[108:111], v[92:95], v[28:31], v[108:111]\n" \
"buffer_load_dwordx4 v[28:31], v3, s[36:39], s57 offen offset:1024 sc1\n" \
"v_mfma_f32_16x16x32_bf16 v[96:99], a[16:19], v[32:35], v[96:99]\n" \
"ds_read_b128 v[112:115], v8 offset:0\n" \
"s_lshr_b32 s58, s83, 8\n" \
"v_mfma_f32_16x16x32_bf16 v[100:103], a[144:147], v[32:35], v[100:103]\n" \
"ds_read_b128 v[92:95], v2 offset:7168\n" \
"v_mfma_f32_16x16x32_bf16 v[104:107], v[144:147], v[32:35], v[104:107]\n" \
"s_waitcnt vmcnt(14) lgkmcnt(4)\n" \
"s_and_b32 s58, s58, 0x2000\n" \
"v_mfma_f32_16x16x32_bf16 v[108:111], v[80:83], v[32:35], v[108:111]\n" \
"buffer_load_dwordx4 v[32:35], v3, s[36:39], s57 offen offset:2048 sc1\n" \
"v_mfma_f32_16x16x32_bf16 v[96:99], a[20:23], v[36:39], v[96:99]\n" \
"ds_read_b128 v[116:119], v8 offset:4096\n" \
"s_xor_b32 s83, s83, s58\n" \
"v_mfma_f32_16x16x32_bf16 v[100:103], a[148:151], v[36:39], v[100:103]\n" \
"ds_read_b128 v[80:83], v2 offset:8192\n" \
"v_mfma_f32_16x16x32_bf16 v[104:107], v[148:151], v[36:39], v[104:107]\n" \
"s_waitcnt vmcnt(14) lgkmcnt(5)\n" \
"v_mfma_f32_16x16x32_bf16 v[108:111], v[84:87], v[36:39], v[108:111]\n" \
"buffer_load_dwordx4 v[36:39], v3, s[36:39], s57 offen offset:3072 sc1\n" \
"v_mfma_f32_16x16x32_bf16 v[96:99], a[24:27], v[40:43], v[96:99]\n" \
"ds_read_b128 v[120:123], v8 offset:8192\n" \
"v_mfma_f32_16x16x32_bf16 v[100:103], a[152:155], v[40:43], v[100:103]\n" \
"ds_read_b128 v[84:87], v2 offset:9216\n" \
"v_mfma_f32_16x16x32_bf16 v[104:107], v[152:155], v[40:43], v[104:107]\n" \
"s_waitcnt vmcnt(14) lgkmcnt(6)\n" \
"v_mfma_f32_16x16x32_bf16 v[108:111], v[88:91], v[40:43], v[108:111]\n" \
"s_xor_b32 s57, s56, s91\n" \
"buffer_load_dwordx4 v[40:43], v3, s[36:39], s57 offen offset:0 sc1\n" \
"v_mfma_f32_16x16x32_bf16 v[96:99], a[28:31], v[44:47], v[96:99]\n" \
"ds_read_b128 v[124:127], v8 offset:12288\n" \
"v_mfma_f32_16x16x32_bf16 v[100:103], a[156:159], v[44:47], v[100:103]\n" \
"ds_read_b128 v[88:91], v2 offset:10240\n" \
"v_mfma_f32_16x16x32_bf16 v[104:107], v[156:159], v[44:47], v[104:107]\n" \
"s_waitcnt vmcnt(14) lgkmcnt(6)\n" \
"v_mfma_f32_16x16x32_bf16 v[108:111], v[92:95], v[44:47], v[108:111]\n" \
"buffer_load_dwordx4 v[44:47], v3, s[36:39], s57 offen offset:1024 sc1\n" \
"v_mfma_f32_16x16x32_bf16 v[96:99], a[32:35], v[48:51], v[96:99]\n" \
"s_lshr_b32 s59, s82, 1\n" \
"s_add_u32 s59, s59, 1\n" \
"v_mfma_f32_16x16x32_bf16 v[100:103], a[160:163], v[48:51], v[100:103]\n" \
"ds_read_b128 v[92:95], v2 offset:11264\n" \
"v_mfma_f32_16x16x32_bf16 v[104:107], v[160:163], v[48:51], v[104:107]\n" \
"s_waitcnt vmcnt(14) lgkmcnt(5)\n" \
"v_mfma_f32_16x16x32_bf16 v[108:111], v[80:83], v[48:51], v[108:111]\n" \
"buffer_load_dwordx4 v[48:51], v3, s[36:39], s57 offen offset:2048 sc1\n" \
"v_mfma_f32_16x16x32_bf16 v[96:99], a[36:39], v[52:55], v[96:99]\n" \
"v_add_f32_e64 v112, v112, v116\n" \
"v_mfma_f32_16x16x32_bf16 v[100:103], a[164:167], v[52:55], v[100:103]\n" \
"ds_read_b128 v[80:83], v2 offset:12288\n" \
"v_add_f32_e64 v113, v113, v117\n" \
"v_mfma_f32_16x16x32_bf16 v[104:107], v[164:167], v[52:55], v[104:107]\n" \
"s_waitcnt vmcnt(13) lgkmcnt(4)\n" \
"v_add_f32 v114, v114, v118\n" \
"v_mfma_f32_16x16x32_bf16 v[108:111], v[84:87], v[52:55], v[108:111]\n" \
"buffer_load_dwordx4 v[52:55], v3, s[36:39], s57 offen offset:3072 sc1\n" \
"v_add_f32_e64 v115, v115, v119\n" \
"v_mfma_f32_16x16x32_bf16 v[96:99], a[40:43], v[56:59], v[96:99]\n" \
"s_waitcnt lgkmcnt(3)\n" \
"v_add_f32 v120, v120, v124\n" \
"v_mfma_f32_16x16x32_bf16 v[100:103], a[168:171], v[56:59], v[100:103]\n" \
"ds_read_b128 v[84:87], v2 offset:13312\n" \
"v_add_f32_e64 v121, v121, v125\n" \
"v_mfma_f32_16x16x32_bf16 v[104:107], v[168:171], v[56:59], v[104:107]\n" \
"s_waitcnt vmcnt(12) lgkmcnt(3)\n" \
"v_add_f32 v122, v122, v126\n" \
"v_mfma_f32_16x16x32_bf16 v[108:111], v[88:91], v[56:59], v[108:111]\n" \
"s_xor_b32 s57, s56, s92\n" \
"buffer_load_dwordx4 v[56:59], v3, s[36:39], s57 offen offset:0 sc1\n" \
"v_mfma_f32_16x16x32_bf16 v[96:99], a[44:47], v[60:63], v[96:99]\n" \
"v_cmp_gt_u32_e64 s[62:63], s59, v11\n" \
"v_mfma_f32_16x16x32_bf16 v[100:103], a[172:175], v[60:63], v[100:103]\n" \
"ds_read_b128 v[88:91], v2 offset:14336\n" \
"v_add_f32 v123, v123, v127\n" \
"v_mfma_f32_16x16x32_bf16 v[104:107], v[172:175], v[60:63], v[104:107]\n" \
"s_waitcnt vmcnt(12) lgkmcnt(3)\n" \
"v_add_f32 v112, v112, v120\n" \
"v_mfma_f32_16x16x32_bf16 v[108:111], v[92:95], v[60:63], v[108:111]\n" \
"buffer_load_dwordx4 v[60:63], v3, s[36:39], s57 offen offset:1024 sc1\n" \
"v_add_f32_e64 v113, v113, v121\n" \
"v_mfma_f32_16x16x32_bf16 v[96:99], a[48:51], v[64:67], v[96:99]\n" \
"s_add_u32 s54, s55, 1\n" \
"s_cmp_lt_u32 s54, s79\n" \
"v_mfma_f32_16x16x32_bf16 v[100:103], a[176:179], v[64:67], v[100:103]\n" \
"ds_read_b128 v[92:95], v2 offset:15360\n" \
"s_cselect_b64 s[62:63], s[62:63], -1\n" \
"v_mfma_f32_16x16x32_bf16 v[104:107], v[176:179], v[64:67], v[104:107]\n" \
"s_waitcnt vmcnt(12) lgkmcnt(3)\n" \
"v_add_f32_e64 v114, v114, v122\n" \
"v_mfma_f32_16x16x32_bf16 v[108:111], v[80:83], v[64:67], v[108:111]\n" \
"buffer_load_dwordx4 v[64:67], v3, s[36:39], s57 offen offset:2048 sc1\n" \
"v_add_f32_e64 v115, v115, v123\n" \
"v_mfma_f32_16x16x32_bf16 v[96:99], a[52:55], v[68:71], v[96:99]\n" \
"s_cmp_lg_u64 s[62:63], 0\n" \
"s_cselect_b32 s81, 1, 0\n" \
"v_mfma_f32_16x16x32_bf16 v[100:103], a[180:183], v[68:71], v[100:103]\n" \
"ds_read_b128 v[80:83], v2 offset:16384\n" \
"s_cselect_b32 s83, 0x70000000, s83\n" \
"v_mfma_f32_16x16x32_bf16 v[104:107], v[180:183], v[68:71], v[104:107]\n" \
"s_waitcnt vmcnt(12) lgkmcnt(3)\n" \
"v_xor_b32 v8, s84, v8\n" \
"v_mfma_f32_16x16x32_bf16 v[108:111], v[84:87], v[68:71], v[108:111]\n" \
"buffer_load_dwordx4 v[68:71], v3, s[36:39], s57 offen offset:3072 sc1\n" \
"v_mul_f32 v116, 0xbfb8aa3b, v112\n" \
"v_mfma_f32_16x16x32_bf16 v[96:99], a[56:59], v[16:19], v[96:99]\n" \
"v_mul_f32 v117, 0xbfb8aa3b, v113\n" \
"v_mfma_f32_16x16x32_bf16 v[100:103], a[184:187], v[16:19], v[100:103]\n" \
"ds_read_b128 v[84:87], v2 offset:17408\n" \
"v_mul_f32 v118, 0xbfb8aa3b, v114\n" \
"v_mfma_f32_16x16x32_bf16 v[104:107], v[184:187], v[16:19], v[104:107]\n" \
"s_waitcnt vmcnt(12) lgkmcnt(3)\n" \
"s_mov_b32 s56, s83\n" \
"v_mfma_f32_16x16x32_bf16 v[108:111], v[88:91], v[16:19], v[108:111]\n" \
"s_xor_b32 s57, s56, s86\n" \
"buffer_load_dwordx4 v[16:19], v3, s[36:39], s57 offen offset:0 sc1\n" \
"v_mfma_f32_16x16x32_bf16 v[96:99], a[60:63], v[20:23], v[96:99]\n" \
"v_mul_f32 v119, 0xbfb8aa3b, v115\n" \
"v_mfma_f32_16x16x32_bf16 v[100:103], a[188:191], v[20:23], v[100:103]\n" \
"ds_read_b128 v[88:91], v2 offset:18432\n" \
"v_mfma_f32_16x16x32_bf16 v[104:107], v[188:191], v[20:23], v[104:107]\n" \
"s_waitcnt vmcnt(12) lgkmcnt(3)\n" \
"v_exp_f32_e64 v116, v116\n" \
"v_mfma_f32_16x16x32_bf16 v[108:111], v[92:95], v[20:23], v[108:111]\n" \
"buffer_load_dwordx4 v[20:23], v3, s[36:39], s57 offen offset:1024 sc1\n" \
"v_mfma_f32_16x16x32_bf16 v[96:99], a[64:67], v[24:27], v[96:99]\n" \
"v_exp_f32_e64 v117, v117\n" \
"v_mfma_f32_16x16x32_bf16 v[100:103], a[192:195], v[24:27], v[100:103]\n" \
"ds_read_b128 v[92:95], v2 offset:19456\n" \
"v_mfma_f32_16x16x32_bf16 v[104:107], v[192:195], v[24:27], v[104:107]\n" \
"s_waitcnt vmcnt(12) lgkmcnt(3)\n" \
"v_exp_f32 v118, v118\n" \
"v_mfma_f32_16x16x32_bf16 v[108:111], v[80:83], v[24:27], v[108:111]\n" \
"buffer_load_dwordx4 v[24:27], v3, s[36:39], s57 offen offset:2048 sc1\n" \
"v_mfma_f32_16x16x32_bf16 v[96:99], a[68:71], v[28:31], v[96:99]\n" \
"v_exp_f32_e64 v119, v119\n" \
"v_mfma_f32_16x16x32_bf16 v[100:103], a[196:199], v[28:31], v[100:103]\n" \
"ds_read_b128 v[80:83], v2 offset:20480\n" \
"v_add_f32_e64 v116, 1.0, v116\n" \
"v_mfma_f32_16x16x32_bf16 v[104:107], v[196:199], v[28:31], v[104:107]\n" \
"s_waitcnt vmcnt(12) lgkmcnt(3)\n" \
"v_add_f32 v117, 1.0, v117\n" \
"v_mfma_f32_16x16x32_bf16 v[108:111], v[84:87], v[28:31], v[108:111]\n" \
"buffer_load_dwordx4 v[28:31], v3, s[36:39], s57 offen offset:3072 sc1\n" \
"v_add_f32_e64 v118, 1.0, v118\n" \
"v_mfma_f32_16x16x32_bf16 v[96:99], a[72:75], v[32:35], v[96:99]\n" \
"v_add_f32_e64 v119, 1.0, v119\n" \
"v_mfma_f32_16x16x32_bf16 v[100:103], a[200:203], v[32:35], v[100:103]\n" \
"ds_read_b128 v[84:87], v2 offset:21504\n" \
"v_mfma_f32_16x16x32_bf16 v[104:107], v[200:203], v[32:35], v[104:107]\n" \
"s_waitcnt vmcnt(12) lgkmcnt(3)\n" \
"v_rcp_f32 v116, v116\n" \
"v_mfma_f32_16x16x32_bf16 v[108:111], v[88:91], v[32:35], v[108:111]\n" \
"s_xor_b32 s57, s56, s87\n" \
"buffer_load_dwordx4 v[32:35], v3, s[36:39], s57 offen offset:0 sc1\n" \
"v_mfma_f32_16x16x32_bf16 v[96:99], a[76:79], v[36:39], v[96:99]\n" \
"v_rcp_f32 v117, v117\n" \
"v_mfma_f32_16x16x32_bf16 v[100:103], a[204:207], v[36:39], v[100:103]\n" \
"ds_read_b128 v[88:91], v2 offset:22528\n" \
"v_mfma_f32_16x16x32_bf16 v[104:107], v[204:207], v[36:39], v[104:107]\n" \
"s_waitcnt vmcnt(12) lgkmcnt(3)\n" \
"v_rcp_f32 v118, v118\n" \
"v_mfma_f32_16x16x32_bf16 v[108:111], v[92:95], v[36:39], v[108:111]\n" \
"buffer_load_dwordx4 v[36:39], v3, s[36:39], s57 offen offset:1024 sc1\n" \
"v_mfma_f32_16x16x32_bf16 v[96:99], a[80:83], v[40:43], v[96:99]\n" \
"v_rcp_f32_e64 v119, v119\n" \
"v_mfma_f32_16x16x32_bf16 v[100:103], a[208:211], v[40:43], v[100:103]\n" \
"ds_read_b128 v[92:95], v2 offset:23552\n" \
"v_mul_f32_e64 v112, v112, v116\n" \
"v_mfma_f32_16x16x32_bf16 v[104:107], v[208:211], v[40:43], v[104:107]\n" \
"s_waitcnt vmcnt(12) lgkmcnt(3)\n" \
"v_mul_f32 v113, v113, v117\n" \
"v_mfma_f32_16x16x32_bf16 v[108:111], v[80:83], v[40:43], v[108:111]\n" \
"buffer_load_dwordx4 v[40:43], v3, s[36:39], s57 offen offset:2048 sc1\n" \
"v_mul_f32_e64 v114, v114, v118\n" \
"v_mfma_f32_16x16x32_bf16 v[96:99], a[84:87], v[44:47], v[96:99]\n" \
"s_lshl_b32 s80, s44, 16\n" \
"v_mul_f32 v115, v115, v119\n" \
"v_mfma_f32_16x16x32_bf16 v[100:103], a[212:215], v[44:47], v[100:103]\n" \
"ds_read_b128 v[80:83], v2 offset:24576\n" \
"v_cvt_pk_bf16_f32 v0, v112, v113\n" \
"v_mfma_f32_16x16x32_bf16 v[104:107], v[212:215], v[44:47], v[104:107]\n" \
"s_waitcnt vmcnt(12) lgkmcnt(3)\n" \
"s_add_u32 s80, s80, s78\n" \
"v_mfma_f32_16x16x32_bf16 v[108:111], v[84:87], v[44:47], v[108:111]\n" \
"buffer_load_dwordx4 v[44:47], v3, s[36:39], s57 offen offset:3072 sc1\n" \
"v_cvt_pk_bf16_f32 v1, v114, v115\n" \
"v_mfma_f32_16x16x32_bf16 v[96:99], a[88:91], v[48:51], v[96:99]\n" \
"s_lshr_b32 s58, s80, 8\n" \
"v_mfma_f32_16x16x32_bf16 v[100:103], a[216:219], v[48:51], v[100:103]\n" \
"ds_read_b128 v[84:87], v2 offset:25600\n" \
"v_mfma_f32_16x16x32_bf16 v[104:107], v[216:219], v[48:51], v[104:107]\n" \
"s_waitcnt vmcnt(12) lgkmcnt(3)\n" \
"s_and_b32 s58, s58, 0x2000\n" \
"v_mfma_f32_16x16x32_bf16 v[108:111], v[88:91], v[48:51], v[108:111]\n" \
"s_xor_b32 s57, s56, s88\n" \
"buffer_load_dwordx4 v[48:51], v3, s[36:39], s57 offen offset:0 sc1\n" \
"v_mfma_f32_16x16x32_bf16 v[96:99], a[92:95], v[52:55], v[96:99]\n" \
"s_xor_b32 s80, s80, s58\n" \
"v_mfma_f32_16x16x32_bf16 v[100:103], a[220:223], v[52:55], v[100:103]\n" \
"ds_read_b128 v[88:91], v2 offset:26624\n" \
"v_mfma_f32_16x16x32_bf16 v[104:107], v[220:223], v[52:55], v[104:107]\n" \
"s_waitcnt vmcnt(12) lgkmcnt(3)\n" \
"s_add_u32 s80, s80, 8388608\n" \
"v_mfma_f32_16x16x32_bf16 v[108:111], v[92:95], v[52:55], v[108:111]\n" \
"buffer_load_dwordx4 v[52:55], v3, s[36:39], s57 offen offset:1024 sc1\n" \
"v_mfma_f32_16x16x32_bf16 v[96:99], a[96:99], v[56:59], v[96:99]\n" \
"buffer_store_dwordx2 v[0:1], v6, s[36:39], s76 offen sc1\n" \
"v_mfma_f32_16x16x32_bf16 v[100:103], a[224:227], v[56:59], v[100:103]\n" \
"ds_read_b128 v[92:95], v2 offset:28672\n" \
"v_mfma_f32_16x16x32_bf16 v[104:107], v[224:227], v[56:59], v[104:107]\n" \
"s_waitcnt vmcnt(13) lgkmcnt(3)\n" \
"v_mfma_f32_16x16x32_bf16 v[108:111], v[80:83], v[56:59], v[108:111]\n" \
"buffer_load_dwordx4 v[56:59], v3, s[36:39], s57 offen offset:2048 sc1\n" \
"v_mfma_f32_16x16x32_bf16 v[96:99], a[100:103], v[60:63], v[96:99]\n" \
"buffer_load_dword v11, v4, s[36:39], 0 offen sc1\n" \
"v_mfma_f32_16x16x32_bf16 v[100:103], a[228:231], v[60:63], v[100:103]\n" \
"ds_read_b128 v[80:83], v2 offset:0\n" \
"v_mfma_f32_16x16x32_bf16 v[104:107], v[228:231], v[60:63], v[104:107]\n" \
"s_waitcnt vmcnt(14) lgkmcnt(3)\n" \
"v_mfma_f32_16x16x32_bf16 v[108:111], v[84:87], v[60:63], v[108:111]\n" \
"buffer_load_dwordx4 v[60:63], v3, s[36:39], s57 offen offset:3072 sc1\n" \
"v_mfma_f32_16x16x32_bf16 v[96:99], a[104:107], v[64:67], v[96:99]\n" \
"s_add_u32 s55, s55, 1\n" \
"s_mov_b32 s84, 0x4000\n" \
"v_mfma_f32_16x16x32_bf16 v[100:103], a[232:235], v[64:67], v[100:103]\n" \
"ds_read_b128 v[84:87], v2 offset:1024\n" \
"v_mfma_f32_16x16x32_bf16 v[104:107], v[232:235], v[64:67], v[104:107]\n" \
"s_waitcnt vmcnt(14) lgkmcnt(3)\n" \
"v_mfma_f32_16x16x32_bf16 v[108:111], v[88:91], v[64:67], v[108:111]\n" \
"s_xor_b32 s57, s56, s89\n" \
"buffer_load_dwordx4 v[64:67], v3, s[36:39], s57 offen offset:0 sc1\n" \
"v_mfma_f32_16x16x32_bf16 v[96:99], a[108:111], v[68:71], v[96:99]\n" \
"s_mov_b32 s76, s80\n" \
"v_mfma_f32_16x16x32_bf16 v[100:103], a[236:239], v[68:71], v[100:103]\n" \
"ds_read_b128 v[88:91], v2 offset:2048\n" \
"s_mov_b32 s44, s82\n" \
"v_mfma_f32_16x16x32_bf16 v[104:107], v[236:239], v[68:71], v[104:107]\n" \
"s_waitcnt vmcnt(14) lgkmcnt(3)\n" \
"ds_write_b128 v7, v[96:99] offset:0\n" \
"v_xor_b32 v7, 64, v7\n" \
"v_mfma_f32_16x16x32_bf16 v[108:111], v[92:95], v[68:71], v[108:111]\n" \
"buffer_load_dwordx4 v[68:71], v3, s[36:39], s57 offen offset:1024 sc1\n" \
"ds_write_b128 v7, v[100:103] offset:1024\n" \
"v_xor_b32 v7, 192, v7\n" \
"s_cmp_eq_u32 s81, 0\n" \
"s_cbranch_scc1 .Lsg2_loop_%=\n" \
"s_nop 7\n" \
"s_nop 7\n" \
"s_waitcnt vmcnt(0) lgkmcnt(0)\n" \
"ds_write_b128 v7, v[104:107] offset:2048\n" \
"v_xor_b32 v7, 64, v7\n" \
"ds_write_b128 v7, v[108:111] offset:3072\n" \
"v_xor_b32 v7, 64, v7\n" \
"s_waitcnt lgkmcnt(0)\n" \
"s_barrier\n" \
"ds_read_b128 v[112:115], v8 offset:0\n" \
"ds_read_b128 v[116:119], v8 offset:4096\n" \
"ds_read_b128 v[120:123], v8 offset:8192\n" \
"ds_read_b128 v[124:127], v8 offset:12288\n" \
"s_waitcnt lgkmcnt(2)\n" \
"v_add_f32 v112, v112, v116\n" \
"v_add_f32 v113, v113, v117\n" \
"v_add_f32 v114, v114, v118\n" \
"v_add_f32 v115, v115, v119\n" \
"s_waitcnt lgkmcnt(0)\n" \
"v_add_f32 v120, v120, v124\n" \
"v_add_f32 v121, v121, v125\n" \
"v_add_f32 v122, v122, v126\n" \
"v_add_f32 v123, v123, v127\n" \
"v_add_f32 v112, v112, v120\n" \
"v_add_f32 v113, v113, v121\n" \
"v_add_f32 v114, v114, v122\n" \
"v_add_f32 v115, v115, v123\n" \
"v_xor_b32 v8, s84, v8\n" \
"v_mul_f32 v116, 0xbfb8aa3b, v112\n" \
"v_mul_f32 v117, 0xbfb8aa3b, v113\n" \
"v_mul_f32 v118, 0xbfb8aa3b, v114\n" \
"v_mul_f32 v119, 0xbfb8aa3b, v115\n" \
"v_exp_f32 v116, v116\n" \
"v_exp_f32 v117, v117\n" \
"v_exp_f32 v118, v118\n" \
"v_exp_f32 v119, v119\n" \
"v_add_f32 v116, 1.0, v116\n" \
"v_add_f32 v117, 1.0, v117\n" \
"v_add_f32 v118, 1.0, v118\n" \
"v_add_f32 v119, 1.0, v119\n" \
"v_rcp_f32 v116, v116\n" \
"v_rcp_f32 v117, v117\n" \
"v_rcp_f32 v118, v118\n" \
"v_rcp_f32 v119, v119\n" \
"v_mul_f32 v112, v112, v116\n" \
"v_mul_f32 v113, v113, v117\n" \
"v_mul_f32 v114, v114, v118\n" \
"v_mul_f32 v115, v115, v119\n" \
"v_cvt_pk_bf16_f32 v0, v112, v113\n" \
"v_cvt_pk_bf16_f32 v1, v114, v115\n" \
"v_xor_b32 v7, 0x4000, v7\n" \
"buffer_store_dwordx2 v[0:1], v6, s[36:39], s76 offen sc1\n" \
"s_cmp_lg_u32 s55, s95\n" \
"s_cbranch_scc1 .Lsg2_g_%=\n" \
"s_waitcnt vmcnt(0) lgkmcnt(0)\n" \
"v_mov_b32 v9, 1\n" \
"s_mov_b64 exec, 1\n" \
"buffer_atomic_add v9, off, s[36:39], s49 offset:64\n" \
"s_mov_b64 exec, -1\n" \
"s_mov_b32 s79, s46\n" \
".Lsg2_g_%=:\n" \
"s_cmp_lt_u32 s55, s46\n" \
"s_cbranch_scc1 .Lsg2_prime_%=\n" \
"s_branch .Lsg2_o_%=\n" \
".Lsg2_ok_%=:\n" \
"s_mov_b32 s44, s94\n" \
".Lsg2_c_%=:\n" \
"s_nop 7\n" \
"s_nop 7\n" \
"s_lshl_b32 s54, s44, 16\n" \
"s_add_u32 s54, s54, s78\n" \
"s_lshr_b32 s58, s54, 8\n" \
"s_and_b32 s58, s58, 0x2000\n" \
"s_xor_b32 s54, s54, s58\n" \
"s_lshl_b32 s80, s44, 15\n" \
"s_cmp_lt_u32 s44, s96\n" \
"s_cselect_b32 s80, s80, 0x70000000\n" \
"s_cselect_b32 s54, s54, 0x70000000\n" \
"s_add_u32 s55, s54, 8388608\n" \
"s_add_u32 s44, s44, s47\n" \
"buffer_load_dwordx2 v[128:129], v6, s[36:39], s54 offen sc1\n" \
"buffer_load_dwordx2 v[130:131], v6, s[36:39], s55 offen sc1\n" \
"buffer_load_dwordx2 v[132:133], v5, s[64:67], s80 offen\n" \
"s_lshl_b32 s54, s44, 16\n" \
"s_add_u32 s54, s54, s78\n" \
"s_lshr_b32 s58, s54, 8\n" \
"s_and_b32 s58, s58, 0x2000\n" \
"s_xor_b32 s54, s54, s58\n" \
"s_lshl_b32 s81, s44, 15\n" \
"s_cmp_lt_u32 s44, s96\n" \
"s_cselect_b32 s81, s81, 0x70000000\n" \
"s_cselect_b32 s54, s54, 0x70000000\n" \
"s_add_u32 s55, s54, 8388608\n" \
"s_add_u32 s44, s44, s47\n" \
"buffer_load_dwordx2 v[134:135], v6, s[36:39], s54 offen sc1\n" \
"buffer_load_dwordx2 v[136:137], v6, s[36:39], s55 offen sc1\n" \
"buffer_load_dwordx2 v[138:139], v5, s[64:67], s81 offen\n" \
"s_lshl_b32 s54, s44, 16\n" \
"s_add_u32 s54, s54, s78\n" \
"s_lshr_b32 s58, s54, 8\n" \
"s_and_b32 s58, s58, 0x2000\n" \
"s_xor_b32 s54, s54, s58\n" \
"s_lshl_b32 s82, s44, 15\n" \
"s_cmp_lt_u32 s44, s96\n" \
"s_cselect_b32 s82, s82, 0x70000000\n" \
"s_cselect_b32 s54, s54, 0x70000000\n" \
"s_add_u32 s55, s54, 8388608\n" \
"s_add_u32 s44, s44, s47\n" \
"buffer_load_dwordx2 v[140:141], v6, s[36:39], s54 offen sc1\n" \
"buffer_load_dwordx2 v[142:143], v6, s[36:39], s55 offen sc1\n" \
"buffer_load_dwordx2 v[144:145], v5, s[64:67], s82 offen\n" \
"s_lshl_b32 s54, s44, 16\n" \
"s_add_u32 s54, s54, s78\n" \
"s_lshr_b32 s58, s54, 8\n" \
"s_and_b32 s58, s58, 0x2000\n" \
"s_xor_b32 s54, s54, s58\n" \
"s_lshl_b32 s83, s44, 15\n" \
"s_cmp_lt_u32 s44, s96\n" \
"s_cselect_b32 s83, s83, 0x70000000\n" \
"s_cselect_b32 s54, s54, 0x70000000\n" \
"s_add_u32 s55, s54, 8388608\n" \
"s_add_u32 s44, s44, s47\n" \
"buffer_load_dwordx2 v[146:147], v6, s[36:39], s54 offen sc1\n" \
"buffer_load_dwordx2 v[148:149], v6, s[36:39], s55 offen sc1\n" \
"buffer_load_dwordx2 v[150:151], v5, s[64:67], s83 offen\n" \
"s_lshl_b32 s54, s44, 16\n" \
"s_add_u32 s54, s54, s78\n" \
"s_lshr_b32 s58, s54, 8\n" \
"s_and_b32 s58, s58, 0x2000\n" \
"s_xor_b32 s54, s54, s58\n" \
"s_lshl_b32 s84, s44, 15\n" \
"s_cmp_lt_u32 s44, s96\n" \
"s_cselect_b32 s84, s84, 0x70000000\n" \
"s_cselect_b32 s54, s54, 0x70000000\n" \
"s_add_u32 s55, s54, 8388608\n" \
"s_add_u32 s44, s44, s47\n" \
"buffer_load_dwordx2 v[152:153], v6, s[36:39], s54 offen sc1\n" \
"buffer_load_dwordx2 v[154:155], v6, s[36:39], s55 offen sc1\n" \
"buffer_load_dwordx2 v[156:157], v5, s[64:67], s84 offen\n" \
"s_lshl_b32 s54, s44, 16\n" \
"s_add_u32 s54, s54, s78\n" \
"s_lshr_b32 s58, s54, 8\n" \
"s_and_b32 s58, s58, 0x2000\n" \
"s_xor_b32 s54, s54, s58\n" \
"s_lshl_b32 s85, s44, 15\n" \
"s_cmp_lt_u32 s44, s96\n" \
"s_cselect_b32 s85, s85, 0x70000000\n" \
"s_cselect_b32 s54, s54, 0x70000000\n" \
"s_add_u32 s55, s54, 8388608\n" \
"s_add_u32 s44, s44, s47\n" \
"buffer_load_dwordx2 v[158:159], v6, s[36:39], s54 offen sc1\n" \
"buffer_load_dwordx2 v[160:161], v6, s[36:39], s55 offen sc1\n" \
"buffer_load_dwordx2 v[162:163], v5, s[64:67], s85 offen\n" \
"s_lshl_b32 s54, s44, 16\n" \
"s_add_u32 s54, s54, s78\n" \
"s_lshr_b32 s58, s54, 8\n" \
"s_and_b32 s58, s58, 0x2000\n" \
"s_xor_b32 s54, s54, s58\n" \
"s_lshl_b32 s86, s44, 15\n" \
"s_cmp_lt_u32 s44, s96\n" \
"s_cselect_b32 s86, s86, 0x70000000\n" \
"s_cselect_b32 s54, s54, 0x70000000\n" \
"s_add_u32 s55, s54, 8388608\n" \
"s_add_u32 s44, s44, s47\n" \
"buffer_load_dwordx2 v[164:165], v6, s[36:39], s54 offen sc1\n" \
"buffer_load_dwordx2 v[166:167], v6, s[36:39], s55 offen sc1\n" \
"buffer_load_dwordx2 v[168:169], v5, s[64:67], s86 offen\n" \
"s_lshl_b32 s54, s44, 16\n" \
"s_add_u32 s54, s54, s78\n" \
"s_lshr_b32 s58, s54, 8\n" \
"s_and_b32 s58, s58, 0x2000\n" \
"s_xor_b32 s54, s54, s58\n" \
"s_lshl_b32 s87, s44, 15\n" \
"s_cmp_lt_u32 s44, s96\n" \
"s_cselect_b32 s87, s87, 0x70000000\n" \
"s_cselect_b32 s54, s54, 0x70000000\n" \
"s_add_u32 s55, s54, 8388608\n" \
"s_add_u32 s44, s44, s47\n" \
"buffer_load_dwordx2 v[170:171], v6, s[36:39], s54 offen sc1\n" \
"buffer_load_dwordx2 v[172:173], v6, s[36:39], s55 offen sc1\n" \
"buffer_load_dwordx2 v[174:175], v5, s[64:67], s87 offen\n" \
"s_lshl_b32 s54, s44, 16\n" \
"s_add_u32 s54, s54, s78\n" \
"s_lshr_b32 s58, s54, 8\n" \
"s_and_b32 s58, s58, 0x2000\n" \
"s_xor_b32 s54, s54, s58\n" \
"s_lshl_b32 s88, s44, 15\n" \
"s_cmp_lt_u32 s44, s96\n" \
"s_cselect_b32 s88, s88, 0x70000000\n" \
"s_cselect_b32 s54, s54, 0x70000000\n" \
"s_add_u32 s55, s54, 8388608\n" \
"s_add_u32 s44, s44, s47\n" \
"buffer_load_dwordx2 v[176:177], v6, s[36:39], s54 offen sc1\n" \
"buffer_load_dwordx2 v[178:179], v6, s[36:39], s55 offen sc1\n" \
"buffer_load_dwordx2 v[180:181], v5, s[64:67], s88 offen\n" \
"s_lshl_b32 s54, s44, 16\n" \
"s_add_u32 s54, s54, s78\n" \
"s_lshr_b32 s58, s54, 8\n" \
"s_and_b32 s58, s58, 0x2000\n" \
"s_xor_b32 s54, s54, s58\n" \
"s_lshl_b32 s89, s44, 15\n" \
"s_cmp_lt_u32 s44, s96\n" \
"s_cselect_b32 s89, s89, 0x70000000\n" \
"s_cselect_b32 s54, s54, 0x70000000\n" \
"s_add_u32 s55, s54, 8388608\n" \
"s_add_u32 s44, s44, s47\n" \
"buffer_load_dwordx2 v[182:183], v6, s[36:39], s54 offen sc1\n" \
"buffer_load_dwordx2 v[184:185], v6, s[36:39], s55 offen sc1\n" \
"buffer_load_dwordx2 v[186:187], v5, s[64:67], s89 offen\n" \
"s_waitcnt vmcnt(29)\n" \
"v_lshlrev_b32 v112, 16, v128\n" \
"v_and_b32 v113, 0xffff0000, v128\n" \
"v_lshlrev_b32 v114, 16, v129\n" \
"v_and_b32 v115, 0xffff0000, v129\n" \
"s_waitcnt vmcnt(28)\n" \
"v_lshlrev_b32 v116, 16, v130\n" \
"v_and_b32 v117, 0xffff0000, v130\n" \
"v_lshlrev_b32 v118, 16, v131\n" \
"v_and_b32 v119, 0xffff0000, v131\n" \
"s_waitcnt vmcnt(27)\n" \
"v_lshlrev_b32 v120, 16, v132\n" \
"v_and_b32 v121, 0xffff0000, v132\n" \
"v_lshlrev_b32 v122, 16, v133\n" \
"v_and_b32 v123, 0xffff0000, v133\n" \
"v_fmac_f32 v120, v112, v116\n" \
"v_fmac_f32 v121, v113, v117\n" \
"v_fmac_f32 v122, v114, v118\n" \
"v_fmac_f32 v123, v115, v119\n" \
"v_cvt_pk_bf16_f32 v132, v120, v121\n" \
"v_cvt_pk_bf16_f32 v133, v122, v123\n" \
"buffer_store_dwordx2 v[132:133], v5, s[68:71], s80 offen\n" \
"s_waitcnt vmcnt(27)\n" \
"v_lshlrev_b32 v112, 16, v134\n" \
"v_and_b32 v113, 0xffff0000, v134\n" \
"v_lshlrev_b32 v114, 16, v135\n" \
"v_and_b32 v115, 0xffff0000, v135\n" \
"s_waitcnt vmcnt(26)\n" \
"v_lshlrev_b32 v116, 16, v136\n" \
"v_and_b32 v117, 0xffff0000, v136\n" \
"v_lshlrev_b32 v118, 16, v137\n" \
"v_and_b32 v119, 0xffff0000, v137\n" \
"s_waitcnt vmcnt(25)\n" \
"v_lshlrev_b32 v120, 16, v138\n" \
"v_and_b32 v121, 0xffff0000, v138\n" \
"v_lshlrev_b32 v122, 16, v139\n" \
"v_and_b32 v123, 0xffff0000, v139\n" \
"v_fmac_f32 v120, v112, v116\n" \
"v_fmac_f32 v121, v113, v117\n" \
"v_fmac_f32 v122, v114, v118\n" \
"v_fmac_f32 v123, v115, v119\n" \
"v_cvt_pk_bf16_f32 v138, v120, v121\n" \
"v_cvt_pk_bf16_f32 v139, v122, v123\n" \
"buffer_store_dwordx2 v[138:139], v5, s[68:71], s81 offen\n" \
"s_waitcnt vmcnt(25)\n" \
"v_lshlrev_b32 v112, 16, v140\n" \
"v_and_b32 v113, 0xffff0000, v140\n" \
"v_lshlrev_b32 v114, 16, v141\n" \
"v_and_b32 v115, 0xffff0000, v141\n" \
"s_waitcnt vmcnt(24)\n" \
"v_lshlrev_b32 v116, 16, v142\n" \
"v_and_b32 v117, 0xffff0000, v142\n" \
"v_lshlrev_b32 v118, 16, v143\n" \
"v_and_b32 v119, 0xffff0000, v143\n" \
"s_waitcnt vmcnt(23)\n" \
"v_lshlrev_b32 v120, 16, v144\n" \
"v_and_b32 v121, 0xffff0000, v144\n" \
"v_lshlrev_b32 v122, 16, v145\n" \
"v_and_b32 v123, 0xffff0000, v145\n" \
"v_fmac_f32 v120, v112, v116\n" \
"v_fmac_f32 v121, v113, v117\n" \
"v_fmac_f32 v122, v114, v118\n" \
"v_fmac_f32 v123, v115, v119\n" \
"v_cvt_pk_bf16_f32 v144, v120, v121\n" \
"v_cvt_pk_bf16_f32 v145, v122, v123\n" \
"buffer_store_dwordx2 v[144:145], v5, s[68:71], s82 offen\n" \
"s_waitcnt vmcnt(23)\n" \
"v_lshlrev_b32 v112, 16, v146\n" \
"v_and_b32 v113, 0xffff0000, v146\n" \
"v_lshlrev_b32 v114, 16, v147\n" \
"v_and_b32 v115, 0xffff0000, v147\n" \
"s_waitcnt vmcnt(22)\n" \
"v_lshlrev_b32 v116, 16, v148\n" \
"v_and_b32 v117, 0xffff0000, v148\n" \
"v_lshlrev_b32 v118, 16, v149\n" \
"v_and_b32 v119, 0xffff0000, v149\n" \
"s_waitcnt vmcnt(21)\n" \
"v_lshlrev_b32 v120, 16, v150\n" \
"v_and_b32 v121, 0xffff0000, v150\n" \
"v_lshlrev_b32 v122, 16, v151\n" \
"v_and_b32 v123, 0xffff0000, v151\n" \
"v_fmac_f32 v120, v112, v116\n" \
"v_fmac_f32 v121, v113, v117\n" \
"v_fmac_f32 v122, v114, v118\n" \
"v_fmac_f32 v123, v115, v119\n" \
"v_cvt_pk_bf16_f32 v150, v120, v121\n" \
"v_cvt_pk_bf16_f32 v151, v122, v123\n" \
"buffer_store_dwordx2 v[150:151], v5, s[68:71], s83 offen\n" \
"s_waitcnt vmcnt(21)\n" \
"v_lshlrev_b32 v112, 16, v152\n" \
"v_and_b32 v113, 0xffff0000, v152\n" \
"v_lshlrev_b32 v114, 16, v153\n" \
"v_and_b32 v115, 0xffff0000, v153\n" \
"s_waitcnt vmcnt(20)\n" \
"v_lshlrev_b32 v116, 16, v154\n" \
"v_and_b32 v117, 0xffff0000, v154\n" \
"v_lshlrev_b32 v118, 16, v155\n" \
"v_and_b32 v119, 0xffff0000, v155\n" \
"s_waitcnt vmcnt(19)\n" \
"v_lshlrev_b32 v120, 16, v156\n" \
"v_and_b32 v121, 0xffff0000, v156\n" \
"v_lshlrev_b32 v122, 16, v157\n" \
"v_and_b32 v123, 0xffff0000, v157\n" \
"v_fmac_f32 v120, v112, v116\n" \
"v_fmac_f32 v121, v113, v117\n" \
"v_fmac_f32 v122, v114, v118\n" \
"v_fmac_f32 v123, v115, v119\n" \
"v_cvt_pk_bf16_f32 v156, v120, v121\n" \
"v_cvt_pk_bf16_f32 v157, v122, v123\n" \
"buffer_store_dwordx2 v[156:157], v5, s[68:71], s84 offen\n" \
"s_waitcnt vmcnt(19)\n" \
"v_lshlrev_b32 v112, 16, v158\n" \
"v_and_b32 v113, 0xffff0000, v158\n" \
"v_lshlrev_b32 v114, 16, v159\n" \
"v_and_b32 v115, 0xffff0000, v159\n" \
"s_waitcnt vmcnt(18)\n" \
"v_lshlrev_b32 v116, 16, v160\n" \
"v_and_b32 v117, 0xffff0000, v160\n" \
"v_lshlrev_b32 v118, 16, v161\n" \
"v_and_b32 v119, 0xffff0000, v161\n" \
"s_waitcnt vmcnt(17)\n" \
"v_lshlrev_b32 v120, 16, v162\n" \
"v_and_b32 v121, 0xffff0000, v162\n" \
"v_lshlrev_b32 v122, 16, v163\n" \
"v_and_b32 v123, 0xffff0000, v163\n" \
"v_fmac_f32 v120, v112, v116\n" \
"v_fmac_f32 v121, v113, v117\n" \
"v_fmac_f32 v122, v114, v118\n" \
"v_fmac_f32 v123, v115, v119\n" \
"v_cvt_pk_bf16_f32 v162, v120, v121\n" \
"v_cvt_pk_bf16_f32 v163, v122, v123\n" \
"buffer_store_dwordx2 v[162:163], v5, s[68:71], s85 offen\n" \
"s_waitcnt vmcnt(17)\n" \
"v_lshlrev_b32 v112, 16, v164\n" \
"v_and_b32 v113, 0xffff0000, v164\n" \
"v_lshlrev_b32 v114, 16, v165\n" \
"v_and_b32 v115, 0xffff0000, v165\n" \
"s_waitcnt vmcnt(16)\n" \
"v_lshlrev_b32 v116, 16, v166\n" \
"v_and_b32 v117, 0xffff0000, v166\n" \
"v_lshlrev_b32 v118, 16, v167\n" \
"v_and_b32 v119, 0xffff0000, v167\n" \
"s_waitcnt vmcnt(15)\n" \
"v_lshlrev_b32 v120, 16, v168\n" \
"v_and_b32 v121, 0xffff0000, v168\n" \
"v_lshlrev_b32 v122, 16, v169\n" \
"v_and_b32 v123, 0xffff0000, v169\n" \
"v_fmac_f32 v120, v112, v116\n" \
"v_fmac_f32 v121, v113, v117\n" \
"v_fmac_f32 v122, v114, v118\n" \
"v_fmac_f32 v123, v115, v119\n" \
"v_cvt_pk_bf16_f32 v168, v120, v121\n" \
"v_cvt_pk_bf16_f32 v169, v122, v123\n" \
"buffer_store_dwordx2 v[168:169], v5, s[68:71], s86 offen\n" \
"s_waitcnt vmcnt(15)\n" \
"v_lshlrev_b32 v112, 16, v170\n" \
"v_and_b32 v113, 0xffff0000, v170\n" \
"v_lshlrev_b32 v114, 16, v171\n" \
"v_and_b32 v115, 0xffff0000, v171\n" \
"s_waitcnt vmcnt(14)\n" \
"v_lshlrev_b32 v116, 16, v172\n" \
"v_and_b32 v117, 0xffff0000, v172\n" \
"v_lshlrev_b32 v118, 16, v173\n" \
"v_and_b32 v119, 0xffff0000, v173\n" \
"s_waitcnt vmcnt(13)\n" \
"v_lshlrev_b32 v120, 16, v174\n" \
"v_and_b32 v121, 0xffff0000, v174\n" \
"v_lshlrev_b32 v122, 16, v175\n" \
"v_and_b32 v123, 0xffff0000, v175\n" \
"v_fmac_f32 v120, v112, v116\n" \
"v_fmac_f32 v121, v113, v117\n" \
"v_fmac_f32 v122, v114, v118\n" \
"v_fmac_f32 v123, v115, v119\n" \
"v_cvt_pk_bf16_f32 v174, v120, v121\n" \
"v_cvt_pk_bf16_f32 v175, v122, v123\n" \
"buffer_store_dwordx2 v[174:175], v5, s[68:71], s87 offen\n" \
"s_waitcnt vmcnt(13)\n" \
"v_lshlrev_b32 v112, 16, v176\n" \
"v_and_b32 v113, 0xffff0000, v176\n" \
"v_lshlrev_b32 v114, 16, v177\n" \
"v_and_b32 v115, 0xffff0000, v177\n" \
"s_waitcnt vmcnt(12)\n" \
"v_lshlrev_b32 v116, 16, v178\n" \
"v_and_b32 v117, 0xffff0000, v178\n" \
"v_lshlrev_b32 v118, 16, v179\n" \
"v_and_b32 v119, 0xffff0000, v179\n" \
"s_waitcnt vmcnt(11)\n" \
"v_lshlrev_b32 v120, 16, v180\n" \
"v_and_b32 v121, 0xffff0000, v180\n" \
"v_lshlrev_b32 v122, 16, v181\n" \
"v_and_b32 v123, 0xffff0000, v181\n" \
"v_fmac_f32 v120, v112, v116\n" \
"v_fmac_f32 v121, v113, v117\n" \
"v_fmac_f32 v122, v114, v118\n" \
"v_fmac_f32 v123, v115, v119\n" \
"v_cvt_pk_bf16_f32 v180, v120, v121\n" \
"v_cvt_pk_bf16_f32 v181, v122, v123\n" \
"buffer_store_dwordx2 v[180:181], v5, s[68:71], s88 offen\n" \
"s_waitcnt vmcnt(11)\n" \
"v_lshlrev_b32 v112, 16, v182\n" \
"v_and_b32 v113, 0xffff0000, v182\n" \
"v_lshlrev_b32 v114, 16, v183\n" \
"v_and_b32 v115, 0xffff0000, v183\n" \
"s_waitcnt vmcnt(10)\n" \
"v_lshlrev_b32 v116, 16, v184\n" \
"v_and_b32 v117, 0xffff0000, v184\n" \
"v_lshlrev_b32 v118, 16, v185\n" \
"v_and_b32 v119, 0xffff0000, v185\n" \
"s_waitcnt vmcnt(9)\n" \
"v_lshlrev_b32 v120, 16, v186\n" \
"v_and_b32 v121, 0xffff0000, v186\n" \
"v_lshlrev_b32 v122, 16, v187\n" \
"v_and_b32 v123, 0xffff0000, v187\n" \
"v_fmac_f32 v120, v112, v116\n" \
"v_fmac_f32 v121, v113, v117\n" \
"v_fmac_f32 v122, v114, v118\n" \
"v_fmac_f32 v123, v115, v119\n" \
"v_cvt_pk_bf16_f32 v186, v120, v121\n" \
"v_cvt_pk_bf16_f32 v187, v122, v123\n" \
"buffer_store_dwordx2 v[186:187], v5, s[68:71], s89 offen\n" \
"s_cmp_lt_u32 s44, s96\n" \
"s_cbranch_scc1 .Lsg2_c_%=\n" \
"s_branch .Lsg2_end_%=\n" \
".Lsg2_w_%=:\n" \
"s_nop 7\n" \
"s_nop 7\n" \
"s_waitcnt vmcnt(0) lgkmcnt(0)\n" \
"s_mov_b32 s61, 0\n" \
".Lsg2_wa_%=:\n" \
"s_cmp_lg_u32 s60, 0\n" \
"s_cbranch_scc1 .Lsg2_wk_%=\n" \
"buffer_load_dword v11, v4, s[36:39], 0 offen sc1\n" \
"s_waitcnt vmcnt(0)\n" \
"v_cmp_gt_u32_e64 s[62:63], s59, v11\n" \
"s_nop 4\n" \
"s_cmp_eq_u64 s[62:63], 0\n" \
"s_cbranch_scc1 .Lsg2_wk_%=\n" \
"s_sleep 1\n" \
"s_add_u32 s61, s61, 1\n" \
"s_cmp_lt_u32 s61, 65536\n" \
"s_cbranch_scc1 .Lsg2_wa_%=\n" \
"s_mov_b32 s60, 1\n" \
"v_mov_b32 v11, 256\n" \
"v_mov_b32 v9, 1\n" \
"buffer_store_dword v9, v11, s[36:39], 0 offen sc1\n" \
"s_waitcnt vmcnt(0) lgkmcnt(0)\n" \
"s_branch .Lsg2_wk_%=\n" \
".Lsg2_o_%=:\n" \
"s_nop 7\n" \
"s_nop 7\n" \
"s_waitcnt vmcnt(0) lgkmcnt(0)\n" \
"s_mov_b32 s61, 0\n" \
".Lsg2_oa_%=:\n" \
"s_cmp_lg_u32 s60, 0\n" \
"s_cbranch_scc1 .Lsg2_ok_%=\n" \
"buffer_load_dword v9, off, s[36:39], s49 offset:0 sc1\n" \
"s_sub_u32 s54, 10, s47\n" \
"s_waitcnt vmcnt(0)\n" \
"v_cmp_gt_u32 vcc, s54, v9\n" \
"s_nop 4\n" \
"s_cbranch_vccz .Lsg2_ok_%=\n" \
"s_sleep 1\n" \
"s_add_u32 s61, s61, 1\n" \
"s_cmp_lt_u32 s61, 65536\n" \
"s_cbranch_scc1 .Lsg2_oa_%=\n" \
"s_mov_b32 s60, 1\n" \
"v_mov_b32 v11, 256\n" \
"v_mov_b32 v9, 1\n" \
"buffer_store_dword v9, v11, s[36:39], 0 offen sc1\n" \
"s_waitcnt vmcnt(0) lgkmcnt(0)\n" \
"s_branch .Lsg2_ok_%=\n" \
".Lsg2_end_%=:\n" \
"s_endpgm\n"
#define TUNEV(i, c) (c)
#define C_MO 6
#define C_MM 6
#define FARP (TUNEV(11, 1) << 13)
#define C_X 104
#define C_Y 78
#define C_DS 36
#define C_VD 12
#define C_YL 9
#define C_D0 50
#define C_Y0 0
#define C_GD 265
#define C_PW3 8
#define C_TK 0
__device__ __forceinline__ int tkbase(const Params& p, int h, int fp) { const int m = TUNEV(8, C_TK); return WS_TK + h * (4 * MB + 65536) + (fp ^ (m == 2 ? (h & 8) << 10 : (m ^ 1) << 13)); }

typedef u32 v32u __attribute__((ext_vector_type(32)));
typedef float f32x2 __attribute__((ext_vector_type(2)));
__device__ __forceinline__ float xsum(float s) {
    auto a = __builtin_amdgcn_permlane16_swap(__builtin_bit_cast(u32, s), __builtin_bit_cast(u32, s), false, false);
    s = __builtin_bit_cast(float, (u32)a[0]) + __builtin_bit_cast(float, (u32)a[1]);
    auto b = __builtin_amdgcn_permlane32_swap(__builtin_bit_cast(u32, s), __builtin_bit_cast(u32, s), false, false);
    return __builtin_bit_cast(float, (u32)b[0]) + __builtin_bit_cast(float, (u32)b[1]);
}
__device__ __forceinline__ void ln_tile(const Params& p, const v4u (&xv)[8], int rt, int w, int lane, int fp) {
    const int n = lane & 15, g = lane >> 4;
    const rsrc_t rs = __builtin_amdgcn_make_buffer_rsrc(p.ws, 0, WS_END, 0x00020000);
    LAS float* red = (LAS float*)(uintptr_t)(136 * 1024);
    f32x2 xf[32], s2 = {0.f, 0.f};
#pragma unroll
    for (int i = 0; i < 32; ++i) { xf[i] = f32x2{bflo(xv[i >> 2][i & 3]), bfhi(xv[i >> 2][i & 3])}; s2 += xf[i]; }
    const float s = xsum(s2.x + s2.y);
    if (g == 0) red[n * 4 + w] = s;
    BAR();
    f32x4 t = *(LAS f32x4*)(red + n * 4);
    const float mean = ((t.x + t.y) + (t.z + t.w)) * (1.0f / DM);
    f32x2 q2 = {0.f, 0.f};
#pragma unroll
    for (int i = 0; i < 32; ++i) { const f32x2 a = xf[i] - mean; q2 += a * a; }
    const float q = xsum(q2.x + q2.y);
    if (g == 0) red[64 + n * 4 + w] = q;
    BAR();
    t = *(LAS f32x4*)(red + 64 + n * 4);
    const float var = ((t.x + t.y) + (t.z + t.w)) * (1.0f / DM);
    const float mb = rbf(mean);
    const float rd = 1.0f / rbf(sqrtf(rbf(rbf(var) + 1e-5f)));
#pragma unroll
    for (int i = 0; i < 8; ++i) {
        v4u o;
#pragma unroll
        for (int e = 0; e < 4; ++e) { const f32x2 a = xf[4 * i + e] - mb; const u32 d = pack2(a.x, a.y); const f32x2 y = f32x2{bflo(d), bfhi(d)} * rd; o[e] = pack2(y.x, y.y); }
        st128s(o, rs, farx(WS_XF + rt * 65536 + w * 16384 + fp) + i * 1024 + lane * 16);
    }
    WAIT_V0();
    __builtin_amdgcn_s_barrier(); CFENCE();
    if (w == 0 && lane == 0) __hip_atomic_fetch_add((GAS u32*)(p.ws + WS_CT) + CT_LN + (rt >> 4) * 32, 1u, RLXA);
}
__device__ __forceinline__ void ln_role(const Params& p, int c, int w, int fp) {
    const int lane = threadIdx.x & 63;
    const u16* xr = p.x + (size_t)(c * 16 + (lane & 15)) * DM + w * 256 + (lane >> 4) * 8;
    v4u xa[8], xb[8];
#pragma unroll
    for (int i = 0; i < 8; ++i) { xa[i] = *(const v4u*)(xr + i * 32); xb[i] = *(const v4u*)(xr + 128 * DM + i * 32); }
#pragma unroll 1
    for (int k = 0; k < 16; ++k) {
        v4u xc[8];
#pragma unroll
        for (int i = 0; i < 8; ++i) xc[i] = *(const v4u*)(xr + (size_t)min(k + 2, 15) * 128 * DM + i * 32);
        ln_tile(p, xa, k * 8 + c, w, lane, fp);
#pragma unroll
        for (int i = 0; i < 8; ++i) { xa[i] = xb[i]; xb[i] = xc[i]; }
    }
}

__device__ __forceinline__ void p1_role(const Params& p, int b, int s0, int s1, int id, int w, int fp) {
    const int lane = __builtin_amdgcn_mbcnt_hi(~0u, __builtin_amdgcn_mbcnt_lo(~0u, 0u)), n = lane & 15, g = lane >> 4;
    const bool g1 = b >= 96;
    const int h = b & 31, kind = b >> 5;
    const int stride = g1 ? FF * 2 : 8192;
    const u16* src = g1 ? colsrc2(p, w >> 1, b - 96) : colsrc(p, kind == 0 ? 1 : (kind == 1 ? 0 : 2), 2 * h + (w >> 1));
    const v4u rs = mksrd(p.ws, WS_END), rw = mksrd(src, 1023 * stride + 128);
    const int v0 = lane * 16;
    const int v1 = (8 * (w & 1) + (lane >> 3)) * stride + ((lane & 7) ^ ((((lane >> 4) & 1) | ((w & 1) << 1)) << 1)) * 16;
    const int swz = ((n >> 3) | ((g & 1) << 1)), rowo = (8 * g + (n >> 2)) * 128 + (n & 3) * 8;
    const int v2 = (g1 ? 0 : (w >> 1) * 8192) + rowo + (((g1 ? w : 2 * (w & 1)) ^ swz) * 32);
    const int v3 = (g1 ? 8192 : (w >> 1) * 8192) + rowo + (((g1 ? w : 2 * (w & 1) + 1) ^ swz) * 32);
    const int v4 = (w >> 1) * 65536 + (w & 1) * 16384 + lane * 16, ks = (b - 96) * 2 + (w >> 1);
    int v5, ostr, od;
    if (g1) { v5 = (g & 1) * 262144 + (ks >> 3) * 16384 + (ks & 7) * 1024 + (2 * (w & 1) + (g >> 1)) * 256 + n * 16; ostr = 19; od = 0; }
    else if (kind == 2) { v5 = 32768 + w * 2048 + lane * 16; ostr = 16; od = 1024; }
    else { v5 = kind * 16384 + w * 1024 + lane * 16; ostr = 16; od = 4096; }
    const int v6 = (w == 0 && lane == 0) ? WS_CT + (g1 ? CT_G1 + (b - 96) * 32 : CT_P1 + h * 32 + kind * 4) * 4 : OOB;
#define P1_IN "{v0}"(v0), "{v1}"(v1), "{v2}"(v2), "{v3}"(v3), "{v4}"(v4), "{s[36:39]}"(rs), "{s[40:43]}"(rw), "{s44}"(16 * stride), "{s46}"(s0), "{s47}"(s1), "{s48}"(w), "{s49}"(ostr), "{s50}"(od), "{s45}"(WS_XF + fp), "{s64}"(g1 ? WS_HF + fp : tkbase(p, h, fp)), "{s51}"(rfl(kind == 1 ? 0x3e0293ee : 0x3f800000))
    int a5 = v5, a6 = v6; u32 tc, tr;
    if (g1) asm volatile(ASM_P1_G1 : "+{v5}"(a5), "+{v6}"(a6), "={s70}"(tc), "={s72}"(tr) : P1_IN : CLOB_P1);
    else if (kind == 2) asm volatile(ASM_P1_V : "+{v5}"(a5), "+{v6}"(a6), "={s70}"(tc), "={s72}"(tr) : P1_IN : CLOB_P1);
    else asm volatile(ASM_P1_QK : "+{v5}"(a5), "+{v6}"(a6), "={s70}"(tc), "={s72}"(tr) : P1_IN : CLOB_P1);
}

__device__ __forceinline__ int mlane(int n, int g) { return (g >> 1) * 512 + (n >> 3) * 256 + (g & 1) * 128 + (n & 7) * 16; }
__device__ __forceinline__ int mq(int j) { return ((j & 1) ? 0x4800 : 0) ^ ((j & 2) ? 0x800 : 0); }
__device__ __forceinline__ int hword(int i, int cls) { return WS_M + (cls ^ 0x1000 ^ ((i & 32) ? 0x4800 : 0)) + ((i >> 4) & 1) * 32768 + (i & 15) * 128; }
__device__ __forceinline__ int oblk(const Params& p, int rg, int q, int cls) { return rg * 32768 + q * 8192; }
__device__ __forceinline__ int mpiece(int l, int cls) { return WS_M + (cls ^ mq(l >> 4)) + ((l >> 3) & 1) * 32768 + (l & 7) * 256; }
__device__ __forceinline__ void qk_role(const Params& p, int h, int par, int rot) {
    const int lane = __builtin_amdgcn_mbcnt_hi(~0u, __builtin_amdgcn_mbcnt_lo(~0u, 0u)), w = rfl(threadIdx.x >> 6), n = lane & 15, g = lane >> 4;
    const v4u rs = mksrd(p.ws, WS_END), rw = mksrd(V2 ? colsrc(p, 0, 2 * h + (w >> 1)) : colsrc(p, w >> 1, 2 * h + (w & 1)), 1023 * 8192 + 128);
    const int l16 = lane * 16, v3 = l16 + (V2 ? 0 : 112 * 1024), lex = V2 ? 40960 : 152 * 1024;
    int v1 = l16 + w * 4096, v2 = v1 + 65536;
    const int v4 = WS_M + (w >> 1) * 32768 + (w & 1) * 1024 + mlane(n, g);
    const int v5 = lex + (32 * w + n) * 32 + (g ^ (n >> 2)) * 8;
    const int v6 = lex + (4 * g + (n >> 2)) * 32 + ((n & 3) ^ g) * 8;
    const int v15 = w == 0 ? min(l16, 64) : OOB;
    const int v7 = (n & 2) == 0 ? WS_O + amap(oblk(p, n >> 3, h >> 3, par) + (h & 7) * 1024 + w * 256 + (g >> 1) * 128 + ((n >> 2) & 1) * 64 + (g & 1) * 32 + (n & 1) * 16, par) : OOB;
    const int v8 = WS_V + amap(h * 2048 + w * 512 + lane * 4, par);
    const int v9 = (w == 0 && lane < 32) ? hword(lane, par) : OOB;
    const int v10 = (w == 0 && lane == 0) ? 0 : OOB;
    const int v11 = 49152 + (w & 1) * 4096 + w * 1024 + l16;
    const int v12 = (n & 2) ? 0xEEEE : 0x4444;
    const int v14 = 16 * (w & 1) + n - 4 * g;
    const int r8 = lane >> 3;
    int v32 = r8 * 8192 + ((lane & 7) ^ (((r8 >> 1) & 1) << 1)) * 16, v33 = (8 + r8) * 8192 + ((lane & 7) ^ ((((r8 >> 1) & 1) | 2) << 1)) * 16;
    const int swz = (n >> 3) | ((g & 1) << 1), rowo = (w >> 1) * 4096 + (8 * g + (n >> 2)) * 128 + (n & 3) * 8;
    const int v34 = rowo + ((2 * (w & 1)) ^ swz) * 32, v35 = rowo + ((2 * (w & 1) + 1) ^ swz) * 32;
    if (V2) { v1 = (w & 1) ? v33 : v32; v2 = v34 + (w >> 1) * 4096; v32 = v35 + (w >> 1) * 4096; }
    asm volatile(ASM_QK :: "{v0}"(mpiece(lane, par)), "{v1}"(v1), "{v[2:3]}"(v2u{(u32)v2, (u32)v3}), "{v[4:7]}"(v4u{(u32)v4, (u32)v5, (u32)v6, (u32)v7}), "{v8}"(v8), "{v9}"(v9), "{v10}"(v10), "{v11}"(v11), "{v12}"(v12),
        "{v14}"(v14), "{v15}"(v15), "{v[32:35]}"(v4u{(u32)v32, (u32)v33, (u32)v34, (u32)v35}), "{s[36:39]}"(rs), "{s[40:43]}"(rw), "{s[44:45]}"(v2u{(u32)TUNEV(0, C_MO), (u32)TUNEV(1, C_MM)}), "{s46}"(TUNEV(2, C_X)), "{s17}"(TUNEV(3, C_Y)), "{s[12:15]}"(v4u{(u32)TUNEV(7, C_D0), (u32)C_Y0, (u32)TUNEV(4, C_DS), (u32)rot << 21}), "{s10}"(par ^ mq(rot)), "{s47}"(WS_DBG + 131072 + (h * 4 + w) * 2048), "{s48}"(w),
        "{s63}"(tkbase(p, h, (par & 8192) ^ FARP)), "{s80}"(WS_CT + (CT_P1 + h * 32) * 4), "{s87}"(WS_CT + (CT_AT + h * 32) * 4), "{s94}"(hword(h, par)) : "v255", "a255", "s99", "memory");
    __builtin_unreachable();
}
__device__ __forceinline__ void vo_role(const Params& p, int c, int par) {
    const int lane = __builtin_amdgcn_mbcnt_hi(~0u, __builtin_amdgcn_mbcnt_lo(~0u, 0u)), w = rfl(threadIdx.x >> 6), n = lane & 15, g = lane >> 4;
    const int rg = RGX ? c >> 5 : (c >> 4) & 1, cs = RGX ? c & 31 : (c & 15) + 16 * (c >> 5);
    const v4u rs = mksrd(p.ws, WS_END), rw = mksrd(colsrc(p, V2 ? 1 : 2, c) + (size_t)(256 * w) * 4096, 255 * 8192 + 128), rv = mksrd(colsrc(p, 2, c) + (size_t)(256 * w) * 4096, 255 * 8192 + 128), ro = mksrd(colsrc(p, 3, cs >> 1) + (size_t)(1024 * w) * 1024 + (cs & 1) * 32, 1023 * 2048 + 64), rm = mksrd(p.mem0, 16384);
    const int v0 = lane * 16, v1 = (lane >> 3) * 8192 + (lane & 7) * 16, v2 = (lane >> 2) * 2048 + (lane & 3) * 16;
    const int v3 = (8 * g + (n >> 2)) * 128 + w * 32 + (n & 3) * 8;
    const int v4 = w * (V2 ? 32768 : 40960) + (16 * (g & 1) + 8 * (g >> 1) + 4 * (n >> 3) + ((n >> 2) & 1)) * 64 + (n & 3) * 8;
    const int v5 = WS_M + (w >> 1) * 32768 + (w & 1) * 1024 + mlane(n, g);
    const int v6 = WS_O + amap(oblk(p, rg, w, par) + (2 * (n >> 3) + (g >> 1)) * 256 + (g & 1) * 128 + (n & 7) * 16, par);
    const int v7 = WS_V + amap(c * 1024 + w * 256 + lane * 4, par);
    const int v8 = (w == 0 && (lane & 3) == 0) ? WS_M + (par ^ mq(cs >> 3)) + ((cs >> 2) & 1) * 32768 + ((cs & 3) * 2 + rg) * 256 + lane * 4 : OOB;
    const int v9 = (w == 0 && lane < 32) ? hword(lane, par) : OOB;
    const int v12 = (n & 2) ? 0xEEEE : 0x4444;
    const int v13 = (4 * rg + 2 * ((lane >> 4) & 1) + ((lane >> 2) & 1)) * 2048 + (32 * cs + 16 * (lane >> 5) + 2 * ((lane >> 3) & 1)) * 2;
    const int v15 = (V2 ? 131072 : 0) + 16384 + (g * 16 + ((n >> 1) & 1) * 8 + (n >> 2)) * 32 + w * 8 + (n & 1) * 4;
    asm volatile(ASM_VO :: "{v10}"(mpiece(lane, par)), "{v0}"(v0), "{v1}"(v1), "{v2}"(v2), "{v3}"(v3), "{v4}"(v4), "{v5}"(v5), "{v6}"(v6), "{v7}"(v7), "{v8}"(v8), "{v9}"(v9), "{v12}"(v12),
        "{v13}"(v13), "{v15}"(v15), "{v28}"((w == 0 && lane == 0) ? WS_CT + (CT_P1 + (c >> 1) * 32 + 12 + (c & 1) * 4) * 4 : OOB), "{s[36:39]}"(rs), "{s[40:43]}"(rw), "{s[64:67]}"(ro), "{s[68:71]}"(rm), "{s[92:95]}"(rv), "{s[44:45]}"(v2u{(u32)TUNEV(0, C_MO), (u32)TUNEV(1, C_MM)}), "{s46}"(TUNEV(12, C_VD)), "{s17}"(TUNEV(3, C_Y)),
        "{s63}"(par), "{s74}"(cs >= 16 ? TUNEV(15, C_YL) : 0), "{s47}"(WS_DBG + 131072 + ((32 + c) * 4 + w) * 2048), "{s48}"(w) : "v255", "a255", "s99", "memory");
    __builtin_unreachable();
}
__device__ __forceinline__ void copy_wd(const Params& p, int u, int fp) {
    const int lane = threadIdx.x & 63;
    const rsrc_t rs = __builtin_amdgcn_make_buffer_rsrc(p.ws + WS_WD, 0, 16 * MB, 0x00020000);
#pragma unroll 1
    for (int k = 0; k < 14; ++k) {
        const int row = (k * 32 + u) * 8;
        v4u t[16];
#pragma unroll
        for (int i = 0; i < 16; ++i) t[i] = __builtin_nontemporal_load((const v4u*)(p.wd + (size_t)row * 1024 + i * 512 + lane * 8));
#pragma unroll
        for (int i = 0; i < 16; ++i) st128s(t[i], rs, farx(row * 4096 + fp) + (i >> 3) * 16384 + ((i >> 1) & 3) * 2048 + ((((i & 1) * 8 + (lane >> 3) + (i >> 1)) & 15) * 128) + (lane & 7) * 16);
    }
    WAIT_V0();
    if (lane == 0) __hip_atomic_fetch_add((GAS u32*)(p.ws + WS_CT) + CT_WD, 1u, RLXA);
}
__device__ __forceinline__ void s64_role(const Params& p, int kind, int cb, int r, int id, int w, int fp) {
    const int lane = __builtin_amdgcn_mbcnt_hi(~0u, __builtin_amdgcn_mbcnt_lo(~0u, 0u)), n = lane & 15, g = lane >> 4;

    if (kind) {
        GAS u32* ct = (GAS u32*)(p.ws + WS_CT);
        for (u32 spins = 0; (u32)rfl(__hip_atomic_load(ct + CT_WD, RLXA)) < 32u; ++spins) {
            if (spins > (1u << 18)) { __hip_atomic_store(ct + CT_TMO, 1u, RLXA); break; }
            __builtin_amdgcn_s_sleep(8);
        }
        CFENCE();
    }
    const v4u rs = mksrd(p.ws, WS_END), rw = mksrd(kind ? p.ws + WS_WD : (const u8*)(colsrc(p, 3, cb) + (size_t)(1024 * w) * 1024), kind ? 16 * MB : 1023 * 2048 + 128), rx = mksrd(p.x, 2048 * 2048), ro = mksrd(p.out, 2048 * 2048);
    const int v2 = lane * 16 + w * 32768, v0 = (kind ? (lane >> 5) * 16384 + ((lane >> 3) & 3) * 2048 + ((cb + (lane >> 3)) & 15) * 128 : (lane >> 3) * 2048) + (lane & 7) * 16;
    const int v1 = w * 32768 + ((kind ? 8 : 4) * g + (n >> 2)) * 128 + (n & 3) * 8;
    const int v3 = (kind ? 0 : 49152) + lane * 16;
    const int v4 = WS_CT + (kind ? CT_G1 + (14 * w + min(n, 13)) * 32 : CT_AT + (8 * w + (n & 7)) * 32) * 4;
    const int v5 = (4 * w + g) * 2048 + cb * 128 + n * 8, v6 = (cb >> 2) * 16384 + (cb & 3) * 2048 + w * 512 + lane * 8;
    const int v7 = 131072 + w * 4096 + g * 256 + (n ^ g ^ 8) * 16, v8 = 131072 + n * 256 + ((4 * w + g) ^ n) * 16;
    const int ha = TUNEV(6, S64_HEAD), r0 = TUNEV(10, S64_R0), nr = kind ? 10 - NWO : NWO, b0 = kind ? r : ha + r, hd = (!kind && r < 2) ? ha / 2 : 0;
    const int lo = (r0 - b0 + nr - 1) / nr, nt = hd + (128 - b0 + nr - 1) / nr;
    const int rt = kind ? r + nr * lo : (r < 2 ? r : b0), n1 = kind ? nt - lo : hd + lo;
#define S64_IO "{s97}"(896 * 4096 * w + fp), "{s78}"(WS_GT + fp), "{v7}"(v7), "{v8}"(v8), "{s44}"(rt), "{v0}"(v0), "{v1}"(v1), "{v2}"(v2), "{v3}"(v3), "{v4}"(v4), "{v5}"(v5), "{v6}"(v6), "{s[36:39]}"(rs), "{s[40:43]}"(rw), "{s[64:67]}"(rx), "{s[68:71]}"(ro), \
        "{s45}"(2), "{s47}"(nr), "{s50}"(kind ? -128 : ha), "{s46}"(nt), "{s48}"(w), "{s49}"(WS_CT + CT_S * 4 + (cb * 4 + w) * 128), "{s94}"(r), "{s95}"(n1), "{s96}"(r0), "{s51}"(w ? OOB : WS_DBG + id * 128), "{s99}"(kind == 0 ? TUNEV(9, C_PW3) : 0), "{s85}"(kind ? WS_HF + fp : tkbase(p, 8 * w, fp))
    if (kind) asm volatile(ASM_S64_G2 :: S64_IO : "v255", "a255", "memory");
    else asm volatile(ASM_S64_WO :: S64_IO : "v255", "a255", "memory");
    __builtin_unreachable();
}
#define MAIN_BODY \
int gx = (id >> 5) - 4, gr = id & 31; \
    if (id < 128) { \
        const int x = id >> 5, t = id & 31; \
        const int nq = x < 2 ? NQ0 : 32 - 2 * NQ0; \
        if (x < 3) { if (t < nq) qk_role(p, NQ0 * x + t, par, x & TUNEV(14, 1)); else vo_role(p, (32 - NQ0) * x + t - nq, par); } \
        gx = -1; \
        id = 96 + gr; \
        STAMP(0); \
    } \
    const int fp = (par & 8192) ^ FARP, u = gx * 30 + gr; \
    const int blk = gx < 0 ? 64 + gr : (PMAP ? (u < 64 ? u : u + 32) : (gr < 16 ? gx * 8 + (gr & 7) + (gr >> 3) * 32 : 96 + gx * 14 + gr - 16)); \
    if (gx < 0 || gr < 30) { \
        for (int i = blk >= 96 ? (TUNEV(5, C_GD) & 255) - ((blk & 7) == slowpos(p.wg) ? 6 : 0) : TUNEV(5, C_GD) >> 8; i > 0; --i) __builtin_amdgcn_s_sleep(32);     \
        p1_role(p, blk, 0, 64, id, wv_, fp); \
    } else { ln_role(p, gx * 2 + gr - 30, wv_, fp); copy_wd(p, (gx * 2 + gr - 30) * 4 + wv_, fp); } \
    STAMP(2); \
    { const int rep = 2 * gx + 2 + (gr >> 4); s64_role(p, rep < NWO ? 0 : 1, gr & 15, rep < NWO ? rep : rep - NWO, id, wv_, fp); }
constexpr int LDS_BYTES = 160 * 1024;
__device__ __forceinline__ int take_role(const Params& p, LAS u32* L) {
    if (threadIdx.x == 0) {
        GAS u32* tk = (GAS u32*)(p.ws + WS_CT) + CT_TICK;
        const u32 x = xcc_id() & 7u;
        const u32 t = __hip_atomic_fetch_add(tk + 64 * x, 1u, RLXA);
        u32 id = 32 * x + t;
        if (t >= 32u) {
            u32 o = __hip_atomic_fetch_add(tk + 64 * 8, 1u, RLXA);
            for (u32 spins = 0; spins < (1u << 20); ++spins) {
                u32 tot = 0;
                for (int k = 0; k < 8; ++k) tot += __hip_atomic_load(tk + 64 * k, RLXA);
                if (tot >= 256u) break;
                __builtin_amdgcn_s_sleep(8);
            }
            id = 255u;
            for (int k = 0; k < 8; ++k) {
                const u32 ck = __hip_atomic_load(tk + 64 * k, RLXA);
                const u32 fr = ck < 32u ? 32u - ck : 0u;
                if (o < fr) { id = 32 * k + ck + o; break; }
                o -= fr;
            }
        }
        *L = id;
    }
    BAR();
    const int id = rfl(*L);
    BAR();
    return id;
}
#define STAMP(i) do {} while (0)
__global__ void __launch_bounds__(256, 1) k_main(Params p) {
    extern __shared__ __attribute__((aligned(16))) u8 smem[];
    int par = rfl(*(const GAS u32*)(p.ws + WS_CT + CT_PAR * 4)) & 1;
    par = (par << 13) | ((TUNEV(13, 0) & 3) << 11) | ((TUNEV(13, 0) & 4) << 12);
    int id = take_role(p, (LAS u32*)smem);
    const int wv_ = rfl(threadIdx.x >> 6);
    STAMP(0);
    MAIN_BODY
    STAMP(7);
}
__global__ void __launch_bounds__(256) k_zero(Params p) {
    __shared__ u32 vote[4];
    u8* ws = p.ws;
    v4u z = {0u, 0u, 0u, 0u};
    if (WARM && blockIdx.x != 0) {
        const int i = (blockIdx.x - 1) * 32 + (threadIdx.x >> 3), m2 = min(i & 7, 2);
        const rsrc_t rc = __builtin_amdgcn_make_buffer_rsrc(ws + WS_WC, 0, 16 * MB, 0x00020000);
        v4u t[7];
#pragma unroll
        for (int m = 0; m < 4; ++m) t[m] = __builtin_nontemporal_load((const v4u*)(wmat(p, m) + (size_t)i * 512 + slowpos(wmat(p, m)) * 64 + (threadIdx.x & 7) * 8));
        if ((i & 7) < 3) t[4] = __builtin_nontemporal_load((const v4u*)(wmat(p, m2) + (size_t)(i >> 3) * 4096 + slowpos2(wmat(p, m2)) * 64 + (threadIdx.x & 7) * 8));
        if (WARM > 1 && i < 7168) {
            t[5] = __builtin_nontemporal_load((const v4u*)(p.wg + (size_t)i * 512 + slowpos(p.wg) * 64 + (threadIdx.x & 7) * 8));
            t[6] = __builtin_nontemporal_load((const v4u*)(p.wu + (size_t)i * 512 + slowpos(p.wu) * 64 + (threadIdx.x & 7) * 8));
        }
#pragma unroll
        for (int m = 0; m < 4; ++m) __builtin_amdgcn_raw_buffer_store_b128(t[m], rc, i * 1024 + (m == 3 ? 4 : m) * 128 + (threadIdx.x & 7) * 16, 0, ZAUX);
        if ((i & 7) < 3) __builtin_amdgcn_raw_buffer_store_b128(t[4], rc, (i >> 3) * 8192 + (5 + m2) * 128 + (threadIdx.x & 7) * 16, 0, ZAUX);
        if (WARM > 1 && i < 7168) {
            __builtin_amdgcn_raw_buffer_store_b128(t[5], rc, WS_WG - WS_WC + i * 1024 + (threadIdx.x & 7) * 16, 0, ZAUX);
            __builtin_amdgcn_raw_buffer_store_b128(t[6], rc, WS_WG - WS_WC + i * 1024 + 128 + (threadIdx.x & 7) * 16, 0, ZAUX);
        }
    }
    if (blockIdx.x == 0) {
        const rsrc_t rs = __builtin_amdgcn_make_buffer_rsrc(ws, 0, 2 * MB, 0x00020000);
        const int lane = threadIdx.x & 63, w = threadIdx.x >> 6, base = 2 * MB - 32768 + (w & 1) * 16384 + (w >> 1) * 2048 + (lane & 31) * 4;
        st32s(0u, rs, base + 1024); st32s(0u, rs, base + 8192 + 1024);
        u32 a = 65535u, b = 65535u;
#pragma unroll 1
        for (int k = 0; k < ZR; ++k) {
            u64 t0, t1, t2;
            asm volatile("s_waitcnt vmcnt(0)\n\ts_memtime %0\n\ts_waitcnt lgkmcnt(0)" : "=s"(t0) :: "memory");
            st32s(0u, rs, base);
            asm volatile("s_waitcnt vmcnt(0)\n\ts_memtime %0\n\ts_waitcnt lgkmcnt(0)" : "=s"(t1) :: "memory");
            st32s(0u, rs, base + 8192);
            asm volatile("s_waitcnt vmcnt(0)\n\ts_memtime %0\n\ts_waitcnt lgkmcnt(0)" : "=s"(t2) :: "memory");
            a = min(a, (u32)(t1 - t0)); b = min(b, (u32)(t2 - t1));
            if (4 * max(a, b) > 5 * min(a, b) && min(a, b) < 600u && max(a, b) < 1000u) break;
        }
        vote[w] = (a << 16) | b;
        __syncthreads();
        if (threadIdx.x == 0) {
            for (int k = 0; k < 4; ++k) { a = min(a, vote[k] >> 16); b = min(b, vote[k] & 65535u); }
            z.x = (b < a ? 1u : 0u) ^ (xcc_id() >= 4 ? 1u : 0u);
        }
    }
    if (blockIdx.x < 80) ((v4u*)(ws + (blockIdx.x < 16 ? 0 : WS_M - 65536)))[blockIdx.x * 256 + threadIdx.x] = z;
}

extern "C" void kernel_launch(void* const* d_in, const int* in_sizes, int n_in, void* d_out, int out_size, void* d_ws, size_t ws_size, hipStream_t stream) {
    Params p;
    p.x = (const u16*)d_in[0]; p.mem0 = (const u16*)d_in[1]; p.wq = (const u16*)d_in[2]; p.wk = (const u16*)d_in[3]; p.wv = (const u16*)d_in[4]; p.wo = (const u16*)d_in[5];
    p.wg = (const u16*)d_in[8]; p.wu = (const u16*)d_in[9]; p.wd = (const u16*)d_in[10];
    p.out = (u16*)d_out;
    p.ws = (u8*)(((uintptr_t)d_ws + (2u * MB - 1)) & ~(uintptr_t)(2u * MB - 1));
    (void)hipFuncSetAttribute((const void*)k_main, hipFuncAttributeMaxDynamicSharedMemorySize, LDS_BYTES);
    hipLaunchKernelGGL(k_zero, dim3(257), dim3(256), 0, stream, p);
    hipLaunchKernelGGL(k_main, dim3(256), dim3(256), LDS_BYTES, stream, p);
}
```

```cpp
#define V2 0
#pragma clang diagnostic ignored "-Wint-to-pointer-cast"
#pragma clang diagnostic ignored "-Winline-asm"
#include <hip/hip_runtime.h>
#include <hip/hip_bf16.h>
#include <stdint.h>

#define LAS __attribute__((address_space(3)))
#define GAS __attribute__((address_space(1)))
typedef unsigned u32;
typedef unsigned char u8;
typedef unsigned short u16;
typedef unsigned long long u64;
typedef u32 v2u __attribute__((ext_vector_type(2)));
typedef u32 v4u __attribute__((ext_vector_type(4)));
typedef float f32x4 __attribute__((ext_vector_type(4)));
typedef __bf16 bf16x2 __attribute__((ext_vector_type(2)));
typedef __amdgpu_buffer_rsrc_t rsrc_t;

constexpr int NSEG = 64, DM = 1024, NH = 32, FF = 3584;
constexpr int MB = 1 << 20;
constexpr int WS_CT = 0;
constexpr int WS_M = 262144;
constexpr int WS_O = 524288, WS_V = 1048576;
constexpr int WS_DBG = 4 * MB;
constexpr int WS_XF = 8 * MB;
constexpr int WS_HF = 32 * MB;
constexpr int WS_GT = 16 * MB;
constexpr int WS_TK = 64 * MB;
constexpr int WS_END = 192 * MB;
__device__ __forceinline__ int farx(int off) { return off ^ ((off >> 8) & 0x2000); }
constexpr int CT_PAR = 0, CT_TMO = 64, CT_TICK = 256;
constexpr int CT_LN = 1024;
constexpr int CT_WD = 1536;
constexpr int CT_P1 = 2048;
constexpr int CT_G1 = 4096;
constexpr int CT_AT = 3072;
constexpr int CT_S = 8192;
#define NQ0 16
#define RGX 0
#define SCRAMBLE 0
#define ZAUX 16
#define ZR 8
#define WARM 1
#define NWO 4
#define PMAP 1
#define S64_HEAD (18 + 10 * 256)
#define S64_R0 116
constexpr int OOB = 0x7ffffff0;
constexpr int AUX_SC1 = 16;

struct Params {
    const u16 *x, *mem0, *wq, *wk, *wv, *wo, *wg, *wu, *wd;
    u16* out;
    u8* ws;
};
__device__ __forceinline__ u32 pack2(float a, float b) { bf16x2 v = {(__bf16)a, (__bf16)b}; return __builtin_bit_cast(u32, v); }
__device__ __forceinline__ float bflo(u32 d) { return __builtin_bit_cast(float, d << 16); }
__device__ __forceinline__ float bfhi(u32 d) { return __builtin_bit_cast(float, d & 0xFFFF0000u); }
__device__ __forceinline__ float rbf(float a) { return (float)(__bf16)a; }
__device__ __forceinline__ void st128s(v4u v, rsrc_t rs, int voff) { __builtin_amdgcn_raw_buffer_store_b128(v, rs, voff, 0, AUX_SC1); }
__device__ __forceinline__ void st32s(u32 v, rsrc_t rs, int voff) { __builtin_amdgcn_raw_buffer_store_b32(v, rs, voff, 0, AUX_SC1); }
__device__ __forceinline__ u32 ld32s(rsrc_t rs, int voff) { return __builtin_amdgcn_raw_buffer_load_b32(rs, voff, 0, AUX_SC1); }
__device__ __forceinline__ u32 xcc_id() { return (u32)__builtin_amdgcn_s_getreg(20 | (3 << 11)) & 0xfu; }
__device__ __forceinline__ int rfl(int x) { return __builtin_amdgcn_readfirstlane(x); }
__device__ __forceinline__ v4u mksrd(const void* p, u32 bytes) {
    const u64 a = (u64)p;
    return v4u{(u32)rfl((u32)a), (u32)rfl((u32)(a >> 32) & 0xFFFFu), (u32)rfl(bytes), 0x00020000u};
}
#define CFENCE() asm volatile("" ::: "memory")
#define WAIT_V0() asm volatile("s_waitcnt vmcnt(0)" ::: "memory")
#define BAR() do { asm volatile("s_waitcnt lgkmcnt(0)" ::: "memory"); __builtin_amdgcn_s_barrier(); asm volatile("" ::: "memory"); } while (0)
#define RLXA __ATOMIC_RELAXED, __HIP_MEMORY_SCOPE_AGENT
__device__ __forceinline__ int amap(int off, int cls) { return ((off >> 13) << 14) + (off & 8191) + (cls & 8192); }

constexpr int WS_WC = 208 * MB;
constexpr int WS_WG = 216 * MB;
constexpr int WS_WD = 192 * MB;
__device__ __forceinline__ const u16* wmat(const Params& p, int m) { return m == 0 ? p.wq : (m == 1 ? p.wk : (m == 2 ? p.wv : p.wo)); }
__device__ __forceinline__ int slowpos(const void* b) { return (3 - (int)((u64)b >> 7)) & 7; }
__device__ __forceinline__ int slowpos2(const void* b) { return (63 - (int)((u64)b >> 7)) & 63; }
__device__ __forceinline__ const u16* colsrc(const Params& p, int m, int j) {
    const u16* b = wmat(p, m);
    if (WARM && m < 3 && j == slowpos2(b)) return (const u16*)(p.ws + WS_WC) + (5 + m) * 64;
    return (WARM && (j & 7) == slowpos(b)) ? (const u16*)(p.ws + WS_WC) + (j >> 3) * 512 + (m == 3 ? 4 : m) * 64 : b + j * 64;
}
__device__ __forceinline__ const u16* colsrc2(const Params& p, int m, int j) {
    const u16* b = m ? p.wu : p.wg;
    return (WARM > 1 && (j & 7) == slowpos(b)) ? (const u16*)(p.ws + WS_WG) + (j >> 3) * 512 + m * 64 : b + j * 64;
}
#define ASM_P1_QK \
"s_nop 7\n" \
"s_nop 7\n" \
"s_lshr_b32 s52, s48, 1\n" \
"s_lshl_b32 s52, s52, 13\n" \
"s_and_b32 s53, s48, 1\n" \
"s_lshl_b32 s53, s53, 10\n" \
"s_add_u32 s52, s52, s53\n" \
"s_mov_b32 s54, 0\n" \
"s_add_u32 m0, s52, 0\n" \
"s_nop 0\n" \
"buffer_load_dwordx4 v1, s[40:43], s54 offen nt lds\n" \
"s_add_u32 s54, s54, s44\n" \
"s_add_u32 m0, s52, 2048\n" \
"s_nop 0\n" \
"buffer_load_dwordx4 v1, s[40:43], s54 offen nt lds\n" \
"s_add_u32 s54, s54, s44\n" \
"s_add_u32 m0, s52, 4096\n" \
"s_nop 0\n" \
"buffer_load_dwordx4 v1, s[40:43], s54 offen nt lds\n" \
"s_add_u32 s54, s54, s44\n" \
"s_add_u32 m0, s52, 6144\n" \
"s_nop 0\n" \
"buffer_load_dwordx4 v1, s[40:43], s54 offen nt lds\n" \
"s_add_u32 s54, s54, s44\n" \
"s_add_u32 m0, s52, 16384\n" \
"s_nop 0\n" \
"buffer_load_dwordx4 v1, s[40:43], s54 offen nt lds\n" \
"s_add_u32 s54, s54, s44\n" \
"s_add_u32 m0, s52, 18432\n" \
"s_nop 0\n" \
"buffer_load_dwordx4 v1, s[40:43], s54 offen nt lds\n" \
"s_add_u32 s54, s54, s44\n" \
"s_add_u32 m0, s52, 20480\n" \
"s_nop 0\n" \
"buffer_load_dwordx4 v1, s[40:43], s54 offen nt lds\n" \
"s_add_u32 s54, s54, s44\n" \
"s_add_u32 m0, s52, 22528\n" \
"s_nop 0\n" \
"buffer_load_dwordx4 v1, s[40:43], s54 offen nt lds\n" \
"s_add_u32 s54, s54, s44\n" \
"s_add_u32 m0, s52, 32768\n" \
"s_nop 0\n" \
"buffer_load_dwordx4 v1, s[40:43], s54 offen nt lds\n" \
"s_add_u32 s54, s54, s44\n" \
"s_add_u32 m0, s52, 34816\n" \
"s_nop 0\n" \
"buffer_load_dwordx4 v1, s[40:43], s54 offen nt lds\n" \
"s_add_u32 s54, s54, s44\n" \
"s_add_u32 m0, s52, 36864\n" \
"s_nop 0\n" \
"buffer_load_dwordx4 v1, s[40:43], s54 offen nt lds\n" \
"s_add_u32 s54, s54, s44\n" \
"s_add_u32 m0, s52, 38912\n" \
"s_nop 0\n" \
"buffer_load_dwordx4 v1, s[40:43], s54 offen nt lds\n" \
"s_add_u32 s54, s54, s44\n" \
"s_add_u32 m0, s52, 49152\n" \
"s_nop 0\n" \
"buffer_load_dwordx4 v1, s[40:43], s54 offen nt lds\n" \
"s_add_u32 s54, s54, s44\n" \
"s_add_u32 m0, s52, 51200\n" \
"s_nop 0\n" \
"buffer_load_dwordx4 v1, s[40:43], s54 offen nt lds\n" \
"s_add_u32 s54, s54, s44\n" \
"s_add_u32 m0, s52, 53248\n" \
"s_nop 0\n" \
"buffer_load_dwordx4 v1, s[40:43], s54 offen nt lds\n" \
"s_add_u32 s54, s54, s44\n" \
"s_add_u32 m0, s52, 55296\n" \
"s_nop 0\n" \
"buffer_load_dwordx4 v1, s[40:43], s54 offen nt lds\n" \
"s_add_u32 s54, s54, s44\n" \
"s_add_u32 m0, s52, 65536\n" \
"s_nop 0\n" \
"buffer_load_dwordx4 v1, s[40:43], s54 offen nt lds\n" \
"s_add_u32 s54, s54, s44\n" \
"s_add_u32 m0, s52, 67584\n" \
"s_nop 0\n" \
"buffer_load_dwordx4 v1, s[40:43], s54 offen nt lds\n" \
"s_add_u32 s54, s54, s44\n" \
"s_add_u32 m0, s52, 69632\n" \
"s_nop 0\n" \
"buffer_load_dwordx4 v1, s[40:43], s54 offen nt lds\n" \
"s_add_u32 s54, s54, s44\n" \
"s_add_u32 m0, s52, 71680\n" \
"s_nop 0\n" \
"buffer_load_dwordx4 v1, s[40:43], s54 offen nt lds\n" \
"s_add_u32 s54, s54, s44\n" \
"s_add_u32 m0, s52, 81920\n" \
"s_nop 0\n" \
"buffer_load_dwordx4 v1, s[40:43], s54 offen nt lds\n" \
"s_add_u32 s54, s54, s44\n" \
"s_add_u32 m0, s52, 83968\n" \
"s_nop 0\n" \
"buffer_load_dwordx4 v1, s[40:43], s54 offen nt lds\n" \
"s_add_u32 s54, s54, s44\n" \
"s_add_u32 m0, s52, 86016\n" \
"s_nop 0\n" \
"buffer_load_dwordx4 v1, s[40:43], s54 offen nt lds\n" \
"s_add_u32 s54, s54, s44\n" \
"s_add_u32 m0, s52, 88064\n" \
"s_nop 0\n" \
"buffer_load_dwordx4 v1, s[40:43], s54 offen nt lds\n" \
"s_add_u32 s54, s54, s44\n" \
"s_add_u32 m0, s52, 98304\n" \
"s_nop 0\n" \
"buffer_load_dwordx4 v1, s[40:43], s54 offen nt lds\n" \
"s_add_u32 s54, s54, s44\n" \
"s_add_u32 m0, s52, 100352\n" \
"s_nop 0\n" \
"buffer_load_dwordx4 v1, s[40:43], s54 offen nt lds\n" \
"s_add_u32 s54, s54, s44\n" \
"s_add_u32 m0, s52, 102400\n" \
"s_nop 0\n" \
"buffer_load_dwordx4 v1, s[40:43], s54 offen nt lds\n" \
"s_add_u32 s54, s54, s44\n" \
"s_add_u32 m0, s52, 104448\n" \
"s_nop 0\n" \
"buffer_load_dwordx4 v1, s[40:43], s54 offen nt lds\n" \
"s_add_u32 s54, s54, s44\n" \
"s_add_u32 m0, s52, 114688\n" \
"s_nop 0\n" \
"buffer_load_dwordx4 v1, s[40:43], s54 offen nt lds\n" \
"s_add_u32 s54, s54, s44\n" \
"s_add_u32 m0, s52, 116736\n" \
"s_nop 0\n" \
"buffer_load_dwordx4 v1, s[40:43], s54 offen nt lds\n" \
"s_add_u32 s54, s54, s44\n" \
"s_add_u32 m0, s52, 118784\n" \
"s_nop 0\n" \
"buffer_load_dwordx4 v1, s[40:43], s54 offen nt lds\n" \
"s_add_u32 s54, s54, s44\n" \
"s_add_u32 m0, s52, 120832\n" \
"s_nop 0\n" \
"buffer_load_dwordx4 v1, s[40:43], s54 offen nt lds\n" \
"s_add_u32 s54, s54, s44\n" \
"v_add_u32 v8, 0x10000, v2\n" \
"v_add_u32 v9, 0x10000, v3\n" \
"s_waitcnt vmcnt(28)\n" \
"s_barrier\n" \
"ds_read_b64_tr_b16 a[0:1], v2 offset:0\n" \
"ds_read_b64_tr_b16 a[2:3], v2 offset:512\n" \
"ds_read_b64_tr_b16 a[128:129], v3 offset:0\n" \
"ds_read_b64_tr_b16 a[130:131], v3 offset:512\n" \
"ds_read_b64_tr_b16 a[4:5], v2 offset:4096\n" \
"ds_read_b64_tr_b16 a[6:7], v2 offset:4608\n" \
"ds_read_b64_tr_b16 a[132:133], v3 offset:4096\n" \
"ds_read_b64_tr_b16 a[134:135], v3 offset:4608\n" \
"s_waitcnt lgkmcnt(0)\n" \
"s_barrier\n" \
"s_add_u32 m0, s52, 0\n" \
"s_nop 0\n" \
"buffer_load_dwordx4 v1, s[40:43], s54 offen nt lds\n" \
"s_add_u32 s54, s54, s44\n" \
"s_add_u32 m0, s52, 2048\n" \
"s_nop 0\n" \
"buffer_load_dwordx4 v1, s[40:43], s54 offen nt lds\n" \
"s_add_u32 s54, s54, s44\n" \
"s_add_u32 m0, s52, 4096\n" \
"s_nop 0\n" \
"buffer_load_dwordx4 v1, s[40:43], s54 offen nt lds\n" \
"s_add_u32 s54, s54, s44\n" \
"s_add_u32 m0, s52, 6144\n" \
"s_nop 0\n" \
"buffer_load_dwordx4 v1, s[40:43], s54 offen nt lds\n" \
"s_add_u32 s54, s54, s44\n" \
"s_waitcnt vmcnt(28)\n" \
"s_barrier\n" \
"ds_read_b64_tr_b16 a[8:9], v2 offset:16384\n" \
"ds_read_b64_tr_b16 a[10:11], v2 offset:16896\n" \
"ds_read_b64_tr_b16 a[136:137], v3 offset:16384\n" \
"ds_read_b64_tr_b16 a[138:139], v3 offset:16896\n" \
"ds_read_b64_tr_b16 a[12:13], v2 offset:20480\n" \
"ds_read_b64_tr_b16 a[14:15], v2 offset:20992\n" \
"ds_read_b64_tr_b16 a[140:141], v3 offset:20480\n" \
"ds_read_b64_tr_b16 a[142:143], v3 offset:20992\n" \
"s_waitcnt lgkmcnt(0)\n" \
"s_barrier\n" \
"s_add_u32 m0, s52, 16384\n" \
"s_nop 0\n" \
"buffer_load_dwordx4 v1, s[40:43], s54 offen nt lds\n" \
"s_add_u32 s54, s54, s44\n" \
"s_add_u32 m0, s52, 18432\n" \
"s_nop 0\n" \
"buffer_load_dwordx4 v1, s[40:43], s54 offen nt lds\n" \
"s_add_u32 s54, s54, s44\n" \
"s_add_u32 m0, s52, 20480\n" \
"s_nop 0\n" \
"buffer_load_dwordx4 v1, s[40:43], s54 offen nt lds\n" \
"s_add_u32 s54, s54, s44\n" \
"s_add_u32 m0, s52, 22528\n" \
"s_nop 0\n" \
"buffer_load_dwordx4 v1, s[40:43], s54 offen nt lds\n" \
"s_add_u32 s54, s54, s44\n" \
"s_waitcnt vmcnt(28)\n" \
"s_barrier\n" \
"ds_read_b64_tr_b16 a[16:17], v2 offset:32768\n" \
"ds_read_b64_tr_b16 a[18:19], v2 offset:33280\n" \
"ds_read_b64_tr_b16 a[144:145], v3 offset:32768\n" \
"ds_read_b64_tr_b16 a[146:147], v3 offset:33280\n" \
"ds_read_b64_tr_b16 a[20:21], v2 offset:36864\n" \
"ds_read_b64_tr_b16 a[22:23], v2 offset:37376\n" \
"ds_read_b64_tr_b16 a[148:149], v3 offset:36864\n" \
"ds_read_b64_tr_b16 a[150:151], v3 offset:37376\n" \
"s_waitcnt lgkmcnt(0)\n" \
"s_barrier\n" \
"s_add_u32 m0, s52, 32768\n" \
"s_nop 0\n" \
"buffer_load_dwordx4 v1, s[40:43], s54 offen nt lds\n" \
"s_add_u32 s54, s54, s44\n" \
"s_add_u32 m0, s52, 34816\n" \
"s_nop 0\n" \
"buffer_load_dwordx4 v1, s[40:43], s54 offen nt lds\n" \
"s_add_u32 s54, s54, s44\n" \
"s_add_u32 m0, s52, 36864\n" \
"s_nop 0\n" \
"buffer_load_dwordx4 v1, s[40:43], s54 offen nt lds\n" \
"s_add_u32 s54, s54, s44\n" \
"s_add_u32 m0, s52, 38912\n" \
"s_nop 0\n" \
"buffer_load_dwordx4 v1, s[40:43], s54 offen nt lds\n" \
"s_add_u32 s54, s54, s44\n" \
"s_waitcnt vmcnt(28)\n" \
"s_barrier\n" \
"ds_read_b64_tr_b16 a[24:25], v2 offset:49152\n" \
"ds_read_b64_tr_b16 a[26:27], v2 offset:49664\n" \
"ds_read_b64_tr_b16 a[152:153], v3 offset:49152\n" \
"ds_read_b64_tr_b16 a[154:155], v3 offset:49664\n" \
"ds_read_b64_tr_b16 a[28:29], v2 offset:53248\n" \
"ds_read_b64_tr_b16 a[30:31], v2 offset:53760\n" \
"ds_read_b64_tr_b16 a[156:157], v3 offset:53248\n" \
"ds_read_b64_tr_b16 a[158:159], v3 offset:53760\n" \
"s_waitcnt lgkmcnt(0)\n" \
"s_barrier\n" \
"s_add_u32 m0, s52, 49152\n" \
"s_nop 0\n" \
"buffer_load_dwordx4 v1, s[40:43], s54 offen nt lds\n" \
"s_add_u32 s54, s54, s44\n" \
"s_add_u32 m0, s52, 51200\n" \
"s_nop 0\n" \
"buffer_load_dwordx4 v1, s[40:43], s54 offen nt lds\n" \
"s_add_u32 s54, s54, s44\n" \
"s_add_u32 m0, s52, 53248\n" \
"s_nop 0\n" \
"buffer_load_dwordx4 v1, s[40:43], s54 offen nt lds\n" \
"s_add_u32 s54, s54, s44\n" \
"s_add_u32 m0, s52, 55296\n" \
"s_nop 0\n" \
"buffer_load_dwordx4 v1, s[40:43], s54 offen nt lds\n" \
"s_add_u32 s54, s54, s44\n" \
"s_waitcnt vmcnt(28)\n" \
"s_barrier\n" \
"ds_read_b64_tr_b16 a[32:33], v8 offset:0\n" \
"ds_read_b64_tr_b16 a[34:35], v8 offset:512\n" \
"ds_read_b64_tr_b16 a[160:161], v9 offset:0\n" \
"ds_read_b64_tr_b16 a[162:163], v9 offset:512\n" \
"ds_read_b64_tr_b16 a[36:37], v8 offset:4096\n" \
"ds_read_b64_tr_b16 a[38:39], v8 offset:4608\n" \
"ds_read_b64_tr_b16 a[164:165], v9 offset:4096\n" \
"ds_read_b64_tr_b16 a[166:167], v9 offset:4608\n" \
"s_waitcnt lgkmcnt(0)\n" \
"s_barrier\n" \
"s_add_u32 m0, s52, 65536\n" \
"s_nop 0\n" \
"buffer_load_dwordx4 v1, s[40:43], s54 offen nt lds\n" \
"s_add_u32 s54, s54, s44\n" \
"s_add_u32 m0, s52, 67584\n" \
"s_nop 0\n" \
"buffer_load_dwordx4 v1, s[40:43], s54 offen nt lds\n" \
"s_add_u32 s54, s54, s44\n" \
"s_add_u32 m0, s52, 69632\n" \
"s_nop 0\n" \
"buffer_load_dwordx4 v1, s[40:43], s54 offen nt lds\n" \
"s_add_u32 s54, s54, s44\n" \
"s_add_u32 m0, s52, 71680\n" \
"s_nop 0\n" \
"buffer_load_dwordx4 v1, s[40:43], s54 offen nt lds\n" \
"s_add_u32 s54, s54, s44\n" \
"s_waitcnt vmcnt(28)\n" \
"s_barrier\n" \
"ds_read_b64_tr_b16 a[40:41], v8 offset:16384\n" \
"ds_read_b64_tr_b16 a[42:43], v8 offset:16896\n" \
"ds_read_b64_tr_b16 a[168:169], v9 offset:16384\n" \
"ds_read_b64_tr_b16 a[170:171], v9 offset:16896\n" \
"ds_read_b64_tr_b16 a[44:45], v8 offset:20480\n" \
"ds_read_b64_tr_b16 a[46:47], v8 offset:20992\n" \
"ds_read_b64_tr_b16 a[172:173], v9 offset:20480\n" \
"ds_read_b64_tr_b16 a[174:175], v9 offset:20992\n" \
"s_waitcnt lgkmcnt(0)\n" \
"s_barrier\n" \
"s_add_u32 m0, s52, 81920\n" \
"s_nop 0\n" \
"buffer_load_dwordx4 v1, s[40:43], s54 offen nt lds\n" \
"s_add_u32 s54, s54, s44\n" \
"s_add_u32 m0, s52, 83968\n" \
"s_nop 0\n" \
"buffer_load_dwordx4 v1, s[40:43], s54 offen nt lds\n" \
"s_add_u32 s54, s54, s44\n" \
"s_add_u32 m0, s52, 86016\n" \
"s_nop 0\n" \
"buffer_load_dwordx4 v1, s[40:43], s54 offen nt lds\n" \
"s_add_u32 s54, s54, s44\n" \
"s_add_u32 m0, s52, 88064\n" \
"s_nop 0\n" \
"buffer_load_dwordx4 v1, s[40:43], s54 offen nt lds\n" \
"s_add_u32 s54, s54, s44\n" \
"s_waitcnt vmcnt(28)\n" \
"s_barrier\n" \
"ds_read_b64_tr_b16 a[48:49], v8 offset:32768\n" \
"ds_read_b64_tr_b16 a[50:51], v8 offset:33280\n" \
"ds_read_b64_tr_b16 a[176:177], v9 offset:32768\n" \
"ds_read_b64_tr_b16 a[178:179], v9 offset:33280\n" \
"ds_read_b64_tr_b16 a[52:53], v8 offset:36864\n" \
"ds_read_b64_tr_b16 a[54:55], v8 offset:37376\n" \
"ds_read_b64_tr_b16 a[180:181], v9 offset:36864\n" \
"ds_read_b64_tr_b16 a[182:183], v9 offset:37376\n" \
"s_waitcnt lgkmcnt(0)\n" \
"s_barrier\n" \
"s_add_u32 m0, s52, 98304\n" \
"s_nop 0\n" \
"buffer_load_dwordx4 v1, s[40:43], s54 offen nt lds\n" \
"s_add_u32 s54, s54, s44\n" \
"s_add_u32 m0, s52, 100352\n" \
"s_nop 0\n" \
"buffer_load_dwordx4 v1, s[40:43], s54 offen nt lds\n" \
"s_add_u32 s54, s54, s44\n" \
"s_add_u32 m0, s52, 102400\n" \
"s_nop 0\n" \
"buffer_load_dwordx4 v1, s[40:43], s54 offen nt lds\n" \
"s_add_u32 s54, s54, s44\n" \
"s_add_u32 m0, s52, 104448\n" \
"s_nop 0\n" \
"buffer_load_dwordx4 v1, s[40:43], s54 offen nt lds\n" \
"s_add_u32 s54, s54, s44\n" \
"s_waitcnt vmcnt(28)\n" \
"s_barrier\n" \
"ds_read_b64_tr_b16 a[56:57], v8 offset:49152\n" \
"ds_read_b64_tr_b16 a[58:59], v8 offset:49664\n" \
"ds_read_b64_tr_b16 a[184:185], v9 offset:49152\n" \
"ds_read_b64_tr_b16 a[186:187], v9 offset:49664\n" \
"ds_read_b64_tr_b16 a[60:61], v8 offset:53248\n" \
"ds_read_b64_tr_b16 a[62:63], v8 offset:53760\n" \
"ds_read_b64_tr_b16 a[188:189], v9 offset:53248\n" \
"ds_read_b64_tr_b16 a[190:191], v9 offset:53760\n" \
"s_waitcnt lgkmcnt(0)\n" \
"s_barrier\n" \
"s_add_u32 m0, s52, 114688\n" \
"s_nop 0\n" \
"buffer_load_dwordx4 v1, s[40:43], s54 offen nt lds\n" \
"s_add_u32 s54, s54, s44\n" \
"s_add_u32 m0, s52, 116736\n" \
"s_nop 0\n" \
"buffer_load_dwordx4 v1, s[40:43], s54 offen nt lds\n" \
"s_add_u32 s54, s54, s44\n" \
"s_add_u32 m0, s52, 118784\n" \
"s_nop 0\n" \
"buffer_load_dwordx4 v1, s[40:43], s54 offen nt lds\n" \
"s_add_u32 s54, s54, s44\n" \
"s_add_u32 m0, s52, 120832\n" \
"s_nop 0\n" \
"buffer_load_dwordx4 v1, s[40:43], s54 offen nt lds\n" \
"s_add_u32 s54, s54, s44\n" \
"s_waitcnt vmcnt(28)\n" \
"s_barrier\n" \
"ds_read_b64_tr_b16 a[64:65], v2 offset:0\n" \
"ds_read_b64_tr_b16 a[66:67], v2 offset:512\n" \
"ds_read_b64_tr_b16 a[192:193], v3 offset:0\n" \
"ds_read_b64_tr_b16 a[194:195], v3 offset:512\n" \
"ds_read_b64_tr_b16 a[68:69], v2 offset:4096\n" \
"ds_read_b64_tr_b16 a[70:71], v2 offset:4608\n" \
"ds_read_b64_tr_b16 a[196:197], v3 offset:4096\n" \
"ds_read_b64_tr_b16 a[198:199], v3 offset:4608\n" \
"s_waitcnt vmcnt(24)\n" \
"s_barrier\n" \
"ds_read_b64_tr_b16 a[72:73], v2 offset:16384\n" \
"ds_read_b64_tr_b16 a[74:75], v2 offset:16896\n" \
"ds_read_b64_tr_b16 a[200:201], v3 offset:16384\n" \
"ds_read_b64_tr_b16 a[202:203], v3 offset:16896\n" \
"ds_read_b64_tr_b16 a[76:77], v2 offset:20480\n" \
"ds_read_b64_tr_b16 a[78:79], v2 offset:20992\n" \
"ds_read_b64_tr_b16 a[204:205], v3 offset:20480\n" \
"ds_read_b64_tr_b16 a[206:207], v3 offset:20992\n" \
"s_waitcnt vmcnt(20)\n" \
"s_barrier\n" \
"ds_read_b64_tr_b16 a[80:81], v2 offset:32768\n" \
"ds_read_b64_tr_b16 a[82:83], v2 offset:33280\n" \
"ds_read_b64_tr_b16 a[208:209], v3 offset:32768\n" \
"ds_read_b64_tr_b16 a[210:211], v3 offset:33280\n" \
"ds_read_b64_tr_b16 a[84:85], v2 offset:36864\n" \
"ds_read_b64_tr_b16 a[86:87], v2 offset:37376\n" \
"ds_read_b64_tr_b16 a[212:213], v3 offset:36864\n" \
"ds_read_b64_tr_b16 a[214:215], v3 offset:37376\n" \
"s_waitcnt vmcnt(16)\n" \
"s_barrier\n" \
"ds_read_b64_tr_b16 a[88:89], v2 offset:49152\n" \
"ds_read_b64_tr_b16 a[90:91], v2 offset:49664\n" \
"ds_read_b64_tr_b16 a[216:217], v3 offset:49152\n" \
"ds_read_b64_tr_b16 a[218:219], v3 offset:49664\n" \
"ds_read_b64_tr_b16 a[92:93], v2 offset:53248\n" \
"ds_read_b64_tr_b16 a[94:95], v2 offset:53760\n" \
"ds_read_b64_tr_b16 a[220:221], v3 offset:53248\n" \
"ds_read_b64_tr_b16 a[222:223], v3 offset:53760\n" \
"s_waitcnt vmcnt(12)\n" \
"s_barrier\n" \
"ds_read_b64_tr_b16 a[96:97], v8 offset:0\n" \
"ds_read_b64_tr_b16 a[98:99], v8 offset:512\n" \
"ds_read_b64_tr_b16 a[224:225], v9 offset:0\n" \
"ds_read_b64_tr_b16 a[226:227], v9 offset:512\n" \
"ds_read_b64_tr_b16 a[100:101], v8 offset:4096\n" \
"ds_read_b64_tr_b16 a[102:103], v8 offset:4608\n" \
"ds_read_b64_tr_b16 a[228:229], v9 offset:4096\n" \
"ds_read_b64_tr_b16 a[230:231], v9 offset:4608\n" \
"s_waitcnt vmcnt(8)\n" \
"s_barrier\n" \
"ds_read_b64_tr_b16 a[104:105], v8 offset:16384\n" \
"ds_read_b64_tr_b16 a[106:107], v8 offset:16896\n" \
"ds_read_b64_tr_b16 a[232:233], v9 offset:16384\n" \
"ds_read_b64_tr_b16 a[234:235], v9 offset:16896\n" \
"ds_read_b64_tr_b16 a[108:109], v8 offset:20480\n" \
"ds_read_b64_tr_b16 a[110:111], v8 offset:20992\n" \
"ds_read_b64_tr_b16 a[236:237], v9 offset:20480\n" \
"ds_read_b64_tr_b16 a[238:239], v9 offset:20992\n" \
"s_waitcnt vmcnt(4)\n" \
"s_barrier\n" \
"ds_read_b64_tr_b16 a[112:113], v8 offset:32768\n" \
"ds_read_b64_tr_b16 a[114:115], v8 offset:33280\n" \
"ds_read_b64_tr_b16 a[240:241], v9 offset:32768\n" \
"ds_read_b64_tr_b16 a[242:243], v9 offset:33280\n" \
"ds_read_b64_tr_b16 a[116:117], v8 offset:36864\n" \
"ds_read_b64_tr_b16 a[118:119], v8 offset:37376\n" \
"ds_read_b64_tr_b16 a[244:245], v9 offset:36864\n" \
"ds_read_b64_tr_b16 a[246:247], v9 offset:37376\n" \
"s_waitcnt vmcnt(0)\n" \
"s_barrier\n" \
"ds_read_b64_tr_b16 a[120:121], v8 offset:49152\n" \
"ds_read_b64_tr_b16 a[122:123], v8 offset:49664\n" \
"ds_read_b64_tr_b16 a[248:249], v9 offset:49152\n" \
"ds_read_b64_tr_b16 a[250:251], v9 offset:49664\n" \
"ds_read_b64_tr_b16 a[124:125], v8 offset:53248\n" \
"ds_read_b64_tr_b16 a[126:127], v8 offset:53760\n" \
"ds_read_b64_tr_b16 a[252:253], v9 offset:53248\n" \
"ds_read_b64_tr_b16 a[254:255], v9 offset:53760\n" \
"s_waitcnt lgkmcnt(0)\n" \
"s_barrier\n" \
"v_mov_b32 v14, 4096\n" \
"s_mov_b32 s59, s46\n" \
"s_add_u32 s63, s59, 0\n" \
"s_min_u32 s63, s63, 63\n" \
"s_lshr_b32 s63, s63, 3\n" \
"s_lshl_b32 s63, s63, 7\n" \
"s_branch .Lp1qk_l0_%=\n" \
".Lp1qk_l0k_%=:\n" \
"s_add_u32 s63, s59, 1\n" \
"s_min_u32 s63, s63, 63\n" \
"s_lshr_b32 s63, s63, 3\n" \
"s_lshl_b32 s63, s63, 7\n" \
"s_branch .Lp1qk_l1_%=\n" \
".Lp1qk_l1k_%=:\n" \
"s_lshl_b32 s55, s48, 13\n" \
"s_lshl_b32 s56, s46, 17\n" \
"s_add_u32 s56, s56, s45\n" \
"s_lshl_b32 s60, s47, 17\n" \
"s_sub_u32 s60, s60, 0x20000\n" \
"s_add_u32 s60, s60, s45\n" \
"s_mov_b32 s57, 0\n" \
"s_mov_b32 s58, 0\n" \
"v_mov_b32 v7, v5\n" \
"v_mov_b32 v5, 0x7ffffff0\n" \
"v_mov_b32 v10, v0\n" \
"v_mov_b32 v11, v6\n" \
"v_mov_b32 v6, 0x7ffffff0\n" \
"s_mov_b32 s59, s46\n" \
"s_min_u32 s61, s56, s60\n" \
"s_lshr_b32 s65, s61, 8\n" \
"s_and_b32 s65, s65, 0x2000\n" \
"s_xor_b32 s61, s61, s65\n" \
"s_add_u32 m0, s55, 0\n" \
"s_nop 0\n" \
"buffer_load_dwordx4 v4, s[36:39], s61 offen offset:0 sc1 lds\n" \
"buffer_load_dwordx4 v4, s[36:39], s61 offen offset:1024 sc1 lds\n" \
"buffer_load_dwordx4 v4, s[36:39], s61 offen offset:2048 sc1 lds\n" \
"buffer_load_dwordx4 v4, s[36:39], s61 offen offset:3072 sc1 lds\n" \
"s_add_u32 m0, s55, 4096\n" \
"s_add_u32 s61, s61, 4096\n" \
"buffer_load_dwordx4 v4, s[36:39], s61 offen offset:0 sc1 lds\n" \
"buffer_load_dwordx4 v4, s[36:39], s61 offen offset:1024 sc1 lds\n" \
"buffer_load_dwordx4 v4, s[36:39], s61 offen offset:2048 sc1 lds\n" \
"buffer_load_dwordx4 v4, s[36:39], s61 offen offset:3072 sc1 lds\n" \
"s_add_u32 s61, s61, 28672\n" \
"s_add_u32 s56, s56, 0x20000\n" \
"s_add_u32 m0, s55, 32768\n" \
"s_nop 0\n" \
"buffer_load_dwordx4 v4, s[36:39], s61 offen offset:0 sc1 lds\n" \
"buffer_load_dwordx4 v4, s[36:39], s61 offen offset:1024 sc1 lds\n" \
"buffer_load_dwordx4 v4, s[36:39], s61 offen offset:2048 sc1 lds\n" \
"buffer_load_dwordx4 v4, s[36:39], s61 offen offset:3072 sc1 lds\n" \
"s_add_u32 m0, s55, 36864\n" \
"s_add_u32 s61, s61, 4096\n" \
"buffer_load_dwordx4 v4, s[36:39], s61 offen offset:0 sc1 lds\n" \
"buffer_load_dwordx4 v4, s[36:39], s61 offen offset:1024 sc1 lds\n" \
"buffer_load_dwordx4 v4, s[36:39], s61 offen offset:2048 sc1 lds\n" \
"buffer_load_dwordx4 v4, s[36:39], s61 offen offset:3072 sc1 lds\n" \
"s_mov_b32 s70, 0\n" \
"s_mov_b32 s72, 0\n" \
"s_waitcnt vmcnt(0) lgkmcnt(0)\n" \
"s_barrier\n" \
"v_add_u32 v12, 0x10000, v10\n" \
"ds_read_b128 v[16:19], v10 offset:0\n" \
"ds_read_b128 v[20:23], v10 offset:16384\n" \
"ds_read_b128 v[24:27], v10 offset:1024\n" \
"ds_read_b128 v[28:31], v10 offset:17408\n" \
"ds_read_b128 v[32:35], v10 offset:2048\n" \
"ds_read_b128 v[36:39], v10 offset:18432\n" \
"s_waitcnt lgkmcnt(4)\n" \
"s_nop 7\n" \
"s_nop 7\n" \
".p2align 6\n" \
".Lp1qk_loop_%=:\n" \
"v_mfma_f32_16x16x32_bf16 v[48:51], a[0:3], v[16:19], 0\n" \
"s_min_u32 s61, s56, s60\n" \
"s_lshr_b32 s65, s61, 8\n" \
"s_and_b32 s65, s65, 0x2000\n" \
"s_xor_b32 s61, s61, s65\n" \
"s_add_u32 m0, s55, 65536\n" \
"s_nop 0\n" \
"buffer_load_dwordx4 v4, s[36:39], s61 offen offset:0 sc1 lds\n" \
"v_mov_b32_e64 v13, s59\n" \
"v_mfma_f32_16x16x32_bf16 v[52:55], a[0:3], v[20:23], 0\n" \
"ds_read_b128 v[40:43], v10 offset:3072\n" \
"s_nop 0\n" \
"v_mfma_f32_16x16x32_bf16 v[56:59], a[128:131], v[16:19], 0\n" \
"ds_read_b128 v[44:47], v10 offset:19456\n" \
"s_nop 0\n" \
"v_mfma_f32_16x16x32_bf16 v[60:63], a[128:131], v[20:23], 0\n" \
"s_waitcnt lgkmcnt(4)\n" \
"v_mul_f32 v64, s51, v64\n" \
"v_mul_f32_e64 v65, s51, v65\n" \
"v_mfma_f32_16x16x32_bf16 v[48:51], a[4:7], v[24:27], v[48:51]\n" \
"buffer_load_dwordx4 v4, s[36:39], s61 offen offset:1024 sc1 lds\n" \
"v_mfma_f32_16x16x32_bf16 v[52:55], a[4:7], v[28:31], v[52:55]\n" \
"ds_read_b128 v[16:19], v10 offset:4096\n" \
"v_mul_f32_e64 v66, s51, v66\n" \
"v_mfma_f32_16x16x32_bf16 v[56:59], a[132:135], v[24:27], v[56:59]\n" \
"ds_read_b128 v[20:23], v10 offset:20480\n" \
"v_mul_f32_e64 v67, s51, v67\n" \
"v_mfma_f32_16x16x32_bf16 v[60:63], a[132:135], v[28:31], v[60:63]\n" \
"s_waitcnt lgkmcnt(4)\n" \
"v_mul_f32 v72, s51, v72\n" \
"v_mul_f32_e64 v73, s51, v73\n" \
"v_mfma_f32_16x16x32_bf16 v[48:51], a[8:11], v[32:35], v[48:51]\n" \
"s_add_u32 s63, s59, 2\n" \
"s_min_u32 s63, s63, 63\n" \
"v_mfma_f32_16x16x32_bf16 v[52:55], a[8:11], v[36:39], v[52:55]\n" \
"ds_read_b128 v[24:27], v10 offset:5120\n" \
"s_lshr_b32 s63, s63, 3\n" \
"s_lshl_b32 s63, s63, 7\n" \
"v_mul_f32_e64 v74, s51, v74\n" \
"v_mfma_f32_16x16x32_bf16 v[56:59], a[136:139], v[32:35], v[56:59]\n" \
"ds_read_b128 v[28:31], v10 offset:21504\n" \
"v_mul_f32_e64 v75, s51, v75\n" \
"v_mfma_f32_16x16x32_bf16 v[60:63], a[136:139], v[36:39], v[60:63]\n" \
"s_waitcnt lgkmcnt(4)\n" \
"v_cvt_pk_bf16_f32 v80, v64, v65\n" \
"v_cvt_pk_bf16_f32 v81, v66, v67\n" \
"v_mfma_f32_16x16x32_bf16 v[48:51], a[12:15], v[40:43], v[48:51]\n" \
"buffer_load_dwordx4 v4, s[36:39], s61 offen offset:2048 sc1 lds\n" \
"v_mfma_f32_16x16x32_bf16 v[52:55], a[12:15], v[44:47], v[52:55]\n" \
"ds_read_b128 v[32:35], v10 offset:6144\n" \
"v_cvt_pk_bf16_f32 v82, v72, v73\n" \
"v_mfma_f32_16x16x32_bf16 v[56:59], a[140:143], v[40:43], v[56:59]\n" \
"ds_read_b128 v[36:39], v10 offset:22528\n" \
"v_cvt_pk_bf16_f32 v83, v74, v75\n" \
"v_mfma_f32_16x16x32_bf16 v[60:63], a[140:143], v[44:47], v[60:63]\n" \
"s_waitcnt lgkmcnt(4)\n" \
"buffer_store_dwordx4 v[80:83], v5, s[36:39], s57 offen sc1\n" \
"v_mul_f32_e64 v68, s51, v68\n" \
"v_mfma_f32_16x16x32_bf16 v[48:51], a[16:19], v[16:19], v[48:51]\n" \
"buffer_load_dwordx4 v4, s[36:39], s61 offen offset:3072 sc1 lds\n" \
"v_mfma_f32_16x16x32_bf16 v[52:55], a[16:19], v[20:23], v[52:55]\n" \
"ds_read_b128 v[40:43], v10 offset:7168\n" \
"v_mul_f32_e64 v69, s51, v69\n" \
"v_mfma_f32_16x16x32_bf16 v[56:59], a[144:147], v[16:19], v[56:59]\n" \
"ds_read_b128 v[44:47], v10 offset:23552\n" \
"v_mul_f32_e64 v70, s51, v70\n" \
"v_mfma_f32_16x16x32_bf16 v[60:63], a[144:147], v[20:23], v[60:63]\n" \
"s_waitcnt lgkmcnt(4)\n" \
"v_mul_f32 v71, s51, v71\n" \
"v_mul_f32_e64 v76, s51, v76\n" \
"v_mfma_f32_16x16x32_bf16 v[48:51], a[20:23], v[24:27], v[48:51]\n" \
"buffer_load_dword v15, v14, s[36:39], s63 offen sc1\n" \
"v_mfma_f32_16x16x32_bf16 v[52:55], a[20:23], v[28:31], v[52:55]\n" \
"ds_read_b128 v[16:19], v10 offset:8192\n" \
"v_mul_f32_e64 v77, s51, v77\n" \
"v_mfma_f32_16x16x32_bf16 v[56:59], a[148:151], v[24:27], v[56:59]\n" \
"ds_read_b128 v[20:23], v10 offset:24576\n" \
"v_mul_f32_e64 v78, s51, v78\n" \
"v_mfma_f32_16x16x32_bf16 v[60:63], a[148:151], v[28:31], v[60:63]\n" \
"s_waitcnt lgkmcnt(4)\n" \
"v_mul_f32 v79, s51, v79\n" \
"v_cvt_pk_bf16_f32 v84, v68, v69\n" \
"v_mfma_f32_16x16x32_bf16 v[48:51], a[24:27], v[32:35], v[48:51]\n" \
"s_add_u32 m0, s55, 69632\n" \
"s_add_u32 s61, s61, 4096\n" \
"buffer_load_dwordx4 v4, s[36:39], s61 offen offset:0 sc1 lds\n" \
"v_mfma_f32_16x16x32_bf16 v[52:55], a[24:27], v[36:39], v[52:55]\n" \
"ds_read_b128 v[24:27], v10 offset:9216\n" \
"v_cvt_pk_bf16_f32 v85, v70, v71\n" \
"v_mfma_f32_16x16x32_bf16 v[56:59], a[152:155], v[32:35], v[56:59]\n" \
"ds_read_b128 v[28:31], v10 offset:25600\n" \
"v_cvt_pk_bf16_f32 v86, v76, v77\n" \
"v_mfma_f32_16x16x32_bf16 v[60:63], a[152:155], v[36:39], v[60:63]\n" \
"s_waitcnt lgkmcnt(4)\n" \
"v_cvt_pk_bf16_f32 v87, v78, v79\n" \
"buffer_store_dwordx4 v[84:87], v5, s[36:39], s58 offen sc1\n" \
"v_mfma_f32_16x16x32_bf16 v[48:51], a[28:31], v[40:43], v[48:51]\n" \
"buffer_load_dwordx4 v4, s[36:39], s61 offen offset:1024 sc1 lds\n" \
"v_mfma_f32_16x16x32_bf16 v[52:55], a[28:31], v[44:47], v[52:55]\n" \
"ds_read_b128 v[32:35], v10 offset:10240\n" \
"v_mfma_f32_16x16x32_bf16 v[56:59], a[156:159], v[40:43], v[56:59]\n" \
"ds_read_b128 v[36:39], v10 offset:26624\n" \
"v_mfma_f32_16x16x32_bf16 v[60:63], a[156:159], v[44:47], v[60:63]\n" \
"s_waitcnt lgkmcnt(4)\n" \
"v_mfma_f32_16x16x32_bf16 v[48:51], a[32:35], v[16:19], v[48:51]\n" \
"v_mfma_f32_16x16x32_bf16 v[52:55], a[32:35], v[20:23], v[52:55]\n" \
"ds_read_b128 v[40:43], v10 offset:11264\n" \
"v_mfma_f32_16x16x32_bf16 v[56:59], a[160:163], v[16:19], v[56:59]\n" \
"ds_read_b128 v[44:47], v10 offset:27648\n" \
"v_mfma_f32_16x16x32_bf16 v[60:63], a[160:163], v[20:23], v[60:63]\n" \
"s_waitcnt lgkmcnt(4)\n" \
"v_mfma_f32_16x16x32_bf16 v[48:51], a[36:39], v[24:27], v[48:51]\n" \
"buffer_load_dwordx4 v4, s[36:39], s61 offen offset:2048 sc1 lds\n" \
"v_mfma_f32_16x16x32_bf16 v[52:55], a[36:39], v[28:31], v[52:55]\n" \
"ds_read_b128 v[16:19], v10 offset:12288\n" \
"v_mfma_f32_16x16x32_bf16 v[56:59], a[164:167], v[24:27], v[56:59]\n" \
"ds_read_b128 v[20:23], v10 offset:28672\n" \
"v_mfma_f32_16x16x32_bf16 v[60:63], a[164:167], v[28:31], v[60:63]\n" \
"s_waitcnt lgkmcnt(4)\n" \
"v_mfma_f32_16x16x32_bf16 v[48:51], a[40:43], v[32:35], v[48:51]\n" \
"buffer_load_dwordx4 v4, s[36:39], s61 offen offset:3072 sc1 lds\n" \
"v_mfma_f32_16x16x32_bf16 v[52:55], a[40:43], v[36:39], v[52:55]\n" \
"ds_read_b128 v[24:27], v10 offset:13312\n" \
"v_mfma_f32_16x16x32_bf16 v[56:59], a[168:171], v[32:35], v[56:59]\n" \
"ds_read_b128 v[28:31], v10 offset:29696\n" \
"v_mfma_f32_16x16x32_bf16 v[60:63], a[168:171], v[36:39], v[60:63]\n" \
"s_waitcnt lgkmcnt(4)\n" \
"v_mfma_f32_16x16x32_bf16 v[48:51], a[44:47], v[40:43], v[48:51]\n" \
"v_mfma_f32_16x16x32_bf16 v[52:55], a[44:47], v[44:47], v[52:55]\n" \
"ds_read_b128 v[32:35], v10 offset:14336\n" \
"v_mfma_f32_16x16x32_bf16 v[56:59], a[172:175], v[40:43], v[56:59]\n" \
"ds_read_b128 v[36:39], v10 offset:30720\n" \
"v_mfma_f32_16x16x32_bf16 v[60:63], a[172:175], v[44:47], v[60:63]\n" \
"s_waitcnt lgkmcnt(4)\n" \
"v_mfma_f32_16x16x32_bf16 v[48:51], a[48:51], v[16:19], v[48:51]\n" \
"v_mfma_f32_16x16x32_bf16 v[52:55], a[48:51], v[20:23], v[52:55]\n" \
"ds_read_b128 v[40:43], v10 offset:15360\n" \
"v_mfma_f32_16x16x32_bf16 v[56:59], a[176:179], v[16:19], v[56:59]\n" \
"ds_read_b128 v[44:47], v10 offset:31744\n" \
"v_mfma_f32_16x16x32_bf16 v[60:63], a[176:179], v[20:23], v[60:63]\n" \
"s_waitcnt lgkmcnt(4)\n" \
"v_mfma_f32_16x16x32_bf16 v[48:51], a[52:55], v[24:27], v[48:51]\n" \
"s_waitcnt vmcnt(12)\n" \
"s_barrier\n" \
"v_mfma_f32_16x16x32_bf16 v[52:55], a[52:55], v[28:31], v[52:55]\n" \
"ds_read_b128 v[16:19], v10 offset:32768\n" \
"v_mfma_f32_16x16x32_bf16 v[56:59], a[180:183], v[24:27], v[56:59]\n" \
"ds_read_b128 v[20:23], v10 offset:49152\n" \
"v_mfma_f32_16x16x32_bf16 v[60:63], a[180:183], v[28:31], v[60:63]\n" \
"s_waitcnt lgkmcnt(4)\n" \
"v_mfma_f32_16x16x32_bf16 v[48:51], a[56:59], v[32:35], v[48:51]\n" \
"v_mfma_f32_16x16x32_bf16 v[52:55], a[56:59], v[36:39], v[52:55]\n" \
"ds_read_b128 v[24:27], v10 offset:33792\n" \
"v_mfma_f32_16x16x32_bf16 v[56:59], a[184:187], v[32:35], v[56:59]\n" \
"ds_read_b128 v[28:31], v10 offset:50176\n" \
"v_mfma_f32_16x16x32_bf16 v[60:63], a[184:187], v[36:39], v[60:63]\n" \
"s_waitcnt lgkmcnt(4)\n" \
"v_mfma_f32_16x16x32_bf16 v[48:51], a[60:63], v[40:43], v[48:51]\n" \
"v_mfma_f32_16x16x32_bf16 v[52:55], a[60:63], v[44:47], v[52:55]\n" \
"ds_read_b128 v[32:35], v10 offset:34816\n" \
"v_mfma_f32_16x16x32_bf16 v[56:59], a[188:191], v[40:43], v[56:59]\n" \
"ds_read_b128 v[36:39], v10 offset:51200\n" \
"v_mfma_f32_16x16x32_bf16 v[60:63], a[188:191], v[44:47], v[60:63]\n" \
"s_waitcnt lgkmcnt(4)\n" \
"v_mfma_f32_16x16x32_bf16 v[48:51], a[64:67], v[16:19], v[48:51]\n" \
"s_add_u32 s61, s61, 28672\n" \
"s_add_u32 s56, s56, 0x20000\n" \
"s_add_u32 m0, s55, 98304\n" \
"s_nop 0\n" \
"buffer_load_dwordx4 v4, s[36:39], s61 offen offset:0 sc1 lds\n" \
"v_mfma_f32_16x16x32_bf16 v[52:55], a[64:67], v[20:23], v[52:55]\n" \
"ds_read_b128 v[40:43], v10 offset:35840\n" \
"v_mfma_f32_16x16x32_bf16 v[56:59], a[192:195], v[16:19], v[56:59]\n" \
"ds_read_b128 v[44:47], v10 offset:52224\n" \
"v_mfma_f32_16x16x32_bf16 v[60:63], a[192:195], v[20:23], v[60:63]\n" \
"s_waitcnt lgkmcnt(4)\n" \
"v_mfma_f32_16x16x32_bf16 v[48:51], a[68:71], v[24:27], v[48:51]\n" \
"buffer_load_dwordx4 v4, s[36:39], s61 offen offset:1024 sc1 lds\n" \
"v_mfma_f32_16x16x32_bf16 v[52:55], a[68:71], v[28:31], v[52:55]\n" \
"ds_read_b128 v[16:19], v10 offset:36864\n" \
"v_mfma_f32_16x16x32_bf16 v[56:59], a[196:199], v[24:27], v[56:59]\n" \
"ds_read_b128 v[20:23], v10 offset:53248\n" \
"v_mfma_f32_16x16x32_bf16 v[60:63], a[196:199], v[28:31], v[60:63]\n" \
"s_waitcnt lgkmcnt(4)\n" \
"v_mfma_f32_16x16x32_bf16 v[48:51], a[72:75], v[32:35], v[48:51]\n" \
"v_mfma_f32_16x16x32_bf16 v[52:55], a[72:75], v[36:39], v[52:55]\n" \
"ds_read_b128 v[24:27], v10 offset:37888\n" \
"v_mfma_f32_16x16x32_bf16 v[56:59], a[200:203], v[32:35], v[56:59]\n" \
"ds_read_b128 v[28:31], v10 offset:54272\n" \
"v_mfma_f32_16x16x32_bf16 v[60:63], a[200:203], v[36:39], v[60:63]\n" \
"s_waitcnt lgkmcnt(4)\n" \
"v_mfma_f32_16x16x32_bf16 v[48:51], a[76:79], v[40:43], v[48:51]\n" \
"buffer_load_dwordx4 v4, s[36:39], s61 offen offset:2048 sc1 lds\n" \
"v_mfma_f32_16x16x32_bf16 v[52:55], a[76:79], v[44:47], v[52:55]\n" \
"ds_read_b128 v[32:35], v10 offset:38912\n" \
"v_mfma_f32_16x16x32_bf16 v[56:59], a[204:207], v[40:43], v[56:59]\n" \
"ds_read_b128 v[36:39], v10 offset:55296\n" \
"v_mfma_f32_16x16x32_bf16 v[60:63], a[204:207], v[44:47], v[60:63]\n" \
"s_waitcnt lgkmcnt(4)\n" \
"v_mfma_f32_16x16x32_bf16 v[48:51], a[80:83], v[16:19], v[48:51]\n" \
"buffer_load_dwordx4 v4, s[36:39], s61 offen offset:3072 sc1 lds\n" \
"v_mfma_f32_16x16x32_bf16 v[52:55], a[80:83], v[20:23], v[52:55]\n" \
"ds_read_b128 v[40:43], v10 offset:39936\n" \
"v_mfma_f32_16x16x32_bf16 v[56:59], a[208:211], v[16:19], v[56:59]\n" \
"ds_read_b128 v[44:47], v10 offset:56320\n" \
"v_mfma_f32_16x16x32_bf16 v[60:63], a[208:211], v[20:23], v[60:63]\n" \
"s_waitcnt lgkmcnt(4)\n" \
"v_mfma_f32_16x16x32_bf16 v[48:51], a[84:87], v[24:27], v[48:51]\n" \
"v_mfma_f32_16x16x32_bf16 v[52:55], a[84:87], v[28:31], v[52:55]\n" \
"ds_read_b128 v[16:19], v10 offset:40960\n" \
"v_mfma_f32_16x16x32_bf16 v[56:59], a[212:215], v[24:27], v[56:59]\n" \
"ds_read_b128 v[20:23], v10 offset:57344\n" \
"v_mfma_f32_16x16x32_bf16 v[60:63], a[212:215], v[28:31], v[60:63]\n" \
"s_waitcnt lgkmcnt(4)\n" \
"v_mfma_f32_16x16x32_bf16 v[48:51], a[88:91], v[32:35], v[48:51]\n" \
"s_add_u32 m0, s55, 102400\n" \
"s_add_u32 s61, s61, 4096\n" \
"buffer_load_dwordx4 v4, s[36:39], s61 offen offset:0 sc1 lds\n" \
"v_mfma_f32_16x16x32_bf16 v[52:55], a[88:91], v[36:39], v[52:55]\n" \
"ds_read_b128 v[24:27], v10 offset:41984\n" \
"v_mfma_f32_16x16x32_bf16 v[56:59], a[216:219], v[32:35], v[56:59]\n" \
"ds_read_b128 v[28:31], v10 offset:58368\n" \
"v_mfma_f32_16x16x32_bf16 v[60:63], a[216:219], v[36:39], v[60:63]\n" \
"s_waitcnt lgkmcnt(4)\n" \
"v_mfma_f32_16x16x32_bf16 v[48:51], a[92:95], v[40:43], v[48:51]\n" \
"buffer_load_dwordx4 v4, s[36:39], s61 offen offset:1024 sc1 lds\n" \
"v_mfma_f32_16x16x32_bf16 v[52:55], a[92:95], v[44:47], v[52:55]\n" \
"ds_read_b128 v[32:35], v10 offset:43008\n" \
"v_mfma_f32_16x16x32_bf16 v[56:59], a[220:223], v[40:43], v[56:59]\n" \
"ds_read_b128 v[36:39], v10 offset:59392\n" \
"v_mfma_f32_16x16x32_bf16 v[60:63], a[220:223], v[44:47], v[60:63]\n" \
"s_waitcnt lgkmcnt(4)\n" \
"v_mfma_f32_16x16x32_bf16 v[48:51], a[96:99], v[16:19], v[48:51]\n" \
"v_mfma_f32_16x16x32_bf16 v[52:55], a[96:99], v[20:23], v[52:55]\n" \
"ds_read_b128 v[40:43], v10 offset:44032\n" \
"v_mfma_f32_16x16x32_bf16 v[56:59], a[224:227], v[16:19], v[56:59]\n" \
"ds_read_b128 v[44:47], v10 offset:60416\n" \
"v_mfma_f32_16x16x32_bf16 v[60:63], a[224:227], v[20:23], v[60:63]\n" \
"s_waitcnt lgkmcnt(4)\n" \
"v_mfma_f32_16x16x32_bf16 v[48:51], a[100:103], v[24:27], v[48:51]\n" \
"buffer_load_dwordx4 v4, s[36:39], s61 offen offset:2048 sc1 lds\n" \
"v_mfma_f32_16x16x32_bf16 v[52:55], a[100:103], v[28:31], v[52:55]\n" \
"ds_read_b128 v[16:19], v10 offset:45056\n" \
"v_mfma_f32_16x16x32_bf16 v[56:59], a[228:231], v[24:27], v[56:59]\n" \
"ds_read_b128 v[20:23], v10 offset:61440\n" \
"v_mfma_f32_16x16x32_bf16 v[60:63], a[228:231], v[28:31], v[60:63]\n" \
"s_waitcnt lgkmcnt(4)\n" \
"v_mfma_f32_16x16x32_bf16 v[48:51], a[104:107], v[32:35], v[48:51]\n" \
"buffer_load_dwordx4 v4, s[36:39], s61 offen offset:3072 sc1 lds\n" \
"v_mfma_f32_16x16x32_bf16 v[52:55], a[104:107], v[36:39], v[52:55]\n" \
"ds_read_b128 v[24:27], v10 offset:46080\n" \
"v_mfma_f32_16x16x32_bf16 v[56:59], a[232:235], v[32:35], v[56:59]\n" \
"ds_read_b128 v[28:31], v10 offset:62464\n" \
"v_mfma_f32_16x16x32_bf16 v[60:63], a[232:235], v[36:39], v[60:63]\n" \
"s_waitcnt lgkmcnt(4)\n" \
"v_mfma_f32_16x16x32_bf16 v[48:51], a[108:111], v[40:43], v[48:51]\n" \
"s_lshl_b32 s66, s59, s49\n" \
"s_lshr_b32 s65, s66, 8\n" \
"v_mfma_f32_16x16x32_bf16 v[52:55], a[108:111], v[44:47], v[52:55]\n" \
"ds_read_b128 v[32:35], v10 offset:47104\n" \
"v_mfma_f32_16x16x32_bf16 v[56:59], a[236:239], v[40:43], v[56:59]\n" \
"ds_read_b128 v[36:39], v10 offset:63488\n" \
"v_mfma_f32_16x16x32_bf16 v[60:63], a[236:239], v[44:47], v[60:63]\n" \
"s_waitcnt lgkmcnt(4)\n" \
"s_and_b32 s65, s65, 0x2000\n" \
"s_xor_b32 s66, s66, s65\n" \
"v_mfma_f32_16x16x32_bf16 v[48:51], a[112:115], v[16:19], v[48:51]\n" \
"v_mfma_f32_16x16x32_bf16 v[52:55], a[112:115], v[20:23], v[52:55]\n" \
"ds_read_b128 v[40:43], v10 offset:48128\n" \
"v_mfma_f32_16x16x32_bf16 v[56:59], a[240:243], v[16:19], v[56:59]\n" \
"ds_read_b128 v[44:47], v10 offset:64512\n" \
"v_mfma_f32_16x16x32_bf16 v[60:63], a[240:243], v[20:23], v[60:63]\n" \
"s_waitcnt lgkmcnt(4)\n" \
"v_mfma_f32_16x16x32_bf16 v[48:51], a[116:119], v[24:27], v[48:51]\n" \
"s_waitcnt vmcnt(8)\n" \
"s_barrier\n" \
"v_mfma_f32_16x16x32_bf16 v[52:55], a[116:119], v[28:31], v[52:55]\n" \
"ds_read_b128 v[16:19], v12 offset:0\n" \
"v_mfma_f32_16x16x32_bf16 v[56:59], a[244:247], v[24:27], v[56:59]\n" \
"ds_read_b128 v[20:23], v12 offset:16384\n" \
"v_mfma_f32_16x16x32_bf16 v[60:63], a[244:247], v[28:31], v[60:63]\n" \
"s_waitcnt lgkmcnt(4)\n" \
"buffer_store_dword v13, v6, s[36:39], 0 offen sc1\n" \
"v_mfma_f32_16x16x32_bf16 v[48:51], a[120:123], v[32:35], v[48:51]\n" \
"v_cmp_gt_u32_e64 vcc, 16, v15\n" \
"v_mfma_f32_16x16x32_bf16 v[52:55], a[120:123], v[36:39], v[52:55]\n" \
"ds_read_b128 v[24:27], v12 offset:1024\n" \
"v_mfma_f32_16x16x32_bf16 v[56:59], a[248:251], v[32:35], v[56:59]\n" \
"ds_read_b128 v[28:31], v12 offset:17408\n" \
"v_mfma_f32_16x16x32_bf16 v[60:63], a[248:251], v[36:39], v[60:63]\n" \
"s_waitcnt lgkmcnt(4)\n" \
"v_mfma_f32_16x16x32_bf16 v[48:51], a[124:127], v[40:43], v[48:51]\n" \
"s_cbranch_vccnz .Lp1qk_m0_%=\n" \
".Lp1qk_m0k_%=:\n" \
"v_mfma_f32_16x16x32_bf16 v[52:55], a[124:127], v[44:47], v[52:55]\n" \
"ds_read_b128 v[32:35], v12 offset:2048\n" \
"v_mfma_f32_16x16x32_bf16 v[56:59], a[252:255], v[40:43], v[56:59]\n" \
"ds_read_b128 v[36:39], v12 offset:18432\n" \
"v_mfma_f32_16x16x32_bf16 v[60:63], a[252:255], v[44:47], v[60:63]\n" \
"s_waitcnt lgkmcnt(4)\n" \
"s_xor_b32 s57, s66, s64\n" \
"s_add_u32 s58, s57, s50\n" \
"s_add_u32 s59, s59, 1\n" \
"v_mov_b32_e64 v5, v7\n" \
"v_mov_b32_e64 v6, v11\n" \
"s_cmp_ge_u32 s59, s47\n" \
"s_nop 0\n" \
"s_cbranch_scc1 .Lp1qk_out0_%=\n" \
"v_mfma_f32_16x16x32_bf16 v[64:67], a[0:3], v[16:19], 0\n" \
"s_min_u32 s61, s56, s60\n" \
"s_lshr_b32 s65, s61, 8\n" \
"s_and_b32 s65, s65, 0x2000\n" \
"s_xor_b32 s61, s61, s65\n" \
"s_add_u32 m0, s55, 0\n" \
"s_nop 0\n" \
"buffer_load_dwordx4 v4, s[36:39], s61 offen offset:0 sc1 lds\n" \
"v_mov_b32_e64 v13, s59\n" \
"v_mfma_f32_16x16x32_bf16 v[68:71], a[0:3], v[20:23], 0\n" \
"ds_read_b128 v[40:43], v12 offset:3072\n" \
"s_nop 0\n" \
"v_mfma_f32_16x16x32_bf16 v[72:75], a[128:131], v[16:19], 0\n" \
"ds_read_b128 v[44:47], v12 offset:19456\n" \
"s_nop 0\n" \
"v_mfma_f32_16x16x32_bf16 v[76:79], a[128:131], v[20:23], 0\n" \
"s_waitcnt lgkmcnt(4)\n" \
"v_mul_f32 v48, s51, v48\n" \
"v_mul_f32_e64 v49, s51, v49\n" \
"v_mfma_f32_16x16x32_bf16 v[64:67], a[4:7], v[24:27], v[64:67]\n" \
"buffer_load_dwordx4 v4, s[36:39], s61 offen offset:1024 sc1 lds\n" \
"v_mfma_f32_16x16x32_bf16 v[68:71], a[4:7], v[28:31], v[68:71]\n" \
"ds_read_b128 v[16:19], v12 offset:4096\n" \
"v_mul_f32_e64 v50, s51, v50\n" \
"v_mfma_f32_16x16x32_bf16 v[72:75], a[132:135], v[24:27], v[72:75]\n" \
"ds_read_b128 v[20:23], v12 offset:20480\n" \
"v_mul_f32_e64 v51, s51, v51\n" \
"v_mfma_f32_16x16x32_bf16 v[76:79], a[132:135], v[28:31], v[76:79]\n" \
"s_waitcnt lgkmcnt(4)\n" \
"v_mul_f32 v56, s51, v56\n" \
"v_mul_f32_e64 v57, s51, v57\n" \
"v_mfma_f32_16x16x32_bf16 v[64:67], a[8:11], v[32:35], v[64:67]\n" \
"s_add_u32 s63, s59, 2\n" \
"s_min_u32 s63, s63, 63\n" \
"v_mfma_f32_16x16x32_bf16 v[68:71], a[8:11], v[36:39], v[68:71]\n" \
"ds_read_b128 v[24:27], v12 offset:5120\n" \
"s_lshr_b32 s63, s63, 3\n" \
"s_lshl_b32 s63, s63, 7\n" \
"v_mul_f32_e64 v58, s51, v58\n" \
"v_mfma_f32_16x16x32_bf16 v[72:75], a[136:139], v[32:35], v[72:75]\n" \
"ds_read_b128 v[28:31], v12 offset:21504\n" \
"v_mul_f32_e64 v59, s51, v59\n" \
"v_mfma_f32_16x16x32_bf16 v[76:79], a[136:139], v[36:39], v[76:79]\n" \
"s_waitcnt lgkmcnt(4)\n" \
"v_cvt_pk_bf16_f32 v80, v48, v49\n" \
"v_cvt_pk_bf16_f32 v81, v50, v51\n" \
"v_mfma_f32_16x16x32_bf16 v[64:67], a[12:15], v[40:43], v[64:67]\n" \
"buffer_load_dwordx4 v4, s[36:39], s61 offen offset:2048 sc1 lds\n" \
"v_mfma_f32_16x16x32_bf16 v[68:71], a[12:15], v[44:47], v[68:71]\n" \
"ds_read_b128 v[32:35], v12 offset:6144\n" \
"v_cvt_pk_bf16_f32 v82, v56, v57\n" \
"v_mfma_f32_16x16x32_bf16 v[72:75], a[140:143], v[40:43], v[72:75]\n" \
"ds_read_b128 v[36:39], v12 offset:22528\n" \
"v_cvt_pk_bf16_f32 v83, v58, v59\n" \
"v_mfma_f32_16x16x32_bf16 v[76:79], a[140:143], v[44:47], v[76:79]\n" \
"s_waitcnt lgkmcnt(4)\n" \
"buffer_store_dwordx4 v[80:83], v5, s[36:39], s57 offen sc1\n" \
"v_mul_f32_e64 v52, s51, v52\n" \
"v_mfma_f32_16x16x32_bf16 v[64:67], a[16:19], v[16:19], v[64:67]\n" \
"buffer_load_dwordx4 v4, s[36:39], s61 offen offset:3072 sc1 lds\n" \
"v_mfma_f32_16x16x32_bf16 v[68:71], a[16:19], v[20:23], v[68:71]\n" \
"ds_read_b128 v[40:43], v12 offset:7168\n" \
"v_mul_f32_e64 v53, s51, v53\n" \
"v_mfma_f32_16x16x32_bf16 v[72:75], a[144:147], v[16:19], v[72:75]\n" \
"ds_read_b128 v[44:47], v12 offset:23552\n" \
"v_mul_f32_e64 v54, s51, v54\n" \
"v_mfma_f32_16x16x32_bf16 v[76:79], a[144:147], v[20:23], v[76:79]\n" \
"s_waitcnt lgkmcnt(4)\n" \
"v_mul_f32 v55, s51, v55\n" \
"v_mul_f32_e64 v60, s51, v60\n" \
"v_mfma_f32_16x16x32_bf16 v[64:67], a[20:23], v[24:27], v[64:67]\n" \
"buffer_load_dword v15, v14, s[36:39], s63 offen sc1\n" \
"v_mfma_f32_16x16x32_bf16 v[68:71], a[20:23], v[28:31], v[68:71]\n" \
"ds_read_b128 v[16:19], v12 offset:8192\n" \
"v_mul_f32_e64 v61, s51, v61\n" \
"v_mfma_f32_16x16x32_bf16 v[72:75], a[148:151], v[24:27], v[72:75]\n" \
"ds_read_b128 v[20:23], v12 offset:24576\n" \
"v_mul_f32_e64 v62, s51, v62\n" \
"v_mfma_f32_16x16x32_bf16 v[76:79], a[148:151], v[28:31], v[76:79]\n" \
"s_waitcnt lgkmcnt(4)\n" \
"v_mul_f32 v63, s51, v63\n" \
"v_cvt_pk_bf16_f32 v84, v52, v53\n" \
"v_mfma_f32_16x16x32_bf16 v[64:67], a[24:27], v[32:35], v[64:67]\n" \
"s_add_u32 m0, s55, 4096\n" \
"s_add_u32 s61, s61, 4096\n" \
"buffer_load_dwordx4 v4, s[36:39], s61 offen offset:0 sc1 lds\n" \
"v_mfma_f32_16x16x32_bf16 v[68:71], a[24:27], v[36:39], v[68:71]\n" \
"ds_read_b128 v[24:27], v12 offset:9216\n" \
"v_cvt_pk_bf16_f32 v85, v54, v55\n" \
"v_mfma_f32_16x16x32_bf16 v[72:75], a[152:155], v[32:35], v[72:75]\n" \
"ds_read_b128 v[28:31], v12 offset:25600\n" \
"v_cvt_pk_bf16_f32 v86, v60, v61\n" \
"v_mfma_f32_16x16x32_bf16 v[76:79], a[152:155], v[36:39], v[76:79]\n" \
"s_waitcnt lgkmcnt(4)\n" \
"v_cvt_pk_bf16_f32 v87, v62, v63\n" \
"buffer_store_dwordx4 v[84:87], v5, s[36:39], s58 offen sc1\n" \
"v_mfma_f32_16x16x32_bf16 v[64:67], a[28:31], v[40:43], v[64:67]\n" \
"buffer_load_dwordx4 v4, s[36:39], s61 offen offset:1024 sc1 lds\n" \
"v_mfma_f32_16x16x32_bf16 v[68:71], a[28:31], v[44:47], v[68:71]\n" \
"ds_read_b128 v[32:35], v12 offset:10240\n" \
"v_mfma_f32_16x16x32_bf16 v[72:75], a[156:159], v[40:43], v[72:75]\n" \
"ds_read_b128 v[36:39], v12 offset:26624\n" \
"v_mfma_f32_16x16x32_bf16 v[76:79], a[156:159], v[44:47], v[76:79]\n" \
"s_waitcnt lgkmcnt(4)\n" \
"v_mfma_f32_16x16x32_bf16 v[64:67], a[32:35], v[16:19], v[64:67]\n" \
"v_mfma_f32_16x16x32_bf16 v[68:71], a[32:35], v[20:23], v[68:71]\n" \
"ds_read_b128 v[40:43], v12 offset:11264\n" \
"v_mfma_f32_16x16x32_bf16 v[72:75], a[160:163], v[16:19], v[72:75]\n" \
"ds_read_b128 v[44:47], v12 offset:27648\n" \
"v_mfma_f32_16x16x32_bf16 v[76:79], a[160:163], v[20:23], v[76:79]\n" \
"s_waitcnt lgkmcnt(4)\n" \
"v_mfma_f32_16x16x32_bf16 v[64:67], a[36:39], v[24:27], v[64:67]\n" \
"buffer_load_dwordx4 v4, s[36:39], s61 offen offset:2048 sc1 lds\n" \
"v_mfma_f32_16x16x32_bf16 v[68:71], a[36:39], v[28:31], v[68:71]\n" \
"ds_read_b128 v[16:19], v12 offset:12288\n" \
"v_mfma_f32_16x16x32_bf16 v[72:75], a[164:167], v[24:27], v[72:75]\n" \
"ds_read_b128 v[20:23], v12 offset:28672\n" \
"v_mfma_f32_16x16x32_bf16 v[76:79], a[164:167], v[28:31], v[76:79]\n" \
"s_waitcnt lgkmcnt(4)\n" \
"v_mfma_f32_16x16x32_bf16 v[64:67], a[40:43], v[32:35], v[64:67]\n" \
"buffer_load_dwordx4 v4, s[36:39], s61 offen offset:3072 sc1 lds\n" \
"v_mfma_f32_16x16x32_bf16 v[68:71], a[40:43], v[36:39], v[68:71]\n" \
"ds_read_b128 v[24:27], v12 offset:13312\n" \
"v_mfma_f32_16x16x32_bf16 v[72:75], a[168:171], v[32:35], v[72:75]\n" \
"ds_read_b128 v[28:31], v12 offset:29696\n" \
"v_mfma_f32_16x16x32_bf16 v[76:79], a[168:171], v[36:39], v[76:79]\n" \
"s_waitcnt lgkmcnt(4)\n" \
"v_mfma_f32_16x16x32_bf16 v[64:67], a[44:47], v[40:43], v[64:67]\n" \
"v_mfma_f32_16x16x32_bf16 v[68:71], a[44:47], v[44:47], v[68:71]\n" \
"ds_read_b128 v[32:35], v12 offset:14336\n" \
"v_mfma_f32_16x16x32_bf16 v[72:75], a[172:175], v[40:43], v[72:75]\n" \
"ds_read_b128 v[36:39], v12 offset:30720\n" \
"v_mfma_f32_16x16x32_bf16 v[76:79], a[172:175], v[44:47], v[76:79]\n" \
"s_waitcnt lgkmcnt(4)\n" \
"v_mfma_f32_16x16x32_bf16 v[64:67], a[48:51], v[16:19], v[64:67]\n" \
"v_mfma_f32_16x16x32_bf16 v[68:71], a[48:51], v[20:23], v[68:71]\n" \
"ds_read_b128 v[40:43], v12 offset:15360\n" \
"v_mfma_f32_16x16x32_bf16 v[72:75], a[176:179], v[16:19], v[72:75]\n" \
"ds_read_b128 v[44:47], v12 offset:31744\n" \
"v_mfma_f32_16x16x32_bf16 v[76:79], a[176:179], v[20:23], v[76:79]\n" \
"s_waitcnt lgkmcnt(4)\n" \
"v_mfma_f32_16x16x32_bf16 v[64:67], a[52:55], v[24:27], v[64:67]\n" \
"s_waitcnt vmcnt(12)\n" \
"s_barrier\n" \
"v_mfma_f32_16x16x32_bf16 v[68:71], a[52:55], v[28:31], v[68:71]\n" \
"ds_read_b128 v[16:19], v12 offset:32768\n" \
"v_mfma_f32_16x16x32_bf16 v[72:75], a[180:183], v[24:27], v[72:75]\n" \
"ds_read_b128 v[20:23], v12 offset:49152\n" \
"v_mfma_f32_16x16x32_bf16 v[76:79], a[180:183], v[28:31], v[76:79]\n" \
"s_waitcnt lgkmcnt(4)\n" \
"v_mfma_f32_16x16x32_bf16 v[64:67], a[56:59], v[32:35], v[64:67]\n" \
"v_mfma_f32_16x16x32_bf16 v[68:71], a[56:59], v[36:39], v[68:71]\n" \
"ds_read_b128 v[24:27], v12 offset:33792\n" \
"v_mfma_f32_16x16x32_bf16 v[72:75], a[184:187], v[32:35], v[72:75]\n" \
"ds_read_b128 v[28:31], v12 offset:50176\n" \
"v_mfma_f32_16x16x32_bf16 v[76:79], a[184:187], v[36:39], v[76:79]\n" \
"s_waitcnt lgkmcnt(4)\n" \
"v_mfma_f32_16x16x32_bf16 v[64:67], a[60:63], v[40:43], v[64:67]\n" \
"v_mfma_f32_16x16x32_bf16 v[68:71], a[60:63], v[44:47], v[68:71]\n" \
"ds_read_b128 v[32:35], v12 offset:34816\n" \
"v_mfma_f32_16x16x32_bf16 v[72:75], a[188:191], v[40:43], v[72:75]\n" \
"ds_read_b128 v[36:39], v12 offset:51200\n" \
"v_mfma_f32_16x16x32_bf16 v[76:79], a[188:191], v[44:47], v[76:79]\n" \
"s_waitcnt lgkmcnt(4)\n" \
"v_mfma_f32_16x16x32_bf16 v[64:67], a[64:67], v[16:19], v[64:67]\n" \
"s_add_u32 s61, s61, 28672\n" \
"s_add_u32 s56, s56, 0x20000\n" \
"s_add_u32 m0, s55, 32768\n" \
"s_nop 0\n" \
"buffer_load_dwordx4 v4, s[36:39], s61 offen offset:0 sc1 lds\n" \
"v_mfma_f32_16x16x32_bf16 v[68:71], a[64:67], v[20:23], v[68:71]\n" \
"ds_read_b128 v[40:43], v12 offset:35840\n" \
"v_mfma_f32_16x16x32_bf16 v[72:75], a[192:195], v[16:19], v[72:75]\n" \
"ds_read_b128 v[44:47], v12 offset:52224\n" \
"v_mfma_f32_16x16x32_bf16 v[76:79], a[192:195], v[20:23], v[76:79]\n" \
"s_waitcnt lgkmcnt(4)\n" \
"v_mfma_f32_16x16x32_bf16 v[64:67], a[68:71], v[24:27], v[64:67]\n" \
"buffer_load_dwordx4 v4, s[36:39], s61 offen offset:1024 sc1 lds\n" \
"v_mfma_f32_16x16x32_bf16 v[68:71], a[68:71], v[28:31], v[68:71]\n" \
"ds_read_b128 v[16:19], v12 offset:36864\n" \
"v_mfma_f32_16x16x32_bf16 v[72:75], a[196:199], v[24:27], v[72:75]\n" \
"ds_read_b128 v[20:23], v12 offset:53248\n" \
"v_mfma_f32_16x16x32_bf16 v[76:79], a[196:199], v[28:31], v[76:79]\n" \
"s_waitcnt lgkmcnt(4)\n" \
"v_mfma_f32_16x16x32_bf16 v[64:67], a[72:75], v[32:35], v[64:67]\n" \
"v_mfma_f32_16x16x32_bf16 v[68:71], a[72:75], v[36:39], v[68:71]\n" \
"ds_read_b128 v[24:27], v12 offset:37888\n" \
"v_mfma_f32_16x16x32_bf16 v[72:75], a[200:203], v[32:35], v[72:75]\n" \
"ds_read_b128 v[28:31], v12 offset:54272\n" \
"v_mfma_f32_16x16x32_bf16 v[76:79], a[200:203], v[36:39], v[76:79]\n" \
"s_waitcnt lgkmcnt(4)\n" \
"v_mfma_f32_16x16x32_bf16 v[64:67], a[76:79], v[40:43], v[64:67]\n" \
"buffer_load_dwordx4 v4, s[36:39], s61 offen offset:2048 sc1 lds\n" \
"v_mfma_f32_16x16x32_bf16 v[68:71], a[76:79], v[44:47], v[68:71]\n" \
"ds_read_b128 v[32:35], v12 offset:38912\n" \
"v_mfma_f32_16x16x32_bf16 v[72:75], a[204:207], v[40:43], v[72:75]\n" \
"ds_read_b128 v[36:39], v12 offset:55296\n" \
"v_mfma_f32_16x16x32_bf16 v[76:79], a[204:207], v[44:47], v[76:79]\n" \
"s_waitcnt lgkmcnt(4)\n" \
"v_mfma_f32_16x16x32_bf16 v[64:67], a[80:83], v[16:19], v[64:67]\n" \
"buffer_load_dwordx4 v4, s[36:39], s61 offen offset:3072 sc1 lds\n" \
"v_mfma_f32_16x16x32_bf16 v[68:71], a[80:83], v[20:23], v[68:71]\n" \
"ds_read_b128 v[40:43], v12 offset:39936\n" \
"v_mfma_f32_16x16x32_bf16 v[72:75], a[208:211], v[16:19], v[72:75]\n" \
"ds_read_b128 v[44:47], v12 offset:56320\n" \
"v_mfma_f32_16x16x32_bf16 v[76:79], a[208:211], v[20:23], v[76:79]\n" \
"s_waitcnt lgkmcnt(4)\n" \
"v_mfma_f32_16x16x32_bf16 v[64:67], a[84:87], v[24:27], v[64:67]\n" \
"v_mfma_f32_16x16x32_bf16 v[68:71], a[84:87], v[28:31], v[68:71]\n" \
"ds_read_b128 v[16:19], v12 offset:40960\n" \
"v_mfma_f32_16x16x32_bf16 v[72:75], a[212:215], v[24:27], v[72:75]\n" \
"ds_read_b128 v[20:23], v12 offset:57344\n" \
"v_mfma_f32_16x16x32_bf16 v[76:79], a[212:215], v[28:31], v[76:79]\n" \
"s_waitcnt lgkmcnt(4)\n" \
"v_mfma_f32_16x16x32_bf16 v[64:67], a[88:91], v[32:35], v[64:67]\n" \
"s_add_u32 m0, s55, 36864\n" \
"s_add_u32 s61, s61, 4096\n" \
"buffer_load_dwordx4 v4, s[36:39], s61 offen offset:0 sc1 lds\n" \
"v_mfma_f32_16x16x32_bf16 v[68:71], a[88:91], v[36:39], v[68:71]\n" \
"ds_read_b128 v[24:27], v12 offset:41984\n" \
"v_mfma_f32_16x16x32_bf16 v[72:75], a[216:219], v[32:35], v[72:75]\n" \
"ds_read_b128 v[28:31], v12 offset:58368\n" \
"v_mfma_f32_16x16x32_bf16 v[76:79], a[216:219], v[36:39], v[76:79]\n" \
"s_waitcnt lgkmcnt(4)\n" \
"v_mfma_f32_16x16x32_bf16 v[64:67], a[92:95], v[40:43], v[64:67]\n" \
"buffer_load_dwordx4 v4, s[36:39], s61 offen offset:1024 sc1 lds\n" \
"v_mfma_f32_16x16x32_bf16 v[68:71], a[92:95], v[44:47], v[68:71]\n" \
"ds_read_b128 v[32:35], v12 offset:43008\n" \
"v_mfma_f32_16x16x32_bf16 v[72:75], a[220:223], v[40:43], v[72:75]\n" \
"ds_read_b128 v[36:39], v12 offset:59392\n" \
"v_mfma_f32_16x16x32_bf16 v[76:79], a[220:223], v[44:47], v[76:79]\n" \
"s_waitcnt lgkmcnt(4)\n" \
"v_mfma_f32_16x16x32_bf16 v[64:67], a[96:99], v[16:19], v[64:67]\n" \
"v_mfma_f32_16x16x32_bf16 v[68:71], a[96:99], v[20:23], v[68:71]\n" \
"ds_read_b128 v[40:43], v12 offset:44032\n" \
"v_mfma_f32_16x16x32_bf16 v[72:75], a[224:227], v[16:19], v[72:75]\n" \
"ds_read_b128 v[44:47], v12 offset:60416\n" \
"v_mfma_f32_16x16x32_bf16 v[76:79], a[224:227], v[20:23], v[76:79]\n" \
"s_waitcnt lgkmcnt(4)\n" \
"v_mfma_f32_16x16x32_bf16 v[64:67], a[100:103], v[24:27], v[64:67]\n" \
"buffer_load_dwordx4 v4, s[36:39], s61 offen offset:2048 sc1 lds\n" \
"v_mfma_f32_16x16x32_bf16 v[68:71], a[100:103], v[28:31], v[68:71]\n" \
"ds_read_b128 v[16:19], v12 offset:45056\n" \
"v_mfma_f32_16x16x32_bf16 v[72:75], a[228:231], v[24:27], v[72:75]\n" \
"ds_read_b128 v[20:23], v12 offset:61440\n" \
"v_mfma_f32_16x16x32_bf16 v[76:79], a[228:231], v[28:31], v[76:79]\n" \
"s_waitcnt lgkmcnt(4)\n" \
"v_mfma_f32_16x16x32_bf16 v[64:67], a[104:107], v[32:35], v[64:67]\n" \
"buffer_load_dwordx4 v4, s[36:39], s61 offen offset:3072 sc1 lds\n" \
"v_mfma_f32_16x16x32_bf16 v[68:71], a[104:107], v[36:39], v[68:71]\n" \
"ds_read_b128 v[24:27], v12 offset:46080\n" \
"v_mfma_f32_16x16x32_bf16 v[72:75], a[232:235], v[32:35], v[72:75]\n" \
"ds_read_b128 v[28:31], v12 offset:62464\n" \
"v_mfma_f32_16x16x32_bf16 v[76:79], a[232:235], v[36:39], v[76:79]\n" \
"s_waitcnt lgkmcnt(4)\n" \
"v_mfma_f32_16x16x32_bf16 v[64:67], a[108:111], v[40:43], v[64:67]\n" \
"s_lshl_b32 s66, s59, s49\n" \
"s_lshr_b32 s65, s66, 8\n" \
"v_mfma_f32_16x16x32_bf16 v[68:71], a[108:111], v[44:47], v[68:71]\n" \
"ds_read_b128 v[32:35], v12 offset:47104\n" \
"v_mfma_f32_16x16x32_bf16 v[72:75], a[236:239], v[40:43], v[72:75]\n" \
"ds_read_b128 v[36:39], v12 offset:63488\n" \
"v_mfma_f32_16x16x32_bf16 v[76:79], a[236:239], v[44:47], v[76:79]\n" \
"s_waitcnt lgkmcnt(4)\n" \
"s_and_b32 s65, s65, 0x2000\n" \
"s_xor_b32 s66, s66, s65\n" \
"v_mfma_f32_16x16x32_bf16 v[64:67], a[112:115], v[16:19], v[64:67]\n" \
"v_mfma_f32_16x16x32_bf16 v[68:71], a[112:115], v[20:23], v[68:71]\n" \
"ds_read_b128 v[40:43], v12 offset:48128\n" \
"v_mfma_f32_16x16x32_bf16 v[72:75], a[240:243], v[16:19], v[72:75]\n" \
"ds_read_b128 v[44:47], v12 offset:64512\n" \
"v_mfma_f32_16x16x32_bf16 v[76:79], a[240:243], v[20:23], v[76:79]\n" \
"s_waitcnt lgkmcnt(4)\n" \
"v_mfma_f32_16x16x32_bf16 v[64:67], a[116:119], v[24:27], v[64:67]\n" \
"s_waitcnt vmcnt(8)\n" \
"s_barrier\n" \
"v_mfma_f32_16x16x32_bf16 v[68:71], a[116:119], v[28:31], v[68:71]\n" \
"ds_read_b128 v[16:19], v10 offset:0\n" \
"v_mfma_f32_16x16x32_bf16 v[72:75], a[244:247], v[24:27], v[72:75]\n" \
"ds_read_b128 v[20:23], v10 offset:16384\n" \
"v_mfma_f32_16x16x32_bf16 v[76:79], a[244:247], v[28:31], v[76:79]\n" \
"s_waitcnt lgkmcnt(4)\n" \
"buffer_store_dword v13, v6, s[36:39], 0 offen sc1\n" \
"v_mfma_f32_16x16x32_bf16 v[64:67], a[120:123], v[32:35], v[64:67]\n" \
"v_cmp_gt_u32_e64 vcc, 16, v15\n" \
"v_mfma_f32_16x16x32_bf16 v[68:71], a[120:123], v[36:39], v[68:71]\n" \
"ds_read_b128 v[24:27], v10 offset:1024\n" \
"v_mfma_f32_16x16x32_bf16 v[72:75], a[248:251], v[32:35], v[72:75]\n" \
"ds_read_b128 v[28:31], v10 offset:17408\n" \
"v_mfma_f32_16x16x32_bf16 v[76:79], a[248:251], v[36:39], v[76:79]\n" \
"s_waitcnt lgkmcnt(4)\n" \
"v_mfma_f32_16x16x32_bf16 v[64:67], a[124:127], v[40:43], v[64:67]\n" \
"s_cbranch_vccnz .Lp1qk_m1_%=\n" \
".Lp1qk_m1k_%=:\n" \
"v_mfma_f32_16x16x32_bf16 v[68:71], a[124:127], v[44:47], v[68:71]\n" \
"ds_read_b128 v[32:35], v10 offset:2048\n" \
"v_mfma_f32_16x16x32_bf16 v[72:75], a[252:255], v[40:43], v[72:75]\n" \
"ds_read_b128 v[36:39], v10 offset:18432\n" \
"v_mfma_f32_16x16x32_bf16 v[76:79], a[252:255], v[44:47], v[76:79]\n" \
"s_waitcnt lgkmcnt(4)\n" \
"s_xor_b32 s57, s66, s64\n" \
"s_add_u32 s58, s57, s50\n" \
"s_add_u32 s59, s59, 1\n" \
"v_mov_b32_e64 v5, v7\n" \
"v_mov_b32_e64 v6, v11\n" \
"s_cmp_ge_u32 s59, s47\n" \
"s_nop 0\n" \
"s_cbranch_scc0 .Lp1qk_loop_%=\n" \
".Lp1qk_out1_%=:\n" \
"s_nop 7\n" \
"s_nop 7\n" \
"v_mul_f32 v64, s51, v64\n" \
"v_mul_f32 v65, s51, v65\n" \
"v_mul_f32 v66, s51, v66\n" \
"v_mul_f32 v67, s51, v67\n" \
"v_mul_f32 v72, s51, v72\n" \
"v_mul_f32 v73, s51, v73\n" \
"v_mul_f32 v74, s51, v74\n" \
"v_mul_f32 v75, s51, v75\n" \
"v_cvt_pk_bf16_f32 v80, v64, v65\n" \
"v_cvt_pk_bf16_f32 v81, v66, v67\n" \
"v_cvt_pk_bf16_f32 v82, v72, v73\n" \
"v_cvt_pk_bf16_f32 v83, v74, v75\n" \
"buffer_store_dwordx4 v[80:83], v5, s[36:39], s57 offen sc1\n" \
"v_mul_f32 v68, s51, v68\n" \
"v_mul_f32 v69, s51, v69\n" \
"v_mul_f32 v70, s51, v70\n" \
"v_mul_f32 v71, s51, v71\n" \
"v_mul_f32 v76, s51, v76\n" \
"v_mul_f32 v77, s51, v77\n" \
"v_mul_f32 v78, s51, v78\n" \
"v_mul_f32 v79, s51, v79\n" \
"v_cvt_pk_bf16_f32 v84, v68, v69\n" \
"v_cvt_pk_bf16_f32 v85, v70, v71\n" \
"v_cvt_pk_bf16_f32 v86, v76, v77\n" \
"v_cvt_pk_bf16_f32 v87, v78, v79\n" \
"buffer_store_dwordx4 v[84:87], v5, s[36:39], s58 offen sc1\n" \
"s_branch .Lp1qk_end_%=\n" \
".Lp1qk_out0_%=:\n" \
"s_nop 7\n" \
"s_nop 7\n" \
"v_mul_f32 v48, s51, v48\n" \
"v_mul_f32 v49, s51, v49\n" \
"v_mul_f32 v50, s51, v50\n" \
"v_mul_f32 v51, s51, v51\n" \
"v_mul_f32 v56, s51, v56\n" \
"v_mul_f32 v57, s51, v57\n" \
"v_mul_f32 v58, s51, v58\n" \
"v_mul_f32 v59, s51, v59\n" \
"v_cvt_pk_bf16_f32 v80, v48, v49\n" \
"v_cvt_pk_bf16_f32 v81, v50, v51\n" \
"v_cvt_pk_bf16_f32 v82, v56, v57\n" \
"v_cvt_pk_bf16_f32 v83, v58, v59\n" \
"buffer_store_dwordx4 v[80:83], v5, s[36:39], s57 offen sc1\n" \
"v_mul_f32 v52, s51, v52\n" \
"v_mul_f32 v53, s51, v53\n" \
"v_mul_f32 v54, s51, v54\n" \
"v_mul_f32 v55, s51, v55\n" \
"v_mul_f32 v60, s51, v60\n" \
"v_mul_f32 v61, s51, v61\n" \
"v_mul_f32 v62, s51, v62\n" \
"v_mul_f32 v63, s51, v63\n" \
"v_cvt_pk_bf16_f32 v84, v52, v53\n" \
"v_cvt_pk_bf16_f32 v85, v54, v55\n" \
"v_cvt_pk_bf16_f32 v86, v60, v61\n" \
"v_cvt_pk_bf16_f32 v87, v62, v63\n" \
"buffer_store_dwordx4 v[84:87], v5, s[36:39], s58 offen sc1\n" \
".Lp1qk_end_%=:\n" \
"s_waitcnt vmcnt(0) lgkmcnt(0)\n" \
"s_barrier\n" \
"v_mov_b32 v13, s59\n" \
"buffer_store_dword v13, v6, s[36:39], 0 offen sc1\n" \
"s_waitcnt vmcnt(0) lgkmcnt(0)\n" \
"s_branch .Lp1qk_x_%=\n" \
".Lp1qk_l0_%=:\n" \
"s_nop 7\n" \
"s_nop 7\n" \
"s_waitcnt vmcnt(0) lgkmcnt(0)\n" \
"s_mov_b32 s62, 0\n" \
".Lp1qk_l0a_%=:\n" \
"buffer_load_dword v15, v14, s[36:39], s63 offen sc1\n" \
"s_waitcnt vmcnt(0)\n" \
"v_cmp_gt_u32 vcc, 16, v15\n" \
"s_nop 4\n" \
"s_cbranch_vccz .Lp1qk_l0k_%=\n" \
"s_sleep 4\n" \
"s_add_u32 s62, s62, 1\n" \
"s_cmp_lt_u32 s62, 0x10000\n" \
"s_cbranch_scc1 .Lp1qk_l0a_%=\n" \
"v_mov_b32 v15, 256\n" \
"buffer_store_dword v15, v15, s[36:39], 0 offen sc1\n" \
"s_waitcnt vmcnt(0) lgkmcnt(0)\n" \
"s_branch .Lp1qk_l0k_%=\n" \
".Lp1qk_l1_%=:\n" \
"s_nop 7\n" \
"s_nop 7\n" \
"s_waitcnt vmcnt(0) lgkmcnt(0)\n" \
"s_mov_b32 s62, 0\n" \
".Lp1qk_l1a_%=:\n" \
"buffer_load_dword v15, v14, s[36:39], s63 offen sc1\n" \
"s_waitcnt vmcnt(0)\n" \
"v_cmp_gt_u32 vcc, 16, v15\n" \
"s_nop 4\n" \
"s_cbranch_vccz .Lp1qk_l1k_%=\n" \
"s_sleep 4\n" \
"s_add_u32 s62, s62, 1\n" \
"s_cmp_lt_u32 s62, 0x10000\n" \
"s_cbranch_scc1 .Lp1qk_l1a_%=\n" \
"v_mov_b32 v15, 256\n" \
"buffer_store_dword v15, v15, s[36:39], 0 offen sc1\n" \
"s_waitcnt vmcnt(0) lgkmcnt(0)\n" \
"s_branch .Lp1qk_l1k_%=\n" \
".Lp1qk_m0_%=:\n" \
"s_nop 7\n" \
"s_nop 7\n" \
"s_waitcnt vmcnt(0) lgkmcnt(0)\n" \
"s_mov_b32 s62, 0\n" \
".Lp1qk_m0a_%=:\n" \
"buffer_load_dword v15, v14, s[36:39], s63 offen sc1\n" \
"s_waitcnt vmcnt(0)\n" \
"v_cmp_gt_u32 vcc, 16, v15\n" \
"s_nop 4\n" \
"s_cbranch_vccz .Lp1qk_m0k_%=\n" \
"s_sleep 4\n" \
"s_add_u32 s62, s62, 1\n" \
"s_cmp_lt_u32 s62, 0x10000\n" \
"s_cbranch_scc1 .Lp1qk_m0a_%=\n" \
"v_mov_b32 v15, 256\n" \
"buffer_store_dword v15, v15, s[36:39], 0 offen sc1\n" \
"s_waitcnt vmcnt(0) lgkmcnt(0)\n" \
"s_branch .Lp1qk_m0k_%=\n" \
".Lp1qk_m1_%=:\n" \
"s_nop 7\n" \
"s_nop 7\n" \
"s_waitcnt vmcnt(0) lgkmcnt(0)\n" \
"s_mov_b32 s62, 0\n" \
".Lp1qk_m1a_%=:\n" \
"buffer_load_dword v15, v14, s[36:39], s63 offen sc1\n" \
"s_waitcnt vmcnt(0)\n" \
"v_cmp_gt_u32 vcc, 16, v15\n" \
"s_nop 4\n" \
"s_cbranch_vccz .Lp1qk_m1k_%=\n" \
"s_sleep 4\n" \
"s_add_u32 s62, s62, 1\n" \
"s_cmp_lt_u32 s62, 0x10000\n" \
"s_cbranch_scc1 .Lp1qk_m1a_%=\n" \
"v_mov_b32 v15, 256\n" \
"buffer_store_dword v15, v15, s[36:39], 0 offen sc1\n" \
"s_waitcnt vmcnt(0) lgkmcnt(0)\n" \
"s_branch .Lp1qk_m1k_%=\n" \
".Lp1qk_x_%=:\n"
#define ASM_P1_V \
"s_nop 7\n" \
"s_nop 7\n" \
"s_lshr_b32 s52, s48, 1\n" \
"s_lshl_b32 s52, s52, 13\n" \
"s_and_b32 s53, s48, 1\n" \
"s_lshl_b32 s53, s53, 10\n" \
"s_add_u32 s52, s52, s53\n" \
"s_mov_b32 s54, 0\n" \
"s_add_u32 m0, s52, 0\n" \
"s_nop 0\n" \
"buffer_load_dwordx4 v1, s[40:43], s54 offen nt lds\n" \
"s_add_u32 s54, s54, s44\n" \
"s_add_u32 m0, s52, 2048\n" \
"s_nop 0\n" \
"buffer_load_dwordx4 v1, s[40:43], s54 offen nt lds\n" \
"s_add_u32 s54, s54, s44\n" \
"s_add_u32 m0, s52, 4096\n" \
"s_nop 0\n" \
"buffer_load_dwordx4 v1, s[40:43], s54 offen nt lds\n" \
"s_add_u32 s54, s54, s44\n" \
"s_add_u32 m0, s52, 6144\n" \
"s_nop 0\n" \
"buffer_load_dwordx4 v1, s[40:43], s54 offen nt lds\n" \
"s_add_u32 s54, s54, s44\n" \
"s_add_u32 m0, s52, 16384\n" \
"s_nop 0\n" \
"buffer_load_dwordx4 v1, s[40:43], s54 offen nt lds\n" \
"s_add_u32 s54, s54, s44\n" \
"s_add_u32 m0, s52, 18432\n" \
"s_nop 0\n" \
"buffer_load_dwordx4 v1, s[40:43], s54 offen nt lds\n" \
"s_add_u32 s54, s54, s44\n" \
"s_add_u32 m0, s52, 20480\n" \
"s_nop 0\n" \
"buffer_load_dwordx4 v1, s[40:43], s54 offen nt lds\n" \
"s_add_u32 s54, s54, s44\n" \
"s_add_u32 m0, s52, 22528\n" \
"s_nop 0\n" \
"buffer_load_dwordx4 v1, s[40:43], s54 offen nt lds\n" \
"s_add_u32 s54, s54, s44\n" \
"s_add_u32 m0, s52, 32768\n" \
"s_nop 0\n" \
"buffer_load_dwordx4 v1, s[40:43], s54 offen nt lds\n" \
"s_add_u32 s54, s54, s44\n" \
"s_add_u32 m0, s52, 34816\n" \
"s_nop 0\n" \
"buffer_load_dwordx4 v1, s[40:43], s54 offen nt lds\n" \
"s_add_u32 s54, s54, s44\n" \
"s_add_u32 m0, s52, 36864\n" \
"s_nop 0\n" \
"buffer_load_dwordx4 v1, s[40:43], s54 offen nt lds\n" \
"s_add_u32 s54, s54, s44\n" \
"s_add_u32 m0, s52, 38912\n" \
"s_nop 0\n" \
"buffer_load_dwordx4 v1, s[40:43], s54 offen nt lds\n" \
"s_add_u32 s54, s54, s44\n" \
"s_add_u32 m0, s52, 49152\n" \
"s_nop 0\n" \
"buffer_load_dwordx4 v1, s[40:43], s54 offen nt lds\n" \
"s_add_u32 s54, s54, s44\n" \
"s_add_u32 m0, s52, 51200\n" \
"s_nop 0\n" \
"buffer_load_dwordx4 v1, s[40:43], s54 offen nt lds\n" \
"s_add_u32 s54, s54, s44\n" \
"s_add_u32 m0, s52, 53248\n" \
"s_nop 0\n" \
"buffer_load_dwordx4 v1, s[40:43], s54 offen nt lds\n" \
"s_add_u32 s54, s54, s44\n" \
"s_add_u32 m0, s52, 55296\n" \
"s_nop 0\n" \
"buffer_load_dwordx4 v1, s[40:43], s54 offen nt lds\n" \
"s_add_u32 s54, s54, s44\n" \
"s_add_u32 m0, s52, 65536\n" \
"s_nop 0\n" \
"buffer_load_dwordx4 v1, s[40:43], s54 offen nt lds\n" \
"s_add_u32 s54, s54, s44\n" \
"s_add_u32 m0, s52, 67584\n" \
"s_nop 0\n" \
"buffer_load_dwordx4 v1, s[40:43], s54 offen nt lds\n" \
"s_add_u32 s54, s54, s44\n" \
"s_add_u32 m0, s52, 69632\n" \
"s_nop 0\n" \
"buffer_load_dwordx4 v1, s[40:43], s54 offen nt lds\n" \
"s_add_u32 s54, s54, s44\n" \
"s_add_u32 m0, s52, 71680\n" \
"s_nop 0\n" \
"buffer_load_dwordx4 v1, s[40:43], s54 offen nt lds\n" \
"s_add_u32 s54, s54, s44\n" \
"s_add_u32 m0, s52, 81920\n" \
"s_nop 0\n" \
"buffer_load_dwordx4 v1, s[40:43], s54 offen nt lds\n" \
"s_add_u32 s54, s54, s44\n" \
"s_add_u32 m0, s52, 83968\n" \
"s_nop 0\n" \
"buffer_load_dwordx4 v1, s[40:43], s54 offen nt lds\n" \
"s_add_u32 s54, s54, s44\n" \
"s_add_u32 m0, s52, 86016\n" \
"s_nop 0\n" \
"buffer_load_dwordx4 v1, s[40:43], s54 offen nt lds\n" \
"s_add_u32 s54, s54, s44\n" \
"s_add_u32 m0, s52, 88064\n" \
"s_nop 0\n" \
"buffer_load_dwordx4 v1, s[40:43], s54 offen nt lds\n" \
"s_add_u32 s54, s54, s44\n" \
"s_add_u32 m0, s52, 98304\n" \
"s_nop 0\n" \
"buffer_load_dwordx4 v1, s[40:43], s54 offen nt lds\n" \
"s_add_u32 s54, s54, s44\n" \
"s_add_u32 m0, s52, 100352\n" \
"s_nop 0\n" \
"buffer_load_dwordx4 v1, s[40:43], s54 offen nt lds\n" \
"s_add_u32 s54, s54, s44\n" \
"s_add_u32 m0, s52, 102400\n" \
"s_nop 0\n" \
"buffer_load_dwordx4 v1, s[40:43], s54 offen nt lds\n" \
"s_add_u32 s54, s54, s44\n" \
"s_add_u32 m0, s52, 104448\n" \
"s_nop 0\n" \
"buffer_load_dwordx4 v1, s[40:43], s54 offen nt lds\n" \
"s_add_u32 s54, s54, s44\n" \
"s_add_u32 m0, s52, 114688\n" \
"s_nop 0\n" \
"buffer_load_dwordx4 v1, s[40:43], s54 offen nt lds\n" \
"s_add_u32 s54, s54, s44\n" \
"s_add_u32 m0, s52, 116736\n" \
"s_nop 0\n" \
"buffer_load_dwordx4 v1, s[40:43], s54 offen nt lds\n" \
"s_add_u32 s54, s54, s44\n" \
"s_add_u32 m0, s52, 118784\n" \
"s_nop 0\n" \
"buffer_load_dwordx4 v1, s[40:43], s54 offen nt lds\n" \
"s_add_u32 s54, s54, s44\n" \
"s_add_u32 m0, s52, 120832\n" \
"s_nop 0\n" \
"buffer_load_dwordx4 v1, s[40:43], s54 offen nt lds\n" \
"s_add_u32 s54, s54, s44\n" \
"v_add_u32 v8, 0x10000, v2\n" \
"v_add_u32 v9, 0x10000, v3\n" \
"s_waitcnt vmcnt(28)\n" \
"s_barrier\n" \
"ds_read_b64_tr_b16 a[0:1], v2 offset:0\n" \
"ds_read_b64_tr_b16 a[2:3], v2 offset:512\n" \
"ds_read_b64_tr_b16 a[128:129], v3 offset:0\n" \
"ds_read_b64_tr_b16 a[130:131], v3 offset:512\n" \
"ds_read_b64_tr_b16 a[4:5], v2 offset:4096\n" \
"ds_read_b64_tr_b16 a[6:7], v2 offset:4608\n" \
"ds_read_b64_tr_b16 a[132:133], v3 offset:4096\n" \
"ds_read_b64_tr_b16 a[134:135], v3 offset:4608\n" \
"s_waitcnt lgkmcnt(0)\n" \
"s_barrier\n" \
"s_add_u32 m0, s52, 0\n" \
"s_nop 0\n" \
"buffer_load_dwordx4 v1, s[40:43], s54 offen nt lds\n" \
"s_add_u32 s54, s54, s44\n" \
"s_add_u32 m0, s52, 2048\n" \
"s_nop 0\n" \
"buffer_load_dwordx4 v1, s[40:43], s54 offen nt lds\n" \
"s_add_u32 s54, s54, s44\n" \
"s_add_u32 m0, s52, 4096\n" \
"s_nop 0\n" \
"buffer_load_dwordx4 v1, s[40:43], s54 offen nt lds\n" \
"s_add_u32 s54, s54, s44\n" \
"s_add_u32 m0, s52, 6144\n" \
"s_nop 0\n" \
"buffer_load_dwordx4 v1, s[40:43], s54 offen nt lds\n" \
"s_add_u32 s54, s54, s44\n" \
"s_waitcnt vmcnt(28)\n" \
"s_barrier\n" \
"ds_read_b64_tr_b16 a[8:9], v2 offset:16384\n" \
"ds_read_b64_tr_b16 a[10:11], v2 offset:16896\n" \
"ds_read_b64_tr_b16 a[136:137], v3 offset:16384\n" \
"ds_read_b64_tr_b16 a[138:139], v3 offset:16896\n" \
"ds_read_b64_tr_b16 a[12:13], v2 offset:20480\n" \
"ds_read_b64_tr_b16 a[14:15], v2 offset:20992\n" \
"ds_read_b64_tr_b16 a[140:141], v3 offset:20480\n" \
"ds_read_b64_tr_b16 a[142:143], v3 offset:20992\n" \
"s_waitcnt lgkmcnt(0)\n" \
"s_barrier\n" \
"s_add_u32 m0, s52, 16384\n" \
"s_nop 0\n" \
"buffer_load_dwordx4 v1, s[40:43], s54 offen nt lds\n" \
"s_add_u32 s54, s54, s44\n" \
"s_add_u32 m0, s52, 18432\n" \
"s_nop 0\n" \
"buffer_load_dwordx4 v1, s[40:43], s54 offen nt lds\n" \
"s_add_u32 s54, s54, s44\n" \
"s_add_u32 m0, s52, 20480\n" \
"s_nop 0\n" \
"buffer_load_dwordx4 v1, s[40:43], s54 offen nt lds\n" \
"s_add_u32 s54, s54, s44\n" \
"s_add_u32 m0, s52, 22528\n" \
"s_nop 0\n" \
"buffer_load_dwordx4 v1, s[40:43], s54 offen nt lds\n" \
"s_add_u32 s54, s54, s44\n" \
"s_waitcnt vmcnt(28)\n" \
"s_barrier\n" \
"ds_read_b64_tr_b16 a[16:17], v2 offset:32768\n" \
"ds_read_b64_tr_b16 a[18:19], v2 offset:33280\n" \
"ds_read_b64_tr_b16 a[144:145], v3 offset:32768\n" \
"ds_read_b64_tr_b16 a[146:147], v3 offset:33280\n" \
"ds_read_b64_tr_b16 a[20:21], v2 offset:36864\n" \
"ds_read_b64_tr_b16 a[22:23], v2 offset:37376\n" \
"ds_read_b64_tr_b16 a[148:149], v3 offset:36864\n" \
"ds_read_b64_tr_b16 a[150:151], v3 offset:37376\n" \
"s_waitcnt lgkmcnt(0)\n" \
"s_barrier\n" \
"s_add_u32 m0, s52, 32768\n" \
"s_nop 0\n" \
"buffer_load_dwordx4 v1, s[40:43], s54 offen nt lds\n" \
"s_add_u32 s54, s54, s44\n" \
"s_add_u32 m0, s52, 34816\n" \
"s_nop 0\n" \
"buffer_load_dwordx4 v1, s[40:43], s54 offen nt lds\n" \
"s_add_u32 s54, s54, s44\n" \
"s_add_u32 m0, s52, 36864\n" \
"s_nop 0\n" \
"buffer_load_dwordx4 v1, s[40:43], s54 offen nt lds\n" \
"s_add_u32 s54, s54, s44\n" \
"s_add_u32 m0, s52, 38912\n" \
"s_nop 0\n" \
"buffer_load_dwordx4 v1, s[40:43], s54 offen nt lds\n" \
"s_add_u32 s54, s54, s44\n" \
"s_waitcnt vmcnt(28)\n" \
"s_barrier\n" \
"ds_read_b64_tr_b16 a[24:25], v2 offset:49152\n" \
"ds_read_b64_tr_b16 a[26:27], v2 offset:49664\n" \
"ds_read_b64_tr_b16 a[152:153], v3 offset:49152\n" \
"ds_read_b64_tr_b16 a[154:155], v3 offset:49664\n" \
"ds_read_b64_tr_b16 a[28:29], v2 offset:53248\n" \
"ds_read_b64_tr_b16 a[30:31], v2 offset:53760\n" \
"ds_read_b64_tr_b16 a[156:157], v3 offset:53248\n" \
"ds_read_b64_tr_b16 a[158:159], v3 offset:53760\n" \
"s_waitcnt lgkmcnt(0)\n" \
"s_barrier\n" \
"s_add_u32 m0, s52, 49152\n" \
"s_nop 0\n" \
"buffer_load_dwordx4 v1, s[40:43], s54 offen nt lds\n" \
"s_add_u32 s54, s54, s44\n" \
"s_add_u32 m0, s52, 51200\n" \
"s_nop 0\n" \
"buffer_load_dwordx4 v1, s[40:43], s54 offen nt lds\n" \
"s_add_u32 s54, s54, s44\n" \
"s_add_u32 m0, s52, 53248\n" \
"s_nop 0\n" \
"buffer_load_dwordx4 v1, s[40:43], s54 offen nt lds\n" \
"s_add_u32 s54, s54, s44\n" \
"s_add_u32 m0, s52, 55296\n" \
"s_nop 0\n" \
"buffer_load_dwordx4 v1, s[40:43], s54 offen nt lds\n" \
"s_add_u32 s54, s54, s44\n" \
"s_waitcnt vmcnt(28)\n" \
"s_barrier\n" \
"ds_read_b64_tr_b16 a[32:33], v8 offset:0\n" \
"ds_read_b64_tr_b16 a[34:35], v8 offset:512\n" \
"ds_read_b64_tr_b16 a[160:161], v9 offset:0\n" \
"ds_read_b64_tr_b16 a[162:163], v9 offset:512\n" \
"ds_read_b64_tr_b16 a[36:37], v8 offset:4096\n" \
"ds_read_b64_tr_b16 a[38:39], v8 offset:4608\n" \
"ds_read_b64_tr_b16 a[164:165], v9 offset:4096\n" \
"ds_read_b64_tr_b16 a[166:167], v9 offset:4608\n" \
"s_waitcnt lgkmcnt(0)\n" \
"s_barrier\n" \
"s_add_u32 m0, s52, 65536\n" \
"s_nop 0\n" \
"buffer_load_dwordx4 v1, s[40:43], s54 offen nt lds\n" \
"s_add_u32 s54, s54, s44\n" \
"s_add_u32 m0, s52, 67584\n" \
"s_nop 0\n" \
"buffer_load_dwordx4 v1, s[40:43], s54 offen nt lds\n" \
"s_add_u32 s54, s54, s44\n" \
"s_add_u32 m0, s52, 69632\n" \
"s_nop 0\n" \
"buffer_load_dwordx4 v1, s[40:43], s54 offen nt lds\n" \
"s_add_u32 s54, s54, s44\n" \
"s_add_u32 m0, s52, 71680\n" \
"s_nop 0\n" \
"buffer_load_dwordx4 v1, s[40:43], s54 offen nt lds\n" \
"s_add_u32 s54, s54, s44\n" \
"s_waitcnt vmcnt(28)\n" \
"s_barrier\n" \
"ds_read_b64_tr_b16 a[40:41], v8 offset:16384\n" \
"ds_read_b64_tr_b16 a[42:43], v8 offset:16896\n" \
"ds_read_b64_tr_b16 a[168:169], v9 offset:16384\n" \
"ds_read_b64_tr_b16 a[170:171], v9 offset:16896\n" \
"ds_read_b64_tr_b16 a[44:45], v8 offset:20480\n" \
"ds_read_b64_tr_b16 a[46:47], v8 offset:20992\n" \
"ds_read_b64_tr_b16 a[172:173], v9 offset:20480\n" \
"ds_read_b64_tr_b16 a[174:175], v9 offset:20992\n" \
"s_waitcnt lgkmcnt(0)\n" \
"s_barrier\n" \
"s_add_u32 m0, s52, 81920\n" \
"s_nop 0\n" \
"buffer_load_dwordx4 v1, s[40:43], s54 offen nt lds\n" \
"s_add_u32 s54, s54, s44\n" \
"s_add_u32 m0, s52, 83968\n" \
"s_nop 0\n" \
"buffer_load_dwordx4 v1, s[40:43], s54 offen nt lds\n" \
"s_add_u32 s54, s54, s44\n" \
"s_add_u32 m0, s52, 86016\n" \
"s_nop 0\n" \
"buffer_load_dwordx4 v1, s[40:43], s54 offen nt lds\n" \
"s_add_u32 s54, s54, s44\n" \
"s_add_u32 m0, s52, 88064\n" \
"s_nop 0\n" \
"buffer_load_dwordx4 v1, s[40:43], s54 offen nt lds\n" \
"s_add_u32 s54, s54, s44\n" \
"s_waitcnt vmcnt(28)\n" \
"s_barrier\n" \
"ds_read_b64_tr_b16 a[48:49], v8 offset:32768\n" \
"ds_read_b64_tr_b16 a[50:51], v8 offset:33280\n" \
"ds_read_b64_tr_b16 a[176:177], v9 offset:32768\n" \
"ds_read_b64_tr_b16 a[178:179], v9 offset:33280\n" \
"ds_read_b64_tr_b16 a[52:53], v8 offset:36864\n" \
"ds_read_b64_tr_b16 a[54:55], v8 offset:37376\n" \
"ds_read_b64_tr_b16 a[180:181], v9 offset:36864\n" \
"ds_read_b64_tr_b16 a[182:183], v9 offset:37376\n" \
"s_waitcnt lgkmcnt(0)\n" \
"s_barrier\n" \
"s_add_u32 m0, s52, 98304\n" \
"s_nop 0\n" \
"buffer_load_dwordx4 v1, s[40:43], s54 offen nt lds\n" \
"s_add_u32 s54, s54, s44\n" \
"s_add_u32 m0, s52, 100352\n" \
"s_nop 0\n" \
"buffer_load_dwordx4 v1, s[40:43], s54 offen nt lds\n" \
"s_add_u32 s54, s54, s44\n" \
"s_add_u32 m0, s52, 102400\n" \
"s_nop 0\n" \
"buffer_load_dwordx4 v1, s[40:43], s54 offen nt lds\n" \
"s_add_u32 s54, s54, s44\n" \
"s_add_u32 m0, s52, 104448\n" \
"s_nop 0\n" \
"buffer_load_dwordx4 v1, s[40:43], s54 offen nt lds\n" \
"s_add_u32 s54, s54, s44\n" \
"s_waitcnt vmcnt(28)\n" \
"s_barrier\n" \
"ds_read_b64_tr_b16 a[56:57], v8 offset:49152\n" \
"ds_read_b64_tr_b16 a[58:59], v8 offset:49664\n" \
"ds_read_b64_tr_b16 a[184:185], v9 offset:49152\n" \
"ds_read_b64_tr_b16 a[186:187], v9 offset:49664\n" \
"ds_read_b64_tr_b16 a[60:61], v8 offset:53248\n" \
"ds_read_b64_tr_b16 a[62:63], v8 offset:53760\n" \
"ds_read_b64_tr_b16 a[188:189], v9 offset:53248\n" \
"ds_read_b64_tr_b16 a[190:191], v9 offset:53760\n" \
"s_waitcnt lgkmcnt(0)\n" \
"s_barrier\n" \
"s_add_u32 m0, s52, 114688\n" \
"s_nop 0\n" \
"buffer_load_dwordx4 v1, s[40:43], s54 offen nt lds\n" \
"s_add_u32 s54, s54, s44\n" \
"s_add_u32 m0, s52, 116736\n" \
"s_nop 0\n" \
"buffer_load_dwordx4 v1, s[40:43], s54 offen nt lds\n" \
"s_add_u32 s54, s54, s44\n" \
"s_add_u32 m0, s52, 118784\n" \
"s_nop 0\n" \
"buffer_load_dwordx4 v1, s[40:43], s54 offen nt lds\n" \
"s_add_u32 s54, s54, s44\n" \
"s_add_u32 m0, s52, 120832\n" \
"s_nop 0\n" \
"buffer_load_dwordx4 v1, s[40:43], s54 offen nt lds\n" \
"s_add_u32 s54, s54, s44\n" \
"s_waitcnt vmcnt(28)\n" \
"s_barrier\n" \
"ds_read_b64_tr_b16 a[64:65], v2 offset:0\n" \
"ds_read_b64_tr_b16 a[66:67], v2 offset:512\n" \
"ds_read_b64_tr_b16 a[192:193], v3 offset:0\n" \
"ds_read_b64_tr_b16 a[194:195], v3 offset:512\n" \
"ds_read_b64_tr_b16 a[68:69], v2 offset:4096\n" \
"ds_read_b64_tr_b16 a[70:71], v2 offset:4608\n" \
"ds_read_b64_tr_b16 a[196:197], v3 offset:4096\n" \
"ds_read_b64_tr_b16 a[198:199], v3 offset:4608\n" \
"s_waitcnt vmcnt(24)\n" \
"s_barrier\n" \
"ds_read_b64_tr_b16 a[72:73], v2 offset:16384\n" \
"ds_read_b64_tr_b16 a[74:75], v2 offset:16896\n" \
"ds_read_b64_tr_b16 a[200:201], v3 offset:16384\n" \
"ds_read_b64_tr_b16 a[202:203], v3 offset:16896\n" \
"ds_read_b64_tr_b16 a[76:77], v2 offset:20480\n" \
"ds_read_b64_tr_b16 a[78:79], v2 offset:20992\n" \
"ds_read_b64_tr_b16 a[204:205], v3 offset:20480\n" \
"ds_read_b64_tr_b16 a[206:207], v3 offset:20992\n" \
"s_waitcnt vmcnt(20)\n" \
"s_barrier\n" \
"ds_read_b64_tr_b16 a[80:81], v2 offset:32768\n" \
"ds_read_b64_tr_b16 a[82:83], v2 offset:33280\n" \
"ds_read_b64_tr_b16 a[208:209], v3 offset:32768\n" \
"ds_read_b64_tr_b16 a[210:211], v3 offset:33280\n" \
"ds_read_b64_tr_b16 a[84:85], v2 offset:36864\n" \
"ds_read_b64_tr_b16 a[86:87], v2 offset:37376\n" \
"ds_read_b64_tr_b16 a[212:213], v3 offset:36864\n" \
"ds_read_b64_tr_b16 a[214:215], v3 offset:37376\n" \
"s_waitcnt vmcnt(16)\n" \
"s_barrier\n" \
"ds_read_b64_tr_b16 a[88:89], v2 offset:49152\n" \
"ds_read_b64_tr_b16 a[90:91], v2 offset:49664\n" \
"ds_read_b64_tr_b16 a[216:217], v3 offset:49152\n" \
"ds_read_b64_tr_b16 a[218:219], v3 offset:49664\n" \
"ds_read_b64_tr_b16 a[92:93], v2 offset:53248\n" \
"ds_read_b64_tr_b16 a[94:95], v2 offset:53760\n" \
"ds_read_b64_tr_b16 a[220:221], v3 offset:53248\n" \
"ds_read_b64_tr_b16 a[222:223], v3 offset:53760\n" \
"s_waitcnt vmcnt(12)\n" \
"s_barrier\n" \
"ds_read_b64_tr_b16 a[96:97], v8 offset:0\n" \
"ds_read_b64_tr_b16 a[98:99], v8 offset:512\n" \
"ds_read_b64_tr_b16 a[224:225], v9 offset:0\n" \
"ds_read_b64_tr_b16 a[226:227], v9 offset:512\n" \
"ds_read_b64_tr_b16 a[100:101], v8 offset:4096\n" \
"ds_read_b64_tr_b16 a[102:103], v8 offset:4608\n" \
"ds_read_b64_tr_b16 a[228:229], v9 offset:4096\n" \
"ds_read_b64_tr_b16 a[230:231], v9 offset:4608\n" \
"s_waitcnt vmcnt(8)\n" \
"s_barrier\n" \
"ds_read_b64_tr_b16 a[104:105], v8 offset:16384\n" \
"ds_read_b64_tr_b16 a[106:107], v8 offset:16896\n" \
"ds_read_b64_tr_b16 a[232:233], v9 offset:16384\n" \
"ds_read_b64_tr_b16 a[234:235], v9 offset:16896\n" \
"ds_read_b64_tr_b16 a[108:109], v8 offset:20480\n" \
"ds_read_b64_tr_b16 a[110:111], v8 offset:20992\n" \
"ds_read_b64_tr_b16 a[236:237], v9 offset:20480\n" \
"ds_read_b64_tr_b16 a[238:239], v9 offset:20992\n" \
"s_waitcnt vmcnt(4)\n" \
"s_barrier\n" \
"ds_read_b64_tr_b16 a[112:113], v8 offset:32768\n" \
"ds_read_b64_tr_b16 a[114:115], v8 offset:33280\n" \
"ds_read_b64_tr_b16 a[240:241], v9 offset:32768\n" \
"ds_read_b64_tr_b16 a[242:243], v9 offset:33280\n" \
"ds_read_b64_tr_b16 a[116:117], v8 offset:36864\n" \
"ds_read_b64_tr_b16 a[118:119], v8 offset:37376\n" \
"ds_read_b64_tr_b16 a[244:245], v9 offset:36864\n" \
"ds_read_b64_tr_b16 a[246:247], v9 offset:37376\n" \
"s_waitcnt vmcnt(0)\n" \
"s_barrier\n" \
"ds_read_b64_tr_b16 a[120:121], v8 offset:49152\n" \
"ds_read_b64_tr_b16 a[122:123], v8 offset:49664\n" \
"ds_read_b64_tr_b16 a[248:249], v9 offset:49152\n" \
"ds_read_b64_tr_b16 a[250:251], v9 offset:49664\n" \
"ds_read_b64_tr_b16 a[124:125], v8 offset:53248\n" \
"ds_read_b64_tr_b16 a[126:127], v8 offset:53760\n" \
"ds_read_b64_tr_b16 a[252:253], v9 offset:53248\n" \
"ds_read_b64_tr_b16 a[254:255], v9 offset:53760\n" \
"s_waitcnt lgkmcnt(0)\n" \
"s_barrier\n" \
"v_mov_b32 v14, 4096\n" \
"s_mov_b32 s59, s46\n" \
"s_add_u32 s63, s59, 0\n" \
"s_min_u32 s63, s63, 63\n" \
"s_lshr_b32 s63, s63, 3\n" \
"s_lshl_b32 s63, s63, 7\n" \
"s_branch .Lp1v_l0_%=\n" \
".Lp1v_l0k_%=:\n" \
"s_add_u32 s63, s59, 1\n" \
"s_min_u32 s63, s63, 63\n" \
"s_lshr_b32 s63, s63, 3\n" \
"s_lshl_b32 s63, s63, 7\n" \
"s_branch .Lp1v_l1_%=\n" \
".Lp1v_l1k_%=:\n" \
"s_lshl_b32 s55, s48, 13\n" \
"s_lshl_b32 s56, s46, 17\n" \
"s_add_u32 s56, s56, s45\n" \
"s_lshl_b32 s60, s47, 17\n" \
"s_sub_u32 s60, s60, 0x20000\n" \
"s_add_u32 s60, s60, s45\n" \
"s_mov_b32 s57, 0\n" \
"s_mov_b32 s58, 0\n" \
"v_mov_b32 v7, v5\n" \
"v_mov_b32 v5, 0x7ffffff0\n" \
"v_mov_b32 v10, v0\n" \
"v_mov_b32 v11, v6\n" \
"v_mov_b32 v6, 0x7ffffff0\n" \
"s_mov_b32 s59, s46\n" \
"s_min_u32 s61, s56, s60\n" \
"s_lshr_b32 s65, s61, 8\n" \
"s_and_b32 s65, s65, 0x2000\n" \
"s_xor_b32 s61, s61, s65\n" \
"s_add_u32 m0, s55, 0\n" \
"s_nop 0\n" \
"buffer_load_dwordx4 v4, s[36:39], s61 offen offset:0 sc1 lds\n" \
"buffer_load_dwordx4 v4, s[36:39], s61 offen offset:1024 sc1 lds\n" \
"buffer_load_dwordx4 v4, s[36:39], s61 offen offset:2048 sc1 lds\n" \
"buffer_load_dwordx4 v4, s[36:39], s61 offen offset:3072 sc1 lds\n" \
"s_add_u32 m0, s55, 4096\n" \
"s_add_u32 s61, s61, 4096\n" \
"buffer_load_dwordx4 v4, s[36:39], s61 offen offset:0 sc1 lds\n" \
"buffer_load_dwordx4 v4, s[36:39], s61 offen offset:1024 sc1 lds\n" \
"buffer_load_dwordx4 v4, s[36:39], s61 offen offset:2048 sc1 lds\n" \
"buffer_load_dwordx4 v4, s[36:39], s61 offen offset:3072 sc1 lds\n" \
"s_add_u32 s61, s61, 28672\n" \
"s_add_u32 s56, s56, 0x20000\n" \
"s_add_u32 m0, s55, 32768\n" \
"s_nop 0\n" \
"buffer_load_dwordx4 v4, s[36:39], s61 offen offset:0 sc1 lds\n" \
"buffer_load_dwordx4 v4, s[36:39], s61 offen offset:1024 sc1 lds\n" \
"buffer_load_dwordx4 v4, s[36:39], s61 offen offset:2048 sc1 lds\n" \
"buffer_load_dwordx4 v4, s[36:39], s61 offen offset:3072 sc1 lds\n" \
"s_add_u32 m0, s55, 36864\n" \
"s_add_u32 s61, s61, 4096\n" \
"buffer_load_dwordx4 v4, s[36:39], s61 offen offset:0 sc1 lds\n" \
"buffer_load_dwordx4 v4, s[36:39], s61 offen offset:1024 sc1 lds\n" \
"buffer_load_dwordx4 v4, s[36:39], s61 offen offset:2048 sc1 lds\n" \
"buffer_load_dwordx4 v4, s[36:39], s61 offen offset:3072 sc1 lds\n" \
"s_mov_b32 s70, 0\n" \
"s_mov_b32 s72, 0\n" \
"s_waitcnt vmcnt(0) lgkmcnt(0)\n" \
"s_barrier\n" \
"v_add_u32 v12, 0x10000, v10\n" \
"ds_read_b128 v[16:19], v10 offset:0\n" \
"ds_read_b128 v[20:23], v10 offset:16384\n" \
"ds_read_b128 v[24:27], v10 offset:1024\n" \
"ds_read_b128 v[28:31], v10 offset:17408\n" \
"ds_read_b128 v[32:35], v10 offset:2048\n" \
"ds_read_b128 v[36:39], v10 offset:18432\n" \
"s_waitcnt lgkmcnt(4)\n" \
"s_nop 7\n" \
"s_nop 7\n" \
".p2align 6\n" \
".Lp1v_loop_%=:\n" \
"v_mfma_f32_16x16x32_bf16 v[48:51], v[16:19], a[0:3], 0\n" \
"s_min_u32 s61, s56, s60\n" \
"s_lshr_b32 s65, s61, 8\n" \
"s_and_b32 s65, s65, 0x2000\n" \
"s_xor_b32 s61, s61, s65\n" \
"s_add_u32 m0, s55, 65536\n" \
"s_nop 0\n" \
"buffer_load_dwordx4 v4, s[36:39], s61 offen offset:0 sc1 lds\n" \
"v_mov_b32_e64 v13, s59\n" \
"v_mfma_f32_16x16x32_bf16 v[52:55], v[20:23], a[0:3], 0\n" \
"ds_read_b128 v[40:43], v10 offset:3072\n" \
"s_nop 0\n" \
"v_mfma_f32_16x16x32_bf16 v[56:59], v[16:19], a[128:131], 0\n" \
"ds_read_b128 v[44:47], v10 offset:19456\n" \
"s_nop 0\n" \
"v_mfma_f32_16x16x32_bf16 v[60:63], v[20:23], a[128:131], 0\n" \
"s_waitcnt lgkmcnt(4)\n" \
"v_cvt_pk_bf16_f32 v80, v64, v65\n" \
"v_cvt_pk_bf16_f32 v81, v66, v67\n" \
"v_mfma_f32_16x16x32_bf16 v[48:51], v[24:27], a[4:7], v[48:51]\n" \
"buffer_load_dwordx4 v4, s[36:39], s61 offen offset:1024 sc1 lds\n" \
"v_mfma_f32_16x16x32_bf16 v[52:55], v[28:31], a[4:7], v[52:55]\n" \
"ds_read_b128 v[16:19], v10 offset:4096\n" \
"v_cvt_pk_bf16_f32 v82, v68, v69\n" \
"v_mfma_f32_16x16x32_bf16 v[56:59], v[24:27], a[132:135], v[56:59]\n" \
"ds_read_b128 v[20:23], v10 offset:20480\n" \
"v_cvt_pk_bf16_f32 v83, v70, v71\n" \
"v_mfma_f32_16x16x32_bf16 v[60:63], v[28:31], a[132:135], v[60:63]\n" \
"s_waitcnt lgkmcnt(4)\n" \
"buffer_store_dwordx4 v[80:83], v5, s[36:39], s57 offen sc1\n" \
"v_cvt_pk_bf16_f32 v84, v72, v73\n" \
"v_mfma_f32_16x16x32_bf16 v[48:51], v[32:35], a[8:11], v[48:51]\n" \
"s_add_u32 s63, s59, 2\n" \
"s_min_u32 s63, s63, 63\n" \
"v_mfma_f32_16x16x32_bf16 v[52:55], v[36:39], a[8:11], v[52:55]\n" \
"ds_read_b128 v[24:27], v10 offset:5120\n" \
"s_lshr_b32 s63, s63, 3\n" \
"s_lshl_b32 s63, s63, 7\n" \
"v_cvt_pk_bf16_f32 v85, v74, v75\n" \
"v_mfma_f32_16x16x32_bf16 v[56:59], v[32:35], a[136:139], v[56:59]\n" \
"ds_read_b128 v[28:31], v10 offset:21504\n" \
"v_cvt_pk_bf16_f32 v86, v76, v77\n" \
"v_mfma_f32_16x16x32_bf16 v[60:63], v[36:39], a[136:139], v[60:63]\n" \
"s_waitcnt lgkmcnt(4)\n" \
"v_cvt_pk_bf16_f32 v87, v78, v79\n" \
"buffer_store_dwordx4 v[84:87], v5, s[36:39], s58 offen sc1\n" \
"v_mfma_f32_16x16x32_bf16 v[48:51], v[40:43], a[12:15], v[48:51]\n" \
"buffer_load_dwordx4 v4, s[36:39], s61 offen offset:2048 sc1 lds\n" \
"v_mfma_f32_16x16x32_bf16 v[52:55], v[44:47], a[12:15], v[52:55]\n" \
"ds_read_b128 v[32:35], v10 offset:6144\n" \
"v_mfma_f32_16x16x32_bf16 v[56:59], v[40:43], a[140:143], v[56:59]\n" \
"ds_read_b128 v[36:39], v10 offset:22528\n" \
"v_mfma_f32_16x16x32_bf16 v[60:63], v[44:47], a[140:143], v[60:63]\n" \
"s_waitcnt lgkmcnt(4)\n" \
"v_mfma_f32_16x16x32_bf16 v[48:51], v[16:19], a[16:19], v[48:51]\n" \
"buffer_load_dwordx4 v4, s[36:39], s61 offen offset:3072 sc1 lds\n" \
"v_mfma_f32_16x16x32_bf16 v[52:55], v[20:23], a[16:19], v[52:55]\n" \
"ds_read_b128 v[40:43], v10 offset:7168\n" \
"v_mfma_f32_16x16x32_bf16 v[56:59], v[16:19], a[144:147], v[56:59]\n" \
"ds_read_b128 v[44:47], v10 offset:23552\n" \
"v_mfma_f32_16x16x32_bf16 v[60:63], v[20:23], a[144:147], v[60:63]\n" \
"s_waitcnt lgkmcnt(4)\n" \
"v_mfma_f32_16x16x32_bf16 v[48:51], v[24:27], a[20:23], v[48:51]\n" \
"buffer_load_dword v15, v14, s[36:39], s63 offen sc1\n" \
"v_mfma_f32_16x16x32_bf16 v[52:55], v[28:31], a[20:23], v[52:55]\n" \
"ds_read_b128 v[16:19], v10 offset:8192\n" \
"v_mfma_f32_16x16x32_bf16 v[56:59], v[24:27], a[148:151], v[56:59]\n" \
"ds_read_b128 v[20:23], v10 offset:24576\n" \
"v_mfma_f32_16x16x32_bf16 v[60:63], v[28:31], a[148:151], v[60:63]\n" \
"s_waitcnt lgkmcnt(4)\n" \
"v_mfma_f32_16x16x32_bf16 v[48:51], v[32:35], a[24:27], v[48:51]\n" \
"s_add_u32 m0, s55, 69632\n" \
"s_add_u32 s61, s61, 4096\n" \
"buffer_load_dwordx4 v4, s[36:39], s61 offen offset:0 sc1 lds\n" \
"v_mfma_f32_16x16x32_bf16 v[52:55], v[36:39], a[24:27], v[52:55]\n" \
"ds_read_b128 v[24:27], v10 offset:9216\n" \
"v_mfma_f32_16x16x32_bf16 v[56:59], v[32:35], a[152:155], v[56:59]\n" \
"ds_read_b128 v[28:31], v10 offset:25600\n" \
"v_mfma_f32_16x16x32_bf16 v[60:63], v[36:39], a[152:155], v[60:63]\n" \
"s_waitcnt lgkmcnt(4)\n" \
"v_mfma_f32_16x16x32_bf16 v[48:51], v[40:43], a[28:31], v[48:51]\n" \
"buffer_load_dwordx4 v4, s[36:39], s61 offen offset:1024 sc1 lds\n" \
"v_mfma_f32_16x16x32_bf16 v[52:55], v[44:47], a[28:31], v[52:55]\n" \
"ds_read_b128 v[32:35], v10 offset:10240\n" \
"v_mfma_f32_16x16x32_bf16 v[56:59], v[40:43], a[156:159], v[56:59]\n" \
"ds_read_b128 v[36:39], v10 offset:26624\n" \
"v_mfma_f32_16x16x32_bf16 v[60:63], v[44:47], a[156:159], v[60:63]\n" \
"s_waitcnt lgkmcnt(4)\n" \
"v_mfma_f32_16x16x32_bf16 v[48:51], v[16:19], a[32:35], v[48:51]\n" \
"v_mfma_f32_16x16x32_bf16 v[52:55], v[20:23], a[32:35], v[52:55]\n" \
"ds_read_b128 v[40:43], v10 offset:11264\n" \
"v_mfma_f32_16x16x32_bf16 v[56:59], v[16:19], a[160:163], v[56:59]\n" \
"ds_read_b128 v[44:47], v10 offset:27648\n" \
"v_mfma_f32_16x16x32_bf16 v[60:63], v[20:23], a[160:163], v[60:63]\n" \
"s_waitcnt lgkmcnt(4)\n" \
"v_mfma_f32_16x16x32_bf16 v[48:51], v[24:27], a[36:39], v[48:51]\n" \
"buffer_load_dwordx4 v4, s[36:39], s61 offen offset:2048 sc1 lds\n" \
"v_mfma_f32_16x16x32_bf16 v[52:55], v[28:31], a[36:39], v[52:55]\n" \
"ds_read_b128 v[16:19], v10 offset:12288\n" \
"v_mfma_f32_16x16x32_bf16 v[56:59], v[24:27], a[164:167], v[56:59]\n" \
"ds_read_b128 v[20:23], v10 offset:28672\n" \
"v_mfma_f32_16x16x32_bf16 v[60:63], v[28:31], a[164:167], v[60:63]\n" \
"s_waitcnt lgkmcnt(4)\n" \
"v_mfma_f32_16x16x32_bf16 v[48:51], v[32:35], a[40:43], v[48:51]\n" \
"buffer_load_dwordx4 v4, s[36:39], s61 offen offset:3072 sc1 lds\n" \
"v_mfma_f32_16x16x32_bf16 v[52:55], v[36:39], a[40:43], v[52:55]\n" \
"ds_read_b128 v[24:27], v10 offset:13312\n" \
"v_mfma_f32_16x16x32_bf16 v[56:59], v[32:35], a[168:171], v[56:59]\n" \
"ds_read_b128 v[28:31], v10 offset:29696\n" \
"v_mfma_f32_16x16x32_bf16 v[60:63], v[36:39], a[168:171], v[60:63]\n" \
"s_waitcnt lgkmcnt(4)\n" \
"v_mfma_f32_16x16x32_bf16 v[48:51], v[40:43], a[44:47], v[48:51]\n" \
"v_mfma_f32_16x16x32_bf16 v[52:55], v[44:47], a[44:47], v[52:55]\n" \
"ds_read_b128 v[32:35], v10 offset:14336\n" \
"v_mfma_f32_16x16x32_bf16 v[56:59], v[40:43], a[172:175], v[56:59]\n" \
"ds_read_b128 v[36:39], v10 offset:30720\n" \
"v_mfma_f32_16x16x32_bf16 v[60:63], v[44:47], a[172:175], v[60:63]\n" \
"s_waitcnt lgkmcnt(4)\n" \
"v_mfma_f32_16x16x32_bf16 v[48:51], v[16:19], a[48:51], v[48:51]\n" \
"v_mfma_f32_16x16x32_bf16 v[52:55], v[20:23], a[48:51], v[52:55]\n" \
"ds_read_b128 v[40:43], v10 offset:15360\n" \
"v_mfma_f32_16x16x32_bf16 v[56:59], v[16:19], a[176:179], v[56:59]\n" \
"ds_read_b128 v[44:47], v10 offset:31744\n" \
"v_mfma_f32_16x16x32_bf16 v[60:63], v[20:23], a[176:179], v[60:63]\n" \
"s_waitcnt lgkmcnt(4)\n" \
"v_mfma_f32_16x16x32_bf16 v[48:51], v[24:27], a[52:55], v[48:51]\n" \
"s_waitcnt vmcnt(12)\n" \
"s_barrier\n" \
"v_mfma_f32_16x16x32_bf16 v[52:55], v[28:31], a[52:55], v[52:55]\n" \
"ds_read_b128 v[16:19], v10 offset:32768\n" \
"v_mfma_f32_16x16x32_bf16 v[56:59], v[24:27], a[180:183], v[56:59]\n" \
"ds_read_b128 v[20:23], v10 offset:49152\n" \
"v_mfma_f32_16x16x32_bf16 v[60:63], v[28:31], a[180:183], v[60:63]\n" \
"s_waitcnt lgkmcnt(4)\n" \
"v_mfma_f32_16x16x32_bf16 v[48:51], v[32:35], a[56:59], v[48:51]\n" \
"v_mfma_f32_16x16x32_bf16 v[52:55], v[36:39], a[56:59], v[52:55]\n" \
"ds_read_b128 v[24:27], v10 offset:33792\n" \
"v_mfma_f32_16x16x32_bf16 v[56:59], v[32:35], a[184:187], v[56:59]\n" \
"ds_read_b128 v[28:31], v10 offset:50176\n" \
"v_mfma_f32_16x16x32_bf16 v[60:63], v[36:39], a[184:187], v[60:63]\n" \
"s_waitcnt lgkmcnt(4)\n" \
"v_mfma_f32_16x16x32_bf16 v[48:51], v[40:43], a[60:63], v[48:51]\n" \
"v_mfma_f32_16x16x32_bf16 v[52:55], v[44:47], a[60:63], v[52:55]\n" \
"ds_read_b128 v[32:35], v10 offset:34816\n" \
"v_mfma_f32_16x16x32_bf16 v[56:59], v[40:43], a[188:191], v[56:59]\n" \
"ds_read_b128 v[36:39], v10 offset:51200\n" \
"v_mfma_f32_16x16x32_bf16 v[60:63], v[44:47], a[188:191], v[60:63]\n" \
"s_waitcnt lgkmcnt(4)\n" \
"v_mfma_f32_16x16x32_bf16 v[48:51], v[16:19], a[64:67], v[48:51]\n" \
"s_add_u32 s61, s61, 28672\n" \
"s_add_u32 s56, s56, 0x20000\n" \
"s_add_u32 m0, s55, 98304\n" \
"s_nop 0\n" \
"buffer_load_dwordx4 v4, s[36:39], s61 offen offset:0 sc1 lds\n" \
"v_mfma_f32_16x16x32_bf16 v[52:55], v[20:23], a[64:67], v[52:55]\n" \
"ds_read_b128 v[40:43], v10 offset:35840\n" \
"v_mfma_f32_16x16x32_bf16 v[56:59], v[16:19], a[192:195], v[56:59]\n" \
"ds_read_b128 v[44:47], v10 offset:52224\n" \
"v_mfma_f32_16x16x32_bf16 v[60:63], v[20:23], a[192:195], v[60:63]\n" \
"s_waitcnt lgkmcnt(4)\n" \
"v_mfma_f32_16x16x32_bf16 v[48:51], v[24:27], a[68:71], v[48:51]\n" \
"buffer_load_dwordx4 v4, s[36:39], s61 offen offset:1024 sc1 lds\n" \
"v_mfma_f32_16x16x32_bf16 v[52:55], v[28:31], a[68:71], v[52:55]\n" \
"ds_read_b128 v[16:19], v10 offset:36864\n" \
"v_mfma_f32_16x16x32_bf16 v[56:59], v[24:27], a[196:199], v[56:59]\n" \
"ds_read_b128 v[20:23], v10 offset:53248\n" \
"v_mfma_f32_16x16x32_bf16 v[60:63], v[28:31], a[196:199], v[60:63]\n" \
"s_waitcnt lgkmcnt(4)\n" \
"v_mfma_f32_16x16x32_bf16 v[48:51], v[32:35], a[72:75], v[48:51]\n" \
"v_mfma_f32_16x16x32_bf16 v[52:55], v[36:39], a[72:75], v[52:55]\n" \
"ds_read_b128 v[24:27], v10 offset:37888\n" \
"v_mfma_f32_16x16x32_bf16 v[56:59], v[32:35], a[200:203], v[56:59]\n" \
"ds_read_b128 v[28:31], v10 offset:54272\n" \
"v_mfma_f32_16x16x32_bf16 v[60:63], v[36:39], a[200:203], v[60:63]\n" \
"s_waitcnt lgkmcnt(4)\n" \
"v_mfma_f32_16x16x32_bf16 v[48:51], v[40:43], a[76:79], v[48:51]\n" \
"buffer_load_dwordx4 v4, s[36:39], s61 offen offset:2048 sc1 lds\n" \
"v_mfma_f32_16x16x32_bf16 v[52:55], v[44:47], a[76:79], v[52:55]\n" \
"ds_read_b128 v[32:35], v10 offset:38912\n" \
"v_mfma_f32_16x16x32_bf16 v[56:59], v[40:43], a[204:207], v[56:59]\n" \
"ds_read_b128 v[36:39], v10 offset:55296\n" \
"v_mfma_f32_16x16x32_bf16 v[60:63], v[44:47], a[204:207], v[60:63]\n" \
"s_waitcnt lgkmcnt(4)\n" \
"v_mfma_f32_16x16x32_bf16 v[48:51], v[16:19], a[80:83], v[48:51]\n" \
"buffer_load_dwordx4 v4, s[36:39], s61 offen offset:3072 sc1 lds\n" \
"v_mfma_f32_16x16x32_bf16 v[52:55], v[20:23], a[80:83], v[52:55]\n" \
"ds_read_b128 v[40:43], v10 offset:39936\n" \
"v_mfma_f32_16x16x32_bf16 v[56:59], v[16:19], a[208:211], v[56:59]\n" \
"ds_read_b128 v[44:47], v10 offset:56320\n" \
"v_mfma_f32_16x16x32_bf16 v[60:63], v[20:23], a[208:211], v[60:63]\n" \
"s_waitcnt lgkmcnt(4)\n" \
"v_mfma_f32_16x16x32_bf16 v[48:51], v[24:27], a[84:87], v[48:51]\n" \
"v_mfma_f32_16x16x32_bf16 v[52:55], v[28:31], a[84:87], v[52:55]\n" \
"ds_read_b128 v[16:19], v10 offset:40960\n" \
"v_mfma_f32_16x16x32_bf16 v[56:59], v[24:27], a[212:215], v[56:59]\n" \
"ds_read_b128 v[20:23], v10 offset:57344\n" \
"v_mfma_f32_16x16x32_bf16 v[60:63], v[28:31], a[212:215], v[60:63]\n" \
"s_waitcnt lgkmcnt(4)\n" \
"v_mfma_f32_16x16x32_bf16 v[48:51], v[32:35], a[88:91], v[48:51]\n" \
"s_add_u32 m0, s55, 102400\n" \
"s_add_u32 s61, s61, 4096\n" \
"buffer_load_dwordx4 v4, s[36:39], s61 offen offset:0 sc1 lds\n" \
"v_mfma_f32_16x16x32_bf16 v[52:55], v[36:39], a[88:91], v[52:55]\n" \
"ds_read_b128 v[24:27], v10 offset:41984\n" \
"v_mfma_f32_16x16x32_bf16 v[56:59], v[32:35], a[216:219], v[56:59]\n" \
"ds_read_b128 v[28:31], v10 offset:58368\n" \
"v_mfma_f32_16x16x32_bf16 v[60:63], v[36:39], a[216:219], v[60:63]\n" \
"s_waitcnt lgkmcnt(4)\n" \
"v_mfma_f32_16x16x32_bf16 v[48:51], v[40:43], a[92:95], v[48:51]\n" \
"buffer_load_dwordx4 v4, s[36:39], s61 offen offset:1024 sc1 lds\n" \
"v_mfma_f32_16x16x32_bf16 v[52:55], v[44:47], a[92:95], v[52:55]\n" \
"ds_read_b128 v[32:35], v10 offset:43008\n" \
"v_mfma_f32_16x16x32_bf16 v[56:59], v[40:43], a[220:223], v[56:59]\n" \
"ds_read_b128 v[36:39], v10 offset:59392\n" \
"v_mfma_f32_16x16x32_bf16 v[60:63], v[44:47], a[220:223], v[60:63]\n" \
"s_waitcnt lgkmcnt(4)\n" \
"v_mfma_f32_16x16x32_bf16 v[48:51], v[16:19], a[96:99], v[48:51]\n" \
"v_mfma_f32_16x16x32_bf16 v[52:55], v[20:23], a[96:99], v[52:55]\n" \
"ds_read_b128 v[40:43], v10 offset:44032\n" \
"v_mfma_f32_16x16x32_bf16 v[56:59], v[16:19], a[224:227], v[56:59]\n" \
"ds_read_b128 v[44:47], v10 offset:60416\n" \
"v_mfma_f32_16x16x32_bf16 v[60:63], v[20:23], a[224:227], v[60:63]\n" \
"s_waitcnt lgkmcnt(4)\n" \
"v_mfma_f32_16x16x32_bf16 v[48:51], v[24:27], a[100:103], v[48:51]\n" \
"buffer_load_dwordx4 v4, s[36:39], s61 offen offset:2048 sc1 lds\n" \
"v_mfma_f32_16x16x32_bf16 v[52:55], v[28:31], a[100:103], v[52:55]\n" \
"ds_read_b128 v[16:19], v10 offset:45056\n" \
"v_mfma_f32_16x16x32_bf16 v[56:59], v[24:27], a[228:231], v[56:59]\n" \
"ds_read_b128 v[20:23], v10 offset:61440\n" \
"v_mfma_f32_16x16x32_bf16 v[60:63], v[28:31], a[228:231], v[60:63]\n" \
"s_waitcnt lgkmcnt(4)\n" \
"v_mfma_f32_16x16x32_bf16 v[48:51], v[32:35], a[104:107], v[48:51]\n" \
"buffer_load_dwordx4 v4, s[36:39], s61 offen offset:3072 sc1 lds\n" \
"v_mfma_f32_16x16x32_bf16 v[52:55], v[36:39], a[104:107], v[52:55]\n" \
"ds_read_b128 v[24:27], v10 offset:46080\n" \
"v_mfma_f32_16x16x32_bf16 v[56:59], v[32:35], a[232:235], v[56:59]\n" \
"ds_read_b128 v[28:31], v10 offset:62464\n" \
"v_mfma_f32_16x16x32_bf16 v[60:63], v[36:39], a[232:235], v[60:63]\n" \
"s_waitcnt lgkmcnt(4)\n" \
"v_mfma_f32_16x16x32_bf16 v[48:51], v[40:43], a[108:111], v[48:51]\n" \
"s_lshl_b32 s66, s59, s49\n" \
"s_lshr_b32 s65, s66, 8\n" \
"v_mfma_f32_16x16x32_bf16 v[52:55], v[44:47], a[108:111], v[52:55]\n" \
"ds_read_b128 v[32:35], v10 offset:47104\n" \
"v_mfma_f32_16x16x32_bf16 v[56:59], v[40:43], a[236:239], v[56:59]\n" \
"ds_read_b128 v[36:39], v10 offset:63488\n" \
"v_mfma_f32_16x16x32_bf16 v[60:63], v[44:47], a[236:239], v[60:63]\n" \
"s_waitcnt lgkmcnt(4)\n" \
"s_and_b32 s65, s65, 0x2000\n" \
"s_xor_b32 s66, s66, s65\n" \
"v_mfma_f32_16x16x32_bf16 v[48:51], v[16:19], a[112:115], v[48:51]\n" \
"v_mfma_f32_16x16x32_bf16 v[52:55], v[20:23], a[112:115], v[52:55]\n" \
"ds_read_b128 v[40:43], v10 offset:48128\n" \
"v_mfma_f32_16x16x32_bf16 v[56:59], v[16:19], a[240:243], v[56:59]\n" \
"ds_read_b128 v[44:47], v10 offset:64512\n" \
"v_mfma_f32_16x16x32_bf16 v[60:63], v[20:23], a[240:243], v[60:63]\n" \
"s_waitcnt lgkmcnt(4)\n" \
"v_mfma_f32_16x16x32_bf16 v[48:51], v[24:27], a[116:119], v[48:51]\n" \
"s_waitcnt vmcnt(8)\n" \
"s_barrier\n" \
"v_mfma_f32_16x16x32_bf16 v[52:55], v[28:31], a[116:119], v[52:55]\n" \
"ds_read_b128 v[16:19], v12 offset:0\n" \
"v_mfma_f32_16x16x32_bf16 v[56:59], v[24:27], a[244:247], v[56:59]\n" \
"ds_read_b128 v[20:23], v12 offset:16384\n" \
"v_mfma_f32_16x16x32_bf16 v[60:63], v[28:31], a[244:247], v[60:63]\n" \
"s_waitcnt lgkmcnt(4)\n" \
"buffer_store_dword v13, v6, s[36:39], 0 offen sc1\n" \
"v_mfma_f32_16x16x32_bf16 v[48:51], v[32:35], a[120:123], v[48:51]\n" \
"v_cmp_gt_u32_e64 vcc, 16, v15\n" \
"v_mfma_f32_16x16x32_bf16 v[52:55], v[36:39], a[120:123], v[52:55]\n" \
"ds_read_b128 v[24:27], v12 offset:1024\n" \
"v_mfma_f32_16x16x32_bf16 v[56:59], v[32:35], a[248:251], v[56:59]\n" \
"ds_read_b128 v[28:31], v12 offset:17408\n" \
"v_mfma_f32_16x16x32_bf16 v[60:63], v[36:39], a[248:251], v[60:63]\n" \
"s_waitcnt lgkmcnt(4)\n" \
"v_mfma_f32_16x16x32_bf16 v[48:51], v[40:43], a[124:127], v[48:51]\n" \
"s_nop 0\n" \
"s_cbranch_vccnz .Lp1v_m0_%=\n" \
".Lp1v_m0k_%=:\n" \
"v_mfma_f32_16x16x32_bf16 v[52:55], v[44:47], a[124:127], v[52:55]\n" \
"ds_read_b128 v[32:35], v12 offset:2048\n" \
"v_mfma_f32_16x16x32_bf16 v[56:59], v[40:43], a[252:255], v[56:59]\n" \
"ds_read_b128 v[36:39], v12 offset:18432\n" \
"v_mfma_f32_16x16x32_bf16 v[60:63], v[44:47], a[252:255], v[60:63]\n" \
"s_waitcnt lgkmcnt(4)\n" \
"s_xor_b32 s57, s66, s64\n" \
"s_add_u32 s58, s57, s50\n" \
"s_add_u32 s59, s59, 1\n" \
"v_mov_b32 v5, v7\n" \
"v_mov_b32 v6, v11\n" \
"s_cmp_ge_u32 s59, s47\n" \
"s_cbranch_scc1 .Lp1v_out0_%=\n" \
"v_mfma_f32_16x16x32_bf16 v[64:67], v[16:19], a[0:3], 0\n" \
"s_min_u32 s61, s56, s60\n" \
"s_lshr_b32 s65, s61, 8\n" \
"s_and_b32 s65, s65, 0x2000\n" \
"s_xor_b32 s61, s61, s65\n" \
"s_add_u32 m0, s55, 0\n" \
"s_nop 0\n" \
"buffer_load_dwordx4 v4, s[36:39], s61 offen offset:0 sc1 lds\n" \
"v_mov_b32_e64 v13, s59\n" \
"v_mfma_f32_16x16x32_bf16 v[68:71], v[20:23], a[0:3], 0\n" \
"ds_read_b128 v[40:43], v12 offset:3072\n" \
"s_nop 0\n" \
"v_mfma_f32_16x16x32_bf16 v[72:75], v[16:19], a[128:131], 0\n" \
"ds_read_b128 v[44:47], v12 offset:19456\n" \
"s_nop 0\n" \
"v_mfma_f32_16x16x32_bf16 v[76:79], v[20:23], a[128:131], 0\n" \
"s_waitcnt lgkmcnt(4)\n" \
"v_cvt_pk_bf16_f32 v80, v48, v49\n" \
"v_cvt_pk_bf16_f32 v81, v50, v51\n" \
"v_mfma_f32_16x16x32_bf16 v[64:67], v[24:27], a[4:7], v[64:67]\n" \
"buffer_load_dwordx4 v4, s[36:39], s61 offen offset:1024 sc1 lds\n" \
"v_mfma_f32_16x16x32_bf16 v[68:71], v[28:31], a[4:7], v[68:71]\n" \
"ds_read_b128 v[16:19], v12 offset:4096\n" \
"v_cvt_pk_bf16_f32 v82, v52, v53\n" \
"v_mfma_f32_16x16x32_bf16 v[72:75], v[24:27], a[132:135], v[72:75]\n" \
"ds_read_b128 v[20:23], v12 offset:20480\n" \
"v_cvt_pk_bf16_f32 v83, v54, v55\n" \
"v_mfma_f32_16x16x32_bf16 v[76:79], v[28:31], a[132:135], v[76:79]\n" \
"s_waitcnt lgkmcnt(4)\n" \
"buffer_store_dwordx4 v[80:83], v5, s[36:39], s57 offen sc1\n" \
"v_cvt_pk_bf16_f32 v84, v56, v57\n" \
"v_mfma_f32_16x16x32_bf16 v[64:67], v[32:35], a[8:11], v[64:67]\n" \
"s_add_u32 s63, s59, 2\n" \
"s_min_u32 s63, s63, 63\n" \
"v_mfma_f32_16x16x32_bf16 v[68:71], v[36:39], a[8:11], v[68:71]\n" \
"ds_read_b128 v[24:27], v12 offset:5120\n" \
"s_lshr_b32 s63, s63, 3\n" \
"s_lshl_b32 s63, s63, 7\n" \
"v_cvt_pk_bf16_f32 v85, v58, v59\n" \
"v_mfma_f32_16x16x32_bf16 v[72:75], v[32:35], a[136:139], v[72:75]\n" \
"ds_read_b128 v[28:31], v12 offset:21504\n" \
"v_cvt_pk_bf16_f32 v86, v60, v61\n" \
"v_mfma_f32_16x16x32_bf16 v[76:79], v[36:39], a[136:139], v[76:79]\n" \
"s_waitcnt lgkmcnt(4)\n" \
"v_cvt_pk_bf16_f32 v87, v62, v63\n" \
"buffer_store_dwordx4 v[84:87], v5, s[36:39], s58 offen sc1\n" \
"v_mfma_f32_16x16x32_bf16 v[64:67], v[40:43], a[12:15], v[64:67]\n" \
"buffer_load_dwordx4 v4, s[36:39], s61 offen offset:2048 sc1 lds\n" \
"v_mfma_f32_16x16x32_bf16 v[68:71], v[44:47], a[12:15], v[68:71]\n" \
"ds_read_b128 v[32:35], v12 offset:6144\n" \
"v_mfma_f32_16x16x32_bf16 v[72:75], v[40:43], a[140:143], v[72:75]\n" \
"ds_read_b128 v[36:39], v12 offset:22528\n" \
"v_mfma_f32_16x16x32_bf16 v[76:79], v[44:47], a[140:143], v[76:79]\n" \
"s_waitcnt lgkmcnt(4)\n" \
"v_mfma_f32_16x16x32_bf16 v[64:67], v[16:19], a[16:19], v[64:67]\n" \
"buffer_load_dwordx4 v4, s[36:39], s61 offen offset:3072 sc1 lds\n" \
"v_mfma_f32_16x16x32_bf16 v[68:71], v[20:23], a[16:19], v[68:71]\n" \
"ds_read_b128 v[40:43], v12 offset:7168\n" \
"v_mfma_f32_16x16x32_bf16 v[72:75], v[16:19], a[144:147], v[72:75]\n" \
"ds_read_b128 v[44:47], v12 offset:23552\n" \
"v_mfma_f32_16x16x32_bf16 v[76:79], v[20:23], a[144:147], v[76:79]\n" \
"s_waitcnt lgkmcnt(4)\n" \
"v_mfma_f32_16x16x32_bf16 v[64:67], v[24:27], a[20:23], v[64:67]\n" \
"buffer_load_dword v15, v14, s[36:39], s63 offen sc1\n" \
"v_mfma_f32_16x16x32_bf16 v[68:71], v[28:31], a[20:23], v[68:71]\n" \
"ds_read_b128 v[16:19], v12 offset:8192\n" \
"v_mfma_f32_16x16x32_bf16 v[72:75], v[24:27], a[148:151], v[72:75]\n" \
"ds_read_b128 v[20:23], v12 offset:24576\n" \
"v_mfma_f32_16x16x32_bf16 v[76:79], v[28:31], a[148:151], v[76:79]\n" \
"s_waitcnt lgkmcnt(4)\n" \
"v_mfma_f32_16x16x32_bf16 v[64:67], v[32:35], a[24:27], v[64:67]\n" \
"s_add_u32 m0, s55, 4096\n" \
"s_add_u32 s61, s61, 4096\n" \
"buffer_load_dwordx4 v4, s[36:39], s61 offen offset:0 sc1 lds\n" \
"v_mfma_f32_16x16x32_bf16 v[68:71], v[36:39], a[24:27], v[68:71]\n" \
"ds_read_b128 v[24:27], v12 offset:9216\n" \
"v_mfma_f32_16x16x32_bf16 v[72:75], v[32:35], a[152:155], v[72:75]\n" \
"ds_read_b128 v[28:31], v12 offset:25600\n" \
"v_mfma_f32_16x16x32_bf16 v[76:79], v[36:39], a[152:155], v[76:79]\n" \
"s_waitcnt lgkmcnt(4)\n" \
"v_mfma_f32_16x16x32_bf16 v[64:67], v[40:43], a[28:31], v[64:67]\n" \
"buffer_load_dwordx4 v4, s[36:39], s61 offen offset:1024 sc1 lds\n" \
"v_mfma_f32_16x16x32_bf16 v[68:71], v[44:47], a[28:31], v[68:71]\n" \
"ds_read_b128 v[32:35], v12 offset:10240\n" \
"v_mfma_f32_16x16x32_bf16 v[72:75], v[40:43], a[156:159], v[72:75]\n" \
"ds_read_b128 v[36:39], v12 offset:26624\n" \
"v_mfma_f32_16x16x32_bf16 v[76:79], v[44:47], a[156:159], v[76:79]\n" \
"s_waitcnt lgkmcnt(4)\n" \
"v_mfma_f32_16x16x32_bf16 v[64:67], v[16:19], a[32:35], v[64:67]\n" \
"v_mfma_f32_16x16x32_bf16 v[68:71], v[20:23], a[32:35], v[68:71]\n" \
"ds_read_b128 v[40:43], v12 offset:11264\n" \
"v_mfma_f32_16x16x32_bf16 v[72:75], v[16:19], a[160:163], v[72:75]\n" \
"ds_read_b128 v[44:47], v12 offset:27648\n" \
"v_mfma_f32_16x16x32_bf16 v[76:79], v[20:23], a[160:163], v[76:79]\n" \
"s_waitcnt lgkmcnt(4)\n" \
"v_mfma_f32_16x16x32_bf16 v[64:67], v[24:27], a[36:39], v[64:67]\n" \
"buffer_load_dwordx4 v4, s[36:39], s61 offen offset:2048 sc1 lds\n" \
"v_mfma_f32_16x16x32_bf16 v[68:71], v[28:31], a[36:39], v[68:71]\n" \
"ds_read_b128 v[16:19], v12 offset:12288\n" \
"v_mfma_f32_16x16x32_bf16 v[72:75], v[24:27], a[164:167], v[72:75]\n" \
"ds_read_b128 v[20:23], v12 offset:28672\n" \
"v_mfma_f32_16x16x32_bf16 v[76:79], v[28:31], a[164:167], v[76:79]\n" \
"s_waitcnt lgkmcnt(4)\n" \
"v_mfma_f32_16x16x32_bf16 v[64:67], v[32:35], a[40:43], v[64:67]\n" \
"buffer_load_dwordx4 v4, s[36:39], s61 offen offset:3072 sc1 lds\n" \
"v_mfma_f32_16x16x32_bf16 v[68:71], v[36:39], a[40:43], v[68:71]\n" \
"ds_read_b128 v[24:27], v12 offset:13312\n" \
"v_mfma_f32_16x16x32_bf16 v[72:75], v[32:35], a[168:171], v[72:75]\n" \
"ds_read_b128 v[28:31], v12 offset:29696\n" \
"v_mfma_f32_16x16x32_bf16 v[76:79], v[36:39], a[168:171], v[76:79]\n" \
"s_waitcnt lgkmcnt(4)\n" \
"v_mfma_f32_16x16x32_bf16 v[64:67], v[40:43], a[44:47], v[64:67]\n" \
"v_mfma_f32_16x16x32_bf16 v[68:71], v[44:47], a[44:47], v[68:71]\n" \
"ds_read_b128 v[32:35], v12 offset:14336\n" \
"v_mfma_f32_16x16x32_bf16 v[72:75], v[40:43], a[172:175], v[72:75]\n" \
"ds_read_b128 v[36:39], v12 offset:30720\n" \
"v_mfma_f32_16x16x32_bf16 v[76:79], v[44:47], a[172:175], v[76:79]\n" \
"s_waitcnt lgkmcnt(4)\n" \
"v_mfma_f32_16x16x32_bf16 v[64:67], v[16:19], a[48:51], v[64:67]\n" \
"v_mfma_f32_16x16x32_bf16 v[68:71], v[20:23], a[48:51], v[68:71]\n" \
"ds_read_b128 v[40:43], v12 offset:15360\n" \
"v_mfma_f32_16x16x32_bf16 v[72:75], v[16:19], a[176:179], v[72:75]\n" \
"ds_read_b128 v[44:47], v12 offset:31744\n" \
"v_mfma_f32_16x16x32_bf16 v[76:79], v[20:23], a[176:179], v[76:79]\n" \
"s_waitcnt lgkmcnt(4)\n" \
"v_mfma_f32_16x16x32_bf16 v[64:67], v[24:27], a[52:55], v[64:67]\n" \
"s_waitcnt vmcnt(12)\n" \
"s_barrier\n" \
"v_mfma_f32_16x16x32_bf16 v[68:71], v[28:31], a[52:55], v[68:71]\n" \
"ds_read_b128 v[16:19], v12 offset:32768\n" \
"v_mfma_f32_16x16x32_bf16 v[72:75], v[24:27], a[180:183], v[72:75]\n" \
"ds_read_b128 v[20:23], v12 offset:49152\n" \
"v_mfma_f32_16x16x32_bf16 v[76:79], v[28:31], a[180:183], v[76:79]\n" \
"s_waitcnt lgkmcnt(4)\n" \
"v_mfma_f32_16x16x32_bf16 v[64:67], v[32:35], a[56:59], v[64:67]\n" \
"v_mfma_f32_16x16x32_bf16 v[68:71], v[36:39], a[56:59], v[68:71]\n" \
"ds_read_b128 v[24:27], v12 offset:33792\n" \
"v_mfma_f32_16x16x32_bf16 v[72:75], v[32:35], a[184:187], v[72:75]\n" \
"ds_read_b128 v[28:31], v12 offset:50176\n" \
"v_mfma_f32_16x16x32_bf16 v[76:79], v[36:39], a[184:187], v[76:79]\n" \
"s_waitcnt lgkmcnt(4)\n" \
"v_mfma_f32_16x16x32_bf16 v[64:67], v[40:43], a[60:63], v[64:67]\n" \
"v_mfma_f32_16x16x32_bf16 v[68:71], v[44:47], a[60:63], v[68:71]\n" \
"ds_read_b128 v[32:35], v12 offset:34816\n" \
"v_mfma_f32_16x16x32_bf16 v[72:75], v[40:43], a[188:191], v[72:75]\n" \
"ds_read_b128 v[36:39], v12 offset:51200\n" \
"v_mfma_f32_16x16x32_bf16 v[76:79], v[44:47], a[188:191], v[76:79]\n" \
"s_waitcnt lgkmcnt(4)\n" \
"v_mfma_f32_16x16x32_bf16 v[64:67], v[16:19], a[64:67], v[64:67]\n" \
"s_add_u32 s61, s61, 28672\n" \
"s_add_u32 s56, s56, 0x20000\n" \
"s_add_u32 m0, s55, 32768\n" \
"s_nop 0\n" \
"buffer_load_dwordx4 v4, s[36:39], s61 offen offset:0 sc1 lds\n" \
"v_mfma_f32_16x16x32_bf16 v[68:71], v[20:23], a[64:67], v[68:71]\n" \
"ds_read_b128 v[40:43], v12 offset:35840\n" \
"v_mfma_f32_16x16x32_bf16 v[72:75], v[16:19], a[192:195], v[72:75]\n" \
"ds_read_b128 v[44:47], v12 offset:52224\n" \
"v_mfma_f32_16x16x32_bf16 v[76:79], v[20:23], a[192:195], v[76:79]\n" \
"s_waitcnt lgkmcnt(4)\n" \
"v_mfma_f32_16x16x32_bf16 v[64:67], v[24:27], a[68:71], v[64:67]\n" \
"buffer_load_dwordx4 v4, s[36:39], s61 offen offset:1024 sc1 lds\n" \
"v_mfma_f32_16x16x32_bf16 v[68:71], v[28:31], a[68:71], v[68:71]\n" \
"ds_read_b128 v[16:19], v12 offset:36864\n" \
"v_mfma_f32_16x16x32_bf16 v[72:75], v[24:27], a[196:199], v[72:75]\n" \
"ds_read_b128 v[20:23], v12 offset:53248\n" \
"v_mfma_f32_16x16x32_bf16 v[76:79], v[28:31], a[196:199], v[76:79]\n" \
"s_waitcnt lgkmcnt(4)\n" \
"v_mfma_f32_16x16x32_bf16 v[64:67], v[32:35], a[72:75], v[64:67]\n" \
"v_mfma_f32_16x16x32_bf16 v[68:71], v[36:39], a[72:75], v[68:71]\n" \
"ds_read_b128 v[24:27], v12 offset:37888\n" \
"v_mfma_f32_16x16x32_bf16 v[72:75], v[32:35], a[200:203], v[72:75]\n" \
"ds_read_b128 v[28:31], v12 offset:54272\n" \
"v_mfma_f32_16x16x32_bf16 v[76:79], v[36:39], a[200:203], v[76:79]\n" \
"s_waitcnt lgkmcnt(4)\n" \
"v_mfma_f32_16x16x32_bf16 v[64:67], v[40:43], a[76:79], v[64:67]\n" \
"buffer_load_dwordx4 v4, s[36:39], s61 offen offset:2048 sc1 lds\n" \
"v_mfma_f32_16x16x32_bf16 v[68:71], v[44:47], a[76:79], v[68:71]\n" \
"ds_read_b128 v[32:35], v12 offset:38912\n" \
"v_mfma_f32_16x16x32_bf16 v[72:75], v[40:43], a[204:207], v[72:75]\n" \
"ds_read_b128 v[36:39], v12 offset:55296\n" \
"v_mfma_f32_16x16x32_bf16 v[76:79], v[44:47], a[204:207], v[76:79]\n" \
"s_waitcnt lgkmcnt(4)\n" \
"v_mfma_f32_16x16x32_bf16 v[64:67], v[16:19], a[80:83], v[64:67]\n" \
"buffer_load_dwordx4 v4, s[36:39], s61 offen offset:3072 sc1 lds\n" \
"v_mfma_f32_16x16x32_bf16 v[68:71], v[20:23], a[80:83], v[68:71]\n" \
"ds_read_b128 v[40:43], v12 offset:39936\n" \
"v_mfma_f32_16x16x32_bf16 v[72:75], v[16:19], a[208:211], v[72:75]\n" \
"ds_read_b128 v[44:47], v12 offset:56320\n" \
"v_mfma_f32_16x16x32_bf16 v[76:79], v[20:23], a[208:211], v[76:79]\n" \
"s_waitcnt lgkmcnt(4)\n" \
"v_mfma_f32_16x16x32_bf16 v[64:67], v[24:27], a[84:87], v[64:67]\n" \
"v_mfma_f32_16x16x32_bf16 v[68:71], v[28:31], a[84:87], v[68:71]\n" \
"ds_read_b128 v[16:19], v12 offset:40960\n" \
"v_mfma_f32_16x16x32_bf16 v[72:75], v[24:27], a[212:215], v[72:75]\n" \
"ds_read_b128 v[20:23], v12 offset:57344\n" \
"v_mfma_f32_16x16x32_bf16 v[76:79], v[28:31], a[212:215], v[76:79]\n" \
"s_waitcnt lgkmcnt(4)\n" \
"v_mfma_f32_16x16x32_bf16 v[64:67], v[32:35], a[88:91], v[64:67]\n" \
"s_add_u32 m0, s55, 36864\n" \
"s_add_u32 s61, s61, 4096\n" \
"buffer_load_dwordx4 v4, s[36:39], s61 offen offset:0 sc1 lds\n" \
"v_mfma_f32_16x16x32_bf16 v[68:71], v[36:39], a[88:91], v[68:71]\n" \
"ds_read_b128 v[24:27], v12 offset:41984\n" \
"v_mfma_f32_16x16x32_bf16 v[72:75], v[32:35], a[216:219], v[72:75]\n" \
"ds_read_b128 v[28:31], v12 offset:58368\n" \
"v_mfma_f32_16x16x32_bf16 v[76:79], v[36:39], a[216:219], v[76:79]\n" \
"s_waitcnt lgkmcnt(4)\n" \
"v_mfma_f32_16x16x32_bf16 v[64:67], v[40:43], a[92:95], v[64:67]\n" \
"buffer_load_dwordx4 v4, s[36:39], s61 offen offset:1024 sc1 lds\n" \
"v_mfma_f32_16x16x32_bf16 v[68:71], v[44:47], a[92:95], v[68:71]\n" \
"ds_read_b128 v[32:35], v12 offset:43008\n" \
"v_mfma_f32_16x16x32_bf16 v[72:75], v[40:43], a[220:223], v[72:75]\n" \
"ds_read_b128 v[36:39], v12 offset:59392\n" \
"v_mfma_f32_16x16x32_bf16 v[76:79], v[44:47], a[220:223], v[76:79]\n" \
"s_waitcnt lgkmcnt(4)\n" \
"v_mfma_f32_16x16x32_bf16 v[64:67], v[16:19], a[96:99], v[64:67]\n" \
"v_mfma_f32_16x16x32_bf16 v[68:71], v[20:23], a[96:99], v[68:71]\n" \
"ds_read_b128 v[40:43], v12 offset:44032\n" \
"v_mfma_f32_16x16x32_bf16 v[72:75], v[16:19], a[224:227], v[72:75]\n" \
"ds_read_b128 v[44:47], v12 offset:60416\n" \
"v_mfma_f32_16x16x32_bf16 v[76:79], v[20:23], a[224:227], v[76:79]\n" \
"s_waitcnt lgkmcnt(4)\n" \
"v_mfma_f32_16x16x32_bf16 v[64:67], v[24:27], a[100:103], v[64:67]\n" \
"buffer_load_dwordx4 v4, s[36:39], s61 offen offset:2048 sc1 lds\n" \
"v_mfma_f32_16x16x32_bf16 v[68:71], v[28:31], a[100:103], v[68:71]\n" \
"ds_read_b128 v[16:19], v12 offset:45056\n" \
"v_mfma_f32_16x16x32_bf16 v[72:75], v[24:27], a[228:231], v[72:75]\n" \
"ds_read_b128 v[20:23], v12 offset:61440\n" \
"v_mfma_f32_16x16x32_bf16 v[76:79], v[28:31], a[228:231], v[76:79]\n" \
"s_waitcnt lgkmcnt(4)\n" \
"v_mfma_f32_16x16x32_bf16 v[64:67], v[32:35], a[104:107], v[64:67]\n" \
"buffer_load_dwordx4 v4, s[36:39], s61 offen offset:3072 sc1 lds\n" \
"v_mfma_f32_16x16x32_bf16 v[68:71], v[36:39], a[104:107], v[68:71]\n" \
"ds_read_b128 v[24:27], v12 offset:46080\n" \
"v_mfma_f32_16x16x32_bf16 v[72:75], v[32:35], a[232:235], v[72:75]\n" \
"ds_read_b128 v[28:31], v12 offset:62464\n" \
"v_mfma_f32_16x16x32_bf16 v[76:79], v[36:39], a[232:235], v[76:79]\n" \
"s_waitcnt lgkmcnt(4)\n" \
"v_mfma_f32_16x16x32_bf16 v[64:67], v[40:43], a[108:111], v[64:67]\n" \
"s_lshl_b32 s66, s59, s49\n" \
"s_lshr_b32 s65, s66, 8\n" \
"v_mfma_f32_16x16x32_bf16 v[68:71], v[44:47], a[108:111], v[68:71]\n" \
"ds_read_b128 v[32:35], v12 offset:47104\n" \
"v_mfma_f32_16x16x32_bf16 v[72:75], v[40:43], a[236:239], v[72:75]\n" \
"ds_read_b128 v[36:39], v12 offset:63488\n" \
"v_mfma_f32_16x16x32_bf16 v[76:79], v[44:47], a[236:239], v[76:79]\n" \
"s_waitcnt lgkmcnt(4)\n" \
"s_and_b32 s65, s65, 0x2000\n" \
"s_xor_b32 s66, s66, s65\n" \
"v_mfma_f32_16x16x32_bf16 v[64:67], v[16:19], a[112:115], v[64:67]\n" \
"v_mfma_f32_16x16x32_bf16 v[68:71], v[20:23], a[112:115], v[68:71]\n" \
"ds_read_b128 v[40:43], v12 offset:48128\n" \
"v_mfma_f32_16x16x32_bf16 v[72:75], v[16:19], a[240:243], v[72:75]\n" \
"ds_read_b128 v[44:47], v12 offset:64512\n" \
"v_mfma_f32_16x16x32_bf16 v[76:79], v[20:23], a[240:243], v[76:79]\n" \
"s_waitcnt lgkmcnt(4)\n" \
"v_mfma_f32_16x16x32_bf16 v[64:67], v[24:27], a[116:119], v[64:67]\n" \
"s_waitcnt vmcnt(8)\n" \
"s_barrier\n" \
"v_mfma_f32_16x16x32_bf16 v[68:71], v[28:31], a[116:119], v[68:71]\n" \
"ds_read_b128 v[16:19], v10 offset:0\n" \
"v_mfma_f32_16x16x32_bf16 v[72:75], v[24:27], a[244:247], v[72:75]\n" \
"ds_read_b128 v[20:23], v10 offset:16384\n" \
"v_mfma_f32_16x16x32_bf16 v[76:79], v[28:31], a[244:247], v[76:79]\n" \
"s_waitcnt lgkmcnt(4)\n" \
"buffer_store_dword v13, v6, s[36:39], 0 offen sc1\n" \
"v_mfma_f32_16x16x32_bf16 v[64:67], v[32:35], a[120:123], v[64:67]\n" \
"v_cmp_gt_u32_e64 vcc, 16, v15\n" \
"v_mfma_f32_16x16x32_bf16 v[68:71], v[36:39], a[120:123], v[68:71]\n" \
"ds_read_b128 v[24:27], v10 offset:1024\n" \
"v_mfma_f32_16x16x32_bf16 v[72:75], v[32:35], a[248:251], v[72:75]\n" \
"ds_read_b128 v[28:31], v10 offset:17408\n" \
"v_mfma_f32_16x16x32_bf16 v[76:79], v[36:39], a[248:251], v[76:79]\n" \
"s_waitcnt lgkmcnt(4)\n" \
"v_mfma_f32_16x16x32_bf16 v[64:67], v[40:43], a[124:127], v[64:67]\n" \
"s_cbranch_vccnz .Lp1v_m1_%=\n" \
".Lp1v_m1k_%=:\n" \
"v_mfma_f32_16x16x32_bf16 v[68:71], v[44:47], a[124:127], v[68:71]\n" \
"ds_read_b128 v[32:35], v10 offset:2048\n" \
"v_mfma_f32_16x16x32_bf16 v[72:75], v[40:43], a[252:255], v[72:75]\n" \
"ds_read_b128 v[36:39], v10 offset:18432\n" \
"v_mfma_f32_16x16x32_bf16 v[76:79], v[44:47], a[252:255], v[76:79]\n" \
"s_waitcnt lgkmcnt(4)\n" \
"s_xor_b32 s57, s66, s64\n" \
"s_add_u32 s58, s57, s50\n" \
"s_add_u32 s59, s59, 1\n" \
"v_mov_b32 v5, v7\n" \
"v_mov_b32 v6, v11\n" \
"s_cmp_ge_u32 s59, s47\n" \
"s_cbranch_scc0 .Lp1v_loop_%=\n" \
".Lp1v_out1_%=:\n" \
"s_nop 7\n" \
"s_nop 7\n" \
"v_cvt_pk_bf16_f32 v80, v64, v65\n" \
"v_cvt_pk_bf16_f32 v81, v66, v67\n" \
"v_cvt_pk_bf16_f32 v82, v68, v69\n" \
"v_cvt_pk_bf16_f32 v83, v70, v71\n" \
"buffer_store_dwordx4 v[80:83], v5, s[36:39], s57 offen sc1\n" \
"v_cvt_pk_bf16_f32 v84, v72, v73\n" \
"v_cvt_pk_bf16_f32 v85, v74, v75\n" \
"v_cvt_pk_bf16_f32 v86, v76, v77\n" \
"v_cvt_pk_bf16_f32 v87, v78, v79\n" \
"buffer_store_dwordx4 v[84:87], v5, s[36:39], s58 offen sc1\n" \
"s_branch .Lp1v_end_%=\n" \
".Lp1v_out0_%=:\n" \
"s_nop 7\n" \
"s_nop 7\n" \
"v_cvt_pk_bf16_f32 v80, v48, v49\n" \
"v_cvt_pk_bf16_f32 v81, v50, v51\n" \
"v_cvt_pk_bf16_f32 v82, v52, v53\n" \
"v_cvt_pk_bf16_f32 v83, v54, v55\n" \
"buffer_store_dwordx4 v[80:83], v5, s[36:39], s57 offen sc1\n" \
"v_cvt_pk_bf16_f32 v84, v56, v57\n" \
"v_cvt_pk_bf16_f32 v85, v58, v59\n" \
"v_cvt_pk_bf16_f32 v86, v60, v61\n" \
"v_cvt_pk_bf16_f32 v87, v62, v63\n" \
"buffer_store_dwordx4 v[84:87], v5, s[36:39], s58 offen sc1\n" \
".Lp1v_end_%=:\n" \
"s_waitcnt vmcnt(0) lgkmcnt(0)\n" \
"s_barrier\n" \
"v_mov_b32 v13, s59\n" \
"buffer_store_dword v13, v6, s[36:39], 0 offen sc1\n" \
"s_waitcnt vmcnt(0) lgkmcnt(0)\n" \
"s_branch .Lp1v_x_%=\n" \
".Lp1v_l0_%=:\n" \
"s_nop 7\n" \
"s_nop 7\n" \
"s_waitcnt vmcnt(0) lgkmcnt(0)\n" \
"s_mov_b32 s62, 0\n" \
".Lp1v_l0a_%=:\n" \
"buffer_load_dword v15, v14, s[36:39], s63 offen sc1\n" \
"s_waitcnt vmcnt(0)\n" \
"v_cmp_gt_u32 vcc, 16, v15\n" \
"s_nop 4\n" \
"s_cbranch_vccz .Lp1v_l0k_%=\n" \
"s_sleep 4\n" \
"s_add_u32 s62, s62, 1\n" \
"s_cmp_lt_u32 s62, 0x10000\n" \
"s_cbranch_scc1 .Lp1v_l0a_%=\n" \
"v_mov_b32 v15, 256\n" \
"buffer_store_dword v15, v15, s[36:39], 0 offen sc1\n" \
"s_waitcnt vmcnt(0) lgkmcnt(0)\n" \
"s_branch .Lp1v_l0k_%=\n" \
".Lp1v_l1_%=:\n" \
"s_nop 7\n" \
"s_nop 7\n" \
"s_waitcnt vmcnt(0) lgkmcnt(0)\n" \
"s_mov_b32 s62, 0\n" \
".Lp1v_l1a_%=:\n" \
"buffer_load_dword v15, v14, s[36:39], s63 offen sc1\n" \
"s_waitcnt vmcnt(0)\n" \
"v_cmp_gt_u32 vcc, 16, v15\n" \
"s_nop 4\n" \
"s_cbranch_vccz .Lp1v_l1k_%=\n" \
"s_sleep 4\n" \
"s_add_u32 s62, s62, 1\n" \
"s_cmp_lt_u32 s62, 0x10000\n" \
"s_cbranch_scc1 .Lp1v_l1a_%=\n" \
"v_mov_b32 v15, 256\n" \
"buffer_store_dword v15, v15, s[36:39], 0 offen sc1\n" \
"s_waitcnt vmcnt(0) lgkmcnt(0)\n" \
"s_branch .Lp1v_l1k_%=\n" \
".Lp1v_m0_%=:\n" \
"s_nop 7\n" \
"s_nop 7\n" \
"s_waitcnt vmcnt(0) lgkmcnt(0)\n" \
"s_mov_b32 s62, 0\n" \
".Lp1v_m0a_%=:\n" \
"buffer_load_dword v15, v14, s[36:39], s63 offen sc1\n" \
"s_waitcnt vmcnt(0)\n" \
"v_cmp_gt_u32 vcc, 16, v15\n" \
"s_nop 4\n" \
"s_cbranch_vccz .Lp1v_m0k_%=\n" \
"s_sleep 4\n" \
"s_add_u32 s62, s62, 1\n" \
"s_cmp_lt_u32 s62, 0x10000\n" \
"s_cbranch_scc1 .Lp1v_m0a_%=\n" \
"v_mov_b32 v15, 256\n" \
"buffer_store_dword v15, v15, s[36:39], 0 offen sc1\n" \
"s_waitcnt vmcnt(0) lgkmcnt(0)\n" \
"s_branch .Lp1v_m0k_%=\n" \
".Lp1v_m1_%=:\n" \
"s_nop 7\n" \
"s_nop 7\n" \
"s_waitcnt vmcnt(0) lgkmcnt(0)\n" \
"s_mov_b32 s62, 0\n" \
".Lp1v_m1a_%=:\n" \
"buffer_load_dword v15, v14, s[36:39], s63 offen sc1\n" \
"s_waitcnt vmcnt(0)\n" \
"v_cmp_gt_u32 vcc, 16, v15\n" \
"s_nop 4\n" \
"s_cbranch_vccz .Lp1v_m1k_%=\n" \
"s_sleep 4\n" \
"s_add_u32 s62, s62, 1\n" \
"s_cmp_lt_u32 s62, 0x10000\n" \
"s_cbranch_scc1 .Lp1v_m1a_%=\n" \
"v_mov_b32 v15, 256\n" \
"buffer_store_dword v15, v15, s[36:39], 0 offen sc1\n" \
"s_waitcnt vmcnt(0) lgkmcnt(0)\n" \
"s_branch .Lp1v_m1k_%=\n" \
".Lp1v_x_%=:\n"
#define ASM_P1_G1 \
"s_nop 7\n" \
"s_nop 7\n" \
"s_lshr_b32 s52, s48, 1\n" \
"s_lshl_b32 s52, s52, 13\n" \
"s_and_b32 s53, s48, 1\n" \
"s_lshl_b32 s53, s53, 10\n" \
"s_add_u32 s52, s52, s53\n" \
"s_mov_b32 s54, 0\n" \
"s_add_u32 m0, s52, 0\n" \
"s_nop 0\n" \
"buffer_load_dwordx4 v1, s[40:43], s54 offen nt lds\n" \
"s_add_u32 s54, s54, s44\n" \
"s_add_u32 m0, s52, 2048\n" \
"s_nop 0\n" \
"buffer_load_dwordx4 v1, s[40:43], s54 offen nt lds\n" \
"s_add_u32 s54, s54, s44\n" \
"s_add_u32 m0, s52, 4096\n" \
"s_nop 0\n" \
"buffer_load_dwordx4 v1, s[40:43], s54 offen nt lds\n" \
"s_add_u32 s54, s54, s44\n" \
"s_add_u32 m0, s52, 6144\n" \
"s_nop 0\n" \
"buffer_load_dwordx4 v1, s[40:43], s54 offen nt lds\n" \
"s_add_u32 s54, s54, s44\n" \
"s_add_u32 m0, s52, 16384\n" \
"s_nop 0\n" \
"buffer_load_dwordx4 v1, s[40:43], s54 offen nt lds\n" \
"s_add_u32 s54, s54, s44\n" \
"s_add_u32 m0, s52, 18432\n" \
"s_nop 0\n" \
"buffer_load_dwordx4 v1, s[40:43], s54 offen nt lds\n" \
"s_add_u32 s54, s54, s44\n" \
"s_add_u32 m0, s52, 20480\n" \
"s_nop 0\n" \
"buffer_load_dwordx4 v1, s[40:43], s54 offen nt lds\n" \
"s_add_u32 s54, s54, s44\n" \
"s_add_u32 m0, s52, 22528\n" \
"s_nop 0\n" \
"buffer_load_dwordx4 v1, s[40:43], s54 offen nt lds\n" \
"s_add_u32 s54, s54, s44\n" \
"s_add_u32 m0, s52, 32768\n" \
"s_nop 0\n" \
"buffer_load_dwordx4 v1, s[40:43], s54 offen nt lds\n" \
"s_add_u32 s54, s54, s44\n" \
"s_add_u32 m0, s52, 34816\n" \
"s_nop 0\n" \
"buffer_load_dwordx4 v1, s[40:43], s54 offen nt lds\n" \
"s_add_u32 s54, s54, s44\n" \
"s_add_u32 m0, s52, 36864\n" \
"s_nop 0\n" \
"buffer_load_dwordx4 v1, s[40:43], s54 offen nt lds\n" \
"s_add_u32 s54, s54, s44\n" \
"s_add_u32 m0, s52, 38912\n" \
"s_nop 0\n" \
"buffer_load_dwordx4 v1, s[40:43], s54 offen nt lds\n" \
"s_add_u32 s54, s54, s44\n" \
"s_add_u32 m0, s52, 49152\n" \
"s_nop 0\n" \
"buffer_load_dwordx4 v1, s[40:43], s54 offen nt lds\n" \
"s_add_u32 s54, s54, s44\n" \
"s_add_u32 m0, s52, 51200\n" \
"s_nop 0\n" \
"buffer_load_dwordx4 v1, s[40:43], s54 offen nt lds\n" \
"s_add_u32 s54, s54, s44\n" \
"s_add_u32 m0, s52, 53248\n" \
"s_nop 0\n" \
"buffer_load_dwordx4 v1, s[40:43], s54 offen nt lds\n" \
"s_add_u32 s54, s54, s44\n" \
"s_add_u32 m0, s52, 55296\n" \
"s_nop 0\n" \
"buffer_load_dwordx4 v1, s[40:43], s54 offen nt lds\n" \
"s_add_u32 s54, s54, s44\n" \
"s_add_u32 m0, s52, 65536\n" \
"s_nop 0\n" \
"buffer_load_dwordx4 v1, s[40:43], s54 offen nt lds\n" \
"s_add_u32 s54, s54, s44\n" \
"s_add_u32 m0, s52, 67584\n" \
"s_nop 0\n" \
"buffer_load_dwordx4 v1, s[40:43], s54 offen nt lds\n" \
"s_add_u32 s54, s54, s44\n" \
"s_add_u32 m0, s52, 69632\n" \
"s_nop 0\n" \
"buffer_load_dwordx4 v1, s[40:43], s54 offen nt lds\n" \
"s_add_u32 s54, s54, s44\n" \
"s_add_u32 m0, s52, 71680\n" \
"s_nop 0\n" \
"buffer_load_dwordx4 v1, s[40:43], s54 offen nt lds\n" \
"s_add_u32 s54, s54, s44\n" \
"s_add_u32 m0, s52, 81920\n" \
"s_nop 0\n" \
"buffer_load_dwordx4 v1, s[40:43], s54 offen nt lds\n" \
"s_add_u32 s54, s54, s44\n" \
"s_add_u32 m0, s52, 83968\n" \
"s_nop 0\n" \
"buffer_load_dwordx4 v1, s[40:43], s54 offen nt lds\n" \
"s_add_u32 s54, s54, s44\n" \
"s_add_u32 m0, s52, 86016\n" \
"s_nop 0\n" \
"buffer_load_dwordx4 v1, s[40:43], s54 offen nt lds\n" \
"s_add_u32 s54, s54, s44\n" \
"s_add_u32 m0, s52, 88064\n" \
"s_nop 0\n" \
"buffer_load_dwordx4 v1, s[40:43], s54 offen nt lds\n" \
"s_add_u32 s54, s54, s44\n" \
"s_add_u32 m0, s52, 98304\n" \
"s_nop 0\n" \
"buffer_load_dwordx4 v1, s[40:43], s54 offen nt lds\n" \
"s_add_u32 s54, s54, s44\n" \
"s_add_u32 m0, s52, 100352\n" \
"s_nop 0\n" \
"buffer_load_dwordx4 v1, s[40:43], s54 offen nt lds\n" \
"s_add_u32 s54, s54, s44\n" \
"s_add_u32 m0, s52, 102400\n" \
"s_nop 0\n" \
"buffer_load_dwordx4 v1, s[40:43], s54 offen nt lds\n" \
"s_add_u32 s54, s54, s44\n" \
"s_add_u32 m0, s52, 104448\n" \
"s_nop 0\n" \
"buffer_load_dwordx4 v1, s[40:43], s54 offen nt lds\n" \
"s_add_u32 s54, s54, s44\n" \
"s_add_u32 m0, s52, 114688\n" \
"s_nop 0\n" \
"buffer_load_dwordx4 v1, s[40:43], s54 offen nt lds\n" \
"s_add_u32 s54, s54, s44\n" \
"s_add_u32 m0, s52, 116736\n" \
"s_nop 0\n" \
"buffer_load_dwordx4 v1, s[40:43], s54 offen nt lds\n" \
"s_add_u32 s54, s54, s44\n" \
"s_add_u32 m0, s52, 118784\n" \
"s_nop 0\n" \
"buffer_load_dwordx4 v1, s[40:43], s54 offen nt lds\n" \
"s_add_u32 s54, s54, s44\n" \
"s_add_u32 m0, s52, 120832\n" \
"s_nop 0\n" \
"buffer_load_dwordx4 v1, s[40:43], s54 offen nt lds\n" \
"s_add_u32 s54, s54, s44\n" \
"v_add_u32 v8, 0x10000, v2\n" \
"v_add_u32 v9, 0x10000, v3\n" \
"s_waitcnt vmcnt(28)\n" \
"s_barrier\n" \
"ds_read_b64_tr_b16 a[0:1], v2 offset:0\n" \
"ds_read_b64_tr_b16 a[2:3], v2 offset:512\n" \
"ds_read_b64_tr_b16 a[128:129], v3 offset:0\n" \
"ds_read_b64_tr_b16 a[130:131], v3 offset:512\n" \
"ds_read_b64_tr_b16 a[4:5], v2 offset:4096\n" \
"ds_read_b64_tr_b16 a[6:7], v2 offset:4608\n" \
"ds_read_b64_tr_b16 a[132:133], v3 offset:4096\n" \
"ds_read_b64_tr_b16 a[134:135], v3 offset:4608\n" \
"s_waitcnt lgkmcnt(0)\n" \
"s_barrier\n" \
"s_add_u32 m0, s52, 0\n" \
"s_nop 0\n" \
"buffer_load_dwordx4 v1, s[40:43], s54 offen nt lds\n" \
"s_add_u32 s54, s54, s44\n" \
"s_add_u32 m0, s52, 2048\n" \
"s_nop 0\n" \
"buffer_load_dwordx4 v1, s[40:43], s54 offen nt lds\n" \
"s_add_u32 s54, s54, s44\n" \
"s_add_u32 m0, s52, 4096\n" \
"s_nop 0\n" \
"buffer_load_dwordx4 v1, s[40:43], s54 offen nt lds\n" \
"s_add_u32 s54, s54, s44\n" \
"s_add_u32 m0, s52, 6144\n" \
"s_nop 0\n" \
"buffer_load_dwordx4 v1, s[40:43], s54 offen nt lds\n" \
"s_add_u32 s54, s54, s44\n" \
"s_waitcnt vmcnt(28)\n" \
"s_barrier\n" \
"ds_read_b64_tr_b16 a[8:9], v2 offset:16384\n" \
"ds_read_b64_tr_b16 a[10:11], v2 offset:16896\n" \
"ds_read_b64_tr_b16 a[136:137], v3 offset:16384\n" \
"ds_read_b64_tr_b16 a[138:139], v3 offset:16896\n" \
"ds_read_b64_tr_b16 a[12:13], v2 offset:20480\n" \
"ds_read_b64_tr_b16 a[14:15], v2 offset:20992\n" \
"ds_read_b64_tr_b16 a[140:141], v3 offset:20480\n" \
"ds_read_b64_tr_b16 a[142:143], v3 offset:20992\n" \
"s_waitcnt lgkmcnt(0)\n" \
"s_barrier\n" \
"s_add_u32 m0, s52, 16384\n" \
"s_nop 0\n" \
"buffer_load_dwordx4 v1, s[40:43], s54 offen nt lds\n" \
"s_add_u32 s54, s54, s44\n" \
"s_add_u32 m0, s52, 18432\n" \
"s_nop 0\n" \
"buffer_load_dwordx4 v1, s[40:43], s54 offen nt lds\n" \
"s_add_u32 s54, s54, s44\n" \
"s_add_u32 m0, s52, 20480\n" \
"s_nop 0\n" \
"buffer_load_dwordx4 v1, s[40:43], s54 offen nt lds\n" \
"s_add_u32 s54, s54, s44\n" \
"s_add_u32 m0, s52, 22528\n" \
"s_nop 0\n" \
"buffer_load_dwordx4 v1, s[40:43], s54 offen nt lds\n" \
"s_add_u32 s54, s54, s44\n" \
"s_waitcnt vmcnt(28)\n" \
"s_barrier\n" \
"ds_read_b64_tr_b16 a[16:17], v2 offset:32768\n" \
"ds_read_b64_tr_b16 a[18:19], v2 offset:33280\n" \
"ds_read_b64_tr_b16 a[144:145], v3 offset:32768\n" \
"ds_read_b64_tr_b16 a[146:147], v3 offset:33280\n" \
"ds_read_b64_tr_b16 a[20:21], v2 offset:36864\n" \
"ds_read_b64_tr_b16 a[22:23], v2 offset:37376\n" \
"ds_read_b64_tr_b16 a[148:149], v3 offset:36864\n" \
"ds_read_b64_tr_b16 a[150:151], v3 offset:37376\n" \
"s_waitcnt lgkmcnt(0)\n" \
"s_barrier\n" \
"s_add_u32 m0, s52, 32768\n" \
"s_nop 0\n" \
"buffer_load_dwordx4 v1, s[40:43], s54 offen nt lds\n" \
"s_add_u32 s54, s54, s44\n" \
"s_add_u32 m0, s52, 34816\n" \
"s_nop 0\n" \
"buffer_load_dwordx4 v1, s[40:43], s54 offen nt lds\n" \
"s_add_u32 s54, s54, s44\n" \
"s_add_u32 m0, s52, 36864\n" \
"s_nop 0\n" \
"buffer_load_dwordx4 v1, s[40:43], s54 offen nt lds\n" \
"s_add_u32 s54, s54, s44\n" \
"s_add_u32 m0, s52, 38912\n" \
"s_nop 0\n" \
"buffer_load_dwordx4 v1, s[40:43], s54 offen nt lds\n" \
"s_add_u32 s54, s54, s44\n" \
"s_waitcnt vmcnt(28)\n" \
"s_barrier\n" \
"ds_read_b64_tr_b16 a[24:25], v2 offset:49152\n" \
"ds_read_b64_tr_b16 a[26:27], v2 offset:49664\n" \
"ds_read_b64_tr_b16 a[152:153], v3 offset:49152\n" \
"ds_read_b64_tr_b16 a[154:155], v3 offset:49664\n" \
"ds_read_b64_tr_b16 a[28:29], v2 offset:53248\n" \
"ds_read_b64_tr_b16 a[30:31], v2 offset:53760\n" \
"ds_read_b64_tr_b16 a[156:157], v3 offset:53248\n" \
"ds_read_b64_tr_b16 a[158:159], v3 offset:53760\n" \
"s_waitcnt lgkmcnt(0)\n" \
"s_barrier\n" \
"s_add_u32 m0, s52, 49152\n" \
"s_nop 0\n" \
"buffer_load_dwordx4 v1, s[40:43], s54 offen nt lds\n" \
"s_add_u32 s54, s54, s44\n" \
"s_add_u32 m0, s52, 51200\n" \
"s_nop 0\n" \
"buffer_load_dwordx4 v1, s[40:43], s54 offen nt lds\n" \
"s_add_u32 s54, s54, s44\n" \
"s_add_u32 m0, s52, 53248\n" \
"s_nop 0\n" \
"buffer_load_dwordx4 v1, s[40:43], s54 offen nt lds\n" \
"s_add_u32 s54, s54, s44\n" \
"s_add_u32 m0, s52, 55296\n" \
"s_nop 0\n" \
"buffer_load_dwordx4 v1, s[40:43], s54 offen nt lds\n" \
"s_add_u32 s54, s54, s44\n" \
"s_waitcnt vmcnt(28)\n" \
"s_barrier\n" \
"ds_read_b64_tr_b16 a[32:33], v8 offset:0\n" \
"ds_read_b64_tr_b16 a[34:35], v8 offset:512\n" \
"ds_read_b64_tr_b16 a[160:161], v9 offset:0\n" \
"ds_read_b64_tr_b16 a[162:163], v9 offset:512\n" \
"ds_read_b64_tr_b16 a[36:37], v8 offset:4096\n" \
"ds_read_b64_tr_b16 a[38:39], v8 offset:4608\n" \
"ds_read_b64_tr_b16 a[164:165], v9 offset:4096\n" \
"ds_read_b64_tr_b16 a[166:167], v9 offset:4608\n" \
"s_waitcnt lgkmcnt(0)\n" \
"s_barrier\n" \
"s_add_u32 m0, s52, 65536\n" \
"s_nop 0\n" \
"buffer_load_dwordx4 v1, s[40:43], s54 offen nt lds\n" \
"s_add_u32 s54, s54, s44\n" \
"s_add_u32 m0, s52, 67584\n" \
"s_nop 0\n" \
"buffer_load_dwordx4 v1, s[40:43], s54 offen nt lds\n" \
"s_add_u32 s54, s54, s44\n" \
"s_add_u32 m0, s52, 69632\n" \
"s_nop 0\n" \
"buffer_load_dwordx4 v1, s[40:43], s54 offen nt lds\n" \
"s_add_u32 s54, s54, s44\n" \
"s_add_u32 m0, s52, 71680\n" \
"s_nop 0\n" \
"buffer_load_dwordx4 v1, s[40:43], s54 offen nt lds\n" \
"s_add_u32 s54, s54, s44\n" \
"s_waitcnt vmcnt(28)\n" \
"s_barrier\n" \
"ds_read_b64_tr_b16 a[40:41], v8 offset:16384\n" \
"ds_read_b64_tr_b16 a[42:43], v8 offset:16896\n" \
"ds_read_b64_tr_b16 a[168:169], v9 offset:16384\n" \
"ds_read_b64_tr_b16 a[170:171], v9 offset:16896\n" \
"ds_read_b64_tr_b16 a[44:45], v8 offset:20480\n" \
"ds_read_b64_tr_b16 a[46:47], v8 offset:20992\n" \
"ds_read_b64_tr_b16 a[172:173], v9 offset:20480\n" \
"ds_read_b64_tr_b16 a[174:175], v9 offset:20992\n" \
"s_waitcnt lgkmcnt(0)\n" \
"s_barrier\n" \
"s_add_u32 m0, s52, 81920\n" \
"s_nop 0\n" \
"buffer_load_dwordx4 v1, s[40:43], s54 offen nt lds\n" \
"s_add_u32 s54, s54, s44\n" \
"s_add_u32 m0, s52, 83968\n" \
"s_nop 0\n" \
"buffer_load_dwordx4 v1, s[40:43], s54 offen nt lds\n" \
"s_add_u32 s54, s54, s44\n" \
"s_add_u32 m0, s52, 86016\n" \
"s_nop 0\n" \
"buffer_load_dwordx4 v1, s[40:43], s54 offen nt lds\n" \
"s_add_u32 s54, s54, s44\n" \
"s_add_u32 m0, s52, 88064\n" \
"s_nop 0\n" \
"buffer_load_dwordx4 v1, s[40:43], s54 offen nt lds\n" \
"s_add_u32 s54, s54, s44\n" \
"s_waitcnt vmcnt(28)\n" \
"s_barrier\n" \
"ds_read_b64_tr_b16 a[48:49], v8 offset:32768\n" \
"ds_read_b64_tr_b16 a[50:51], v8 offset:33280\n" \
"ds_read_b64_tr_b16 a[176:177], v9 offset:32768\n" \
"ds_read_b64_tr_b16 a[178:179], v9 offset:33280\n" \
"ds_read_b64_tr_b16 a[52:53], v8 offset:36864\n" \
"ds_read_b64_tr_b16 a[54:55], v8 offset:37376\n" \
"ds_read_b64_tr_b16 a[180:181], v9 offset:36864\n" \
"ds_read_b64_tr_b16 a[182:183], v9 offset:37376\n" \
"s_waitcnt lgkmcnt(0)\n" \
"s_barrier\n" \
"s_add_u32 m0, s52, 98304\n" \
"s_nop 0\n" \
"buffer_load_dwordx4 v1, s[40:43], s54 offen nt lds\n" \
"s_add_u32 s54, s54, s44\n" \
"s_add_u32 m0, s52, 100352\n" \
"s_nop 0\n" \
"buffer_load_dwordx4 v1, s[40:43], s54 offen nt lds\n" \
"s_add_u32 s54, s54, s44\n" \
"s_add_u32 m0, s52, 102400\n" \
"s_nop 0\n" \
"buffer_load_dwordx4 v1, s[40:43], s54 offen nt lds\n" \
"s_add_u32 s54, s54, s44\n" \
"s_add_u32 m0, s52, 104448\n" \
"s_nop 0\n" \
"buffer_load_dwordx4 v1, s[40:43], s54 offen nt lds\n" \
"s_add_u32 s54, s54, s44\n" \
"s_waitcnt vmcnt(28)\n" \
"s_barrier\n" \
"ds_read_b64_tr_b16 a[56:57], v8 offset:49152\n" \
"ds_read_b64_tr_b16 a[58:59], v8 offset:49664\n" \
"ds_read_b64_tr_b16 a[184:185], v9 offset:49152\n" \
"ds_read_b64_tr_b16 a[186:187], v9 offset:49664\n" \
"ds_read_b64_tr_b16 a[60:61], v8 offset:53248\n" \
"ds_read_b64_tr_b16 a[62:63], v8 offset:53760\n" \
"ds_read_b64_tr_b16 a[188:189], v9 offset:53248\n" \
"ds_read_b64_tr_b16 a[190:191], v9 offset:53760\n" \
"s_waitcnt lgkmcnt(0)\n" \
"s_barrier\n" \
"s_add_u32 m0, s52, 114688\n" \
"s_nop 0\n" \
"buffer_load_dwordx4 v1, s[40:43], s54 offen nt lds\n" \
"s_add_u32 s54, s54, s44\n" \
"s_add_u32 m0, s52, 116736\n" \
"s_nop 0\n" \
"buffer_load_dwordx4 v1, s[40:43], s54 offen nt lds\n" \
"s_add_u32 s54, s54, s44\n" \
"s_add_u32 m0, s52, 118784\n" \
"s_nop 0\n" \
"buffer_load_dwordx4 v1, s[40:43], s54 offen nt lds\n" \
"s_add_u32 s54, s54, s44\n" \
"s_add_u32 m0, s52, 120832\n" \
"s_nop 0\n" \
"buffer_load_dwordx4 v1, s[40:43], s54 offen nt lds\n" \
"s_add_u32 s54, s54, s44\n" \
"s_waitcnt vmcnt(28)\n" \
"s_barrier\n" \
"ds_read_b64_tr_b16 a[64:65], v2 offset:0\n" \
"ds_read_b64_tr_b16 a[66:67], v2 offset:512\n" \
"ds_read_b64_tr_b16 a[192:193], v3 offset:0\n" \
"ds_read_b64_tr_b16 a[194:195], v3 offset:512\n" \
"ds_read_b64_tr_b16 a[68:69], v2 offset:4096\n" \
"ds_read_b64_tr_b16 a[70:71], v2 offset:4608\n" \
"ds_read_b64_tr_b16 a[196:197], v3 offset:4096\n" \
"ds_read_b64_tr_b16 a[198:199], v3 offset:4608\n" \
"s_waitcnt vmcnt(24)\n" \
"s_barrier\n" \
"ds_read_b64_tr_b16 a[72:73], v2 offset:16384\n" \
"ds_read_b64_tr_b16 a[74:75], v2 offset:16896\n" \
"ds_read_b64_tr_b16 a[200:201], v3 offset:16384\n" \
"ds_read_b64_tr_b16 a[202:203], v3 offset:16896\n" \
"ds_read_b64_tr_b16 a[76:77], v2 offset:20480\n" \
"ds_read_b64_tr_b16 a[78:79], v2 offset:20992\n" \
"ds_read_b64_tr_b16 a[204:205], v3 offset:20480\n" \
"ds_read_b64_tr_b16 a[206:207], v3 offset:20992\n" \
"s_waitcnt vmcnt(20)\n" \
"s_barrier\n" \
"ds_read_b64_tr_b16 a[80:81], v2 offset:32768\n" \
"ds_read_b64_tr_b16 a[82:83], v2 offset:33280\n" \
"ds_read_b64_tr_b16 a[208:209], v3 offset:32768\n" \
"ds_read_b64_tr_b16 a[210:211], v3 offset:33280\n" \
"ds_read_b64_tr_b16 a[84:85], v2 offset:36864\n" \
"ds_read_b64_tr_b16 a[86:87], v2 offset:37376\n" \
"ds_read_b64_tr_b16 a[212:213], v3 offset:36864\n" \
"ds_read_b64_tr_b16 a[214:215], v3 offset:37376\n" \
"s_waitcnt vmcnt(16)\n" \
"s_barrier\n" \
"ds_read_b64_tr_b16 a[88:89], v2 offset:49152\n" \
"ds_read_b64_tr_b16 a[90:91], v2 offset:49664\n" \
"ds_read_b64_tr_b16 a[216:217], v3 offset:49152\n" \
"ds_read_b64_tr_b16 a[218:219], v3 offset:49664\n" \
"ds_read_b64_tr_b16 a[92:93], v2 offset:53248\n" \
"ds_read_b64_tr_b16 a[94:95], v2 offset:53760\n" \
"ds_read_b64_tr_b16 a[220:221], v3 offset:53248\n" \
"ds_read_b64_tr_b16 a[222:223], v3 offset:53760\n" \
"s_waitcnt vmcnt(12)\n" \
"s_barrier\n" \
"ds_read_b64_tr_b16 a[96:97], v8 offset:0\n" \
"ds_read_b64_tr_b16 a[98:99], v8 offset:512\n" \
"ds_read_b64_tr_b16 a[224:225], v9 offset:0\n" \
"ds_read_b64_tr_b16 a[226:227], v9 offset:512\n" \
"ds_read_b64_tr_b16 a[100:101], v8 offset:4096\n" \
"ds_read_b64_tr_b16 a[102:103], v8 offset:4608\n" \
"ds_read_b64_tr_b16 a[228:229], v9 offset:4096\n" \
"ds_read_b64_tr_b16 a[230:231], v9 offset:4608\n" \
"s_waitcnt vmcnt(8)\n" \
"s_barrier\n" \
"ds_read_b64_tr_b16 a[104:105], v8 offset:16384\n" \
"ds_read_b64_tr_b16 a[106:107], v8 offset:16896\n" \
"ds_read_b64_tr_b16 a[232:233], v9 offset:16384\n" \
"ds_read_b64_tr_b16 a[234:235], v9 offset:16896\n" \
"ds_read_b64_tr_b16 a[108:109], v8 offset:20480\n" \
"ds_read_b64_tr_b16 a[110:111], v8 offset:20992\n" \
"ds_read_b64_tr_b16 a[236:237], v9 offset:20480\n" \
"ds_read_b64_tr_b16 a[238:239], v9 offset:20992\n" \
"s_waitcnt vmcnt(4)\n" \
"s_barrier\n" \
"ds_read_b64_tr_b16 a[112:113], v8 offset:32768\n" \
"ds_read_b64_tr_b16 a[114:115], v8 offset:33280\n" \
"ds_read_b64_tr_b16 a[240:241], v9 offset:32768\n" \
"ds_read_b64_tr_b16 a[242:243], v9 offset:33280\n" \
"ds_read_b64_tr_b16 a[116:117], v8 offset:36864\n" \
"ds_read_b64_tr_b16 a[118:119], v8 offset:37376\n" \
"ds_read_b64_tr_b16 a[244:245], v9 offset:36864\n" \
"ds_read_b64_tr_b16 a[246:247], v9 offset:37376\n" \
"s_waitcnt vmcnt(0)\n" \
"s_barrier\n" \
"ds_read_b64_tr_b16 a[120:121], v8 offset:49152\n" \
"ds_read_b64_tr_b16 a[122:123], v8 offset:49664\n" \
"ds_read_b64_tr_b16 a[248:249], v9 offset:49152\n" \
"ds_read_b64_tr_b16 a[250:251], v9 offset:49664\n" \
"ds_read_b64_tr_b16 a[124:125], v8 offset:53248\n" \
"ds_read_b64_tr_b16 a[126:127], v8 offset:53760\n" \
"ds_read_b64_tr_b16 a[252:253], v9 offset:53248\n" \
"ds_read_b64_tr_b16 a[254:255], v9 offset:53760\n" \
"s_waitcnt lgkmcnt(0)\n" \
"s_barrier\n" \
"v_mov_b32 v14, 4096\n" \
"s_mov_b32 s59, s46\n" \
"s_add_u32 s63, s59, 0\n" \
"s_min_u32 s63, s63, 63\n" \
"s_lshr_b32 s63, s63, 3\n" \
"s_lshl_b32 s63, s63, 7\n" \
"s_branch .Lp1g1_l0_%=\n" \
".Lp1g1_l0k_%=:\n" \
"s_add_u32 s63, s59, 1\n" \
"s_min_u32 s63, s63, 63\n" \
"s_lshr_b32 s63, s63, 3\n" \
"s_lshl_b32 s63, s63, 7\n" \
"s_branch .Lp1g1_l1_%=\n" \
".Lp1g1_l1k_%=:\n" \
"s_lshl_b32 s55, s48, 13\n" \
"s_lshl_b32 s56, s46, 17\n" \
"s_add_u32 s56, s56, s45\n" \
"s_lshl_b32 s60, s47, 17\n" \
"s_sub_u32 s60, s60, 0x20000\n" \
"s_add_u32 s60, s60, s45\n" \
"s_mov_b32 s57, 0\n" \
"s_mov_b32 s58, 0\n" \
"v_mov_b32 v7, v5\n" \
"v_mov_b32 v5, 0x7ffffff0\n" \
"v_mov_b32 v10, v0\n" \
"v_mov_b32 v11, v6\n" \
"v_mov_b32 v6, 0x7ffffff0\n" \
"s_mov_b32 s59, s46\n" \
"s_min_u32 s61, s56, s60\n" \
"s_lshr_b32 s65, s61, 8\n" \
"s_and_b32 s65, s65, 0x2000\n" \
"s_xor_b32 s61, s61, s65\n" \
"s_add_u32 m0, s55, 0\n" \
"s_nop 0\n" \
"buffer_load_dwordx4 v4, s[36:39], s61 offen offset:0 sc1 lds\n" \
"buffer_load_dwordx4 v4, s[36:39], s61 offen offset:1024 sc1 lds\n" \
"buffer_load_dwordx4 v4, s[36:39], s61 offen offset:2048 sc1 lds\n" \
"buffer_load_dwordx4 v4, s[36:39], s61 offen offset:3072 sc1 lds\n" \
"s_add_u32 m0, s55, 4096\n" \
"s_add_u32 s61, s61, 4096\n" \
"buffer_load_dwordx4 v4, s[36:39], s61 offen offset:0 sc1 lds\n" \
"buffer_load_dwordx4 v4, s[36:39], s61 offen offset:1024 sc1 lds\n" \
"buffer_load_dwordx4 v4, s[36:39], s61 offen offset:2048 sc1 lds\n" \
"buffer_load_dwordx4 v4, s[36:39], s61 offen offset:3072 sc1 lds\n" \
"s_add_u32 s61, s61, 28672\n" \
"s_add_u32 s56, s56, 0x20000\n" \
"s_add_u32 m0, s55, 32768\n" \
"s_nop 0\n" \
"buffer_load_dwordx4 v4, s[36:39], s61 offen offset:0 sc1 lds\n" \
"buffer_load_dwordx4 v4, s[36:39], s61 offen offset:1024 sc1 lds\n" \
"buffer_load_dwordx4 v4, s[36:39], s61 offen offset:2048 sc1 lds\n" \
"buffer_load_dwordx4 v4, s[36:39], s61 offen offset:3072 sc1 lds\n" \
"s_add_u32 m0, s55, 36864\n" \
"s_add_u32 s61, s61, 4096\n" \
"buffer_load_dwordx4 v4, s[36:39], s61 offen offset:0 sc1 lds\n" \
"buffer_load_dwordx4 v4, s[36:39], s61 offen offset:1024 sc1 lds\n" \
"buffer_load_dwordx4 v4, s[36:39], s61 offen offset:2048 sc1 lds\n" \
"buffer_load_dwordx4 v4, s[36:39], s61 offen offset:3072 sc1 lds\n" \
"s_mov_b32 s70, 0\n" \
"s_mov_b32 s72, 0\n" \
"s_waitcnt vmcnt(0) lgkmcnt(0)\n" \
"s_barrier\n" \
"v_add_u32 v12, 0x10000, v10\n" \
"ds_read_b128 v[16:19], v10 offset:0\n" \
"ds_read_b128 v[20:23], v10 offset:16384\n" \
"ds_read_b128 v[24:27], v10 offset:1024\n" \
"ds_read_b128 v[28:31], v10 offset:17408\n" \
"ds_read_b128 v[32:35], v10 offset:2048\n" \
"ds_read_b128 v[36:39], v10 offset:18432\n" \
"s_waitcnt lgkmcnt(4)\n" \
"s_nop 7\n" \
"s_nop 7\n" \
".p2align 6\n" \
".Lp1g1_loop_%=:\n" \
"v_mfma_f32_16x16x32_bf16 v[48:51], a[0:3], v[16:19], 0\n" \
"s_min_u32 s61, s56, s60\n" \
"s_lshr_b32 s65, s61, 8\n" \
"s_and_b32 s65, s65, 0x2000\n" \
"s_xor_b32 s61, s61, s65\n" \
"s_add_u32 m0, s55, 65536\n" \
"s_nop 0\n" \
"buffer_load_dwordx4 v4, s[36:39], s61 offen offset:0 sc1 lds\n" \
"v_add_u32_e64 v13, -1, s59\n" \
"v_mfma_f32_16x16x32_bf16 v[52:55], a[0:3], v[20:23], 0\n" \
"ds_read_b128 v[40:43], v10 offset:3072\n" \
"s_nop 0\n" \
"v_mfma_f32_16x16x32_bf16 v[56:59], a[128:131], v[16:19], 0\n" \
"ds_read_b128 v[44:47], v10 offset:19456\n" \
"s_nop 0\n" \
"v_mfma_f32_16x16x32_bf16 v[60:63], a[128:131], v[20:23], 0\n" \
"s_waitcnt lgkmcnt(4)\n" \
"v_mul_f32 v64, 0xbfb8aa3b, v64\n" \
"v_mul_f32 v65, 0xbfb8aa3b, v65\n" \
"v_mfma_f32_16x16x32_bf16 v[48:51], a[4:7], v[24:27], v[48:51]\n" \
"buffer_load_dwordx4 v4, s[36:39], s61 offen offset:1024 sc1 lds\n" \
"v_mfma_f32_16x16x32_bf16 v[52:55], a[4:7], v[28:31], v[52:55]\n" \
"ds_read_b128 v[16:19], v10 offset:4096\n" \
"v_mul_f32 v66, 0xbfb8aa3b, v66\n" \
"v_mfma_f32_16x16x32_bf16 v[56:59], a[132:135], v[24:27], v[56:59]\n" \
"ds_read_b128 v[20:23], v10 offset:20480\n" \
"v_mul_f32 v67, 0xbfb8aa3b, v67\n" \
"v_mfma_f32_16x16x32_bf16 v[60:63], a[132:135], v[28:31], v[60:63]\n" \
"s_waitcnt lgkmcnt(4)\n" \
"v_exp_f32_e64 v64, v64\n" \
"v_mfma_f32_16x16x32_bf16 v[48:51], a[8:11], v[32:35], v[48:51]\n" \
"s_add_u32 s63, s59, 2\n" \
"s_min_u32 s63, s63, 63\n" \
"v_mfma_f32_16x16x32_bf16 v[52:55], a[8:11], v[36:39], v[52:55]\n" \
"ds_read_b128 v[24:27], v10 offset:5120\n" \
"s_lshr_b32 s63, s63, 3\n" \
"s_lshl_b32 s63, s63, 7\n" \
"v_mfma_f32_16x16x32_bf16 v[56:59], a[136:139], v[32:35], v[56:59]\n" \
"ds_read_b128 v[28:31], v10 offset:21504\n" \
"v_mfma_f32_16x16x32_bf16 v[60:63], a[136:139], v[36:39], v[60:63]\n" \
"s_waitcnt lgkmcnt(4)\n" \
"v_exp_f32 v65, v65\n" \
"v_mfma_f32_16x16x32_bf16 v[48:51], a[12:15], v[40:43], v[48:51]\n" \
"buffer_load_dwordx4 v4, s[36:39], s61 offen offset:2048 sc1 lds\n" \
"v_mfma_f32_16x16x32_bf16 v[52:55], a[12:15], v[44:47], v[52:55]\n" \
"ds_read_b128 v[32:35], v10 offset:6144\n" \
"v_mfma_f32_16x16x32_bf16 v[56:59], a[140:143], v[40:43], v[56:59]\n" \
"ds_read_b128 v[36:39], v10 offset:22528\n" \
"v_mfma_f32_16x16x32_bf16 v[60:63], a[140:143], v[44:47], v[60:63]\n" \
"s_waitcnt lgkmcnt(4)\n" \
"v_exp_f32 v66, v66\n" \
"v_mfma_f32_16x16x32_bf16 v[48:51], a[16:19], v[16:19], v[48:51]\n" \
"buffer_load_dwordx4 v4, s[36:39], s61 offen offset:3072 sc1 lds\n" \
"v_mfma_f32_16x16x32_bf16 v[52:55], a[16:19], v[20:23], v[52:55]\n" \
"ds_read_b128 v[40:43], v10 offset:7168\n" \
"v_mfma_f32_16x16x32_bf16 v[56:59], a[144:147], v[16:19], v[56:59]\n" \
"ds_read_b128 v[44:47], v10 offset:23552\n" \
"v_mfma_f32_16x16x32_bf16 v[60:63], a[144:147], v[20:23], v[60:63]\n" \
"s_waitcnt lgkmcnt(4)\n" \
"v_exp_f32 v67, v67\n" \
"v_mfma_f32_16x16x32_bf16 v[48:51], a[20:23], v[24:27], v[48:51]\n" \
"buffer_load_dword v15, v14, s[36:39], s63 offen sc1\n" \
"v_mfma_f32_16x16x32_bf16 v[52:55], a[20:23], v[28:31], v[52:55]\n" \
"ds_read_b128 v[16:19], v10 offset:8192\n" \
"v_add_f32_e64 v64, 1.0, v64\n" \
"v_mfma_f32_16x16x32_bf16 v[56:59], a[148:151], v[24:27], v[56:59]\n" \
"ds_read_b128 v[20:23], v10 offset:24576\n" \
"v_add_f32_e64 v65, 1.0, v65\n" \
"v_mfma_f32_16x16x32_bf16 v[60:63], a[148:151], v[28:31], v[60:63]\n" \
"s_waitcnt lgkmcnt(4)\n" \
"v_add_f32 v66, 1.0, v66\n" \
"v_add_f32_e64 v67, 1.0, v67\n" \
"v_mfma_f32_16x16x32_bf16 v[48:51], a[24:27], v[32:35], v[48:51]\n" \
"s_add_u32 m0, s55, 69632\n" \
"s_add_u32 s61, s61, 4096\n" \
"buffer_load_dwordx4 v4, s[36:39], s61 offen offset:0 sc1 lds\n" \
"v_mfma_f32_16x16x32_bf16 v[52:55], a[24:27], v[36:39], v[52:55]\n" \
"ds_read_b128 v[24:27], v10 offset:9216\n" \
"v_mfma_f32_16x16x32_bf16 v[56:59], a[152:155], v[32:35], v[56:59]\n" \
"ds_read_b128 v[28:31], v10 offset:25600\n" \
"v_mfma_f32_16x16x32_bf16 v[60:63], a[152:155], v[36:39], v[60:63]\n" \
"s_waitcnt lgkmcnt(4)\n" \
"v_rcp_f32 v64, v64\n" \
"v_mfma_f32_16x16x32_bf16 v[48:51], a[28:31], v[40:43], v[48:51]\n" \
"buffer_load_dwordx4 v4, s[36:39], s61 offen offset:1024 sc1 lds\n" \
"v_mfma_f32_16x16x32_bf16 v[52:55], a[28:31], v[44:47], v[52:55]\n" \
"ds_read_b128 v[32:35], v10 offset:10240\n" \
"v_mfma_f32_16x16x32_bf16 v[56:59], a[156:159], v[40:43], v[56:59]\n" \
"ds_read_b128 v[36:39], v10 offset:26624\n" \
"v_mfma_f32_16x16x32_bf16 v[60:63], a[156:159], v[44:47], v[60:63]\n" \
"s_waitcnt lgkmcnt(4)\n" \
"v_rcp_f32 v65, v65\n" \
"v_mfma_f32_16x16x32_bf16 v[48:51], a[32:35], v[16:19], v[48:51]\n" \
"v_rcp_f32_e64 v66, v66\n" \
"v_mfma_f32_16x16x32_bf16 v[52:55], a[32:35], v[20:23], v[52:55]\n" \
"ds_read_b128 v[40:43], v10 offset:11264\n" \
"v_mfma_f32_16x16x32_bf16 v[56:59], a[160:163], v[16:19], v[56:59]\n" \
"ds_read_b128 v[44:47], v10 offset:27648\n" \
"v_mfma_f32_16x16x32_bf16 v[60:63], a[160:163], v[20:23], v[60:63]\n" \
"s_waitcnt lgkmcnt(4)\n" \
"v_rcp_f32 v67, v67\n" \
"v_mfma_f32_16x16x32_bf16 v[48:51], a[36:39], v[24:27], v[48:51]\n" \
"buffer_load_dwordx4 v4, s[36:39], s61 offen offset:2048 sc1 lds\n" \
"v_mfma_f32_16x16x32_bf16 v[52:55], a[36:39], v[28:31], v[52:55]\n" \
"ds_read_b128 v[16:19], v10 offset:12288\n" \
"v_mul_f32_e64 v64, v64, v72\n" \
"v_mfma_f32_16x16x32_bf16 v[56:59], a[164:167], v[24:27], v[56:59]\n" \
"ds_read_b128 v[20:23], v10 offset:28672\n" \
"v_mul_f32_e64 v65, v65, v73\n" \
"v_mfma_f32_16x16x32_bf16 v[60:63], a[164:167], v[28:31], v[60:63]\n" \
"s_waitcnt lgkmcnt(4)\n" \
"v_mul_f32 v66, v66, v74\n" \
"v_mul_f32_e64 v67, v67, v75\n" \
"v_mfma_f32_16x16x32_bf16 v[48:51], a[40:43], v[32:35], v[48:51]\n" \
"buffer_load_dwordx4 v4, s[36:39], s61 offen offset:3072 sc1 lds\n" \
"v_mfma_f32_16x16x32_bf16 v[52:55], a[40:43], v[36:39], v[52:55]\n" \
"ds_read_b128 v[24:27], v10 offset:13312\n" \
"v_cvt_pk_bf16_f32 v80, v64, v65\n" \
"v_mfma_f32_16x16x32_bf16 v[56:59], a[168:171], v[32:35], v[56:59]\n" \
"ds_read_b128 v[28:31], v10 offset:29696\n" \
"v_cvt_pk_bf16_f32 v81, v66, v67\n" \
"v_mfma_f32_16x16x32_bf16 v[60:63], a[168:171], v[36:39], v[60:63]\n" \
"s_waitcnt lgkmcnt(4)\n" \
"v_mul_f32 v68, 0xbfb8aa3b, v68\n" \
"v_mul_f32 v69, 0xbfb8aa3b, v69\n" \
"v_mfma_f32_16x16x32_bf16 v[48:51], a[44:47], v[40:43], v[48:51]\n" \
"v_mul_f32 v70, 0xbfb8aa3b, v70\n" \
"v_mul_f32 v71, 0xbfb8aa3b, v71\n" \
"v_mfma_f32_16x16x32_bf16 v[52:55], a[44:47], v[44:47], v[52:55]\n" \
"ds_read_b128 v[32:35], v10 offset:14336\n" \
"v_mfma_f32_16x16x32_bf16 v[56:59], a[172:175], v[40:43], v[56:59]\n" \
"ds_read_b128 v[36:39], v10 offset:30720\n" \
"v_mfma_f32_16x16x32_bf16 v[60:63], a[172:175], v[44:47], v[60:63]\n" \
"s_waitcnt lgkmcnt(4)\n" \
"v_exp_f32_e64 v68, v68\n" \
"v_mfma_f32_16x16x32_bf16 v[48:51], a[48:51], v[16:19], v[48:51]\n" \
"v_exp_f32_e64 v69, v69\n" \
"v_mfma_f32_16x16x32_bf16 v[52:55], a[48:51], v[20:23], v[52:55]\n" \
"ds_read_b128 v[40:43], v10 offset:15360\n" \
"v_mfma_f32_16x16x32_bf16 v[56:59], a[176:179], v[16:19], v[56:59]\n" \
"ds_read_b128 v[44:47], v10 offset:31744\n" \
"v_mfma_f32_16x16x32_bf16 v[60:63], a[176:179], v[20:23], v[60:63]\n" \
"s_waitcnt lgkmcnt(4)\n" \
"v_exp_f32 v70, v70\n" \
"v_mfma_f32_16x16x32_bf16 v[48:51], a[52:55], v[24:27], v[48:51]\n" \
"s_waitcnt vmcnt(9)\n" \
"s_barrier\n" \
"v_mfma_f32_16x16x32_bf16 v[52:55], a[52:55], v[28:31], v[52:55]\n" \
"ds_read_b128 v[16:19], v10 offset:32768\n" \
"v_mfma_f32_16x16x32_bf16 v[56:59], a[180:183], v[24:27], v[56:59]\n" \
"ds_read_b128 v[20:23], v10 offset:49152\n" \
"v_mfma_f32_16x16x32_bf16 v[60:63], a[180:183], v[28:31], v[60:63]\n" \
"s_waitcnt lgkmcnt(4)\n" \
"buffer_store_dword v13, v6, s[36:39], 0 offen sc1\n" \
"v_exp_f32 v71, v71\n" \
"v_mfma_f32_16x16x32_bf16 v[48:51], a[56:59], v[32:35], v[48:51]\n" \
"v_add_f32 v68, 1.0, v68\n" \
"v_add_f32 v69, 1.0, v69\n" \
"v_mfma_f32_16x16x32_bf16 v[52:55], a[56:59], v[36:39], v[52:55]\n" \
"ds_read_b128 v[24:27], v10 offset:33792\n" \
"v_add_f32_e64 v70, 1.0, v70\n" \
"v_mfma_f32_16x16x32_bf16 v[56:59], a[184:187], v[32:35], v[56:59]\n" \
"ds_read_b128 v[28:31], v10 offset:50176\n" \
"v_add_f32_e64 v71, 1.0, v71\n" \
"v_mfma_f32_16x16x32_bf16 v[60:63], a[184:187], v[36:39], v[60:63]\n" \
"s_waitcnt lgkmcnt(4)\n" \
"v_rcp_f32 v68, v68\n" \
"v_mfma_f32_16x16x32_bf16 v[48:51], a[60:63], v[40:43], v[48:51]\n" \
"v_rcp_f32_e64 v69, v69\n" \
"v_mfma_f32_16x16x32_bf16 v[52:55], a[60:63], v[44:47], v[52:55]\n" \
"ds_read_b128 v[32:35], v10 offset:34816\n" \
"v_mfma_f32_16x16x32_bf16 v[56:59], a[188:191], v[40:43], v[56:59]\n" \
"ds_read_b128 v[36:39], v10 offset:51200\n" \
"v_mfma_f32_16x16x32_bf16 v[60:63], a[188:191], v[44:47], v[60:63]\n" \
"s_waitcnt lgkmcnt(4)\n" \
"v_rcp_f32 v70, v70\n" \
"v_mfma_f32_16x16x32_bf16 v[48:51], a[64:67], v[16:19], v[48:51]\n" \
"s_add_u32 s61, s61, 28672\n" \
"s_add_u32 s56, s56, 0x20000\n" \
"s_add_u32 m0, s55, 98304\n" \
"s_nop 0\n" \
"buffer_load_dwordx4 v4, s[36:39], s61 offen offset:0 sc1 lds\n" \
"v_mfma_f32_16x16x32_bf16 v[52:55], a[64:67], v[20:23], v[52:55]\n" \
"ds_read_b128 v[40:43], v10 offset:35840\n" \
"v_mfma_f32_16x16x32_bf16 v[56:59], a[192:195], v[16:19], v[56:59]\n" \
"ds_read_b128 v[44:47], v10 offset:52224\n" \
"v_mfma_f32_16x16x32_bf16 v[60:63], a[192:195], v[20:23], v[60:63]\n" \
"s_waitcnt lgkmcnt(4)\n" \
"v_rcp_f32_e64 v71, v71\n" \
"v_mfma_f32_16x16x32_bf16 v[48:51], a[68:71], v[24:27], v[48:51]\n" \
"buffer_load_dwordx4 v4, s[36:39], s61 offen offset:1024 sc1 lds\n" \
"v_mfma_f32_16x16x32_bf16 v[52:55], a[68:71], v[28:31], v[52:55]\n" \
"ds_read_b128 v[16:19], v10 offset:36864\n" \
"v_mul_f32_e64 v68, v68, v76\n" \
"v_mfma_f32_16x16x32_bf16 v[56:59], a[196:199], v[24:27], v[56:59]\n" \
"ds_read_b128 v[20:23], v10 offset:53248\n" \
"v_mul_f32_e64 v69, v69, v77\n" \
"v_mfma_f32_16x16x32_bf16 v[60:63], a[196:199], v[28:31], v[60:63]\n" \
"s_waitcnt lgkmcnt(4)\n" \
"v_mul_f32 v70, v70, v78\n" \
"v_mul_f32_e64 v71, v71, v79\n" \
"v_mfma_f32_16x16x32_bf16 v[48:51], a[72:75], v[32:35], v[48:51]\n" \
"v_cvt_pk_bf16_f32 v82, v68, v69\n" \
"v_cvt_pk_bf16_f32 v83, v70, v71\n" \
"v_mfma_f32_16x16x32_bf16 v[52:55], a[72:75], v[36:39], v[52:55]\n" \
"ds_read_b128 v[24:27], v10 offset:37888\n" \
"s_nop 0\n" \
"v_mfma_f32_16x16x32_bf16 v[56:59], a[200:203], v[32:35], v[56:59]\n" \
"ds_read_b128 v[28:31], v10 offset:54272\n" \
"s_nop 0\n" \
"v_mfma_f32_16x16x32_bf16 v[60:63], a[200:203], v[36:39], v[60:63]\n" \
"s_waitcnt lgkmcnt(4)\n" \
"v_permlane16_swap_b32 v80, v82\n" \
"v_permlane16_swap_b32 v81, v83\n" \
"v_mfma_f32_16x16x32_bf16 v[48:51], a[76:79], v[40:43], v[48:51]\n" \
"buffer_load_dwordx4 v4, s[36:39], s61 offen offset:2048 sc1 lds\n" \
"v_mfma_f32_16x16x32_bf16 v[52:55], a[76:79], v[44:47], v[52:55]\n" \
"ds_read_b128 v[32:35], v10 offset:38912\n" \
"buffer_store_dwordx4 v[80:83], v5, s[36:39], s57 offen sc1\n" \
"v_mfma_f32_16x16x32_bf16 v[56:59], a[204:207], v[40:43], v[56:59]\n" \
"ds_read_b128 v[36:39], v10 offset:55296\n" \
"v_mfma_f32_16x16x32_bf16 v[60:63], a[204:207], v[44:47], v[60:63]\n" \
"s_waitcnt lgkmcnt(4)\n" \
"v_mfma_f32_16x16x32_bf16 v[48:51], a[80:83], v[16:19], v[48:51]\n" \
"buffer_load_dwordx4 v4, s[36:39], s61 offen offset:3072 sc1 lds\n" \
"v_mfma_f32_16x16x32_bf16 v[52:55], a[80:83], v[20:23], v[52:55]\n" \
"ds_read_b128 v[40:43], v10 offset:39936\n" \
"v_mfma_f32_16x16x32_bf16 v[56:59], a[208:211], v[16:19], v[56:59]\n" \
"ds_read_b128 v[44:47], v10 offset:56320\n" \
"v_mfma_f32_16x16x32_bf16 v[60:63], a[208:211], v[20:23], v[60:63]\n" \
"s_waitcnt lgkmcnt(4)\n" \
"v_mfma_f32_16x16x32_bf16 v[48:51], a[84:87], v[24:27], v[48:51]\n" \
"v_mfma_f32_16x16x32_bf16 v[52:55], a[84:87], v[28:31], v[52:55]\n" \
"ds_read_b128 v[16:19], v10 offset:40960\n" \
"v_mfma_f32_16x16x32_bf16 v[56:59], a[212:215], v[24:27], v[56:59]\n" \
"ds_read_b128 v[20:23], v10 offset:57344\n" \
"v_mfma_f32_16x16x32_bf16 v[60:63], a[212:215], v[28:31], v[60:63]\n" \
"s_waitcnt lgkmcnt(4)\n" \
"v_mfma_f32_16x16x32_bf16 v[48:51], a[88:91], v[32:35], v[48:51]\n" \
"s_add_u32 m0, s55, 102400\n" \
"s_add_u32 s61, s61, 4096\n" \
"buffer_load_dwordx4 v4, s[36:39], s61 offen offset:0 sc1 lds\n" \
"v_mfma_f32_16x16x32_bf16 v[52:55], a[88:91], v[36:39], v[52:55]\n" \
"ds_read_b128 v[24:27], v10 offset:41984\n" \
"v_mfma_f32_16x16x32_bf16 v[56:59], a[216:219], v[32:35], v[56:59]\n" \
"ds_read_b128 v[28:31], v10 offset:58368\n" \
"v_mfma_f32_16x16x32_bf16 v[60:63], a[216:219], v[36:39], v[60:63]\n" \
"s_waitcnt lgkmcnt(4)\n" \
"v_mfma_f32_16x16x32_bf16 v[48:51], a[92:95], v[40:43], v[48:51]\n" \
"buffer_load_dwordx4 v4, s[36:39], s61 offen offset:1024 sc1 lds\n" \
"v_mfma_f32_16x16x32_bf16 v[52:55], a[92:95], v[44:47], v[52:55]\n" \
"ds_read_b128 v[32:35], v10 offset:43008\n" \
"v_mfma_f32_16x16x32_bf16 v[56:59], a[220:223], v[40:43], v[56:59]\n" \
"ds_read_b128 v[36:39], v10 offset:59392\n" \
"v_mfma_f32_16x16x32_bf16 v[60:63], a[220:223], v[44:47], v[60:63]\n" \
"s_waitcnt lgkmcnt(4)\n" \
"v_mfma_f32_16x16x32_bf16 v[48:51], a[96:99], v[16:19], v[48:51]\n" \
"v_mfma_f32_16x16x32_bf16 v[52:55], a[96:99], v[20:23], v[52:55]\n" \
"ds_read_b128 v[40:43], v10 offset:44032\n" \
"v_mfma_f32_16x16x32_bf16 v[56:59], a[224:227], v[16:19], v[56:59]\n" \
"ds_read_b128 v[44:47], v10 offset:60416\n" \
"v_mfma_f32_16x16x32_bf16 v[60:63], a[224:227], v[20:23], v[60:63]\n" \
"s_waitcnt lgkmcnt(4)\n" \
"v_mfma_f32_16x16x32_bf16 v[48:51], a[100:103], v[24:27], v[48:51]\n" \
"buffer_load_dwordx4 v4, s[36:39], s61 offen offset:2048 sc1 lds\n" \
"v_mfma_f32_16x16x32_bf16 v[52:55], a[100:103], v[28:31], v[52:55]\n" \
"ds_read_b128 v[16:19], v10 offset:45056\n" \
"v_mfma_f32_16x16x32_bf16 v[56:59], a[228:231], v[24:27], v[56:59]\n" \
"ds_read_b128 v[20:23], v10 offset:61440\n" \
"v_mfma_f32_16x16x32_bf16 v[60:63], a[228:231], v[28:31], v[60:63]\n" \
"s_waitcnt lgkmcnt(4)\n" \
"v_mfma_f32_16x16x32_bf16 v[48:51], a[104:107], v[32:35], v[48:51]\n" \
"buffer_load_dwordx4 v4, s[36:39], s61 offen offset:3072 sc1 lds\n" \
"v_mfma_f32_16x16x32_bf16 v[52:55], a[104:107], v[36:39], v[52:55]\n" \
"ds_read_b128 v[24:27], v10 offset:46080\n" \
"v_mfma_f32_16x16x32_bf16 v[56:59], a[232:235], v[32:35], v[56:59]\n" \
"ds_read_b128 v[28:31], v10 offset:62464\n" \
"v_mfma_f32_16x16x32_bf16 v[60:63], a[232:235], v[36:39], v[60:63]\n" \
"s_waitcnt lgkmcnt(4)\n" \
"v_mfma_f32_16x16x32_bf16 v[48:51], a[108:111], v[40:43], v[48:51]\n" \
"s_lshl_b32 s66, s59, s49\n" \
"s_lshr_b32 s65, s66, 8\n" \
"v_mfma_f32_16x16x32_bf16 v[52:55], a[108:111], v[44:47], v[52:55]\n" \
"ds_read_b128 v[32:35], v10 offset:47104\n" \
"v_mfma_f32_16x16x32_bf16 v[56:59], a[236:239], v[40:43], v[56:59]\n" \
"ds_read_b128 v[36:39], v10 offset:63488\n" \
"v_mfma_f32_16x16x32_bf16 v[60:63], a[236:239], v[44:47], v[60:63]\n" \
"s_waitcnt lgkmcnt(4)\n" \
"s_and_b32 s65, s65, 0x2000\n" \
"s_xor_b32 s66, s66, s65\n" \
"v_mfma_f32_16x16x32_bf16 v[48:51], a[112:115], v[16:19], v[48:51]\n" \
"v_mfma_f32_16x16x32_bf16 v[52:55], a[112:115], v[20:23], v[52:55]\n" \
"ds_read_b128 v[40:43], v10 offset:48128\n" \
"v_mfma_f32_16x16x32_bf16 v[56:59], a[240:243], v[16:19], v[56:59]\n" \
"ds_read_b128 v[44:47], v10 offset:64512\n" \
"v_mfma_f32_16x16x32_bf16 v[60:63], a[240:243], v[20:23], v[60:63]\n" \
"s_waitcnt lgkmcnt(4)\n" \
"v_mfma_f32_16x16x32_bf16 v[48:51], a[116:119], v[24:27], v[48:51]\n" \
"s_waitcnt vmcnt(10)\n" \
"s_barrier\n" \
"v_mfma_f32_16x16x32_bf16 v[52:55], a[116:119], v[28:31], v[52:55]\n" \
"ds_read_b128 v[16:19], v12 offset:0\n" \
"v_mfma_f32_16x16x32_bf16 v[56:59], a[244:247], v[24:27], v[56:59]\n" \
"ds_read_b128 v[20:23], v12 offset:16384\n" \
"v_mfma_f32_16x16x32_bf16 v[60:63], a[244:247], v[28:31], v[60:63]\n" \
"s_waitcnt lgkmcnt(4)\n" \
"v_mfma_f32_16x16x32_bf16 v[48:51], a[120:123], v[32:35], v[48:51]\n" \
"v_cmp_gt_u32 vcc, 16, v15\n" \
"v_mfma_f32_16x16x32_bf16 v[52:55], a[120:123], v[36:39], v[52:55]\n" \
"ds_read_b128 v[24:27], v12 offset:1024\n" \
"v_mfma_f32_16x16x32_bf16 v[56:59], a[248:251], v[32:35], v[56:59]\n" \
"ds_read_b128 v[28:31], v12 offset:17408\n" \
"v_mfma_f32_16x16x32_bf16 v[60:63], a[248:251], v[36:39], v[60:63]\n" \
"s_waitcnt lgkmcnt(4)\n" \
"v_mfma_f32_16x16x32_bf16 v[48:51], a[124:127], v[40:43], v[48:51]\n" \
"s_cbranch_vccnz .Lp1g1_m0_%=\n" \
".Lp1g1_m0k_%=:\n" \
"v_mfma_f32_16x16x32_bf16 v[52:55], a[124:127], v[44:47], v[52:55]\n" \
"ds_read_b128 v[32:35], v12 offset:2048\n" \
"v_mfma_f32_16x16x32_bf16 v[56:59], a[252:255], v[40:43], v[56:59]\n" \
"ds_read_b128 v[36:39], v12 offset:18432\n" \
"v_mfma_f32_16x16x32_bf16 v[60:63], a[252:255], v[44:47], v[60:63]\n" \
"s_waitcnt lgkmcnt(4)\n" \
"s_xor_b32 s57, s66, s64\n" \
"s_add_u32 s58, s57, s50\n" \
"s_add_u32 s59, s59, 1\n" \
"v_mov_b32 v5, v7\n" \
"v_mov_b32 v6, v11\n" \
"s_cmp_ge_u32 s59, s47\n" \
"s_cbranch_scc1 .Lp1g1_out0_%=\n" \
"v_mfma_f32_16x16x32_bf16 v[64:67], a[0:3], v[16:19], 0\n" \
"s_min_u32 s61, s56, s60\n" \
"s_lshr_b32 s65, s61, 8\n" \
"s_and_b32 s65, s65, 0x2000\n" \
"s_xor_b32 s61, s61, s65\n" \
"s_add_u32 m0, s55, 0\n" \
"s_nop 0\n" \
"buffer_load_dwordx4 v4, s[36:39], s61 offen offset:0 sc1 lds\n" \
"v_add_u32_e64 v13, -1, s59\n" \
"v_mfma_f32_16x16x32_bf16 v[68:71], a[0:3], v[20:23], 0\n" \
"ds_read_b128 v[40:43], v12 offset:3072\n" \
"s_nop 0\n" \
"v_mfma_f32_16x16x32_bf16 v[72:75], a[128:131], v[16:19], 0\n" \
"ds_read_b128 v[44:47], v12 offset:19456\n" \
"s_nop 0\n" \
"v_mfma_f32_16x16x32_bf16 v[76:79], a[128:131], v[20:23], 0\n" \
"s_waitcnt lgkmcnt(4)\n" \
"v_mul_f32 v48, 0xbfb8aa3b, v48\n" \
"v_mul_f32 v49, 0xbfb8aa3b, v49\n" \
"v_mfma_f32_16x16x32_bf16 v[64:67], a[4:7], v[24:27], v[64:67]\n" \
"buffer_load_dwordx4 v4, s[36:39], s61 offen offset:1024 sc1 lds\n" \
"v_mfma_f32_16x16x32_bf16 v[68:71], a[4:7], v[28:31], v[68:71]\n" \
"ds_read_b128 v[16:19], v12 offset:4096\n" \
"v_mul_f32 v50, 0xbfb8aa3b, v50\n" \
"v_mfma_f32_16x16x32_bf16 v[72:75], a[132:135], v[24:27], v[72:75]\n" \
"ds_read_b128 v[20:23], v12 offset:20480\n" \
"v_mul_f32 v51, 0xbfb8aa3b, v51\n" \
"v_mfma_f32_16x16x32_bf16 v[76:79], a[132:135], v[28:31], v[76:79]\n" \
"s_waitcnt lgkmcnt(4)\n" \
"v_exp_f32 v48, v48\n" \
"v_mfma_f32_16x16x32_bf16 v[64:67], a[8:11], v[32:35], v[64:67]\n" \
"s_add_u32 s63, s59, 2\n" \
"s_min_u32 s63, s63, 63\n" \
"v_mfma_f32_16x16x32_bf16 v[68:71], a[8:11], v[36:39], v[68:71]\n" \
"ds_read_b128 v[24:27], v12 offset:5120\n" \
"s_lshr_b32 s63, s63, 3\n" \
"s_lshl_b32 s63, s63, 7\n" \
"v_mfma_f32_16x16x32_bf16 v[72:75], a[136:139], v[32:35], v[72:75]\n" \
"ds_read_b128 v[28:31], v12 offset:21504\n" \
"v_mfma_f32_16x16x32_bf16 v[76:79], a[136:139], v[36:39], v[76:79]\n" \
"s_waitcnt lgkmcnt(4)\n" \
"v_exp_f32 v49, v49\n" \
"v_mfma_f32_16x16x32_bf16 v[64:67], a[12:15], v[40:43], v[64:67]\n" \
"buffer_load_dwordx4 v4, s[36:39], s61 offen offset:2048 sc1 lds\n" \
"v_mfma_f32_16x16x32_bf16 v[68:71], a[12:15], v[44:47], v[68:71]\n" \
"ds_read_b128 v[32:35], v12 offset:6144\n" \
"v_mfma_f32_16x16x32_bf16 v[72:75], a[140:143], v[40:43], v[72:75]\n" \
"ds_read_b128 v[36:39], v12 offset:22528\n" \
"v_mfma_f32_16x16x32_bf16 v[76:79], a[140:143], v[44:47], v[76:79]\n" \
"s_waitcnt lgkmcnt(4)\n" \
"v_exp_f32 v50, v50\n" \
"v_mfma_f32_16x16x32_bf16 v[64:67], a[16:19], v[16:19], v[64:67]\n" \
"buffer_load_dwordx4 v4, s[36:39], s61 offen offset:3072 sc1 lds\n" \
"v_mfma_f32_16x16x32_bf16 v[68:71], a[16:19], v[20:23], v[68:71]\n" \
"ds_read_b128 v[40:43], v12 offset:7168\n" \
"v_mfma_f32_16x16x32_bf16 v[72:75], a[144:147], v[16:19], v[72:75]\n" \
"ds_read_b128 v[44:47], v12 offset:23552\n" \
"v_mfma_f32_16x16x32_bf16 v[76:79], a[144:147], v[20:23], v[76:79]\n" \
"s_waitcnt lgkmcnt(4)\n" \
"v_exp_f32 v51, v51\n" \
"v_mfma_f32_16x16x32_bf16 v[64:67], a[20:23], v[24:27], v[64:67]\n" \
"buffer_load_dword v15, v14, s[36:39], s63 offen sc1\n" \
"v_mfma_f32_16x16x32_bf16 v[68:71], a[20:23], v[28:31], v[68:71]\n" \
"ds_read_b128 v[16:19], v12 offset:8192\n" \
"v_add_f32_e64 v48, 1.0, v48\n" \
"v_mfma_f32_16x16x32_bf16 v[72:75], a[148:151], v[24:27], v[72:75]\n" \
"ds_read_b128 v[20:23], v12 offset:24576\n" \
"v_add_f32_e64 v49, 1.0, v49\n" \
"v_mfma_f32_16x16x32_bf16 v[76:79], a[148:151], v[28:31], v[76:79]\n" \
"s_waitcnt lgkmcnt(4)\n" \
"v_add_f32 v50, 1.0, v50\n" \
"v_add_f32_e64 v51, 1.0, v51\n" \
"v_mfma_f32_16x16x32_bf16 v[64:67], a[24:27], v[32:35], v[64:67]\n" \
"s_add_u32 m0, s55, 4096\n" \
"s_add_u32 s61, s61, 4096\n" \
"buffer_load_dwordx4 v4, s[36:39], s61 offen offset:0 sc1 lds\n" \
"v_mfma_f32_16x16x32_bf16 v[68:71], a[24:27], v[36:39], v[68:71]\n" \
"ds_read_b128 v[24:27], v12 offset:9216\n" \
"v_mfma_f32_16x16x32_bf16 v[72:75], a[152:155], v[32:35], v[72:75]\n" \
"ds_read_b128 v[28:31], v12 offset:25600\n" \
"v_mfma_f32_16x16x32_bf16 v[76:79], a[152:155], v[36:39], v[76:79]\n" \
"s_waitcnt lgkmcnt(4)\n" \
"v_rcp_f32 v48, v48\n" \
"v_mfma_f32_16x16x32_bf16 v[64:67], a[28:31], v[40:43], v[64:67]\n" \
"buffer_load_dwordx4 v4, s[36:39], s61 offen offset:1024 sc1 lds\n" \
"v_mfma_f32_16x16x32_bf16 v[68:71], a[28:31], v[44:47], v[68:71]\n" \
"ds_read_b128 v[32:35], v12 offset:10240\n" \
"v_mfma_f32_16x16x32_bf16 v[72:75], a[156:159], v[40:43], v[72:75]\n" \
"ds_read_b128 v[36:39], v12 offset:26624\n" \
"v_mfma_f32_16x16x32_bf16 v[76:79], a[156:159], v[44:47], v[76:79]\n" \
"s_waitcnt lgkmcnt(4)\n" \
"v_rcp_f32 v49, v49\n" \
"v_mfma_f32_16x16x32_bf16 v[64:67], a[32:35], v[16:19], v[64:67]\n" \
"v_rcp_f32_e64 v50, v50\n" \
"v_mfma_f32_16x16x32_bf16 v[68:71], a[32:35], v[20:23], v[68:71]\n" \
"ds_read_b128 v[40:43], v12 offset:11264\n" \
"v_mfma_f32_16x16x32_bf16 v[72:75], a[160:163], v[16:19], v[72:75]\n" \
"ds_read_b128 v[44:47], v12 offset:27648\n" \
"v_mfma_f32_16x16x32_bf16 v[76:79], a[160:163], v[20:23], v[76:79]\n" \
"s_waitcnt lgkmcnt(4)\n" \
"v_rcp_f32 v51, v51\n" \
"v_mfma_f32_16x16x32_bf16 v[64:67], a[36:39], v[24:27], v[64:67]\n" \
"buffer_load_dwordx4 v4, s[36:39], s61 offen offset:2048 sc1 lds\n" \
"v_mfma_f32_16x16x32_bf16 v[68:71], a[36:39], v[28:31], v[68:71]\n" \
"ds_read_b128 v[16:19], v12 offset:12288\n" \
"v_mul_f32_e64 v48, v48, v56\n" \
"v_mfma_f32_16x16x32_bf16 v[72:75], a[164:167], v[24:27], v[72:75]\n" \
"ds_read_b128 v[20:23], v12 offset:28672\n" \
"v_mul_f32_e64 v49, v49, v57\n" \
"v_mfma_f32_16x16x32_bf16 v[76:79], a[164:167], v[28:31], v[76:79]\n" \
"s_waitcnt lgkmcnt(4)\n" \
"v_mul_f32 v50, v50, v58\n" \
"v_mul_f32_e64 v51, v51, v59\n" \
"v_mfma_f32_16x16x32_bf16 v[64:67], a[40:43], v[32:35], v[64:67]\n" \
"buffer_load_dwordx4 v4, s[36:39], s61 offen offset:3072 sc1 lds\n" \
"v_mfma_f32_16x16x32_bf16 v[68:71], a[40:43], v[36:39], v[68:71]\n" \
"ds_read_b128 v[24:27], v12 offset:13312\n" \
"v_cvt_pk_bf16_f32 v80, v48, v49\n" \
"v_mfma_f32_16x16x32_bf16 v[72:75], a[168:171], v[32:35], v[72:75]\n" \
"ds_read_b128 v[28:31], v12 offset:29696\n" \
"v_cvt_pk_bf16_f32 v81, v50, v51\n" \
"v_mfma_f32_16x16x32_bf16 v[76:79], a[168:171], v[36:39], v[76:79]\n" \
"s_waitcnt lgkmcnt(4)\n" \
"v_mul_f32 v52, 0xbfb8aa3b, v52\n" \
"v_mul_f32 v53, 0xbfb8aa3b, v53\n" \
"v_mfma_f32_16x16x32_bf16 v[64:67], a[44:47], v[40:43], v[64:67]\n" \
"v_mul_f32 v54, 0xbfb8aa3b, v54\n" \
"v_mul_f32 v55, 0xbfb8aa3b, v55\n" \
"v_mfma_f32_16x16x32_bf16 v[68:71], a[44:47], v[44:47], v[68:71]\n" \
"ds_read_b128 v[32:35], v12 offset:14336\n" \
"v_mfma_f32_16x16x32_bf16 v[72:75], a[172:175], v[40:43], v[72:75]\n" \
"ds_read_b128 v[36:39], v12 offset:30720\n" \
"v_mfma_f32_16x16x32_bf16 v[76:79], a[172:175], v[44:47], v[76:79]\n" \
"s_waitcnt lgkmcnt(4)\n" \
"v_exp_f32_e64 v52, v52\n" \
"v_mfma_f32_16x16x32_bf16 v[64:67], a[48:51], v[16:19], v[64:67]\n" \
"v_exp_f32_e64 v53, v53\n" \
"v_mfma_f32_16x16x32_bf16 v[68:71], a[48:51], v[20:23], v[68:71]\n" \
"ds_read_b128 v[40:43], v12 offset:15360\n" \
"v_mfma_f32_16x16x32_bf16 v[72:75], a[176:179], v[16:19], v[72:75]\n" \
"ds_read_b128 v[44:47], v12 offset:31744\n" \
"v_mfma_f32_16x16x32_bf16 v[76:79], a[176:179], v[20:23], v[76:79]\n" \
"s_waitcnt lgkmcnt(4)\n" \
"v_exp_f32 v54, v54\n" \
"v_mfma_f32_16x16x32_bf16 v[64:67], a[52:55], v[24:27], v[64:67]\n" \
"s_waitcnt vmcnt(9)\n" \
"s_barrier\n" \
"v_mfma_f32_16x16x32_bf16 v[68:71], a[52:55], v[28:31], v[68:71]\n" \
"ds_read_b128 v[16:19], v12 offset:32768\n" \
"v_mfma_f32_16x16x32_bf16 v[72:75], a[180:183], v[24:27], v[72:75]\n" \
"ds_read_b128 v[20:23], v12 offset:49152\n" \
"v_mfma_f32_16x16x32_bf16 v[76:79], a[180:183], v[28:31], v[76:79]\n" \
"s_waitcnt lgkmcnt(4)\n" \
"buffer_store_dword v13, v6, s[36:39], 0 offen sc1\n" \
"v_exp_f32 v55, v55\n" \
"v_mfma_f32_16x16x32_bf16 v[64:67], a[56:59], v[32:35], v[64:67]\n" \
"v_add_f32 v52, 1.0, v52\n" \
"v_add_f32 v53, 1.0, v53\n" \
"v_mfma_f32_16x16x32_bf16 v[68:71], a[56:59], v[36:39], v[68:71]\n" \
"ds_read_b128 v[24:27], v12 offset:33792\n" \
"v_add_f32_e64 v54, 1.0, v54\n" \
"v_mfma_f32_16x16x32_bf16 v[72:75], a[184:187], v[32:35], v[72:75]\n" \
"ds_read_b128 v[28:31], v12 offset:50176\n" \
"v_add_f32_e64 v55, 1.0, v55\n" \
"v_mfma_f32_16x16x32_bf16 v[76:79], a[184:187], v[36:39], v[76:79]\n" \
"s_waitcnt lgkmcnt(4)\n" \
"v_rcp_f32 v52, v52\n" \
"v_mfma_f32_16x16x32_bf16 v[64:67], a[60:63], v[40:43], v[64:67]\n" \
"v_rcp_f32_e64 v53, v53\n" \
"v_mfma_f32_16x16x32_bf16 v[68:71], a[60:63], v[44:47], v[68:71]\n" \
"ds_read_b128 v[32:35], v12 offset:34816\n" \
"v_mfma_f32_16x16x32_bf16 v[72:75], a[188:191], v[40:43], v[72:75]\n" \
"ds_read_b128 v[36:39], v12 offset:51200\n" \
"v_mfma_f32_16x16x32_bf16 v[76:79], a[188:191], v[44:47], v[76:79]\n" \
"s_waitcnt lgkmcnt(4)\n" \
"v_rcp_f32 v54, v54\n" \
"v_mfma_f32_16x16x32_bf16 v[64:67], a[64:67], v[16:19], v[64:67]\n" \
"s_add_u32 s61, s61, 28672\n" \
"s_add_u32 s56, s56, 0x20000\n" \
"s_add_u32 m0, s55, 32768\n" \
"s_nop 0\n" \
"buffer_load_dwordx4 v4, s[36:39], s61 offen offset:0 sc1 lds\n" \
"v_mfma_f32_16x16x32_bf16 v[68:71], a[64:67], v[20:23], v[68:71]\n" \
"ds_read_b128 v[40:43], v12 offset:35840\n" \
"v_mfma_f32_16x16x32_bf16 v[72:75], a[192:195], v[16:19], v[72:75]\n" \
"ds_read_b128 v[44:47], v12 offset:52224\n" \
"v_mfma_f32_16x16x32_bf16 v[76:79], a[192:195], v[20:23], v[76:79]\n" \
"s_waitcnt lgkmcnt(4)\n" \
"v_rcp_f32_e64 v55, v55\n" \
"v_mfma_f32_16x16x32_bf16 v[64:67], a[68:71], v[24:27], v[64:67]\n" \
"buffer_load_dwordx4 v4, s[36:39], s61 offen offset:1024 sc1 lds\n" \
"v_mfma_f32_16x16x32_bf16 v[68:71], a[68:71], v[28:31], v[68:71]\n" \
"ds_read_b128 v[16:19], v12 offset:36864\n" \
"v_mul_f32_e64 v52, v52, v60\n" \
"v_mfma_f32_16x16x32_bf16 v[72:75], a[196:199], v[24:27], v[72:75]\n" \
"ds_read_b128 v[20:23], v12 offset:53248\n" \
"v_mul_f32_e64 v53, v53, v61\n" \
"v_mfma_f32_16x16x32_bf16 v[76:79], a[196:199], v[28:31], v[76:79]\n" \
"s_waitcnt lgkmcnt(4)\n" \
"v_mul_f32 v54, v54, v62\n" \
"v_mul_f32_e64 v55, v55, v63\n" \
"v_mfma_f32_16x16x32_bf16 v[64:67], a[72:75], v[32:35], v[64:67]\n" \
"v_cvt_pk_bf16_f32 v82, v52, v53\n" \
"v_cvt_pk_bf16_f32 v83, v54, v55\n" \
"v_mfma_f32_16x16x32_bf16 v[68:71], a[72:75], v[36:39], v[68:71]\n" \
"ds_read_b128 v[24:27], v12 offset:37888\n" \
"s_nop 0\n" \
"v_mfma_f32_16x16x32_bf16 v[72:75], a[200:203], v[32:35], v[72:75]\n" \
"ds_read_b128 v[28:31], v12 offset:54272\n" \
"s_nop 0\n" \
"v_mfma_f32_16x16x32_bf16 v[76:79], a[200:203], v[36:39], v[76:79]\n" \
"s_waitcnt lgkmcnt(4)\n" \
"v_permlane16_swap_b32 v80, v82\n" \
"v_permlane16_swap_b32 v81, v83\n" \
"v_mfma_f32_16x16x32_bf16 v[64:67], a[76:79], v[40:43], v[64:67]\n" \
"buffer_load_dwordx4 v4, s[36:39], s61 offen offset:2048 sc1 lds\n" \
"v_mfma_f32_16x16x32_bf16 v[68:71], a[76:79], v[44:47], v[68:71]\n" \
"ds_read_b128 v[32:35], v12 offset:38912\n" \
"buffer_store_dwordx4 v[80:83], v5, s[36:39], s57 offen sc1\n" \
"v_mfma_f32_16x16x32_bf16 v[72:75], a[204:207], v[40:43], v[72:75]\n" \
"ds_read_b128 v[36:39], v12 offset:55296\n" \
"v_mfma_f32_16x16x32_bf16 v[76:79], a[204:207], v[44:47], v[76:79]\n" \
"s_waitcnt lgkmcnt(4)\n" \
"v_mfma_f32_16x16x32_bf16 v[64:67], a[80:83], v[16:19], v[64:67]\n" \
"buffer_load_dwordx4 v4, s[36:39], s61 offen offset:3072 sc1 lds\n" \
"v_mfma_f32_16x16x32_bf16 v[68:71], a[80:83], v[20:23], v[68:71]\n" \
"ds_read_b128 v[40:43], v12 offset:39936\n" \
"v_mfma_f32_16x16x32_bf16 v[72:75], a[208:211], v[16:19], v[72:75]\n" \
"ds_read_b128 v[44:47], v12 offset:56320\n" \
"v_mfma_f32_16x16x32_bf16 v[76:79], a[208:211], v[20:23], v[76:79]\n" \
"s_waitcnt lgkmcnt(4)\n" \
"v_mfma_f32_16x16x32_bf16 v[64:67], a[84:87], v[24:27], v[64:67]\n" \
"v_mfma_f32_16x16x32_bf16 v[68:71], a[84:87], v[28:31], v[68:71]\n" \
"ds_read_b128 v[16:19], v12 offset:40960\n" \
"v_mfma_f32_16x16x32_bf16 v[72:75], a[212:215], v[24:27], v[72:75]\n" \
"ds_read_b128 v[20:23], v12 offset:57344\n" \
"v_mfma_f32_16x16x32_bf16 v[76:79], a[212:215], v[28:31], v[76:79]\n" \
"s_waitcnt lgkmcnt(4)\n" \
"v_mfma_f32_16x16x32_bf16 v[64:67], a[88:91], v[32:35], v[64:67]\n" \
"s_add_u32 m0, s55, 36864\n" \
"s_add_u32 s61, s61, 4096\n" \
"buffer_load_dwordx4 v4, s[36:39], s61 offen offset:0 sc1 lds\n" \
"v_mfma_f32_16x16x32_bf16 v[68:71], a[88:91], v[36:39], v[68:71]\n" \
"ds_read_b128 v[24:27], v12 offset:41984\n" \
"v_mfma_f32_16x16x32_bf16 v[72:75], a[216:219], v[32:35], v[72:75]\n" \
"ds_read_b128 v[28:31], v12 offset:58368\n" \
"v_mfma_f32_16x16x32_bf16 v[76:79], a[216:219], v[36:39], v[76:79]\n" \
"s_waitcnt lgkmcnt(4)\n" \
"v_mfma_f32_16x16x32_bf16 v[64:67], a[92:95], v[40:43], v[64:67]\n" \
"buffer_load_dwordx4 v4, s[36:39], s61 offen offset:1024 sc1 lds\n" \
"v_mfma_f32_16x16x32_bf16 v[68:71], a[92:95], v[44:47], v[68:71]\n" \
"ds_read_b128 v[32:35], v12 offset:43008\n" \
"v_mfma_f32_16x16x32_bf16 v[72:75], a[220:223], v[40:43], v[72:75]\n" \
"ds_read_b128 v[36:39], v12 offset:59392\n" \
"v_mfma_f32_16x16x32_bf16 v[76:79], a[220:223], v[44:47], v[76:79]\n" \
"s_waitcnt lgkmcnt(4)\n" \
"v_mfma_f32_16x16x32_bf16 v[64:67], a[96:99], v[16:19], v[64:67]\n" \
"v_mfma_f32_16x16x32_bf16 v[68:71], a[96:99], v[20:23], v[68:71]\n" \
"ds_read_b128 v[40:43], v12 offset:44032\n" \
"v_mfma_f32_16x16x32_bf16 v[72:75], a[224:227], v[16:19], v[72:75]\n" \
"ds_read_b128 v[44:47], v12 offset:60416\n" \
"v_mfma_f32_16x16x32_bf16 v[76:79], a[224:227], v[20:23], v[76:79]\n" \
"s_waitcnt lgkmcnt(4)\n" \
"v_mfma_f32_16x16x32_bf16 v[64:67], a[100:103], v[24:27], v[64:67]\n" \
"buffer_load_dwordx4 v4, s[36:39], s61 offen offset:2048 sc1 lds\n" \
"v_mfma_f32_16x16x32_bf16 v[68:71], a[100:103], v[28:31], v[68:71]\n" \
"ds_read_b128 v[16:19], v12 offset:45056\n" \
"v_mfma_f32_16x16x32_bf16 v[72:75], a[228:231], v[24:27], v[72:75]\n" \
"ds_read_b128 v[20:23], v12 offset:61440\n" \
"v_mfma_f32_16x16x32_bf16 v[76:79], a[228:231], v[28:31], v[76:79]\n" \
"s_waitcnt lgkmcnt(4)\n" \
"v_mfma_f32_16x16x32_bf16 v[64:67], a[104:107], v[32:35], v[64:67]\n" \
"buffer_load_dwordx4 v4, s[36:39], s61 offen offset:3072 sc1 lds\n" \
"v_mfma_f32_16x16x32_bf16 v[68:71], a[104:107], v[36:39], v[68:71]\n" \
"ds_read_b128 v[24:27], v12 offset:46080\n" \
"v_mfma_f32_16x16x32_bf16 v[72:75], a[232:235], v[32:35], v[72:75]\n" \
"ds_read_b128 v[28:31], v12 offset:62464\n" \
"v_mfma_f32_16x16x32_bf16 v[76:79], a[232:235], v[36:39], v[76:79]\n" \
"s_waitcnt lgkmcnt(4)\n" \
"v_mfma_f32_16x16x32_bf16 v[64:67], a[108:111], v[40:43], v[64:67]\n" \
"s_lshl_b32 s66, s59, s49\n" \
"s_lshr_b32 s65, s66, 8\n" \
"v_mfma_f32_16x16x32_bf16 v[68:71], a[108:111], v[44:47], v[68:71]\n" \
"ds_read_b128 v[32:35], v12 offset:47104\n" \
"v_mfma_f32_16x16x32_bf16 v[72:75], a[236:239], v[40:43], v[72:75]\n" \
"ds_read_b128 v[36:39], v12 offset:63488\n" \
"v_mfma_f32_16x16x32_bf16 v[76:79], a[236:239], v[44:47], v[76:79]\n" \
"s_waitcnt lgkmcnt(4)\n" \
"s_and_b32 s65, s65, 0x2000\n" \
"s_xor_b32 s66, s66, s65\n" \
"v_mfma_f32_16x16x32_bf16 v[64:67], a[112:115], v[16:19], v[64:67]\n" \
"v_mfma_f32_16x16x32_bf16 v[68:71], a[112:115], v[20:23], v[68:71]\n" \
"ds_read_b128 v[40:43], v12 offset:48128\n" \
"v_mfma_f32_16x16x32_bf16 v[72:75], a[240:243], v[16:19], v[72:75]\n" \
"ds_read_b128 v[44:47], v12 offset:64512\n" \
"v_mfma_f32_16x16x32_bf16 v[76:79], a[240:243], v[20:23], v[76:79]\n" \
"s_waitcnt lgkmcnt(4)\n" \
"v_mfma_f32_16x16x32_bf16 v[64:67], a[116:119], v[24:27], v[64:67]\n" \
"s_waitcnt vmcnt(10)\n" \
"s_barrier\n" \
"v_mfma_f32_16x16x32_bf16 v[68:71], a[116:119], v[28:31], v[68:71]\n" \
"ds_read_b128 v[16:19], v10 offset:0\n" \
"v_mfma_f32_16x16x32_bf16 v[72:75], a[244:247], v[24:27], v[72:75]\n" \
"ds_read_b128 v[20:23], v10 offset:16384\n" \
"v_mfma_f32_16x16x32_bf16 v[76:79], a[244:247], v[28:31], v[76:79]\n" \
"s_waitcnt lgkmcnt(4)\n" \
"v_mfma_f32_16x16x32_bf16 v[64:67], a[120:123], v[32:35], v[64:67]\n" \
"v_cmp_gt_u32 vcc, 16, v15\n" \
"v_mfma_f32_16x16x32_bf16 v[68:71], a[120:123], v[36:39], v[68:71]\n" \
"ds_read_b128 v[24:27], v10 offset:1024\n" \
"v_mfma_f32_16x16x32_bf16 v[72:75], a[248:251], v[32:35], v[72:75]\n" \
"ds_read_b128 v[28:31], v10 offset:17408\n" \
"v_mfma_f32_16x16x32_bf16 v[76:79], a[248:251], v[36:39], v[76:79]\n" \
"s_waitcnt lgkmcnt(4)\n" \
"v_mfma_f32_16x16x32_bf16 v[64:67], a[124:127], v[40:43], v[64:67]\n" \
"s_cbranch_vccnz .Lp1g1_m1_%=\n" \
".Lp1g1_m1k_%=:\n" \
"v_mfma_f32_16x16x32_bf16 v[68:71], a[124:127], v[44:47], v[68:71]\n" \
"ds_read_b128 v[32:35], v10 offset:2048\n" \
"v_mfma_f32_16x16x32_bf16 v[72:75], a[252:255], v[40:43], v[72:75]\n" \
"ds_read_b128 v[36:39], v10 offset:18432\n" \
"v_mfma_f32_16x16x32_bf16 v[76:79], a[252:255], v[44:47], v[76:79]\n" \
"s_waitcnt lgkmcnt(4)\n" \
"s_xor_b32 s57, s66, s64\n" \
"s_add_u32 s58, s57, s50\n" \
"s_add_u32 s59, s59, 1\n" \
"v_mov_b32_e64 v5, v7\n" \
"v_mov_b32_e64 v6, v11\n" \
"s_cmp_ge_u32 s59, s47\n" \
"s_nop 0\n" \
"s_cbranch_scc0 .Lp1g1_loop_%=\n" \
".Lp1g1_out1_%=:\n" \
"s_nop 7\n" \
"s_nop 7\n" \
"v_mul_f32 v64, 0xbfb8aa3b, v64\n" \
"v_mul_f32 v65, 0xbfb8aa3b, v65\n" \
"v_mul_f32 v66, 0xbfb8aa3b, v66\n" \
"v_mul_f32 v67, 0xbfb8aa3b, v67\n" \
"v_exp_f32 v64, v64\n" \
"v_exp_f32 v65, v65\n" \
"v_exp_f32 v66, v66\n" \
"v_exp_f32 v67, v67\n" \
"v_add_f32 v64, 1.0, v64\n" \
"v_add_f32 v65, 1.0, v65\n" \
"v_add_f32 v66, 1.0, v66\n" \
"v_add_f32 v67, 1.0, v67\n" \
"v_rcp_f32 v64, v64\n" \
"v_rcp_f32 v65, v65\n" \
"v_rcp_f32 v66, v66\n" \
"v_rcp_f32 v67, v67\n" \
"v_mul_f32 v64, v64, v72\n" \
"v_mul_f32 v65, v65, v73\n" \
"v_mul_f32 v66, v66, v74\n" \
"v_mul_f32 v67, v67, v75\n" \
"v_cvt_pk_bf16_f32 v80, v64, v65\n" \
"v_cvt_pk_bf16_f32 v81, v66, v67\n" \
"v_mul_f32 v68, 0xbfb8aa3b, v68\n" \
"v_mul_f32 v69, 0xbfb8aa3b, v69\n" \
"v_mul_f32 v70, 0xbfb8aa3b, v70\n" \
"v_mul_f32 v71, 0xbfb8aa3b, v71\n" \
"v_exp_f32 v68, v68\n" \
"v_exp_f32 v69, v69\n" \
"v_exp_f32 v70, v70\n" \
"v_exp_f32 v71, v71\n" \
"v_add_f32 v68, 1.0, v68\n" \
"v_add_f32 v69, 1.0, v69\n" \
"v_add_f32 v70, 1.0, v70\n" \
"v_add_f32 v71, 1.0, v71\n" \
"v_rcp_f32 v68, v68\n" \
"v_rcp_f32 v69, v69\n" \
"v_rcp_f32 v70, v70\n" \
"v_rcp_f32 v71, v71\n" \
"v_mul_f32 v68, v68, v76\n" \
"v_mul_f32 v69, v69, v77\n" \
"v_mul_f32 v70, v70, v78\n" \
"v_mul_f32 v71, v71, v79\n" \
"v_cvt_pk_bf16_f32 v82, v68, v69\n" \
"v_cvt_pk_bf16_f32 v83, v70, v71\n" \
"s_nop 0\n" \
"s_nop 0\n" \
"v_permlane16_swap_b32 v80, v82\n" \
"v_permlane16_swap_b32 v81, v83\n" \
"buffer_store_dwordx4 v[80:83], v5, s[36:39], s57 offen sc1\n" \
"s_branch .Lp1g1_end_%=\n" \
".Lp1g1_out0_%=:\n" \
"s_nop 7\n" \
"s_nop 7\n" \
"v_mul_f32 v48, 0xbfb8aa3b, v48\n" \
"v_mul_f32 v49, 0xbfb8aa3b, v49\n" \
"v_mul_f32 v50, 0xbfb8aa3b, v50\n" \
"v_mul_f32 v51, 0xbfb8aa3b, v51\n" \
"v_exp_f32 v48, v48\n" \
"v_exp_f32 v49, v49\n" \
"v_exp_f32 v50, v50\n" \
"v_exp_f32 v51, v51\n" \
"v_add_f32 v48, 1.0, v48\n" \
"v_add_f32 v49, 1.0, v49\n" \
"v_add_f32 v50, 1.0, v50\n" \
"v_add_f32 v51, 1.0, v51\n" \
"v_rcp_f32 v48, v48\n" \
"v_rcp_f32 v49, v49\n" \
"v_rcp_f32 v50, v50\n" \
"v_rcp_f32 v51, v51\n" \
"v_mul_f32 v48, v48, v56\n" \
"v_mul_f32 v49, v49, v57\n" \
"v_mul_f32 v50, v50, v58\n" \
"v_mul_f32 v51, v51, v59\n" \
"v_cvt_pk_bf16_f32 v80, v48, v49\n" \
"v_cvt_pk_bf16_f32 v81, v50, v51\n" \
"v_mul_f32 v52, 0xbfb8aa3b, v52\n" \
"v_mul_f32 v53, 0xbfb8aa3b, v53\n" \
"v_mul_f32 v54, 0xbfb8aa3b, v54\n" \
"v_mul_f32 v55, 0xbfb8aa3b, v55\n" \
"v_exp_f32 v52, v52\n" \
"v_exp_f32 v53, v53\n" \
"v_exp_f32 v54, v54\n" \
"v_exp_f32 v55, v55\n" \
"v_add_f32 v52, 1.0, v52\n" \
"v_add_f32 v53, 1.0, v53\n" \
"v_add_f32 v54, 1.0, v54\n" \
"v_add_f32 v55, 1.0, v55\n" \
"v_rcp_f32 v52, v52\n" \
"v_rcp_f32 v53, v53\n" \
"v_rcp_f32 v54, v54\n" \
"v_rcp_f32 v55, v55\n" \
"v_mul_f32 v52, v52, v60\n" \
"v_mul_f32 v53, v53, v61\n" \
"v_mul_f32 v54, v54, v62\n" \
"v_mul_f32 v55, v55, v63\n" \
"v_cvt_pk_bf16_f32 v82, v52, v53\n" \
"v_cvt_pk_bf16_f32 v83, v54, v55\n" \
"s_nop 0\n" \
"s_nop 0\n" \
"v_permlane16_swap_b32 v80, v82\n" \
"v_permlane16_swap_b32 v81, v83\n" \
"buffer_store_dwordx4 v[80:83], v5, s[36:39], s57 offen sc1\n" \
".Lp1g1_end_%=:\n" \
"s_waitcnt vmcnt(0) lgkmcnt(0)\n" \
"s_barrier\n" \
"v_mov_b32 v13, s59\n" \
"buffer_store_dword v13, v6, s[36:39], 0 offen sc1\n" \
"s_waitcnt vmcnt(0) lgkmcnt(0)\n" \
"s_branch .Lp1g1_x_%=\n" \
".Lp1g1_l0_%=:\n" \
"s_nop 7\n" \
"s_nop 7\n" \
"s_waitcnt vmcnt(0) lgkmcnt(0)\n" \
"s_mov_b32 s62, 0\n" \
".Lp1g1_l0a_%=:\n" \
"buffer_load_dword v15, v14, s[36:39], s63 offen sc1\n" \
"s_waitcnt vmcnt(0)\n" \
"v_cmp_gt_u32 vcc, 16, v15\n" \
"s_nop 4\n" \
"s_cbranch_vccz .Lp1g1_l0k_%=\n" \
"s_sleep 4\n" \
"s_add_u32 s62, s62, 1\n" \
"s_cmp_lt_u32 s62, 0x10000\n" \
"s_cbranch_scc1 .Lp1g1_l0a_%=\n" \
"v_mov_b32 v15, 256\n" \
"buffer_store_dword v15, v15, s[36:39], 0 offen sc1\n" \
"s_waitcnt vmcnt(0) lgkmcnt(0)\n" \
"s_branch .Lp1g1_l0k_%=\n" \
".Lp1g1_l1_%=:\n" \
"s_nop 7\n" \
"s_nop 7\n" \
"s_waitcnt vmcnt(0) lgkmcnt(0)\n" \
"s_mov_b32 s62, 0\n" \
".Lp1g1_l1a_%=:\n" \
"buffer_load_dword v15, v14, s[36:39], s63 offen sc1\n" \
"s_waitcnt vmcnt(0)\n" \
"v_cmp_gt_u32 vcc, 16, v15\n" \
"s_nop 4\n" \
"s_cbranch_vccz .Lp1g1_l1k_%=\n" \
"s_sleep 4\n" \
"s_add_u32 s62, s62, 1\n" \
"s_cmp_lt_u32 s62, 0x10000\n" \
"s_cbranch_scc1 .Lp1g1_l1a_%=\n" \
"v_mov_b32 v15, 256\n" \
"buffer_store_dword v15, v15, s[36:39], 0 offen sc1\n" \
"s_waitcnt vmcnt(0) lgkmcnt(0)\n" \
"s_branch .Lp1g1_l1k_%=\n" \
".Lp1g1_m0_%=:\n" \
"s_nop 7\n" \
"s_nop 7\n" \
"s_waitcnt vmcnt(0) lgkmcnt(0)\n" \
"s_mov_b32 s62, 0\n" \
".Lp1g1_m0a_%=:\n" \
"buffer_load_dword v15, v14, s[36:39], s63 offen sc1\n" \
"s_waitcnt vmcnt(0)\n" \
"v_cmp_gt_u32 vcc, 16, v15\n" \
"s_nop 4\n" \
"s_cbranch_vccz .Lp1g1_m0k_%=\n" \
"s_sleep 4\n" \
"s_add_u32 s62, s62, 1\n" \
"s_cmp_lt_u32 s62, 0x10000\n" \
"s_cbranch_scc1 .Lp1g1_m0a_%=\n" \
"v_mov_b32 v15, 256\n" \
"buffer_store_dword v15, v15, s[36:39], 0 offen sc1\n" \
"s_waitcnt vmcnt(0) lgkmcnt(0)\n" \
"s_branch .Lp1g1_m0k_%=\n" \
".Lp1g1_m1_%=:\n" \
"s_nop 7\n" \
"s_nop 7\n" \
"s_waitcnt vmcnt(0) lgkmcnt(0)\n" \
"s_mov_b32 s62, 0\n" \
".Lp1g1_m1a_%=:\n" \
"buffer_load_dword v15, v14, s[36:39], s63 offen sc1\n" \
"s_waitcnt vmcnt(0)\n" \
"v_cmp_gt_u32 vcc, 16, v15\n" \
"s_nop 4\n" \
"s_cbranch_vccz .Lp1g1_m1k_%=\n" \
"s_sleep 4\n" \
"s_add_u32 s62, s62, 1\n" \
"s_cmp_lt_u32 s62, 0x10000\n" \
"s_cbranch_scc1 .Lp1g1_m1a_%=\n" \
"v_mov_b32 v15, 256\n" \
"buffer_store_dword v15, v15, s[36:39], 0 offen sc1\n" \
"s_waitcnt vmcnt(0) lgkmcnt(0)\n" \
"s_branch .Lp1g1_m1k_%=\n" \
".Lp1g1_x_%=:\n"
#define CLOB_P1 \
"a0", "a1", "a2", "a3", "a4", "a5", "a6", "a7", "a8", "a9", "a10", "a11", "a12", "a13", "a14", "a15", "a16", "a17", "a18", "a19", "a20", "a21", "a22", "a23", "a24", "a25", "a26", "a27", "a28", "a29", "a30", "a31", "a32", "a33", "a34", "a35", "a36", "a37", "a38", "a39", "a40", "a41", "a42", "a43", "a44", "a45", "a46", "a47", "a48", "a49", "a50", "a51", "a52", "a53", "a54", "a55", "a56", "a57", "a58", "a59", "a60", "a61", "a62", "a63", "a64", "a65", "a66", "a67", "a68", "a69", "a70", "a71", "a72", "a73", "a74", "a75", "a76", "a77", "a78", "a79", "a80", "a81", "a82", "a83", "a84", "a85", "a86", "a87", "a88", "a89", "a90", "a91", "a92", "a93", "a94", "a95", "a96", "a97", "a98", "a99", "a100", "a101", "a102", "a103", "a104", "a105", "a106", "a107", "a108", "a109", "a110", "a111", "a112", "a113", "a114", "a115", "a116", "a117", "a118", "a119", "a120", "a121", "a122", "a123", "a124", "a125", "a126", "a127", "a128", "a129", "a130", "a131", "a132", "a133", "a134", "a135", "a136", "a137", "a138", "a139", "a140", "a141", "a142", "a143", "a144", "a145", "a146", "a147", "a148", "a149", "a150", "a151", "a152", "a153", "a154", "a155", "a156", "a157", "a158", "a159", "a160", "a161", "a162", "a163", "a164", "a165", "a166", "a167", "a168", "a169", "a170", "a171", "a172", "a173", "a174", "a175", "a176", "a177", "a178", "a179", "a180", "a181", "a182", "a183", "a184", "a185", "a186", "a187", "a188", "a189", "a190", "a191", "a192", "a193", "a194", "a195", "a196", "a197", "a198", "a199", "a200", "a201", "a202", "a203", "a204", "a205", "a206", "a207", "a208", "a209", "a210", "a211", "a212", "a213", "a214", "a215", "a216", "a217", "a218", "a219", "a220", "a221", "a222", "a223", "a224", "a225", "a226", "a227", "a228", "a229", "a230", "a231", "a232", "a233", "a234", "a235", "a236", "a237", "a238", "a239", "a240", "a241", "a242", "a243", "a244", "a245", "a246", "a247", "a248", "a249", "a250", "a251", "a252", "a253", "a254", "a255", "s52", "s53", "s54", "s55", "s56", "s57", "s58", "s59", "s60", "s61", "s62", "s63", "s65", "s66", "v7", "v8", "v9", "v10", "v11", "v12", "v13", "v14", "v15", "v16", "v17", "v18", "v19", "v20", "v21", "v22", "v23", "v24", "v25", "v26", "v27", "v28", "v29", "v30", "v31", "v32", "v33", "v34", "v35", "v36", "v37", "v38", "v39", "v40", "v41", "v42", "v43", "v44", "v45", "v46", "v47", "v48", "v49", "v50", "v51", "v52", "v53", "v54", "v55", "v56", "v57", "v58", "v59", "v60", "v61", "v62", "v63", "v64", "v65", "v66", "v67", "v68", "v69", "v70", "v71", "v72", "v73", "v74", "v75", "v76", "v77", "v78", "v79", "v80", "v81", "v82", "v83", "v84", "v85", "v86", "v87", "vcc", "scc", "memory"
#define ASM_QK \
"s_nop 7\n" \
"s_nop 7\n" \
"v_add_u32 v36, 0x10000, v34\n" \
"v_add_u32 v38, 0x20000, v34\n" \
"v_add_u32 v37, 0x10000, v35\n" \
"v_add_u32 v39, 0x20000, v35\n" \
"s_lshl_b32 s62, s48, 12\n" \
"s_add_u32 m0, s62, 147456\n" \
"s_xor_b32 s54, s15, 524288\n" \
"buffer_load_dwordx4 v32, s[40:43], s54 offen nt lds\n" \
"s_add_u32 m0, s62, 148480\n" \
"s_xor_b32 s54, s15, 524288\n" \
"buffer_load_dwordx4 v33, s[40:43], s54 offen nt lds\n" \
"s_add_u32 m0, s62, 149504\n" \
"s_xor_b32 s54, s15, 655360\n" \
"buffer_load_dwordx4 v32, s[40:43], s54 offen nt lds\n" \
"s_add_u32 m0, s62, 150528\n" \
"s_xor_b32 s54, s15, 655360\n" \
"buffer_load_dwordx4 v33, s[40:43], s54 offen nt lds\n" \
"s_add_u32 m0, s62, 131072\n" \
"s_xor_b32 s54, s15, 786432\n" \
"buffer_load_dwordx4 v32, s[40:43], s54 offen nt lds\n" \
"s_add_u32 m0, s62, 132096\n" \
"s_xor_b32 s54, s15, 786432\n" \
"buffer_load_dwordx4 v33, s[40:43], s54 offen nt lds\n" \
"s_add_u32 m0, s62, 133120\n" \
"s_xor_b32 s54, s15, 917504\n" \
"buffer_load_dwordx4 v32, s[40:43], s54 offen nt lds\n" \
"s_add_u32 m0, s62, 134144\n" \
"s_xor_b32 s54, s15, 917504\n" \
"buffer_load_dwordx4 v33, s[40:43], s54 offen nt lds\n" \
"s_add_u32 m0, s62, 114688\n" \
"s_xor_b32 s54, s15, 1572864\n" \
"buffer_load_dwordx4 v32, s[40:43], s54 offen nt lds\n" \
"s_add_u32 m0, s62, 115712\n" \
"s_xor_b32 s54, s15, 1572864\n" \
"buffer_load_dwordx4 v33, s[40:43], s54 offen nt lds\n" \
"s_add_u32 m0, s62, 116736\n" \
"s_xor_b32 s54, s15, 1703936\n" \
"buffer_load_dwordx4 v32, s[40:43], s54 offen nt lds\n" \
"s_add_u32 m0, s62, 117760\n" \
"s_xor_b32 s54, s15, 1703936\n" \
"buffer_load_dwordx4 v33, s[40:43], s54 offen nt lds\n" \
"s_add_u32 m0, s62, 98304\n" \
"s_xor_b32 s54, s15, 1835008\n" \
"buffer_load_dwordx4 v32, s[40:43], s54 offen nt lds\n" \
"s_add_u32 m0, s62, 99328\n" \
"s_xor_b32 s54, s15, 1835008\n" \
"buffer_load_dwordx4 v33, s[40:43], s54 offen nt lds\n" \
"s_add_u32 m0, s62, 100352\n" \
"s_xor_b32 s54, s15, 1966080\n" \
"buffer_load_dwordx4 v32, s[40:43], s54 offen nt lds\n" \
"s_add_u32 m0, s62, 101376\n" \
"s_xor_b32 s54, s15, 1966080\n" \
"buffer_load_dwordx4 v33, s[40:43], s54 offen nt lds\n" \
"s_add_u32 m0, s62, 81920\n" \
"s_xor_b32 s54, s15, 2621440\n" \
"buffer_load_dwordx4 v32, s[40:43], s54 offen nt lds\n" \
"s_add_u32 m0, s62, 82944\n" \
"s_xor_b32 s54, s15, 2621440\n" \
"buffer_load_dwordx4 v33, s[40:43], s54 offen nt lds\n" \
"s_add_u32 m0, s62, 83968\n" \
"s_xor_b32 s54, s15, 2752512\n" \
"buffer_load_dwordx4 v32, s[40:43], s54 offen nt lds\n" \
"s_add_u32 m0, s62, 84992\n" \
"s_xor_b32 s54, s15, 2752512\n" \
"buffer_load_dwordx4 v33, s[40:43], s54 offen nt lds\n" \
"s_add_u32 m0, s62, 65536\n" \
"s_xor_b32 s54, s15, 2883584\n" \
"buffer_load_dwordx4 v32, s[40:43], s54 offen nt lds\n" \
"s_add_u32 m0, s62, 66560\n" \
"s_xor_b32 s54, s15, 2883584\n" \
"buffer_load_dwordx4 v33, s[40:43], s54 offen nt lds\n" \
"s_add_u32 m0, s62, 67584\n" \
"s_xor_b32 s54, s15, 3014656\n" \
"buffer_load_dwordx4 v32, s[40:43], s54 offen nt lds\n" \
"s_add_u32 m0, s62, 68608\n" \
"s_xor_b32 s54, s15, 3014656\n" \
"buffer_load_dwordx4 v33, s[40:43], s54 offen nt lds\n" \
"s_add_u32 m0, s62, 49152\n" \
"s_xor_b32 s54, s15, 3670016\n" \
"buffer_load_dwordx4 v32, s[40:43], s54 offen nt lds\n" \
"s_add_u32 m0, s62, 50176\n" \
"s_xor_b32 s54, s15, 3670016\n" \
"buffer_load_dwordx4 v33, s[40:43], s54 offen nt lds\n" \
"s_add_u32 m0, s62, 51200\n" \
"s_xor_b32 s54, s15, 3801088\n" \
"buffer_load_dwordx4 v32, s[40:43], s54 offen nt lds\n" \
"s_add_u32 m0, s62, 52224\n" \
"s_xor_b32 s54, s15, 3801088\n" \
"buffer_load_dwordx4 v33, s[40:43], s54 offen nt lds\n" \
"s_add_u32 m0, s62, 32768\n" \
"s_xor_b32 s54, s15, 3932160\n" \
"buffer_load_dwordx4 v32, s[40:43], s54 offen nt lds\n" \
"s_add_u32 m0, s62, 33792\n" \
"s_xor_b32 s54, s15, 3932160\n" \
"buffer_load_dwordx4 v33, s[40:43], s54 offen nt lds\n" \
"s_add_u32 m0, s62, 34816\n" \
"s_xor_b32 s54, s15, 4063232\n" \
"buffer_load_dwordx4 v32, s[40:43], s54 offen nt lds\n" \
"s_add_u32 m0, s62, 35840\n" \
"s_xor_b32 s54, s15, 4063232\n" \
"buffer_load_dwordx4 v33, s[40:43], s54 offen nt lds\n" \
"s_add_u32 m0, s62, 16384\n" \
"s_xor_b32 s54, s15, 4718592\n" \
"buffer_load_dwordx4 v32, s[40:43], s54 offen nt lds\n" \
"s_add_u32 m0, s62, 17408\n" \
"s_xor_b32 s54, s15, 4718592\n" \
"buffer_load_dwordx4 v33, s[40:43], s54 offen nt lds\n" \
"s_add_u32 m0, s62, 18432\n" \
"s_xor_b32 s54, s15, 4849664\n" \
"buffer_load_dwordx4 v32, s[40:43], s54 offen nt lds\n" \
"s_add_u32 m0, s62, 19456\n" \
"s_xor_b32 s54, s15, 4849664\n" \
"buffer_load_dwordx4 v33, s[40:43], s54 offen nt lds\n" \
"s_add_u32 m0, s62, 0\n" \
"s_xor_b32 s54, s15, 4980736\n" \
"buffer_load_dwordx4 v32, s[40:43], s54 offen nt lds\n" \
"s_add_u32 m0, s62, 1024\n" \
"s_xor_b32 s54, s15, 4980736\n" \
"buffer_load_dwordx4 v33, s[40:43], s54 offen nt lds\n" \
"s_add_u32 m0, s62, 2048\n" \
"s_xor_b32 s54, s15, 5111808\n" \
"buffer_load_dwordx4 v32, s[40:43], s54 offen nt lds\n" \
"s_add_u32 m0, s62, 3072\n" \
"s_xor_b32 s54, s15, 5111808\n" \
"buffer_load_dwordx4 v33, s[40:43], s54 offen nt lds\n" \
"s_waitcnt vmcnt(32)\n" \
"s_barrier\n" \
"ds_read_b64_tr_b16 a[16:17], v38 offset:16384\n" \
"ds_read_b64_tr_b16 a[18:19], v38 offset:16896\n" \
"ds_read_b64_tr_b16 a[24:25], v39 offset:16384\n" \
"ds_read_b64_tr_b16 a[26:27], v39 offset:16896\n" \
"ds_read_b64_tr_b16 v[112:113], v38 offset:24576\n" \
"ds_read_b64_tr_b16 v[114:115], v38 offset:25088\n" \
"ds_read_b64_tr_b16 v[120:121], v39 offset:24576\n" \
"ds_read_b64_tr_b16 v[122:123], v39 offset:25088\n" \
"ds_read_b64_tr_b16 a[20:21], v38 offset:0\n" \
"ds_read_b64_tr_b16 a[22:23], v38 offset:512\n" \
"ds_read_b64_tr_b16 a[28:29], v39 offset:0\n" \
"ds_read_b64_tr_b16 a[30:31], v39 offset:512\n" \
"ds_read_b64_tr_b16 v[116:117], v38 offset:8192\n" \
"ds_read_b64_tr_b16 v[118:119], v38 offset:8704\n" \
"ds_read_b64_tr_b16 v[124:125], v39 offset:8192\n" \
"ds_read_b64_tr_b16 v[126:127], v39 offset:8704\n" \
"s_waitcnt lgkmcnt(0)\n" \
"s_barrier\n" \
"s_add_u32 m0, s62, 147456\n" \
"s_xor_b32 s54, s15, 5767168\n" \
"buffer_load_dwordx4 v32, s[40:43], s54 offen nt lds\n" \
"s_add_u32 m0, s62, 148480\n" \
"s_xor_b32 s54, s15, 5767168\n" \
"buffer_load_dwordx4 v33, s[40:43], s54 offen nt lds\n" \
"s_add_u32 m0, s62, 149504\n" \
"s_xor_b32 s54, s15, 5898240\n" \
"buffer_load_dwordx4 v32, s[40:43], s54 offen nt lds\n" \
"s_add_u32 m0, s62, 150528\n" \
"s_xor_b32 s54, s15, 5898240\n" \
"buffer_load_dwordx4 v33, s[40:43], s54 offen nt lds\n" \
"s_add_u32 m0, s62, 131072\n" \
"s_xor_b32 s54, s15, 6029312\n" \
"buffer_load_dwordx4 v32, s[40:43], s54 offen nt lds\n" \
"s_add_u32 m0, s62, 132096\n" \
"s_xor_b32 s54, s15, 6029312\n" \
"buffer_load_dwordx4 v33, s[40:43], s54 offen nt lds\n" \
"s_add_u32 m0, s62, 133120\n" \
"s_xor_b32 s54, s15, 6160384\n" \
"buffer_load_dwordx4 v32, s[40:43], s54 offen nt lds\n" \
"s_add_u32 m0, s62, 134144\n" \
"s_xor_b32 s54, s15, 6160384\n" \
"buffer_load_dwordx4 v33, s[40:43], s54 offen nt lds\n" \
"s_waitcnt vmcnt(32)\n" \
"s_barrier\n" \
"ds_read_b64_tr_b16 a[48:49], v36 offset:49152\n" \
"ds_read_b64_tr_b16 a[50:51], v36 offset:49664\n" \
"ds_read_b64_tr_b16 a[56:57], v37 offset:49152\n" \
"ds_read_b64_tr_b16 a[58:59], v37 offset:49664\n" \
"ds_read_b64_tr_b16 v[128:129], v36 offset:57344\n" \
"ds_read_b64_tr_b16 v[130:131], v36 offset:57856\n" \
"ds_read_b64_tr_b16 v[136:137], v37 offset:57344\n" \
"ds_read_b64_tr_b16 v[138:139], v37 offset:57856\n" \
"ds_read_b64_tr_b16 a[52:53], v36 offset:32768\n" \
"ds_read_b64_tr_b16 a[54:55], v36 offset:33280\n" \
"ds_read_b64_tr_b16 a[60:61], v37 offset:32768\n" \
"ds_read_b64_tr_b16 a[62:63], v37 offset:33280\n" \
"ds_read_b64_tr_b16 v[132:133], v36 offset:40960\n" \
"ds_read_b64_tr_b16 v[134:135], v36 offset:41472\n" \
"ds_read_b64_tr_b16 v[140:141], v37 offset:40960\n" \
"ds_read_b64_tr_b16 v[142:143], v37 offset:41472\n" \
"s_waitcnt lgkmcnt(0)\n" \
"s_barrier\n" \
"s_add_u32 m0, s62, 114688\n" \
"s_xor_b32 s54, s15, 6815744\n" \
"buffer_load_dwordx4 v32, s[40:43], s54 offen nt lds\n" \
"s_add_u32 m0, s62, 115712\n" \
"s_xor_b32 s54, s15, 6815744\n" \
"buffer_load_dwordx4 v33, s[40:43], s54 offen nt lds\n" \
"s_add_u32 m0, s62, 116736\n" \
"s_xor_b32 s54, s15, 6946816\n" \
"buffer_load_dwordx4 v32, s[40:43], s54 offen nt lds\n" \
"s_add_u32 m0, s62, 117760\n" \
"s_xor_b32 s54, s15, 6946816\n" \
"buffer_load_dwordx4 v33, s[40:43], s54 offen nt lds\n" \
"s_add_u32 m0, s62, 98304\n" \
"s_xor_b32 s54, s15, 7077888\n" \
"buffer_load_dwordx4 v32, s[40:43], s54 offen nt lds\n" \
"s_add_u32 m0, s62, 99328\n" \
"s_xor_b32 s54, s15, 7077888\n" \
"buffer_load_dwordx4 v33, s[40:43], s54 offen nt lds\n" \
"s_add_u32 m0, s62, 100352\n" \
"s_xor_b32 s54, s15, 7208960\n" \
"buffer_load_dwordx4 v32, s[40:43], s54 offen nt lds\n" \
"s_add_u32 m0, s62, 101376\n" \
"s_xor_b32 s54, s15, 7208960\n" \
"buffer_load_dwordx4 v33, s[40:43], s54 offen nt lds\n" \
"s_waitcnt vmcnt(32)\n" \
"s_barrier\n" \
"ds_read_b64_tr_b16 a[80:81], v36 offset:16384\n" \
"ds_read_b64_tr_b16 a[82:83], v36 offset:16896\n" \
"ds_read_b64_tr_b16 a[88:89], v37 offset:16384\n" \
"ds_read_b64_tr_b16 a[90:91], v37 offset:16896\n" \
"ds_read_b64_tr_b16 v[144:145], v36 offset:24576\n" \
"ds_read_b64_tr_b16 v[146:147], v36 offset:25088\n" \
"ds_read_b64_tr_b16 v[152:153], v37 offset:24576\n" \
"ds_read_b64_tr_b16 v[154:155], v37 offset:25088\n" \
"ds_read_b64_tr_b16 a[84:85], v36 offset:0\n" \
"ds_read_b64_tr_b16 a[86:87], v36 offset:512\n" \
"ds_read_b64_tr_b16 a[92:93], v37 offset:0\n" \
"ds_read_b64_tr_b16 a[94:95], v37 offset:512\n" \
"ds_read_b64_tr_b16 v[148:149], v36 offset:8192\n" \
"ds_read_b64_tr_b16 v[150:151], v36 offset:8704\n" \
"ds_read_b64_tr_b16 v[156:157], v37 offset:8192\n" \
"ds_read_b64_tr_b16 v[158:159], v37 offset:8704\n" \
"s_waitcnt lgkmcnt(0)\n" \
"s_barrier\n" \
"s_add_u32 m0, s62, 81920\n" \
"s_xor_b32 s54, s15, 7340032\n" \
"buffer_load_dwordx4 v32, s[40:43], s54 offen nt lds\n" \
"s_add_u32 m0, s62, 82944\n" \
"s_xor_b32 s54, s15, 7340032\n" \
"buffer_load_dwordx4 v33, s[40:43], s54 offen nt lds\n" \
"s_add_u32 m0, s62, 83968\n" \
"s_xor_b32 s54, s15, 7471104\n" \
"buffer_load_dwordx4 v32, s[40:43], s54 offen nt lds\n" \
"s_add_u32 m0, s62, 84992\n" \
"s_xor_b32 s54, s15, 7471104\n" \
"buffer_load_dwordx4 v33, s[40:43], s54 offen nt lds\n" \
"s_add_u32 m0, s62, 65536\n" \
"s_xor_b32 s54, s15, 7602176\n" \
"buffer_load_dwordx4 v32, s[40:43], s54 offen nt lds\n" \
"s_add_u32 m0, s62, 66560\n" \
"s_xor_b32 s54, s15, 7602176\n" \
"buffer_load_dwordx4 v33, s[40:43], s54 offen nt lds\n" \
"s_add_u32 m0, s62, 67584\n" \
"s_xor_b32 s54, s15, 7733248\n" \
"buffer_load_dwordx4 v32, s[40:43], s54 offen nt lds\n" \
"s_add_u32 m0, s62, 68608\n" \
"s_xor_b32 s54, s15, 7733248\n" \
"buffer_load_dwordx4 v33, s[40:43], s54 offen nt lds\n" \
"s_waitcnt vmcnt(32)\n" \
"s_barrier\n" \
"ds_read_b64_tr_b16 a[112:113], v34 offset:49152\n" \
"ds_read_b64_tr_b16 a[114:115], v34 offset:49664\n" \
"ds_read_b64_tr_b16 a[120:121], v35 offset:49152\n" \
"ds_read_b64_tr_b16 a[122:123], v35 offset:49664\n" \
"ds_read_b64_tr_b16 v[160:161], v34 offset:57344\n" \
"ds_read_b64_tr_b16 v[162:163], v34 offset:57856\n" \
"ds_read_b64_tr_b16 v[168:169], v35 offset:57344\n" \
"ds_read_b64_tr_b16 v[170:171], v35 offset:57856\n" \
"ds_read_b64_tr_b16 a[116:117], v34 offset:32768\n" \
"ds_read_b64_tr_b16 a[118:119], v34 offset:33280\n" \
"ds_read_b64_tr_b16 a[124:125], v35 offset:32768\n" \
"ds_read_b64_tr_b16 a[126:127], v35 offset:33280\n" \
"ds_read_b64_tr_b16 v[164:165], v34 offset:40960\n" \
"ds_read_b64_tr_b16 v[166:167], v34 offset:41472\n" \
"ds_read_b64_tr_b16 v[172:173], v35 offset:40960\n" \
"ds_read_b64_tr_b16 v[174:175], v35 offset:41472\n" \
"s_waitcnt lgkmcnt(0)\n" \
"s_barrier\n" \
"s_add_u32 m0, s62, 49152\n" \
"s_xor_b32 s54, s15, 7864320\n" \
"buffer_load_dwordx4 v32, s[40:43], s54 offen nt lds\n" \
"s_add_u32 m0, s62, 50176\n" \
"s_xor_b32 s54, s15, 7864320\n" \
"buffer_load_dwordx4 v33, s[40:43], s54 offen nt lds\n" \
"s_add_u32 m0, s62, 51200\n" \
"s_xor_b32 s54, s15, 7995392\n" \
"buffer_load_dwordx4 v32, s[40:43], s54 offen nt lds\n" \
"s_add_u32 m0, s62, 52224\n" \
"s_xor_b32 s54, s15, 7995392\n" \
"buffer_load_dwordx4 v33, s[40:43], s54 offen nt lds\n" \
"s_add_u32 m0, s62, 32768\n" \
"s_xor_b32 s54, s15, 8126464\n" \
"buffer_load_dwordx4 v32, s[40:43], s54 offen nt lds\n" \
"s_add_u32 m0, s62, 33792\n" \
"s_xor_b32 s54, s15, 8126464\n" \
"buffer_load_dwordx4 v33, s[40:43], s54 offen nt lds\n" \
"s_add_u32 m0, s62, 34816\n" \
"s_xor_b32 s54, s15, 8257536\n" \
"buffer_load_dwordx4 v32, s[40:43], s54 offen nt lds\n" \
"s_add_u32 m0, s62, 35840\n" \
"s_xor_b32 s54, s15, 8257536\n" \
"buffer_load_dwordx4 v33, s[40:43], s54 offen nt lds\n" \
"s_waitcnt vmcnt(32)\n" \
"s_barrier\n" \
"ds_read_b64_tr_b16 a[144:145], v34 offset:16384\n" \
"ds_read_b64_tr_b16 a[146:147], v34 offset:16896\n" \
"ds_read_b64_tr_b16 a[152:153], v35 offset:16384\n" \
"ds_read_b64_tr_b16 a[154:155], v35 offset:16896\n" \
"ds_read_b64_tr_b16 v[176:177], v34 offset:24576\n" \
"ds_read_b64_tr_b16 v[178:179], v34 offset:25088\n" \
"ds_read_b64_tr_b16 v[184:185], v35 offset:24576\n" \
"ds_read_b64_tr_b16 v[186:187], v35 offset:25088\n" \
"ds_read_b64_tr_b16 a[148:149], v34 offset:0\n" \
"ds_read_b64_tr_b16 a[150:151], v34 offset:512\n" \
"ds_read_b64_tr_b16 a[156:157], v35 offset:0\n" \
"ds_read_b64_tr_b16 a[158:159], v35 offset:512\n" \
"ds_read_b64_tr_b16 v[180:181], v34 offset:8192\n" \
"ds_read_b64_tr_b16 v[182:183], v34 offset:8704\n" \
"ds_read_b64_tr_b16 v[188:189], v35 offset:8192\n" \
"ds_read_b64_tr_b16 v[190:191], v35 offset:8704\n" \
"s_waitcnt lgkmcnt(0)\n" \
"s_barrier\n" \
"s_add_u32 m0, s62, 16384\n" \
"s_xor_b32 s54, s15, 0\n" \
"buffer_load_dwordx4 v32, s[40:43], s54 offen nt lds\n" \
"s_add_u32 m0, s62, 17408\n" \
"s_xor_b32 s54, s15, 0\n" \
"buffer_load_dwordx4 v33, s[40:43], s54 offen nt lds\n" \
"s_add_u32 m0, s62, 18432\n" \
"s_xor_b32 s54, s15, 131072\n" \
"buffer_load_dwordx4 v32, s[40:43], s54 offen nt lds\n" \
"s_add_u32 m0, s62, 19456\n" \
"s_xor_b32 s54, s15, 131072\n" \
"buffer_load_dwordx4 v33, s[40:43], s54 offen nt lds\n" \
"s_add_u32 m0, s62, 0\n" \
"s_xor_b32 s54, s15, 262144\n" \
"buffer_load_dwordx4 v32, s[40:43], s54 offen nt lds\n" \
"s_add_u32 m0, s62, 1024\n" \
"s_xor_b32 s54, s15, 262144\n" \
"buffer_load_dwordx4 v33, s[40:43], s54 offen nt lds\n" \
"s_add_u32 m0, s62, 2048\n" \
"s_xor_b32 s54, s15, 393216\n" \
"buffer_load_dwordx4 v32, s[40:43], s54 offen nt lds\n" \
"s_add_u32 m0, s62, 3072\n" \
"s_xor_b32 s54, s15, 393216\n" \
"buffer_load_dwordx4 v33, s[40:43], s54 offen nt lds\n" \
"s_waitcnt vmcnt(32)\n" \
"s_barrier\n" \
"ds_read_b64_tr_b16 a[176:177], v38 offset:16384\n" \
"ds_read_b64_tr_b16 a[178:179], v38 offset:16896\n" \
"ds_read_b64_tr_b16 a[184:185], v39 offset:16384\n" \
"ds_read_b64_tr_b16 a[186:187], v39 offset:16896\n" \
"ds_read_b64_tr_b16 v[192:193], v38 offset:24576\n" \
"ds_read_b64_tr_b16 v[194:195], v38 offset:25088\n" \
"ds_read_b64_tr_b16 v[200:201], v39 offset:24576\n" \
"ds_read_b64_tr_b16 v[202:203], v39 offset:25088\n" \
"ds_read_b64_tr_b16 a[180:181], v38 offset:0\n" \
"ds_read_b64_tr_b16 a[182:183], v38 offset:512\n" \
"ds_read_b64_tr_b16 a[188:189], v39 offset:0\n" \
"ds_read_b64_tr_b16 a[190:191], v39 offset:512\n" \
"ds_read_b64_tr_b16 v[196:197], v38 offset:8192\n" \
"ds_read_b64_tr_b16 v[198:199], v38 offset:8704\n" \
"ds_read_b64_tr_b16 v[204:205], v39 offset:8192\n" \
"ds_read_b64_tr_b16 v[206:207], v39 offset:8704\n" \
"s_waitcnt lgkmcnt(0)\n" \
"s_barrier\n" \
"s_add_u32 m0, s62, 147456\n" \
"s_xor_b32 s54, s15, 1048576\n" \
"buffer_load_dwordx4 v32, s[40:43], s54 offen nt lds\n" \
"s_add_u32 m0, s62, 148480\n" \
"s_xor_b32 s54, s15, 1048576\n" \
"buffer_load_dwordx4 v33, s[40:43], s54 offen nt lds\n" \
"s_add_u32 m0, s62, 149504\n" \
"s_xor_b32 s54, s15, 1179648\n" \
"buffer_load_dwordx4 v32, s[40:43], s54 offen nt lds\n" \
"s_add_u32 m0, s62, 150528\n" \
"s_xor_b32 s54, s15, 1179648\n" \
"buffer_load_dwordx4 v33, s[40:43], s54 offen nt lds\n" \
"s_add_u32 m0, s62, 131072\n" \
"s_xor_b32 s54, s15, 1310720\n" \
"buffer_load_dwordx4 v32, s[40:43], s54 offen nt lds\n" \
"s_add_u32 m0, s62, 132096\n" \
"s_xor_b32 s54, s15, 1310720\n" \
"buffer_load_dwordx4 v33, s[40:43], s54 offen nt lds\n" \
"s_add_u32 m0, s62, 133120\n" \
"s_xor_b32 s54, s15, 1441792\n" \
"buffer_load_dwordx4 v32, s[40:43], s54 offen nt lds\n" \
"s_add_u32 m0, s62, 134144\n" \
"s_xor_b32 s54, s15, 1441792\n" \
"buffer_load_dwordx4 v33, s[40:43], s54 offen nt lds\n" \
"s_waitcnt vmcnt(32)\n" \
"s_barrier\n" \
"ds_read_b64_tr_b16 a[208:209], v36 offset:49152\n" \
"ds_read_b64_tr_b16 a[210:211], v36 offset:49664\n" \
"ds_read_b64_tr_b16 a[216:217], v37 offset:49152\n" \
"ds_read_b64_tr_b16 a[218:219], v37 offset:49664\n" \
"ds_read_b64_tr_b16 v[208:209], v36 offset:57344\n" \
"ds_read_b64_tr_b16 v[210:211], v36 offset:57856\n" \
"ds_read_b64_tr_b16 v[216:217], v37 offset:57344\n" \
"ds_read_b64_tr_b16 v[218:219], v37 offset:57856\n" \
"ds_read_b64_tr_b16 a[212:213], v36 offset:32768\n" \
"ds_read_b64_tr_b16 a[214:215], v36 offset:33280\n" \
"ds_read_b64_tr_b16 a[220:221], v37 offset:32768\n" \
"ds_read_b64_tr_b16 a[222:223], v37 offset:33280\n" \
"ds_read_b64_tr_b16 v[212:213], v36 offset:40960\n" \
"ds_read_b64_tr_b16 v[214:215], v36 offset:41472\n" \
"ds_read_b64_tr_b16 v[220:221], v37 offset:40960\n" \
"ds_read_b64_tr_b16 v[222:223], v37 offset:41472\n" \
"s_waitcnt lgkmcnt(0)\n" \
"s_barrier\n" \
"s_add_u32 m0, s62, 114688\n" \
"s_xor_b32 s54, s15, 2097152\n" \
"buffer_load_dwordx4 v32, s[40:43], s54 offen nt lds\n" \
"s_add_u32 m0, s62, 115712\n" \
"s_xor_b32 s54, s15, 2097152\n" \
"buffer_load_dwordx4 v33, s[40:43], s54 offen nt lds\n" \
"s_add_u32 m0, s62, 116736\n" \
"s_xor_b32 s54, s15, 2228224\n" \
"buffer_load_dwordx4 v32, s[40:43], s54 offen nt lds\n" \
"s_add_u32 m0, s62, 117760\n" \
"s_xor_b32 s54, s15, 2228224\n" \
"buffer_load_dwordx4 v33, s[40:43], s54 offen nt lds\n" \
"s_add_u32 m0, s62, 98304\n" \
"s_xor_b32 s54, s15, 2359296\n" \
"buffer_load_dwordx4 v32, s[40:43], s54 offen nt lds\n" \
"s_add_u32 m0, s62, 99328\n" \
"s_xor_b32 s54, s15, 2359296\n" \
"buffer_load_dwordx4 v33, s[40:43], s54 offen nt lds\n" \
"s_add_u32 m0, s62, 100352\n" \
"s_xor_b32 s54, s15, 2490368\n" \
"buffer_load_dwordx4 v32, s[40:43], s54 offen nt lds\n" \
"s_add_u32 m0, s62, 101376\n" \
"s_xor_b32 s54, s15, 2490368\n" \
"buffer_load_dwordx4 v33, s[40:43], s54 offen nt lds\n" \
"s_waitcnt vmcnt(32)\n" \
"s_barrier\n" \
"ds_read_b64_tr_b16 a[224:225], v36 offset:16384\n" \
"ds_read_b64_tr_b16 a[226:227], v36 offset:16896\n" \
"ds_read_b64_tr_b16 a[232:233], v37 offset:16384\n" \
"ds_read_b64_tr_b16 a[234:235], v37 offset:16896\n" \
"ds_read_b64_tr_b16 v[224:225], v36 offset:24576\n" \
"ds_read_b64_tr_b16 v[226:227], v36 offset:25088\n" \
"ds_read_b64_tr_b16 v[232:233], v37 offset:24576\n" \
"ds_read_b64_tr_b16 v[234:235], v37 offset:25088\n" \
"ds_read_b64_tr_b16 a[228:229], v36 offset:0\n" \
"ds_read_b64_tr_b16 a[230:231], v36 offset:512\n" \
"ds_read_b64_tr_b16 a[236:237], v37 offset:0\n" \
"ds_read_b64_tr_b16 a[238:239], v37 offset:512\n" \
"ds_read_b64_tr_b16 v[228:229], v36 offset:8192\n" \
"ds_read_b64_tr_b16 v[230:231], v36 offset:8704\n" \
"ds_read_b64_tr_b16 v[236:237], v37 offset:8192\n" \
"ds_read_b64_tr_b16 v[238:239], v37 offset:8704\n" \
"s_waitcnt lgkmcnt(0)\n" \
"s_barrier\n" \
"s_add_u32 m0, s62, 81920\n" \
"s_xor_b32 s54, s15, 3145728\n" \
"buffer_load_dwordx4 v32, s[40:43], s54 offen nt lds\n" \
"s_add_u32 m0, s62, 82944\n" \
"s_xor_b32 s54, s15, 3145728\n" \
"buffer_load_dwordx4 v33, s[40:43], s54 offen nt lds\n" \
"s_add_u32 m0, s62, 83968\n" \
"s_xor_b32 s54, s15, 3276800\n" \
"buffer_load_dwordx4 v32, s[40:43], s54 offen nt lds\n" \
"s_add_u32 m0, s62, 84992\n" \
"s_xor_b32 s54, s15, 3276800\n" \
"buffer_load_dwordx4 v33, s[40:43], s54 offen nt lds\n" \
"s_add_u32 m0, s62, 65536\n" \
"s_xor_b32 s54, s15, 3407872\n" \
"buffer_load_dwordx4 v32, s[40:43], s54 offen nt lds\n" \
"s_add_u32 m0, s62, 66560\n" \
"s_xor_b32 s54, s15, 3407872\n" \
"buffer_load_dwordx4 v33, s[40:43], s54 offen nt lds\n" \
"s_add_u32 m0, s62, 67584\n" \
"s_xor_b32 s54, s15, 3538944\n" \
"buffer_load_dwordx4 v32, s[40:43], s54 offen nt lds\n" \
"s_add_u32 m0, s62, 68608\n" \
"s_xor_b32 s54, s15, 3538944\n" \
"buffer_load_dwordx4 v33, s[40:43], s54 offen nt lds\n" \
"s_waitcnt vmcnt(32)\n" \
"s_barrier\n" \
"ds_read_b64_tr_b16 a[240:241], v34 offset:49152\n" \
"ds_read_b64_tr_b16 a[242:243], v34 offset:49664\n" \
"ds_read_b64_tr_b16 a[248:249], v35 offset:49152\n" \
"ds_read_b64_tr_b16 a[250:251], v35 offset:49664\n" \
"ds_read_b64_tr_b16 v[240:241], v34 offset:57344\n" \
"ds_read_b64_tr_b16 v[242:243], v34 offset:57856\n" \
"ds_read_b64_tr_b16 v[248:249], v35 offset:57344\n" \
"ds_read_b64_tr_b16 v[250:251], v35 offset:57856\n" \
"ds_read_b64_tr_b16 a[244:245], v34 offset:32768\n" \
"ds_read_b64_tr_b16 a[246:247], v34 offset:33280\n" \
"ds_read_b64_tr_b16 a[252:253], v35 offset:32768\n" \
"ds_read_b64_tr_b16 a[254:255], v35 offset:33280\n" \
"ds_read_b64_tr_b16 v[244:245], v34 offset:40960\n" \
"ds_read_b64_tr_b16 v[246:247], v34 offset:41472\n" \
"ds_read_b64_tr_b16 v[252:253], v35 offset:40960\n" \
"ds_read_b64_tr_b16 v[254:255], v35 offset:41472\n" \
"s_waitcnt lgkmcnt(0)\n" \
"s_barrier\n" \
"s_add_u32 m0, s62, 49152\n" \
"s_xor_b32 s54, s15, 4194304\n" \
"buffer_load_dwordx4 v32, s[40:43], s54 offen nt lds\n" \
"s_add_u32 m0, s62, 50176\n" \
"s_xor_b32 s54, s15, 4194304\n" \
"buffer_load_dwordx4 v33, s[40:43], s54 offen nt lds\n" \
"s_add_u32 m0, s62, 51200\n" \
"s_xor_b32 s54, s15, 4325376\n" \
"buffer_load_dwordx4 v32, s[40:43], s54 offen nt lds\n" \
"s_add_u32 m0, s62, 52224\n" \
"s_xor_b32 s54, s15, 4325376\n" \
"buffer_load_dwordx4 v33, s[40:43], s54 offen nt lds\n" \
"s_add_u32 m0, s62, 32768\n" \
"s_xor_b32 s54, s15, 4456448\n" \
"buffer_load_dwordx4 v32, s[40:43], s54 offen nt lds\n" \
"s_add_u32 m0, s62, 33792\n" \
"s_xor_b32 s54, s15, 4456448\n" \
"buffer_load_dwordx4 v33, s[40:43], s54 offen nt lds\n" \
"s_add_u32 m0, s62, 34816\n" \
"s_xor_b32 s54, s15, 4587520\n" \
"buffer_load_dwordx4 v32, s[40:43], s54 offen nt lds\n" \
"s_add_u32 m0, s62, 35840\n" \
"s_xor_b32 s54, s15, 4587520\n" \
"buffer_load_dwordx4 v33, s[40:43], s54 offen nt lds\n" \
"s_waitcnt vmcnt(32)\n" \
"s_barrier\n" \
"ds_read_b64_tr_b16 a[0:1], v34 offset:16384\n" \
"ds_read_b64_tr_b16 a[2:3], v34 offset:16896\n" \
"ds_read_b64_tr_b16 a[8:9], v35 offset:16384\n" \
"ds_read_b64_tr_b16 a[10:11], v35 offset:16896\n" \
"ds_read_b64_tr_b16 v[48:49], v34 offset:24576\n" \
"ds_read_b64_tr_b16 v[50:51], v34 offset:25088\n" \
"ds_read_b64_tr_b16 v[56:57], v35 offset:24576\n" \
"ds_read_b64_tr_b16 v[58:59], v35 offset:25088\n" \
"ds_read_b64_tr_b16 a[4:5], v34 offset:0\n" \
"ds_read_b64_tr_b16 a[6:7], v34 offset:512\n" \
"ds_read_b64_tr_b16 a[12:13], v35 offset:0\n" \
"ds_read_b64_tr_b16 a[14:15], v35 offset:512\n" \
"ds_read_b64_tr_b16 v[52:53], v34 offset:8192\n" \
"ds_read_b64_tr_b16 v[54:55], v34 offset:8704\n" \
"ds_read_b64_tr_b16 v[60:61], v35 offset:8192\n" \
"ds_read_b64_tr_b16 v[62:63], v35 offset:8704\n" \
"s_waitcnt lgkmcnt(0)\n" \
"s_barrier\n" \
"s_add_u32 m0, s62, 16384\n" \
"s_xor_b32 s54, s15, 5242880\n" \
"buffer_load_dwordx4 v32, s[40:43], s54 offen nt lds\n" \
"s_add_u32 m0, s62, 17408\n" \
"s_xor_b32 s54, s15, 5242880\n" \
"buffer_load_dwordx4 v33, s[40:43], s54 offen nt lds\n" \
"s_add_u32 m0, s62, 18432\n" \
"s_xor_b32 s54, s15, 5373952\n" \
"buffer_load_dwordx4 v32, s[40:43], s54 offen nt lds\n" \
"s_add_u32 m0, s62, 19456\n" \
"s_xor_b32 s54, s15, 5373952\n" \
"buffer_load_dwordx4 v33, s[40:43], s54 offen nt lds\n" \
"s_add_u32 m0, s62, 0\n" \
"s_xor_b32 s54, s15, 5505024\n" \
"buffer_load_dwordx4 v32, s[40:43], s54 offen nt lds\n" \
"s_add_u32 m0, s62, 1024\n" \
"s_xor_b32 s54, s15, 5505024\n" \
"buffer_load_dwordx4 v33, s[40:43], s54 offen nt lds\n" \
"s_add_u32 m0, s62, 2048\n" \
"s_xor_b32 s54, s15, 5636096\n" \
"buffer_load_dwordx4 v32, s[40:43], s54 offen nt lds\n" \
"s_add_u32 m0, s62, 3072\n" \
"s_xor_b32 s54, s15, 5636096\n" \
"buffer_load_dwordx4 v33, s[40:43], s54 offen nt lds\n" \
"s_waitcnt vmcnt(32)\n" \
"s_barrier\n" \
"ds_read_b64_tr_b16 a[32:33], v38 offset:16384\n" \
"ds_read_b64_tr_b16 a[34:35], v38 offset:16896\n" \
"ds_read_b64_tr_b16 a[40:41], v39 offset:16384\n" \
"ds_read_b64_tr_b16 a[42:43], v39 offset:16896\n" \
"ds_read_b64_tr_b16 v[64:65], v38 offset:24576\n" \
"ds_read_b64_tr_b16 v[66:67], v38 offset:25088\n" \
"ds_read_b64_tr_b16 v[72:73], v39 offset:24576\n" \
"ds_read_b64_tr_b16 v[74:75], v39 offset:25088\n" \
"ds_read_b64_tr_b16 a[36:37], v38 offset:0\n" \
"ds_read_b64_tr_b16 a[38:39], v38 offset:512\n" \
"ds_read_b64_tr_b16 a[44:45], v39 offset:0\n" \
"ds_read_b64_tr_b16 a[46:47], v39 offset:512\n" \
"ds_read_b64_tr_b16 v[68:69], v38 offset:8192\n" \
"ds_read_b64_tr_b16 v[70:71], v38 offset:8704\n" \
"ds_read_b64_tr_b16 v[76:77], v39 offset:8192\n" \
"ds_read_b64_tr_b16 v[78:79], v39 offset:8704\n" \
"s_waitcnt lgkmcnt(0)\n" \
"s_barrier\n" \
"s_add_u32 m0, s62, 147456\n" \
"s_xor_b32 s54, s15, 6291456\n" \
"buffer_load_dwordx4 v32, s[40:43], s54 offen nt lds\n" \
"s_add_u32 m0, s62, 148480\n" \
"s_xor_b32 s54, s15, 6291456\n" \
"buffer_load_dwordx4 v33, s[40:43], s54 offen nt lds\n" \
"s_add_u32 m0, s62, 149504\n" \
"s_xor_b32 s54, s15, 6422528\n" \
"buffer_load_dwordx4 v32, s[40:43], s54 offen nt lds\n" \
"s_add_u32 m0, s62, 150528\n" \
"s_xor_b32 s54, s15, 6422528\n" \
"buffer_load_dwordx4 v33, s[40:43], s54 offen nt lds\n" \
"s_add_u32 m0, s62, 131072\n" \
"s_xor_b32 s54, s15, 6553600\n" \
"buffer_load_dwordx4 v32, s[40:43], s54 offen nt lds\n" \
"s_add_u32 m0, s62, 132096\n" \
"s_xor_b32 s54, s15, 6553600\n" \
"buffer_load_dwordx4 v33, s[40:43], s54 offen nt lds\n" \
"s_add_u32 m0, s62, 133120\n" \
"s_xor_b32 s54, s15, 6684672\n" \
"buffer_load_dwordx4 v32, s[40:43], s54 offen nt lds\n" \
"s_add_u32 m0, s62, 134144\n" \
"s_xor_b32 s54, s15, 6684672\n" \
"buffer_load_dwordx4 v33, s[40:43], s54 offen nt lds\n" \
"s_waitcnt vmcnt(32)\n" \
"s_barrier\n" \
"ds_read_b64_tr_b16 a[64:65], v36 offset:49152\n" \
"ds_read_b64_tr_b16 a[66:67], v36 offset:49664\n" \
"ds_read_b64_tr_b16 a[72:73], v37 offset:49152\n" \
"ds_read_b64_tr_b16 a[74:75], v37 offset:49664\n" \
"ds_read_b64_tr_b16 v[80:81], v36 offset:57344\n" \
"ds_read_b64_tr_b16 v[82:83], v36 offset:57856\n" \
"ds_read_b64_tr_b16 v[88:89], v37 offset:57344\n" \
"ds_read_b64_tr_b16 v[90:91], v37 offset:57856\n" \
"ds_read_b64_tr_b16 a[68:69], v36 offset:32768\n" \
"ds_read_b64_tr_b16 a[70:71], v36 offset:33280\n" \
"ds_read_b64_tr_b16 a[76:77], v37 offset:32768\n" \
"ds_read_b64_tr_b16 a[78:79], v37 offset:33280\n" \
"ds_read_b64_tr_b16 v[84:85], v36 offset:40960\n" \
"ds_read_b64_tr_b16 v[86:87], v36 offset:41472\n" \
"ds_read_b64_tr_b16 v[92:93], v37 offset:40960\n" \
"ds_read_b64_tr_b16 v[94:95], v37 offset:41472\n" \
"s_waitcnt lgkmcnt(0)\n" \
"s_barrier\n" \
"ds_write_b128 v2, v[48:51] offset:32768\n" \
"ds_write_b128 v2, v[52:55] offset:33792\n" \
"ds_write_b128 v2, v[56:59] offset:34816\n" \
"ds_write_b128 v2, v[60:63] offset:35840\n" \
"s_waitcnt vmcnt(24)\n" \
"s_barrier\n" \
"ds_read_b64_tr_b16 a[96:97], v36 offset:16384\n" \
"ds_read_b64_tr_b16 a[98:99], v36 offset:16896\n" \
"ds_read_b64_tr_b16 a[104:105], v37 offset:16384\n" \
"ds_read_b64_tr_b16 a[106:107], v37 offset:16896\n" \
"ds_read_b64_tr_b16 v[96:97], v36 offset:24576\n" \
"ds_read_b64_tr_b16 v[98:99], v36 offset:25088\n" \
"ds_read_b64_tr_b16 v[104:105], v37 offset:24576\n" \
"ds_read_b64_tr_b16 v[106:107], v37 offset:25088\n" \
"ds_read_b64_tr_b16 a[100:101], v36 offset:0\n" \
"ds_read_b64_tr_b16 a[102:103], v36 offset:512\n" \
"ds_read_b64_tr_b16 a[108:109], v37 offset:0\n" \
"ds_read_b64_tr_b16 a[110:111], v37 offset:512\n" \
"ds_read_b64_tr_b16 v[100:101], v36 offset:8192\n" \
"ds_read_b64_tr_b16 v[102:103], v36 offset:8704\n" \
"ds_read_b64_tr_b16 v[108:109], v37 offset:8192\n" \
"ds_read_b64_tr_b16 v[110:111], v37 offset:8704\n" \
"s_waitcnt lgkmcnt(0)\n" \
"s_barrier\n" \
"ds_write_b128 v2, v[64:67] offset:0\n" \
"ds_write_b128 v2, v[68:71] offset:1024\n" \
"ds_write_b128 v2, v[72:75] offset:2048\n" \
"ds_write_b128 v2, v[76:79] offset:3072\n" \
"ds_write_b128 v2, v[80:83] offset:16384\n" \
"ds_write_b128 v2, v[84:87] offset:17408\n" \
"ds_write_b128 v2, v[88:91] offset:18432\n" \
"ds_write_b128 v2, v[92:95] offset:19456\n" \
"s_waitcnt vmcnt(16)\n" \
"s_barrier\n" \
"ds_read_b64_tr_b16 a[128:129], v34 offset:49152\n" \
"ds_read_b64_tr_b16 a[130:131], v34 offset:49664\n" \
"ds_read_b64_tr_b16 a[136:137], v35 offset:49152\n" \
"ds_read_b64_tr_b16 a[138:139], v35 offset:49664\n" \
"ds_read_b64_tr_b16 v[48:49], v34 offset:57344\n" \
"ds_read_b64_tr_b16 v[50:51], v34 offset:57856\n" \
"ds_read_b64_tr_b16 v[56:57], v35 offset:57344\n" \
"ds_read_b64_tr_b16 v[58:59], v35 offset:57856\n" \
"ds_read_b64_tr_b16 a[132:133], v34 offset:32768\n" \
"ds_read_b64_tr_b16 a[134:135], v34 offset:33280\n" \
"ds_read_b64_tr_b16 a[140:141], v35 offset:32768\n" \
"ds_read_b64_tr_b16 a[142:143], v35 offset:33280\n" \
"ds_read_b64_tr_b16 v[52:53], v34 offset:40960\n" \
"ds_read_b64_tr_b16 v[54:55], v34 offset:41472\n" \
"ds_read_b64_tr_b16 v[60:61], v35 offset:40960\n" \
"ds_read_b64_tr_b16 v[62:63], v35 offset:41472\n" \
"s_waitcnt lgkmcnt(0)\n" \
"s_barrier\n" \
"ds_write_b128 v1, v[96:99] offset:32768\n" \
"ds_write_b128 v1, v[100:103] offset:33792\n" \
"ds_write_b128 v1, v[104:107] offset:34816\n" \
"ds_write_b128 v1, v[108:111] offset:35840\n" \
"ds_write_b128 v1, v[48:51] offset:49152\n" \
"ds_write_b128 v1, v[52:55] offset:50176\n" \
"ds_write_b128 v1, v[56:59] offset:51200\n" \
"ds_write_b128 v1, v[60:63] offset:52224\n" \
"s_waitcnt vmcnt(8)\n" \
"s_barrier\n" \
"ds_read_b64_tr_b16 a[160:161], v34 offset:16384\n" \
"ds_read_b64_tr_b16 a[162:163], v34 offset:16896\n" \
"ds_read_b64_tr_b16 a[168:169], v35 offset:16384\n" \
"ds_read_b64_tr_b16 a[170:171], v35 offset:16896\n" \
"ds_read_b64_tr_b16 v[64:65], v34 offset:24576\n" \
"ds_read_b64_tr_b16 v[66:67], v34 offset:25088\n" \
"ds_read_b64_tr_b16 v[72:73], v35 offset:24576\n" \
"ds_read_b64_tr_b16 v[74:75], v35 offset:25088\n" \
"ds_read_b64_tr_b16 a[164:165], v34 offset:0\n" \
"ds_read_b64_tr_b16 a[166:167], v34 offset:512\n" \
"ds_read_b64_tr_b16 a[172:173], v35 offset:0\n" \
"ds_read_b64_tr_b16 a[174:175], v35 offset:512\n" \
"ds_read_b64_tr_b16 v[68:69], v34 offset:8192\n" \
"ds_read_b64_tr_b16 v[70:71], v34 offset:8704\n" \
"ds_read_b64_tr_b16 v[76:77], v35 offset:8192\n" \
"ds_read_b64_tr_b16 v[78:79], v35 offset:8704\n" \
"s_waitcnt lgkmcnt(0)\n" \
"s_barrier\n" \
"ds_write_b128 v1, v[64:67] offset:0\n" \
"ds_write_b128 v1, v[68:71] offset:1024\n" \
"ds_write_b128 v1, v[72:75] offset:2048\n" \
"ds_write_b128 v1, v[76:79] offset:3072\n" \
"s_waitcnt vmcnt(0)\n" \
"s_barrier\n" \
"ds_read_b64_tr_b16 a[192:193], v38 offset:16384\n" \
"ds_read_b64_tr_b16 a[194:195], v38 offset:16896\n" \
"ds_read_b64_tr_b16 a[200:201], v39 offset:16384\n" \
"ds_read_b64_tr_b16 a[202:203], v39 offset:16896\n" \
"ds_read_b64_tr_b16 v[80:81], v38 offset:24576\n" \
"ds_read_b64_tr_b16 v[82:83], v38 offset:25088\n" \
"ds_read_b64_tr_b16 v[88:89], v39 offset:24576\n" \
"ds_read_b64_tr_b16 v[90:91], v39 offset:25088\n" \
"ds_read_b64_tr_b16 a[196:197], v38 offset:0\n" \
"ds_read_b64_tr_b16 a[198:199], v38 offset:512\n" \
"ds_read_b64_tr_b16 a[204:205], v39 offset:0\n" \
"ds_read_b64_tr_b16 a[206:207], v39 offset:512\n" \
"ds_read_b64_tr_b16 v[84:85], v38 offset:8192\n" \
"ds_read_b64_tr_b16 v[86:87], v38 offset:8704\n" \
"ds_read_b64_tr_b16 v[92:93], v39 offset:8192\n" \
"ds_read_b64_tr_b16 v[94:95], v39 offset:8704\n" \
"s_waitcnt lgkmcnt(0)\n" \
"s_barrier\n" \
"ds_write_b128 v1, v[80:83] offset:16384\n" \
"ds_write_b128 v1, v[84:87] offset:17408\n" \
"ds_write_b128 v1, v[88:91] offset:18432\n" \
"ds_write_b128 v1, v[92:95] offset:19456\n" \
"s_waitcnt vmcnt(0) lgkmcnt(0)\n" \
"s_barrier\n" \
"s_getpc_b64 s[18:19]\n" \
".Lq_pc_%=:\n" \
"v_mbcnt_lo_u32_b32 v40, -1, 0\n" \
"v_mbcnt_hi_u32_b32 v40, -1, v40\n" \
"v_lshl_add_u32 v40, s48, 6, v40\n" \
"v_lshlrev_b32 v40, 6, v40\n" \
"v_min_u32 v40, .Lq_fin_%=-.Lq_pc_%=, v40\n" \
"global_load_dword v41, v40, s[18:19]\n" \
"s_lshl_b32 s62, s48, 11\n" \
"s_mov_b32 s21, s12\n" \
"s_add_u32 s12, s63, s62\n" \
"s_mov_b32 s49, 0\n" \
"s_mov_b32 s52, 0\n" \
"s_mov_b32 s81, 0\n" \
"s_mov_b32 s88, 0\n" \
"s_mov_b32 s95, 0x7ffffff0\n" \
"s_mov_b32 s98, 0x3e0293ee\n" \
"s_mov_b32 s99, 0x3e0293ee\n" \
"v_mov_b32 v20, 0x3f803f80\n" \
"v_mov_b32 v21, 0x3f803f80\n" \
"v_mov_b32 v22, 0x3f803f80\n" \
"v_mov_b32 v23, 0x3f803f80\n" \
"v_mov_b32 v24, 0\n" \
"v_mov_b32 v25, 0\n" \
"v_mov_b32 v26, 0\n" \
"v_mov_b32 v27, 0\n" \
"v_mov_b32 v28, 0\n" \
"v_mov_b32 v29, 0\n" \
"v_mov_b32 v104, 0\n" \
"v_mov_b32 v105, 0\n" \
"v_mov_b32 v106, 0\n" \
"v_mov_b32 v107, 0\n" \
"v_mov_b32 v108, 0\n" \
"v_mov_b32 v109, 0\n" \
"v_mov_b32 v110, 0\n" \
"v_mov_b32 v111, 0\n" \
"v_cmp_le_i32_e64 s[64:65], 0, v14\n" \
"v_cmp_le_i32_e64 s[66:67], 1, v14\n" \
"v_cmp_le_i32_e64 s[68:69], 2, v14\n" \
"v_cmp_le_i32_e64 s[70:71], 3, v14\n" \
"v_cmp_le_i32_e64 s[72:73], 16, v14\n" \
"v_cmp_le_i32_e64 s[74:75], 17, v14\n" \
"v_cmp_le_i32_e64 s[76:77], 18, v14\n" \
"v_cmp_le_i32_e64 s[78:79], 19, v14\n" \
"s_mov_b32 s82, -1\n" \
"s_mov_b32 s83, 0\n" \
"s_mov_b32 s6, 0x33333333\n" \
"s_mov_b32 s7, 0x33333333\n" \
"s_mov_b32 s8, 0x53800000\n" \
"s_mov_b32 s41, s10\n" \
"s_mov_b32 s22, 0xfffffc00\n" \
"s_mov_b32 s23, -1\n" \
"s_mul_i32 s32, s46, 1\n" \
"s_mov_b32 s33, s32\n" \
"s_mov_b32 s34, s46\n" \
"s_add_u32 s35, s17, s13\n" \
"s_mov_b32 s10, s14\n" \
"s_mov_b32 s14, s21\n" \
"s_lshl_b32 s42, s48, 10\n" \
"s_add_u32 s42, s42, 114688\n" \
"s_and_b32 s43, s48, 1\n" \
"s_lshl_b32 s43, s43, 12\n" \
"s_mov_b32 s54, 0\n" \
"buffer_store_dwordx4 v[24:27], v7, s[36:39], s54 offen sc1\n" \
"s_mov_b32 s54, 131072\n" \
"buffer_store_dwordx4 v[24:27], v7, s[36:39], s54 offen sc1\n" \
"s_add_u32 s54, s94, 65536\n" \
"buffer_store_dword v24, v10, s[36:39], s54 offen sc1\n" \
"s_mov_b32 s54, 262144\n" \
"buffer_store_dwordx4 v[24:27], v7, s[36:39], s54 offen sc1\n" \
"s_add_u32 s54, s94, 131072\n" \
"buffer_store_dword v24, v10, s[36:39], s54 offen sc1\n" \
"s_mov_b32 s54, 393216\n" \
"buffer_store_dwordx4 v[24:27], v7, s[36:39], s54 offen sc1\n" \
"s_add_u32 s54, s94, 196608\n" \
"buffer_store_dword v24, v10, s[36:39], s54 offen sc1\n" \
"s_cmp_eq_u32 s48, 0\n" \
"s_cselect_b32 s9, 7, 0\n" \
"buffer_load_dword v30, v15, s[36:39], s80 offen sc1\n" \
"s_waitcnt vmcnt(0)\n" \
"v_cmp_ge_u32_e64 s[96:97], s81, v30\n" \
"s_nop 4\n" \
"s_and_b32 s96, s96, s9\n" \
"s_cbranch_scc1 .Lq_f0_cold_%=\n" \
".Lq_f0_ok_%=:\n" \
"s_barrier\n" \
"s_add_u32 m0, s62, 131072\n" \
"s_sub_u32 s15, s12, 114688\n" \
"buffer_load_dwordx4 v3, s[36:39], s15 offen offset:0 sc1 lds\n" \
"buffer_load_dwordx4 v3, s[36:39], s15 offen offset:1024 sc1 lds\n" \
"s_add_u32 s54, s49, 0\n" \
"s_and_b32 s54, s54, 3\n" \
"s_mul_i32 s58, s54, 131072\n" \
"s_add_u32 s54, s49, 1\n" \
"s_and_b32 s54, s54, 3\n" \
"s_mul_i32 s59, s54, 131072\n" \
"s_add_u32 s54, s49, 2\n" \
"s_and_b32 s54, s54, 3\n" \
"s_mul_i32 s60, s54, 131072\n" \
"s_add_u32 s54, s49, 3\n" \
"s_and_b32 s54, s54, 3\n" \
"s_mul_i32 s61, s54, 131072\n" \
"s_mov_b32 s53, 0\n" \
"s_cmp_lg_u32 s48, 0\n" \
"s_cbranch_scc1 .Lq_go0x_%=\n" \
".Lq_go0_%=:\n" \
"buffer_load_dword v13, v0, s[36:39], 0 offen sc1\n" \
"s_waitcnt vmcnt(0)\n" \
"v_cmp_eq_u32 vcc, 0, v13\n" \
"s_add_u32 s53, s53, 1\n" \
"s_cmp_lt_u32 s53, 32768\n" \
"s_cselect_b64 s[54:55], -1, 0\n" \
"s_and_b64 vcc, vcc, s[54:55]\n" \
"s_cbranch_vccz .Lq_go0x_%=\n" \
"s_sleep 4\n" \
"s_branch .Lq_go0_%=\n" \
".Lq_go0x_%=:\n" \
"s_lshr_b32 s53, s58, 1\n" \
"s_add_u32 s92, s53, s94\n" \
"s_add_u32 s55, s53, s41\n" \
"s_xor_b32 s89, s55, 0x4800\n" \
"s_xor_b32 s90, s55, 0x800\n" \
"s_xor_b32 s91, s55, 0x4000\n" \
"s_lshr_b32 s93, s59, 1\n" \
"s_add_u32 s93, s93, s94\n" \
"s_add_u32 s81, s49, 2\n" \
"s_min_u32 s81, s81, 63\n" \
"ds_read_b128 v[32:35], v2 offset:32768\n" \
"ds_read_b128 v[36:39], v2 offset:33792\n" \
"ds_read_b128 v[40:43], v2 offset:34816\n" \
"ds_read_b128 v[44:47], v2 offset:35840\n" \
"ds_read_b128 v[48:51], v2 offset:0\n" \
"ds_read_b128 v[52:55], v2 offset:1024\n" \
"ds_read_b128 v[56:59], v2 offset:2048\n" \
"ds_read_b128 v[60:63], v2 offset:3072\n" \
"v_mov_b64 v[80:81], 0\n" \
"v_mov_b64 v[82:83], 0\n" \
"v_mov_b64 v[84:85], 0\n" \
"v_mov_b64 v[86:87], 0\n" \
"v_mov_b64 v[88:89], 0\n" \
"v_mov_b64 v[90:91], 0\n" \
"v_mov_b64 v[92:93], 0\n" \
"v_mov_b64 v[94:95], 0\n" \
"v_mov_b32 v13, -1\n" \
"v_add_u32 v14, s62, v3\n" \
"v_xor_b32 v31, 0x400, v11\n" \
"s_max_u32 s53, s49, 1\n" \
"s_sub_u32 s53, s53, 1\n" \
"v_mov_b32 v30, s53\n" \
"s_mov_b32 m0, s42\n" \
"s_memrealtime s[56:57]\n" \
"s_waitcnt lgkmcnt(0)\n" \
"s_sub_u32 s50, s56, 20\n" \
"s_waitcnt vmcnt(0) lgkmcnt(0)\n" \
"s_nop 7\n" \
"s_nop 7\n" \
".p2align 6\n" \
".Lq_loop_%=:\n" \
"s_getpc_b64 s[18:19]\n" \
".Lq_wp_%=:\n" \
"s_add_u32 s18, s18, .Lq_ws_%=-.Lq_wp_%=\n" \
"s_addc_u32 s19, s19, 0\n" \
"s_sub_u32 s21, s50, 5\n" \
"s_nop 0\n" \
".Lq_w_%=:\n" \
"s_memrealtime s[56:57]\n" \
"s_waitcnt lgkmcnt(0)\n" \
"s_sub_u32 s54, s56, s21\n" \
"s_cmp_gt_u32 s54, s22\n" \
"s_cbranch_scc1 .Lq_w_%=\n" \
"s_min_u32 s21, s54, 3\n" \
"s_lshl2_add_u32 s18, s21, s18\n" \
"s_setpc_b64 s[18:19]\n" \
"s_nop 0\n" \
".Lq_ws_%=:\n" \
"s_nop 4\n" \
"s_nop 5\n" \
"s_nop 4\n" \
".Lq_w4_%=:\n" \
"s_barrier\n" \
".Lq_go_%=:\n" \
"buffer_load_dwordx4 v4, s[36:39], s55 offen sc1 lds\n" \
"s_memtime s[0:1]\n" \
"s_cmp_lt_u32 s54, 8\n" \
"s_cselect_b32 s51, s50, s56\n" \
"s_cselect_b32 s23, s23, s49\n" \
"s_cselect_b32 s53, 0, s14\n" \
"s_add_u32 s53, s53, s34\n" \
"v_mov_b32 v17, s35\n" \
"s_nop 7\n" \
"s_nop 7\n" \
"s_add_u32 m0, s42, 4096\n" \
"s_nop 0\n" \
"buffer_load_dwordx4 v4, s[36:39], s89 offen sc1 lds\n" \
"s_nop 7\n" \
"s_nop 7\n" \
"s_nop 7\n" \
"s_add_u32 m0, s42, 8192\n" \
"s_nop 0\n" \
"buffer_load_dwordx4 v4, s[36:39], s90 offen sc1 lds\n" \
"s_add_u32 m0, s42, 12288\n" \
"s_nop 0\n" \
"buffer_load_dwordx4 v4, s[36:39], s91 offen sc1 lds\n" \
"s_add_u32 m0, s62, 147456\n" \
"s_sub_u32 s15, s12, 81920\n" \
"buffer_load_dwordx4 v3, s[36:39], s15 offen offset:0 sc1 lds\n" \
"buffer_load_dwordx4 v3, s[36:39], s15 offen offset:1024 sc1 lds\n" \
"s_add_u32 m0, s62, 139264\n" \
"s_sub_u32 s15, s12, 98304\n" \
"buffer_load_dwordx4 v3, s[36:39], s15 offen offset:0 sc1 lds\n" \
"buffer_load_dwordx4 v3, s[36:39], s15 offen offset:1024 sc1 lds\n" \
"s_waitcnt lgkmcnt(0)\n" \
"s_waitcnt vmcnt(7)\n" \
"s_barrier\n" \
"s_memtime s[2:3]\n" \
"ds_read_b128 v[64:67], v3 offset:0\n" \
"ds_read_b128 v[68:71], v3 offset:1024\n" \
"ds_read_b128 v[72:75], v3 offset:2048\n" \
"ds_read_b128 v[76:79], v3 offset:3072\n" \
"s_waitcnt lgkmcnt(3)\n" \
"v_smfmac_f32_16x16x64_bf16 v[80:83], v[64:67], a[0:7], v12\n" \
"v_min3_u32 v13, v13, v64, v66\n" \
"v_smfmac_f32_16x16x64_bf16 v[84:87], v[64:67], a[8:15], v12\n" \
"s_waitcnt lgkmcnt(2)\n" \
"v_min3_u32 v13, v13, v68, v70\n" \
"v_smfmac_f32_16x16x64_bf16 v[88:91], v[64:67], v[32:39], v12\n" \
"s_waitcnt lgkmcnt(1)\n" \
"v_min3_u32 v13, v13, v72, v74\n" \
"v_smfmac_f32_16x16x64_bf16 v[92:95], v[64:67], v[40:47], v12\n" \
"s_waitcnt lgkmcnt(0)\n" \
"v_min3_u32 v13, v13, v76, v78\n" \
"v_smfmac_f32_16x16x64_bf16 v[80:83], v[68:71], a[16:23], v12\n" \
"s_sub_u32 s2, s2, s0\n" \
"s_mul_i32 s2, s2, 23\n" \
"v_cmp_eq_u32 vcc, 0, v13\n" \
"v_smfmac_f32_16x16x64_bf16 v[84:87], v[68:71], a[24:31], v12\n" \
"ds_read_b128 v[32:35], v2 offset:16384\n" \
"v_smfmac_f32_16x16x64_bf16 v[88:91], v[68:71], v[112:119], v12\n" \
"ds_read_b128 v[36:39], v2 offset:17408\n" \
"v_smfmac_f32_16x16x64_bf16 v[92:95], v[68:71], v[120:127], v12\n" \
"s_cbranch_vccnz .Lq_retry_%=\n" \
"s_waitcnt vmcnt(6)\n" \
"s_barrier\n" \
"ds_read_b128 v[64:67], v3 offset:4096\n" \
"v_smfmac_f32_16x16x64_bf16 v[80:83], v[72:75], a[32:39], v12\n" \
"ds_read_b128 v[68:71], v3 offset:5120\n" \
"s_lshr_b32 s2, s2, 9\n" \
"v_smfmac_f32_16x16x64_bf16 v[84:87], v[72:75], a[40:47], v12\n" \
"ds_read_b128 v[40:43], v2 offset:18432\n" \
"s_add_u32 s3, s51, s2\n" \
"v_smfmac_f32_16x16x64_bf16 v[88:91], v[72:75], v[48:55], v12\n" \
"ds_read_b128 v[44:47], v2 offset:19456\n" \
"s_add_u32 s53, s53, s3\n" \
"v_smfmac_f32_16x16x64_bf16 v[92:95], v[72:75], v[56:63], v12\n" \
"ds_read_b128 v[72:75], v3 offset:6144\n" \
"s_max_u32 s53, s53, 1\n" \
"v_mov_b32 v16, s53\n" \
"v_smfmac_f32_16x16x64_bf16 v[80:83], v[76:79], a[48:55], v12\n" \
"buffer_store_dwordx2 v[16:17], v10, s[36:39], s92 offen sc1\n" \
"v_smfmac_f32_16x16x64_bf16 v[84:87], v[76:79], a[56:63], v12\n" \
"ds_read_b128 v[48:51], v1 offset:32768\n" \
"v_smfmac_f32_16x16x64_bf16 v[88:91], v[76:79], v[128:135], v12\n" \
"ds_read_b128 v[52:55], v1 offset:33792\n" \
"v_smfmac_f32_16x16x64_bf16 v[92:95], v[76:79], v[136:143], v12\n" \
"ds_read_b128 v[76:79], v3 offset:7168\n" \
"s_waitcnt lgkmcnt(7)\n" \
"v_smfmac_f32_16x16x64_bf16 v[80:83], v[64:67], a[64:71], v12\n" \
"v_min3_u32 v13, v13, v64, v66\n" \
"v_smfmac_f32_16x16x64_bf16 v[84:87], v[64:67], a[72:79], v12\n" \
"ds_read_b128 v[56:59], v1 offset:34816\n" \
"v_smfmac_f32_16x16x64_bf16 v[88:91], v[64:67], v[32:39], v12\n" \
"ds_read_b128 v[60:63], v1 offset:35840\n" \
"s_waitcnt lgkmcnt(6)\n" \
"v_smfmac_f32_16x16x64_bf16 v[92:95], v[64:67], v[40:47], v12\n" \
"v_smfmac_f32_16x16x64_bf16 v[80:83], v[68:71], a[80:87], v12\n" \
"v_min3_u32 v13, v13, v68, v70\n" \
"v_smfmac_f32_16x16x64_bf16 v[84:87], v[68:71], a[88:95], v12\n" \
"ds_read_b128 v[32:35], v1 offset:49152\n" \
"v_smfmac_f32_16x16x64_bf16 v[88:91], v[68:71], v[144:151], v12\n" \
"ds_read_b128 v[36:39], v1 offset:50176\n" \
"v_smfmac_f32_16x16x64_bf16 v[92:95], v[68:71], v[152:159], v12\n" \
"s_waitcnt lgkmcnt(7)\n" \
"v_smfmac_f32_16x16x64_bf16 v[80:83], v[72:75], a[96:103], v12\n" \
"v_min3_u32 v13, v13, v72, v74\n" \
"v_smfmac_f32_16x16x64_bf16 v[84:87], v[72:75], a[104:111], v12\n" \
"ds_read_b128 v[40:43], v1 offset:51200\n" \
"s_waitcnt lgkmcnt(6)\n" \
"v_smfmac_f32_16x16x64_bf16 v[88:91], v[72:75], v[48:55], v12\n" \
"ds_read_b128 v[44:47], v1 offset:52224\n" \
"s_waitcnt lgkmcnt(4)\n" \
"v_smfmac_f32_16x16x64_bf16 v[92:95], v[72:75], v[56:63], v12\n" \
"s_waitcnt vmcnt(5)\n" \
"s_barrier\n" \
"ds_read_b128 v[64:67], v3 offset:8192\n" \
"v_smfmac_f32_16x16x64_bf16 v[80:83], v[76:79], a[112:119], v12\n" \
"ds_read_b128 v[68:71], v3 offset:9216\n" \
"v_min3_u32 v13, v13, v76, v78\n" \
"v_smfmac_f32_16x16x64_bf16 v[84:87], v[76:79], a[120:127], v12\n" \
"ds_read_b128 v[48:51], v1 offset:0\n" \
"v_smfmac_f32_16x16x64_bf16 v[88:91], v[76:79], v[160:167], v12\n" \
"ds_read_b128 v[52:55], v1 offset:1024\n" \
"v_smfmac_f32_16x16x64_bf16 v[92:95], v[76:79], v[168:175], v12\n" \
"ds_read_b128 v[72:75], v3 offset:10240\n" \
"ds_read_b128 v[76:79], v3 offset:11264\n" \
"s_waitcnt lgkmcnt(5)\n" \
"v_smfmac_f32_16x16x64_bf16 v[80:83], v[64:67], a[128:135], v12\n" \
"buffer_store_dwordx4 v[96:99], v11, s[36:39], s95 offen sc1\n" \
"v_min3_u32 v13, v13, v64, v66\n" \
"v_smfmac_f32_16x16x64_bf16 v[84:87], v[64:67], a[136:143], v12\n" \
"ds_read_b128 v[56:59], v1 offset:2048\n" \
"v_smfmac_f32_16x16x64_bf16 v[88:91], v[64:67], v[32:39], v12\n" \
"ds_read_b128 v[60:63], v1 offset:3072\n" \
"v_smfmac_f32_16x16x64_bf16 v[92:95], v[64:67], v[40:47], v12\n" \
"ds_read_b128 v[64:67], v3 offset:12288\n" \
"s_waitcnt lgkmcnt(7)\n" \
"v_smfmac_f32_16x16x64_bf16 v[80:83], v[68:71], a[144:151], v12\n" \
"v_min3_u32 v13, v13, v68, v70\n" \
"v_smfmac_f32_16x16x64_bf16 v[84:87], v[68:71], a[152:159], v12\n" \
"ds_read_b128 v[32:35], v1 offset:16384\n" \
"v_smfmac_f32_16x16x64_bf16 v[88:91], v[68:71], v[176:183], v12\n" \
"ds_read_b128 v[36:39], v1 offset:17408\n" \
"v_smfmac_f32_16x16x64_bf16 v[92:95], v[68:71], v[184:191], v12\n" \
"ds_read_b128 v[68:71], v3 offset:13312\n" \
"s_waitcnt lgkmcnt(7)\n" \
"v_smfmac_f32_16x16x64_bf16 v[80:83], v[72:75], a[160:167], v12\n" \
"v_min3_u32 v13, v13, v72, v74\n" \
"v_smfmac_f32_16x16x64_bf16 v[84:87], v[72:75], a[168:175], v12\n" \
"ds_read_b128 v[40:43], v1 offset:18432\n" \
"v_smfmac_f32_16x16x64_bf16 v[88:91], v[72:75], v[48:55], v12\n" \
"ds_read_b128 v[44:47], v1 offset:19456\n" \
"s_waitcnt lgkmcnt(6)\n" \
"v_smfmac_f32_16x16x64_bf16 v[92:95], v[72:75], v[56:63], v12\n" \
"ds_read_b128 v[72:75], v3 offset:14336\n" \
"v_smfmac_f32_16x16x64_bf16 v[80:83], v[76:79], a[176:183], v12\n" \
"buffer_store_dwordx4 v[100:103], v31, s[36:39], s95 offen sc1\n" \
"v_min3_u32 v13, v13, v76, v78\n" \
"v_smfmac_f32_16x16x64_bf16 v[84:87], v[76:79], a[184:191], v12\n" \
"ds_read_b128 v[48:51], v3 offset:20480\n" \
"v_smfmac_f32_16x16x64_bf16 v[88:91], v[76:79], v[192:199], v12\n" \
"ds_read_b128 v[52:55], v3 offset:21504\n" \
"v_smfmac_f32_16x16x64_bf16 v[92:95], v[76:79], v[200:207], v12\n" \
"ds_read_b128 v[76:79], v3 offset:15360\n" \
"s_waitcnt lgkmcnt(9)\n" \
"v_smfmac_f32_16x16x64_bf16 v[80:83], v[64:67], a[192:199], v12\n" \
"v_min3_u32 v13, v13, v64, v66\n" \
"v_smfmac_f32_16x16x64_bf16 v[84:87], v[64:67], a[200:207], v12\n" \
"ds_read_b128 v[56:59], v3 offset:22528\n" \
"s_waitcnt lgkmcnt(8)\n" \
"v_smfmac_f32_16x16x64_bf16 v[88:91], v[64:67], v[32:39], v12\n" \
"ds_read_b128 v[60:63], v3 offset:23552\n" \
"s_waitcnt lgkmcnt(6)\n" \
"v_smfmac_f32_16x16x64_bf16 v[92:95], v[64:67], v[40:47], v12\n" \
"v_smfmac_f32_16x16x64_bf16 v[80:83], v[68:71], a[208:215], v12\n" \
"v_min3_u32 v13, v13, v68, v70\n" \
"v_smfmac_f32_16x16x64_bf16 v[84:87], v[68:71], a[216:223], v12\n" \
"ds_read_b128 v[32:35], v3 offset:16384\n" \
"v_smfmac_f32_16x16x64_bf16 v[88:91], v[68:71], v[208:215], v12\n" \
"ds_read_b128 v[36:39], v3 offset:17408\n" \
"v_smfmac_f32_16x16x64_bf16 v[92:95], v[68:71], v[216:223], v12\n" \
"s_waitcnt lgkmcnt(7)\n" \
"v_smfmac_f32_16x16x64_bf16 v[80:83], v[72:75], a[224:231], v12\n" \
"v_min3_u32 v13, v13, v72, v74\n" \
"v_smfmac_f32_16x16x64_bf16 v[84:87], v[72:75], a[232:239], v12\n" \
"ds_read_b128 v[40:43], v3 offset:18432\n" \
"s_waitcnt lgkmcnt(5)\n" \
"v_smfmac_f32_16x16x64_bf16 v[80:83], v[76:79], a[240:247], v12\n" \
"ds_read_b128 v[44:47], v3 offset:19456\n" \
"v_min3_u32 v13, v13, v76, v78\n" \
"v_smfmac_f32_16x16x64_bf16 v[84:87], v[76:79], a[248:255], v12\n" \
"v_cmp_eq_u32_e64 vcc, 0, v13\n" \
"v_smfmac_f32_16x16x64_bf16 v[88:91], v[72:75], v[224:231], v12\n" \
"v_add_f32_e64 v80, v80, v82\n" \
"v_smfmac_f32_16x16x64_bf16 v[92:95], v[72:75], v[232:239], v12\n" \
"v_add_f32_e64 v81, v81, v83\n" \
"v_smfmac_f32_16x16x64_bf16 v[88:91], v[76:79], v[240:247], v12\n" \
"s_cbranch_vccnz .Lq_retry_%=\n" \
"v_add_f32 v84, v84, v86\n" \
"v_smfmac_f32_16x16x64_bf16 v[92:95], v[76:79], v[248:255], v12\n" \
"v_add_f32_e64 v85, v85, v87\n" \
"v_pk_mul_f32 v[80:81], v[80:81], s[98:99]\n" \
"v_pk_mul_f32 v[84:85], v[84:85], s[98:99]\n" \
"v_cvt_pk_bf16_f32 v80, v80, v81\n" \
"v_cvt_pk_bf16_f32 v84, v84, v85\n" \
"v_pk_add_f32 v[88:89], v[88:89], v[90:91]\n" \
"v_cvt_pk_bf16_f32 v81, v88, v89\n" \
"s_nop 0\n" \
"v_pk_add_f32 v[92:93], v[92:93], v[94:95]\n" \
"v_cvt_pk_bf16_f32 v85, v92, v93\n" \
"ds_write2_b64 v5, v[80:81], v[84:85] offset1:64\n" \
"buffer_load_dword v104, v8, s[36:39], s58 offen offset:0 sc1\n" \
"buffer_load_dword v106, v8, s[36:39], s58 offen offset:256 sc1\n" \
"s_cmp_eq_u32 s49, 0\n" \
"s_nop 0\n" \
"s_nop 0\n" \
"s_cbranch_scc1 .Lq_first_%=\n" \
".Lq_first2_%=:\n" \
"s_cmp_eq_u32 s49, 63\n" \
"s_waitcnt vmcnt(5)\n" \
"s_waitcnt lgkmcnt(0)\n" \
"s_barrier\n" \
"ds_read_b64_tr_b16 v[64:65], v6 offset:0\n" \
"ds_read_b64_tr_b16 v[66:67], v6 offset:512\n" \
"ds_read_b64_tr_b16 v[68:69], v6 offset:1024\n" \
"ds_read_b64_tr_b16 v[70:71], v6 offset:1536\n" \
"ds_read_b64_tr_b16 v[72:73], v6 offset:2048\n" \
"ds_read_b64_tr_b16 v[74:75], v6 offset:2560\n" \
"s_cbranch_scc1 .Lq_last_%=\n" \
"s_waitcnt lgkmcnt(4)\n" \
"v_mfma_f32_16x16x32_bf16 v[88:91], v[64:67], v[64:67], 0\n" \
"ds_read_b64_tr_b16 v[76:77], v6 offset:3072\n" \
"v_mfma_f32_16x16x32_bf16 v[80:83], v[32:35], v[64:67], 0\n" \
"ds_read_b64_tr_b16 v[78:79], v6 offset:3584\n" \
"s_waitcnt lgkmcnt(4)\n" \
"v_mfma_f32_16x16x32_bf16 v[88:91], v[68:71], v[68:71], v[88:91]\n" \
"ds_read_b128 v[96:99], v14 offset:32768\n" \
"s_waitcnt lgkmcnt(3)\n" \
"v_mfma_f32_16x16x32_bf16 v[88:91], v[72:75], v[72:75], v[88:91]\n" \
"ds_read_b128 v[100:103], v14 offset:33792\n" \
"v_mfma_f32_16x16x32_bf16 v[80:83], v[36:39], v[68:71], v[80:83]\n" \
"buffer_store_dword v30, v10, s[36:39], s87 offen sc1\n" \
"s_waitcnt lgkmcnt(2)\n" \
"v_mfma_f32_16x16x32_bf16 v[88:91], v[76:79], v[76:79], v[88:91]\n" \
"buffer_load_dword v30, v15, s[36:39], s80 offen sc1\n" \
"v_mfma_f32_16x16x32_bf16 v[80:83], v[40:43], v[72:75], v[80:83]\n" \
"v_mfma_f32_16x16x32_bf16 v[80:83], v[44:47], v[76:79], v[80:83]\n" \
"v_max_f32 v13, v90, v91\n" \
"v_max_f32 v14, v90, v91\n" \
"v_mfma_f32_16x16x32_bf16 v[84:87], v[48:51], v[64:67], 0\n" \
"s_nop 0\n" \
"v_permlane16_swap_b32 v13, v14\n" \
"v_mfma_f32_16x16x32_bf16 v[84:87], v[52:55], v[68:71], v[84:87]\n" \
"v_max_f32 v13, v13, v14\n" \
"v_max_f32 v14, v13, v14\n" \
"v_mfma_f32_16x16x32_bf16 v[84:87], v[56:59], v[72:75], v[84:87]\n" \
"s_nop 0\n" \
"v_permlane32_swap_b32 v13, v14\n" \
"v_mfma_f32_16x16x32_bf16 v[84:87], v[60:63], v[76:79], v[84:87]\n" \
"v_max_f32 v14, v13, v14\n" \
"v_sub_f32 v80, v80, v14\n" \
"v_sub_f32 v81, v81, v14\n" \
"v_exp_f32 v80, v80\n" \
"v_pk_add_f32 v[82:83], v[82:83], v[14:15] op_sel_hi:[1,0] neg_lo:[0,1] neg_hi:[0,1]\n" \
"v_exp_f32 v81, v81\n" \
"v_exp_f32 v82, v82\n" \
"v_exp_f32_e64 v83, v83\n" \
"v_pk_add_f32 v[84:85], v[84:85], v[14:15] op_sel_hi:[1,0] neg_lo:[0,1] neg_hi:[0,1]\n" \
"v_pk_add_f32 v[86:87], v[86:87], v[14:15] op_sel_hi:[1,0] neg_lo:[0,1] neg_hi:[0,1]\n" \
"v_pk_add_f32 v[90:91], v[90:91], v[14:15] op_sel_hi:[1,0] neg_lo:[0,1] neg_hi:[0,1]\n" \
"v_exp_f32 v84, v84\n" \
"v_exp_f32 v85, v85\n" \
"v_exp_f32 v86, v86\n" \
"v_exp_f32 v87, v87\n" \
"v_cvt_pk_bf16_f32 v80, v80, v81\n" \
"v_cvt_pk_bf16_f32 v81, v82, v83\n" \
"v_cvt_pk_bf16_f32 v82, v84, v85\n" \
"v_cvt_pk_bf16_f32 v83, v86, v87\n" \
"v_exp_f32 v90, v90\n" \
"s_waitcnt lgkmcnt(1)\n" \
"v_mfma_f32_16x16x32_bf16 v[84:87], v[96:99], v[80:83], 0\n" \
"v_exp_f32 v91, v91\n" \
"s_waitcnt lgkmcnt(0)\n" \
"v_mfma_f32_16x16x32_bf16 v[92:95], v[100:103], v[80:83], 0\n" \
"v_cvt_pk_bf16_f32 v26, v90, v91\n" \
"v_mfma_f32_16x16x32_bf16 v[16:19], v[20:23], v[80:83], 0\n" \
"s_nop 0\n" \
"v_mfma_f32_16x16x32_bf16 v[16:19], v[20:23], v[26:29], v[16:19]\n" \
"s_waitcnt vmcnt(2)\n" \
"v_mfma_f32_16x16x32_bf16 v[84:87], v[104:107], v[26:29], v[84:87]\n" \
"v_mfma_f32_16x16x32_bf16 v[92:95], v[104:107], v[24:27], v[92:95]\n" \
"v_min_u32 v13, v104, v106\n" \
"v_cmp_eq_u32 vcc, 0, v13\n" \
"v_rcp_f32_e64 v14, v16\n" \
"v_cmp_nlt_f32_e64 s[26:27], v16, s8\n" \
".Lq_vr2_%=:\n" \
"v_pk_mul_f32 v[84:85], v[84:85], v[14:15] op_sel_hi:[1,0]\n" \
"v_pk_mul_f32 v[86:87], v[86:87], v[14:15] op_sel_hi:[1,0]\n" \
"v_cvt_pk_bf16_f32 v88, v84, v85\n" \
"v_cvt_pk_bf16_f32 v89, v86, v87\n" \
"v_max_u32 v88, 1, v88\n" \
"s_and_b64 s[26:27], s[26:27], s[6:7]\n" \
"s_or_b64 vcc, vcc, s[26:27]\n" \
"s_cbranch_vccnz .Lq_vr_%=\n" \
"v_pk_mul_f32 v[92:93], v[92:93], v[14:15] op_sel_hi:[1,0]\n" \
"v_pk_mul_f32 v[94:95], v[94:95], v[14:15] op_sel_hi:[1,0]\n" \
"v_cvt_pk_bf16_f32 v90, v92, v93\n" \
"v_cvt_pk_bf16_f32 v91, v94, v95\n" \
"v_max_u32_e64 v90, 1, v90\n" \
"buffer_store_dwordx4 v[88:91], v7, s[36:39], s58 offen sc1\n" \
".Lq_tok_%=:\n" \
"s_memrealtime s[24:25]\n" \
"v_add_u32_e64 v14, s43, v3\n" \
"ds_read_b128 v[96:99], v14 offset:24576\n" \
"ds_read_b128 v[100:103], v14 offset:25600\n" \
"ds_read_b128 v[92:95], v14 offset:26624\n" \
"s_memrealtime s[26:27]\n" \
"ds_read_b128 v[16:19], v14 offset:27648\n" \
"v_xor_b32 v0, 512, v8\n" \
"s_nop 2\n" \
"s_memrealtime s[28:29]\n" \
"s_nop 4\n" \
"s_memrealtime s[30:31]\n" \
"s_waitcnt lgkmcnt(0)\n" \
"v_mfma_f32_16x16x32_bf16 v[88:91], v[64:67], v[96:99], 0\n" \
"s_sub_u32 s13, s12, s62\n" \
"s_add_u32 s54, s49, 1\n" \
"s_min_u32 s54, s54, 63\n" \
"s_lshl_b32 s54, s54, 16\n" \
"v_mfma_f32_16x16x32_bf16 v[80:83], v[32:35], v[96:99], 0\n" \
"s_lshr_b32 s53, s54, 8\n" \
"s_and_b32 s53, s53, 0x2000\n" \
"s_xor_b32 s54, s54, s53\n" \
"buffer_load_dword v108, v0, s[36:39], s58 offen offset:0 sc1\n" \
"v_mfma_f32_16x16x32_bf16 v[88:91], v[68:71], v[100:103], v[88:91]\n" \
"s_xor_b32 s12, s54, s63\n" \
"s_add_u32 s12, s12, s62\n" \
"buffer_load_dword v110, v0, s[36:39], s58 offen offset:256 sc1\n" \
"v_mfma_f32_16x16x32_bf16 v[88:91], v[72:75], v[92:95], v[88:91]\n" \
"s_add_u32 m0, s62, 131072\n" \
"s_sub_u32 s15, s12, 114688\n" \
"buffer_load_dwordx4 v3, s[36:39], s15 offen offset:0 sc1 lds\n" \
"v_mfma_f32_16x16x32_bf16 v[80:83], v[36:39], v[100:103], v[80:83]\n" \
"buffer_load_dwordx4 v3, s[36:39], s15 offen offset:1024 sc1 lds\n" \
"v_mfma_f32_16x16x32_bf16 v[88:91], v[76:79], v[16:19], v[88:91]\n" \
"s_add_u32 s25, s24, 3\n" \
"s_cmp_eq_u32 s26, s24\n" \
"s_subb_u32 s25, s25, 0\n" \
"v_mfma_f32_16x16x32_bf16 v[80:83], v[40:43], v[92:95], v[80:83]\n" \
"s_cmp_eq_u32 s28, s24\n" \
"s_subb_u32 s25, s25, 0\n" \
"v_mfma_f32_16x16x32_bf16 v[80:83], v[44:47], v[16:19], v[80:83]\n" \
"s_cmp_eq_u32 s30, s24\n" \
"s_subb_u32 s25, s25, 0\n" \
"s_max_u32 s16, s25, 1\n" \
"v_add_u32 v14, s62, v3\n" \
"v_mfma_f32_16x16x32_bf16 v[84:87], v[48:51], v[96:99], 0\n" \
"s_sub_u32 s24, s25, s3\n" \
"s_min_u32 s24, s24, 255\n" \
"v_xor_b32 v0, 0x800, v14\n" \
"v_mfma_f32_16x16x32_bf16 v[84:87], v[52:55], v[100:103], v[84:87]\n" \
"ds_read_b128 v[32:35], v14 offset:32768\n" \
"ds_read_b128 v[36:39], v14 offset:33792\n" \
"s_cmp_eq_u32 s23, s49\n" \
"s_cselect_b32 s24, s34, s24\n" \
"v_mov_b32 v13, 0xceabdc6e\n" \
"v_mfma_f32_16x16x32_bf16 v[84:87], v[56:59], v[92:95], v[84:87]\n" \
"ds_read_b128 v[40:43], v0 offset:32768\n" \
"ds_read_b128 v[44:47], v0 offset:33792\n" \
"s_min_u32 s34, s24, s32\n" \
"s_max_u32 s25, s24, s32\n" \
"v_mfma_f32_16x16x32_bf16 v[84:87], v[60:63], v[16:19], v[84:87]\n" \
"s_min_u32 s25, s25, s33\n" \
"s_max_u32 s34, s34, s25\n" \
"s_mov_b32 s33, s32\n" \
"s_mov_b32 s32, s24\n" \
"v_cndmask_b32_e64 v80, v13, v80, s[64:65]\n" \
"v_cndmask_b32_e64 v81, v13, v81, s[66:67]\n" \
"v_cndmask_b32_e64 v82, v13, v82, s[68:69]\n" \
"v_cndmask_b32_e64 v83, v13, v83, s[70:71]\n" \
"s_waitcnt vmcnt(5)\n" \
"v_cmp_ge_u32_e64 s[96:97], s81, v30\n" \
"v_readlane_b32 s35, v30, 3\n" \
"v_readlane_b32 s53, v30, 4\n" \
"v_cndmask_b32_e64 v84, v13, v84, s[72:73]\n" \
"v_cndmask_b32_e64 v85, v13, v85, s[74:75]\n" \
"v_cndmask_b32_e64 v86, v13, v86, s[76:77]\n" \
"v_cndmask_b32_e64 v87, v13, v87, s[78:79]\n" \
"s_max_u32 s35, s35, s53\n" \
"s_cmp_eq_u32 s35, 0\n" \
"s_cselect_b32 s35, s17, s35\n" \
"s_and_b32 s96, s96, s9\n" \
"s_cbranch_scc1 .Lq_f_cold_%=\n" \
".Lq_f_ok_%=:\n" \
"s_lshr_b32 s11, s58, 1\n" \
"s_mov_b32 s54, s58\n" \
"s_mov_b32 s58, s59\n" \
"s_mov_b32 s59, s60\n" \
"s_mov_b32 s60, s61\n" \
"s_mov_b32 s61, s54\n" \
"buffer_load_dwordx2 v[64:65], v9, s[36:39], s11 offen sc1\n" \
"v_max3_f32 v13, v90, v91, v80\n" \
"v_max3_f32 v14, v81, v82, v83\n" \
"v_max3_f32 v14, v14, v84, v85\n" \
"v_max3_f32 v13, v13, v86, v87\n" \
"v_max_f32 v13, v13, v14\n" \
"v_max_f32 v14, v13, v14\n" \
"s_nop 1\n" \
"v_permlane16_swap_b32 v13, v14\n" \
"v_max_f32 v13, v13, v14\n" \
"v_max_f32 v14, v13, v14\n" \
"s_nop 1\n" \
"v_permlane32_swap_b32 v13, v14\n" \
"v_max_f32 v14, v13, v14\n" \
"v_pk_add_f32 v[80:81], v[80:81], v[14:15] op_sel_hi:[1,0] neg_lo:[0,1] neg_hi:[0,1]\n" \
"v_pk_add_f32 v[82:83], v[82:83], v[14:15] op_sel_hi:[1,0] neg_lo:[0,1] neg_hi:[0,1]\n" \
"v_pk_add_f32 v[84:85], v[84:85], v[14:15] op_sel_hi:[1,0] neg_lo:[0,1] neg_hi:[0,1]\n" \
"v_pk_add_f32 v[86:87], v[86:87], v[14:15] op_sel_hi:[1,0] neg_lo:[0,1] neg_hi:[0,1]\n" \
"v_pk_add_f32 v[90:91], v[90:91], v[14:15] op_sel_hi:[1,0] neg_lo:[0,1] neg_hi:[0,1]\n" \
"v_exp_f32 v80, v80\n" \
"v_exp_f32 v81, v81\n" \
"v_exp_f32 v82, v82\n" \
"v_exp_f32 v83, v83\n" \
"v_exp_f32 v84, v84\n" \
"v_exp_f32 v85, v85\n" \
"v_exp_f32 v86, v86\n" \
"v_exp_f32 v87, v87\n" \
"v_cvt_pk_bf16_f32 v80, v80, v81\n" \
"v_cvt_pk_bf16_f32 v81, v82, v83\n" \
"v_cvt_pk_bf16_f32 v82, v84, v85\n" \
"v_cvt_pk_bf16_f32 v83, v86, v87\n" \
"v_exp_f32 v90, v90\n" \
"s_waitcnt lgkmcnt(3)\n" \
"v_mfma_f32_16x16x32_bf16 v[48:51], v[32:35], v[80:83], 0\n" \
"v_exp_f32 v91, v91\n" \
"s_waitcnt lgkmcnt(2)\n" \
"v_mfma_f32_16x16x32_bf16 v[52:55], v[36:39], v[80:83], 0\n" \
"v_cvt_pk_bf16_f32 v26, v90, v91\n" \
"s_waitcnt lgkmcnt(1)\n" \
"v_mfma_f32_16x16x32_bf16 v[56:59], v[40:43], v[80:83], 0\n" \
"s_waitcnt lgkmcnt(0)\n" \
"v_mfma_f32_16x16x32_bf16 v[60:63], v[44:47], v[80:83], 0\n" \
"v_mfma_f32_16x16x32_bf16 v[92:95], v[20:23], v[80:83], 0\n" \
"s_waitcnt vmcnt(4)\n" \
"v_min3_u32 v13, v104, v106, v108\n" \
"s_waitcnt vmcnt(3)\n" \
"v_min3_u32 v13, v13, v110, v110\n" \
"v_cmp_eq_u32_e64 vcc, 0, v13\n" \
"s_nop 0\n" \
"s_nop 0\n" \
"s_cbranch_vccnz .Lq_v2_cold_%=\n" \
".Lq_v2_ok_%=:\n" \
"v_mfma_f32_16x16x32_bf16 v[92:95], v[20:23], v[26:29], v[92:95]\n" \
"v_mfma_f32_16x16x32_bf16 v[48:51], v[104:107], v[26:29], v[48:51]\n" \
"ds_read_b128 v[32:35], v2 offset:32768\n" \
"s_mov_b32 s95, s13\n" \
"s_add_u32 s49, s49, 1\n" \
"v_mfma_f32_16x16x32_bf16 v[52:55], v[104:107], v[24:27], v[52:55]\n" \
"ds_read_b128 v[36:39], v2 offset:33792\n" \
"s_lshr_b32 s53, s58, 1\n" \
"s_add_u32 s92, s53, s94\n" \
"v_mfma_f32_16x16x32_bf16 v[56:59], v[108:111], v[26:29], v[56:59]\n" \
"ds_read_b128 v[40:43], v2 offset:34816\n" \
"s_add_u32 s55, s53, s41\n" \
"s_xor_b32 s89, s55, 0x4800\n" \
"v_mfma_f32_16x16x32_bf16 v[60:63], v[108:111], v[24:27], v[60:63]\n" \
"ds_read_b128 v[44:47], v2 offset:35840\n" \
"s_xor_b32 s90, s55, 0x800\n" \
"s_xor_b32 s91, s55, 0x4000\n" \
"s_lshr_b32 s93, s59, 1\n" \
"s_add_u32 s93, s93, s94\n" \
"s_add_u32 s81, s49, 2\n" \
"s_min_u32 s81, s81, 63\n" \
"s_mov_b32 s14, s10\n" \
"v_rcp_f32 v14, v92\n" \
"s_nop 0\n" \
"v_pk_mul_f32 v[48:49], v[48:49], v[14:15] op_sel_hi:[1,0]\n" \
"v_pk_mul_f32 v[50:51], v[50:51], v[14:15] op_sel_hi:[1,0]\n" \
"v_pk_mul_f32 v[52:53], v[52:53], v[14:15] op_sel_hi:[1,0]\n" \
"v_pk_mul_f32 v[54:55], v[54:55], v[14:15] op_sel_hi:[1,0]\n" \
"v_cvt_pk_bf16_f32 v96, v48, v49\n" \
"v_cvt_pk_bf16_f32 v97, v50, v51\n" \
"v_cvt_pk_bf16_f32 v98, v52, v53\n" \
"v_cvt_pk_bf16_f32 v99, v54, v55\n" \
"ds_read_b128 v[48:51], v2 offset:0\n" \
"ds_read_b128 v[52:55], v2 offset:1024\n" \
"v_pk_mul_f32 v[56:57], v[56:57], v[14:15] op_sel_hi:[1,0]\n" \
"v_pk_mul_f32 v[58:59], v[58:59], v[14:15] op_sel_hi:[1,0]\n" \
"v_pk_mul_f32 v[60:61], v[60:61], v[14:15] op_sel_hi:[1,0]\n" \
"v_pk_mul_f32 v[62:63], v[62:63], v[14:15] op_sel_hi:[1,0]\n" \
"v_cvt_pk_bf16_f32 v100, v56, v57\n" \
"v_cvt_pk_bf16_f32 v101, v58, v59\n" \
"v_cvt_pk_bf16_f32 v102, v60, v61\n" \
"v_cvt_pk_bf16_f32 v103, v62, v63\n" \
"ds_read_b128 v[56:59], v2 offset:2048\n" \
"ds_read_b128 v[60:63], v2 offset:3072\n" \
"s_cmp_eq_u32 s49, 64\n" \
"s_nop 0\n" \
"s_nop 0\n" \
"s_cbranch_scc1 .Lq_end_%=\n" \
"v_mov_b64 v[80:81], 0\n" \
"v_mov_b64 v[82:83], 0\n" \
"v_mov_b64 v[84:85], 0\n" \
"v_mov_b64 v[86:87], 0\n" \
"v_mov_b64 v[88:89], 0\n" \
"v_mov_b64 v[90:91], 0\n" \
"v_mov_b64 v[92:93], 0\n" \
"v_mov_b64 v[94:95], 0\n" \
"v_mov_b32 v13, -1\n" \
"v_add_u32_e64 v14, s62, v3\n" \
"v_xor_b32 v31, 0x400, v11\n" \
"s_max_u32 s53, s49, 1\n" \
"s_sub_u32 s53, s53, 1\n" \
"v_mov_b32 v30, s53\n" \
"s_mov_b32 m0, s42\n" \
"buffer_store_dwordx4 v[80:83], v7, s[36:39], s59 offen sc1\n" \
"buffer_store_dword v24, v10, s[36:39], s93 offen sc1\n" \
"s_waitcnt lgkmcnt(0)\n" \
"s_mov_b32 s54, 0\n" \
"s_cmp_lg_u32 s48, 0\n" \
"s_cbranch_scc1 .Lq_w4_%=\n" \
"s_waitcnt vmcnt(2)\n" \
"v_cmp_eq_u32_e64 vcc, 0, v64\n" \
"s_nop 0\n" \
"s_and_b64 vcc, vcc, s[82:83]\n" \
"s_nop 0\n" \
"s_cbranch_vccnz .Lq_h_cold_%=\n" \
".Lq_h_ok_%=:\n" \
"v_subrev_u32 v64, s51, v64\n" \
"v_max_i32_dpp v65, v65, v65 row_ror:8 row_mask:0xf bank_mask:0xf\n" \
"s_nop 0\n" \
"v_max_i32_dpp v64, v64, v64 row_ror:8 row_mask:0xf bank_mask:0xf\n" \
"v_max_i32_dpp v65, v65, v65 row_ror:4 row_mask:0xf bank_mask:0xf\n" \
"s_nop 0\n" \
"v_max_i32_dpp v64, v64, v64 row_ror:4 row_mask:0xf bank_mask:0xf\n" \
"v_max_i32_dpp v65, v65, v65 row_ror:2 row_mask:0xf bank_mask:0xf\n" \
"s_nop 0\n" \
"v_max_i32_dpp v64, v64, v64 row_ror:2 row_mask:0xf bank_mask:0xf\n" \
"v_max_i32_dpp v65, v65, v65 row_ror:1 row_mask:0xf bank_mask:0xf\n" \
"s_nop 0\n" \
"v_max_i32_dpp v64, v64, v64 row_ror:1 row_mask:0xf bank_mask:0xf\n" \
"v_readlane_b32 s53, v65, 0\n" \
"v_readlane_b32 s50, v65, 16\n" \
"v_readlane_b32 s54, v64, 0\n" \
"v_readlane_b32 s40, v64, 16\n" \
"s_max_i32 s50, s50, s53\n" \
"s_max_i32 s40, s40, s54\n" \
"s_add_u32 s40, s40, s51\n" \
"s_add_u32 s40, s40, s44\n" \
"s_add_u32 s50, s50, s40\n" \
"s_add_u32 s50, s50, s45\n" \
"s_branch .Lq_loop_%=\n" \
".Lq_f0_cold_%=:\n" \
"s_nop 7\n" \
"s_nop 7\n" \
"s_waitcnt vmcnt(0) lgkmcnt(0)\n" \
"s_mov_b32 s53, 0\n" \
".Lq_f0_again_%=:\n" \
"s_cmp_lg_u32 s52, 0\n" \
"s_cbranch_scc1 .Lq_f0_ok_%=\n" \
"buffer_load_dword v30, v15, s[36:39], s80 offen sc1\n" \
"s_waitcnt vmcnt(0)\n" \
"v_cmp_ge_u32_e64 s[96:97], s81, v30\n" \
"s_nop 4\n" \
"s_and_b32 s96, s96, s9\n" \
"s_cbranch_scc0 .Lq_f0_ok_%=\n" \
"s_sleep 3\n" \
"s_add_u32 s53, s53, 1\n" \
"s_cmp_lt_u32 s53, 32768\n" \
"s_cbranch_scc1 .Lq_f0_again_%=\n" \
"s_mov_b32 s52, 1\n" \
"v_mov_b32 v30, 256\n" \
"v_mov_b32 v31, 1\n" \
"buffer_store_dword v31, v30, s[36:39], 0 offen sc1\n" \
"s_waitcnt vmcnt(0) lgkmcnt(0)\n" \
"s_branch .Lq_f0_ok_%=\n" \
".Lq_f1_cold_%=:\n" \
"s_nop 7\n" \
"s_nop 7\n" \
"s_waitcnt vmcnt(0) lgkmcnt(0)\n" \
"s_mov_b32 s53, 0\n" \
".Lq_f1_again_%=:\n" \
"s_cmp_lg_u32 s52, 0\n" \
"s_cbranch_scc1 .Lq_f1_ok_%=\n" \
"buffer_load_dword v30, v15, s[36:39], s80 offen sc1\n" \
"s_waitcnt vmcnt(0)\n" \
"v_cmp_ge_u32_e64 s[96:97], s81, v30\n" \
"s_nop 4\n" \
"s_and_b32 s96, s96, s9\n" \
"s_cbranch_scc0 .Lq_f1_ok_%=\n" \
"s_sleep 3\n" \
"s_add_u32 s53, s53, 1\n" \
"s_cmp_lt_u32 s53, 32768\n" \
"s_cbranch_scc1 .Lq_f1_again_%=\n" \
"s_mov_b32 s52, 1\n" \
"v_mov_b32 v30, 256\n" \
"v_mov_b32 v31, 1\n" \
"buffer_store_dword v31, v30, s[36:39], 0 offen sc1\n" \
"s_waitcnt vmcnt(0) lgkmcnt(0)\n" \
"s_branch .Lq_f1_ok_%=\n" \
".Lq_first_%=:\n" \
"s_mov_b32 s81, 1\n" \
"s_mov_b32 s96, -1\n" \
"s_and_b32 s96, s96, s9\n" \
"s_cbranch_scc1 .Lq_f1_cold_%=\n" \
".Lq_f1_ok_%=:\n" \
"s_mov_b32 s81, 2\n" \
"v_mov_b32 v30, 0\n" \
"s_branch .Lq_first2_%=\n" \
".Lq_last_%=:\n" \
"ds_read_b64_tr_b16 v[76:77], v6 offset:3072\n" \
"ds_read_b64_tr_b16 v[78:79], v6 offset:3584\n" \
"ds_read_b128 v[96:99], v14 offset:32768\n" \
"ds_read_b128 v[100:103], v14 offset:33792\n" \
"buffer_store_dword v30, v10, s[36:39], s87 offen sc1\n" \
"buffer_load_dword v30, v15, s[36:39], s80 offen sc1\n" \
"s_waitcnt vmcnt(0) lgkmcnt(0)\n" \
"s_barrier\n" \
"v_mov_b32 v13, 63\n" \
"buffer_store_dword v13, v10, s[36:39], s87 offen sc1\n" \
"s_nop 7\n" \
"s_nop 7\n" \
"s_branch .Lq_tok_%=\n" \
".Lq_vr_%=:\n" \
"s_nop 7\n" \
"s_nop 7\n" \
"s_waitcnt vmcnt(0) lgkmcnt(0)\n" \
"v_min3_u32 v13, v104, v106, v106\n" \
"v_cmp_eq_u32 vcc, 0, v13\n" \
"s_nop 0\n" \
"s_cbranch_vccnz .Lq_v_cold_%=\n" \
".Lq_v_ok_%=:\n" \
"s_mov_b32 s23, s49\n" \
"s_cmp_eq_u64 s[26:27], 0\n" \
"s_cbranch_scc1 .Lq_vr3_%=\n" \
"v_mfma_f32_16x16x32_bf16 v[88:91], v[64:67], v[64:67], 0\n" \
"v_mfma_f32_16x16x32_bf16 v[80:83], v[32:35], v[64:67], 0\n" \
"v_mfma_f32_16x16x32_bf16 v[88:91], v[68:71], v[68:71], v[88:91]\n" \
"v_mfma_f32_16x16x32_bf16 v[88:91], v[72:75], v[72:75], v[88:91]\n" \
"v_mfma_f32_16x16x32_bf16 v[80:83], v[36:39], v[68:71], v[80:83]\n" \
"v_mfma_f32_16x16x32_bf16 v[88:91], v[76:79], v[76:79], v[88:91]\n" \
"v_mfma_f32_16x16x32_bf16 v[80:83], v[40:43], v[72:75], v[80:83]\n" \
"v_mfma_f32_16x16x32_bf16 v[80:83], v[44:47], v[76:79], v[80:83]\n" \
"v_mfma_f32_16x16x32_bf16 v[84:87], v[48:51], v[64:67], 0\n" \
"v_mfma_f32_16x16x32_bf16 v[84:87], v[52:55], v[68:71], v[84:87]\n" \
"v_mfma_f32_16x16x32_bf16 v[84:87], v[56:59], v[72:75], v[84:87]\n" \
"v_max3_f32 v13, v90, v91, v80\n" \
"v_mfma_f32_16x16x32_bf16 v[84:87], v[60:63], v[76:79], v[84:87]\n" \
"v_max3_f32 v14, v81, v82, v83\n" \
"s_nop 6\n" \
"v_max3_f32 v14, v14, v84, v85\n" \
"v_max3_f32 v13, v13, v86, v87\n" \
"v_max_f32 v13, v13, v14\n" \
"v_max_f32 v14, v13, v14\n" \
"s_nop 1\n" \
"v_permlane16_swap_b32 v13, v14\n" \
"v_max_f32 v13, v13, v14\n" \
"v_max_f32 v14, v13, v14\n" \
"s_nop 1\n" \
"v_permlane32_swap_b32 v13, v14\n" \
"v_max_f32 v14, v13, v14\n" \
"v_pk_add_f32 v[80:81], v[80:81], v[14:15] op_sel_hi:[1,0] neg_lo:[0,1] neg_hi:[0,1]\n" \
"v_pk_add_f32 v[82:83], v[82:83], v[14:15] op_sel_hi:[1,0] neg_lo:[0,1] neg_hi:[0,1]\n" \
"v_pk_add_f32 v[84:85], v[84:85], v[14:15] op_sel_hi:[1,0] neg_lo:[0,1] neg_hi:[0,1]\n" \
"v_pk_add_f32 v[86:87], v[86:87], v[14:15] op_sel_hi:[1,0] neg_lo:[0,1] neg_hi:[0,1]\n" \
"v_pk_add_f32 v[90:91], v[90:91], v[14:15] op_sel_hi:[1,0] neg_lo:[0,1] neg_hi:[0,1]\n" \
"v_exp_f32 v80, v80\n" \
"v_exp_f32 v81, v81\n" \
"v_exp_f32 v82, v82\n" \
"v_exp_f32 v83, v83\n" \
"v_exp_f32 v84, v84\n" \
"v_exp_f32 v85, v85\n" \
"v_exp_f32 v86, v86\n" \
"v_exp_f32 v87, v87\n" \
"v_cvt_pk_bf16_f32 v80, v80, v81\n" \
"v_cvt_pk_bf16_f32 v81, v82, v83\n" \
"v_cvt_pk_bf16_f32 v82, v84, v85\n" \
"v_cvt_pk_bf16_f32 v83, v86, v87\n" \
"v_exp_f32 v90, v90\n" \
"s_nop 0\n" \
"v_mfma_f32_16x16x32_bf16 v[84:87], v[96:99], v[80:83], 0\n" \
"v_exp_f32 v91, v91\n" \
"v_mfma_f32_16x16x32_bf16 v[92:95], v[100:103], v[80:83], 0\n" \
"v_cvt_pk_bf16_f32 v26, v90, v91\n" \
"v_mfma_f32_16x16x32_bf16 v[16:19], v[20:23], v[80:83], 0\n" \
".Lq_vr3_%=:\n" \
"s_nop 7\n" \
"s_nop 7\n" \
"v_mfma_f32_16x16x32_bf16 v[84:87], v[96:99], v[80:83], 0\n" \
"v_mfma_f32_16x16x32_bf16 v[92:95], v[100:103], v[80:83], 0\n" \
"v_mfma_f32_16x16x32_bf16 v[16:19], v[20:23], v[80:83], 0\n" \
"v_mfma_f32_16x16x32_bf16 v[16:19], v[20:23], v[26:29], v[16:19]\n" \
"v_mfma_f32_16x16x32_bf16 v[84:87], v[104:107], v[26:29], v[84:87]\n" \
"v_mfma_f32_16x16x32_bf16 v[92:95], v[104:107], v[24:27], v[92:95]\n" \
"v_rcp_f32 v14, v16\n" \
"s_mov_b64 vcc, 0\n" \
"s_mov_b64 s[26:27], 0\n" \
"s_nop 7\n" \
"s_nop 7\n" \
"s_branch .Lq_vr2_%=\n" \
".Lq_v_cold_%=:\n" \
"s_nop 7\n" \
"s_nop 7\n" \
"s_waitcnt vmcnt(0) lgkmcnt(0)\n" \
"s_mov_b32 s53, 0\n" \
".Lq_v_again_%=:\n" \
"s_cmp_lg_u32 s52, 0\n" \
"s_cbranch_scc1 .Lq_v_ok_%=\n" \
"buffer_load_dword v104, v8, s[36:39], s58 offen offset:0 sc1\n" \
"buffer_load_dword v106, v8, s[36:39], s58 offen offset:256 sc1\n" \
"s_waitcnt vmcnt(0)\n" \
"v_min3_u32 v13, v104, v106, v106\n" \
"v_cmp_eq_u32 vcc, 0, v13\n" \
"s_nop 4\n" \
"s_cbranch_vccz .Lq_v_ok_%=\n" \
"s_add_u32 s53, s53, 1\n" \
"s_cmp_lt_u32 s53, 32768\n" \
"s_cbranch_scc1 .Lq_v_again_%=\n" \
"s_mov_b32 s52, 1\n" \
"v_mov_b32 v13, 256\n" \
"v_mov_b32 v14, 1\n" \
"buffer_store_dword v14, v13, s[36:39], 0 offen sc1\n" \
"s_waitcnt vmcnt(0) lgkmcnt(0)\n" \
"s_branch .Lq_v_ok_%=\n" \
".Lq_f_cold_%=:\n" \
"s_nop 7\n" \
"s_nop 7\n" \
"s_waitcnt vmcnt(0) lgkmcnt(0)\n" \
"s_mov_b32 s53, 0\n" \
".Lq_f_again_%=:\n" \
"s_cmp_lg_u32 s52, 0\n" \
"s_cbranch_scc1 .Lq_f_ok_%=\n" \
"buffer_load_dword v30, v15, s[36:39], s80 offen sc1\n" \
"s_waitcnt vmcnt(0)\n" \
"v_cmp_ge_u32_e64 s[96:97], s81, v30\n" \
"s_nop 4\n" \
"s_and_b32 s96, s96, s9\n" \
"s_cbranch_scc0 .Lq_f_ok_%=\n" \
"s_sleep 3\n" \
"s_add_u32 s53, s53, 1\n" \
"s_cmp_lt_u32 s53, 32768\n" \
"s_cbranch_scc1 .Lq_f_again_%=\n" \
"s_mov_b32 s52, 1\n" \
"v_mov_b32 v30, 256\n" \
"v_mov_b32 v31, 1\n" \
"buffer_store_dword v31, v30, s[36:39], 0 offen sc1\n" \
"s_waitcnt vmcnt(0) lgkmcnt(0)\n" \
"s_branch .Lq_f_ok_%=\n" \
".Lq_v2_cold_%=:\n" \
"s_nop 7\n" \
"s_nop 7\n" \
"s_waitcnt vmcnt(0) lgkmcnt(0)\n" \
"s_mov_b32 s53, 0\n" \
".Lq_v2_again_%=:\n" \
"s_cmp_lg_u32 s52, 0\n" \
"s_cbranch_scc1 .Lq_v2_ok_%=\n" \
"s_sleep 1\n" \
"v_xor_b32 v31, 512, v8\n" \
"buffer_load_dword v104, v8, s[36:39], s61 offen offset:0 sc1\n" \
"buffer_load_dword v106, v8, s[36:39], s61 offen offset:256 sc1\n" \
"buffer_load_dword v108, v31, s[36:39], s61 offen offset:0 sc1\n" \
"buffer_load_dword v110, v31, s[36:39], s61 offen offset:256 sc1\n" \
"s_waitcnt vmcnt(1)\n" \
"v_min3_u32 v13, v104, v106, v108\n" \
"s_waitcnt vmcnt(0)\n" \
"v_min3_u32 v13, v13, v110, v110\n" \
"v_cmp_eq_u32 vcc, 0, v13\n" \
"s_nop 4\n" \
"s_cbranch_vccz .Lq_v2_ok_%=\n" \
"s_add_u32 s53, s53, 1\n" \
"s_cmp_lt_u32 s53, 32768\n" \
"s_cbranch_scc1 .Lq_v2_again_%=\n" \
"s_mov_b32 s52, 1\n" \
"v_mov_b32 v13, 256\n" \
"v_mov_b32 v14, 1\n" \
"buffer_store_dword v14, v13, s[36:39], 0 offen sc1\n" \
"s_waitcnt vmcnt(0) lgkmcnt(0)\n" \
"s_branch .Lq_v2_ok_%=\n" \
".Lq_h_cold_%=:\n" \
"s_nop 7\n" \
"s_nop 7\n" \
"s_waitcnt vmcnt(0) lgkmcnt(0)\n" \
"s_mov_b32 s53, 0\n" \
".Lq_h_again_%=:\n" \
"s_cmp_lg_u32 s52, 0\n" \
"s_cbranch_scc1 .Lq_h_ok_%=\n" \
"s_sleep 1\n" \
"buffer_load_dwordx2 v[64:65], v9, s[36:39], s11 offen sc1\n" \
"s_waitcnt vmcnt(0)\n" \
"v_cmp_eq_u32 vcc, 0, v64\n" \
"s_nop 0\n" \
"s_and_b64 vcc, vcc, s[82:83]\n" \
"s_nop 4\n" \
"s_cbranch_vccz .Lq_h_ok_%=\n" \
"s_add_u32 s53, s53, 1\n" \
"s_cmp_lt_u32 s53, 32768\n" \
"s_cbranch_scc1 .Lq_h_again_%=\n" \
"s_mov_b32 s52, 1\n" \
"v_mov_b32 v18, 256\n" \
"v_mov_b32 v19, 1\n" \
"buffer_store_dword v19, v18, s[36:39], 0 offen sc1\n" \
"s_waitcnt vmcnt(0) lgkmcnt(0)\n" \
"s_branch .Lq_h_ok_%=\n" \
".Lq_retry_%=:\n" \
"s_nop 7\n" \
"s_nop 7\n" \
"s_waitcnt vmcnt(0) lgkmcnt(0)\n" \
"ds_read_b128 v[32:35], v2 offset:32768\n" \
"ds_read_b128 v[36:39], v2 offset:33792\n" \
"ds_read_b128 v[40:43], v2 offset:34816\n" \
"ds_read_b128 v[44:47], v2 offset:35840\n" \
"ds_read_b128 v[48:51], v2 offset:0\n" \
"ds_read_b128 v[52:55], v2 offset:1024\n" \
"ds_read_b128 v[56:59], v2 offset:2048\n" \
"ds_read_b128 v[60:63], v2 offset:3072\n" \
"v_mov_b64 v[80:81], 0\n" \
"v_mov_b64 v[82:83], 0\n" \
"v_mov_b64 v[84:85], 0\n" \
"v_mov_b64 v[86:87], 0\n" \
"v_mov_b64 v[88:89], 0\n" \
"v_mov_b64 v[90:91], 0\n" \
"v_mov_b64 v[92:93], 0\n" \
"v_mov_b64 v[94:95], 0\n" \
"v_mov_b32 v13, -1\n" \
"v_add_u32 v14, s62, v3\n" \
"v_xor_b32 v31, 0x400, v11\n" \
"s_max_u32 s53, s49, 1\n" \
"s_sub_u32 s53, s53, 1\n" \
"v_mov_b32 v30, s53\n" \
"s_mov_b32 m0, s42\n" \
"s_memrealtime s[56:57]\n" \
"s_mov_b32 s54, 100\n" \
"s_waitcnt vmcnt(0) lgkmcnt(0)\n" \
"s_barrier\n" \
"s_add_u32 s88, s88, 1\n" \
"s_cmp_lt_u32 s88, 32768\n" \
"s_cbranch_scc1 .Lq_r1_%=\n" \
"s_mov_b32 s52, 1\n" \
"v_mov_b32 v13, 256\n" \
"v_mov_b32 v14, 1\n" \
"buffer_store_dword v14, v13, s[36:39], 0 offen sc1\n" \
"s_waitcnt vmcnt(0) lgkmcnt(0)\n" \
"s_endpgm\n" \
".Lq_r1_%=:\n" \
"s_cmp_lg_u32 s49, 0\n" \
"s_cbranch_scc1 .Lq_r2_%=\n" \
"s_sleep 8\n" \
".Lq_r2_%=:\n" \
"s_branch .Lq_go_%=\n" \
".Lq_end_%=:\n" \
"s_nop 7\n" \
"s_nop 7\n" \
"v_xor_b32 v31, 0x400, v11\n" \
"buffer_store_dwordx4 v[96:99], v11, s[36:39], s95 offen sc1\n" \
"buffer_store_dwordx4 v[100:103], v31, s[36:39], s95 offen sc1\n" \
"s_waitcnt vmcnt(0) lgkmcnt(0)\n" \
"s_barrier\n" \
"v_mov_b32 v31, 64\n" \
"buffer_store_dword v31, v10, s[36:39], s87 offen sc1\n" \
"s_waitcnt vmcnt(0) lgkmcnt(0)\n" \
".Lq_fin_%=:\n" \
"s_endpgm\n"
#define ASM_VO \
"s_nop 7\n" \
"s_nop 7\n" \
"buffer_load_dword v20, v13, s[68:71], 0 offen offset:0\n" \
"buffer_load_dword v21, v13, s[68:71], 0 offen offset:8\n" \
"buffer_load_dword v22, v13, s[68:71], 0 offen offset:16\n" \
"buffer_load_dword v23, v13, s[68:71], 0 offen offset:24\n" \
"v_mov_b32 v16, 0\n" \
"v_mov_b32 v17, 0\n" \
"v_mov_b32 v18, 0\n" \
"v_mov_b32 v19, 0\n" \
"s_mov_b32 s54, 0\n" \
"buffer_store_dword v16, v7, s[36:39], s54 offen sc1\n" \
"s_mov_b32 s54, 131072\n" \
"buffer_store_dword v16, v7, s[36:39], s54 offen sc1\n" \
"s_mov_b32 s54, 65536\n" \
"buffer_store_dwordx4 v[16:19], v8, s[36:39], s54 offen sc1\n" \
"s_mov_b32 s54, 262144\n" \
"buffer_store_dword v16, v7, s[36:39], s54 offen sc1\n" \
"s_mov_b32 s54, 131072\n" \
"buffer_store_dwordx4 v[16:19], v8, s[36:39], s54 offen sc1\n" \
"s_mov_b32 s54, 393216\n" \
"buffer_store_dword v16, v7, s[36:39], s54 offen sc1\n" \
"s_mov_b32 s54, 196608\n" \
"buffer_store_dwordx4 v[16:19], v8, s[36:39], s54 offen sc1\n" \
"s_mul_i32 s62, s48, 40960\n" \
"v_add_u32 v32, 40960, v3\n" \
"v_add_u32 v33, 81920, v3\n" \
"v_add_u32 v34, 122880, v3\n" \
"s_add_u32 m0, s62, 0\n" \
"s_mov_b32 s54, 0\n" \
"buffer_load_dwordx4 v1, s[40:43], s54 offen nt lds\n" \
"s_add_u32 m0, s62, 1024\n" \
"s_mov_b32 s54, 65536\n" \
"buffer_load_dwordx4 v1, s[40:43], s54 offen nt lds\n" \
"s_add_u32 m0, s62, 2048\n" \
"s_mov_b32 s54, 131072\n" \
"buffer_load_dwordx4 v1, s[40:43], s54 offen nt lds\n" \
"s_add_u32 m0, s62, 3072\n" \
"s_mov_b32 s54, 196608\n" \
"buffer_load_dwordx4 v1, s[40:43], s54 offen nt lds\n" \
"s_add_u32 m0, s62, 4096\n" \
"s_mov_b32 s54, 262144\n" \
"buffer_load_dwordx4 v1, s[40:43], s54 offen nt lds\n" \
"s_add_u32 m0, s62, 5120\n" \
"s_mov_b32 s54, 327680\n" \
"buffer_load_dwordx4 v1, s[40:43], s54 offen nt lds\n" \
"s_add_u32 m0, s62, 6144\n" \
"s_mov_b32 s54, 393216\n" \
"buffer_load_dwordx4 v1, s[40:43], s54 offen nt lds\n" \
"s_add_u32 m0, s62, 7168\n" \
"s_mov_b32 s54, 458752\n" \
"buffer_load_dwordx4 v1, s[40:43], s54 offen nt lds\n" \
"s_add_u32 m0, s62, 8192\n" \
"s_mov_b32 s54, 524288\n" \
"buffer_load_dwordx4 v1, s[40:43], s54 offen nt lds\n" \
"s_add_u32 m0, s62, 9216\n" \
"s_mov_b32 s54, 589824\n" \
"buffer_load_dwordx4 v1, s[40:43], s54 offen nt lds\n" \
"s_add_u32 m0, s62, 10240\n" \
"s_mov_b32 s54, 655360\n" \
"buffer_load_dwordx4 v1, s[40:43], s54 offen nt lds\n" \
"s_add_u32 m0, s62, 11264\n" \
"s_mov_b32 s54, 720896\n" \
"buffer_load_dwordx4 v1, s[40:43], s54 offen nt lds\n" \
"s_add_u32 m0, s62, 12288\n" \
"s_mov_b32 s54, 786432\n" \
"buffer_load_dwordx4 v1, s[40:43], s54 offen nt lds\n" \
"s_add_u32 m0, s62, 13312\n" \
"s_mov_b32 s54, 851968\n" \
"buffer_load_dwordx4 v1, s[40:43], s54 offen nt lds\n" \
"s_add_u32 m0, s62, 14336\n" \
"s_mov_b32 s54, 917504\n" \
"buffer_load_dwordx4 v1, s[40:43], s54 offen nt lds\n" \
"s_add_u32 m0, s62, 15360\n" \
"s_mov_b32 s54, 983040\n" \
"buffer_load_dwordx4 v1, s[40:43], s54 offen nt lds\n" \
"s_add_u32 m0, s62, 16384\n" \
"s_mov_b32 s54, 1048576\n" \
"buffer_load_dwordx4 v1, s[40:43], s54 offen nt lds\n" \
"s_add_u32 m0, s62, 17408\n" \
"s_mov_b32 s54, 1114112\n" \
"buffer_load_dwordx4 v1, s[40:43], s54 offen nt lds\n" \
"s_add_u32 m0, s62, 18432\n" \
"s_mov_b32 s54, 1179648\n" \
"buffer_load_dwordx4 v1, s[40:43], s54 offen nt lds\n" \
"s_add_u32 m0, s62, 19456\n" \
"s_mov_b32 s54, 1245184\n" \
"buffer_load_dwordx4 v1, s[40:43], s54 offen nt lds\n" \
"s_add_u32 m0, s62, 20480\n" \
"s_mov_b32 s54, 1310720\n" \
"buffer_load_dwordx4 v1, s[40:43], s54 offen nt lds\n" \
"s_add_u32 m0, s62, 21504\n" \
"s_mov_b32 s54, 1376256\n" \
"buffer_load_dwordx4 v1, s[40:43], s54 offen nt lds\n" \
"s_add_u32 m0, s62, 22528\n" \
"s_mov_b32 s54, 1441792\n" \
"buffer_load_dwordx4 v1, s[40:43], s54 offen nt lds\n" \
"s_add_u32 m0, s62, 23552\n" \
"s_mov_b32 s54, 1507328\n" \
"buffer_load_dwordx4 v1, s[40:43], s54 offen nt lds\n" \
"s_add_u32 m0, s62, 24576\n" \
"s_mov_b32 s54, 1572864\n" \
"buffer_load_dwordx4 v1, s[40:43], s54 offen nt lds\n" \
"s_add_u32 m0, s62, 25600\n" \
"s_mov_b32 s54, 1638400\n" \
"buffer_load_dwordx4 v1, s[40:43], s54 offen nt lds\n" \
"s_add_u32 m0, s62, 26624\n" \
"s_mov_b32 s54, 1703936\n" \
"buffer_load_dwordx4 v1, s[40:43], s54 offen nt lds\n" \
"s_add_u32 m0, s62, 27648\n" \
"s_mov_b32 s54, 1769472\n" \
"buffer_load_dwordx4 v1, s[40:43], s54 offen nt lds\n" \
"s_add_u32 m0, s62, 28672\n" \
"s_mov_b32 s54, 1835008\n" \
"buffer_load_dwordx4 v1, s[40:43], s54 offen nt lds\n" \
"s_add_u32 m0, s62, 29696\n" \
"s_mov_b32 s54, 1900544\n" \
"buffer_load_dwordx4 v1, s[40:43], s54 offen nt lds\n" \
"s_add_u32 m0, s62, 30720\n" \
"s_mov_b32 s54, 1966080\n" \
"buffer_load_dwordx4 v1, s[40:43], s54 offen nt lds\n" \
"s_add_u32 m0, s62, 31744\n" \
"s_mov_b32 s54, 2031616\n" \
"buffer_load_dwordx4 v1, s[40:43], s54 offen nt lds\n" \
"s_add_u32 m0, s62, 32768\n" \
"s_mov_b32 s54, 0\n" \
"buffer_load_dwordx4 v2, s[64:67], s54 offen lds\n" \
"s_add_u32 m0, s62, 33792\n" \
"s_mov_b32 s54, 32768\n" \
"buffer_load_dwordx4 v2, s[64:67], s54 offen lds\n" \
"s_add_u32 m0, s62, 34816\n" \
"s_mov_b32 s54, 65536\n" \
"buffer_load_dwordx4 v2, s[64:67], s54 offen lds\n" \
"s_add_u32 m0, s62, 35840\n" \
"s_mov_b32 s54, 98304\n" \
"buffer_load_dwordx4 v2, s[64:67], s54 offen lds\n" \
"s_add_u32 m0, s62, 36864\n" \
"s_mov_b32 s54, 131072\n" \
"buffer_load_dwordx4 v2, s[64:67], s54 offen lds\n" \
"s_add_u32 m0, s62, 37888\n" \
"s_mov_b32 s54, 163840\n" \
"buffer_load_dwordx4 v2, s[64:67], s54 offen lds\n" \
"s_add_u32 m0, s62, 38912\n" \
"s_mov_b32 s54, 196608\n" \
"buffer_load_dwordx4 v2, s[64:67], s54 offen lds\n" \
"s_add_u32 m0, s62, 39936\n" \
"s_mov_b32 s54, 229376\n" \
"buffer_load_dwordx4 v2, s[64:67], s54 offen lds\n" \
"s_waitcnt vmcnt(32)\n" \
"s_barrier\n" \
"ds_read_b64_tr_b16 v[128:129], v3 offset:0\n" \
"ds_read_b64_tr_b16 v[130:131], v3 offset:512\n" \
"ds_read_b64_tr_b16 v[132:133], v3 offset:4096\n" \
"ds_read_b64_tr_b16 v[134:135], v3 offset:4608\n" \
"ds_read_b64_tr_b16 v[160:161], v32 offset:0\n" \
"ds_read_b64_tr_b16 v[162:163], v32 offset:512\n" \
"ds_read_b64_tr_b16 v[164:165], v32 offset:4096\n" \
"ds_read_b64_tr_b16 v[166:167], v32 offset:4608\n" \
"ds_read_b64_tr_b16 v[192:193], v33 offset:0\n" \
"ds_read_b64_tr_b16 v[194:195], v33 offset:512\n" \
"ds_read_b64_tr_b16 v[196:197], v33 offset:4096\n" \
"ds_read_b64_tr_b16 v[198:199], v33 offset:4608\n" \
"ds_read_b64_tr_b16 v[224:225], v34 offset:0\n" \
"ds_read_b64_tr_b16 v[226:227], v34 offset:512\n" \
"ds_read_b64_tr_b16 v[228:229], v34 offset:4096\n" \
"ds_read_b64_tr_b16 v[230:231], v34 offset:4608\n" \
"s_waitcnt lgkmcnt(0)\n" \
"s_barrier\n" \
"s_add_u32 m0, s62, 0\n" \
"s_mov_b32 s54, 262144\n" \
"buffer_load_dwordx4 v2, s[64:67], s54 offen lds\n" \
"s_add_u32 m0, s62, 1024\n" \
"s_mov_b32 s54, 294912\n" \
"buffer_load_dwordx4 v2, s[64:67], s54 offen lds\n" \
"s_add_u32 m0, s62, 2048\n" \
"s_mov_b32 s54, 327680\n" \
"buffer_load_dwordx4 v2, s[64:67], s54 offen lds\n" \
"s_add_u32 m0, s62, 3072\n" \
"s_mov_b32 s54, 360448\n" \
"buffer_load_dwordx4 v2, s[64:67], s54 offen lds\n" \
"s_add_u32 m0, s62, 4096\n" \
"s_mov_b32 s54, 393216\n" \
"buffer_load_dwordx4 v2, s[64:67], s54 offen lds\n" \
"s_add_u32 m0, s62, 5120\n" \
"s_mov_b32 s54, 425984\n" \
"buffer_load_dwordx4 v2, s[64:67], s54 offen lds\n" \
"s_add_u32 m0, s62, 6144\n" \
"s_mov_b32 s54, 458752\n" \
"buffer_load_dwordx4 v2, s[64:67], s54 offen lds\n" \
"s_add_u32 m0, s62, 7168\n" \
"s_mov_b32 s54, 491520\n" \
"buffer_load_dwordx4 v2, s[64:67], s54 offen lds\n" \
"s_waitcnt vmcnt(32)\n" \
"s_barrier\n" \
"ds_read_b64_tr_b16 v[136:137], v3 offset:8192\n" \
"ds_read_b64_tr_b16 v[138:139], v3 offset:8704\n" \
"ds_read_b64_tr_b16 v[140:141], v3 offset:12288\n" \
"ds_read_b64_tr_b16 v[142:143], v3 offset:12800\n" \
"ds_read_b64_tr_b16 v[168:169], v32 offset:8192\n" \
"ds_read_b64_tr_b16 v[170:171], v32 offset:8704\n" \
"ds_read_b64_tr_b16 v[172:173], v32 offset:12288\n" \
"ds_read_b64_tr_b16 v[174:175], v32 offset:12800\n" \
"ds_read_b64_tr_b16 v[200:201], v33 offset:8192\n" \
"ds_read_b64_tr_b16 v[202:203], v33 offset:8704\n" \
"ds_read_b64_tr_b16 v[204:205], v33 offset:12288\n" \
"ds_read_b64_tr_b16 v[206:207], v33 offset:12800\n" \
"ds_read_b64_tr_b16 v[232:233], v34 offset:8192\n" \
"ds_read_b64_tr_b16 v[234:235], v34 offset:8704\n" \
"ds_read_b64_tr_b16 v[236:237], v34 offset:12288\n" \
"ds_read_b64_tr_b16 v[238:239], v34 offset:12800\n" \
"s_waitcnt lgkmcnt(0)\n" \
"s_barrier\n" \
"s_add_u32 m0, s62, 8192\n" \
"s_mov_b32 s54, 524288\n" \
"buffer_load_dwordx4 v2, s[64:67], s54 offen lds\n" \
"s_add_u32 m0, s62, 9216\n" \
"s_mov_b32 s54, 557056\n" \
"buffer_load_dwordx4 v2, s[64:67], s54 offen lds\n" \
"s_add_u32 m0, s62, 10240\n" \
"s_mov_b32 s54, 589824\n" \
"buffer_load_dwordx4 v2, s[64:67], s54 offen lds\n" \
"s_add_u32 m0, s62, 11264\n" \
"s_mov_b32 s54, 622592\n" \
"buffer_load_dwordx4 v2, s[64:67], s54 offen lds\n" \
"s_add_u32 m0, s62, 12288\n" \
"s_mov_b32 s54, 655360\n" \
"buffer_load_dwordx4 v2, s[64:67], s54 offen lds\n" \
"s_add_u32 m0, s62, 13312\n" \
"s_mov_b32 s54, 688128\n" \
"buffer_load_dwordx4 v2, s[64:67], s54 offen lds\n" \
"s_add_u32 m0, s62, 14336\n" \
"s_mov_b32 s54, 720896\n" \
"buffer_load_dwordx4 v2, s[64:67], s54 offen lds\n" \
"s_add_u32 m0, s62, 15360\n" \
"s_mov_b32 s54, 753664\n" \
"buffer_load_dwordx4 v2, s[64:67], s54 offen lds\n" \
"s_waitcnt vmcnt(32)\n" \
"s_barrier\n" \
"ds_read_b64_tr_b16 v[144:145], v3 offset:16384\n" \
"ds_read_b64_tr_b16 v[146:147], v3 offset:16896\n" \
"ds_read_b64_tr_b16 v[148:149], v3 offset:20480\n" \
"ds_read_b64_tr_b16 v[150:151], v3 offset:20992\n" \
"ds_read_b64_tr_b16 v[176:177], v32 offset:16384\n" \
"ds_read_b64_tr_b16 v[178:179], v32 offset:16896\n" \
"ds_read_b64_tr_b16 v[180:181], v32 offset:20480\n" \
"ds_read_b64_tr_b16 v[182:183], v32 offset:20992\n" \
"ds_read_b64_tr_b16 v[208:209], v33 offset:16384\n" \
"ds_read_b64_tr_b16 v[210:211], v33 offset:16896\n" \
"ds_read_b64_tr_b16 v[212:213], v33 offset:20480\n" \
"ds_read_b64_tr_b16 v[214:215], v33 offset:20992\n" \
"ds_read_b64_tr_b16 v[240:241], v34 offset:16384\n" \
"ds_read_b64_tr_b16 v[242:243], v34 offset:16896\n" \
"ds_read_b64_tr_b16 v[244:245], v34 offset:20480\n" \
"ds_read_b64_tr_b16 v[246:247], v34 offset:20992\n" \
"s_waitcnt lgkmcnt(0)\n" \
"s_barrier\n" \
"s_add_u32 m0, s62, 16384\n" \
"s_mov_b32 s54, 786432\n" \
"buffer_load_dwordx4 v2, s[64:67], s54 offen lds\n" \
"s_add_u32 m0, s62, 17408\n" \
"s_mov_b32 s54, 819200\n" \
"buffer_load_dwordx4 v2, s[64:67], s54 offen lds\n" \
"s_add_u32 m0, s62, 18432\n" \
"s_mov_b32 s54, 851968\n" \
"buffer_load_dwordx4 v2, s[64:67], s54 offen lds\n" \
"s_add_u32 m0, s62, 19456\n" \
"s_mov_b32 s54, 884736\n" \
"buffer_load_dwordx4 v2, s[64:67], s54 offen lds\n" \
"s_add_u32 m0, s62, 20480\n" \
"s_mov_b32 s54, 917504\n" \
"buffer_load_dwordx4 v2, s[64:67], s54 offen lds\n" \
"s_add_u32 m0, s62, 21504\n" \
"s_mov_b32 s54, 950272\n" \
"buffer_load_dwordx4 v2, s[64:67], s54 offen lds\n" \
"s_add_u32 m0, s62, 22528\n" \
"s_mov_b32 s54, 983040\n" \
"buffer_load_dwordx4 v2, s[64:67], s54 offen lds\n" \
"s_add_u32 m0, s62, 23552\n" \
"s_mov_b32 s54, 1015808\n" \
"buffer_load_dwordx4 v2, s[64:67], s54 offen lds\n" \
"s_waitcnt vmcnt(32)\n" \
"s_barrier\n" \
"ds_read_b64_tr_b16 v[152:153], v3 offset:24576\n" \
"ds_read_b64_tr_b16 v[154:155], v3 offset:25088\n" \
"ds_read_b64_tr_b16 v[156:157], v3 offset:28672\n" \
"ds_read_b64_tr_b16 v[158:159], v3 offset:29184\n" \
"ds_read_b64_tr_b16 v[184:185], v32 offset:24576\n" \
"ds_read_b64_tr_b16 v[186:187], v32 offset:25088\n" \
"ds_read_b64_tr_b16 v[188:189], v32 offset:28672\n" \
"ds_read_b64_tr_b16 v[190:191], v32 offset:29184\n" \
"ds_read_b64_tr_b16 v[216:217], v33 offset:24576\n" \
"ds_read_b64_tr_b16 v[218:219], v33 offset:25088\n" \
"ds_read_b64_tr_b16 v[220:221], v33 offset:28672\n" \
"ds_read_b64_tr_b16 v[222:223], v33 offset:29184\n" \
"ds_read_b64_tr_b16 v[248:249], v34 offset:24576\n" \
"ds_read_b64_tr_b16 v[250:251], v34 offset:25088\n" \
"ds_read_b64_tr_b16 v[252:253], v34 offset:28672\n" \
"ds_read_b64_tr_b16 v[254:255], v34 offset:29184\n" \
"s_waitcnt lgkmcnt(0)\n" \
"s_barrier\n" \
"s_add_u32 m0, s62, 24576\n" \
"s_mov_b32 s54, 1048576\n" \
"buffer_load_dwordx4 v2, s[64:67], s54 offen lds\n" \
"s_add_u32 m0, s62, 25600\n" \
"s_mov_b32 s54, 1081344\n" \
"buffer_load_dwordx4 v2, s[64:67], s54 offen lds\n" \
"s_add_u32 m0, s62, 26624\n" \
"s_mov_b32 s54, 1114112\n" \
"buffer_load_dwordx4 v2, s[64:67], s54 offen lds\n" \
"s_add_u32 m0, s62, 27648\n" \
"s_mov_b32 s54, 1146880\n" \
"buffer_load_dwordx4 v2, s[64:67], s54 offen lds\n" \
"s_add_u32 m0, s62, 28672\n" \
"s_mov_b32 s54, 1179648\n" \
"buffer_load_dwordx4 v2, s[64:67], s54 offen lds\n" \
"s_add_u32 m0, s62, 29696\n" \
"s_mov_b32 s54, 1212416\n" \
"buffer_load_dwordx4 v2, s[64:67], s54 offen lds\n" \
"s_add_u32 m0, s62, 30720\n" \
"s_mov_b32 s54, 1245184\n" \
"buffer_load_dwordx4 v2, s[64:67], s54 offen lds\n" \
"s_add_u32 m0, s62, 31744\n" \
"s_mov_b32 s54, 1277952\n" \
"buffer_load_dwordx4 v2, s[64:67], s54 offen lds\n" \
"s_waitcnt vmcnt(36)\n" \
"ds_read_b64_tr_b16 a[0:1], v4 offset:32768\n" \
"ds_read_b64_tr_b16 a[2:3], v4 offset:32896\n" \
"ds_read_b64_tr_b16 a[4:5], v4 offset:34816\n" \
"ds_read_b64_tr_b16 a[6:7], v4 offset:34944\n" \
"ds_read_b64_tr_b16 a[8:9], v4 offset:32800\n" \
"ds_read_b64_tr_b16 a[10:11], v4 offset:32928\n" \
"ds_read_b64_tr_b16 a[12:13], v4 offset:34848\n" \
"ds_read_b64_tr_b16 a[14:15], v4 offset:34976\n" \
"s_waitcnt lgkmcnt(0)\n" \
"s_add_u32 m0, s62, 32768\n" \
"s_mov_b32 s54, 1310720\n" \
"buffer_load_dwordx4 v2, s[64:67], s54 offen lds\n" \
"s_add_u32 m0, s62, 33792\n" \
"s_mov_b32 s54, 1343488\n" \
"buffer_load_dwordx4 v2, s[64:67], s54 offen lds\n" \
"s_add_u32 m0, s62, 34816\n" \
"s_mov_b32 s54, 1376256\n" \
"buffer_load_dwordx4 v2, s[64:67], s54 offen lds\n" \
"s_add_u32 m0, s62, 35840\n" \
"s_mov_b32 s54, 1409024\n" \
"buffer_load_dwordx4 v2, s[64:67], s54 offen lds\n" \
"s_waitcnt vmcnt(36)\n" \
"ds_read_b64_tr_b16 a[16:17], v4 offset:36864\n" \
"ds_read_b64_tr_b16 a[18:19], v4 offset:36992\n" \
"ds_read_b64_tr_b16 a[20:21], v4 offset:38912\n" \
"ds_read_b64_tr_b16 a[22:23], v4 offset:39040\n" \
"ds_read_b64_tr_b16 a[24:25], v4 offset:36896\n" \
"ds_read_b64_tr_b16 a[26:27], v4 offset:37024\n" \
"ds_read_b64_tr_b16 a[28:29], v4 offset:38944\n" \
"ds_read_b64_tr_b16 a[30:31], v4 offset:39072\n" \
"s_waitcnt lgkmcnt(0)\n" \
"s_add_u32 m0, s62, 36864\n" \
"s_mov_b32 s54, 1441792\n" \
"buffer_load_dwordx4 v2, s[64:67], s54 offen lds\n" \
"s_add_u32 m0, s62, 37888\n" \
"s_mov_b32 s54, 1474560\n" \
"buffer_load_dwordx4 v2, s[64:67], s54 offen lds\n" \
"s_add_u32 m0, s62, 38912\n" \
"s_mov_b32 s54, 1507328\n" \
"buffer_load_dwordx4 v2, s[64:67], s54 offen lds\n" \
"s_add_u32 m0, s62, 39936\n" \
"s_mov_b32 s54, 1540096\n" \
"buffer_load_dwordx4 v2, s[64:67], s54 offen lds\n" \
"s_waitcnt vmcnt(36)\n" \
"ds_read_b64_tr_b16 a[32:33], v4 offset:0\n" \
"ds_read_b64_tr_b16 a[34:35], v4 offset:128\n" \
"ds_read_b64_tr_b16 a[36:37], v4 offset:2048\n" \
"ds_read_b64_tr_b16 a[38:39], v4 offset:2176\n" \
"ds_read_b64_tr_b16 a[40:41], v4 offset:32\n" \
"ds_read_b64_tr_b16 a[42:43], v4 offset:160\n" \
"ds_read_b64_tr_b16 a[44:45], v4 offset:2080\n" \
"ds_read_b64_tr_b16 a[46:47], v4 offset:2208\n" \
"s_waitcnt lgkmcnt(0)\n" \
"s_add_u32 m0, s62, 0\n" \
"s_mov_b32 s54, 1572864\n" \
"buffer_load_dwordx4 v2, s[64:67], s54 offen lds\n" \
"s_add_u32 m0, s62, 1024\n" \
"s_mov_b32 s54, 1605632\n" \
"buffer_load_dwordx4 v2, s[64:67], s54 offen lds\n" \
"s_add_u32 m0, s62, 2048\n" \
"s_mov_b32 s54, 1638400\n" \
"buffer_load_dwordx4 v2, s[64:67], s54 offen lds\n" \
"s_add_u32 m0, s62, 3072\n" \
"s_mov_b32 s54, 1671168\n" \
"buffer_load_dwordx4 v2, s[64:67], s54 offen lds\n" \
"s_waitcnt vmcnt(36)\n" \
"ds_read_b64_tr_b16 a[48:49], v4 offset:4096\n" \
"ds_read_b64_tr_b16 a[50:51], v4 offset:4224\n" \
"ds_read_b64_tr_b16 a[52:53], v4 offset:6144\n" \
"ds_read_b64_tr_b16 a[54:55], v4 offset:6272\n" \
"ds_read_b64_tr_b16 a[56:57], v4 offset:4128\n" \
"ds_read_b64_tr_b16 a[58:59], v4 offset:4256\n" \
"ds_read_b64_tr_b16 a[60:61], v4 offset:6176\n" \
"ds_read_b64_tr_b16 a[62:63], v4 offset:6304\n" \
"s_waitcnt lgkmcnt(0)\n" \
"s_add_u32 m0, s62, 4096\n" \
"s_mov_b32 s54, 1703936\n" \
"buffer_load_dwordx4 v2, s[64:67], s54 offen lds\n" \
"s_add_u32 m0, s62, 5120\n" \
"s_mov_b32 s54, 1736704\n" \
"buffer_load_dwordx4 v2, s[64:67], s54 offen lds\n" \
"s_add_u32 m0, s62, 6144\n" \
"s_mov_b32 s54, 1769472\n" \
"buffer_load_dwordx4 v2, s[64:67], s54 offen lds\n" \
"s_add_u32 m0, s62, 7168\n" \
"s_mov_b32 s54, 1802240\n" \
"buffer_load_dwordx4 v2, s[64:67], s54 offen lds\n" \
"s_waitcnt vmcnt(36)\n" \
"ds_read_b64_tr_b16 a[64:65], v4 offset:8192\n" \
"ds_read_b64_tr_b16 a[66:67], v4 offset:8320\n" \
"ds_read_b64_tr_b16 a[68:69], v4 offset:10240\n" \
"ds_read_b64_tr_b16 a[70:71], v4 offset:10368\n" \
"ds_read_b64_tr_b16 a[72:73], v4 offset:8224\n" \
"ds_read_b64_tr_b16 a[74:75], v4 offset:8352\n" \
"ds_read_b64_tr_b16 a[76:77], v4 offset:10272\n" \
"ds_read_b64_tr_b16 a[78:79], v4 offset:10400\n" \
"s_waitcnt lgkmcnt(0)\n" \
"s_add_u32 m0, s62, 8192\n" \
"s_mov_b32 s54, 1835008\n" \
"buffer_load_dwordx4 v2, s[64:67], s54 offen lds\n" \
"s_add_u32 m0, s62, 9216\n" \
"s_mov_b32 s54, 1867776\n" \
"buffer_load_dwordx4 v2, s[64:67], s54 offen lds\n" \
"s_add_u32 m0, s62, 10240\n" \
"s_mov_b32 s54, 1900544\n" \
"buffer_load_dwordx4 v2, s[64:67], s54 offen lds\n" \
"s_add_u32 m0, s62, 11264\n" \
"s_mov_b32 s54, 1933312\n" \
"buffer_load_dwordx4 v2, s[64:67], s54 offen lds\n" \
"s_waitcnt vmcnt(36)\n" \
"ds_read_b64_tr_b16 a[80:81], v4 offset:12288\n" \
"ds_read_b64_tr_b16 a[82:83], v4 offset:12416\n" \
"ds_read_b64_tr_b16 a[84:85], v4 offset:14336\n" \
"ds_read_b64_tr_b16 a[86:87], v4 offset:14464\n" \
"ds_read_b64_tr_b16 a[88:89], v4 offset:12320\n" \
"ds_read_b64_tr_b16 a[90:91], v4 offset:12448\n" \
"ds_read_b64_tr_b16 a[92:93], v4 offset:14368\n" \
"ds_read_b64_tr_b16 a[94:95], v4 offset:14496\n" \
"s_waitcnt lgkmcnt(0)\n" \
"s_add_u32 m0, s62, 12288\n" \
"s_mov_b32 s54, 1966080\n" \
"buffer_load_dwordx4 v2, s[64:67], s54 offen lds\n" \
"s_add_u32 m0, s62, 13312\n" \
"s_mov_b32 s54, 1998848\n" \
"buffer_load_dwordx4 v2, s[64:67], s54 offen lds\n" \
"s_add_u32 m0, s62, 14336\n" \
"s_mov_b32 s54, 2031616\n" \
"buffer_load_dwordx4 v2, s[64:67], s54 offen lds\n" \
"s_add_u32 m0, s62, 15360\n" \
"s_mov_b32 s54, 2064384\n" \
"buffer_load_dwordx4 v2, s[64:67], s54 offen lds\n" \
"s_waitcnt vmcnt(36)\n" \
"ds_read_b64_tr_b16 a[96:97], v4 offset:16384\n" \
"ds_read_b64_tr_b16 a[98:99], v4 offset:16512\n" \
"ds_read_b64_tr_b16 a[100:101], v4 offset:18432\n" \
"ds_read_b64_tr_b16 a[102:103], v4 offset:18560\n" \
"ds_read_b64_tr_b16 a[104:105], v4 offset:16416\n" \
"ds_read_b64_tr_b16 a[106:107], v4 offset:16544\n" \
"ds_read_b64_tr_b16 a[108:109], v4 offset:18464\n" \
"ds_read_b64_tr_b16 a[110:111], v4 offset:18592\n" \
"s_waitcnt vmcnt(32)\n" \
"ds_read_b64_tr_b16 a[112:113], v4 offset:20480\n" \
"ds_read_b64_tr_b16 a[114:115], v4 offset:20608\n" \
"ds_read_b64_tr_b16 a[116:117], v4 offset:22528\n" \
"ds_read_b64_tr_b16 a[118:119], v4 offset:22656\n" \
"ds_read_b64_tr_b16 a[120:121], v4 offset:20512\n" \
"ds_read_b64_tr_b16 a[122:123], v4 offset:20640\n" \
"ds_read_b64_tr_b16 a[124:125], v4 offset:22560\n" \
"ds_read_b64_tr_b16 a[126:127], v4 offset:22688\n" \
"s_waitcnt vmcnt(28)\n" \
"ds_read_b64_tr_b16 a[128:129], v4 offset:24576\n" \
"ds_read_b64_tr_b16 a[130:131], v4 offset:24704\n" \
"ds_read_b64_tr_b16 a[132:133], v4 offset:26624\n" \
"ds_read_b64_tr_b16 a[134:135], v4 offset:26752\n" \
"ds_read_b64_tr_b16 a[136:137], v4 offset:24608\n" \
"ds_read_b64_tr_b16 a[138:139], v4 offset:24736\n" \
"ds_read_b64_tr_b16 a[140:141], v4 offset:26656\n" \
"ds_read_b64_tr_b16 a[142:143], v4 offset:26784\n" \
"s_waitcnt vmcnt(24)\n" \
"ds_read_b64_tr_b16 a[144:145], v4 offset:28672\n" \
"ds_read_b64_tr_b16 a[146:147], v4 offset:28800\n" \
"ds_read_b64_tr_b16 a[148:149], v4 offset:30720\n" \
"ds_read_b64_tr_b16 a[150:151], v4 offset:30848\n" \
"ds_read_b64_tr_b16 a[152:153], v4 offset:28704\n" \
"ds_read_b64_tr_b16 a[154:155], v4 offset:28832\n" \
"ds_read_b64_tr_b16 a[156:157], v4 offset:30752\n" \
"ds_read_b64_tr_b16 a[158:159], v4 offset:30880\n" \
"s_waitcnt vmcnt(20)\n" \
"ds_read_b64_tr_b16 a[160:161], v4 offset:32768\n" \
"ds_read_b64_tr_b16 a[162:163], v4 offset:32896\n" \
"ds_read_b64_tr_b16 a[164:165], v4 offset:34816\n" \
"ds_read_b64_tr_b16 a[166:167], v4 offset:34944\n" \
"ds_read_b64_tr_b16 a[168:169], v4 offset:32800\n" \
"ds_read_b64_tr_b16 a[170:171], v4 offset:32928\n" \
"ds_read_b64_tr_b16 a[172:173], v4 offset:34848\n" \
"ds_read_b64_tr_b16 a[174:175], v4 offset:34976\n" \
"s_waitcnt vmcnt(16)\n" \
"ds_read_b64_tr_b16 a[176:177], v4 offset:36864\n" \
"ds_read_b64_tr_b16 a[178:179], v4 offset:36992\n" \
"ds_read_b64_tr_b16 a[180:181], v4 offset:38912\n" \
"ds_read_b64_tr_b16 a[182:183], v4 offset:39040\n" \
"ds_read_b64_tr_b16 a[184:185], v4 offset:36896\n" \
"ds_read_b64_tr_b16 a[186:187], v4 offset:37024\n" \
"ds_read_b64_tr_b16 a[188:189], v4 offset:38944\n" \
"ds_read_b64_tr_b16 a[190:191], v4 offset:39072\n" \
"s_waitcnt vmcnt(12)\n" \
"ds_read_b64_tr_b16 a[192:193], v4 offset:0\n" \
"ds_read_b64_tr_b16 a[194:195], v4 offset:128\n" \
"ds_read_b64_tr_b16 a[196:197], v4 offset:2048\n" \
"ds_read_b64_tr_b16 a[198:199], v4 offset:2176\n" \
"ds_read_b64_tr_b16 a[200:201], v4 offset:32\n" \
"ds_read_b64_tr_b16 a[202:203], v4 offset:160\n" \
"ds_read_b64_tr_b16 a[204:205], v4 offset:2080\n" \
"ds_read_b64_tr_b16 a[206:207], v4 offset:2208\n" \
"s_waitcnt vmcnt(8)\n" \
"ds_read_b64_tr_b16 a[208:209], v4 offset:4096\n" \
"ds_read_b64_tr_b16 a[210:211], v4 offset:4224\n" \
"ds_read_b64_tr_b16 a[212:213], v4 offset:6144\n" \
"ds_read_b64_tr_b16 a[214:215], v4 offset:6272\n" \
"ds_read_b64_tr_b16 a[216:217], v4 offset:4128\n" \
"ds_read_b64_tr_b16 a[218:219], v4 offset:4256\n" \
"ds_read_b64_tr_b16 a[220:221], v4 offset:6176\n" \
"ds_read_b64_tr_b16 a[222:223], v4 offset:6304\n" \
"s_waitcnt vmcnt(4)\n" \
"ds_read_b64_tr_b16 a[224:225], v4 offset:8192\n" \
"ds_read_b64_tr_b16 a[226:227], v4 offset:8320\n" \
"ds_read_b64_tr_b16 a[228:229], v4 offset:10240\n" \
"ds_read_b64_tr_b16 a[230:231], v4 offset:10368\n" \
"ds_read_b64_tr_b16 a[232:233], v4 offset:8224\n" \
"ds_read_b64_tr_b16 a[234:235], v4 offset:8352\n" \
"ds_read_b64_tr_b16 a[236:237], v4 offset:10272\n" \
"ds_read_b64_tr_b16 a[238:239], v4 offset:10400\n" \
"s_waitcnt vmcnt(0)\n" \
"ds_read_b64_tr_b16 a[240:241], v4 offset:12288\n" \
"ds_read_b64_tr_b16 a[242:243], v4 offset:12416\n" \
"ds_read_b64_tr_b16 a[244:245], v4 offset:14336\n" \
"ds_read_b64_tr_b16 a[246:247], v4 offset:14464\n" \
"ds_read_b64_tr_b16 a[248:249], v4 offset:12320\n" \
"ds_read_b64_tr_b16 a[250:251], v4 offset:12448\n" \
"ds_read_b64_tr_b16 a[252:253], v4 offset:14368\n" \
"ds_read_b64_tr_b16 a[254:255], v4 offset:14496\n" \
"s_waitcnt vmcnt(0) lgkmcnt(0)\n" \
"s_barrier\n" \
"s_getpc_b64 s[18:19]\n" \
".Lv_pc_%=:\n" \
"v_mbcnt_lo_u32_b32 v40, -1, 0\n" \
"v_mbcnt_hi_u32_b32 v40, -1, v40\n" \
"v_lshl_add_u32 v40, s48, 6, v40\n" \
"v_lshlrev_b32 v40, 6, v40\n" \
"v_min_u32 v40, .Lv_fin_%=-.Lv_pc_%=, v40\n" \
"global_load_dword v41, v40, s[18:19]\n" \
"s_mov_b32 s49, 0\n" \
"s_mov_b32 s52, 0\n" \
"s_mov_b32 s88, 0\n" \
"s_mov_b32 s76, 0\n" \
"s_mov_b32 s22, 0xfffffc00\n" \
"s_mov_b32 s23, -1\n" \
"s_mul_i32 s32, s17, 1\n" \
"s_mov_b32 s33, s32\n" \
"s_mov_b32 s34, s17\n" \
"v_max_u32 v20, 1, v20\n" \
"v_max_u32 v21, 1, v21\n" \
"v_max_u32 v22, 1, v22\n" \
"v_max_u32 v23, 1, v23\n" \
"buffer_store_dwordx4 v[20:23], v8, s[36:39], 0 offen sc1\n" \
"s_mov_b32 s72, 0\n" \
"s_mov_b32 s73, -1\n" \
"v_lshlrev_b32 v31, 1, v0\n" \
"v_add_u32 v31, 16384, v31\n" \
"s_lshl_b32 s79, s48, 10\n" \
"s_add_u32 s79, s79, 0\n" \
"v_xor_b32 v30, 0x1000, v6\n" \
"s_mov_b32 s53, 0\n" \
"s_cmp_lg_u32 s48, 0\n" \
"s_cbranch_scc1 .Lv_go0x_%=\n" \
".Lv_go0_%=:\n" \
"buffer_load_dword v13, v10, s[36:39], 0 offen sc1\n" \
"s_waitcnt vmcnt(0)\n" \
"v_cmp_eq_u32 vcc, 0, v13\n" \
"s_add_u32 s53, s53, 1\n" \
"s_cmp_lt_u32 s53, 32768\n" \
"s_cselect_b64 s[54:55], -1, 0\n" \
"s_and_b64 vcc, vcc, s[54:55]\n" \
"s_cbranch_vccz .Lv_go0x_%=\n" \
"s_sleep 4\n" \
"s_branch .Lv_go0_%=\n" \
".Lv_go0x_%=:\n" \
"s_memrealtime s[56:57]\n" \
"s_waitcnt lgkmcnt(0)\n" \
"s_sub_u32 s50, s56, 20\n" \
"s_waitcnt vmcnt(0) lgkmcnt(0)\n" \
"s_add_u32 s54, s49, 0\n" \
"s_and_b32 s54, s54, 3\n" \
"s_mul_i32 s58, s54, 131072\n" \
"s_add_u32 s54, s49, 1\n" \
"s_and_b32 s54, s54, 3\n" \
"s_mul_i32 s59, s54, 131072\n" \
"s_add_u32 s54, s49, 2\n" \
"s_and_b32 s54, s54, 3\n" \
"s_mul_i32 s60, s54, 131072\n" \
"s_add_u32 s54, s49, 3\n" \
"s_and_b32 s54, s54, 3\n" \
"s_mul_i32 s61, s54, 131072\n" \
"s_mov_b32 s77, 0\n" \
"s_add_u32 s78, s77, s63\n" \
"s_xor_b32 s89, s78, 0x4800\n" \
"s_xor_b32 s90, s78, 0x800\n" \
"s_xor_b32 s91, s78, 0x4000\n" \
"s_mov_b32 m0, s79\n" \
"s_add_u32 s51, s50, s46\n" \
"v_mov_b64 v[80:81], 0\n" \
"v_mov_b64 v[82:83], 0\n" \
"v_mov_b32 v13, -1\n" \
"s_mov_b32 s23, -1\n" \
"s_cmp_eq_u32 s48, 0\n" \
"s_cselect_b32 s80, -1, 0\n" \
"s_mov_b32 s81, 0\n" \
"s_nop 7\n" \
"s_nop 7\n" \
".p2align 6\n" \
".Lv_loop_%=:\n" \
"s_getpc_b64 s[18:19]\n" \
".Lv_wp_%=:\n" \
"s_add_u32 s18, s18, .Lv_ws_%=-.Lv_wp_%=\n" \
"s_addc_u32 s19, s19, 0\n" \
"s_sub_u32 s21, s51, 5\n" \
"s_nop 0\n" \
".Lv_w_%=:\n" \
"s_memrealtime s[56:57]\n" \
"s_waitcnt lgkmcnt(0)\n" \
"s_sub_u32 s54, s56, s21\n" \
"s_cmp_gt_u32 s54, s22\n" \
"s_cbranch_scc1 .Lv_w_%=\n" \
"s_min_u32 s21, s54, 3\n" \
"s_lshl2_add_u32 s18, s21, s18\n" \
"s_setpc_b64 s[18:19]\n" \
"s_nop 0\n" \
".Lv_ws_%=:\n" \
"s_nop 4\n" \
"s_nop 5\n" \
"s_nop 4\n" \
".Lv_w4_%=:\n" \
"s_barrier\n" \
".Lv_go_%=:\n" \
"buffer_load_dwordx4 v5, s[36:39], s78 offen sc1 lds\n" \
"s_nop 7\n" \
"s_add_u32 m0, s79, 4096\n" \
"s_nop 0\n" \
"buffer_load_dwordx4 v5, s[36:39], s89 offen sc1 lds\n" \
"s_nop 7\n" \
"s_add_u32 m0, s79, 8192\n" \
"s_nop 0\n" \
"buffer_load_dwordx4 v5, s[36:39], s90 offen sc1 lds\n" \
"s_nop 7\n" \
"s_add_u32 m0, s79, 12288\n" \
"s_nop 0\n" \
"buffer_load_dwordx4 v5, s[36:39], s91 offen sc1 lds\n" \
".Lv_x_%=:\n" \
"s_waitcnt vmcnt(2)\n" \
"s_barrier\n" \
"ds_read_b128 v[32:35], v0 offset:0\n" \
"ds_read_b128 v[36:39], v0 offset:1024\n" \
"ds_read_b128 v[40:43], v0 offset:2048\n" \
"ds_read_b128 v[44:47], v0 offset:3072\n" \
"ds_read_b128 v[48:51], v0 offset:4096\n" \
"s_waitcnt lgkmcnt(4)\n" \
"v_smfmac_f32_16x16x64_bf16 v[80:83], v[32:35], v[128:135], v12\n" \
"ds_read_b128 v[52:55], v0 offset:5120\n" \
"s_waitcnt lgkmcnt(4)\n" \
"v_min3_u32 v13, v13, v32, v34\n" \
"v_smfmac_f32_16x16x64_bf16 v[80:83], v[36:39], v[136:143], v12\n" \
"ds_read_b128 v[56:59], v0 offset:6144\n" \
"s_waitcnt lgkmcnt(4)\n" \
"v_min3_u32 v13, v13, v36, v38\n" \
"v_smfmac_f32_16x16x64_bf16 v[80:83], v[40:43], v[144:151], v12\n" \
"ds_read_b128 v[60:63], v0 offset:7168\n" \
"s_waitcnt lgkmcnt(4)\n" \
"v_min3_u32 v13, v13, v40, v42\n" \
"v_smfmac_f32_16x16x64_bf16 v[80:83], v[44:47], v[152:159], v12\n" \
"s_waitcnt vmcnt(0)\n" \
"s_barrier\n" \
"ds_read_b128 v[32:35], v0 offset:8192\n" \
"s_waitcnt lgkmcnt(4)\n" \
"v_min3_u32 v13, v13, v44, v46\n" \
"v_smfmac_f32_16x16x64_bf16 v[80:83], v[48:51], v[160:167], v12\n" \
"ds_read_b128 v[36:39], v0 offset:9216\n" \
"s_waitcnt lgkmcnt(4)\n" \
"v_min3_u32 v13, v13, v48, v50\n" \
"v_cmp_eq_u32_e64 vcc, 0, v13\n" \
"v_smfmac_f32_16x16x64_bf16 v[80:83], v[52:55], v[168:175], v12\n" \
"ds_read_b128 v[40:43], v0 offset:10240\n" \
"s_waitcnt lgkmcnt(4)\n" \
"v_min3_u32 v13, v13, v52, v54\n" \
"v_smfmac_f32_16x16x64_bf16 v[80:83], v[56:59], v[176:183], v12\n" \
"ds_read_b128 v[44:47], v0 offset:11264\n" \
"s_waitcnt lgkmcnt(4)\n" \
"v_min3_u32 v13, v13, v56, v58\n" \
"v_smfmac_f32_16x16x64_bf16 v[80:83], v[60:63], v[184:191], v12\n" \
"ds_read_b128 v[48:51], v0 offset:12288\n" \
"s_waitcnt lgkmcnt(4)\n" \
"s_nop 0\n" \
"s_cbranch_vccnz .Lv_mretry_%=\n" \
"v_min3_u32 v13, v13, v60, v62\n" \
"v_smfmac_f32_16x16x64_bf16 v[80:83], v[32:35], v[192:199], v12\n" \
"ds_read_b128 v[52:55], v0 offset:13312\n" \
"s_waitcnt lgkmcnt(4)\n" \
"v_min3_u32 v13, v13, v32, v34\n" \
"v_smfmac_f32_16x16x64_bf16 v[80:83], v[36:39], v[200:207], v12\n" \
"ds_read_b128 v[56:59], v0 offset:14336\n" \
"s_waitcnt lgkmcnt(4)\n" \
"v_min3_u32 v13, v13, v36, v38\n" \
"v_smfmac_f32_16x16x64_bf16 v[80:83], v[40:43], v[208:215], v12\n" \
"ds_read_b128 v[60:63], v0 offset:15360\n" \
"s_waitcnt lgkmcnt(4)\n" \
"v_min3_u32 v13, v13, v40, v42\n" \
"v_smfmac_f32_16x16x64_bf16 v[80:83], v[44:47], v[216:223], v12\n" \
"s_waitcnt lgkmcnt(3)\n" \
"v_min3_u32 v13, v13, v44, v46\n" \
"v_smfmac_f32_16x16x64_bf16 v[80:83], v[48:51], v[224:231], v12\n" \
"s_waitcnt lgkmcnt(2)\n" \
"v_min3_u32 v13, v13, v48, v50\n" \
"v_smfmac_f32_16x16x64_bf16 v[80:83], v[52:55], v[232:239], v12\n" \
"s_waitcnt lgkmcnt(1)\n" \
"v_min3_u32 v13, v13, v52, v54\n" \
"v_smfmac_f32_16x16x64_bf16 v[80:83], v[56:59], v[240:247], v12\n" \
"s_waitcnt lgkmcnt(0)\n" \
"v_min3_u32 v13, v13, v56, v58\n" \
"v_smfmac_f32_16x16x64_bf16 v[80:83], v[60:63], v[248:255], v12\n" \
"v_min3_u32 v13, v13, v60, v62\n" \
"v_cmp_eq_u32 vcc, 0, v13\n" \
"s_nop 5\n" \
"v_pk_add_f32 v[80:81], v[80:81], v[82:83]\n" \
"s_cbranch_vccnz .Lv_mretry_%=\n" \
"v_cvt_pk_bf16_f32 v84, v80, v81\n" \
"v_max_u32 v84, 1, v84\n" \
"buffer_store_dword v84, v7, s[36:39], s58 offen sc1\n" \
"s_mov_b32 s75, s77\n" \
"s_lshr_b32 s77, s59, 1\n" \
"buffer_load_dwordx2 v[64:65], v9, s[36:39], s75 offen sc1\n" \
"s_sub_u32 s53, s49, 1\n" \
"s_add_u32 s25, s24, 3\n" \
"s_cmp_eq_u32 s26, s24\n" \
"s_subb_u32 s25, s25, 0\n" \
"s_cmp_eq_u32 s28, s24\n" \
"s_subb_u32 s25, s25, 0\n" \
"s_cmp_eq_u32 s30, s24\n" \
"s_subb_u32 s25, s25, 0\n" \
"s_max_u32 s16, s25, 1\n" \
"s_sub_u32 s24, s25, s55\n" \
"s_min_u32 s24, s24, 255\n" \
"s_cmp_eq_u32 s23, s53\n" \
"s_cselect_b32 s24, s34, s24\n" \
"s_min_u32 s34, s24, s32\n" \
"s_max_u32 s25, s24, s32\n" \
"s_min_u32 s25, s25, s33\n" \
"s_max_u32 s34, s34, s25\n" \
"s_mov_b32 s33, s32\n" \
"s_mov_b32 s32, s24\n" \
"s_cmp_eq_u32 s49, 63\n" \
"s_cbranch_scc1 .Lv_end_%=\n" \
"v_mov_b64 v[80:81], 0\n" \
"v_mov_b64 v[82:83], 0\n" \
"v_mov_b64 v[84:85], 0\n" \
"v_mov_b64 v[86:87], 0\n" \
"v_mov_b64 v[88:89], 0\n" \
"v_mov_b64 v[90:91], 0\n" \
"v_mov_b64 v[92:93], 0\n" \
"v_mov_b64 v[94:95], 0\n" \
"buffer_store_dword v16, v7, s[36:39], s60 offen sc1\n" \
"s_lshr_b32 s54, s61, 1\n" \
"buffer_store_dwordx4 v[16:19], v8, s[36:39], s54 offen sc1\n" \
"s_sub_u32 s54, s34, s74\n" \
"v_mov_b32_e64 v13, s54\n" \
"buffer_store_dword v13, v28, s[36:39], 0 offen sc1\n" \
"v_mov_b32_e64 v13, -1\n" \
"s_mov_b32 s54, 0\n" \
"s_cmp_lg_u32 s48, 0\n" \
"s_cbranch_scc1 .Lv_wo4_%=\n" \
"s_waitcnt vmcnt(3)\n" \
"v_cmp_eq_u32_e64 vcc, 0, v64\n" \
"s_nop 0\n" \
"s_and_b64 vcc, vcc, s[80:81]\n" \
"s_cbranch_vccnz .Lv_h_cold_%=\n" \
".Lv_h_ok_%=:\n" \
"v_subrev_u32 v64, s51, v64\n" \
"v_max_i32_dpp v65, v65, v65 row_ror:8 row_mask:0xf bank_mask:0xf\n" \
"s_nop 0\n" \
"v_max_i32_dpp v64, v64, v64 row_ror:8 row_mask:0xf bank_mask:0xf\n" \
"v_max_i32_dpp v65, v65, v65 row_ror:4 row_mask:0xf bank_mask:0xf\n" \
"s_nop 0\n" \
"v_max_i32_dpp v64, v64, v64 row_ror:4 row_mask:0xf bank_mask:0xf\n" \
"v_max_i32_dpp v65, v65, v65 row_ror:2 row_mask:0xf bank_mask:0xf\n" \
"s_nop 0\n" \
"v_max_i32_dpp v64, v64, v64 row_ror:2 row_mask:0xf bank_mask:0xf\n" \
"v_max_i32_dpp v65, v65, v65 row_ror:1 row_mask:0xf bank_mask:0xf\n" \
"s_nop 0\n" \
"v_max_i32_dpp v64, v64, v64 row_ror:1 row_mask:0xf bank_mask:0xf\n" \
"v_readlane_b32 s53, v65, 0\n" \
"v_readlane_b32 s50, v65, 16\n" \
"v_readlane_b32 s54, v64, 0\n" \
"v_readlane_b32 s55, v64, 16\n" \
"s_max_i32 s50, s50, s53\n" \
"s_max_i32 s55, s55, s54\n" \
"s_add_u32 s55, s55, s51\n" \
"s_add_u32 s55, s55, s44\n" \
"s_add_u32 s50, s50, s55\n" \
"s_add_u32 s50, s50, s45\n" \
"s_getpc_b64 s[18:19]\n" \
".Lv_wop_%=:\n" \
"s_add_u32 s18, s18, .Lv_wos_%=-.Lv_wop_%=\n" \
"s_addc_u32 s19, s19, 0\n" \
"s_sub_u32 s21, s55, 5\n" \
"s_nop 0\n" \
"s_nop 0\n" \
"s_nop 0\n" \
"s_nop 0\n" \
"s_nop 0\n" \
".Lv_wo_%=:\n" \
"s_memrealtime s[56:57]\n" \
"s_waitcnt lgkmcnt(0)\n" \
"s_sub_u32 s54, s56, s21\n" \
"s_cmp_gt_u32 s54, s22\n" \
"s_cbranch_scc1 .Lv_wo_%=\n" \
"s_min_u32 s21, s54, 3\n" \
"s_lshl2_add_u32 s18, s21, s18\n" \
"s_setpc_b64 s[18:19]\n" \
"s_nop 0\n" \
".Lv_wos_%=:\n" \
"s_nop 4\n" \
"s_nop 5\n" \
"s_nop 4\n" \
".Lv_wo4_%=:\n" \
"s_barrier\n" \
".Lv_o_%=:\n" \
"buffer_load_dwordx4 v[32:35], v6, s[36:39], s58 offen offset:0 sc1\n" \
"s_cmp_lt_u32 s54, 8\n" \
"s_cselect_b32 s23, s23, s49\n" \
"s_nop 8\n" \
"buffer_load_dwordx4 v[36:39], v6, s[36:39], s58 offen offset:1024 sc1\n" \
"s_nop 10\n" \
"buffer_load_dwordx4 v[40:43], v6, s[36:39], s58 offen offset:2048 sc1\n" \
"s_nop 10\n" \
"buffer_load_dwordx4 v[44:47], v6, s[36:39], s58 offen offset:3072 sc1\n" \
"s_nop 10\n" \
"buffer_load_dwordx4 v[48:51], v30, s[36:39], s58 offen offset:0 sc1\n" \
"s_nop 10\n" \
"buffer_load_dwordx4 v[52:55], v30, s[36:39], s58 offen offset:1024 sc1\n" \
"s_nop 10\n" \
"buffer_load_dwordx4 v[56:59], v30, s[36:39], s58 offen offset:2048 sc1\n" \
"s_nop 10\n" \
"buffer_load_dwordx4 v[60:63], v30, s[36:39], s58 offen offset:3072 sc1\n" \
".Lv_y_%=:\n" \
"s_waitcnt vmcnt(7)\n" \
"v_smfmac_f32_16x16x64_bf16 v[80:83], v[32:35], a[0:7], v12\n" \
"v_min3_u32 v13, v13, v32, v34\n" \
"v_smfmac_f32_16x16x64_bf16 v[84:87], v[32:35], a[8:15], v12\n" \
"v_smfmac_f32_16x16x64_bf16 v[88:91], v[32:35], a[16:23], v12\n" \
"v_smfmac_f32_16x16x64_bf16 v[92:95], v[32:35], a[24:31], v12\n" \
"s_waitcnt vmcnt(6)\n" \
"v_smfmac_f32_16x16x64_bf16 v[80:83], v[36:39], a[32:39], v12\n" \
"v_min3_u32 v13, v13, v36, v38\n" \
"v_smfmac_f32_16x16x64_bf16 v[84:87], v[36:39], a[40:47], v12\n" \
"v_cmp_eq_u32 vcc, 0, v13\n" \
"v_smfmac_f32_16x16x64_bf16 v[88:91], v[36:39], a[48:55], v12\n" \
"v_smfmac_f32_16x16x64_bf16 v[92:95], v[36:39], a[56:63], v12\n" \
"s_cbranch_vccnz .Lv_retry_%=\n" \
"s_waitcnt vmcnt(5)\n" \
"v_smfmac_f32_16x16x64_bf16 v[80:83], v[40:43], a[64:71], v12\n" \
"v_min3_u32 v13, v13, v40, v42\n" \
"v_smfmac_f32_16x16x64_bf16 v[84:87], v[40:43], a[72:79], v12\n" \
"v_smfmac_f32_16x16x64_bf16 v[88:91], v[40:43], a[80:87], v12\n" \
"v_smfmac_f32_16x16x64_bf16 v[92:95], v[40:43], a[88:95], v12\n" \
"s_waitcnt vmcnt(4)\n" \
"v_smfmac_f32_16x16x64_bf16 v[80:83], v[44:47], a[96:103], v12\n" \
"v_min3_u32 v13, v13, v44, v46\n" \
"v_smfmac_f32_16x16x64_bf16 v[84:87], v[44:47], a[104:111], v12\n" \
"v_smfmac_f32_16x16x64_bf16 v[88:91], v[44:47], a[112:119], v12\n" \
"v_smfmac_f32_16x16x64_bf16 v[92:95], v[44:47], a[120:127], v12\n" \
"s_waitcnt vmcnt(3)\n" \
"v_smfmac_f32_16x16x64_bf16 v[80:83], v[48:51], a[128:135], v12\n" \
"v_min3_u32 v13, v13, v48, v50\n" \
"v_smfmac_f32_16x16x64_bf16 v[84:87], v[48:51], a[136:143], v12\n" \
"v_smfmac_f32_16x16x64_bf16 v[88:91], v[48:51], a[144:151], v12\n" \
"v_smfmac_f32_16x16x64_bf16 v[92:95], v[48:51], a[152:159], v12\n" \
"s_waitcnt vmcnt(2)\n" \
"v_smfmac_f32_16x16x64_bf16 v[80:83], v[52:55], a[160:167], v12\n" \
"v_min3_u32 v13, v13, v52, v54\n" \
"v_smfmac_f32_16x16x64_bf16 v[84:87], v[52:55], a[168:175], v12\n" \
"v_smfmac_f32_16x16x64_bf16 v[88:91], v[52:55], a[176:183], v12\n" \
"v_smfmac_f32_16x16x64_bf16 v[92:95], v[52:55], a[184:191], v12\n" \
"s_waitcnt vmcnt(1)\n" \
"v_smfmac_f32_16x16x64_bf16 v[80:83], v[56:59], a[192:199], v12\n" \
"v_min3_u32 v13, v13, v56, v58\n" \
"v_smfmac_f32_16x16x64_bf16 v[84:87], v[56:59], a[200:207], v12\n" \
"v_smfmac_f32_16x16x64_bf16 v[88:91], v[56:59], a[208:215], v12\n" \
"v_smfmac_f32_16x16x64_bf16 v[92:95], v[56:59], a[216:223], v12\n" \
"s_waitcnt vmcnt(0)\n" \
"v_smfmac_f32_16x16x64_bf16 v[80:83], v[60:63], a[224:231], v12\n" \
"v_min3_u32 v13, v13, v60, v62\n" \
"v_smfmac_f32_16x16x64_bf16 v[84:87], v[60:63], a[232:239], v12\n" \
"v_cmp_eq_u32 vcc, 0, v13\n" \
"v_smfmac_f32_16x16x64_bf16 v[88:91], v[60:63], a[240:247], v12\n" \
"v_smfmac_f32_16x16x64_bf16 v[92:95], v[60:63], a[248:255], v12\n" \
"s_cbranch_vccnz .Lv_retry_%=\n" \
".Lv_y2_%=:\n" \
"v_pk_add_f32 v[80:81], v[80:81], v[82:83]\n" \
"v_pk_add_f32 v[84:85], v[84:85], v[86:87]\n" \
"s_nop 0\n" \
"v_pk_add_f32 v[88:89], v[88:89], v[90:91]\n" \
"s_nop 2\n" \
"v_pk_add_f32 v[92:93], v[92:93], v[94:95]\n" \
"v_cndmask_b32_e64 v80, v80, v88, s[72:73]\n" \
"v_cndmask_b32_e64 v81, v81, v89, s[72:73]\n" \
"v_cndmask_b32_e64 v84, v84, v92, s[72:73]\n" \
"v_cndmask_b32_e64 v85, v85, v93, s[72:73]\n" \
"s_nop 0\n" \
"v_permlane32_swap_b32 v80, v84\n" \
"v_permlane32_swap_b32 v81, v85\n" \
"v_pk_add_f32 v[80:81], v[80:81], v[84:85]\n" \
"ds_write2_b32 v15, v80, v81 offset1:32\n" \
"s_cmp_lg_u32 s48, 0\n" \
"s_waitcnt lgkmcnt(0)\n" \
"s_barrier\n" \
"s_nop 0\n" \
"s_nop 0\n" \
"s_nop 0\n" \
"s_cbranch_scc1 .Lv_skip_%=\n" \
"ds_read_b128 v[64:67], v31\n" \
"ds_read_b128 v[68:71], v31 offset:16\n" \
"s_waitcnt lgkmcnt(1)\n" \
"v_pk_add_f32 v[64:65], v[64:65], v[66:67]\n" \
"s_waitcnt lgkmcnt(0)\n" \
"v_pk_add_f32 v[68:69], v[68:69], v[70:71]\n" \
"v_pk_add_f32 v[64:65], v[64:65], v[68:69]\n" \
"v_cvt_pk_bf16_f32 v72, v64, v65\n" \
"v_max_u32 v72, 1, v72\n" \
"s_nop 1\n" \
"v_mov_b32_dpp v73, v72 quad_perm:[1,1,1,1] row_mask:0xf bank_mask:0xf\n" \
"v_mov_b32_dpp v74, v72 quad_perm:[2,2,2,2] row_mask:0xf bank_mask:0xf\n" \
"v_mov_b32_dpp v75, v72 quad_perm:[3,3,3,3] row_mask:0xf bank_mask:0xf\n" \
".Lv_skip_%=:\n" \
"buffer_store_dwordx4 v[72:75], v8, s[36:39], s77 offen sc1\n" \
"s_memrealtime s[24:25]\n" \
"s_mov_b32 s54, s58\n" \
"s_mov_b32 s58, s59\n" \
"s_mov_b32 s59, s60\n" \
"s_mov_b32 s60, s61\n" \
"s_mov_b32 s61, s54\n" \
"s_memrealtime s[26:27]\n" \
"s_add_u32 s78, s77, s63\n" \
"s_xor_b32 s89, s78, 0x4800\n" \
"s_xor_b32 s90, s78, 0x800\n" \
"s_xor_b32 s91, s78, 0x4000\n" \
"s_mov_b32 m0, s79\n" \
"s_memrealtime s[28:29]\n" \
"s_add_u32 s51, s50, s46\n" \
"v_mov_b64 v[80:81], 0\n" \
"v_mov_b64 v[82:83], 0\n" \
"v_mov_b32 v13, -1\n" \
"s_memrealtime s[30:31]\n" \
"s_add_u32 s49, s49, 1\n" \
"s_waitcnt lgkmcnt(0)\n" \
"s_cmp_lg_u32 s48, 0\n" \
"s_nop 0\n" \
"s_nop 0\n" \
"s_nop 0\n" \
"s_cbranch_scc1 .Lv_w4_%=\n" \
"s_branch .Lv_loop_%=\n" \
".Lv_h_cold_%=:\n" \
"s_nop 7\n" \
"s_nop 7\n" \
"s_waitcnt vmcnt(0) lgkmcnt(0)\n" \
"s_mov_b32 s53, 0\n" \
".Lv_h_again_%=:\n" \
"s_cmp_lg_u32 s52, 0\n" \
"s_cbranch_scc1 .Lv_h_ok_%=\n" \
"s_sleep 1\n" \
"buffer_load_dwordx2 v[64:65], v9, s[36:39], s75 offen sc1\n" \
"s_waitcnt vmcnt(0)\n" \
"v_cmp_eq_u32 vcc, 0, v64\n" \
"s_nop 0\n" \
"s_and_b64 vcc, vcc, s[80:81]\n" \
"s_nop 4\n" \
"s_cbranch_vccz .Lv_h_ok_%=\n" \
"s_add_u32 s53, s53, 1\n" \
"s_cmp_lt_u32 s53, 32768\n" \
"s_cbranch_scc1 .Lv_h_again_%=\n" \
"s_mov_b32 s52, 1\n" \
"v_mov_b32 v10, 256\n" \
"v_mov_b32 v11, 1\n" \
"buffer_store_dword v11, v10, s[36:39], 0 offen sc1\n" \
"s_waitcnt vmcnt(0) lgkmcnt(0)\n" \
"s_branch .Lv_h_ok_%=\n" \
".Lv_retry_%=:\n" \
"s_nop 7\n" \
"s_nop 7\n" \
"s_cmp_lg_u32 s52, 0\n" \
"s_cbranch_scc1 .Lv_dead_%=\n" \
"v_mov_b64 v[80:81], 0\n" \
"v_mov_b64 v[82:83], 0\n" \
"v_mov_b64 v[84:85], 0\n" \
"v_mov_b64 v[86:87], 0\n" \
"v_mov_b64 v[88:89], 0\n" \
"v_mov_b64 v[90:91], 0\n" \
"v_mov_b64 v[92:93], 0\n" \
"v_mov_b64 v[94:95], 0\n" \
"v_mov_b32 v13, -1\n" \
"s_mov_b32 s54, 100\n" \
"s_add_u32 s76, s76, 1\n" \
"s_cmp_lt_u32 s76, 32768\n" \
"s_cbranch_scc1 .Lv_o_%=\n" \
"s_mov_b32 s52, 1\n" \
"v_mov_b32 v13, 256\n" \
"v_mov_b32 v14, 1\n" \
"buffer_store_dword v14, v13, s[36:39], 0 offen sc1\n" \
".Lv_dead_%=:\n" \
"s_waitcnt vmcnt(0) lgkmcnt(0)\n" \
"s_branch .Lv_y2_%=\n" \
".Lv_mretry_%=:\n" \
"s_nop 7\n" \
"s_nop 7\n" \
"v_mov_b64 v[80:81], 0\n" \
"v_mov_b64 v[82:83], 0\n" \
"v_mov_b32 v13, -1\n" \
"s_mov_b32 m0, s79\n" \
"s_waitcnt vmcnt(0) lgkmcnt(0)\n" \
"s_barrier\n" \
"s_add_u32 s88, s88, 1\n" \
"s_cmp_lt_u32 s88, 32768\n" \
"s_cbranch_scc1 .Lv_r1_%=\n" \
"s_mov_b32 s52, 1\n" \
"v_mov_b32 v13, 256\n" \
"v_mov_b32 v14, 1\n" \
"buffer_store_dword v14, v13, s[36:39], 0 offen sc1\n" \
"s_waitcnt vmcnt(0) lgkmcnt(0)\n" \
"s_endpgm\n" \
".Lv_r1_%=:\n" \
"s_cmp_lg_u32 s49, 0\n" \
"s_cbranch_scc1 .Lv_r2_%=\n" \
"s_sleep 8\n" \
".Lv_r2_%=:\n" \
"s_branch .Lv_go_%=\n" \
".Lv_end_%=:\n" \
"s_nop 7\n" \
"s_nop 7\n" \
"s_waitcnt vmcnt(0) lgkmcnt(0)\n" \
"s_waitcnt vmcnt(0) lgkmcnt(0)\n" \
".Lv_fin_%=:\n" \
"s_endpgm\n"
#define ASM_S64_WO \
"s_nop 7\n" \
"s_nop 7\n" \
"s_lshl_b32 s52, s48, 15\n" \
"s_lshl_b32 s53, s48, 13\n" \
"s_add_u32 s53, s53, 98304\n" \
"s_sub_u32 s54, s53, s52\n" \
"v_add_u32 v9, s54, v1\n" \
"s_add_u32 m0, s52, 0\n" \
"s_mov_b32 s54, 0\n" \
"buffer_load_dwordx4 v0, s[40:43], s54 offen sc1 lds\n" \
"s_add_u32 m0, s52, 1024\n" \
"s_mov_b32 s54, 16384\n" \
"buffer_load_dwordx4 v0, s[40:43], s54 offen sc1 lds\n" \
"s_add_u32 m0, s52, 2048\n" \
"s_mov_b32 s54, 32768\n" \
"buffer_load_dwordx4 v0, s[40:43], s54 offen sc1 lds\n" \
"s_add_u32 m0, s52, 3072\n" \
"s_mov_b32 s54, 49152\n" \
"buffer_load_dwordx4 v0, s[40:43], s54 offen sc1 lds\n" \
"s_mov_b32 s54, s99\n" \
".Lswo_z0_%=:\n" \
"s_sleep 1\n" \
"s_sub_u32 s54, s54, 1\n" \
"s_cbranch_scc0 .Lswo_z0_%=\n" \
"s_add_u32 m0, s52, 4096\n" \
"s_mov_b32 s54, 65536\n" \
"buffer_load_dwordx4 v0, s[40:43], s54 offen sc1 lds\n" \
"s_add_u32 m0, s52, 5120\n" \
"s_mov_b32 s54, 81920\n" \
"buffer_load_dwordx4 v0, s[40:43], s54 offen sc1 lds\n" \
"s_add_u32 m0, s52, 6144\n" \
"s_mov_b32 s54, 98304\n" \
"buffer_load_dwordx4 v0, s[40:43], s54 offen sc1 lds\n" \
"s_add_u32 m0, s52, 7168\n" \
"s_mov_b32 s54, 114688\n" \
"buffer_load_dwordx4 v0, s[40:43], s54 offen sc1 lds\n" \
"s_mov_b32 s54, s99\n" \
".Lswo_z1_%=:\n" \
"s_sleep 1\n" \
"s_sub_u32 s54, s54, 1\n" \
"s_cbranch_scc0 .Lswo_z1_%=\n" \
"s_add_u32 m0, s52, 8192\n" \
"s_mov_b32 s54, 131072\n" \
"buffer_load_dwordx4 v0, s[40:43], s54 offen sc1 lds\n" \
"s_add_u32 m0, s52, 9216\n" \
"s_mov_b32 s54, 147456\n" \
"buffer_load_dwordx4 v0, s[40:43], s54 offen sc1 lds\n" \
"s_add_u32 m0, s52, 10240\n" \
"s_mov_b32 s54, 163840\n" \
"buffer_load_dwordx4 v0, s[40:43], s54 offen sc1 lds\n" \
"s_add_u32 m0, s52, 11264\n" \
"s_mov_b32 s54, 180224\n" \
"buffer_load_dwordx4 v0, s[40:43], s54 offen sc1 lds\n" \
"s_mov_b32 s54, s99\n" \
".Lswo_z2_%=:\n" \
"s_sleep 1\n" \
"s_sub_u32 s54, s54, 1\n" \
"s_cbranch_scc0 .Lswo_z2_%=\n" \
"s_add_u32 m0, s52, 12288\n" \
"s_mov_b32 s54, 196608\n" \
"buffer_load_dwordx4 v0, s[40:43], s54 offen sc1 lds\n" \
"s_add_u32 m0, s52, 13312\n" \
"s_mov_b32 s54, 212992\n" \
"buffer_load_dwordx4 v0, s[40:43], s54 offen sc1 lds\n" \
"s_add_u32 m0, s52, 14336\n" \
"s_mov_b32 s54, 229376\n" \
"buffer_load_dwordx4 v0, s[40:43], s54 offen sc1 lds\n" \
"s_add_u32 m0, s52, 15360\n" \
"s_mov_b32 s54, 245760\n" \
"buffer_load_dwordx4 v0, s[40:43], s54 offen sc1 lds\n" \
"s_mov_b32 s54, s99\n" \
".Lswo_z3_%=:\n" \
"s_sleep 1\n" \
"s_sub_u32 s54, s54, 1\n" \
"s_cbranch_scc0 .Lswo_z3_%=\n" \
"s_add_u32 m0, s52, 16384\n" \
"s_mov_b32 s54, 262144\n" \
"buffer_load_dwordx4 v0, s[40:43], s54 offen sc1 lds\n" \
"s_add_u32 m0, s52, 17408\n" \
"s_mov_b32 s54, 278528\n" \
"buffer_load_dwordx4 v0, s[40:43], s54 offen sc1 lds\n" \
"s_add_u32 m0, s52, 18432\n" \
"s_mov_b32 s54, 294912\n" \
"buffer_load_dwordx4 v0, s[40:43], s54 offen sc1 lds\n" \
"s_add_u32 m0, s52, 19456\n" \
"s_mov_b32 s54, 311296\n" \
"buffer_load_dwordx4 v0, s[40:43], s54 offen sc1 lds\n" \
"s_mov_b32 s54, s99\n" \
".Lswo_z4_%=:\n" \
"s_sleep 1\n" \
"s_sub_u32 s54, s54, 1\n" \
"s_cbranch_scc0 .Lswo_z4_%=\n" \
"s_add_u32 m0, s52, 20480\n" \
"s_mov_b32 s54, 327680\n" \
"buffer_load_dwordx4 v0, s[40:43], s54 offen sc1 lds\n" \
"s_add_u32 m0, s52, 21504\n" \
"s_mov_b32 s54, 344064\n" \
"buffer_load_dwordx4 v0, s[40:43], s54 offen sc1 lds\n" \
"s_add_u32 m0, s52, 22528\n" \
"s_mov_b32 s54, 360448\n" \
"buffer_load_dwordx4 v0, s[40:43], s54 offen sc1 lds\n" \
"s_add_u32 m0, s52, 23552\n" \
"s_mov_b32 s54, 376832\n" \
"buffer_load_dwordx4 v0, s[40:43], s54 offen sc1 lds\n" \
"s_mov_b32 s54, s99\n" \
".Lswo_z5_%=:\n" \
"s_sleep 1\n" \
"s_sub_u32 s54, s54, 1\n" \
"s_cbranch_scc0 .Lswo_z5_%=\n" \
"s_add_u32 m0, s52, 24576\n" \
"s_mov_b32 s54, 393216\n" \
"buffer_load_dwordx4 v0, s[40:43], s54 offen sc1 lds\n" \
"s_add_u32 m0, s52, 25600\n" \
"s_mov_b32 s54, 409600\n" \
"buffer_load_dwordx4 v0, s[40:43], s54 offen sc1 lds\n" \
"s_add_u32 m0, s52, 26624\n" \
"s_mov_b32 s54, 425984\n" \
"buffer_load_dwordx4 v0, s[40:43], s54 offen sc1 lds\n" \
"s_add_u32 m0, s52, 27648\n" \
"s_mov_b32 s54, 442368\n" \
"buffer_load_dwordx4 v0, s[40:43], s54 offen sc1 lds\n" \
"s_mov_b32 s54, s99\n" \
".Lswo_z6_%=:\n" \
"s_sleep 1\n" \
"s_sub_u32 s54, s54, 1\n" \
"s_cbranch_scc0 .Lswo_z6_%=\n" \
"s_add_u32 m0, s52, 28672\n" \
"s_mov_b32 s54, 458752\n" \
"buffer_load_dwordx4 v0, s[40:43], s54 offen sc1 lds\n" \
"s_add_u32 m0, s52, 29696\n" \
"s_mov_b32 s54, 475136\n" \
"buffer_load_dwordx4 v0, s[40:43], s54 offen sc1 lds\n" \
"s_add_u32 m0, s52, 30720\n" \
"s_mov_b32 s54, 491520\n" \
"buffer_load_dwordx4 v0, s[40:43], s54 offen sc1 lds\n" \
"s_add_u32 m0, s52, 31744\n" \
"s_mov_b32 s54, 507904\n" \
"buffer_load_dwordx4 v0, s[40:43], s54 offen sc1 lds\n" \
"s_mov_b32 s54, s99\n" \
".Lswo_z7_%=:\n" \
"s_sleep 1\n" \
"s_sub_u32 s54, s54, 1\n" \
"s_cbranch_scc0 .Lswo_z7_%=\n" \
"s_add_u32 m0, s53, 32768\n" \
"s_mov_b32 s54, 524288\n" \
"buffer_load_dwordx4 v0, s[40:43], s54 offen sc1 lds\n" \
"s_add_u32 m0, s53, 33792\n" \
"s_mov_b32 s54, 540672\n" \
"buffer_load_dwordx4 v0, s[40:43], s54 offen sc1 lds\n" \
"s_add_u32 m0, s53, 34816\n" \
"s_mov_b32 s54, 557056\n" \
"buffer_load_dwordx4 v0, s[40:43], s54 offen sc1 lds\n" \
"s_add_u32 m0, s53, 35840\n" \
"s_mov_b32 s54, 573440\n" \
"buffer_load_dwordx4 v0, s[40:43], s54 offen sc1 lds\n" \
"s_mov_b32 s54, s99\n" \
".Lswo_z8_%=:\n" \
"s_sleep 1\n" \
"s_sub_u32 s54, s54, 1\n" \
"s_cbranch_scc0 .Lswo_z8_%=\n" \
"s_add_u32 m0, s53, 36864\n" \
"s_mov_b32 s54, 589824\n" \
"buffer_load_dwordx4 v0, s[40:43], s54 offen sc1 lds\n" \
"s_add_u32 m0, s53, 37888\n" \
"s_mov_b32 s54, 606208\n" \
"buffer_load_dwordx4 v0, s[40:43], s54 offen sc1 lds\n" \
"s_add_u32 m0, s53, 38912\n" \
"s_mov_b32 s54, 622592\n" \
"buffer_load_dwordx4 v0, s[40:43], s54 offen sc1 lds\n" \
"s_add_u32 m0, s53, 39936\n" \
"s_mov_b32 s54, 638976\n" \
"buffer_load_dwordx4 v0, s[40:43], s54 offen sc1 lds\n" \
"s_mov_b32 s54, s99\n" \
".Lswo_z9_%=:\n" \
"s_sleep 1\n" \
"s_sub_u32 s54, s54, 1\n" \
"s_cbranch_scc0 .Lswo_z9_%=\n" \
"s_waitcnt vmcnt(36)\n" \
"ds_read_b64_tr_b16 a[0:1], v1 offset:0\n" \
"ds_read_b64_tr_b16 a[2:3], v1 offset:2048\n" \
"ds_read_b64_tr_b16 a[128:129], v1 offset:32\n" \
"ds_read_b64_tr_b16 a[130:131], v1 offset:2080\n" \
"ds_read_b64_tr_b16 v[128:129], v1 offset:64\n" \
"ds_read_b64_tr_b16 v[130:131], v1 offset:2112\n" \
"ds_read_b64_tr_b16 v[16:17], v1 offset:96\n" \
"ds_read_b64_tr_b16 v[18:19], v1 offset:2144\n" \
"s_waitcnt lgkmcnt(0)\n" \
"s_add_u32 m0, s52, 0\n" \
"s_mov_b32 s54, 655360\n" \
"buffer_load_dwordx4 v0, s[40:43], s54 offen sc1 lds\n" \
"s_add_u32 m0, s52, 1024\n" \
"s_mov_b32 s54, 671744\n" \
"buffer_load_dwordx4 v0, s[40:43], s54 offen sc1 lds\n" \
"s_add_u32 m0, s52, 2048\n" \
"s_mov_b32 s54, 688128\n" \
"buffer_load_dwordx4 v0, s[40:43], s54 offen sc1 lds\n" \
"s_add_u32 m0, s52, 3072\n" \
"s_mov_b32 s54, 704512\n" \
"buffer_load_dwordx4 v0, s[40:43], s54 offen sc1 lds\n" \
"s_mov_b32 s54, s99\n" \
".Lswo_z10_%=:\n" \
"s_sleep 1\n" \
"s_sub_u32 s54, s54, 1\n" \
"s_cbranch_scc0 .Lswo_z10_%=\n" \
"s_waitcnt vmcnt(36)\n" \
"ds_read_b64_tr_b16 a[4:5], v1 offset:4096\n" \
"ds_read_b64_tr_b16 a[6:7], v1 offset:6144\n" \
"ds_read_b64_tr_b16 a[132:133], v1 offset:4128\n" \
"ds_read_b64_tr_b16 a[134:135], v1 offset:6176\n" \
"ds_read_b64_tr_b16 v[132:133], v1 offset:4160\n" \
"ds_read_b64_tr_b16 v[134:135], v1 offset:6208\n" \
"ds_read_b64_tr_b16 v[20:21], v1 offset:4192\n" \
"ds_read_b64_tr_b16 v[22:23], v1 offset:6240\n" \
"s_waitcnt lgkmcnt(0)\n" \
"s_add_u32 m0, s52, 4096\n" \
"s_mov_b32 s54, 720896\n" \
"buffer_load_dwordx4 v0, s[40:43], s54 offen sc1 lds\n" \
"s_add_u32 m0, s52, 5120\n" \
"s_mov_b32 s54, 737280\n" \
"buffer_load_dwordx4 v0, s[40:43], s54 offen sc1 lds\n" \
"s_add_u32 m0, s52, 6144\n" \
"s_mov_b32 s54, 753664\n" \
"buffer_load_dwordx4 v0, s[40:43], s54 offen sc1 lds\n" \
"s_add_u32 m0, s52, 7168\n" \
"s_mov_b32 s54, 770048\n" \
"buffer_load_dwordx4 v0, s[40:43], s54 offen sc1 lds\n" \
"s_mov_b32 s54, s99\n" \
".Lswo_z11_%=:\n" \
"s_sleep 1\n" \
"s_sub_u32 s54, s54, 1\n" \
"s_cbranch_scc0 .Lswo_z11_%=\n" \
"s_waitcnt vmcnt(36)\n" \
"ds_read_b64_tr_b16 a[8:9], v1 offset:8192\n" \
"ds_read_b64_tr_b16 a[10:11], v1 offset:10240\n" \
"ds_read_b64_tr_b16 a[136:137], v1 offset:8224\n" \
"ds_read_b64_tr_b16 a[138:139], v1 offset:10272\n" \
"ds_read_b64_tr_b16 v[136:137], v1 offset:8256\n" \
"ds_read_b64_tr_b16 v[138:139], v1 offset:10304\n" \
"ds_read_b64_tr_b16 v[24:25], v1 offset:8288\n" \
"ds_read_b64_tr_b16 v[26:27], v1 offset:10336\n" \
"s_waitcnt lgkmcnt(0)\n" \
"s_add_u32 m0, s52, 8192\n" \
"s_mov_b32 s54, 786432\n" \
"buffer_load_dwordx4 v0, s[40:43], s54 offen sc1 lds\n" \
"s_add_u32 m0, s52, 9216\n" \
"s_mov_b32 s54, 802816\n" \
"buffer_load_dwordx4 v0, s[40:43], s54 offen sc1 lds\n" \
"s_add_u32 m0, s52, 10240\n" \
"s_mov_b32 s54, 819200\n" \
"buffer_load_dwordx4 v0, s[40:43], s54 offen sc1 lds\n" \
"s_add_u32 m0, s52, 11264\n" \
"s_mov_b32 s54, 835584\n" \
"buffer_load_dwordx4 v0, s[40:43], s54 offen sc1 lds\n" \
"s_mov_b32 s54, s99\n" \
".Lswo_z12_%=:\n" \
"s_sleep 1\n" \
"s_sub_u32 s54, s54, 1\n" \
"s_cbranch_scc0 .Lswo_z12_%=\n" \
"s_waitcnt vmcnt(36)\n" \
"ds_read_b64_tr_b16 a[12:13], v1 offset:12288\n" \
"ds_read_b64_tr_b16 a[14:15], v1 offset:14336\n" \
"ds_read_b64_tr_b16 a[140:141], v1 offset:12320\n" \
"ds_read_b64_tr_b16 a[142:143], v1 offset:14368\n" \
"ds_read_b64_tr_b16 v[140:141], v1 offset:12352\n" \
"ds_read_b64_tr_b16 v[142:143], v1 offset:14400\n" \
"ds_read_b64_tr_b16 v[28:29], v1 offset:12384\n" \
"ds_read_b64_tr_b16 v[30:31], v1 offset:14432\n" \
"s_waitcnt lgkmcnt(0)\n" \
"s_add_u32 m0, s52, 12288\n" \
"s_mov_b32 s54, 851968\n" \
"buffer_load_dwordx4 v0, s[40:43], s54 offen sc1 lds\n" \
"s_add_u32 m0, s52, 13312\n" \
"s_mov_b32 s54, 868352\n" \
"buffer_load_dwordx4 v0, s[40:43], s54 offen sc1 lds\n" \
"s_add_u32 m0, s52, 14336\n" \
"s_mov_b32 s54, 884736\n" \
"buffer_load_dwordx4 v0, s[40:43], s54 offen sc1 lds\n" \
"s_add_u32 m0, s52, 15360\n" \
"s_mov_b32 s54, 901120\n" \
"buffer_load_dwordx4 v0, s[40:43], s54 offen sc1 lds\n" \
"s_mov_b32 s54, s99\n" \
".Lswo_z13_%=:\n" \
"s_sleep 1\n" \
"s_sub_u32 s54, s54, 1\n" \
"s_cbranch_scc0 .Lswo_z13_%=\n" \
"s_waitcnt vmcnt(36)\n" \
"ds_read_b64_tr_b16 a[16:17], v1 offset:16384\n" \
"ds_read_b64_tr_b16 a[18:19], v1 offset:18432\n" \
"ds_read_b64_tr_b16 a[144:145], v1 offset:16416\n" \
"ds_read_b64_tr_b16 a[146:147], v1 offset:18464\n" \
"ds_read_b64_tr_b16 v[144:145], v1 offset:16448\n" \
"ds_read_b64_tr_b16 v[146:147], v1 offset:18496\n" \
"ds_read_b64_tr_b16 v[32:33], v1 offset:16480\n" \
"ds_read_b64_tr_b16 v[34:35], v1 offset:18528\n" \
"s_waitcnt lgkmcnt(0)\n" \
"s_add_u32 m0, s52, 16384\n" \
"s_mov_b32 s54, 917504\n" \
"buffer_load_dwordx4 v0, s[40:43], s54 offen sc1 lds\n" \
"s_add_u32 m0, s52, 17408\n" \
"s_mov_b32 s54, 933888\n" \
"buffer_load_dwordx4 v0, s[40:43], s54 offen sc1 lds\n" \
"s_add_u32 m0, s52, 18432\n" \
"s_mov_b32 s54, 950272\n" \
"buffer_load_dwordx4 v0, s[40:43], s54 offen sc1 lds\n" \
"s_add_u32 m0, s52, 19456\n" \
"s_mov_b32 s54, 966656\n" \
"buffer_load_dwordx4 v0, s[40:43], s54 offen sc1 lds\n" \
"s_mov_b32 s54, s99\n" \
".Lswo_z14_%=:\n" \
"s_sleep 1\n" \
"s_sub_u32 s54, s54, 1\n" \
"s_cbranch_scc0 .Lswo_z14_%=\n" \
"s_waitcnt vmcnt(36)\n" \
"ds_read_b64_tr_b16 a[20:21], v1 offset:20480\n" \
"ds_read_b64_tr_b16 a[22:23], v1 offset:22528\n" \
"ds_read_b64_tr_b16 a[148:149], v1 offset:20512\n" \
"ds_read_b64_tr_b16 a[150:151], v1 offset:22560\n" \
"ds_read_b64_tr_b16 v[148:149], v1 offset:20544\n" \
"ds_read_b64_tr_b16 v[150:151], v1 offset:22592\n" \
"ds_read_b64_tr_b16 v[36:37], v1 offset:20576\n" \
"ds_read_b64_tr_b16 v[38:39], v1 offset:22624\n" \
"s_waitcnt lgkmcnt(0)\n" \
"s_add_u32 m0, s52, 20480\n" \
"s_mov_b32 s54, 983040\n" \
"buffer_load_dwordx4 v0, s[40:43], s54 offen sc1 lds\n" \
"s_add_u32 m0, s52, 21504\n" \
"s_mov_b32 s54, 999424\n" \
"buffer_load_dwordx4 v0, s[40:43], s54 offen sc1 lds\n" \
"s_add_u32 m0, s52, 22528\n" \
"s_mov_b32 s54, 1015808\n" \
"buffer_load_dwordx4 v0, s[40:43], s54 offen sc1 lds\n" \
"s_add_u32 m0, s52, 23552\n" \
"s_mov_b32 s54, 1032192\n" \
"buffer_load_dwordx4 v0, s[40:43], s54 offen sc1 lds\n" \
"s_mov_b32 s54, s99\n" \
".Lswo_z15_%=:\n" \
"s_sleep 1\n" \
"s_sub_u32 s54, s54, 1\n" \
"s_cbranch_scc0 .Lswo_z15_%=\n" \
"s_waitcnt vmcnt(36)\n" \
"ds_read_b64_tr_b16 a[24:25], v1 offset:24576\n" \
"ds_read_b64_tr_b16 a[26:27], v1 offset:26624\n" \
"ds_read_b64_tr_b16 a[152:153], v1 offset:24608\n" \
"ds_read_b64_tr_b16 a[154:155], v1 offset:26656\n" \
"ds_read_b64_tr_b16 v[152:153], v1 offset:24640\n" \
"ds_read_b64_tr_b16 v[154:155], v1 offset:26688\n" \
"ds_read_b64_tr_b16 v[40:41], v1 offset:24672\n" \
"ds_read_b64_tr_b16 v[42:43], v1 offset:26720\n" \
"s_waitcnt lgkmcnt(0)\n" \
"s_add_u32 m0, s52, 24576\n" \
"s_mov_b32 s54, 1048576\n" \
"buffer_load_dwordx4 v0, s[40:43], s54 offen sc1 lds\n" \
"s_add_u32 m0, s52, 25600\n" \
"s_mov_b32 s54, 1064960\n" \
"buffer_load_dwordx4 v0, s[40:43], s54 offen sc1 lds\n" \
"s_add_u32 m0, s52, 26624\n" \
"s_mov_b32 s54, 1081344\n" \
"buffer_load_dwordx4 v0, s[40:43], s54 offen sc1 lds\n" \
"s_add_u32 m0, s52, 27648\n" \
"s_mov_b32 s54, 1097728\n" \
"buffer_load_dwordx4 v0, s[40:43], s54 offen sc1 lds\n" \
"s_mov_b32 s54, s99\n" \
".Lswo_z16_%=:\n" \
"s_sleep 1\n" \
"s_sub_u32 s54, s54, 1\n" \
"s_cbranch_scc0 .Lswo_z16_%=\n" \
"s_waitcnt vmcnt(36)\n" \
"ds_read_b64_tr_b16 a[28:29], v1 offset:28672\n" \
"ds_read_b64_tr_b16 a[30:31], v1 offset:30720\n" \
"ds_read_b64_tr_b16 a[156:157], v1 offset:28704\n" \
"ds_read_b64_tr_b16 a[158:159], v1 offset:30752\n" \
"ds_read_b64_tr_b16 v[156:157], v1 offset:28736\n" \
"ds_read_b64_tr_b16 v[158:159], v1 offset:30784\n" \
"ds_read_b64_tr_b16 v[44:45], v1 offset:28768\n" \
"ds_read_b64_tr_b16 v[46:47], v1 offset:30816\n" \
"s_waitcnt lgkmcnt(0)\n" \
"s_add_u32 m0, s52, 28672\n" \
"s_mov_b32 s54, 1114112\n" \
"buffer_load_dwordx4 v0, s[40:43], s54 offen sc1 lds\n" \
"s_add_u32 m0, s52, 29696\n" \
"s_mov_b32 s54, 1130496\n" \
"buffer_load_dwordx4 v0, s[40:43], s54 offen sc1 lds\n" \
"s_add_u32 m0, s52, 30720\n" \
"s_mov_b32 s54, 1146880\n" \
"buffer_load_dwordx4 v0, s[40:43], s54 offen sc1 lds\n" \
"s_add_u32 m0, s52, 31744\n" \
"s_mov_b32 s54, 1163264\n" \
"buffer_load_dwordx4 v0, s[40:43], s54 offen sc1 lds\n" \
"s_mov_b32 s54, s99\n" \
".Lswo_z17_%=:\n" \
"s_sleep 1\n" \
"s_sub_u32 s54, s54, 1\n" \
"s_cbranch_scc0 .Lswo_z17_%=\n" \
"s_waitcnt vmcnt(36)\n" \
"ds_read_b64_tr_b16 a[32:33], v9 offset:32768\n" \
"ds_read_b64_tr_b16 a[34:35], v9 offset:34816\n" \
"ds_read_b64_tr_b16 a[160:161], v9 offset:32800\n" \
"ds_read_b64_tr_b16 a[162:163], v9 offset:34848\n" \
"ds_read_b64_tr_b16 v[160:161], v9 offset:32832\n" \
"ds_read_b64_tr_b16 v[162:163], v9 offset:34880\n" \
"ds_read_b64_tr_b16 v[48:49], v9 offset:32864\n" \
"ds_read_b64_tr_b16 v[50:51], v9 offset:34912\n" \
"s_waitcnt lgkmcnt(0)\n" \
"s_add_u32 m0, s53, 32768\n" \
"s_mov_b32 s54, 1179648\n" \
"buffer_load_dwordx4 v0, s[40:43], s54 offen sc1 lds\n" \
"s_add_u32 m0, s53, 33792\n" \
"s_mov_b32 s54, 1196032\n" \
"buffer_load_dwordx4 v0, s[40:43], s54 offen sc1 lds\n" \
"s_add_u32 m0, s53, 34816\n" \
"s_mov_b32 s54, 1212416\n" \
"buffer_load_dwordx4 v0, s[40:43], s54 offen sc1 lds\n" \
"s_add_u32 m0, s53, 35840\n" \
"s_mov_b32 s54, 1228800\n" \
"buffer_load_dwordx4 v0, s[40:43], s54 offen sc1 lds\n" \
"s_mov_b32 s54, s99\n" \
".Lswo_z18_%=:\n" \
"s_sleep 1\n" \
"s_sub_u32 s54, s54, 1\n" \
"s_cbranch_scc0 .Lswo_z18_%=\n" \
"s_waitcnt vmcnt(36)\n" \
"ds_read_b64_tr_b16 a[36:37], v9 offset:36864\n" \
"ds_read_b64_tr_b16 a[38:39], v9 offset:38912\n" \
"ds_read_b64_tr_b16 a[164:165], v9 offset:36896\n" \
"ds_read_b64_tr_b16 a[166:167], v9 offset:38944\n" \
"ds_read_b64_tr_b16 v[164:165], v9 offset:36928\n" \
"ds_read_b64_tr_b16 v[166:167], v9 offset:38976\n" \
"ds_read_b64_tr_b16 v[52:53], v9 offset:36960\n" \
"ds_read_b64_tr_b16 v[54:55], v9 offset:39008\n" \
"s_waitcnt lgkmcnt(0)\n" \
"s_add_u32 m0, s53, 36864\n" \
"s_mov_b32 s54, 1245184\n" \
"buffer_load_dwordx4 v0, s[40:43], s54 offen sc1 lds\n" \
"s_add_u32 m0, s53, 37888\n" \
"s_mov_b32 s54, 1261568\n" \
"buffer_load_dwordx4 v0, s[40:43], s54 offen sc1 lds\n" \
"s_add_u32 m0, s53, 38912\n" \
"s_mov_b32 s54, 1277952\n" \
"buffer_load_dwordx4 v0, s[40:43], s54 offen sc1 lds\n" \
"s_add_u32 m0, s53, 39936\n" \
"s_mov_b32 s54, 1294336\n" \
"buffer_load_dwordx4 v0, s[40:43], s54 offen sc1 lds\n" \
"s_mov_b32 s54, s99\n" \
".Lswo_z19_%=:\n" \
"s_sleep 1\n" \
"s_sub_u32 s54, s54, 1\n" \
"s_cbranch_scc0 .Lswo_z19_%=\n" \
"s_waitcnt vmcnt(36)\n" \
"ds_read_b64_tr_b16 a[40:41], v1 offset:0\n" \
"ds_read_b64_tr_b16 a[42:43], v1 offset:2048\n" \
"ds_read_b64_tr_b16 a[168:169], v1 offset:32\n" \
"ds_read_b64_tr_b16 a[170:171], v1 offset:2080\n" \
"ds_read_b64_tr_b16 v[168:169], v1 offset:64\n" \
"ds_read_b64_tr_b16 v[170:171], v1 offset:2112\n" \
"ds_read_b64_tr_b16 v[56:57], v1 offset:96\n" \
"ds_read_b64_tr_b16 v[58:59], v1 offset:2144\n" \
"s_waitcnt lgkmcnt(0)\n" \
"s_add_u32 m0, s52, 0\n" \
"s_mov_b32 s54, 1310720\n" \
"buffer_load_dwordx4 v0, s[40:43], s54 offen sc1 lds\n" \
"s_add_u32 m0, s52, 1024\n" \
"s_mov_b32 s54, 1327104\n" \
"buffer_load_dwordx4 v0, s[40:43], s54 offen sc1 lds\n" \
"s_add_u32 m0, s52, 2048\n" \
"s_mov_b32 s54, 1343488\n" \
"buffer_load_dwordx4 v0, s[40:43], s54 offen sc1 lds\n" \
"s_add_u32 m0, s52, 3072\n" \
"s_mov_b32 s54, 1359872\n" \
"buffer_load_dwordx4 v0, s[40:43], s54 offen sc1 lds\n" \
"s_mov_b32 s54, s99\n" \
".Lswo_z20_%=:\n" \
"s_sleep 1\n" \
"s_sub_u32 s54, s54, 1\n" \
"s_cbranch_scc0 .Lswo_z20_%=\n" \
"s_waitcnt vmcnt(36)\n" \
"ds_read_b64_tr_b16 a[44:45], v1 offset:4096\n" \
"ds_read_b64_tr_b16 a[46:47], v1 offset:6144\n" \
"ds_read_b64_tr_b16 a[172:173], v1 offset:4128\n" \
"ds_read_b64_tr_b16 a[174:175], v1 offset:6176\n" \
"ds_read_b64_tr_b16 v[172:173], v1 offset:4160\n" \
"ds_read_b64_tr_b16 v[174:175], v1 offset:6208\n" \
"ds_read_b64_tr_b16 v[60:61], v1 offset:4192\n" \
"ds_read_b64_tr_b16 v[62:63], v1 offset:6240\n" \
"s_waitcnt lgkmcnt(0)\n" \
"s_add_u32 m0, s52, 4096\n" \
"s_mov_b32 s54, 1376256\n" \
"buffer_load_dwordx4 v0, s[40:43], s54 offen sc1 lds\n" \
"s_add_u32 m0, s52, 5120\n" \
"s_mov_b32 s54, 1392640\n" \
"buffer_load_dwordx4 v0, s[40:43], s54 offen sc1 lds\n" \
"s_add_u32 m0, s52, 6144\n" \
"s_mov_b32 s54, 1409024\n" \
"buffer_load_dwordx4 v0, s[40:43], s54 offen sc1 lds\n" \
"s_add_u32 m0, s52, 7168\n" \
"s_mov_b32 s54, 1425408\n" \
"buffer_load_dwordx4 v0, s[40:43], s54 offen sc1 lds\n" \
"s_mov_b32 s54, s99\n" \
".Lswo_z21_%=:\n" \
"s_sleep 1\n" \
"s_sub_u32 s54, s54, 1\n" \
"s_cbranch_scc0 .Lswo_z21_%=\n" \
"s_waitcnt vmcnt(36)\n" \
"ds_read_b64_tr_b16 a[48:49], v1 offset:8192\n" \
"ds_read_b64_tr_b16 a[50:51], v1 offset:10240\n" \
"ds_read_b64_tr_b16 a[176:177], v1 offset:8224\n" \
"ds_read_b64_tr_b16 a[178:179], v1 offset:10272\n" \
"ds_read_b64_tr_b16 v[176:177], v1 offset:8256\n" \
"ds_read_b64_tr_b16 v[178:179], v1 offset:10304\n" \
"ds_read_b64_tr_b16 v[64:65], v1 offset:8288\n" \
"ds_read_b64_tr_b16 v[66:67], v1 offset:10336\n" \
"s_waitcnt lgkmcnt(0)\n" \
"s_add_u32 m0, s52, 8192\n" \
"s_mov_b32 s54, 1441792\n" \
"buffer_load_dwordx4 v0, s[40:43], s54 offen sc1 lds\n" \
"s_add_u32 m0, s52, 9216\n" \
"s_mov_b32 s54, 1458176\n" \
"buffer_load_dwordx4 v0, s[40:43], s54 offen sc1 lds\n" \
"s_add_u32 m0, s52, 10240\n" \
"s_mov_b32 s54, 1474560\n" \
"buffer_load_dwordx4 v0, s[40:43], s54 offen sc1 lds\n" \
"s_add_u32 m0, s52, 11264\n" \
"s_mov_b32 s54, 1490944\n" \
"buffer_load_dwordx4 v0, s[40:43], s54 offen sc1 lds\n" \
"s_mov_b32 s54, s99\n" \
".Lswo_z22_%=:\n" \
"s_sleep 1\n" \
"s_sub_u32 s54, s54, 1\n" \
"s_cbranch_scc0 .Lswo_z22_%=\n" \
"s_waitcnt vmcnt(36)\n" \
"ds_read_b64_tr_b16 a[52:53], v1 offset:12288\n" \
"ds_read_b64_tr_b16 a[54:55], v1 offset:14336\n" \
"ds_read_b64_tr_b16 a[180:181], v1 offset:12320\n" \
"ds_read_b64_tr_b16 a[182:183], v1 offset:14368\n" \
"ds_read_b64_tr_b16 v[180:181], v1 offset:12352\n" \
"ds_read_b64_tr_b16 v[182:183], v1 offset:14400\n" \
"ds_read_b64_tr_b16 v[68:69], v1 offset:12384\n" \
"ds_read_b64_tr_b16 v[70:71], v1 offset:14432\n" \
"s_waitcnt lgkmcnt(0)\n" \
"s_add_u32 m0, s52, 12288\n" \
"s_mov_b32 s54, 1507328\n" \
"buffer_load_dwordx4 v0, s[40:43], s54 offen sc1 lds\n" \
"s_add_u32 m0, s52, 13312\n" \
"s_mov_b32 s54, 1523712\n" \
"buffer_load_dwordx4 v0, s[40:43], s54 offen sc1 lds\n" \
"s_add_u32 m0, s52, 14336\n" \
"s_mov_b32 s54, 1540096\n" \
"buffer_load_dwordx4 v0, s[40:43], s54 offen sc1 lds\n" \
"s_add_u32 m0, s52, 15360\n" \
"s_mov_b32 s54, 1556480\n" \
"buffer_load_dwordx4 v0, s[40:43], s54 offen sc1 lds\n" \
"s_mov_b32 s54, s99\n" \
".Lswo_z23_%=:\n" \
"s_sleep 1\n" \
"s_sub_u32 s54, s54, 1\n" \
"s_cbranch_scc0 .Lswo_z23_%=\n" \
"s_waitcnt vmcnt(36)\n" \
"ds_read_b64_tr_b16 a[56:57], v1 offset:16384\n" \
"ds_read_b64_tr_b16 a[58:59], v1 offset:18432\n" \
"ds_read_b64_tr_b16 a[184:185], v1 offset:16416\n" \
"ds_read_b64_tr_b16 a[186:187], v1 offset:18464\n" \
"ds_read_b64_tr_b16 v[184:185], v1 offset:16448\n" \
"ds_read_b64_tr_b16 v[186:187], v1 offset:18496\n" \
"ds_read_b64_tr_b16 v[72:73], v1 offset:16480\n" \
"ds_read_b64_tr_b16 v[74:75], v1 offset:18528\n" \
"s_waitcnt lgkmcnt(0)\n" \
"s_add_u32 m0, s52, 16384\n" \
"s_mov_b32 s54, 1572864\n" \
"buffer_load_dwordx4 v0, s[40:43], s54 offen sc1 lds\n" \
"s_add_u32 m0, s52, 17408\n" \
"s_mov_b32 s54, 1589248\n" \
"buffer_load_dwordx4 v0, s[40:43], s54 offen sc1 lds\n" \
"s_add_u32 m0, s52, 18432\n" \
"s_mov_b32 s54, 1605632\n" \
"buffer_load_dwordx4 v0, s[40:43], s54 offen sc1 lds\n" \
"s_add_u32 m0, s52, 19456\n" \
"s_mov_b32 s54, 1622016\n" \
"buffer_load_dwordx4 v0, s[40:43], s54 offen sc1 lds\n" \
"s_mov_b32 s54, s99\n" \
".Lswo_z24_%=:\n" \
"s_sleep 1\n" \
"s_sub_u32 s54, s54, 1\n" \
"s_cbranch_scc0 .Lswo_z24_%=\n" \
"s_waitcnt vmcnt(36)\n" \
"ds_read_b64_tr_b16 a[60:61], v1 offset:20480\n" \
"ds_read_b64_tr_b16 a[62:63], v1 offset:22528\n" \
"ds_read_b64_tr_b16 a[188:189], v1 offset:20512\n" \
"ds_read_b64_tr_b16 a[190:191], v1 offset:22560\n" \
"ds_read_b64_tr_b16 v[188:189], v1 offset:20544\n" \
"ds_read_b64_tr_b16 v[190:191], v1 offset:22592\n" \
"ds_read_b64_tr_b16 v[76:77], v1 offset:20576\n" \
"ds_read_b64_tr_b16 v[78:79], v1 offset:22624\n" \
"s_waitcnt lgkmcnt(0)\n" \
"s_add_u32 m0, s52, 20480\n" \
"s_mov_b32 s54, 1638400\n" \
"buffer_load_dwordx4 v0, s[40:43], s54 offen sc1 lds\n" \
"s_add_u32 m0, s52, 21504\n" \
"s_mov_b32 s54, 1654784\n" \
"buffer_load_dwordx4 v0, s[40:43], s54 offen sc1 lds\n" \
"s_add_u32 m0, s52, 22528\n" \
"s_mov_b32 s54, 1671168\n" \
"buffer_load_dwordx4 v0, s[40:43], s54 offen sc1 lds\n" \
"s_add_u32 m0, s52, 23552\n" \
"s_mov_b32 s54, 1687552\n" \
"buffer_load_dwordx4 v0, s[40:43], s54 offen sc1 lds\n" \
"s_mov_b32 s54, s99\n" \
".Lswo_z25_%=:\n" \
"s_sleep 1\n" \
"s_sub_u32 s54, s54, 1\n" \
"s_cbranch_scc0 .Lswo_z25_%=\n" \
"s_waitcnt vmcnt(36)\n" \
"ds_read_b64_tr_b16 a[64:65], v1 offset:24576\n" \
"ds_read_b64_tr_b16 a[66:67], v1 offset:26624\n" \
"ds_read_b64_tr_b16 a[192:193], v1 offset:24608\n" \
"ds_read_b64_tr_b16 a[194:195], v1 offset:26656\n" \
"ds_read_b64_tr_b16 v[192:193], v1 offset:24640\n" \
"ds_read_b64_tr_b16 v[194:195], v1 offset:26688\n" \
"ds_read_b64_tr_b16 v[80:81], v1 offset:24672\n" \
"ds_read_b64_tr_b16 v[82:83], v1 offset:26720\n" \
"s_waitcnt lgkmcnt(0)\n" \
"s_add_u32 m0, s52, 24576\n" \
"s_mov_b32 s54, 1703936\n" \
"buffer_load_dwordx4 v0, s[40:43], s54 offen sc1 lds\n" \
"s_add_u32 m0, s52, 25600\n" \
"s_mov_b32 s54, 1720320\n" \
"buffer_load_dwordx4 v0, s[40:43], s54 offen sc1 lds\n" \
"s_add_u32 m0, s52, 26624\n" \
"s_mov_b32 s54, 1736704\n" \
"buffer_load_dwordx4 v0, s[40:43], s54 offen sc1 lds\n" \
"s_add_u32 m0, s52, 27648\n" \
"s_mov_b32 s54, 1753088\n" \
"buffer_load_dwordx4 v0, s[40:43], s54 offen sc1 lds\n" \
"s_mov_b32 s54, s99\n" \
".Lswo_z26_%=:\n" \
"s_sleep 1\n" \
"s_sub_u32 s54, s54, 1\n" \
"s_cbranch_scc0 .Lswo_z26_%=\n" \
"s_waitcnt vmcnt(36)\n" \
"ds_read_b64_tr_b16 a[68:69], v1 offset:28672\n" \
"ds_read_b64_tr_b16 a[70:71], v1 offset:30720\n" \
"ds_read_b64_tr_b16 a[196:197], v1 offset:28704\n" \
"ds_read_b64_tr_b16 a[198:199], v1 offset:30752\n" \
"ds_read_b64_tr_b16 v[196:197], v1 offset:28736\n" \
"ds_read_b64_tr_b16 v[198:199], v1 offset:30784\n" \
"ds_read_b64_tr_b16 v[84:85], v1 offset:28768\n" \
"ds_read_b64_tr_b16 v[86:87], v1 offset:30816\n" \
"s_waitcnt lgkmcnt(0)\n" \
"s_add_u32 m0, s52, 28672\n" \
"s_mov_b32 s54, 1769472\n" \
"buffer_load_dwordx4 v0, s[40:43], s54 offen sc1 lds\n" \
"s_add_u32 m0, s52, 29696\n" \
"s_mov_b32 s54, 1785856\n" \
"buffer_load_dwordx4 v0, s[40:43], s54 offen sc1 lds\n" \
"s_add_u32 m0, s52, 30720\n" \
"s_mov_b32 s54, 1802240\n" \
"buffer_load_dwordx4 v0, s[40:43], s54 offen sc1 lds\n" \
"s_add_u32 m0, s52, 31744\n" \
"s_mov_b32 s54, 1818624\n" \
"buffer_load_dwordx4 v0, s[40:43], s54 offen sc1 lds\n" \
"s_mov_b32 s54, s99\n" \
".Lswo_z27_%=:\n" \
"s_sleep 1\n" \
"s_sub_u32 s54, s54, 1\n" \
"s_cbranch_scc0 .Lswo_z27_%=\n" \
"s_waitcnt vmcnt(36)\n" \
"ds_read_b64_tr_b16 a[72:73], v9 offset:32768\n" \
"ds_read_b64_tr_b16 a[74:75], v9 offset:34816\n" \
"ds_read_b64_tr_b16 a[200:201], v9 offset:32800\n" \
"ds_read_b64_tr_b16 a[202:203], v9 offset:34848\n" \
"ds_read_b64_tr_b16 v[200:201], v9 offset:32832\n" \
"ds_read_b64_tr_b16 v[202:203], v9 offset:34880\n" \
"ds_read_b64_tr_b16 v[88:89], v9 offset:32864\n" \
"ds_read_b64_tr_b16 v[90:91], v9 offset:34912\n" \
"s_waitcnt lgkmcnt(0)\n" \
"s_add_u32 m0, s53, 32768\n" \
"s_mov_b32 s54, 1835008\n" \
"buffer_load_dwordx4 v0, s[40:43], s54 offen sc1 lds\n" \
"s_add_u32 m0, s53, 33792\n" \
"s_mov_b32 s54, 1851392\n" \
"buffer_load_dwordx4 v0, s[40:43], s54 offen sc1 lds\n" \
"s_add_u32 m0, s53, 34816\n" \
"s_mov_b32 s54, 1867776\n" \
"buffer_load_dwordx4 v0, s[40:43], s54 offen sc1 lds\n" \
"s_add_u32 m0, s53, 35840\n" \
"s_mov_b32 s54, 1884160\n" \
"buffer_load_dwordx4 v0, s[40:43], s54 offen sc1 lds\n" \
"s_mov_b32 s54, s99\n" \
".Lswo_z28_%=:\n" \
"s_sleep 1\n" \
"s_sub_u32 s54, s54, 1\n" \
"s_cbranch_scc0 .Lswo_z28_%=\n" \
"s_waitcnt vmcnt(36)\n" \
"ds_read_b64_tr_b16 a[76:77], v9 offset:36864\n" \
"ds_read_b64_tr_b16 a[78:79], v9 offset:38912\n" \
"ds_read_b64_tr_b16 a[204:205], v9 offset:36896\n" \
"ds_read_b64_tr_b16 a[206:207], v9 offset:38944\n" \
"ds_read_b64_tr_b16 v[204:205], v9 offset:36928\n" \
"ds_read_b64_tr_b16 v[206:207], v9 offset:38976\n" \
"ds_read_b64_tr_b16 v[92:93], v9 offset:36960\n" \
"ds_read_b64_tr_b16 v[94:95], v9 offset:39008\n" \
"s_waitcnt lgkmcnt(0)\n" \
"s_add_u32 m0, s53, 36864\n" \
"s_mov_b32 s54, 1900544\n" \
"buffer_load_dwordx4 v0, s[40:43], s54 offen sc1 lds\n" \
"s_add_u32 m0, s53, 37888\n" \
"s_mov_b32 s54, 1916928\n" \
"buffer_load_dwordx4 v0, s[40:43], s54 offen sc1 lds\n" \
"s_add_u32 m0, s53, 38912\n" \
"s_mov_b32 s54, 1933312\n" \
"buffer_load_dwordx4 v0, s[40:43], s54 offen sc1 lds\n" \
"s_add_u32 m0, s53, 39936\n" \
"s_mov_b32 s54, 1949696\n" \
"buffer_load_dwordx4 v0, s[40:43], s54 offen sc1 lds\n" \
"s_mov_b32 s54, s99\n" \
".Lswo_z29_%=:\n" \
"s_sleep 1\n" \
"s_sub_u32 s54, s54, 1\n" \
"s_cbranch_scc0 .Lswo_z29_%=\n" \
"s_waitcnt vmcnt(36)\n" \
"ds_read_b64_tr_b16 a[80:81], v1 offset:0\n" \
"ds_read_b64_tr_b16 a[82:83], v1 offset:2048\n" \
"ds_read_b64_tr_b16 a[208:209], v1 offset:32\n" \
"ds_read_b64_tr_b16 a[210:211], v1 offset:2080\n" \
"ds_read_b64_tr_b16 v[208:209], v1 offset:64\n" \
"ds_read_b64_tr_b16 v[210:211], v1 offset:2112\n" \
"ds_read_b64_tr_b16 v[96:97], v1 offset:96\n" \
"ds_read_b64_tr_b16 v[98:99], v1 offset:2144\n" \
"s_waitcnt lgkmcnt(0)\n" \
"s_add_u32 m0, s52, 0\n" \
"s_mov_b32 s54, 1966080\n" \
"buffer_load_dwordx4 v0, s[40:43], s54 offen sc1 lds\n" \
"s_add_u32 m0, s52, 1024\n" \
"s_mov_b32 s54, 1982464\n" \
"buffer_load_dwordx4 v0, s[40:43], s54 offen sc1 lds\n" \
"s_add_u32 m0, s52, 2048\n" \
"s_mov_b32 s54, 1998848\n" \
"buffer_load_dwordx4 v0, s[40:43], s54 offen sc1 lds\n" \
"s_add_u32 m0, s52, 3072\n" \
"s_mov_b32 s54, 2015232\n" \
"buffer_load_dwordx4 v0, s[40:43], s54 offen sc1 lds\n" \
"s_mov_b32 s54, s99\n" \
".Lswo_z30_%=:\n" \
"s_sleep 1\n" \
"s_sub_u32 s54, s54, 1\n" \
"s_cbranch_scc0 .Lswo_z30_%=\n" \
"s_waitcnt vmcnt(36)\n" \
"ds_read_b64_tr_b16 a[84:85], v1 offset:4096\n" \
"ds_read_b64_tr_b16 a[86:87], v1 offset:6144\n" \
"ds_read_b64_tr_b16 a[212:213], v1 offset:4128\n" \
"ds_read_b64_tr_b16 a[214:215], v1 offset:6176\n" \
"ds_read_b64_tr_b16 v[212:213], v1 offset:4160\n" \
"ds_read_b64_tr_b16 v[214:215], v1 offset:6208\n" \
"ds_read_b64_tr_b16 v[100:101], v1 offset:4192\n" \
"ds_read_b64_tr_b16 v[102:103], v1 offset:6240\n" \
"s_waitcnt lgkmcnt(0)\n" \
"s_add_u32 m0, s52, 4096\n" \
"s_mov_b32 s54, 2031616\n" \
"buffer_load_dwordx4 v0, s[40:43], s54 offen sc1 lds\n" \
"s_add_u32 m0, s52, 5120\n" \
"s_mov_b32 s54, 2048000\n" \
"buffer_load_dwordx4 v0, s[40:43], s54 offen sc1 lds\n" \
"s_add_u32 m0, s52, 6144\n" \
"s_mov_b32 s54, 2064384\n" \
"buffer_load_dwordx4 v0, s[40:43], s54 offen sc1 lds\n" \
"s_add_u32 m0, s52, 7168\n" \
"s_mov_b32 s54, 2080768\n" \
"buffer_load_dwordx4 v0, s[40:43], s54 offen sc1 lds\n" \
"s_mov_b32 s54, s99\n" \
".Lswo_z31_%=:\n" \
"s_sleep 1\n" \
"s_sub_u32 s54, s54, 1\n" \
"s_cbranch_scc0 .Lswo_z31_%=\n" \
"s_waitcnt vmcnt(36)\n" \
"ds_read_b64_tr_b16 a[88:89], v1 offset:8192\n" \
"ds_read_b64_tr_b16 a[90:91], v1 offset:10240\n" \
"ds_read_b64_tr_b16 a[216:217], v1 offset:8224\n" \
"ds_read_b64_tr_b16 a[218:219], v1 offset:10272\n" \
"ds_read_b64_tr_b16 v[216:217], v1 offset:8256\n" \
"ds_read_b64_tr_b16 v[218:219], v1 offset:10304\n" \
"ds_read_b64_tr_b16 v[104:105], v1 offset:8288\n" \
"ds_read_b64_tr_b16 v[106:107], v1 offset:10336\n" \
"s_waitcnt lgkmcnt(0)\n" \
"ds_write_b128 v2, v[16:19] offset:8192\n" \
"ds_write_b128 v2, v[20:23] offset:9216\n" \
"ds_write_b128 v2, v[24:27] offset:10240\n" \
"ds_write_b128 v2, v[28:31] offset:11264\n" \
"s_waitcnt vmcnt(32)\n" \
"ds_read_b64_tr_b16 a[92:93], v1 offset:12288\n" \
"ds_read_b64_tr_b16 a[94:95], v1 offset:14336\n" \
"ds_read_b64_tr_b16 a[220:221], v1 offset:12320\n" \
"ds_read_b64_tr_b16 a[222:223], v1 offset:14368\n" \
"ds_read_b64_tr_b16 v[220:221], v1 offset:12352\n" \
"ds_read_b64_tr_b16 v[222:223], v1 offset:14400\n" \
"ds_read_b64_tr_b16 v[108:109], v1 offset:12384\n" \
"ds_read_b64_tr_b16 v[110:111], v1 offset:14432\n" \
"s_waitcnt lgkmcnt(0)\n" \
"ds_write_b128 v2, v[32:35] offset:12288\n" \
"ds_write_b128 v2, v[36:39] offset:13312\n" \
"ds_write_b128 v2, v[40:43] offset:14336\n" \
"ds_write_b128 v2, v[44:47] offset:15360\n" \
"s_waitcnt vmcnt(28)\n" \
"ds_read_b64_tr_b16 a[96:97], v1 offset:16384\n" \
"ds_read_b64_tr_b16 a[98:99], v1 offset:18432\n" \
"ds_read_b64_tr_b16 a[224:225], v1 offset:16416\n" \
"ds_read_b64_tr_b16 a[226:227], v1 offset:18464\n" \
"ds_read_b64_tr_b16 v[224:225], v1 offset:16448\n" \
"ds_read_b64_tr_b16 v[226:227], v1 offset:18496\n" \
"ds_read_b64_tr_b16 v[16:17], v1 offset:16480\n" \
"ds_read_b64_tr_b16 v[18:19], v1 offset:18528\n" \
"s_waitcnt lgkmcnt(0)\n" \
"ds_write_b128 v2, v[48:51] offset:16384\n" \
"ds_write_b128 v2, v[52:55] offset:17408\n" \
"ds_write_b128 v2, v[56:59] offset:18432\n" \
"ds_write_b128 v2, v[60:63] offset:19456\n" \
"s_waitcnt vmcnt(24)\n" \
"ds_read_b64_tr_b16 a[100:101], v1 offset:20480\n" \
"ds_read_b64_tr_b16 a[102:103], v1 offset:22528\n" \
"ds_read_b64_tr_b16 a[228:229], v1 offset:20512\n" \
"ds_read_b64_tr_b16 a[230:231], v1 offset:22560\n" \
"ds_read_b64_tr_b16 v[228:229], v1 offset:20544\n" \
"ds_read_b64_tr_b16 v[230:231], v1 offset:22592\n" \
"ds_read_b64_tr_b16 v[20:21], v1 offset:20576\n" \
"ds_read_b64_tr_b16 v[22:23], v1 offset:22624\n" \
"s_waitcnt lgkmcnt(0)\n" \
"ds_write_b128 v2, v[64:67] offset:20480\n" \
"ds_write_b128 v2, v[68:71] offset:21504\n" \
"ds_write_b128 v2, v[72:75] offset:22528\n" \
"ds_write_b128 v2, v[76:79] offset:23552\n" \
"s_waitcnt vmcnt(20)\n" \
"ds_read_b64_tr_b16 a[104:105], v1 offset:24576\n" \
"ds_read_b64_tr_b16 a[106:107], v1 offset:26624\n" \
"ds_read_b64_tr_b16 a[232:233], v1 offset:24608\n" \
"ds_read_b64_tr_b16 a[234:235], v1 offset:26656\n" \
"ds_read_b64_tr_b16 v[232:233], v1 offset:24640\n" \
"ds_read_b64_tr_b16 v[234:235], v1 offset:26688\n" \
"ds_read_b64_tr_b16 v[24:25], v1 offset:24672\n" \
"ds_read_b64_tr_b16 v[26:27], v1 offset:26720\n" \
"s_waitcnt lgkmcnt(0)\n" \
"ds_write_b128 v2, v[80:83] offset:24576\n" \
"ds_write_b128 v2, v[84:87] offset:25600\n" \
"ds_write_b128 v2, v[88:91] offset:26624\n" \
"ds_write_b128 v2, v[92:95] offset:27648\n" \
"s_waitcnt vmcnt(16)\n" \
"ds_read_b64_tr_b16 a[108:109], v1 offset:28672\n" \
"ds_read_b64_tr_b16 a[110:111], v1 offset:30720\n" \
"ds_read_b64_tr_b16 a[236:237], v1 offset:28704\n" \
"ds_read_b64_tr_b16 a[238:239], v1 offset:30752\n" \
"ds_read_b64_tr_b16 v[236:237], v1 offset:28736\n" \
"ds_read_b64_tr_b16 v[238:239], v1 offset:30784\n" \
"ds_read_b64_tr_b16 v[28:29], v1 offset:28768\n" \
"ds_read_b64_tr_b16 v[30:31], v1 offset:30816\n" \
"s_waitcnt lgkmcnt(0)\n" \
"ds_write_b128 v2, v[96:99] offset:28672\n" \
"ds_write_b128 v2, v[100:103] offset:29696\n" \
"ds_write_b128 v2, v[104:107] offset:30720\n" \
"ds_write_b128 v2, v[108:111] offset:31744\n" \
"s_waitcnt vmcnt(12)\n" \
"ds_read_b64_tr_b16 a[112:113], v9 offset:32768\n" \
"ds_read_b64_tr_b16 a[114:115], v9 offset:34816\n" \
"ds_read_b64_tr_b16 a[240:241], v9 offset:32800\n" \
"ds_read_b64_tr_b16 a[242:243], v9 offset:34848\n" \
"ds_read_b64_tr_b16 v[240:241], v9 offset:32832\n" \
"ds_read_b64_tr_b16 v[242:243], v9 offset:34880\n" \
"ds_read_b64_tr_b16 v[32:33], v9 offset:32864\n" \
"ds_read_b64_tr_b16 v[34:35], v9 offset:34912\n" \
"s_waitcnt lgkmcnt(0)\n" \
"s_waitcnt vmcnt(8)\n" \
"ds_read_b64_tr_b16 a[116:117], v9 offset:36864\n" \
"ds_read_b64_tr_b16 a[118:119], v9 offset:38912\n" \
"ds_read_b64_tr_b16 a[244:245], v9 offset:36896\n" \
"ds_read_b64_tr_b16 a[246:247], v9 offset:38944\n" \
"ds_read_b64_tr_b16 v[244:245], v9 offset:36928\n" \
"ds_read_b64_tr_b16 v[246:247], v9 offset:38976\n" \
"ds_read_b64_tr_b16 v[36:37], v9 offset:36960\n" \
"ds_read_b64_tr_b16 v[38:39], v9 offset:39008\n" \
"s_waitcnt lgkmcnt(0)\n" \
"s_waitcnt vmcnt(4)\n" \
"ds_read_b64_tr_b16 a[120:121], v1 offset:0\n" \
"ds_read_b64_tr_b16 a[122:123], v1 offset:2048\n" \
"ds_read_b64_tr_b16 a[248:249], v1 offset:32\n" \
"ds_read_b64_tr_b16 a[250:251], v1 offset:2080\n" \
"ds_read_b64_tr_b16 v[248:249], v1 offset:64\n" \
"ds_read_b64_tr_b16 v[250:251], v1 offset:2112\n" \
"ds_read_b64_tr_b16 v[40:41], v1 offset:96\n" \
"ds_read_b64_tr_b16 v[42:43], v1 offset:2144\n" \
"s_waitcnt lgkmcnt(0)\n" \
"ds_write_b128 v2, v[16:19] offset:0\n" \
"ds_write_b128 v2, v[20:23] offset:1024\n" \
"ds_write_b128 v2, v[24:27] offset:2048\n" \
"ds_write_b128 v2, v[28:31] offset:3072\n" \
"s_waitcnt vmcnt(0)\n" \
"ds_read_b64_tr_b16 a[124:125], v1 offset:4096\n" \
"ds_read_b64_tr_b16 a[126:127], v1 offset:6144\n" \
"ds_read_b64_tr_b16 a[252:253], v1 offset:4128\n" \
"ds_read_b64_tr_b16 a[254:255], v1 offset:6176\n" \
"ds_read_b64_tr_b16 v[252:253], v1 offset:4160\n" \
"ds_read_b64_tr_b16 v[254:255], v1 offset:6208\n" \
"ds_read_b64_tr_b16 v[44:45], v1 offset:4192\n" \
"ds_read_b64_tr_b16 v[46:47], v1 offset:6240\n" \
"s_waitcnt lgkmcnt(0)\n" \
"ds_write_b128 v2, v[32:35] offset:4096\n" \
"ds_write_b128 v2, v[36:39] offset:5120\n" \
"ds_write_b128 v2, v[40:43] offset:6144\n" \
"ds_write_b128 v2, v[44:47] offset:7168\n" \
"s_waitcnt vmcnt(0) lgkmcnt(0)\n" \
"s_barrier\n" \
"s_add_u32 s86, s85, 0\n" \
"s_add_u32 s87, s85, 4259840\n" \
"s_add_u32 s88, s85, 8519680\n" \
"s_add_u32 s89, s85, 12779520\n" \
"s_add_u32 s90, s85, 17039360\n" \
"s_add_u32 s91, s85, 21299200\n" \
"s_add_u32 s92, s85, 25559040\n" \
"s_add_u32 s93, s85, 29818880\n" \
"s_mov_b32 s55, 0\n" \
"s_mov_b32 s60, 0\n" \
"s_mov_b32 s79, s95\n" \
"s_mov_b32 s98, 0x70000000\n" \
".Lswo_prime_%=:\n" \
"s_lshr_b32 s59, s44, 1\n" \
"s_add_u32 s59, s59, 1\n" \
"s_lshr_b32 s56, s44, 1\n" \
"s_lshl_b32 s56, s56, 16\n" \
"s_lshr_b32 s58, s56, 8\n" \
"s_and_b32 s58, s58, 0x2000\n" \
"s_xor_b32 s56, s56, s58\n" \
"s_and_b32 s58, s44, 1\n" \
"s_lshl_b32 s58, s58, 12\n" \
"s_or_b32 s56, s56, s58\n" \
"s_lshl_b32 s54, s44, 16\n" \
"s_add_u32 s54, s54, s78\n" \
"s_lshr_b32 s58, s54, 8\n" \
"s_and_b32 s58, s58, 0x2000\n" \
"s_xor_b32 s54, s54, s58\n" \
"s_add_u32 s54, s54, 8388608\n" \
"s_lshl_b32 s77, s44, 15\n" \
"s_or_b32 s77, s77, s98\n" \
"s_or_b32 s54, s54, s98\n" \
"buffer_load_dwordx2 v[14:15], v6, s[36:39], s54 offen sc1\n" \
"buffer_load_dwordx2 v[12:13], v5, s[64:67], s77 offen\n" \
"buffer_load_dword v11, v4, s[36:39], 0 offen sc1\n" \
"s_waitcnt vmcnt(0)\n" \
"v_cmp_gt_u32_e64 s[62:63], s59, v11\n" \
"s_nop 4\n" \
"s_cmp_lg_u64 s[62:63], 0\n" \
"s_cbranch_scc1 .Lswo_w_%=\n" \
".Lswo_wk_%=:\n" \
"s_mov_b32 s76, 0x7ffffff0\n" \
"s_mov_b32 s81, 1\n" \
"s_mov_b32 s84, 0\n" \
"s_waitcnt vmcnt(0) lgkmcnt(0)\n" \
"s_xor_b32 s57, s56, s86\n" \
"buffer_load_dwordx4 v[16:19], v3, s[36:39], s57 offen offset:0 sc1\n" \
"buffer_load_dwordx4 v[20:23], v3, s[36:39], s57 offen offset:1024 sc1\n" \
"buffer_load_dwordx4 v[24:27], v3, s[36:39], s57 offen offset:2048 sc1\n" \
"buffer_load_dwordx4 v[28:31], v3, s[36:39], s57 offen offset:3072 sc1\n" \
"s_xor_b32 s57, s56, s87\n" \
"buffer_load_dwordx4 v[32:35], v3, s[36:39], s57 offen offset:0 sc1\n" \
"buffer_load_dwordx4 v[36:39], v3, s[36:39], s57 offen offset:1024 sc1\n" \
"buffer_load_dwordx4 v[40:43], v3, s[36:39], s57 offen offset:2048 sc1\n" \
"buffer_load_dwordx4 v[44:47], v3, s[36:39], s57 offen offset:3072 sc1\n" \
"s_xor_b32 s57, s56, s88\n" \
"buffer_load_dwordx4 v[48:51], v3, s[36:39], s57 offen offset:0 sc1\n" \
"buffer_load_dwordx4 v[52:55], v3, s[36:39], s57 offen offset:1024 sc1\n" \
"buffer_load_dwordx4 v[56:59], v3, s[36:39], s57 offen offset:2048 sc1\n" \
"buffer_load_dwordx4 v[60:63], v3, s[36:39], s57 offen offset:3072 sc1\n" \
"s_xor_b32 s57, s56, s89\n" \
"buffer_load_dwordx4 v[64:67], v3, s[36:39], s57 offen offset:0 sc1\n" \
"buffer_load_dwordx4 v[68:71], v3, s[36:39], s57 offen offset:1024 sc1\n" \
"buffer_load_dwordx4 v[72:75], v3, s[36:39], s57 offen offset:2048 sc1\n" \
"buffer_load_dwordx4 v[76:79], v3, s[36:39], s57 offen offset:3072 sc1\n" \
"ds_read_b128 v[80:83], v2 offset:8192\n" \
"ds_read_b128 v[84:87], v2 offset:9216\n" \
"ds_read_b128 v[88:91], v2 offset:10240\n" \
"s_waitcnt lgkmcnt(0)\n" \
"v_mov_b32 v9, 0x7ffffff0\n" \
"buffer_store_dword v9, v9, s[36:39], 0 offen\n" \
"s_waitcnt vmcnt(16)\n" \
"s_nop 7\n" \
"s_nop 7\n" \
".p2align 6\n" \
".Lswo_loop_%=:\n" \
"v_mfma_f32_16x16x32_bf16 v[96:99], a[0:3], v[16:19], 0\n" \
"ds_write_b128 v7, v[112:115] offset:0\n" \
"v_mfma_f32_16x16x32_bf16 v[100:103], a[128:131], v[16:19], 0\n" \
"ds_read_b128 v[92:95], v2 offset:11264\n" \
"v_xor_b32_e64 v7, 64, v7\n" \
"v_mfma_f32_16x16x32_bf16 v[104:107], v[128:131], v[16:19], 0\n" \
"s_waitcnt vmcnt(15) lgkmcnt(4)\n" \
"v_mfma_f32_16x16x32_bf16 v[108:111], v[80:83], v[16:19], 0\n" \
"s_xor_b32 s57, s56, s90\n" \
"buffer_load_dwordx4 v[16:19], v3, s[36:39], s57 offen offset:0 sc1\n" \
"v_mfma_f32_16x16x32_bf16 v[96:99], a[4:7], v[20:23], v[96:99]\n" \
"ds_write_b128 v7, v[116:119] offset:1024\n" \
"v_mfma_f32_16x16x32_bf16 v[100:103], a[132:135], v[20:23], v[100:103]\n" \
"ds_read_b128 v[80:83], v2 offset:12288\n" \
"s_cmp_lt_i32 s44, s50\n" \
"v_xor_b32 v7, 192, v7\n" \
"v_mfma_f32_16x16x32_bf16 v[104:107], v[132:135], v[20:23], v[104:107]\n" \
"s_waitcnt vmcnt(15) lgkmcnt(5)\n" \
"s_cselect_b32 s82, s45, s47\n" \
"v_mfma_f32_16x16x32_bf16 v[108:111], v[84:87], v[20:23], v[108:111]\n" \
"buffer_load_dwordx4 v[20:23], v3, s[36:39], s57 offen offset:1024 sc1\n" \
"v_mfma_f32_16x16x32_bf16 v[96:99], a[8:11], v[24:27], v[96:99]\n" \
"ds_write_b128 v7, v[120:123] offset:2048\n" \
"v_mfma_f32_16x16x32_bf16 v[100:103], a[136:139], v[24:27], v[100:103]\n" \
"ds_read_b128 v[84:87], v2 offset:13312\n" \
"s_add_u32 s82, s82, s44\n" \
"s_cmp_lt_u32 s82, 128\n" \
"v_xor_b32_e64 v7, 64, v7\n" \
"v_mfma_f32_16x16x32_bf16 v[104:107], v[136:139], v[24:27], v[104:107]\n" \
"s_waitcnt vmcnt(15) lgkmcnt(6)\n" \
"s_cselect_b32 s82, s82, s94\n" \
"v_mfma_f32_16x16x32_bf16 v[108:111], v[88:91], v[24:27], v[108:111]\n" \
"buffer_load_dwordx4 v[24:27], v3, s[36:39], s57 offen offset:2048 sc1\n" \
"v_mfma_f32_16x16x32_bf16 v[96:99], a[12:15], v[28:31], v[96:99]\n" \
"ds_write_b128 v7, v[124:127] offset:3072\n" \
"v_mfma_f32_16x16x32_bf16 v[100:103], a[140:143], v[28:31], v[100:103]\n" \
"ds_read_b128 v[88:91], v2 offset:14336\n" \
"s_lshr_b32 s83, s82, 1\n" \
"v_xor_b32 v7, 192, v7\n" \
"v_mfma_f32_16x16x32_bf16 v[104:107], v[140:143], v[28:31], v[104:107]\n" \
"s_waitcnt vmcnt(15) lgkmcnt(6)\n" \
"s_lshl_b32 s83, s83, 16\n" \
"v_mfma_f32_16x16x32_bf16 v[108:111], v[92:95], v[28:31], v[108:111]\n" \
"buffer_load_dwordx4 v[28:31], v3, s[36:39], s57 offen offset:3072 sc1\n" \
"v_mfma_f32_16x16x32_bf16 v[96:99], a[16:19], v[32:35], v[96:99]\n" \
"v_mfma_f32_16x16x32_bf16 v[100:103], a[144:147], v[32:35], v[100:103]\n" \
"ds_read_b128 v[92:95], v2 offset:15360\n" \
"s_lshr_b32 s58, s83, 8\n" \
"v_mfma_f32_16x16x32_bf16 v[104:107], v[144:147], v[32:35], v[104:107]\n" \
"s_waitcnt vmcnt(15) lgkmcnt(5)\n" \
"s_and_b32 s58, s58, 0x2000\n" \
"v_mfma_f32_16x16x32_bf16 v[108:111], v[80:83], v[32:35], v[108:111]\n" \
"s_xor_b32 s57, s56, s91\n" \
"buffer_load_dwordx4 v[32:35], v3, s[36:39], s57 offen offset:0 sc1\n" \
"v_mfma_f32_16x16x32_bf16 v[96:99], a[20:23], v[36:39], v[96:99]\n" \
"s_waitcnt lgkmcnt(2)\n" \
"s_cmp_lg_u32 s81, 0\n" \
"s_cbranch_scc1 .Lswo_nb_%=\n" \
"s_barrier\n" \
".Lswo_nb_%=:\n" \
"v_mfma_f32_16x16x32_bf16 v[100:103], a[148:151], v[36:39], v[100:103]\n" \
"ds_read_b128 v[80:83], v2 offset:16384\n" \
"s_xor_b32 s83, s83, s58\n" \
"v_xor_b32 v7, s84, v7\n" \
"v_mfma_f32_16x16x32_bf16 v[104:107], v[148:151], v[36:39], v[104:107]\n" \
"s_waitcnt vmcnt(15)\n" \
"s_and_b32 s58, s82, 1\n" \
"v_mfma_f32_16x16x32_bf16 v[108:111], v[84:87], v[36:39], v[108:111]\n" \
"buffer_load_dwordx4 v[36:39], v3, s[36:39], s57 offen offset:1024 sc1\n" \
"v_mfma_f32_16x16x32_bf16 v[96:99], a[24:27], v[40:43], v[96:99]\n" \
"ds_read_b128 v[112:115], v8 offset:0\n" \
"v_mfma_f32_16x16x32_bf16 v[100:103], a[152:155], v[40:43], v[100:103]\n" \
"ds_read_b128 v[84:87], v2 offset:17408\n" \
"s_lshl_b32 s58, s58, 12\n" \
"v_mfma_f32_16x16x32_bf16 v[104:107], v[152:155], v[40:43], v[104:107]\n" \
"s_waitcnt vmcnt(15) lgkmcnt(4)\n" \
"s_or_b32 s83, s83, s58\n" \
"v_mfma_f32_16x16x32_bf16 v[108:111], v[88:91], v[40:43], v[108:111]\n" \
"buffer_load_dwordx4 v[40:43], v3, s[36:39], s57 offen offset:2048 sc1\n" \
"v_mfma_f32_16x16x32_bf16 v[96:99], a[28:31], v[44:47], v[96:99]\n" \
"ds_read_b128 v[116:119], v8 offset:4096\n" \
"v_mfma_f32_16x16x32_bf16 v[100:103], a[156:159], v[44:47], v[100:103]\n" \
"ds_read_b128 v[88:91], v2 offset:18432\n" \
"v_mfma_f32_16x16x32_bf16 v[104:107], v[156:159], v[44:47], v[104:107]\n" \
"s_waitcnt vmcnt(15) lgkmcnt(5)\n" \
"v_mfma_f32_16x16x32_bf16 v[108:111], v[92:95], v[44:47], v[108:111]\n" \
"buffer_load_dwordx4 v[44:47], v3, s[36:39], s57 offen offset:3072 sc1\n" \
"v_mfma_f32_16x16x32_bf16 v[96:99], a[32:35], v[48:51], v[96:99]\n" \
"ds_read_b128 v[120:123], v8 offset:8192\n" \
"s_lshr_b32 s59, s82, 1\n" \
"s_add_u32 s59, s59, 1\n" \
"v_mfma_f32_16x16x32_bf16 v[100:103], a[160:163], v[48:51], v[100:103]\n" \
"ds_read_b128 v[92:95], v2 offset:19456\n" \
"v_mfma_f32_16x16x32_bf16 v[104:107], v[160:163], v[48:51], v[104:107]\n" \
"s_waitcnt vmcnt(15) lgkmcnt(6)\n" \
"v_mfma_f32_16x16x32_bf16 v[108:111], v[80:83], v[48:51], v[108:111]\n" \
"s_xor_b32 s57, s56, s92\n" \
"buffer_load_dwordx4 v[48:51], v3, s[36:39], s57 offen offset:0 sc1\n" \
"v_mfma_f32_16x16x32_bf16 v[96:99], a[36:39], v[52:55], v[96:99]\n" \
"ds_read_b128 v[124:127], v8 offset:12288\n" \
"v_mfma_f32_16x16x32_bf16 v[100:103], a[164:167], v[52:55], v[100:103]\n" \
"ds_read_b128 v[80:83], v2 offset:20480\n" \
"v_mfma_f32_16x16x32_bf16 v[104:107], v[164:167], v[52:55], v[104:107]\n" \
"s_waitcnt vmcnt(15) lgkmcnt(6)\n" \
"v_mfma_f32_16x16x32_bf16 v[108:111], v[84:87], v[52:55], v[108:111]\n" \
"buffer_load_dwordx4 v[52:55], v3, s[36:39], s57 offen offset:1024 sc1\n" \
"v_mfma_f32_16x16x32_bf16 v[96:99], a[40:43], v[56:59], v[96:99]\n" \
"v_mfma_f32_16x16x32_bf16 v[100:103], a[168:171], v[56:59], v[100:103]\n" \
"ds_read_b128 v[84:87], v2 offset:21504\n" \
"v_mfma_f32_16x16x32_bf16 v[104:107], v[168:171], v[56:59], v[104:107]\n" \
"s_waitcnt vmcnt(15) lgkmcnt(5)\n" \
"v_add_f32_e64 v112, v112, v116\n" \
"v_mfma_f32_16x16x32_bf16 v[108:111], v[88:91], v[56:59], v[108:111]\n" \
"buffer_load_dwordx4 v[56:59], v3, s[36:39], s57 offen offset:2048 sc1\n" \
"v_add_f32_e64 v113, v113, v117\n" \
"v_mfma_f32_16x16x32_bf16 v[96:99], a[44:47], v[60:63], v[96:99]\n" \
"v_add_f32_e64 v114, v114, v118\n" \
"v_mfma_f32_16x16x32_bf16 v[100:103], a[172:175], v[60:63], v[100:103]\n" \
"ds_read_b128 v[88:91], v2 offset:22528\n" \
"v_add_f32_e64 v115, v115, v119\n" \
"v_mfma_f32_16x16x32_bf16 v[104:107], v[172:175], v[60:63], v[104:107]\n" \
"s_waitcnt vmcnt(15) lgkmcnt(4)\n" \
"s_waitcnt lgkmcnt(3)\n" \
"v_add_f32_e64 v120, v120, v124\n" \
"v_mfma_f32_16x16x32_bf16 v[108:111], v[92:95], v[60:63], v[108:111]\n" \
"buffer_load_dwordx4 v[60:63], v3, s[36:39], s57 offen offset:3072 sc1\n" \
"v_add_f32_e64 v121, v121, v125\n" \
"v_mfma_f32_16x16x32_bf16 v[96:99], a[48:51], v[64:67], v[96:99]\n" \
"v_add_f32_e64 v122, v122, v126\n" \
"v_mfma_f32_16x16x32_bf16 v[100:103], a[176:179], v[64:67], v[100:103]\n" \
"ds_read_b128 v[92:95], v2 offset:23552\n" \
"v_add_f32_e64 v123, v123, v127\n" \
"v_mfma_f32_16x16x32_bf16 v[104:107], v[176:179], v[64:67], v[104:107]\n" \
"s_waitcnt vmcnt(14) lgkmcnt(3)\n" \
"v_add_f32 v112, v112, v120\n" \
"v_mfma_f32_16x16x32_bf16 v[108:111], v[80:83], v[64:67], v[108:111]\n" \
"s_xor_b32 s57, s56, s93\n" \
"buffer_load_dwordx4 v[64:67], v3, s[36:39], s57 offen offset:0 sc1\n" \
"v_mfma_f32_16x16x32_bf16 v[96:99], a[52:55], v[68:71], v[96:99]\n" \
"v_cmp_gt_u32_e64 s[62:63], s59, v11\n" \
"v_mfma_f32_16x16x32_bf16 v[100:103], a[180:183], v[68:71], v[100:103]\n" \
"ds_read_b128 v[80:83], v2 offset:24576\n" \
"v_add_f32 v113, v113, v121\n" \
"v_mfma_f32_16x16x32_bf16 v[104:107], v[180:183], v[68:71], v[104:107]\n" \
"s_waitcnt vmcnt(14) lgkmcnt(3)\n" \
"v_add_f32 v114, v114, v122\n" \
"v_mfma_f32_16x16x32_bf16 v[108:111], v[84:87], v[68:71], v[108:111]\n" \
"buffer_load_dwordx4 v[68:71], v3, s[36:39], s57 offen offset:1024 sc1\n" \
"v_add_f32_e64 v115, v115, v123\n" \
"v_mfma_f32_16x16x32_bf16 v[96:99], a[56:59], v[72:75], v[96:99]\n" \
"s_add_u32 s54, s55, 1\n" \
"s_cmp_lt_u32 s54, s79\n" \
"v_mfma_f32_16x16x32_bf16 v[100:103], a[184:187], v[72:75], v[100:103]\n" \
"ds_read_b128 v[84:87], v2 offset:25600\n" \
"s_cselect_b64 s[62:63], s[62:63], -1\n" \
"v_mfma_f32_16x16x32_bf16 v[104:107], v[184:187], v[72:75], v[104:107]\n" \
"s_waitcnt vmcnt(14) lgkmcnt(3)\n" \
"v_xor_b32_e64 v8, s84, v8\n" \
"v_mfma_f32_16x16x32_bf16 v[108:111], v[88:91], v[72:75], v[108:111]\n" \
"buffer_load_dwordx4 v[72:75], v3, s[36:39], s57 offen offset:2048 sc1\n" \
"v_cvt_pk_bf16_f32 v0, v112, v113\n" \
"v_mfma_f32_16x16x32_bf16 v[96:99], a[60:63], v[76:79], v[96:99]\n" \
"s_cmp_lg_u64 s[62:63], 0\n" \
"s_cselect_b32 s81, 1, 0\n" \
"v_mfma_f32_16x16x32_bf16 v[100:103], a[188:191], v[76:79], v[100:103]\n" \
"ds_read_b128 v[88:91], v2 offset:26624\n" \
"s_cselect_b32 s83, 0x70000000, s83\n" \
"v_mfma_f32_16x16x32_bf16 v[104:107], v[188:191], v[76:79], v[104:107]\n" \
"s_waitcnt vmcnt(14) lgkmcnt(3)\n" \
"v_cvt_pk_bf16_f32 v1, v114, v115\n" \
"v_mfma_f32_16x16x32_bf16 v[108:111], v[92:95], v[76:79], v[108:111]\n" \
"buffer_load_dwordx4 v[76:79], v3, s[36:39], s57 offen offset:3072 sc1\n" \
"v_mfma_f32_16x16x32_bf16 v[96:99], a[64:67], v[16:19], v[96:99]\n" \
"buffer_store_dwordx2 v[0:1], v6, s[36:39], s76 offen sc1\n" \
"v_mfma_f32_16x16x32_bf16 v[100:103], a[192:195], v[16:19], v[100:103]\n" \
"ds_read_b128 v[92:95], v2 offset:27648\n" \
"v_mfma_f32_16x16x32_bf16 v[104:107], v[192:195], v[16:19], v[104:107]\n" \
"s_waitcnt vmcnt(15) lgkmcnt(3)\n" \
"s_mov_b32 s56, s83\n" \
"v_mfma_f32_16x16x32_bf16 v[108:111], v[80:83], v[16:19], v[108:111]\n" \
"s_xor_b32 s57, s56, s86\n" \
"buffer_load_dwordx4 v[16:19], v3, s[36:39], s57 offen offset:0 sc1\n" \
"v_mfma_f32_16x16x32_bf16 v[96:99], a[68:71], v[20:23], v[96:99]\n" \
"v_mfma_f32_16x16x32_bf16 v[100:103], a[196:199], v[20:23], v[100:103]\n" \
"ds_read_b128 v[80:83], v2 offset:28672\n" \
"v_mfma_f32_16x16x32_bf16 v[104:107], v[196:199], v[20:23], v[104:107]\n" \
"s_waitcnt vmcnt(15) lgkmcnt(3)\n" \
"v_mfma_f32_16x16x32_bf16 v[108:111], v[84:87], v[20:23], v[108:111]\n" \
"buffer_load_dwordx4 v[20:23], v3, s[36:39], s57 offen offset:1024 sc1\n" \
"v_mfma_f32_16x16x32_bf16 v[96:99], a[72:75], v[24:27], v[96:99]\n" \
"v_mfma_f32_16x16x32_bf16 v[100:103], a[200:203], v[24:27], v[100:103]\n" \
"ds_read_b128 v[84:87], v2 offset:29696\n" \
"v_mfma_f32_16x16x32_bf16 v[104:107], v[200:203], v[24:27], v[104:107]\n" \
"s_waitcnt vmcnt(15) lgkmcnt(3)\n" \
"v_mfma_f32_16x16x32_bf16 v[108:111], v[88:91], v[24:27], v[108:111]\n" \
"buffer_load_dwordx4 v[24:27], v3, s[36:39], s57 offen offset:2048 sc1\n" \
"v_mfma_f32_16x16x32_bf16 v[96:99], a[76:79], v[28:31], v[96:99]\n" \
"v_mfma_f32_16x16x32_bf16 v[100:103], a[204:207], v[28:31], v[100:103]\n" \
"ds_read_b128 v[88:91], v2 offset:30720\n" \
"v_mfma_f32_16x16x32_bf16 v[104:107], v[204:207], v[28:31], v[104:107]\n" \
"s_waitcnt vmcnt(15) lgkmcnt(3)\n" \
"v_mfma_f32_16x16x32_bf16 v[108:111], v[92:95], v[28:31], v[108:111]\n" \
"buffer_load_dwordx4 v[28:31], v3, s[36:39], s57 offen offset:3072 sc1\n" \
"v_mfma_f32_16x16x32_bf16 v[96:99], a[80:83], v[32:35], v[96:99]\n" \
"v_mfma_f32_16x16x32_bf16 v[100:103], a[208:211], v[32:35], v[100:103]\n" \
"ds_read_b128 v[92:95], v2 offset:31744\n" \
"v_mfma_f32_16x16x32_bf16 v[104:107], v[208:211], v[32:35], v[104:107]\n" \
"s_waitcnt vmcnt(15) lgkmcnt(3)\n" \
"v_mfma_f32_16x16x32_bf16 v[108:111], v[80:83], v[32:35], v[108:111]\n" \
"s_xor_b32 s57, s56, s87\n" \
"buffer_load_dwordx4 v[32:35], v3, s[36:39], s57 offen offset:0 sc1\n" \
"v_mfma_f32_16x16x32_bf16 v[96:99], a[84:87], v[36:39], v[96:99]\n" \
"v_mfma_f32_16x16x32_bf16 v[100:103], a[212:215], v[36:39], v[100:103]\n" \
"ds_read_b128 v[80:83], v2 offset:0\n" \
"v_mfma_f32_16x16x32_bf16 v[104:107], v[212:215], v[36:39], v[104:107]\n" \
"s_waitcnt vmcnt(15) lgkmcnt(3)\n" \
"v_mfma_f32_16x16x32_bf16 v[108:111], v[84:87], v[36:39], v[108:111]\n" \
"buffer_load_dwordx4 v[36:39], v3, s[36:39], s57 offen offset:1024 sc1\n" \
"v_mfma_f32_16x16x32_bf16 v[96:99], a[88:91], v[40:43], v[96:99]\n" \
"v_mfma_f32_16x16x32_bf16 v[100:103], a[216:219], v[40:43], v[100:103]\n" \
"ds_read_b128 v[84:87], v2 offset:1024\n" \
"v_mfma_f32_16x16x32_bf16 v[104:107], v[216:219], v[40:43], v[104:107]\n" \
"s_waitcnt vmcnt(15) lgkmcnt(3)\n" \
"v_mfma_f32_16x16x32_bf16 v[108:111], v[88:91], v[40:43], v[108:111]\n" \
"buffer_load_dwordx4 v[40:43], v3, s[36:39], s57 offen offset:2048 sc1\n" \
"v_mfma_f32_16x16x32_bf16 v[96:99], a[92:95], v[44:47], v[96:99]\n" \
"v_mfma_f32_16x16x32_bf16 v[100:103], a[220:223], v[44:47], v[100:103]\n" \
"ds_read_b128 v[88:91], v2 offset:2048\n" \
"v_mfma_f32_16x16x32_bf16 v[104:107], v[220:223], v[44:47], v[104:107]\n" \
"s_waitcnt vmcnt(15) lgkmcnt(3)\n" \
"v_mfma_f32_16x16x32_bf16 v[108:111], v[92:95], v[44:47], v[108:111]\n" \
"buffer_load_dwordx4 v[44:47], v3, s[36:39], s57 offen offset:3072 sc1\n" \
"v_mfma_f32_16x16x32_bf16 v[96:99], a[96:99], v[48:51], v[96:99]\n" \
"v_mfma_f32_16x16x32_bf16 v[100:103], a[224:227], v[48:51], v[100:103]\n" \
"ds_read_b128 v[92:95], v2 offset:3072\n" \
"v_mfma_f32_16x16x32_bf16 v[104:107], v[224:227], v[48:51], v[104:107]\n" \
"s_waitcnt vmcnt(15) lgkmcnt(3)\n" \
"v_mfma_f32_16x16x32_bf16 v[108:111], v[80:83], v[48:51], v[108:111]\n" \
"s_xor_b32 s57, s56, s88\n" \
"buffer_load_dwordx4 v[48:51], v3, s[36:39], s57 offen offset:0 sc1\n" \
"v_mfma_f32_16x16x32_bf16 v[96:99], a[100:103], v[52:55], v[96:99]\n" \
"v_mfma_f32_16x16x32_bf16 v[100:103], a[228:231], v[52:55], v[100:103]\n" \
"ds_read_b128 v[80:83], v2 offset:4096\n" \
"v_mfma_f32_16x16x32_bf16 v[104:107], v[228:231], v[52:55], v[104:107]\n" \
"s_waitcnt vmcnt(15) lgkmcnt(3)\n" \
"v_mfma_f32_16x16x32_bf16 v[108:111], v[84:87], v[52:55], v[108:111]\n" \
"buffer_load_dwordx4 v[52:55], v3, s[36:39], s57 offen offset:1024 sc1\n" \
"v_mfma_f32_16x16x32_bf16 v[96:99], a[104:107], v[56:59], v[96:99]\n" \
"v_mfma_f32_16x16x32_bf16 v[100:103], a[232:235], v[56:59], v[100:103]\n" \
"ds_read_b128 v[84:87], v2 offset:5120\n" \
"s_lshl_b32 s80, s44, 16\n" \
"v_mfma_f32_16x16x32_bf16 v[104:107], v[232:235], v[56:59], v[104:107]\n" \
"s_waitcnt vmcnt(15) lgkmcnt(3)\n" \
"s_add_u32 s80, s80, s78\n" \
"v_mfma_f32_16x16x32_bf16 v[108:111], v[88:91], v[56:59], v[108:111]\n" \
"buffer_load_dwordx4 v[56:59], v3, s[36:39], s57 offen offset:2048 sc1\n" \
"v_mfma_f32_16x16x32_bf16 v[96:99], a[108:111], v[60:63], v[96:99]\n" \
"v_mfma_f32_16x16x32_bf16 v[100:103], a[236:239], v[60:63], v[100:103]\n" \
"ds_read_b128 v[88:91], v2 offset:6144\n" \
"s_lshr_b32 s58, s80, 8\n" \
"v_mfma_f32_16x16x32_bf16 v[104:107], v[236:239], v[60:63], v[104:107]\n" \
"s_waitcnt vmcnt(15) lgkmcnt(3)\n" \
"s_and_b32 s58, s58, 0x2000\n" \
"v_mfma_f32_16x16x32_bf16 v[108:111], v[92:95], v[60:63], v[108:111]\n" \
"buffer_load_dwordx4 v[60:63], v3, s[36:39], s57 offen offset:3072 sc1\n" \
"v_mfma_f32_16x16x32_bf16 v[96:99], a[112:115], v[64:67], v[96:99]\n" \
"v_mfma_f32_16x16x32_bf16 v[100:103], a[240:243], v[64:67], v[100:103]\n" \
"ds_read_b128 v[92:95], v2 offset:7168\n" \
"s_xor_b32 s80, s80, s58\n" \
"v_mfma_f32_16x16x32_bf16 v[104:107], v[240:243], v[64:67], v[104:107]\n" \
"s_waitcnt vmcnt(15) lgkmcnt(3)\n" \
"v_mfma_f32_16x16x32_bf16 v[108:111], v[80:83], v[64:67], v[108:111]\n" \
"s_xor_b32 s57, s56, s89\n" \
"buffer_load_dwordx4 v[64:67], v3, s[36:39], s57 offen offset:0 sc1\n" \
"v_mfma_f32_16x16x32_bf16 v[96:99], a[116:119], v[68:71], v[96:99]\n" \
"buffer_load_dword v11, v4, s[36:39], 0 offen sc1\n" \
"v_mfma_f32_16x16x32_bf16 v[100:103], a[244:247], v[68:71], v[100:103]\n" \
"ds_read_b128 v[80:83], v2 offset:8192\n" \
"v_mfma_f32_16x16x32_bf16 v[104:107], v[244:247], v[68:71], v[104:107]\n" \
"s_waitcnt vmcnt(16) lgkmcnt(3)\n" \
"v_mfma_f32_16x16x32_bf16 v[108:111], v[84:87], v[68:71], v[108:111]\n" \
"buffer_load_dwordx4 v[68:71], v3, s[36:39], s57 offen offset:1024 sc1\n" \
"v_mfma_f32_16x16x32_bf16 v[96:99], a[120:123], v[72:75], v[96:99]\n" \
"s_add_u32 s55, s55, 1\n" \
"s_mov_b32 s84, 0x4000\n" \
"v_mfma_f32_16x16x32_bf16 v[100:103], a[248:251], v[72:75], v[100:103]\n" \
"ds_read_b128 v[84:87], v2 offset:9216\n" \
"v_mfma_f32_16x16x32_bf16 v[104:107], v[248:251], v[72:75], v[104:107]\n" \
"s_waitcnt vmcnt(16) lgkmcnt(3)\n" \
"v_mfma_f32_16x16x32_bf16 v[108:111], v[88:91], v[72:75], v[108:111]\n" \
"buffer_load_dwordx4 v[72:75], v3, s[36:39], s57 offen offset:2048 sc1\n" \
"v_mfma_f32_16x16x32_bf16 v[112:115], a[124:127], v[76:79], v[96:99]\n" \
"s_mov_b32 s76, s80\n" \
"v_mfma_f32_16x16x32_bf16 v[116:119], a[252:255], v[76:79], v[100:103]\n" \
"ds_read_b128 v[88:91], v2 offset:10240\n" \
"s_mov_b32 s44, s82\n" \
"v_mfma_f32_16x16x32_bf16 v[120:123], v[252:255], v[76:79], v[104:107]\n" \
"s_waitcnt vmcnt(15) lgkmcnt(3)\n" \
"v_mfma_f32_16x16x32_bf16 v[124:127], v[92:95], v[76:79], v[108:111]\n" \
"buffer_load_dwordx4 v[76:79], v3, s[36:39], s57 offen offset:3072 sc1\n" \
"s_cmp_eq_u32 s81, 0\n" \
"s_cbranch_scc1 .Lswo_loop_%=\n" \
"s_nop 7\n" \
"s_nop 7\n" \
"s_waitcnt vmcnt(0) lgkmcnt(0)\n" \
"ds_write_b128 v7, v[112:115] offset:0\n" \
"v_xor_b32 v7, 64, v7\n" \
"ds_write_b128 v7, v[116:119] offset:1024\n" \
"v_xor_b32 v7, 192, v7\n" \
"ds_write_b128 v7, v[120:123] offset:2048\n" \
"v_xor_b32 v7, 64, v7\n" \
"ds_write_b128 v7, v[124:127] offset:3072\n" \
"v_xor_b32 v7, 192, v7\n" \
"s_waitcnt lgkmcnt(0)\n" \
"s_barrier\n" \
"ds_read_b128 v[112:115], v8 offset:0\n" \
"ds_read_b128 v[116:119], v8 offset:4096\n" \
"ds_read_b128 v[120:123], v8 offset:8192\n" \
"ds_read_b128 v[124:127], v8 offset:12288\n" \
"s_waitcnt lgkmcnt(2)\n" \
"v_add_f32 v112, v112, v116\n" \
"v_add_f32 v113, v113, v117\n" \
"v_add_f32 v114, v114, v118\n" \
"v_add_f32 v115, v115, v119\n" \
"s_waitcnt lgkmcnt(0)\n" \
"v_add_f32 v120, v120, v124\n" \
"v_add_f32 v121, v121, v125\n" \
"v_add_f32 v122, v122, v126\n" \
"v_add_f32 v123, v123, v127\n" \
"v_add_f32 v112, v112, v120\n" \
"v_add_f32 v113, v113, v121\n" \
"v_add_f32 v114, v114, v122\n" \
"v_add_f32 v115, v115, v123\n" \
"v_xor_b32 v8, s84, v8\n" \
"v_cvt_pk_bf16_f32 v0, v112, v113\n" \
"v_cvt_pk_bf16_f32 v1, v114, v115\n" \
"v_xor_b32 v7, 0x4000, v7\n" \
"s_cmp_eq_u32 s98, 0\n" \
"s_cbranch_scc0 .Lswo_f_%=\n" \
"v_lshlrev_b32 v112, 16, v0\n" \
"v_and_b32 v113, 0xffff0000, v0\n" \
"v_lshlrev_b32 v114, 16, v1\n" \
"v_and_b32 v115, 0xffff0000, v1\n" \
"v_lshlrev_b32 v116, 16, v14\n" \
"v_and_b32 v117, 0xffff0000, v14\n" \
"v_lshlrev_b32 v118, 16, v15\n" \
"v_and_b32 v119, 0xffff0000, v15\n" \
"v_lshlrev_b32 v120, 16, v12\n" \
"v_and_b32 v121, 0xffff0000, v12\n" \
"v_lshlrev_b32 v122, 16, v13\n" \
"v_and_b32 v123, 0xffff0000, v13\n" \
"v_fmac_f32 v120, v112, v116\n" \
"v_fmac_f32 v121, v113, v117\n" \
"v_fmac_f32 v122, v114, v118\n" \
"v_fmac_f32 v123, v115, v119\n" \
"v_cvt_pk_bf16_f32 v12, v120, v121\n" \
"v_cvt_pk_bf16_f32 v13, v122, v123\n" \
"buffer_store_dwordx2 v[12:13], v5, s[68:71], s77 offen\n" \
"s_branch .Lswo_g_%=\n" \
".Lswo_f_%=:\n" \
"buffer_store_dwordx2 v[0:1], v6, s[36:39], s76 offen sc1\n" \
"s_cmp_lg_u32 s55, s95\n" \
"s_cbranch_scc1 .Lswo_g_%=\n" \
"s_waitcnt vmcnt(0) lgkmcnt(0)\n" \
"v_mov_b32 v9, 1\n" \
"s_mov_b64 exec, 1\n" \
"buffer_atomic_add v9, off, s[36:39], s49 offset:0\n" \
"s_mov_b64 exec, -1\n" \
"s_mov_b32 s79, 0\n" \
"s_mov_b32 s98, 0\n" \
"s_branch .Lswo_o_%=\n" \
".Lswo_ok_%=:\n" \
".Lswo_g_%=:\n" \
"s_cmp_lt_u32 s55, s46\n" \
"s_cbranch_scc1 .Lswo_prime_%=\n" \
"s_branch .Lswo_end_%=\n" \
".Lswo_w_%=:\n" \
"s_nop 7\n" \
"s_nop 7\n" \
"s_waitcnt vmcnt(0) lgkmcnt(0)\n" \
"s_mov_b32 s61, 0\n" \
".Lswo_wa_%=:\n" \
"s_cmp_lg_u32 s60, 0\n" \
"s_cbranch_scc1 .Lswo_wk_%=\n" \
"buffer_load_dword v11, v4, s[36:39], 0 offen sc1\n" \
"s_waitcnt vmcnt(0)\n" \
"v_cmp_gt_u32_e64 s[62:63], s59, v11\n" \
"s_nop 4\n" \
"s_cmp_eq_u64 s[62:63], 0\n" \
"s_cbranch_scc1 .Lswo_wk_%=\n" \
"s_sleep 1\n" \
"s_add_u32 s61, s61, 1\n" \
"s_cmp_lt_u32 s61, 65536\n" \
"s_cbranch_scc1 .Lswo_wa_%=\n" \
"s_mov_b32 s60, 1\n" \
"v_mov_b32 v11, 256\n" \
"v_mov_b32 v9, 1\n" \
"buffer_store_dword v9, v11, s[36:39], 0 offen sc1\n" \
"s_waitcnt vmcnt(0) lgkmcnt(0)\n" \
"s_branch .Lswo_wk_%=\n" \
".Lswo_o_%=:\n" \
"s_nop 7\n" \
"s_nop 7\n" \
"s_waitcnt vmcnt(0) lgkmcnt(0)\n" \
"s_mov_b32 s61, 0\n" \
".Lswo_oa_%=:\n" \
"s_cmp_lg_u32 s60, 0\n" \
"s_cbranch_scc1 .Lswo_ok_%=\n" \
"buffer_load_dword v9, off, s[36:39], s49 offset:64 sc1\n" \
"s_sub_u32 s54, 10, s47\n" \
"s_waitcnt vmcnt(0)\n" \
"v_cmp_gt_u32 vcc, s54, v9\n" \
"s_nop 4\n" \
"s_cbranch_vccz .Lswo_ok_%=\n" \
"s_sleep 1\n" \
"s_add_u32 s61, s61, 1\n" \
"s_cmp_lt_u32 s61, 65536\n" \
"s_cbranch_scc1 .Lswo_oa_%=\n" \
"s_mov_b32 s60, 1\n" \
"v_mov_b32 v11, 256\n" \
"v_mov_b32 v9, 1\n" \
"buffer_store_dword v9, v11, s[36:39], 0 offen sc1\n" \
"s_waitcnt vmcnt(0) lgkmcnt(0)\n" \
"s_branch .Lswo_ok_%=\n" \
".Lswo_end_%=:\n" \
"s_endpgm\n"
#define ASM_S64_G2 \
"s_nop 7\n" \
"s_nop 7\n" \
"s_lshl_b32 s52, s48, 15\n" \
"s_lshl_b32 s53, s48, 13\n" \
"s_add_u32 s53, s53, 98304\n" \
"s_sub_u32 s54, s53, s52\n" \
"v_add_u32 v9, s54, v1\n" \
"s_add_u32 m0, s52, 0\n" \
"s_add_u32 s54, s97, 0\n" \
"s_lshr_b32 s55, s54, 8\n" \
"s_and_b32 s55, s55, 0x2000\n" \
"s_xor_b32 s54, s54, s55\n" \
"buffer_load_dwordx4 v0, s[40:43], s54 offen sc1 lds\n" \
"s_add_u32 m0, s52, 1024\n" \
"s_add_u32 s54, s97, 32768\n" \
"s_lshr_b32 s55, s54, 8\n" \
"s_and_b32 s55, s55, 0x2000\n" \
"s_xor_b32 s54, s54, s55\n" \
"buffer_load_dwordx4 v0, s[40:43], s54 offen sc1 lds\n" \
"s_add_u32 m0, s52, 2048\n" \
"s_add_u32 s54, s97, 65536\n" \
"s_lshr_b32 s55, s54, 8\n" \
"s_and_b32 s55, s55, 0x2000\n" \
"s_xor_b32 s54, s54, s55\n" \
"buffer_load_dwordx4 v0, s[40:43], s54 offen sc1 lds\n" \
"s_add_u32 m0, s52, 3072\n" \
"s_add_u32 s54, s97, 98304\n" \
"s_lshr_b32 s55, s54, 8\n" \
"s_and_b32 s55, s55, 0x2000\n" \
"s_xor_b32 s54, s54, s55\n" \
"buffer_load_dwordx4 v0, s[40:43], s54 offen sc1 lds\n" \
"s_mov_b32 s54, s99\n" \
".Lsg2_z0_%=:\n" \
"s_sleep 1\n" \
"s_sub_u32 s54, s54, 1\n" \
"s_cbranch_scc0 .Lsg2_z0_%=\n" \
"s_add_u32 m0, s52, 4096\n" \
"s_add_u32 s54, s97, 131072\n" \
"s_lshr_b32 s55, s54, 8\n" \
"s_and_b32 s55, s55, 0x2000\n" \
"s_xor_b32 s54, s54, s55\n" \
"buffer_load_dwordx4 v0, s[40:43], s54 offen sc1 lds\n" \
"s_add_u32 m0, s52, 5120\n" \
"s_add_u32 s54, s97, 163840\n" \
"s_lshr_b32 s55, s54, 8\n" \
"s_and_b32 s55, s55, 0x2000\n" \
"s_xor_b32 s54, s54, s55\n" \
"buffer_load_dwordx4 v0, s[40:43], s54 offen sc1 lds\n" \
"s_add_u32 m0, s52, 6144\n" \
"s_add_u32 s54, s97, 196608\n" \
"s_lshr_b32 s55, s54, 8\n" \
"s_and_b32 s55, s55, 0x2000\n" \
"s_xor_b32 s54, s54, s55\n" \
"buffer_load_dwordx4 v0, s[40:43], s54 offen sc1 lds\n" \
"s_add_u32 m0, s52, 7168\n" \
"s_add_u32 s54, s97, 229376\n" \
"s_lshr_b32 s55, s54, 8\n" \
"s_and_b32 s55, s55, 0x2000\n" \
"s_xor_b32 s54, s54, s55\n" \
"buffer_load_dwordx4 v0, s[40:43], s54 offen sc1 lds\n" \
"s_mov_b32 s54, s99\n" \
".Lsg2_z1_%=:\n" \
"s_sleep 1\n" \
"s_sub_u32 s54, s54, 1\n" \
"s_cbranch_scc0 .Lsg2_z1_%=\n" \
"s_add_u32 m0, s52, 8192\n" \
"s_add_u32 s54, s97, 262144\n" \
"s_lshr_b32 s55, s54, 8\n" \
"s_and_b32 s55, s55, 0x2000\n" \
"s_xor_b32 s54, s54, s55\n" \
"buffer_load_dwordx4 v0, s[40:43], s54 offen sc1 lds\n" \
"s_add_u32 m0, s52, 9216\n" \
"s_add_u32 s54, s97, 294912\n" \
"s_lshr_b32 s55, s54, 8\n" \
"s_and_b32 s55, s55, 0x2000\n" \
"s_xor_b32 s54, s54, s55\n" \
"buffer_load_dwordx4 v0, s[40:43], s54 offen sc1 lds\n" \
"s_add_u32 m0, s52, 10240\n" \
"s_add_u32 s54, s97, 327680\n" \
"s_lshr_b32 s55, s54, 8\n" \
"s_and_b32 s55, s55, 0x2000\n" \
"s_xor_b32 s54, s54, s55\n" \
"buffer_load_dwordx4 v0, s[40:43], s54 offen sc1 lds\n" \
"s_add_u32 m0, s52, 11264\n" \
"s_add_u32 s54, s97, 360448\n" \
"s_lshr_b32 s55, s54, 8\n" \
"s_and_b32 s55, s55, 0x2000\n" \
"s_xor_b32 s54, s54, s55\n" \
"buffer_load_dwordx4 v0, s[40:43], s54 offen sc1 lds\n" \
"s_mov_b32 s54, s99\n" \
".Lsg2_z2_%=:\n" \
"s_sleep 1\n" \
"s_sub_u32 s54, s54, 1\n" \
"s_cbranch_scc0 .Lsg2_z2_%=\n" \
"s_add_u32 m0, s52, 12288\n" \
"s_add_u32 s54, s97, 393216\n" \
"s_lshr_b32 s55, s54, 8\n" \
"s_and_b32 s55, s55, 0x2000\n" \
"s_xor_b32 s54, s54, s55\n" \
"buffer_load_dwordx4 v0, s[40:43], s54 offen sc1 lds\n" \
"s_add_u32 m0, s52, 13312\n" \
"s_add_u32 s54, s97, 425984\n" \
"s_lshr_b32 s55, s54, 8\n" \
"s_and_b32 s55, s55, 0x2000\n" \
"s_xor_b32 s54, s54, s55\n" \
"buffer_load_dwordx4 v0, s[40:43], s54 offen sc1 lds\n" \
"s_add_u32 m0, s52, 14336\n" \
"s_add_u32 s54, s97, 458752\n" \
"s_lshr_b32 s55, s54, 8\n" \
"s_and_b32 s55, s55, 0x2000\n" \
"s_xor_b32 s54, s54, s55\n" \
"buffer_load_dwordx4 v0, s[40:43], s54 offen sc1 lds\n" \
"s_add_u32 m0, s52, 15360\n" \
"s_add_u32 s54, s97, 491520\n" \
"s_lshr_b32 s55, s54, 8\n" \
"s_and_b32 s55, s55, 0x2000\n" \
"s_xor_b32 s54, s54, s55\n" \
"buffer_load_dwordx4 v0, s[40:43], s54 offen sc1 lds\n" \
"s_mov_b32 s54, s99\n" \
".Lsg2_z3_%=:\n" \
"s_sleep 1\n" \
"s_sub_u32 s54, s54, 1\n" \
"s_cbranch_scc0 .Lsg2_z3_%=\n" \
"s_add_u32 m0, s52, 16384\n" \
"s_add_u32 s54, s97, 524288\n" \
"s_lshr_b32 s55, s54, 8\n" \
"s_and_b32 s55, s55, 0x2000\n" \
"s_xor_b32 s54, s54, s55\n" \
"buffer_load_dwordx4 v0, s[40:43], s54 offen sc1 lds\n" \
"s_add_u32 m0, s52, 17408\n" \
"s_add_u32 s54, s97, 557056\n" \
"s_lshr_b32 s55, s54, 8\n" \
"s_and_b32 s55, s55, 0x2000\n" \
"s_xor_b32 s54, s54, s55\n" \
"buffer_load_dwordx4 v0, s[40:43], s54 offen sc1 lds\n" \
"s_add_u32 m0, s52, 18432\n" \
"s_add_u32 s54, s97, 589824\n" \
"s_lshr_b32 s55, s54, 8\n" \
"s_and_b32 s55, s55, 0x2000\n" \
"s_xor_b32 s54, s54, s55\n" \
"buffer_load_dwordx4 v0, s[40:43], s54 offen sc1 lds\n" \
"s_add_u32 m0, s52, 19456\n" \
"s_add_u32 s54, s97, 622592\n" \
"s_lshr_b32 s55, s54, 8\n" \
"s_and_b32 s55, s55, 0x2000\n" \
"s_xor_b32 s54, s54, s55\n" \
"buffer_load_dwordx4 v0, s[40:43], s54 offen sc1 lds\n" \
"s_mov_b32 s54, s99\n" \
".Lsg2_z4_%=:\n" \
"s_sleep 1\n" \
"s_sub_u32 s54, s54, 1\n" \
"s_cbranch_scc0 .Lsg2_z4_%=\n" \
"s_add_u32 m0, s52, 20480\n" \
"s_add_u32 s54, s97, 655360\n" \
"s_lshr_b32 s55, s54, 8\n" \
"s_and_b32 s55, s55, 0x2000\n" \
"s_xor_b32 s54, s54, s55\n" \
"buffer_load_dwordx4 v0, s[40:43], s54 offen sc1 lds\n" \
"s_add_u32 m0, s52, 21504\n" \
"s_add_u32 s54, s97, 688128\n" \
"s_lshr_b32 s55, s54, 8\n" \
"s_and_b32 s55, s55, 0x2000\n" \
"s_xor_b32 s54, s54, s55\n" \
"buffer_load_dwordx4 v0, s[40:43], s54 offen sc1 lds\n" \
"s_add_u32 m0, s52, 22528\n" \
"s_add_u32 s54, s97, 720896\n" \
"s_lshr_b32 s55, s54, 8\n" \
"s_and_b32 s55, s55, 0x2000\n" \
"s_xor_b32 s54, s54, s55\n" \
"buffer_load_dwordx4 v0, s[40:43], s54 offen sc1 lds\n" \
"s_add_u32 m0, s52, 23552\n" \
"s_add_u32 s54, s97, 753664\n" \
"s_lshr_b32 s55, s54, 8\n" \
"s_and_b32 s55, s55, 0x2000\n" \
"s_xor_b32 s54, s54, s55\n" \
"buffer_load_dwordx4 v0, s[40:43], s54 offen sc1 lds\n" \
"s_mov_b32 s54, s99\n" \
".Lsg2_z5_%=:\n" \
"s_sleep 1\n" \
"s_sub_u32 s54, s54, 1\n" \
"s_cbranch_scc0 .Lsg2_z5_%=\n" \
"s_add_u32 m0, s52, 24576\n" \
"s_add_u32 s54, s97, 786432\n" \
"s_lshr_b32 s55, s54, 8\n" \
"s_and_b32 s55, s55, 0x2000\n" \
"s_xor_b32 s54, s54, s55\n" \
"buffer_load_dwordx4 v0, s[40:43], s54 offen sc1 lds\n" \
"s_add_u32 m0, s52, 25600\n" \
"s_add_u32 s54, s97, 819200\n" \
"s_lshr_b32 s55, s54, 8\n" \
"s_and_b32 s55, s55, 0x2000\n" \
"s_xor_b32 s54, s54, s55\n" \
"buffer_load_dwordx4 v0, s[40:43], s54 offen sc1 lds\n" \
"s_add_u32 m0, s52, 26624\n" \
"s_add_u32 s54, s97, 851968\n" \
"s_lshr_b32 s55, s54, 8\n" \
"s_and_b32 s55, s55, 0x2000\n" \
"s_xor_b32 s54, s54, s55\n" \
"buffer_load_dwordx4 v0, s[40:43], s54 offen sc1 lds\n" \
"s_add_u32 m0, s52, 27648\n" \
"s_add_u32 s54, s97, 884736\n" \
"s_lshr_b32 s55, s54, 8\n" \
"s_and_b32 s55, s55, 0x2000\n" \
"s_xor_b32 s54, s54, s55\n" \
"buffer_load_dwordx4 v0, s[40:43], s54 offen sc1 lds\n" \
"s_mov_b32 s54, s99\n" \
".Lsg2_z6_%=:\n" \
"s_sleep 1\n" \
"s_sub_u32 s54, s54, 1\n" \
"s_cbranch_scc0 .Lsg2_z6_%=\n" \
"s_add_u32 m0, s52, 28672\n" \
"s_add_u32 s54, s97, 917504\n" \
"s_lshr_b32 s55, s54, 8\n" \
"s_and_b32 s55, s55, 0x2000\n" \
"s_xor_b32 s54, s54, s55\n" \
"buffer_load_dwordx4 v0, s[40:43], s54 offen sc1 lds\n" \
"s_add_u32 m0, s52, 29696\n" \
"s_add_u32 s54, s97, 950272\n" \
"s_lshr_b32 s55, s54, 8\n" \
"s_and_b32 s55, s55, 0x2000\n" \
"s_xor_b32 s54, s54, s55\n" \
"buffer_load_dwordx4 v0, s[40:43], s54 offen sc1 lds\n" \
"s_add_u32 m0, s52, 30720\n" \
"s_add_u32 s54, s97, 983040\n" \
"s_lshr_b32 s55, s54, 8\n" \
"s_and_b32 s55, s55, 0x2000\n" \
"s_xor_b32 s54, s54, s55\n" \
"buffer_load_dwordx4 v0, s[40:43], s54 offen sc1 lds\n" \
"s_add_u32 m0, s52, 31744\n" \
"s_add_u32 s54, s97, 1015808\n" \
"s_lshr_b32 s55, s54, 8\n" \
"s_and_b32 s55, s55, 0x2000\n" \
"s_xor_b32 s54, s54, s55\n" \
"buffer_load_dwordx4 v0, s[40:43], s54 offen sc1 lds\n" \
"s_mov_b32 s54, s99\n" \
".Lsg2_z7_%=:\n" \
"s_sleep 1\n" \
"s_sub_u32 s54, s54, 1\n" \
"s_cbranch_scc0 .Lsg2_z7_%=\n" \
"s_add_u32 m0, s53, 32768\n" \
"s_add_u32 s54, s97, 1048576\n" \
"s_lshr_b32 s55, s54, 8\n" \
"s_and_b32 s55, s55, 0x2000\n" \
"s_xor_b32 s54, s54, s55\n" \
"buffer_load_dwordx4 v0, s[40:43], s54 offen sc1 lds\n" \
"s_add_u32 m0, s53, 33792\n" \
"s_add_u32 s54, s97, 1081344\n" \
"s_lshr_b32 s55, s54, 8\n" \
"s_and_b32 s55, s55, 0x2000\n" \
"s_xor_b32 s54, s54, s55\n" \
"buffer_load_dwordx4 v0, s[40:43], s54 offen sc1 lds\n" \
"s_add_u32 m0, s53, 34816\n" \
"s_add_u32 s54, s97, 1114112\n" \
"s_lshr_b32 s55, s54, 8\n" \
"s_and_b32 s55, s55, 0x2000\n" \
"s_xor_b32 s54, s54, s55\n" \
"buffer_load_dwordx4 v0, s[40:43], s54 offen sc1 lds\n" \
"s_add_u32 m0, s53, 35840\n" \
"s_add_u32 s54, s97, 1146880\n" \
"s_lshr_b32 s55, s54, 8\n" \
"s_and_b32 s55, s55, 0x2000\n" \
"s_xor_b32 s54, s54, s55\n" \
"buffer_load_dwordx4 v0, s[40:43], s54 offen sc1 lds\n" \
"s_mov_b32 s54, s99\n" \
".Lsg2_z8_%=:\n" \
"s_sleep 1\n" \
"s_sub_u32 s54, s54, 1\n" \
"s_cbranch_scc0 .Lsg2_z8_%=\n" \
"s_add_u32 m0, s53, 36864\n" \
"s_add_u32 s54, s97, 1179648\n" \
"s_lshr_b32 s55, s54, 8\n" \
"s_and_b32 s55, s55, 0x2000\n" \
"s_xor_b32 s54, s54, s55\n" \
"buffer_load_dwordx4 v0, s[40:43], s54 offen sc1 lds\n" \
"s_add_u32 m0, s53, 37888\n" \
"s_add_u32 s54, s97, 1212416\n" \
"s_lshr_b32 s55, s54, 8\n" \
"s_and_b32 s55, s55, 0x2000\n" \
"s_xor_b32 s54, s54, s55\n" \
"buffer_load_dwordx4 v0, s[40:43], s54 offen sc1 lds\n" \
"s_add_u32 m0, s53, 38912\n" \
"s_add_u32 s54, s97, 1245184\n" \
"s_lshr_b32 s55, s54, 8\n" \
"s_and_b32 s55, s55, 0x2000\n" \
"s_xor_b32 s54, s54, s55\n" \
"buffer_load_dwordx4 v0, s[40:43], s54 offen sc1 lds\n" \
"s_add_u32 m0, s53, 39936\n" \
"s_add_u32 s54, s97, 1277952\n" \
"s_lshr_b32 s55, s54, 8\n" \
"s_and_b32 s55, s55, 0x2000\n" \
"s_xor_b32 s54, s54, s55\n" \
"buffer_load_dwordx4 v0, s[40:43], s54 offen sc1 lds\n" \
"s_mov_b32 s54, s99\n" \
".Lsg2_z9_%=:\n" \
"s_sleep 1\n" \
"s_sub_u32 s54, s54, 1\n" \
"s_cbranch_scc0 .Lsg2_z9_%=\n" \
"s_waitcnt vmcnt(36)\n" \
"ds_read_b64_tr_b16 a[0:1], v1 offset:0\n" \
"ds_read_b64_tr_b16 a[2:3], v1 offset:512\n" \
"ds_read_b64_tr_b16 a[128:129], v1 offset:32\n" \
"ds_read_b64_tr_b16 a[130:131], v1 offset:544\n" \
"ds_read_b64_tr_b16 v[128:129], v1 offset:64\n" \
"ds_read_b64_tr_b16 v[130:131], v1 offset:576\n" \
"ds_read_b64_tr_b16 v[16:17], v1 offset:96\n" \
"ds_read_b64_tr_b16 v[18:19], v1 offset:608\n" \
"s_waitcnt lgkmcnt(0)\n" \
"s_add_u32 m0, s52, 0\n" \
"s_add_u32 s54, s97, 1310720\n" \
"s_lshr_b32 s55, s54, 8\n" \
"s_and_b32 s55, s55, 0x2000\n" \
"s_xor_b32 s54, s54, s55\n" \
"buffer_load_dwordx4 v0, s[40:43], s54 offen sc1 lds\n" \
"s_add_u32 m0, s52, 1024\n" \
"s_add_u32 s54, s97, 1343488\n" \
"s_lshr_b32 s55, s54, 8\n" \
"s_and_b32 s55, s55, 0x2000\n" \
"s_xor_b32 s54, s54, s55\n" \
"buffer_load_dwordx4 v0, s[40:43], s54 offen sc1 lds\n" \
"s_add_u32 m0, s52, 2048\n" \
"s_add_u32 s54, s97, 1376256\n" \
"s_lshr_b32 s55, s54, 8\n" \
"s_and_b32 s55, s55, 0x2000\n" \
"s_xor_b32 s54, s54, s55\n" \
"buffer_load_dwordx4 v0, s[40:43], s54 offen sc1 lds\n" \
"s_add_u32 m0, s52, 3072\n" \
"s_add_u32 s54, s97, 1409024\n" \
"s_lshr_b32 s55, s54, 8\n" \
"s_and_b32 s55, s55, 0x2000\n" \
"s_xor_b32 s54, s54, s55\n" \
"buffer_load_dwordx4 v0, s[40:43], s54 offen sc1 lds\n" \
"s_mov_b32 s54, s99\n" \
".Lsg2_z10_%=:\n" \
"s_sleep 1\n" \
"s_sub_u32 s54, s54, 1\n" \
"s_cbranch_scc0 .Lsg2_z10_%=\n" \
"s_waitcnt vmcnt(36)\n" \
"ds_read_b64_tr_b16 a[4:5], v1 offset:4096\n" \
"ds_read_b64_tr_b16 a[6:7], v1 offset:4608\n" \
"ds_read_b64_tr_b16 a[132:133], v1 offset:4128\n" \
"ds_read_b64_tr_b16 a[134:135], v1 offset:4640\n" \
"ds_read_b64_tr_b16 v[132:133], v1 offset:4160\n" \
"ds_read_b64_tr_b16 v[134:135], v1 offset:4672\n" \
"ds_read_b64_tr_b16 v[20:21], v1 offset:4192\n" \
"ds_read_b64_tr_b16 v[22:23], v1 offset:4704\n" \
"s_waitcnt lgkmcnt(0)\n" \
"s_add_u32 m0, s52, 4096\n" \
"s_add_u32 s54, s97, 1441792\n" \
"s_lshr_b32 s55, s54, 8\n" \
"s_and_b32 s55, s55, 0x2000\n" \
"s_xor_b32 s54, s54, s55\n" \
"buffer_load_dwordx4 v0, s[40:43], s54 offen sc1 lds\n" \
"s_add_u32 m0, s52, 5120\n" \
"s_add_u32 s54, s97, 1474560\n" \
"s_lshr_b32 s55, s54, 8\n" \
"s_and_b32 s55, s55, 0x2000\n" \
"s_xor_b32 s54, s54, s55\n" \
"buffer_load_dwordx4 v0, s[40:43], s54 offen sc1 lds\n" \
"s_add_u32 m0, s52, 6144\n" \
"s_add_u32 s54, s97, 1507328\n" \
"s_lshr_b32 s55, s54, 8\n" \
"s_and_b32 s55, s55, 0x2000\n" \
"s_xor_b32 s54, s54, s55\n" \
"buffer_load_dwordx4 v0, s[40:43], s54 offen sc1 lds\n" \
"s_add_u32 m0, s52, 7168\n" \
"s_add_u32 s54, s97, 1540096\n" \
"s_lshr_b32 s55, s54, 8\n" \
"s_and_b32 s55, s55, 0x2000\n" \
"s_xor_b32 s54, s54, s55\n" \
"buffer_load_dwordx4 v0, s[40:43], s54 offen sc1 lds\n" \
"s_mov_b32 s54, s99\n" \
".Lsg2_z11_%=:\n" \
"s_sleep 1\n" \
"s_sub_u32 s54, s54, 1\n" \
"s_cbranch_scc0 .Lsg2_z11_%=\n" \
"s_waitcnt vmcnt(36)\n" \
"ds_read_b64_tr_b16 a[8:9], v1 offset:8192\n" \
"ds_read_b64_tr_b16 a[10:11], v1 offset:8704\n" \
"ds_read_b64_tr_b16 a[136:137], v1 offset:8224\n" \
"ds_read_b64_tr_b16 a[138:139], v1 offset:8736\n" \
"ds_read_b64_tr_b16 v[136:137], v1 offset:8256\n" \
"ds_read_b64_tr_b16 v[138:139], v1 offset:8768\n" \
"ds_read_b64_tr_b16 v[24:25], v1 offset:8288\n" \
"ds_read_b64_tr_b16 v[26:27], v1 offset:8800\n" \
"s_waitcnt lgkmcnt(0)\n" \
"s_add_u32 m0, s52, 8192\n" \
"s_add_u32 s54, s97, 1572864\n" \
"s_lshr_b32 s55, s54, 8\n" \
"s_and_b32 s55, s55, 0x2000\n" \
"s_xor_b32 s54, s54, s55\n" \
"buffer_load_dwordx4 v0, s[40:43], s54 offen sc1 lds\n" \
"s_add_u32 m0, s52, 9216\n" \
"s_add_u32 s54, s97, 1605632\n" \
"s_lshr_b32 s55, s54, 8\n" \
"s_and_b32 s55, s55, 0x2000\n" \
"s_xor_b32 s54, s54, s55\n" \
"buffer_load_dwordx4 v0, s[40:43], s54 offen sc1 lds\n" \
"s_add_u32 m0, s52, 10240\n" \
"s_add_u32 s54, s97, 1638400\n" \
"s_lshr_b32 s55, s54, 8\n" \
"s_and_b32 s55, s55, 0x2000\n" \
"s_xor_b32 s54, s54, s55\n" \
"buffer_load_dwordx4 v0, s[40:43], s54 offen sc1 lds\n" \
"s_add_u32 m0, s52, 11264\n" \
"s_add_u32 s54, s97, 1671168\n" \
"s_lshr_b32 s55, s54, 8\n" \
"s_and_b32 s55, s55, 0x2000\n" \
"s_xor_b32 s54, s54, s55\n" \
"buffer_load_dwordx4 v0, s[40:43], s54 offen sc1 lds\n" \
"s_mov_b32 s54, s99\n" \
".Lsg2_z12_%=:\n" \
"s_sleep 1\n" \
"s_sub_u32 s54, s54, 1\n" \
"s_cbranch_scc0 .Lsg2_z12_%=\n" \
"s_waitcnt vmcnt(36)\n" \
"ds_read_b64_tr_b16 a[12:13], v1 offset:12288\n" \
"ds_read_b64_tr_b16 a[14:15], v1 offset:12800\n" \
"ds_read_b64_tr_b16 a[140:141], v1 offset:12320\n" \
"ds_read_b64_tr_b16 a[142:143], v1 offset:12832\n" \
"ds_read_b64_tr_b16 v[140:141], v1 offset:12352\n" \
"ds_read_b64_tr_b16 v[142:143], v1 offset:12864\n" \
"ds_read_b64_tr_b16 v[28:29], v1 offset:12384\n" \
"ds_read_b64_tr_b16 v[30:31], v1 offset:12896\n" \
"s_waitcnt lgkmcnt(0)\n" \
"s_add_u32 m0, s52, 12288\n" \
"s_add_u32 s54, s97, 1703936\n" \
"s_lshr_b32 s55, s54, 8\n" \
"s_and_b32 s55, s55, 0x2000\n" \
"s_xor_b32 s54, s54, s55\n" \
"buffer_load_dwordx4 v0, s[40:43], s54 offen sc1 lds\n" \
"s_add_u32 m0, s52, 13312\n" \
"s_add_u32 s54, s97, 1736704\n" \
"s_lshr_b32 s55, s54, 8\n" \
"s_and_b32 s55, s55, 0x2000\n" \
"s_xor_b32 s54, s54, s55\n" \
"buffer_load_dwordx4 v0, s[40:43], s54 offen sc1 lds\n" \
"s_add_u32 m0, s52, 14336\n" \
"s_add_u32 s54, s97, 1769472\n" \
"s_lshr_b32 s55, s54, 8\n" \
"s_and_b32 s55, s55, 0x2000\n" \
"s_xor_b32 s54, s54, s55\n" \
"buffer_load_dwordx4 v0, s[40:43], s54 offen sc1 lds\n" \
"s_add_u32 m0, s52, 15360\n" \
"s_add_u32 s54, s97, 1802240\n" \
"s_lshr_b32 s55, s54, 8\n" \
"s_and_b32 s55, s55, 0x2000\n" \
"s_xor_b32 s54, s54, s55\n" \
"buffer_load_dwordx4 v0, s[40:43], s54 offen sc1 lds\n" \
"s_mov_b32 s54, s99\n" \
".Lsg2_z13_%=:\n" \
"s_sleep 1\n" \
"s_sub_u32 s54, s54, 1\n" \
"s_cbranch_scc0 .Lsg2_z13_%=\n" \
"s_waitcnt vmcnt(36)\n" \
"ds_read_b64_tr_b16 a[16:17], v1 offset:16384\n" \
"ds_read_b64_tr_b16 a[18:19], v1 offset:16896\n" \
"ds_read_b64_tr_b16 a[144:145], v1 offset:16416\n" \
"ds_read_b64_tr_b16 a[146:147], v1 offset:16928\n" \
"ds_read_b64_tr_b16 v[144:145], v1 offset:16448\n" \
"ds_read_b64_tr_b16 v[146:147], v1 offset:16960\n" \
"ds_read_b64_tr_b16 v[32:33], v1 offset:16480\n" \
"ds_read_b64_tr_b16 v[34:35], v1 offset:16992\n" \
"s_waitcnt lgkmcnt(0)\n" \
"s_add_u32 m0, s52, 16384\n" \
"s_add_u32 s54, s97, 1835008\n" \
"s_lshr_b32 s55, s54, 8\n" \
"s_and_b32 s55, s55, 0x2000\n" \
"s_xor_b32 s54, s54, s55\n" \
"buffer_load_dwordx4 v0, s[40:43], s54 offen sc1 lds\n" \
"s_add_u32 m0, s52, 17408\n" \
"s_add_u32 s54, s97, 1867776\n" \
"s_lshr_b32 s55, s54, 8\n" \
"s_and_b32 s55, s55, 0x2000\n" \
"s_xor_b32 s54, s54, s55\n" \
"buffer_load_dwordx4 v0, s[40:43], s54 offen sc1 lds\n" \
"s_add_u32 m0, s52, 18432\n" \
"s_add_u32 s54, s97, 1900544\n" \
"s_lshr_b32 s55, s54, 8\n" \
"s_and_b32 s55, s55, 0x2000\n" \
"s_xor_b32 s54, s54, s55\n" \
"buffer_load_dwordx4 v0, s[40:43], s54 offen sc1 lds\n" \
"s_add_u32 m0, s52, 19456\n" \
"s_add_u32 s54, s97, 1933312\n" \
"s_lshr_b32 s55, s54, 8\n" \
"s_and_b32 s55, s55, 0x2000\n" \
"s_xor_b32 s54, s54, s55\n" \
"buffer_load_dwordx4 v0, s[40:43], s54 offen sc1 lds\n" \
"s_mov_b32 s54, s99\n" \
".Lsg2_z14_%=:\n" \
"s_sleep 1\n" \
"s_sub_u32 s54, s54, 1\n" \
"s_cbranch_scc0 .Lsg2_z14_%=\n" \
"s_waitcnt vmcnt(36)\n" \
"ds_read_b64_tr_b16 a[20:21], v1 offset:20480\n" \
"ds_read_b64_tr_b16 a[22:23], v1 offset:20992\n" \
"ds_read_b64_tr_b16 a[148:149], v1 offset:20512\n" \
"ds_read_b64_tr_b16 a[150:151], v1 offset:21024\n" \
"ds_read_b64_tr_b16 v[148:149], v1 offset:20544\n" \
"ds_read_b64_tr_b16 v[150:151], v1 offset:21056\n" \
"ds_read_b64_tr_b16 v[36:37], v1 offset:20576\n" \
"ds_read_b64_tr_b16 v[38:39], v1 offset:21088\n" \
"s_waitcnt lgkmcnt(0)\n" \
"s_add_u32 m0, s52, 20480\n" \
"s_add_u32 s54, s97, 1966080\n" \
"s_lshr_b32 s55, s54, 8\n" \
"s_and_b32 s55, s55, 0x2000\n" \
"s_xor_b32 s54, s54, s55\n" \
"buffer_load_dwordx4 v0, s[40:43], s54 offen sc1 lds\n" \
"s_add_u32 m0, s52, 21504\n" \
"s_add_u32 s54, s97, 1998848\n" \
"s_lshr_b32 s55, s54, 8\n" \
"s_and_b32 s55, s55, 0x2000\n" \
"s_xor_b32 s54, s54, s55\n" \
"buffer_load_dwordx4 v0, s[40:43], s54 offen sc1 lds\n" \
"s_add_u32 m0, s52, 22528\n" \
"s_add_u32 s54, s97, 2031616\n" \
"s_lshr_b32 s55, s54, 8\n" \
"s_and_b32 s55, s55, 0x2000\n" \
"s_xor_b32 s54, s54, s55\n" \
"buffer_load_dwordx4 v0, s[40:43], s54 offen sc1 lds\n" \
"s_add_u32 m0, s52, 23552\n" \
"s_add_u32 s54, s97, 2064384\n" \
"s_lshr_b32 s55, s54, 8\n" \
"s_and_b32 s55, s55, 0x2000\n" \
"s_xor_b32 s54, s54, s55\n" \
"buffer_load_dwordx4 v0, s[40:43], s54 offen sc1 lds\n" \
"s_mov_b32 s54, s99\n" \
".Lsg2_z15_%=:\n" \
"s_sleep 1\n" \
"s_sub_u32 s54, s54, 1\n" \
"s_cbranch_scc0 .Lsg2_z15_%=\n" \
"s_waitcnt vmcnt(36)\n" \
"ds_read_b64_tr_b16 a[24:25], v1 offset:24576\n" \
"ds_read_b64_tr_b16 a[26:27], v1 offset:25088\n" \
"ds_read_b64_tr_b16 a[152:153], v1 offset:24608\n" \
"ds_read_b64_tr_b16 a[154:155], v1 offset:25120\n" \
"ds_read_b64_tr_b16 v[152:153], v1 offset:24640\n" \
"ds_read_b64_tr_b16 v[154:155], v1 offset:25152\n" \
"ds_read_b64_tr_b16 v[40:41], v1 offset:24672\n" \
"ds_read_b64_tr_b16 v[42:43], v1 offset:25184\n" \
"s_waitcnt lgkmcnt(0)\n" \
"s_add_u32 m0, s52, 24576\n" \
"s_add_u32 s54, s97, 2097152\n" \
"s_lshr_b32 s55, s54, 8\n" \
"s_and_b32 s55, s55, 0x2000\n" \
"s_xor_b32 s54, s54, s55\n" \
"buffer_load_dwordx4 v0, s[40:43], s54 offen sc1 lds\n" \
"s_add_u32 m0, s52, 25600\n" \
"s_add_u32 s54, s97, 2129920\n" \
"s_lshr_b32 s55, s54, 8\n" \
"s_and_b32 s55, s55, 0x2000\n" \
"s_xor_b32 s54, s54, s55\n" \
"buffer_load_dwordx4 v0, s[40:43], s54 offen sc1 lds\n" \
"s_add_u32 m0, s52, 26624\n" \
"s_add_u32 s54, s97, 2162688\n" \
"s_lshr_b32 s55, s54, 8\n" \
"s_and_b32 s55, s55, 0x2000\n" \
"s_xor_b32 s54, s54, s55\n" \
"buffer_load_dwordx4 v0, s[40:43], s54 offen sc1 lds\n" \
"s_add_u32 m0, s52, 27648\n" \
"s_add_u32 s54, s97, 2195456\n" \
"s_lshr_b32 s55, s54, 8\n" \
"s_and_b32 s55, s55, 0x2000\n" \
"s_xor_b32 s54, s54, s55\n" \
"buffer_load_dwordx4 v0, s[40:43], s54 offen sc1 lds\n" \
"s_mov_b32 s54, s99\n" \
".Lsg2_z16_%=:\n" \
"s_sleep 1\n" \
"s_sub_u32 s54, s54, 1\n" \
"s_cbranch_scc0 .Lsg2_z16_%=\n" \
"s_waitcnt vmcnt(36)\n" \
"ds_read_b64_tr_b16 a[28:29], v1 offset:28672\n" \
"ds_read_b64_tr_b16 a[30:31], v1 offset:29184\n" \
"ds_read_b64_tr_b16 a[156:157], v1 offset:28704\n" \
"ds_read_b64_tr_b16 a[158:159], v1 offset:29216\n" \
"ds_read_b64_tr_b16 v[156:157], v1 offset:28736\n" \
"ds_read_b64_tr_b16 v[158:159], v1 offset:29248\n" \
"ds_read_b64_tr_b16 v[44:45], v1 offset:28768\n" \
"ds_read_b64_tr_b16 v[46:47], v1 offset:29280\n" \
"s_waitcnt lgkmcnt(0)\n" \
"s_add_u32 m0, s52, 28672\n" \
"s_add_u32 s54, s97, 2228224\n" \
"s_lshr_b32 s55, s54, 8\n" \
"s_and_b32 s55, s55, 0x2000\n" \
"s_xor_b32 s54, s54, s55\n" \
"buffer_load_dwordx4 v0, s[40:43], s54 offen sc1 lds\n" \
"s_add_u32 m0, s52, 29696\n" \
"s_add_u32 s54, s97, 2260992\n" \
"s_lshr_b32 s55, s54, 8\n" \
"s_and_b32 s55, s55, 0x2000\n" \
"s_xor_b32 s54, s54, s55\n" \
"buffer_load_dwordx4 v0, s[40:43], s54 offen sc1 lds\n" \
"s_add_u32 m0, s52, 30720\n" \
"s_add_u32 s54, s97, 2293760\n" \
"s_lshr_b32 s55, s54, 8\n" \
"s_and_b32 s55, s55, 0x2000\n" \
"s_xor_b32 s54, s54, s55\n" \
"buffer_load_dwordx4 v0, s[40:43], s54 offen sc1 lds\n" \
"s_add_u32 m0, s52, 31744\n" \
"s_add_u32 s54, s97, 2326528\n" \
"s_lshr_b32 s55, s54, 8\n" \
"s_and_b32 s55, s55, 0x2000\n" \
"s_xor_b32 s54, s54, s55\n" \
"buffer_load_dwordx4 v0, s[40:43], s54 offen sc1 lds\n" \
"s_mov_b32 s54, s99\n" \
".Lsg2_z17_%=:\n" \
"s_sleep 1\n" \
"s_sub_u32 s54, s54, 1\n" \
"s_cbranch_scc0 .Lsg2_z17_%=\n" \
"s_waitcnt vmcnt(36)\n" \
"ds_read_b64_tr_b16 a[32:33], v9 offset:32768\n" \
"ds_read_b64_tr_b16 a[34:35], v9 offset:33280\n" \
"ds_read_b64_tr_b16 a[160:161], v9 offset:32800\n" \
"ds_read_b64_tr_b16 a[162:163], v9 offset:33312\n" \
"ds_read_b64_tr_b16 v[160:161], v9 offset:32832\n" \
"ds_read_b64_tr_b16 v[162:163], v9 offset:33344\n" \
"ds_read_b64_tr_b16 v[48:49], v9 offset:32864\n" \
"ds_read_b64_tr_b16 v[50:51], v9 offset:33376\n" \
"s_waitcnt lgkmcnt(0)\n" \
"s_add_u32 m0, s53, 32768\n" \
"s_add_u32 s54, s97, 2359296\n" \
"s_lshr_b32 s55, s54, 8\n" \
"s_and_b32 s55, s55, 0x2000\n" \
"s_xor_b32 s54, s54, s55\n" \
"buffer_load_dwordx4 v0, s[40:43], s54 offen sc1 lds\n" \
"s_add_u32 m0, s53, 33792\n" \
"s_add_u32 s54, s97, 2392064\n" \
"s_lshr_b32 s55, s54, 8\n" \
"s_and_b32 s55, s55, 0x2000\n" \
"s_xor_b32 s54, s54, s55\n" \
"buffer_load_dwordx4 v0, s[40:43], s54 offen sc1 lds\n" \
"s_add_u32 m0, s53, 34816\n" \
"s_add_u32 s54, s97, 2424832\n" \
"s_lshr_b32 s55, s54, 8\n" \
"s_and_b32 s55, s55, 0x2000\n" \
"s_xor_b32 s54, s54, s55\n" \
"buffer_load_dwordx4 v0, s[40:43], s54 offen sc1 lds\n" \
"s_add_u32 m0, s53, 35840\n" \
"s_add_u32 s54, s97, 2457600\n" \
"s_lshr_b32 s55, s54, 8\n" \
"s_and_b32 s55, s55, 0x2000\n" \
"s_xor_b32 s54, s54, s55\n" \
"buffer_load_dwordx4 v0, s[40:43], s54 offen sc1 lds\n" \
"s_mov_b32 s54, s99\n" \
".Lsg2_z18_%=:\n" \
"s_sleep 1\n" \
"s_sub_u32 s54, s54, 1\n" \
"s_cbranch_scc0 .Lsg2_z18_%=\n" \
"s_waitcnt vmcnt(36)\n" \
"ds_read_b64_tr_b16 a[36:37], v9 offset:36864\n" \
"ds_read_b64_tr_b16 a[38:39], v9 offset:37376\n" \
"ds_read_b64_tr_b16 a[164:165], v9 offset:36896\n" \
"ds_read_b64_tr_b16 a[166:167], v9 offset:37408\n" \
"ds_read_b64_tr_b16 v[164:165], v9 offset:36928\n" \
"ds_read_b64_tr_b16 v[166:167], v9 offset:37440\n" \
"ds_read_b64_tr_b16 v[52:53], v9 offset:36960\n" \
"ds_read_b64_tr_b16 v[54:55], v9 offset:37472\n" \
"s_waitcnt lgkmcnt(0)\n" \
"s_add_u32 m0, s53, 36864\n" \
"s_add_u32 s54, s97, 2490368\n" \
"s_lshr_b32 s55, s54, 8\n" \
"s_and_b32 s55, s55, 0x2000\n" \
"s_xor_b32 s54, s54, s55\n" \
"buffer_load_dwordx4 v0, s[40:43], s54 offen sc1 lds\n" \
"s_add_u32 m0, s53, 37888\n" \
"s_add_u32 s54, s97, 2523136\n" \
"s_lshr_b32 s55, s54, 8\n" \
"s_and_b32 s55, s55, 0x2000\n" \
"s_xor_b32 s54, s54, s55\n" \
"buffer_load_dwordx4 v0, s[40:43], s54 offen sc1 lds\n" \
"s_add_u32 m0, s53, 38912\n" \
"s_add_u32 s54, s97, 2555904\n" \
"s_lshr_b32 s55, s54, 8\n" \
"s_and_b32 s55, s55, 0x2000\n" \
"s_xor_b32 s54, s54, s55\n" \
"buffer_load_dwordx4 v0, s[40:43], s54 offen sc1 lds\n" \
"s_add_u32 m0, s53, 39936\n" \
"s_add_u32 s54, s97, 2588672\n" \
"s_lshr_b32 s55, s54, 8\n" \
"s_and_b32 s55, s55, 0x2000\n" \
"s_xor_b32 s54, s54, s55\n" \
"buffer_load_dwordx4 v0, s[40:43], s54 offen sc1 lds\n" \
"s_mov_b32 s54, s99\n" \
".Lsg2_z19_%=:\n" \
"s_sleep 1\n" \
"s_sub_u32 s54, s54, 1\n" \
"s_cbranch_scc0 .Lsg2_z19_%=\n" \
"s_waitcnt vmcnt(36)\n" \
"ds_read_b64_tr_b16 a[40:41], v1 offset:0\n" \
"ds_read_b64_tr_b16 a[42:43], v1 offset:512\n" \
"ds_read_b64_tr_b16 a[168:169], v1 offset:32\n" \
"ds_read_b64_tr_b16 a[170:171], v1 offset:544\n" \
"ds_read_b64_tr_b16 v[168:169], v1 offset:64\n" \
"ds_read_b64_tr_b16 v[170:171], v1 offset:576\n" \
"ds_read_b64_tr_b16 v[56:57], v1 offset:96\n" \
"ds_read_b64_tr_b16 v[58:59], v1 offset:608\n" \
"s_waitcnt lgkmcnt(0)\n" \
"s_add_u32 m0, s52, 0\n" \
"s_add_u32 s54, s97, 2621440\n" \
"s_lshr_b32 s55, s54, 8\n" \
"s_and_b32 s55, s55, 0x2000\n" \
"s_xor_b32 s54, s54, s55\n" \
"buffer_load_dwordx4 v0, s[40:43], s54 offen sc1 lds\n" \
"s_add_u32 m0, s52, 1024\n" \
"s_add_u32 s54, s97, 2654208\n" \
"s_lshr_b32 s55, s54, 8\n" \
"s_and_b32 s55, s55, 0x2000\n" \
"s_xor_b32 s54, s54, s55\n" \
"buffer_load_dwordx4 v0, s[40:43], s54 offen sc1 lds\n" \
"s_add_u32 m0, s52, 2048\n" \
"s_add_u32 s54, s97, 2686976\n" \
"s_lshr_b32 s55, s54, 8\n" \
"s_and_b32 s55, s55, 0x2000\n" \
"s_xor_b32 s54, s54, s55\n" \
"buffer_load_dwordx4 v0, s[40:43], s54 offen sc1 lds\n" \
"s_add_u32 m0, s52, 3072\n" \
"s_add_u32 s54, s97, 2719744\n" \
"s_lshr_b32 s55, s54, 8\n" \
"s_and_b32 s55, s55, 0x2000\n" \
"s_xor_b32 s54, s54, s55\n" \
"buffer_load_dwordx4 v0, s[40:43], s54 offen sc1 lds\n" \
"s_mov_b32 s54, s99\n" \
".Lsg2_z20_%=:\n" \
"s_sleep 1\n" \
"s_sub_u32 s54, s54, 1\n" \
"s_cbranch_scc0 .Lsg2_z20_%=\n" \
"s_waitcnt vmcnt(36)\n" \
"ds_read_b64_tr_b16 a[44:45], v1 offset:4096\n" \
"ds_read_b64_tr_b16 a[46:47], v1 offset:4608\n" \
"ds_read_b64_tr_b16 a[172:173], v1 offset:4128\n" \
"ds_read_b64_tr_b16 a[174:175], v1 offset:4640\n" \
"ds_read_b64_tr_b16 v[172:173], v1 offset:4160\n" \
"ds_read_b64_tr_b16 v[174:175], v1 offset:4672\n" \
"ds_read_b64_tr_b16 v[60:61], v1 offset:4192\n" \
"ds_read_b64_tr_b16 v[62:63], v1 offset:4704\n" \
"s_waitcnt lgkmcnt(0)\n" \
"s_add_u32 m0, s52, 4096\n" \
"s_add_u32 s54, s97, 2752512\n" \
"s_lshr_b32 s55, s54, 8\n" \
"s_and_b32 s55, s55, 0x2000\n" \
"s_xor_b32 s54, s54, s55\n" \
"buffer_load_dwordx4 v0, s[40:43], s54 offen sc1 lds\n" \
"s_add_u32 m0, s52, 5120\n" \
"s_add_u32 s54, s97, 2785280\n" \
"s_lshr_b32 s55, s54, 8\n" \
"s_and_b32 s55, s55, 0x2000\n" \
"s_xor_b32 s54, s54, s55\n" \
"buffer_load_dwordx4 v0, s[40:43], s54 offen sc1 lds\n" \
"s_add_u32 m0, s52, 6144\n" \
"s_add_u32 s54, s97, 2818048\n" \
"s_lshr_b32 s55, s54, 8\n" \
"s_and_b32 s55, s55, 0x2000\n" \
"s_xor_b32 s54, s54, s55\n" \
"buffer_load_dwordx4 v0, s[40:43], s54 offen sc1 lds\n" \
"s_add_u32 m0, s52, 7168\n" \
"s_add_u32 s54, s97, 2850816\n" \
"s_lshr_b32 s55, s54, 8\n" \
"s_and_b32 s55, s55, 0x2000\n" \
"s_xor_b32 s54, s54, s55\n" \
"buffer_load_dwordx4 v0, s[40:43], s54 offen sc1 lds\n" \
"s_mov_b32 s54, s99\n" \
".Lsg2_z21_%=:\n" \
"s_sleep 1\n" \
"s_sub_u32 s54, s54, 1\n" \
"s_cbranch_scc0 .Lsg2_z21_%=\n" \
"s_waitcnt vmcnt(36)\n" \
"ds_read_b64_tr_b16 a[48:49], v1 offset:8192\n" \
"ds_read_b64_tr_b16 a[50:51], v1 offset:8704\n" \
"ds_read_b64_tr_b16 a[176:177], v1 offset:8224\n" \
"ds_read_b64_tr_b16 a[178:179], v1 offset:8736\n" \
"ds_read_b64_tr_b16 v[176:177], v1 offset:8256\n" \
"ds_read_b64_tr_b16 v[178:179], v1 offset:8768\n" \
"ds_read_b64_tr_b16 v[64:65], v1 offset:8288\n" \
"ds_read_b64_tr_b16 v[66:67], v1 offset:8800\n" \
"s_waitcnt lgkmcnt(0)\n" \
"s_add_u32 m0, s52, 8192\n" \
"s_add_u32 s54, s97, 2883584\n" \
"s_lshr_b32 s55, s54, 8\n" \
"s_and_b32 s55, s55, 0x2000\n" \
"s_xor_b32 s54, s54, s55\n" \
"buffer_load_dwordx4 v0, s[40:43], s54 offen sc1 lds\n" \
"s_add_u32 m0, s52, 9216\n" \
"s_add_u32 s54, s97, 2916352\n" \
"s_lshr_b32 s55, s54, 8\n" \
"s_and_b32 s55, s55, 0x2000\n" \
"s_xor_b32 s54, s54, s55\n" \
"buffer_load_dwordx4 v0, s[40:43], s54 offen sc1 lds\n" \
"s_add_u32 m0, s52, 10240\n" \
"s_add_u32 s54, s97, 2949120\n" \
"s_lshr_b32 s55, s54, 8\n" \
"s_and_b32 s55, s55, 0x2000\n" \
"s_xor_b32 s54, s54, s55\n" \
"buffer_load_dwordx4 v0, s[40:43], s54 offen sc1 lds\n" \
"s_add_u32 m0, s52, 11264\n" \
"s_add_u32 s54, s97, 2981888\n" \
"s_lshr_b32 s55, s54, 8\n" \
"s_and_b32 s55, s55, 0x2000\n" \
"s_xor_b32 s54, s54, s55\n" \
"buffer_load_dwordx4 v0, s[40:43], s54 offen sc1 lds\n" \
"s_mov_b32 s54, s99\n" \
".Lsg2_z22_%=:\n" \
"s_sleep 1\n" \
"s_sub_u32 s54, s54, 1\n" \
"s_cbranch_scc0 .Lsg2_z22_%=\n" \
"s_waitcnt vmcnt(36)\n" \
"ds_read_b64_tr_b16 a[52:53], v1 offset:12288\n" \
"ds_read_b64_tr_b16 a[54:55], v1 offset:12800\n" \
"ds_read_b64_tr_b16 a[180:181], v1 offset:12320\n" \
"ds_read_b64_tr_b16 a[182:183], v1 offset:12832\n" \
"ds_read_b64_tr_b16 v[180:181], v1 offset:12352\n" \
"ds_read_b64_tr_b16 v[182:183], v1 offset:12864\n" \
"ds_read_b64_tr_b16 v[68:69], v1 offset:12384\n" \
"ds_read_b64_tr_b16 v[70:71], v1 offset:12896\n" \
"s_waitcnt lgkmcnt(0)\n" \
"s_add_u32 m0, s52, 12288\n" \
"s_add_u32 s54, s97, 3014656\n" \
"s_lshr_b32 s55, s54, 8\n" \
"s_and_b32 s55, s55, 0x2000\n" \
"s_xor_b32 s54, s54, s55\n" \
"buffer_load_dwordx4 v0, s[40:43], s54 offen sc1 lds\n" \
"s_add_u32 m0, s52, 13312\n" \
"s_add_u32 s54, s97, 3047424\n" \
"s_lshr_b32 s55, s54, 8\n" \
"s_and_b32 s55, s55, 0x2000\n" \
"s_xor_b32 s54, s54, s55\n" \
"buffer_load_dwordx4 v0, s[40:43], s54 offen sc1 lds\n" \
"s_add_u32 m0, s52, 14336\n" \
"s_add_u32 s54, s97, 3080192\n" \
"s_lshr_b32 s55, s54, 8\n" \
"s_and_b32 s55, s55, 0x2000\n" \
"s_xor_b32 s54, s54, s55\n" \
"buffer_load_dwordx4 v0, s[40:43], s54 offen sc1 lds\n" \
"s_add_u32 m0, s52, 15360\n" \
"s_add_u32 s54, s97, 3112960\n" \
"s_lshr_b32 s55, s54, 8\n" \
"s_and_b32 s55, s55, 0x2000\n" \
"s_xor_b32 s54, s54, s55\n" \
"buffer_load_dwordx4 v0, s[40:43], s54 offen sc1 lds\n" \
"s_mov_b32 s54, s99\n" \
".Lsg2_z23_%=:\n" \
"s_sleep 1\n" \
"s_sub_u32 s54, s54, 1\n" \
"s_cbranch_scc0 .Lsg2_z23_%=\n" \
"s_waitcnt vmcnt(36)\n" \
"ds_read_b64_tr_b16 a[56:57], v1 offset:16384\n" \
"ds_read_b64_tr_b16 a[58:59], v1 offset:16896\n" \
"ds_read_b64_tr_b16 a[184:185], v1 offset:16416\n" \
"ds_read_b64_tr_b16 a[186:187], v1 offset:16928\n" \
"ds_read_b64_tr_b16 v[184:185], v1 offset:16448\n" \
"ds_read_b64_tr_b16 v[186:187], v1 offset:16960\n" \
"ds_read_b64_tr_b16 v[72:73], v1 offset:16480\n" \
"ds_read_b64_tr_b16 v[74:75], v1 offset:16992\n" \
"s_waitcnt lgkmcnt(0)\n" \
"s_add_u32 m0, s52, 16384\n" \
"s_add_u32 s54, s97, 3145728\n" \
"s_lshr_b32 s55, s54, 8\n" \
"s_and_b32 s55, s55, 0x2000\n" \
"s_xor_b32 s54, s54, s55\n" \
"buffer_load_dwordx4 v0, s[40:43], s54 offen sc1 lds\n" \
"s_add_u32 m0, s52, 17408\n" \
"s_add_u32 s54, s97, 3178496\n" \
"s_lshr_b32 s55, s54, 8\n" \
"s_and_b32 s55, s55, 0x2000\n" \
"s_xor_b32 s54, s54, s55\n" \
"buffer_load_dwordx4 v0, s[40:43], s54 offen sc1 lds\n" \
"s_add_u32 m0, s52, 18432\n" \
"s_add_u32 s54, s97, 3211264\n" \
"s_lshr_b32 s55, s54, 8\n" \
"s_and_b32 s55, s55, 0x2000\n" \
"s_xor_b32 s54, s54, s55\n" \
"buffer_load_dwordx4 v0, s[40:43], s54 offen sc1 lds\n" \
"s_add_u32 m0, s52, 19456\n" \
"s_add_u32 s54, s97, 3244032\n" \
"s_lshr_b32 s55, s54, 8\n" \
"s_and_b32 s55, s55, 0x2000\n" \
"s_xor_b32 s54, s54, s55\n" \
"buffer_load_dwordx4 v0, s[40:43], s54 offen sc1 lds\n" \
"s_mov_b32 s54, s99\n" \
".Lsg2_z24_%=:\n" \
"s_sleep 1\n" \
"s_sub_u32 s54, s54, 1\n" \
"s_cbranch_scc0 .Lsg2_z24_%=\n" \
"s_waitcnt vmcnt(36)\n" \
"ds_read_b64_tr_b16 a[60:61], v1 offset:20480\n" \
"ds_read_b64_tr_b16 a[62:63], v1 offset:20992\n" \
"ds_read_b64_tr_b16 a[188:189], v1 offset:20512\n" \
"ds_read_b64_tr_b16 a[190:191], v1 offset:21024\n" \
"ds_read_b64_tr_b16 v[188:189], v1 offset:20544\n" \
"ds_read_b64_tr_b16 v[190:191], v1 offset:21056\n" \
"ds_read_b64_tr_b16 v[76:77], v1 offset:20576\n" \
"ds_read_b64_tr_b16 v[78:79], v1 offset:21088\n" \
"s_waitcnt lgkmcnt(0)\n" \
"s_add_u32 m0, s52, 20480\n" \
"s_add_u32 s54, s97, 3276800\n" \
"s_lshr_b32 s55, s54, 8\n" \
"s_and_b32 s55, s55, 0x2000\n" \
"s_xor_b32 s54, s54, s55\n" \
"buffer_load_dwordx4 v0, s[40:43], s54 offen sc1 lds\n" \
"s_add_u32 m0, s52, 21504\n" \
"s_add_u32 s54, s97, 3309568\n" \
"s_lshr_b32 s55, s54, 8\n" \
"s_and_b32 s55, s55, 0x2000\n" \
"s_xor_b32 s54, s54, s55\n" \
"buffer_load_dwordx4 v0, s[40:43], s54 offen sc1 lds\n" \
"s_add_u32 m0, s52, 22528\n" \
"s_add_u32 s54, s97, 3342336\n" \
"s_lshr_b32 s55, s54, 8\n" \
"s_and_b32 s55, s55, 0x2000\n" \
"s_xor_b32 s54, s54, s55\n" \
"buffer_load_dwordx4 v0, s[40:43], s54 offen sc1 lds\n" \
"s_add_u32 m0, s52, 23552\n" \
"s_add_u32 s54, s97, 3375104\n" \
"s_lshr_b32 s55, s54, 8\n" \
"s_and_b32 s55, s55, 0x2000\n" \
"s_xor_b32 s54, s54, s55\n" \
"buffer_load_dwordx4 v0, s[40:43], s54 offen sc1 lds\n" \
"s_mov_b32 s54, s99\n" \
".Lsg2_z25_%=:\n" \
"s_sleep 1\n" \
"s_sub_u32 s54, s54, 1\n" \
"s_cbranch_scc0 .Lsg2_z25_%=\n" \
"s_waitcnt vmcnt(36)\n" \
"ds_read_b64_tr_b16 a[64:65], v1 offset:24576\n" \
"ds_read_b64_tr_b16 a[66:67], v1 offset:25088\n" \
"ds_read_b64_tr_b16 a[192:193], v1 offset:24608\n" \
"ds_read_b64_tr_b16 a[194:195], v1 offset:25120\n" \
"ds_read_b64_tr_b16 v[192:193], v1 offset:24640\n" \
"ds_read_b64_tr_b16 v[194:195], v1 offset:25152\n" \
"ds_read_b64_tr_b16 v[80:81], v1 offset:24672\n" \
"ds_read_b64_tr_b16 v[82:83], v1 offset:25184\n" \
"s_waitcnt lgkmcnt(0)\n" \
"s_add_u32 m0, s52, 24576\n" \
"s_add_u32 s54, s97, 3407872\n" \
"s_lshr_b32 s55, s54, 8\n" \
"s_and_b32 s55, s55, 0x2000\n" \
"s_xor_b32 s54, s54, s55\n" \
"buffer_load_dwordx4 v0, s[40:43], s54 offen sc1 lds\n" \
"s_add_u32 m0, s52, 25600\n" \
"s_add_u32 s54, s97, 3440640\n" \
"s_lshr_b32 s55, s54, 8\n" \
"s_and_b32 s55, s55, 0x2000\n" \
"s_xor_b32 s54, s54, s55\n" \
"buffer_load_dwordx4 v0, s[40:43], s54 offen sc1 lds\n" \
"s_add_u32 m0, s52, 26624\n" \
"s_add_u32 s54, s97, 3473408\n" \
"s_lshr_b32 s55, s54, 8\n" \
"s_and_b32 s55, s55, 0x2000\n" \
"s_xor_b32 s54, s54, s55\n" \
"buffer_load_dwordx4 v0, s[40:43], s54 offen sc1 lds\n" \
"s_add_u32 m0, s52, 27648\n" \
"s_add_u32 s54, s97, 3506176\n" \
"s_lshr_b32 s55, s54, 8\n" \
"s_and_b32 s55, s55, 0x2000\n" \
"s_xor_b32 s54, s54, s55\n" \
"buffer_load_dwordx4 v0, s[40:43], s54 offen sc1 lds\n" \
"s_mov_b32 s54, s99\n" \
".Lsg2_z26_%=:\n" \
"s_sleep 1\n" \
"s_sub_u32 s54, s54, 1\n" \
"s_cbranch_scc0 .Lsg2_z26_%=\n" \
"s_waitcnt vmcnt(36)\n" \
"ds_read_b64_tr_b16 a[68:69], v1 offset:28672\n" \
"ds_read_b64_tr_b16 a[70:71], v1 offset:29184\n" \
"ds_read_b64_tr_b16 a[196:197], v1 offset:28704\n" \
"ds_read_b64_tr_b16 a[198:199], v1 offset:29216\n" \
"ds_read_b64_tr_b16 v[196:197], v1 offset:28736\n" \
"ds_read_b64_tr_b16 v[198:199], v1 offset:29248\n" \
"ds_read_b64_tr_b16 v[84:85], v1 offset:28768\n" \
"ds_read_b64_tr_b16 v[86:87], v1 offset:29280\n" \
"s_waitcnt lgkmcnt(0)\n" \
"s_add_u32 m0, s52, 28672\n" \
"s_add_u32 s54, s97, 3538944\n" \
"s_lshr_b32 s55, s54, 8\n" \
"s_and_b32 s55, s55, 0x2000\n" \
"s_xor_b32 s54, s54, s55\n" \
"buffer_load_dwordx4 v0, s[40:43], s54 offen sc1 lds\n" \
"s_add_u32 m0, s52, 29696\n" \
"s_add_u32 s54, s97, 3571712\n" \
"s_lshr_b32 s55, s54, 8\n" \
"s_and_b32 s55, s55, 0x2000\n" \
"s_xor_b32 s54, s54, s55\n" \
"buffer_load_dwordx4 v0, s[40:43], s54 offen sc1 lds\n" \
"s_add_u32 m0, s52, 30720\n" \
"s_add_u32 s54, s97, 3604480\n" \
"s_lshr_b32 s55, s54, 8\n" \
"s_and_b32 s55, s55, 0x2000\n" \
"s_xor_b32 s54, s54, s55\n" \
"buffer_load_dwordx4 v0, s[40:43], s54 offen sc1 lds\n" \
"s_add_u32 m0, s52, 31744\n" \
"s_add_u32 s54, s97, 3637248\n" \
"s_lshr_b32 s55, s54, 8\n" \
"s_and_b32 s55, s55, 0x2000\n" \
"s_xor_b32 s54, s54, s55\n" \
"buffer_load_dwordx4 v0, s[40:43], s54 offen sc1 lds\n" \
"s_mov_b32 s54, s99\n" \
".Lsg2_z27_%=:\n" \
"s_sleep 1\n" \
"s_sub_u32 s54, s54, 1\n" \
"s_cbranch_scc0 .Lsg2_z27_%=\n" \
"s_waitcnt vmcnt(36)\n" \
"ds_read_b64_tr_b16 a[72:73], v9 offset:32768\n" \
"ds_read_b64_tr_b16 a[74:75], v9 offset:33280\n" \
"ds_read_b64_tr_b16 a[200:201], v9 offset:32800\n" \
"ds_read_b64_tr_b16 a[202:203], v9 offset:33312\n" \
"ds_read_b64_tr_b16 v[200:201], v9 offset:32832\n" \
"ds_read_b64_tr_b16 v[202:203], v9 offset:33344\n" \
"ds_read_b64_tr_b16 v[88:89], v9 offset:32864\n" \
"ds_read_b64_tr_b16 v[90:91], v9 offset:33376\n" \
"s_waitcnt lgkmcnt(0)\n" \
"s_waitcnt vmcnt(32)\n" \
"ds_read_b64_tr_b16 a[76:77], v9 offset:36864\n" \
"ds_read_b64_tr_b16 a[78:79], v9 offset:37376\n" \
"ds_read_b64_tr_b16 a[204:205], v9 offset:36896\n" \
"ds_read_b64_tr_b16 a[206:207], v9 offset:37408\n" \
"ds_read_b64_tr_b16 v[204:205], v9 offset:36928\n" \
"ds_read_b64_tr_b16 v[206:207], v9 offset:37440\n" \
"ds_read_b64_tr_b16 v[92:93], v9 offset:36960\n" \
"ds_read_b64_tr_b16 v[94:95], v9 offset:37472\n" \
"s_waitcnt lgkmcnt(0)\n" \
"s_waitcnt vmcnt(28)\n" \
"ds_read_b64_tr_b16 a[80:81], v1 offset:0\n" \
"ds_read_b64_tr_b16 a[82:83], v1 offset:512\n" \
"ds_read_b64_tr_b16 a[208:209], v1 offset:32\n" \
"ds_read_b64_tr_b16 a[210:211], v1 offset:544\n" \
"ds_read_b64_tr_b16 v[208:209], v1 offset:64\n" \
"ds_read_b64_tr_b16 v[210:211], v1 offset:576\n" \
"ds_read_b64_tr_b16 v[96:97], v1 offset:96\n" \
"ds_read_b64_tr_b16 v[98:99], v1 offset:608\n" \
"s_waitcnt lgkmcnt(0)\n" \
"ds_write_b128 v2, v[16:19] offset:0\n" \
"ds_write_b128 v2, v[20:23] offset:1024\n" \
"ds_write_b128 v2, v[24:27] offset:2048\n" \
"ds_write_b128 v2, v[28:31] offset:3072\n" \
"s_waitcnt vmcnt(24)\n" \
"ds_read_b64_tr_b16 a[84:85], v1 offset:4096\n" \
"ds_read_b64_tr_b16 a[86:87], v1 offset:4608\n" \
"ds_read_b64_tr_b16 a[212:213], v1 offset:4128\n" \
"ds_read_b64_tr_b16 a[214:215], v1 offset:4640\n" \
"ds_read_b64_tr_b16 v[212:213], v1 offset:4160\n" \
"ds_read_b64_tr_b16 v[214:215], v1 offset:4672\n" \
"ds_read_b64_tr_b16 v[100:101], v1 offset:4192\n" \
"ds_read_b64_tr_b16 v[102:103], v1 offset:4704\n" \
"s_waitcnt lgkmcnt(0)\n" \
"ds_write_b128 v2, v[32:35] offset:4096\n" \
"ds_write_b128 v2, v[36:39] offset:5120\n" \
"ds_write_b128 v2, v[40:43] offset:6144\n" \
"ds_write_b128 v2, v[44:47] offset:7168\n" \
"s_waitcnt vmcnt(20)\n" \
"ds_read_b64_tr_b16 a[88:89], v1 offset:8192\n" \
"ds_read_b64_tr_b16 a[90:91], v1 offset:8704\n" \
"ds_read_b64_tr_b16 a[216:217], v1 offset:8224\n" \
"ds_read_b64_tr_b16 a[218:219], v1 offset:8736\n" \
"ds_read_b64_tr_b16 v[216:217], v1 offset:8256\n" \
"ds_read_b64_tr_b16 v[218:219], v1 offset:8768\n" \
"ds_read_b64_tr_b16 v[104:105], v1 offset:8288\n" \
"ds_read_b64_tr_b16 v[106:107], v1 offset:8800\n" \
"s_waitcnt lgkmcnt(0)\n" \
"ds_write_b128 v2, v[48:51] offset:8192\n" \
"ds_write_b128 v2, v[52:55] offset:9216\n" \
"ds_write_b128 v2, v[56:59] offset:10240\n" \
"ds_write_b128 v2, v[60:63] offset:11264\n" \
"s_waitcnt vmcnt(16)\n" \
"ds_read_b64_tr_b16 a[92:93], v1 offset:12288\n" \
"ds_read_b64_tr_b16 a[94:95], v1 offset:12800\n" \
"ds_read_b64_tr_b16 a[220:221], v1 offset:12320\n" \
"ds_read_b64_tr_b16 a[222:223], v1 offset:12832\n" \
"ds_read_b64_tr_b16 v[220:221], v1 offset:12352\n" \
"ds_read_b64_tr_b16 v[222:223], v1 offset:12864\n" \
"ds_read_b64_tr_b16 v[108:109], v1 offset:12384\n" \
"ds_read_b64_tr_b16 v[110:111], v1 offset:12896\n" \
"s_waitcnt lgkmcnt(0)\n" \
"ds_write_b128 v2, v[64:67] offset:12288\n" \
"ds_write_b128 v2, v[68:71] offset:13312\n" \
"ds_write_b128 v2, v[72:75] offset:14336\n" \
"ds_write_b128 v2, v[76:79] offset:15360\n" \
"s_waitcnt vmcnt(12)\n" \
"ds_read_b64_tr_b16 a[96:97], v1 offset:16384\n" \
"ds_read_b64_tr_b16 a[98:99], v1 offset:16896\n" \
"ds_read_b64_tr_b16 a[224:225], v1 offset:16416\n" \
"ds_read_b64_tr_b16 a[226:227], v1 offset:16928\n" \
"ds_read_b64_tr_b16 v[224:225], v1 offset:16448\n" \
"ds_read_b64_tr_b16 v[226:227], v1 offset:16960\n" \
"ds_read_b64_tr_b16 v[16:17], v1 offset:16480\n" \
"ds_read_b64_tr_b16 v[18:19], v1 offset:16992\n" \
"s_waitcnt lgkmcnt(0)\n" \
"ds_write_b128 v2, v[80:83] offset:16384\n" \
"ds_write_b128 v2, v[84:87] offset:17408\n" \
"ds_write_b128 v2, v[88:91] offset:18432\n" \
"ds_write_b128 v2, v[92:95] offset:19456\n" \
"s_waitcnt vmcnt(8)\n" \
"ds_read_b64_tr_b16 a[100:101], v1 offset:20480\n" \
"ds_read_b64_tr_b16 a[102:103], v1 offset:20992\n" \
"ds_read_b64_tr_b16 a[228:229], v1 offset:20512\n" \
"ds_read_b64_tr_b16 a[230:231], v1 offset:21024\n" \
"ds_read_b64_tr_b16 v[228:229], v1 offset:20544\n" \
"ds_read_b64_tr_b16 v[230:231], v1 offset:21056\n" \
"ds_read_b64_tr_b16 v[20:21], v1 offset:20576\n" \
"ds_read_b64_tr_b16 v[22:23], v1 offset:21088\n" \
"s_waitcnt lgkmcnt(0)\n" \
"ds_write_b128 v2, v[96:99] offset:20480\n" \
"ds_write_b128 v2, v[100:103] offset:21504\n" \
"ds_write_b128 v2, v[104:107] offset:22528\n" \
"ds_write_b128 v2, v[108:111] offset:23552\n" \
"s_waitcnt vmcnt(4)\n" \
"ds_read_b64_tr_b16 a[104:105], v1 offset:24576\n" \
"ds_read_b64_tr_b16 a[106:107], v1 offset:25088\n" \
"ds_read_b64_tr_b16 a[232:233], v1 offset:24608\n" \
"ds_read_b64_tr_b16 a[234:235], v1 offset:25120\n" \
"ds_read_b64_tr_b16 v[232:233], v1 offset:24640\n" \
"ds_read_b64_tr_b16 v[234:235], v1 offset:25152\n" \
"ds_read_b64_tr_b16 v[24:25], v1 offset:24672\n" \
"ds_read_b64_tr_b16 v[26:27], v1 offset:25184\n" \
"s_waitcnt lgkmcnt(0)\n" \
"ds_write_b128 v2, v[16:19] offset:24576\n" \
"ds_write_b128 v2, v[20:23] offset:25600\n" \
"ds_write_b128 v2, v[24:27] offset:26624\n" \
"s_waitcnt vmcnt(0)\n" \
"ds_read_b64_tr_b16 a[108:109], v1 offset:28672\n" \
"ds_read_b64_tr_b16 a[110:111], v1 offset:29184\n" \
"ds_read_b64_tr_b16 a[236:237], v1 offset:28704\n" \
"ds_read_b64_tr_b16 a[238:239], v1 offset:29216\n" \
"ds_read_b64_tr_b16 v[236:237], v1 offset:28736\n" \
"ds_read_b64_tr_b16 v[238:239], v1 offset:29248\n" \
"ds_read_b64_tr_b16 v[28:29], v1 offset:28768\n" \
"ds_read_b64_tr_b16 v[30:31], v1 offset:29280\n" \
"s_waitcnt lgkmcnt(0)\n" \
"ds_write_b128 v2, v[28:31] offset:28672\n" \
"s_waitcnt vmcnt(0) lgkmcnt(0)\n" \
"s_barrier\n" \
"s_mul_i32 s54, s48, 7\n" \
"s_add_u32 s54, s54, 0\n" \
"s_lshr_b32 s55, s54, 1\n" \
"s_lshl_b32 s55, s55, 14\n" \
"s_and_b32 s54, s54, 1\n" \
"s_lshl_b32 s54, s54, 12\n" \
"s_add_u32 s54, s54, s55\n" \
"s_add_u32 s86, s54, s85\n" \
"s_mul_i32 s54, s48, 7\n" \
"s_add_u32 s54, s54, 1\n" \
"s_lshr_b32 s55, s54, 1\n" \
"s_lshl_b32 s55, s55, 14\n" \
"s_and_b32 s54, s54, 1\n" \
"s_lshl_b32 s54, s54, 12\n" \
"s_add_u32 s54, s54, s55\n" \
"s_add_u32 s87, s54, s85\n" \
"s_mul_i32 s54, s48, 7\n" \
"s_add_u32 s54, s54, 2\n" \
"s_lshr_b32 s55, s54, 1\n" \
"s_lshl_b32 s55, s55, 14\n" \
"s_and_b32 s54, s54, 1\n" \
"s_lshl_b32 s54, s54, 12\n" \
"s_add_u32 s54, s54, s55\n" \
"s_add_u32 s88, s54, s85\n" \
"s_mul_i32 s54, s48, 7\n" \
"s_add_u32 s54, s54, 3\n" \
"s_lshr_b32 s55, s54, 1\n" \
"s_lshl_b32 s55, s55, 14\n" \
"s_and_b32 s54, s54, 1\n" \
"s_lshl_b32 s54, s54, 12\n" \
"s_add_u32 s54, s54, s55\n" \
"s_add_u32 s89, s54, s85\n" \
"s_mul_i32 s54, s48, 7\n" \
"s_add_u32 s54, s54, 4\n" \
"s_lshr_b32 s55, s54, 1\n" \
"s_lshl_b32 s55, s55, 14\n" \
"s_and_b32 s54, s54, 1\n" \
"s_lshl_b32 s54, s54, 12\n" \
"s_add_u32 s54, s54, s55\n" \
"s_add_u32 s90, s54, s85\n" \
"s_mul_i32 s54, s48, 7\n" \
"s_add_u32 s54, s54, 5\n" \
"s_lshr_b32 s55, s54, 1\n" \
"s_lshl_b32 s55, s55, 14\n" \
"s_and_b32 s54, s54, 1\n" \
"s_lshl_b32 s54, s54, 12\n" \
"s_add_u32 s54, s54, s55\n" \
"s_add_u32 s91, s54, s85\n" \
"s_mul_i32 s54, s48, 7\n" \
"s_add_u32 s54, s54, 6\n" \
"s_lshr_b32 s55, s54, 1\n" \
"s_lshl_b32 s55, s55, 14\n" \
"s_and_b32 s54, s54, 1\n" \
"s_lshl_b32 s54, s54, 12\n" \
"s_add_u32 s54, s54, s55\n" \
"s_add_u32 s92, s54, s85\n" \
"s_mov_b32 s55, 0\n" \
"s_mov_b32 s60, 0\n" \
"s_mov_b32 s79, s95\n" \
"s_mov_b32 s98, 0x70000000\n" \
".Lsg2_prime_%=:\n" \
"s_lshr_b32 s59, s44, 1\n" \
"s_add_u32 s59, s59, 1\n" \
"s_lshl_b32 s56, s44, 18\n" \
"s_lshr_b32 s58, s56, 8\n" \
"s_and_b32 s58, s58, 0x2000\n" \
"s_xor_b32 s56, s56, s58\n" \
"buffer_load_dword v11, v4, s[36:39], 0 offen sc1\n" \
"s_waitcnt vmcnt(0)\n" \
"v_cmp_gt_u32_e64 s[62:63], s59, v11\n" \
"s_nop 4\n" \
"s_cmp_lg_u64 s[62:63], 0\n" \
"s_cbranch_scc1 .Lsg2_w_%=\n" \
".Lsg2_wk_%=:\n" \
"s_mov_b32 s76, 0x7ffffff0\n" \
"s_mov_b32 s81, 1\n" \
"s_mov_b32 s84, 0\n" \
"s_waitcnt vmcnt(0) lgkmcnt(0)\n" \
"s_xor_b32 s57, s56, s86\n" \
"buffer_load_dwordx4 v[16:19], v3, s[36:39], s57 offen offset:0 sc1\n" \
"buffer_load_dwordx4 v[20:23], v3, s[36:39], s57 offen offset:1024 sc1\n" \
"buffer_load_dwordx4 v[24:27], v3, s[36:39], s57 offen offset:2048 sc1\n" \
"buffer_load_dwordx4 v[28:31], v3, s[36:39], s57 offen offset:3072 sc1\n" \
"s_xor_b32 s57, s56, s87\n" \
"buffer_load_dwordx4 v[32:35], v3, s[36:39], s57 offen offset:0 sc1\n" \
"buffer_load_dwordx4 v[36:39], v3, s[36:39], s57 offen offset:1024 sc1\n" \
"buffer_load_dwordx4 v[40:43], v3, s[36:39], s57 offen offset:2048 sc1\n" \
"buffer_load_dwordx4 v[44:47], v3, s[36:39], s57 offen offset:3072 sc1\n" \
"s_xor_b32 s57, s56, s88\n" \
"buffer_load_dwordx4 v[48:51], v3, s[36:39], s57 offen offset:0 sc1\n" \
"buffer_load_dwordx4 v[52:55], v3, s[36:39], s57 offen offset:1024 sc1\n" \
"buffer_load_dwordx4 v[56:59], v3, s[36:39], s57 offen offset:2048 sc1\n" \
"buffer_load_dwordx4 v[60:63], v3, s[36:39], s57 offen offset:3072 sc1\n" \
"s_xor_b32 s57, s56, s89\n" \
"buffer_load_dwordx4 v[64:67], v3, s[36:39], s57 offen offset:0 sc1\n" \
"buffer_load_dwordx4 v[68:71], v3, s[36:39], s57 offen offset:1024 sc1\n" \
"ds_read_b128 v[80:83], v2 offset:0\n" \
"ds_read_b128 v[84:87], v2 offset:1024\n" \
"ds_read_b128 v[88:91], v2 offset:2048\n" \
"s_waitcnt lgkmcnt(0)\n" \
"v_mov_b32 v9, 0x7ffffff0\n" \
"buffer_store_dword v9, v9, s[36:39], 0 offen\n" \
"buffer_store_dword v9, v9, s[36:39], 0 offen\n" \
"s_waitcnt vmcnt(15)\n" \
"s_nop 7\n" \
"s_nop 7\n" \
".p2align 6\n" \
".Lsg2_loop_%=:\n" \
"v_mfma_f32_16x16x32_bf16 v[96:99], a[0:3], v[16:19], 0\n" \
"ds_write_b128 v7, v[112:115] offset:0\n" \
"v_mfma_f32_16x16x32_bf16 v[100:103], a[128:131], v[16:19], 0\n" \
"ds_read_b128 v[92:95], v2 offset:3072\n" \
"v_xor_b32_e64 v7, 64, v7\n" \
"v_mfma_f32_16x16x32_bf16 v[104:107], v[128:131], v[16:19], 0\n" \
"s_waitcnt vmcnt(14) lgkmcnt(4)\n" \
"v_mfma_f32_16x16x32_bf16 v[108:111], v[80:83], v[16:19], 0\n" \
"buffer_load_dwordx4 v[16:19], v3, s[36:39], s57 offen offset:2048 sc1\n" \
"v_mfma_f32_16x16x32_bf16 v[96:99], a[4:7], v[20:23], v[96:99]\n" \
"ds_write_b128 v7, v[116:119] offset:1024\n" \
"v_mfma_f32_16x16x32_bf16 v[100:103], a[132:135], v[20:23], v[100:103]\n" \
"ds_read_b128 v[80:83], v2 offset:4096\n" \
"s_cmp_lt_i32 s44, s50\n" \
"v_xor_b32 v7, 192, v7\n" \
"v_mfma_f32_16x16x32_bf16 v[104:107], v[132:135], v[20:23], v[104:107]\n" \
"s_waitcnt vmcnt(14) lgkmcnt(5)\n" \
"s_cselect_b32 s82, s45, s47\n" \
"v_mfma_f32_16x16x32_bf16 v[108:111], v[84:87], v[20:23], v[108:111]\n" \
"buffer_load_dwordx4 v[20:23], v3, s[36:39], s57 offen offset:3072 sc1\n" \
"v_mfma_f32_16x16x32_bf16 v[96:99], a[8:11], v[24:27], v[96:99]\n" \
"ds_write_b128 v7, v[120:123] offset:2048\n" \
"v_mfma_f32_16x16x32_bf16 v[100:103], a[136:139], v[24:27], v[100:103]\n" \
"ds_read_b128 v[84:87], v2 offset:5120\n" \
"s_add_u32 s82, s82, s44\n" \
"s_cmp_lt_u32 s82, 128\n" \
"v_xor_b32 v7, 64, v7\n" \
"v_mfma_f32_16x16x32_bf16 v[104:107], v[136:139], v[24:27], v[104:107]\n" \
"s_waitcnt vmcnt(14) lgkmcnt(6)\n" \
"s_cselect_b32 s82, s82, s94\n" \
"v_mfma_f32_16x16x32_bf16 v[108:111], v[88:91], v[24:27], v[108:111]\n" \
"s_xor_b32 s57, s56, s90\n" \
"buffer_load_dwordx4 v[24:27], v3, s[36:39], s57 offen offset:0 sc1\n" \
"v_mfma_f32_16x16x32_bf16 v[96:99], a[12:15], v[28:31], v[96:99]\n" \
"ds_write_b128 v7, v[124:127] offset:3072\n" \
"v_mfma_f32_16x16x32_bf16 v[100:103], a[140:143], v[28:31], v[100:103]\n" \
"ds_read_b128 v[88:91], v2 offset:6144\n" \
"s_lshl_b32 s83, s82, 18\n" \
"v_xor_b32 v7, 192, v7\n" \
"v_mfma_f32_16x16x32_bf16 v[104:107], v[140:143], v[28:31], v[104:107]\n" \
"s_waitcnt vmcnt(14) lgkmcnt(6)\n" \
"s_lshr_b32 s58, s83, 8\n" \
"v_mfma_f32_16x16x32_bf16 v[108:111], v[92:95], v[28:31], v[108:111]\n" \
"buffer_load_dwordx4 v[28:31], v3, s[36:39], s57 offen offset:1024 sc1\n" \
"v_mfma_f32_16x16x32_bf16 v[96:99], a[16:19], v[32:35], v[96:99]\n" \
"v_mfma_f32_16x16x32_bf16 v[100:103], a[144:147], v[32:35], v[100:103]\n" \
"ds_read_b128 v[92:95], v2 offset:7168\n" \
"s_and_b32 s58, s58, 0x2000\n" \
"v_mfma_f32_16x16x32_bf16 v[104:107], v[144:147], v[32:35], v[104:107]\n" \
"s_waitcnt vmcnt(14) lgkmcnt(5)\n" \
"s_xor_b32 s83, s83, s58\n" \
"v_mfma_f32_16x16x32_bf16 v[108:111], v[80:83], v[32:35], v[108:111]\n" \
"buffer_load_dwordx4 v[32:35], v3, s[36:39], s57 offen offset:2048 sc1\n" \
"v_mfma_f32_16x16x32_bf16 v[96:99], a[20:23], v[36:39], v[96:99]\n" \
"s_waitcnt lgkmcnt(2)\n" \
"s_cmp_lg_u32 s81, 0\n" \
"s_cbranch_scc1 .Lsg2_nb_%=\n" \
"s_barrier\n" \
".Lsg2_nb_%=:\n" \
"v_mfma_f32_16x16x32_bf16 v[100:103], a[148:151], v[36:39], v[100:103]\n" \
"ds_read_b128 v[80:83], v2 offset:8192\n" \
"v_xor_b32_e64 v7, s84, v7\n" \
"v_mfma_f32_16x16x32_bf16 v[104:107], v[148:151], v[36:39], v[104:107]\n" \
"s_waitcnt vmcnt(14)\n" \
"v_mfma_f32_16x16x32_bf16 v[108:111], v[84:87], v[36:39], v[108:111]\n" \
"buffer_load_dwordx4 v[36:39], v3, s[36:39], s57 offen offset:3072 sc1\n" \
"v_mfma_f32_16x16x32_bf16 v[96:99], a[24:27], v[40:43], v[96:99]\n" \
"ds_read_b128 v[112:115], v8 offset:0\n" \
"v_mfma_f32_16x16x32_bf16 v[100:103], a[152:155], v[40:43], v[100:103]\n" \
"ds_read_b128 v[84:87], v2 offset:9216\n" \
"v_mfma_f32_16x16x32_bf16 v[104:107], v[152:155], v[40:43], v[104:107]\n" \
"s_waitcnt vmcnt(14) lgkmcnt(4)\n" \
"v_mfma_f32_16x16x32_bf16 v[108:111], v[88:91], v[40:43], v[108:111]\n" \
"s_xor_b32 s57, s56, s91\n" \
"buffer_load_dwordx4 v[40:43], v3, s[36:39], s57 offen offset:0 sc1\n" \
"v_mfma_f32_16x16x32_bf16 v[96:99], a[28:31], v[44:47], v[96:99]\n" \
"ds_read_b128 v[116:119], v8 offset:4096\n" \
"v_mfma_f32_16x16x32_bf16 v[100:103], a[156:159], v[44:47], v[100:103]\n" \
"ds_read_b128 v[88:91], v2 offset:10240\n" \
"v_mfma_f32_16x16x32_bf16 v[104:107], v[156:159], v[44:47], v[104:107]\n" \
"s_waitcnt vmcnt(14) lgkmcnt(5)\n" \
"v_mfma_f32_16x16x32_bf16 v[108:111], v[92:95], v[44:47], v[108:111]\n" \
"buffer_load_dwordx4 v[44:47], v3, s[36:39], s57 offen offset:1024 sc1\n" \
"v_mfma_f32_16x16x32_bf16 v[96:99], a[32:35], v[48:51], v[96:99]\n" \
"ds_read_b128 v[120:123], v8 offset:8192\n" \
"s_lshr_b32 s59, s82, 1\n" \
"s_add_u32 s59, s59, 1\n" \
"v_mfma_f32_16x16x32_bf16 v[100:103], a[160:163], v[48:51], v[100:103]\n" \
"ds_read_b128 v[92:95], v2 offset:11264\n" \
"v_mfma_f32_16x16x32_bf16 v[104:107], v[160:163], v[48:51], v[104:107]\n" \
"s_waitcnt vmcnt(14) lgkmcnt(6)\n" \
"v_mfma_f32_16x16x32_bf16 v[108:111], v[80:83], v[48:51], v[108:111]\n" \
"buffer_load_dwordx4 v[48:51], v3, s[36:39], s57 offen offset:2048 sc1\n" \
"v_mfma_f32_16x16x32_bf16 v[96:99], a[36:39], v[52:55], v[96:99]\n" \
"ds_read_b128 v[124:127], v8 offset:12288\n" \
"v_mfma_f32_16x16x32_bf16 v[100:103], a[164:167], v[52:55], v[100:103]\n" \
"ds_read_b128 v[80:83], v2 offset:12288\n" \
"v_mfma_f32_16x16x32_bf16 v[104:107], v[164:167], v[52:55], v[104:107]\n" \
"s_waitcnt vmcnt(14) lgkmcnt(6)\n" \
"v_mfma_f32_16x16x32_bf16 v[108:111], v[84:87], v[52:55], v[108:111]\n" \
"buffer_load_dwordx4 v[52:55], v3, s[36:39], s57 offen offset:3072 sc1\n" \
"v_mfma_f32_16x16x32_bf16 v[96:99], a[40:43], v[56:59], v[96:99]\n" \
"v_mfma_f32_16x16x32_bf16 v[100:103], a[168:171], v[56:59], v[100:103]\n" \
"ds_read_b128 v[84:87], v2 offset:13312\n" \
"v_mfma_f32_16x16x32_bf16 v[104:107], v[168:171], v[56:59], v[104:107]\n" \
"s_waitcnt vmcnt(12) lgkmcnt(5)\n" \
"v_add_f32 v112, v112, v116\n" \
"v_mfma_f32_16x16x32_bf16 v[108:111], v[88:91], v[56:59], v[108:111]\n" \
"s_xor_b32 s57, s56, s92\n" \
"buffer_load_dwordx4 v[56:59], v3, s[36:39], s57 offen offset:0 sc1\n" \
"v_mfma_f32_16x16x32_bf16 v[96:99], a[44:47], v[60:63], v[96:99]\n" \
"v_cmp_gt_u32_e64 s[62:63], s59, v11\n" \
"v_mfma_f32_16x16x32_bf16 v[100:103], a[172:175], v[60:63], v[100:103]\n" \
"ds_read_b128 v[88:91], v2 offset:14336\n" \
"v_add_f32 v113, v113, v117\n" \
"v_mfma_f32_16x16x32_bf16 v[104:107], v[172:175], v[60:63], v[104:107]\n" \
"s_waitcnt vmcnt(12) lgkmcnt(4)\n" \
"v_add_f32 v114, v114, v118\n" \
"v_mfma_f32_16x16x32_bf16 v[108:111], v[92:95], v[60:63], v[108:111]\n" \
"buffer_load_dwordx4 v[60:63], v3, s[36:39], s57 offen offset:1024 sc1\n" \
"v_add_f32_e64 v115, v115, v119\n" \
"v_mfma_f32_16x16x32_bf16 v[96:99], a[48:51], v[64:67], v[96:99]\n" \
"s_add_u32 s54, s55, 1\n" \
"s_cmp_lt_u32 s54, s79\n" \
"v_mfma_f32_16x16x32_bf16 v[100:103], a[176:179], v[64:67], v[100:103]\n" \
"ds_read_b128 v[92:95], v2 offset:15360\n" \
"s_cselect_b64 s[62:63], s[62:63], -1\n" \
"v_mfma_f32_16x16x32_bf16 v[104:107], v[176:179], v[64:67], v[104:107]\n" \
"s_waitcnt vmcnt(12) lgkmcnt(3)\n" \
"v_add_f32_e64 v120, v120, v124\n" \
"v_mfma_f32_16x16x32_bf16 v[108:111], v[80:83], v[64:67], v[108:111]\n" \
"buffer_load_dwordx4 v[64:67], v3, s[36:39], s57 offen offset:2048 sc1\n" \
"v_add_f32_e64 v121, v121, v125\n" \
"v_mfma_f32_16x16x32_bf16 v[96:99], a[52:55], v[68:71], v[96:99]\n" \
"s_cmp_lg_u64 s[62:63], 0\n" \
"s_cselect_b32 s81, 1, 0\n" \
"v_mfma_f32_16x16x32_bf16 v[100:103], a[180:183], v[68:71], v[100:103]\n" \
"ds_read_b128 v[80:83], v2 offset:16384\n" \
"s_cselect_b32 s83, 0x70000000, s83\n" \
"v_mfma_f32_16x16x32_bf16 v[104:107], v[180:183], v[68:71], v[104:107]\n" \
"s_waitcnt vmcnt(12) lgkmcnt(3)\n" \
"v_add_f32 v122, v122, v126\n" \
"v_mfma_f32_16x16x32_bf16 v[108:111], v[84:87], v[68:71], v[108:111]\n" \
"buffer_load_dwordx4 v[68:71], v3, s[36:39], s57 offen offset:3072 sc1\n" \
"v_add_f32_e64 v123, v123, v127\n" \
"v_mfma_f32_16x16x32_bf16 v[96:99], a[56:59], v[16:19], v[96:99]\n" \
"v_add_f32_e64 v112, v112, v120\n" \
"v_mfma_f32_16x16x32_bf16 v[100:103], a[184:187], v[16:19], v[100:103]\n" \
"ds_read_b128 v[84:87], v2 offset:17408\n" \
"v_add_f32_e64 v113, v113, v121\n" \
"v_mfma_f32_16x16x32_bf16 v[104:107], v[184:187], v[16:19], v[104:107]\n" \
"s_waitcnt vmcnt(12) lgkmcnt(3)\n" \
"s_mov_b32 s56, s83\n" \
"v_mfma_f32_16x16x32_bf16 v[108:111], v[88:91], v[16:19], v[108:111]\n" \
"s_xor_b32 s57, s56, s86\n" \
"buffer_load_dwordx4 v[16:19], v3, s[36:39], s57 offen offset:0 sc1\n" \
"v_mfma_f32_16x16x32_bf16 v[96:99], a[60:63], v[20:23], v[96:99]\n" \
"v_add_f32 v114, v114, v122\n" \
"v_mfma_f32_16x16x32_bf16 v[100:103], a[188:191], v[20:23], v[100:103]\n" \
"ds_read_b128 v[88:91], v2 offset:18432\n" \
"v_add_f32_e64 v115, v115, v123\n" \
"v_mfma_f32_16x16x32_bf16 v[104:107], v[188:191], v[20:23], v[104:107]\n" \
"s_waitcnt vmcnt(12) lgkmcnt(3)\n" \
"v_xor_b32 v8, s84, v8\n" \
"v_mfma_f32_16x16x32_bf16 v[108:111], v[92:95], v[20:23], v[108:111]\n" \
"buffer_load_dwordx4 v[20:23], v3, s[36:39], s57 offen offset:1024 sc1\n" \
"v_mul_f32 v116, 0xbfb8aa3b, v112\n" \
"v_mfma_f32_16x16x32_bf16 v[96:99], a[64:67], v[24:27], v[96:99]\n" \
"v_mul_f32 v117, 0xbfb8aa3b, v113\n" \
"v_mfma_f32_16x16x32_bf16 v[100:103], a[192:195], v[24:27], v[100:103]\n" \
"ds_read_b128 v[92:95], v2 offset:19456\n" \
"v_mul_f32 v118, 0xbfb8aa3b, v114\n" \
"v_mfma_f32_16x16x32_bf16 v[104:107], v[192:195], v[24:27], v[104:107]\n" \
"s_waitcnt vmcnt(12) lgkmcnt(3)\n" \
"v_mul_f32 v119, 0xbfb8aa3b, v115\n" \
"v_mfma_f32_16x16x32_bf16 v[108:111], v[80:83], v[24:27], v[108:111]\n" \
"buffer_load_dwordx4 v[24:27], v3, s[36:39], s57 offen offset:2048 sc1\n" \
"v_mfma_f32_16x16x32_bf16 v[96:99], a[68:71], v[28:31], v[96:99]\n" \
"v_exp_f32 v116, v116\n" \
"v_mfma_f32_16x16x32_bf16 v[100:103], a[196:199], v[28:31], v[100:103]\n" \
"ds_read_b128 v[80:83], v2 offset:20480\n" \
"v_mfma_f32_16x16x32_bf16 v[104:107], v[196:199], v[28:31], v[104:107]\n" \
"s_waitcnt vmcnt(12) lgkmcnt(3)\n" \
"v_exp_f32 v117, v117\n" \
"v_mfma_f32_16x16x32_bf16 v[108:111], v[84:87], v[28:31], v[108:111]\n" \
"buffer_load_dwordx4 v[28:31], v3, s[36:39], s57 offen offset:3072 sc1\n" \
"v_mfma_f32_16x16x32_bf16 v[96:99], a[72:75], v[32:35], v[96:99]\n" \
"v_exp_f32_e64 v118, v118\n" \
"v_mfma_f32_16x16x32_bf16 v[100:103], a[200:203], v[32:35], v[100:103]\n" \
"ds_read_b128 v[84:87], v2 offset:21504\n" \
"v_mfma_f32_16x16x32_bf16 v[104:107], v[200:203], v[32:35], v[104:107]\n" \
"s_waitcnt vmcnt(12) lgkmcnt(3)\n" \
"v_exp_f32 v119, v119\n" \
"v_mfma_f32_16x16x32_bf16 v[108:111], v[88:91], v[32:35], v[108:111]\n" \
"s_xor_b32 s57, s56, s87\n" \
"buffer_load_dwordx4 v[32:35], v3, s[36:39], s57 offen offset:0 sc1\n" \
"v_mfma_f32_16x16x32_bf16 v[96:99], a[76:79], v[36:39], v[96:99]\n" \
"v_add_f32 v116, 1.0, v116\n" \
"v_mfma_f32_16x16x32_bf16 v[100:103], a[204:207], v[36:39], v[100:103]\n" \
"ds_read_b128 v[88:91], v2 offset:22528\n" \
"v_add_f32_e64 v117, 1.0, v117\n" \
"v_mfma_f32_16x16x32_bf16 v[104:107], v[204:207], v[36:39], v[104:107]\n" \
"s_waitcnt vmcnt(12) lgkmcnt(3)\n" \
"v_add_f32 v118, 1.0, v118\n" \
"v_mfma_f32_16x16x32_bf16 v[108:111], v[92:95], v[36:39], v[108:111]\n" \
"buffer_load_dwordx4 v[36:39], v3, s[36:39], s57 offen offset:1024 sc1\n" \
"v_add_f32_e64 v119, 1.0, v119\n" \
"v_mfma_f32_16x16x32_bf16 v[96:99], a[80:83], v[40:43], v[96:99]\n" \
"v_rcp_f32_e64 v116, v116\n" \
"v_mfma_f32_16x16x32_bf16 v[100:103], a[208:211], v[40:43], v[100:103]\n" \
"ds_read_b128 v[92:95], v2 offset:23552\n" \
"v_mfma_f32_16x16x32_bf16 v[104:107], v[208:211], v[40:43], v[104:107]\n" \
"s_waitcnt vmcnt(12) lgkmcnt(3)\n" \
"v_rcp_f32 v117, v117\n" \
"v_mfma_f32_16x16x32_bf16 v[108:111], v[80:83], v[40:43], v[108:111]\n" \
"buffer_load_dwordx4 v[40:43], v3, s[36:39], s57 offen offset:2048 sc1\n" \
"v_mfma_f32_16x16x32_bf16 v[96:99], a[84:87], v[44:47], v[96:99]\n" \
"v_rcp_f32_e64 v118, v118\n" \
"v_mfma_f32_16x16x32_bf16 v[100:103], a[212:215], v[44:47], v[100:103]\n" \
"ds_read_b128 v[80:83], v2 offset:24576\n" \
"v_mfma_f32_16x16x32_bf16 v[104:107], v[212:215], v[44:47], v[104:107]\n" \
"s_waitcnt vmcnt(12) lgkmcnt(3)\n" \
"v_rcp_f32 v119, v119\n" \
"v_mfma_f32_16x16x32_bf16 v[108:111], v[84:87], v[44:47], v[108:111]\n" \
"buffer_load_dwordx4 v[44:47], v3, s[36:39], s57 offen offset:3072 sc1\n" \
"v_mul_f32_e64 v112, v112, v116\n" \
"v_mfma_f32_16x16x32_bf16 v[96:99], a[88:91], v[48:51], v[96:99]\n" \
"v_mul_f32_e64 v113, v113, v117\n" \
"v_mfma_f32_16x16x32_bf16 v[100:103], a[216:219], v[48:51], v[100:103]\n" \
"ds_read_b128 v[84:87], v2 offset:25600\n" \
"s_lshl_b32 s80, s44, 16\n" \
"v_mfma_f32_16x16x32_bf16 v[104:107], v[216:219], v[48:51], v[104:107]\n" \
"s_waitcnt vmcnt(12) lgkmcnt(3)\n" \
"s_add_u32 s80, s80, s78\n" \
"v_mfma_f32_16x16x32_bf16 v[108:111], v[88:91], v[48:51], v[108:111]\n" \
"s_xor_b32 s57, s56, s88\n" \
"buffer_load_dwordx4 v[48:51], v3, s[36:39], s57 offen offset:0 sc1\n" \
"v_mfma_f32_16x16x32_bf16 v[96:99], a[92:95], v[52:55], v[96:99]\n" \
"v_mul_f32_e64 v114, v114, v118\n" \
"v_mfma_f32_16x16x32_bf16 v[100:103], a[220:223], v[52:55], v[100:103]\n" \
"ds_read_b128 v[88:91], v2 offset:26624\n" \
"s_lshr_b32 s58, s80, 8\n" \
"v_mfma_f32_16x16x32_bf16 v[104:107], v[220:223], v[52:55], v[104:107]\n" \
"s_waitcnt vmcnt(12) lgkmcnt(3)\n" \
"s_and_b32 s58, s58, 0x2000\n" \
"v_mfma_f32_16x16x32_bf16 v[108:111], v[92:95], v[52:55], v[108:111]\n" \
"buffer_load_dwordx4 v[52:55], v3, s[36:39], s57 offen offset:1024 sc1\n" \
"v_mul_f32_e64 v115, v115, v119\n" \
"v_mfma_f32_16x16x32_bf16 v[96:99], a[96:99], v[56:59], v[96:99]\n" \
"v_cvt_pk_bf16_f32 v0, v112, v113\n" \
"v_mfma_f32_16x16x32_bf16 v[100:103], a[224:227], v[56:59], v[100:103]\n" \
"ds_read_b128 v[92:95], v2 offset:28672\n" \
"s_xor_b32 s80, s80, s58\n" \
"v_mfma_f32_16x16x32_bf16 v[104:107], v[224:227], v[56:59], v[104:107]\n" \
"s_waitcnt vmcnt(12) lgkmcnt(3)\n" \
"s_add_u32 s80, s80, 8388608\n" \
"v_mfma_f32_16x16x32_bf16 v[108:111], v[80:83], v[56:59], v[108:111]\n" \
"buffer_load_dwordx4 v[56:59], v3, s[36:39], s57 offen offset:2048 sc1\n" \
"v_cvt_pk_bf16_f32 v1, v114, v115\n" \
"v_mfma_f32_16x16x32_bf16 v[96:99], a[100:103], v[60:63], v[96:99]\n" \
"buffer_load_dword v11, v4, s[36:39], 0 offen sc1\n" \
"buffer_store_dwordx2 v[0:1], v6, s[36:39], s76 offen sc1\n" \
"v_mfma_f32_16x16x32_bf16 v[100:103], a[228:231], v[60:63], v[100:103]\n" \
"ds_read_b128 v[80:83], v2 offset:0\n" \
"v_mfma_f32_16x16x32_bf16 v[104:107], v[228:231], v[60:63], v[104:107]\n" \
"s_waitcnt vmcnt(14) lgkmcnt(3)\n" \
"v_mfma_f32_16x16x32_bf16 v[108:111], v[84:87], v[60:63], v[108:111]\n" \
"buffer_load_dwordx4 v[60:63], v3, s[36:39], s57 offen offset:3072 sc1\n" \
"v_mfma_f32_16x16x32_bf16 v[96:99], a[104:107], v[64:67], v[96:99]\n" \
"s_add_u32 s55, s55, 1\n" \
"s_mov_b32 s84, 0x4000\n" \
"v_mfma_f32_16x16x32_bf16 v[100:103], a[232:235], v[64:67], v[100:103]\n" \
"ds_read_b128 v[84:87], v2 offset:1024\n" \
"v_mfma_f32_16x16x32_bf16 v[104:107], v[232:235], v[64:67], v[104:107]\n" \
"s_waitcnt vmcnt(14) lgkmcnt(3)\n" \
"v_mfma_f32_16x16x32_bf16 v[108:111], v[88:91], v[64:67], v[108:111]\n" \
"s_xor_b32 s57, s56, s89\n" \
"buffer_load_dwordx4 v[64:67], v3, s[36:39], s57 offen offset:0 sc1\n" \
"v_mfma_f32_16x16x32_bf16 v[112:115], a[108:111], v[68:71], v[96:99]\n" \
"s_mov_b32 s76, s80\n" \
"v_mfma_f32_16x16x32_bf16 v[116:119], a[236:239], v[68:71], v[100:103]\n" \
"ds_read_b128 v[88:91], v2 offset:2048\n" \
"s_mov_b32 s44, s82\n" \
"v_mfma_f32_16x16x32_bf16 v[120:123], v[236:239], v[68:71], v[104:107]\n" \
"s_waitcnt vmcnt(14) lgkmcnt(3)\n" \
"v_mfma_f32_16x16x32_bf16 v[124:127], v[92:95], v[68:71], v[108:111]\n" \
"buffer_load_dwordx4 v[68:71], v3, s[36:39], s57 offen offset:1024 sc1\n" \
"s_cmp_eq_u32 s81, 0\n" \
"s_cbranch_scc1 .Lsg2_loop_%=\n" \
"s_nop 7\n" \
"s_nop 7\n" \
"s_waitcnt vmcnt(0) lgkmcnt(0)\n" \
"ds_write_b128 v7, v[112:115] offset:0\n" \
"v_xor_b32 v7, 64, v7\n" \
"ds_write_b128 v7, v[116:119] offset:1024\n" \
"v_xor_b32 v7, 192, v7\n" \
"ds_write_b128 v7, v[120:123] offset:2048\n" \
"v_xor_b32 v7, 64, v7\n" \
"ds_write_b128 v7, v[124:127] offset:3072\n" \
"v_xor_b32 v7, 192, v7\n" \
"s_waitcnt lgkmcnt(0)\n" \
"s_barrier\n" \
"ds_read_b128 v[112:115], v8 offset:0\n" \
"ds_read_b128 v[116:119], v8 offset:4096\n" \
"ds_read_b128 v[120:123], v8 offset:8192\n" \
"ds_read_b128 v[124:127], v8 offset:12288\n" \
"s_waitcnt lgkmcnt(2)\n" \
"v_add_f32 v112, v112, v116\n" \
"v_add_f32 v113, v113, v117\n" \
"v_add_f32 v114, v114, v118\n" \
"v_add_f32 v115, v115, v119\n" \
"s_waitcnt lgkmcnt(0)\n" \
"v_add_f32 v120, v120, v124\n" \
"v_add_f32 v121, v121, v125\n" \
"v_add_f32 v122, v122, v126\n" \
"v_add_f32 v123, v123, v127\n" \
"v_add_f32 v112, v112, v120\n" \
"v_add_f32 v113, v113, v121\n" \
"v_add_f32 v114, v114, v122\n" \
"v_add_f32 v115, v115, v123\n" \
"v_xor_b32 v8, s84, v8\n" \
"v_mul_f32 v116, 0xbfb8aa3b, v112\n" \
"v_mul_f32 v117, 0xbfb8aa3b, v113\n" \
"v_mul_f32 v118, 0xbfb8aa3b, v114\n" \
"v_mul_f32 v119, 0xbfb8aa3b, v115\n" \
"v_exp_f32 v116, v116\n" \
"v_exp_f32 v117, v117\n" \
"v_exp_f32 v118, v118\n" \
"v_exp_f32 v119, v119\n" \
"v_add_f32 v116, 1.0, v116\n" \
"v_add_f32 v117, 1.0, v117\n" \
"v_add_f32 v118, 1.0, v118\n" \
"v_add_f32 v119, 1.0, v119\n" \
"v_rcp_f32 v116, v116\n" \
"v_rcp_f32 v117, v117\n" \
"v_rcp_f32 v118, v118\n" \
"v_rcp_f32 v119, v119\n" \
"v_mul_f32 v112, v112, v116\n" \
"v_mul_f32 v113, v113, v117\n" \
"v_mul_f32 v114, v114, v118\n" \
"v_mul_f32 v115, v115, v119\n" \
"v_cvt_pk_bf16_f32 v0, v112, v113\n" \
"v_cvt_pk_bf16_f32 v1, v114, v115\n" \
"v_xor_b32 v7, 0x4000, v7\n" \
"buffer_store_dwordx2 v[0:1], v6, s[36:39], s76 offen sc1\n" \
"s_cmp_lg_u32 s55, s95\n" \
"s_cbranch_scc1 .Lsg2_g_%=\n" \
"s_waitcnt vmcnt(0) lgkmcnt(0)\n" \
"v_mov_b32 v9, 1\n" \
"s_mov_b64 exec, 1\n" \
"buffer_atomic_add v9, off, s[36:39], s49 offset:64\n" \
"s_mov_b64 exec, -1\n" \
"s_mov_b32 s79, s46\n" \
".Lsg2_g_%=:\n" \
"s_cmp_lt_u32 s55, s46\n" \
"s_cbranch_scc1 .Lsg2_prime_%=\n" \
"s_branch .Lsg2_o_%=\n" \
".Lsg2_ok_%=:\n" \
"s_mov_b32 s44, s94\n" \
".Lsg2_c_%=:\n" \
"s_nop 7\n" \
"s_nop 7\n" \
"s_lshl_b32 s54, s44, 16\n" \
"s_add_u32 s54, s54, s78\n" \
"s_lshr_b32 s58, s54, 8\n" \
"s_and_b32 s58, s58, 0x2000\n" \
"s_xor_b32 s54, s54, s58\n" \
"s_lshl_b32 s80, s44, 15\n" \
"s_cmp_lt_u32 s44, s96\n" \
"s_cselect_b32 s80, s80, 0x70000000\n" \
"s_cselect_b32 s54, s54, 0x70000000\n" \
"s_add_u32 s55, s54, 8388608\n" \
"s_cmp_lt_i32 s44, s50\n" \
"s_cselect_b32 s58, s45, s47\n" \
"s_add_u32 s44, s44, s58\n" \
"buffer_load_dwordx2 v[128:129], v6, s[36:39], s54 offen sc1\n" \
"buffer_load_dwordx2 v[130:131], v6, s[36:39], s55 offen sc1\n" \
"buffer_load_dwordx2 v[132:133], v5, s[64:67], s80 offen\n" \
"s_lshl_b32 s54, s44, 16\n" \
"s_add_u32 s54, s54, s78\n" \
"s_lshr_b32 s58, s54, 8\n" \
"s_and_b32 s58, s58, 0x2000\n" \
"s_xor_b32 s54, s54, s58\n" \
"s_lshl_b32 s81, s44, 15\n" \
"s_cmp_lt_u32 s44, s96\n" \
"s_cselect_b32 s81, s81, 0x70000000\n" \
"s_cselect_b32 s54, s54, 0x70000000\n" \
"s_add_u32 s55, s54, 8388608\n" \
"s_cmp_lt_i32 s44, s50\n" \
"s_cselect_b32 s58, s45, s47\n" \
"s_add_u32 s44, s44, s58\n" \
"buffer_load_dwordx2 v[134:135], v6, s[36:39], s54 offen sc1\n" \
"buffer_load_dwordx2 v[136:137], v6, s[36:39], s55 offen sc1\n" \
"buffer_load_dwordx2 v[138:139], v5, s[64:67], s81 offen\n" \
"s_lshl_b32 s54, s44, 16\n" \
"s_add_u32 s54, s54, s78\n" \
"s_lshr_b32 s58, s54, 8\n" \
"s_and_b32 s58, s58, 0x2000\n" \
"s_xor_b32 s54, s54, s58\n" \
"s_lshl_b32 s82, s44, 15\n" \
"s_cmp_lt_u32 s44, s96\n" \
"s_cselect_b32 s82, s82, 0x70000000\n" \
"s_cselect_b32 s54, s54, 0x70000000\n" \
"s_add_u32 s55, s54, 8388608\n" \
"s_cmp_lt_i32 s44, s50\n" \
"s_cselect_b32 s58, s45, s47\n" \
"s_add_u32 s44, s44, s58\n" \
"buffer_load_dwordx2 v[140:141], v6, s[36:39], s54 offen sc1\n" \
"buffer_load_dwordx2 v[142:143], v6, s[36:39], s55 offen sc1\n" \
"buffer_load_dwordx2 v[144:145], v5, s[64:67], s82 offen\n" \
"s_lshl_b32 s54, s44, 16\n" \
"s_add_u32 s54, s54, s78\n" \
"s_lshr_b32 s58, s54, 8\n" \
"s_and_b32 s58, s58, 0x2000\n" \
"s_xor_b32 s54, s54, s58\n" \
"s_lshl_b32 s83, s44, 15\n" \
"s_cmp_lt_u32 s44, s96\n" \
"s_cselect_b32 s83, s83, 0x70000000\n" \
"s_cselect_b32 s54, s54, 0x70000000\n" \
"s_add_u32 s55, s54, 8388608\n" \
"s_cmp_lt_i32 s44, s50\n" \
"s_cselect_b32 s58, s45, s47\n" \
"s_add_u32 s44, s44, s58\n" \
"buffer_load_dwordx2 v[146:147], v6, s[36:39], s54 offen sc1\n" \
"buffer_load_dwordx2 v[148:149], v6, s[36:39], s55 offen sc1\n" \
"buffer_load_dwordx2 v[150:151], v5, s[64:67], s83 offen\n" \
"s_lshl_b32 s54, s44, 16\n" \
"s_add_u32 s54, s54, s78\n" \
"s_lshr_b32 s58, s54, 8\n" \
"s_and_b32 s58, s58, 0x2000\n" \
"s_xor_b32 s54, s54, s58\n" \
"s_lshl_b32 s84, s44, 15\n" \
"s_cmp_lt_u32 s44, s96\n" \
"s_cselect_b32 s84, s84, 0x70000000\n" \
"s_cselect_b32 s54, s54, 0x70000000\n" \
"s_add_u32 s55, s54, 8388608\n" \
"s_cmp_lt_i32 s44, s50\n" \
"s_cselect_b32 s58, s45, s47\n" \
"s_add_u32 s44, s44, s58\n" \
"buffer_load_dwordx2 v[152:153], v6, s[36:39], s54 offen sc1\n" \
"buffer_load_dwordx2 v[154:155], v6, s[36:39], s55 offen sc1\n" \
"buffer_load_dwordx2 v[156:157], v5, s[64:67], s84 offen\n" \
"s_lshl_b32 s54, s44, 16\n" \
"s_add_u32 s54, s54, s78\n" \
"s_lshr_b32 s58, s54, 8\n" \
"s_and_b32 s58, s58, 0x2000\n" \
"s_xor_b32 s54, s54, s58\n" \
"s_lshl_b32 s85, s44, 15\n" \
"s_cmp_lt_u32 s44, s96\n" \
"s_cselect_b32 s85, s85, 0x70000000\n" \
"s_cselect_b32 s54, s54, 0x70000000\n" \
"s_add_u32 s55, s54, 8388608\n" \
"s_cmp_lt_i32 s44, s50\n" \
"s_cselect_b32 s58, s45, s47\n" \
"s_add_u32 s44, s44, s58\n" \
"buffer_load_dwordx2 v[158:159], v6, s[36:39], s54 offen sc1\n" \
"buffer_load_dwordx2 v[160:161], v6, s[36:39], s55 offen sc1\n" \
"buffer_load_dwordx2 v[162:163], v5, s[64:67], s85 offen\n" \
"s_lshl_b32 s54, s44, 16\n" \
"s_add_u32 s54, s54, s78\n" \
"s_lshr_b32 s58, s54, 8\n" \
"s_and_b32 s58, s58, 0x2000\n" \
"s_xor_b32 s54, s54, s58\n" \
"s_lshl_b32 s86, s44, 15\n" \
"s_cmp_lt_u32 s44, s96\n" \
"s_cselect_b32 s86, s86, 0x70000000\n" \
"s_cselect_b32 s54, s54, 0x70000000\n" \
"s_add_u32 s55, s54, 8388608\n" \
"s_cmp_lt_i32 s44, s50\n" \
"s_cselect_b32 s58, s45, s47\n" \
"s_add_u32 s44, s44, s58\n" \
"buffer_load_dwordx2 v[164:165], v6, s[36:39], s54 offen sc1\n" \
"buffer_load_dwordx2 v[166:167], v6, s[36:39], s55 offen sc1\n" \
"buffer_load_dwordx2 v[168:169], v5, s[64:67], s86 offen\n" \
"s_lshl_b32 s54, s44, 16\n" \
"s_add_u32 s54, s54, s78\n" \
"s_lshr_b32 s58, s54, 8\n" \
"s_and_b32 s58, s58, 0x2000\n" \
"s_xor_b32 s54, s54, s58\n" \
"s_lshl_b32 s87, s44, 15\n" \
"s_cmp_lt_u32 s44, s96\n" \
"s_cselect_b32 s87, s87, 0x70000000\n" \
"s_cselect_b32 s54, s54, 0x70000000\n" \
"s_add_u32 s55, s54, 8388608\n" \
"s_cmp_lt_i32 s44, s50\n" \
"s_cselect_b32 s58, s45, s47\n" \
"s_add_u32 s44, s44, s58\n" \
"buffer_load_dwordx2 v[170:171], v6, s[36:39], s54 offen sc1\n" \
"buffer_load_dwordx2 v[172:173], v6, s[36:39], s55 offen sc1\n" \
"buffer_load_dwordx2 v[174:175], v5, s[64:67], s87 offen\n" \
"s_lshl_b32 s54, s44, 16\n" \
"s_add_u32 s54, s54, s78\n" \
"s_lshr_b32 s58, s54, 8\n" \
"s_and_b32 s58, s58, 0x2000\n" \
"s_xor_b32 s54, s54, s58\n" \
"s_lshl_b32 s88, s44, 15\n" \
"s_cmp_lt_u32 s44, s96\n" \
"s_cselect_b32 s88, s88, 0x70000000\n" \
"s_cselect_b32 s54, s54, 0x70000000\n" \
"s_add_u32 s55, s54, 8388608\n" \
"s_cmp_lt_i32 s44, s50\n" \
"s_cselect_b32 s58, s45, s47\n" \
"s_add_u32 s44, s44, s58\n" \
"buffer_load_dwordx2 v[176:177], v6, s[36:39], s54 offen sc1\n" \
"buffer_load_dwordx2 v[178:179], v6, s[36:39], s55 offen sc1\n" \
"buffer_load_dwordx2 v[180:181], v5, s[64:67], s88 offen\n" \
"s_lshl_b32 s54, s44, 16\n" \
"s_add_u32 s54, s54, s78\n" \
"s_lshr_b32 s58, s54, 8\n" \
"s_and_b32 s58, s58, 0x2000\n" \
"s_xor_b32 s54, s54, s58\n" \
"s_lshl_b32 s89, s44, 15\n" \
"s_cmp_lt_u32 s44, s96\n" \
"s_cselect_b32 s89, s89, 0x70000000\n" \
"s_cselect_b32 s54, s54, 0x70000000\n" \
"s_add_u32 s55, s54, 8388608\n" \
"s_cmp_lt_i32 s44, s50\n" \
"s_cselect_b32 s58, s45, s47\n" \
"s_add_u32 s44, s44, s58\n" \
"buffer_load_dwordx2 v[182:183], v6, s[36:39], s54 offen sc1\n" \
"buffer_load_dwordx2 v[184:185], v6, s[36:39], s55 offen sc1\n" \
"buffer_load_dwordx2 v[186:187], v5, s[64:67], s89 offen\n" \
"s_waitcnt vmcnt(29)\n" \
"v_lshlrev_b32 v112, 16, v128\n" \
"v_and_b32 v113, 0xffff0000, v128\n" \
"v_lshlrev_b32 v114, 16, v129\n" \
"v_and_b32 v115, 0xffff0000, v129\n" \
"s_waitcnt vmcnt(28)\n" \
"v_lshlrev_b32 v116, 16, v130\n" \
"v_and_b32 v117, 0xffff0000, v130\n" \
"v_lshlrev_b32 v118, 16, v131\n" \
"v_and_b32 v119, 0xffff0000, v131\n" \
"s_waitcnt vmcnt(27)\n" \
"v_lshlrev_b32 v120, 16, v132\n" \
"v_and_b32 v121, 0xffff0000, v132\n" \
"v_lshlrev_b32 v122, 16, v133\n" \
"v_and_b32 v123, 0xffff0000, v133\n" \
"v_fmac_f32 v120, v112, v116\n" \
"v_fmac_f32 v121, v113, v117\n" \
"v_fmac_f32 v122, v114, v118\n" \
"v_fmac_f32 v123, v115, v119\n" \
"v_cvt_pk_bf16_f32 v132, v120, v121\n" \
"v_cvt_pk_bf16_f32 v133, v122, v123\n" \
"buffer_store_dwordx2 v[132:133], v5, s[68:71], s80 offen\n" \
"s_waitcnt vmcnt(27)\n" \
"v_lshlrev_b32 v112, 16, v134\n" \
"v_and_b32 v113, 0xffff0000, v134\n" \
"v_lshlrev_b32 v114, 16, v135\n" \
"v_and_b32 v115, 0xffff0000, v135\n" \
"s_waitcnt vmcnt(26)\n" \
"v_lshlrev_b32 v116, 16, v136\n" \
"v_and_b32 v117, 0xffff0000, v136\n" \
"v_lshlrev_b32 v118, 16, v137\n" \
"v_and_b32 v119, 0xffff0000, v137\n" \
"s_waitcnt vmcnt(25)\n" \
"v_lshlrev_b32 v120, 16, v138\n" \
"v_and_b32 v121, 0xffff0000, v138\n" \
"v_lshlrev_b32 v122, 16, v139\n" \
"v_and_b32 v123, 0xffff0000, v139\n" \
"v_fmac_f32 v120, v112, v116\n" \
"v_fmac_f32 v121, v113, v117\n" \
"v_fmac_f32 v122, v114, v118\n" \
"v_fmac_f32 v123, v115, v119\n" \
"v_cvt_pk_bf16_f32 v138, v120, v121\n" \
"v_cvt_pk_bf16_f32 v139, v122, v123\n" \
"buffer_store_dwordx2 v[138:139], v5, s[68:71], s81 offen\n" \
"s_waitcnt vmcnt(25)\n" \
"v_lshlrev_b32 v112, 16, v140\n" \
"v_and_b32 v113, 0xffff0000, v140\n" \
"v_lshlrev_b32 v114, 16, v141\n" \
"v_and_b32 v115, 0xffff0000, v141\n" \
"s_waitcnt vmcnt(24)\n" \
"v_lshlrev_b32 v116, 16, v142\n" \
"v_and_b32 v117, 0xffff0000, v142\n" \
"v_lshlrev_b32 v118, 16, v143\n" \
"v_and_b32 v119, 0xffff0000, v143\n" \
"s_waitcnt vmcnt(23)\n" \
"v_lshlrev_b32 v120, 16, v144\n" \
"v_and_b32 v121, 0xffff0000, v144\n" \
"v_lshlrev_b32 v122, 16, v145\n" \
"v_and_b32 v123, 0xffff0000, v145\n" \
"v_fmac_f32 v120, v112, v116\n" \
"v_fmac_f32 v121, v113, v117\n" \
"v_fmac_f32 v122, v114, v118\n" \
"v_fmac_f32 v123, v115, v119\n" \
"v_cvt_pk_bf16_f32 v144, v120, v121\n" \
"v_cvt_pk_bf16_f32 v145, v122, v123\n" \
"buffer_store_dwordx2 v[144:145], v5, s[68:71], s82 offen\n" \
"s_waitcnt vmcnt(23)\n" \
"v_lshlrev_b32 v112, 16, v146\n" \
"v_and_b32 v113, 0xffff0000, v146\n" \
"v_lshlrev_b32 v114, 16, v147\n" \
"v_and_b32 v115, 0xffff0000, v147\n" \
"s_waitcnt vmcnt(22)\n" \
"v_lshlrev_b32 v116, 16, v148\n" \
"v_and_b32 v117, 0xffff0000, v148\n" \
"v_lshlrev_b32 v118, 16, v149\n" \
"v_and_b32 v119, 0xffff0000, v149\n" \
"s_waitcnt vmcnt(21)\n" \
"v_lshlrev_b32 v120, 16, v150\n" \
"v_and_b32 v121, 0xffff0000, v150\n" \
"v_lshlrev_b32 v122, 16, v151\n" \
"v_and_b32 v123, 0xffff0000, v151\n" \
"v_fmac_f32 v120, v112, v116\n" \
"v_fmac_f32 v121, v113, v117\n" \
"v_fmac_f32 v122, v114, v118\n" \
"v_fmac_f32 v123, v115, v119\n" \
"v_cvt_pk_bf16_f32 v150, v120, v121\n" \
"v_cvt_pk_bf16_f32 v151, v122, v123\n" \
"buffer_store_dwordx2 v[150:151], v5, s[68:71], s83 offen\n" \
"s_waitcnt vmcnt(21)\n" \
"v_lshlrev_b32 v112, 16, v152\n" \
"v_and_b32 v113, 0xffff0000, v152\n" \
"v_lshlrev_b32 v114, 16, v153\n" \
"v_and_b32 v115, 0xffff0000, v153\n" \
"s_waitcnt vmcnt(20)\n" \
"v_lshlrev_b32 v116, 16, v154\n" \
"v_and_b32 v117, 0xffff0000, v154\n" \
"v_lshlrev_b32 v118, 16, v155\n" \
"v_and_b32 v119, 0xffff0000, v155\n" \
"s_waitcnt vmcnt(19)\n" \
"v_lshlrev_b32 v120, 16, v156\n" \
"v_and_b32 v121, 0xffff0000, v156\n" \
"v_lshlrev_b32 v122, 16, v157\n" \
"v_and_b32 v123, 0xffff0000, v157\n" \
"v_fmac_f32 v120, v112, v116\n" \
"v_fmac_f32 v121, v113, v117\n" \
"v_fmac_f32 v122, v114, v118\n" \
"v_fmac_f32 v123, v115, v119\n" \
"v_cvt_pk_bf16_f32 v156, v120, v121\n" \
"v_cvt_pk_bf16_f32 v157, v122, v123\n" \
"buffer_store_dwordx2 v[156:157], v5, s[68:71], s84 offen\n" \
"s_waitcnt vmcnt(19)\n" \
"v_lshlrev_b32 v112, 16, v158\n" \
"v_and_b32 v113, 0xffff0000, v158\n" \
"v_lshlrev_b32 v114, 16, v159\n" \
"v_and_b32 v115, 0xffff0000, v159\n" \
"s_waitcnt vmcnt(18)\n" \
"v_lshlrev_b32 v116, 16, v160\n" \
"v_and_b32 v117, 0xffff0000, v160\n" \
"v_lshlrev_b32 v118, 16, v161\n" \
"v_and_b32 v119, 0xffff0000, v161\n" \
"s_waitcnt vmcnt(17)\n" \
"v_lshlrev_b32 v120, 16, v162\n" \
"v_and_b32 v121, 0xffff0000, v162\n" \
"v_lshlrev_b32 v122, 16, v163\n" \
"v_and_b32 v123, 0xffff0000, v163\n" \
"v_fmac_f32 v120, v112, v116\n" \
"v_fmac_f32 v121, v113, v117\n" \
"v_fmac_f32 v122, v114, v118\n" \
"v_fmac_f32 v123, v115, v119\n" \
"v_cvt_pk_bf16_f32 v162, v120, v121\n" \
"v_cvt_pk_bf16_f32 v163, v122, v123\n" \
"buffer_store_dwordx2 v[162:163], v5, s[68:71], s85 offen\n" \
"s_waitcnt vmcnt(17)\n" \
"v_lshlrev_b32 v112, 16, v164\n" \
"v_and_b32 v113, 0xffff0000, v164\n" \
"v_lshlrev_b32 v114, 16, v165\n" \
"v_and_b32 v115, 0xffff0000, v165\n" \
"s_waitcnt vmcnt(16)\n" \
"v_lshlrev_b32 v116, 16, v166\n" \
"v_and_b32 v117, 0xffff0000, v166\n" \
"v_lshlrev_b32 v118, 16, v167\n" \
"v_and_b32 v119, 0xffff0000, v167\n" \
"s_waitcnt vmcnt(15)\n" \
"v_lshlrev_b32 v120, 16, v168\n" \
"v_and_b32 v121, 0xffff0000, v168\n" \
"v_lshlrev_b32 v122, 16, v169\n" \
"v_and_b32 v123, 0xffff0000, v169\n" \
"v_fmac_f32 v120, v112, v116\n" \
"v_fmac_f32 v121, v113, v117\n" \
"v_fmac_f32 v122, v114, v118\n" \
"v_fmac_f32 v123, v115, v119\n" \
"v_cvt_pk_bf16_f32 v168, v120, v121\n" \
"v_cvt_pk_bf16_f32 v169, v122, v123\n" \
"buffer_store_dwordx2 v[168:169], v5, s[68:71], s86 offen\n" \
"s_waitcnt vmcnt(15)\n" \
"v_lshlrev_b32 v112, 16, v170\n" \
"v_and_b32 v113, 0xffff0000, v170\n" \
"v_lshlrev_b32 v114, 16, v171\n" \
"v_and_b32 v115, 0xffff0000, v171\n" \
"s_waitcnt vmcnt(14)\n" \
"v_lshlrev_b32 v116, 16, v172\n" \
"v_and_b32 v117, 0xffff0000, v172\n" \
"v_lshlrev_b32 v118, 16, v173\n" \
"v_and_b32 v119, 0xffff0000, v173\n" \
"s_waitcnt vmcnt(13)\n" \
"v_lshlrev_b32 v120, 16, v174\n" \
"v_and_b32 v121, 0xffff0000, v174\n" \
"v_lshlrev_b32 v122, 16, v175\n" \
"v_and_b32 v123, 0xffff0000, v175\n" \
"v_fmac_f32 v120, v112, v116\n" \
"v_fmac_f32 v121, v113, v117\n" \
"v_fmac_f32 v122, v114, v118\n" \
"v_fmac_f32 v123, v115, v119\n" \
"v_cvt_pk_bf16_f32 v174, v120, v121\n" \
"v_cvt_pk_bf16_f32 v175, v122, v123\n" \
"buffer_store_dwordx2 v[174:175], v5, s[68:71], s87 offen\n" \
"s_waitcnt vmcnt(13)\n" \
"v_lshlrev_b32 v112, 16, v176\n" \
"v_and_b32 v113, 0xffff0000, v176\n" \
"v_lshlrev_b32 v114, 16, v177\n" \
"v_and_b32 v115, 0xffff0000, v177\n" \
"s_waitcnt vmcnt(12)\n" \
"v_lshlrev_b32 v116, 16, v178\n" \
"v_and_b32 v117, 0xffff0000, v178\n" \
"v_lshlrev_b32 v118, 16, v179\n" \
"v_and_b32 v119, 0xffff0000, v179\n" \
"s_waitcnt vmcnt(11)\n" \
"v_lshlrev_b32 v120, 16, v180\n" \
"v_and_b32 v121, 0xffff0000, v180\n" \
"v_lshlrev_b32 v122, 16, v181\n" \
"v_and_b32 v123, 0xffff0000, v181\n" \
"v_fmac_f32 v120, v112, v116\n" \
"v_fmac_f32 v121, v113, v117\n" \
"v_fmac_f32 v122, v114, v118\n" \
"v_fmac_f32 v123, v115, v119\n" \
"v_cvt_pk_bf16_f32 v180, v120, v121\n" \
"v_cvt_pk_bf16_f32 v181, v122, v123\n" \
"buffer_store_dwordx2 v[180:181], v5, s[68:71], s88 offen\n" \
"s_waitcnt vmcnt(11)\n" \
"v_lshlrev_b32 v112, 16, v182\n" \
"v_and_b32 v113, 0xffff0000, v182\n" \
"v_lshlrev_b32 v114, 16, v183\n" \
"v_and_b32 v115, 0xffff0000, v183\n" \
"s_waitcnt vmcnt(10)\n" \
"v_lshlrev_b32 v116, 16, v184\n" \
"v_and_b32 v117, 0xffff0000, v184\n" \
"v_lshlrev_b32 v118, 16, v185\n" \
"v_and_b32 v119, 0xffff0000, v185\n" \
"s_waitcnt vmcnt(9)\n" \
"v_lshlrev_b32 v120, 16, v186\n" \
"v_and_b32 v121, 0xffff0000, v186\n" \
"v_lshlrev_b32 v122, 16, v187\n" \
"v_and_b32 v123, 0xffff0000, v187\n" \
"v_fmac_f32 v120, v112, v116\n" \
"v_fmac_f32 v121, v113, v117\n" \
"v_fmac_f32 v122, v114, v118\n" \
"v_fmac_f32 v123, v115, v119\n" \
"v_cvt_pk_bf16_f32 v186, v120, v121\n" \
"v_cvt_pk_bf16_f32 v187, v122, v123\n" \
"buffer_store_dwordx2 v[186:187], v5, s[68:71], s89 offen\n" \
"s_cmp_lt_u32 s44, s96\n" \
"s_cbranch_scc1 .Lsg2_c_%=\n" \
"s_branch .Lsg2_end_%=\n" \
".Lsg2_w_%=:\n" \
"s_nop 7\n" \
"s_nop 7\n" \
"s_waitcnt vmcnt(0) lgkmcnt(0)\n" \
"s_mov_b32 s61, 0\n" \
".Lsg2_wa_%=:\n" \
"s_cmp_lg_u32 s60, 0\n" \
"s_cbranch_scc1 .Lsg2_wk_%=\n" \
"buffer_load_dword v11, v4, s[36:39], 0 offen sc1\n" \
"s_waitcnt vmcnt(0)\n" \
"v_cmp_gt_u32_e64 s[62:63], s59, v11\n" \
"s_nop 4\n" \
"s_cmp_eq_u64 s[62:63], 0\n" \
"s_cbranch_scc1 .Lsg2_wk_%=\n" \
"s_sleep 1\n" \
"s_add_u32 s61, s61, 1\n" \
"s_cmp_lt_u32 s61, 65536\n" \
"s_cbranch_scc1 .Lsg2_wa_%=\n" \
"s_mov_b32 s60, 1\n" \
"v_mov_b32 v11, 256\n" \
"v_mov_b32 v9, 1\n" \
"buffer_store_dword v9, v11, s[36:39], 0 offen sc1\n" \
"s_waitcnt vmcnt(0) lgkmcnt(0)\n" \
"s_branch .Lsg2_wk_%=\n" \
".Lsg2_o_%=:\n" \
"s_nop 7\n" \
"s_nop 7\n" \
"s_waitcnt vmcnt(0) lgkmcnt(0)\n" \
"s_mov_b32 s61, 0\n" \
".Lsg2_oa_%=:\n" \
"s_cmp_lg_u32 s60, 0\n" \
"s_cbranch_scc1 .Lsg2_ok_%=\n" \
"buffer_load_dword v9, off, s[36:39], s49 offset:0 sc1\n" \
"s_sub_u32 s54, 10, s47\n" \
"s_waitcnt vmcnt(0)\n" \
"v_cmp_gt_u32 vcc, s54, v9\n" \
"s_nop 4\n" \
"s_cbranch_vccz .Lsg2_ok_%=\n" \
"s_sleep 1\n" \
"s_add_u32 s61, s61, 1\n" \
"s_cmp_lt_u32 s61, 65536\n" \
"s_cbranch_scc1 .Lsg2_oa_%=\n" \
"s_mov_b32 s60, 1\n" \
"v_mov_b32 v11, 256\n" \
"v_mov_b32 v9, 1\n" \
"buffer_store_dword v9, v11, s[36:39], 0 offen sc1\n" \
"s_waitcnt vmcnt(0) lgkmcnt(0)\n" \
"s_branch .Lsg2_ok_%=\n" \
".Lsg2_end_%=:\n" \
"s_endpgm\n"
#define OHACK 0
#define TUNEV(i, c) (c)
#define C_MO 5
#define C_MM 6
#define FARP (TUNEV(11, 1) << 13)
#define C_X 104
#define C_Y 78
#define C_DS 36
#define C_VD 12
#define C_YL 9
#define C_D0 50
#define C_Y0 0
#define C_GD 265
#define C_PW3 4
#define C_TK 0
__device__ __forceinline__ int tkbase(const Params& p, int h, int fp) { const int m = TUNEV(8, C_TK); return WS_TK + h * (4 * MB + 65536) + (fp ^ (m == 2 ? (h & 8) << 10 : (m ^ 1) << 13)); }

typedef u32 v32u __attribute__((ext_vector_type(32)));
typedef float f32x2 __attribute__((ext_vector_type(2)));
__device__ __forceinline__ float xsum(float s) {
    auto a = __builtin_amdgcn_permlane16_swap(__builtin_bit_cast(u32, s), __builtin_bit_cast(u32, s), false, false);
    s = __builtin_bit_cast(float, (u32)a[0]) + __builtin_bit_cast(float, (u32)a[1]);
    auto b = __builtin_amdgcn_permlane32_swap(__builtin_bit_cast(u32, s), __builtin_bit_cast(u32, s), false, false);
    return __builtin_bit_cast(float, (u32)b[0]) + __builtin_bit_cast(float, (u32)b[1]);
}
__device__ __forceinline__ void ln_tile(const Params& p, const v4u (&xv)[8], int rt, int w, int lane, int fp) {
    const int n = lane & 15, g = lane >> 4;
    const rsrc_t rs = __builtin_amdgcn_make_buffer_rsrc(p.ws, 0, WS_END, 0x00020000);
    LAS float* red = (LAS float*)(uintptr_t)(136 * 1024);
    f32x2 xf[32], s2 = {0.f, 0.f};
#pragma unroll
    for (int i = 0; i < 32; ++i) { xf[i] = f32x2{bflo(xv[i >> 2][i & 3]), bfhi(xv[i >> 2][i & 3])}; s2 += xf[i]; }
    const float s = xsum(s2.x + s2.y);
    if (g == 0) red[n * 4 + w] = s;
    BAR();
    f32x4 t = *(LAS f32x4*)(red + n * 4);
    const float mean = ((t.x + t.y) + (t.z + t.w)) * (1.0f / DM);
    f32x2 q2 = {0.f, 0.f};
#pragma unroll
    for (int i = 0; i < 32; ++i) { const f32x2 a = xf[i] - mean; q2 += a * a; }
    const float q = xsum(q2.x + q2.y);
    if (g == 0) red[64 + n * 4 + w] = q;
    BAR();
    t = *(LAS f32x4*)(red + 64 + n * 4);
    const float var = ((t.x + t.y) + (t.z + t.w)) * (1.0f / DM);
    const float mb = rbf(mean);
    const float rd = 1.0f / rbf(sqrtf(rbf(rbf(var) + 1e-5f)));
#pragma unroll
    for (int i = 0; i < 8; ++i) {
        v4u o;
#pragma unroll
        for (int e = 0; e < 4; ++e) { const f32x2 a = xf[4 * i + e] - mb; const u32 d = pack2(a.x, a.y); const f32x2 y = f32x2{bflo(d), bfhi(d)} * rd; o[e] = pack2(y.x, y.y); }
        st128s(o, rs, farx(WS_XF + rt * 65536 + w * 16384 + fp) + i * 1024 + lane * 16);
    }
    WAIT_V0();
    __builtin_amdgcn_s_barrier(); CFENCE();
    if (w == 0 && lane == 0) __hip_atomic_fetch_add((GAS u32*)(p.ws + WS_CT) + CT_LN + (rt >> 4) * 32, 1u, RLXA);
}
__device__ __forceinline__ void ln_role(const Params& p, int c, int w, int fp) {
    const int lane = threadIdx.x & 63;
    const u16* xr = p.x + (size_t)(c * 16 + (lane & 15)) * DM + w * 256 + (lane >> 4) * 8;
    v4u xa[8], xb[8];
#pragma unroll
    for (int i = 0; i < 8; ++i) { xa[i] = *(const v4u*)(xr + i * 32); xb[i] = *(const v4u*)(xr + 128 * DM + i * 32); }
#pragma unroll 1
    for (int k = 0; k < 16; ++k) {
        v4u xc[8];
#pragma unroll
        for (int i = 0; i < 8; ++i) xc[i] = *(const v4u*)(xr + (size_t)min(k + 2, 15) * 128 * DM + i * 32);
        ln_tile(p, xa, k * 8 + c, w, lane, fp);
#pragma unroll
        for (int i = 0; i < 8; ++i) { xa[i] = xb[i]; xb[i] = xc[i]; }
    }
}

__device__ __forceinline__ void p1_role(const Params& p, int b, int s0, int s1, int id, int w, int fp) {
    const int lane = __builtin_amdgcn_mbcnt_hi(~0u, __builtin_amdgcn_mbcnt_lo(~0u, 0u)), n = lane & 15, g = lane >> 4;
    const bool g1 = b >= 96;
    const int h = b & 31, kind = b >> 5;
    const int stride = g1 ? FF * 2 : 8192;
    const u16* src = g1 ? colsrc2(p, w >> 1, b - 96) : colsrc(p, kind == 0 ? 1 : (kind == 1 ? 0 : 2), 2 * h + (w >> 1));
    const v4u rs = mksrd(p.ws, WS_END), rw = mksrd(src, 1023 * stride + 128);
    const int v0 = lane * 16;
    const int v1 = (8 * (w & 1) + (lane >> 3)) * stride + ((lane & 7) ^ ((((lane >> 4) & 1) | ((w & 1) << 1)) << 1)) * 16;
    const int swz = ((n >> 3) | ((g & 1) << 1)), rowo = (8 * g + (n >> 2)) * 128 + (n & 3) * 8;
    const int v2 = (g1 ? 0 : (w >> 1) * 8192) + rowo + (((g1 ? w : 2 * (w & 1)) ^ swz) * 32);
    const int v3 = (g1 ? 8192 : (w >> 1) * 8192) + rowo + (((g1 ? w : 2 * (w & 1) + 1) ^ swz) * 32);
    const int v4 = (w >> 1) * 65536 + (w & 1) * 16384 + lane * 16, ks = (b - 96) * 2 + (w >> 1);
    int v5, ostr, od;
    if (g1) { v5 = (g & 1) * 262144 + (ks >> 3) * 16384 + (ks & 7) * 1024 + (2 * (w & 1) + (g >> 1)) * 256 + n * 16; ostr = 19; od = 0; }
    else if (kind == 2) { v5 = 32768 + w * 2048 + lane * 16; ostr = 16; od = 1024; }
    else { v5 = kind * 16384 + w * 1024 + lane * 16; ostr = 16; od = 4096; }
    const int v6 = (w == 0 && lane == 0) ? WS_CT + (g1 ? CT_G1 + (b - 96) * 32 : CT_P1 + h * 32 + kind * 4) * 4 : OOB;
#define P1_IN "{v0}"(v0), "{v1}"(v1), "{v2}"(v2), "{v3}"(v3), "{v4}"(v4), "{s[36:39]}"(rs), "{s[40:43]}"(rw), "{s44}"(16 * stride), "{s46}"(s0), "{s47}"(s1), "{s48}"(w), "{s49}"(ostr), "{s50}"(od), "{s45}"(WS_XF + fp), "{s64}"(g1 ? WS_HF + fp : tkbase(p, h, fp)), "{s51}"(rfl(kind == 1 ? 0x3e0293ee : 0x3f800000))
    int a5 = v5, a6 = v6; u32 tc, tr;
    if (g1) asm volatile(ASM_P1_G1 : "+{v5}"(a5), "+{v6}"(a6), "={s70}"(tc), "={s72}"(tr) : P1_IN : CLOB_P1);
    else if (kind == 2) asm volatile(ASM_P1_V : "+{v5}"(a5), "+{v6}"(a6), "={s70}"(tc), "={s72}"(tr) : P1_IN : CLOB_P1);
    else asm volatile(ASM_P1_QK : "+{v5}"(a5), "+{v6}"(a6), "={s70}"(tc), "={s72}"(tr) : P1_IN : CLOB_P1);
}

__device__ __forceinline__ int mlane(int n, int g) { return (g >> 1) * 512 + (n >> 3) * 256 + (g & 1) * 128 + (n & 7) * 16; }
__device__ __forceinline__ int mq(int j) { return ((j & 1) ? 0x4800 : 0) ^ ((j & 2) ? 0x800 : 0); }
__device__ __forceinline__ int hword(int i, int cls) { return WS_M + (cls ^ 0x1000 ^ ((i & 32) ? 0x4800 : 0)) + ((i >> 4) & 1) * 32768 + (i & 15) * 128; }
__device__ __forceinline__ int oblk(const Params& p, int rg, int q, int cls) { return rg * 32768 + q * 8192; }
__device__ __forceinline__ int mpiece(int l, int cls) { return WS_M + (cls ^ mq(l >> 4)) + ((l >> 3) & 1) * 32768 + (l & 7) * 256; }
__device__ __forceinline__ void qk_role(const Params& p, int h, int par, int rot) {
    const int lane = __builtin_amdgcn_mbcnt_hi(~0u, __builtin_amdgcn_mbcnt_lo(~0u, 0u)), w = rfl(threadIdx.x >> 6), n = lane & 15, g = lane >> 4;
    const v4u rs = mksrd(p.ws, WS_END), rw = mksrd(V2 ? colsrc(p, 0, 2 * h + (w >> 1)) : colsrc(p, w >> 1, 2 * h + (w & 1)), 1023 * 8192 + 128);
    const int l16 = lane * 16, v3 = l16 + (V2 ? 0 : 112 * 1024), lex = V2 ? 40960 : 152 * 1024;
    int v1 = l16 + w * 4096, v2 = v1 + 65536;
    const int v4 = WS_M + (w >> 1) * 32768 + (w & 1) * 1024 + mlane(n, g);
    const int v5 = lex + (32 * w + n) * 32 + (g ^ (n >> 2)) * 8;
    const int v6 = lex + (4 * g + (n >> 2)) * 32 + ((n & 3) ^ g) * 8;
    const int v15 = w == 0 ? min(l16, 64) : OOB;
    const int v7 = (OHACK ? (g & 1) : (n & 2)) == 0 ? WS_O + amap(oblk(p, n >> 3, h >> 3, par) + (h & 7) * 1024 + w * 256 + (g >> 1) * 128 + ((n >> 2) & 1) * 64 + (OHACK ? (n & 2) * 16 : (g & 1) * 32) + (n & 1) * 16, par) : OOB;
    const int v8 = WS_V + amap(h * 2048 + w * 512 + lane * 4, par);
    const int v9 = (w == 0 && lane < 32) ? hword(lane, par) : OOB;
    const int v10 = (w == 0 && lane == 0) ? 0 : OOB;
    const int v11 = 49152 + (w & 1) * 4096 + w * 1024 + l16;
    const int v12 = (n & 2) ? 0xEEEE : 0x4444;
    const int v14 = 16 * (w & 1) + n - 4 * g;
    const int r8 = lane >> 3;
    int v32 = r8 * 8192 + ((lane & 7) ^ (((r8 >> 1) & 1) << 1)) * 16, v33 = (8 + r8) * 8192 + ((lane & 7) ^ ((((r8 >> 1) & 1) | 2) << 1)) * 16;
    const int swz = (n >> 3) | ((g & 1) << 1), rowo = (w >> 1) * 4096 + (8 * g + (n >> 2)) * 128 + (n & 3) * 8;
    const int v34 = rowo + ((2 * (w & 1)) ^ swz) * 32, v35 = rowo + ((2 * (w & 1) + 1) ^ swz) * 32;
    if (V2) { v1 = (w & 1) ? v33 : v32; v2 = v34 + (w >> 1) * 4096; v32 = v35 + (w >> 1) * 4096; }
    asm volatile(ASM_QK :: "{v0}"(mpiece(lane, par)), "{v1}"(v1), "{v[2:3]}"(v2u{(u32)v2, (u32)v3}), "{v[4:7]}"(v4u{(u32)v4, (u32)v5, (u32)v6, (u32)v7}), "{v8}"(v8), "{v9}"(v9), "{v10}"(v10), "{v11}"(v11), "{v12}"(v12),
        "{v14}"(v14), "{v15}"(v15), "{v[32:35]}"(v4u{(u32)v32, (u32)v33, (u32)v34, (u32)v35}), "{s[36:39]}"(rs), "{s[40:43]}"(rw), "{s[44:45]}"(v2u{(u32)TUNEV(0, C_MO), (u32)TUNEV(1, C_MM)}), "{s46}"(TUNEV(2, C_X)), "{s17}"(TUNEV(3, C_Y)), "{s[12:15]}"(v4u{(u32)TUNEV(7, C_D0), (u32)C_Y0, (u32)TUNEV(4, C_DS), (u32)rot << 21}), "{s10}"(rfl(par ^ mq(rot))), "{s47}"(WS_DBG + 131072 + (h * 4 + w) * 2048), "{s48}"(w),
        "{s63}"(rfl(tkbase(p, h, (par & 8192) ^ FARP))), "{s80}"(WS_CT + (CT_P1 + h * 32) * 4), "{s87}"(WS_CT + (CT_AT + h * 32) * 4), "{s94}"(rfl(hword(h, par))) : "v255", "a255", "s99", "memory");
    __builtin_unreachable();
}
__device__ __forceinline__ void vo_role(const Params& p, int c, int par) {
    const int lane = __builtin_amdgcn_mbcnt_hi(~0u, __builtin_amdgcn_mbcnt_lo(~0u, 0u)), w = rfl(threadIdx.x >> 6), n = lane & 15, g = lane >> 4;
    const int rg = RGX ? c >> 5 : (c >> 4) & 1, cs = RGX ? c & 31 : (c & 15) + 16 * (c >> 5);
    const v4u rs = mksrd(p.ws, WS_END), rw = mksrd(colsrc(p, V2 ? 1 : 2, c) + (size_t)(256 * w) * 4096, 255 * 8192 + 128), rv = mksrd(colsrc(p, 2, c) + (size_t)(256 * w) * 4096, 255 * 8192 + 128), ro = mksrd(colsrc(p, 3, cs >> 1) + (size_t)(1024 * w) * 1024 + (cs & 1) * 32, 1023 * 2048 + 64), rm = mksrd(p.mem0, 16384);
    const int v0 = lane * 16, v1 = (lane >> 3) * 8192 + (lane & 7) * 16, v2 = (lane >> 2) * 2048 + (lane & 3) * 16;
    const int v3 = (8 * g + (n >> 2)) * 128 + w * 32 + (n & 3) * 8;
    const int v4 = w * (V2 ? 32768 : 40960) + (16 * (g & 1) + 8 * (g >> 1) + 4 * (n >> 3) + ((n >> 2) & 1)) * 64 + (n & 3) * 8;
    const int v5 = WS_M + (w >> 1) * 32768 + (w & 1) * 1024 + mlane(n, g);
    const int v6 = WS_O + amap(oblk(p, rg, w, par) + (2 * (n >> 3) + (g >> 1)) * 256 + (g & 1) * 128 + (n & 7) * 16, par);
    const int v7 = WS_V + amap(c * 1024 + w * 256 + lane * 4, par);
    const int v8 = (w == 0 && (lane & 3) == 0) ? WS_M + (par ^ mq(cs >> 3)) + ((cs >> 2) & 1) * 32768 + ((cs & 3) * 2 + rg) * 256 + lane * 4 : OOB;
    const int v9 = (w == 0 && lane < 32) ? hword(lane, par) : OOB;
    const int v12 = (n & 2) ? 0xEEEE : 0x4444;
    const int v13 = (4 * rg + 2 * ((lane >> 4) & 1) + ((lane >> 2) & 1)) * 2048 + (32 * cs + 16 * (lane >> 5) + 2 * ((lane >> 3) & 1)) * 2;
    const int v15 = (V2 ? 131072 : 0) + 16384 + (g * 16 + ((n >> 1) & 1) * 8 + (n >> 2)) * 32 + w * 8 + (n & 1) * 4;
    asm volatile(ASM_VO :: "{v10}"(mpiece(lane, par)), "{v0}"(v0), "{v1}"(v1), "{v2}"(v2), "{v3}"(v3), "{v4}"(v4), "{v5}"(v5), "{v6}"(v6), "{v7}"(v7), "{v8}"(v8), "{v9}"(v9), "{v12}"(v12),
        "{v13}"(v13), "{v15}"(v15), "{v28}"((w == 0 && lane == 0) ? WS_CT + (CT_P1 + (c >> 1) * 32 + 12 + (c & 1) * 4) * 4 : OOB), "{s[36:39]}"(rs), "{s[40:43]}"(rw), "{s[64:67]}"(ro), "{s[68:71]}"(rm), "{s[92:95]}"(rv), "{s[44:45]}"(v2u{(u32)TUNEV(0, C_MO), (u32)TUNEV(1, C_MM)}), "{s46}"(TUNEV(12, C_VD)), "{s17}"(TUNEV(3, C_Y)),
        "{s63}"(par), "{s74}"(cs >= 16 ? TUNEV(15, C_YL) : 0), "{s47}"(WS_DBG + 131072 + ((32 + c) * 4 + w) * 2048), "{s48}"(w) : "v255", "a255", "s99", "memory");
    __builtin_unreachable();
}
__device__ __forceinline__ void copy_wd(const Params& p, int u, int fp) {
    const int lane = threadIdx.x & 63;
    const rsrc_t rs = __builtin_amdgcn_make_buffer_rsrc(p.ws + WS_WD, 0, 16 * MB, 0x00020000);
#pragma unroll 1
    for (int k = 0; k < 14; ++k) {
        const int row = (k * 32 + u) * 8;
        v4u t[16];
#pragma unroll
        for (int i = 0; i < 16; ++i) t[i] = __builtin_nontemporal_load((const v4u*)(p.wd + (size_t)row * 1024 + i * 512 + lane * 8));
#pragma unroll
        for (int i = 0; i < 16; ++i) st128s(t[i], rs, farx(row * 4096 + fp) + (i >> 3) * 16384 + ((i >> 1) & 3) * 2048 + ((((i & 1) * 8 + (lane >> 3) + (i >> 1)) & 15) * 128) + (lane & 7) * 16);
    }
    WAIT_V0();
    u32 add = 1u;
    if (u == 0) {
        GAS u32* ct = (GAS u32*)(p.ws + WS_CT);
        const int a = __hip_atomic_load(ct + CT_P1 + 8 * 32 + 4, RLXA), b = __hip_atomic_load(ct + CT_G1 + 12 * 32, RLXA) + 1, c = __hip_atomic_load(ct + CT_G1 + 44 * 32, RLXA) + 1;
        const int r2 = TUNEV(10, S64_R0) / 2, k = TUNEV(6, S64_HEAD) >> 8;
        const int na = min(max((r2 * 8 + (2 * a - b - c) * k + 12) / 24, 8), 30), nb = min(max((r2 * 8 + (2 * b - a - c) * k + 12) / 24, 8), r2 - na - 4);
        add += (na << 8) | (nb << 16);
    }
    if (lane == 0) __hip_atomic_fetch_add((GAS u32*)(p.ws + WS_CT) + CT_WD, add, RLXA);
}
__device__ __forceinline__ void s64_role(const Params& p, int kind, int cb, int r, int id, int w, int fp) {
    const int lane = __builtin_amdgcn_mbcnt_hi(~0u, __builtin_amdgcn_mbcnt_lo(~0u, 0u)), n = lane & 15, g = lane >> 4;

    u32 wd = 0;
    if (kind) {
        GAS u32* ct = (GAS u32*)(p.ws + WS_CT);
        for (u32 spins = 0; ((wd = rfl(__hip_atomic_load(ct + CT_WD, RLXA))) & 255u) < 32u; ++spins) {
            if (spins > (1u << 18)) { __hip_atomic_store(ct + CT_TMO, 1u, RLXA); break; }
            __builtin_amdgcn_s_sleep(8);
        }
        CFENCE();
    }
    const v4u rs = mksrd(p.ws, WS_END), rw = mksrd(kind ? p.ws + WS_WD : (const u8*)(colsrc(p, 3, cb) + (size_t)(1024 * w) * 1024), kind ? 16 * MB : 1023 * 2048 + 128), rx = mksrd(p.x, 2048 * 2048), ro = mksrd(p.out, 2048 * 2048);
    const int v2 = lane * 16 + w * 32768, v0 = (kind ? (lane >> 5) * 16384 + ((lane >> 3) & 3) * 2048 + ((cb + (lane >> 3)) & 15) * 128 : (lane >> 3) * 2048) + (lane & 7) * 16;
    const int v1 = w * 32768 + ((kind ? 8 : 4) * g + (n >> 2)) * 128 + (n & 3) * 8;
    const int v3 = (kind ? 0 : 49152) + lane * 16;
    const int v4 = WS_CT + (kind ? CT_G1 + (14 * w + min(n, 13)) * 32 : CT_AT + (8 * w + (n & 7)) * 32) * 4;
    const int v5 = (4 * w + g) * 2048 + cb * 128 + n * 8, v6 = (cb >> 2) * 16384 + (cb & 3) * 2048 + w * 512 + lane * 8;
    const int v7 = 131072 + w * 4096 + g * 256 + (n ^ g) * 16, v8 = 131072 + n * 256 + ((4 * w + g) ^ n) * 16;
    static_assert(NWO == 4, "three pairs of w_down replicas");
    const int r0 = TUNEV(10, S64_R0), nr = kind ? 6 : 4;
    int ha, nt, first, rt, n1, lim;
    if (kind) {
        const int na = (wd >> 8) & 255, nb = (wd >> 16) & 255, lo = r < 2 ? 0 : (r < 4 ? na : na + nb), hi = r < 2 ? na : (r < 4 ? na + nb : r0 / 2);
        ha = r0; first = 2 * lo + (r & 1); lim = 2 * hi; rt = r0 + r; n1 = (128 - rt + 5) / 6; nt = n1 + hi - lo;
    } else {
        ha = TUNEV(6, S64_HEAD) & 255;
        const int hd = r < 2 ? ha / 2 : 0;
        first = rt = r < 2 ? r : ha + r; lim = r0; n1 = hd + (r0 - ha - r + 3) / 4; nt = hd + (128 - ha - r + 3) / 4;
    }
#define S64_IO "{s97}"(896 * 4096 * w + fp), "{s78}"(WS_GT + fp), "{v7}"(v7), "{v8}"(v8), "{s44}"(rt), "{v0}"(v0), "{v1}"(v1), "{v2}"(v2), "{v3}"(v3), "{v4}"(v4), "{v5}"(v5), "{v6}"(v6), "{s[36:39]}"(rs), "{s[40:43]}"(rw), "{s[64:67]}"(rx), "{s[68:71]}"(ro), \
        "{s45}"(2), "{s47}"(nr), "{s50}"(ha), "{s46}"(nt), "{s48}"(w), "{s49}"(WS_CT + CT_S * 4 + (cb * 4 + w) * 128), "{s94}"(first), "{s95}"(n1), "{s96}"(lim), "{s51}"(w ? OOB : WS_DBG + id * 128), "{s99}"(kind == 0 ? TUNEV(9, C_PW3) : 0), "{s85}"(kind ? WS_HF + fp : tkbase(p, 8 * w, fp))
    if (kind) asm volatile(ASM_S64_G2 :: S64_IO : "v255", "a255", "memory");
    else asm volatile(ASM_S64_WO :: S64_IO : "v255", "a255", "memory");
    __builtin_unreachable();
}
#define MAIN_BODY \
int gx = (id >> 5) - 4, gr = id & 31; \
    if (id < 128) { \
        const int x = id >> 5, t = id & 31; \
        const int nq = x < 2 ? NQ0 : 32 - 2 * NQ0; \
        if (x < 3) { if (t < nq) qk_role(p, NQ0 * x + t, par, x & TUNEV(14, 1)); else vo_role(p, (32 - NQ0) * x + t - nq, par); } \
        gx = -1; \
        id = 96 + gr; \
        STAMP(0); \
    } \
    const int fp = (par & 8192) ^ FARP; \
    const int u = PMAP == 2 ? gx * 32 + gr - (gx ? 8 : 0) : gx * 30 + gr, sp = PMAP == 2 ? (gx == 0 ? gr - 24 : -1) : (gx >= 0 && gr >= 30 ? gx * 2 + gr - 30 : -1); \
    const int blk = gx < 0 ? 64 + gr : (u < 64 ? u : u + 32); \
    if (sp < 0) { \
        for (int i = blk >= 96 ? (TUNEV(5, C_GD) & 255) - ((blk & 7) == slowpos(p.wg) ? 6 : 0) : TUNEV(5, C_GD) >> 8; i > 0; --i) __builtin_amdgcn_s_sleep(32);     \
        p1_role(p, blk, 0, 64, id, wv_, fp); \
    } else { ln_role(p, sp, wv_, fp); copy_wd(p, sp * 4 + wv_, fp); } \
    STAMP(2); \
    { const int rep = 2 * gx + 2 + (gr >> 4); s64_role(p, rep < NWO ? 0 : 1, gr & 15, rep < NWO ? rep : rep - NWO, id, wv_, fp); }
constexpr int LDS_BYTES = 160 * 1024;
__device__ __forceinline__ int take_role(const Params& p, LAS u32* L) {
    if (threadIdx.x == 0) {
        GAS u32* tk = (GAS u32*)(p.ws + WS_CT) + CT_TICK;
        const u32 x = SCRAMBLE == 1 ? (blockIdx.x * 37u >> 3) & 7u : (SCRAMBLE == 2 ? (xcc_id() & 3u) : xcc_id() & 7u);
        const u32 t = __hip_atomic_fetch_add(tk + 64 * x, 1u, RLXA);
        u32 id = 32 * x + t;
        if (t >= 32u) {
            u32 o = __hip_atomic_fetch_add(tk + 64 * 8, 1u, RLXA);
            for (u32 spins = 0; spins < (1u << 20); ++spins) {
                u32 tot = 0;
                for (int k = 0; k < 8; ++k) tot += __hip_atomic_load(tk + 64 * k, RLXA);
                if (tot >= 256u) break;
                __builtin_amdgcn_s_sleep(8);
            }
            id = 255u;
            for (int k = 0; k < 8; ++k) {
                const u32 ck = __hip_atomic_load(tk + 64 * k, RLXA);
                const u32 fr = ck < 32u ? 32u - ck : 0u;
                if (o < fr) { id = 32 * k + ck + o; break; }
                o -= fr;
            }
        }
        *L = id;
    }
    BAR();
    const int id = rfl(*L);
    BAR();
    return id;
}
#define STAMP(i) do {} while (0)
__global__ void __launch_bounds__(256, 1) k_main(Params p) {
    extern __shared__ __attribute__((aligned(16))) u8 smem[];
    int par = rfl(*(const GAS u32*)(p.ws + WS_CT + CT_PAR * 4)) & 1;
    par = (par << 13) | ((TUNEV(13, 0) & 3) << 11) | ((TUNEV(13, 0) & 4) << 12);
    int id = take_role(p, (LAS u32*)smem);
    const int wv_ = rfl(threadIdx.x >> 6);
    STAMP(0);
    MAIN_BODY
    STAMP(7);
}
__global__ void __launch_bounds__(256) k_zero(Params p) {
    __shared__ u32 vote[4];
    u8* ws = p.ws;
    v4u z = {0u, 0u, 0u, 0u};
    if (WARM && blockIdx.x != 0) {
        const int i = (blockIdx.x - 1) * 32 + (threadIdx.x >> 3), m2 = min(i & 7, 2);
        const rsrc_t rc = __builtin_amdgcn_make_buffer_rsrc(ws + WS_WC, 0, 16 * MB, 0x00020000);
        v4u t[7];
#pragma unroll
        for (int m = 0; m < 4; ++m) t[m] = __builtin_nontemporal_load((const v4u*)(wmat(p, m) + (size_t)i * 512 + slowpos(wmat(p, m)) * 64 + (threadIdx.x & 7) * 8));
        if ((i & 7) < 3) t[4] = __builtin_nontemporal_load((const v4u*)(wmat(p, m2) + (size_t)(i >> 3) * 4096 + slowpos2(wmat(p, m2)) * 64 + (threadIdx.x & 7) * 8));
        if (WARM > 1 && i < 7168) {
            t[5] = __builtin_nontemporal_load((const v4u*)(p.wg + (size_t)i * 512 + slowpos(p.wg) * 64 + (threadIdx.x & 7) * 8));
            t[6] = __builtin_nontemporal_load((const v4u*)(p.wu + (size_t)i * 512 + slowpos(p.wu) * 64 + (threadIdx.x & 7) * 8));
        }
#pragma unroll
        for (int m = 0; m < 4; ++m) __builtin_amdgcn_raw_buffer_store_b128(t[m], rc, i * 1024 + (m == 3 ? 4 : m) * 128 + (threadIdx.x & 7) * 16, 0, ZAUX);
        if ((i & 7) < 3) __builtin_amdgcn_raw_buffer_store_b128(t[4], rc, (i >> 3) * 8192 + (5 + m2) * 128 + (threadIdx.x & 7) * 16, 0, ZAUX);
        if (WARM > 1 && i < 7168) {
            __builtin_amdgcn_raw_buffer_store_b128(t[5], rc, WS_WG - WS_WC + i * 1024 + (threadIdx.x & 7) * 16, 0, ZAUX);
            __builtin_amdgcn_raw_buffer_store_b128(t[6], rc, WS_WG - WS_WC + i * 1024 + 128 + (threadIdx.x & 7) * 16, 0, ZAUX);
        }
    }
    if (blockIdx.x == 0) {
        const rsrc_t rs = __builtin_amdgcn_make_buffer_rsrc(ws, 0, 2 * MB, 0x00020000);
        const int lane = threadIdx.x & 63, w = threadIdx.x >> 6, base = 2 * MB - 32768 + (w & 1) * 16384 + (w >> 1) * 2048 + (lane & 31) * 4;
        st32s(0u, rs, base + 1024); st32s(0u, rs, base + 8192 + 1024);
        u32 a = 65535u, b = 65535u;
#pragma unroll 1
        for (int k = 0; k < ZR; ++k) {
            u64 t0, t1, t2;
            asm volatile("s_waitcnt vmcnt(0)\n\ts_memtime %0\n\ts_waitcnt lgkmcnt(0)" : "=s"(t0) :: "memory");
            st32s(0u, rs, base);
            asm volatile("s_waitcnt vmcnt(0)\n\ts_memtime %0\n\ts_waitcnt lgkmcnt(0)" : "=s"(t1) :: "memory");
            st32s(0u, rs, base + 8192);
            asm volatile("s_waitcnt vmcnt(0)\n\ts_memtime %0\n\ts_waitcnt lgkmcnt(0)" : "=s"(t2) :: "memory");
            a = min(a, (u32)(t1 - t0)); b = min(b, (u32)(t2 - t1));
            if (4 * max(a, b) > 5 * min(a, b) && min(a, b) < 600u && max(a, b) < 1000u) break;
        }
        vote[w] = (a << 16) | b;
        __syncthreads();
        if (threadIdx.x == 0) {
            for (int k = 0; k < 4; ++k) { a = min(a, vote[k] >> 16); b = min(b, vote[k] & 65535u); }
            z.x = (b < a ? 1u : 0u) ^ (xcc_id() >= 4 ? 1u : 0u);
        }
    }
    if (blockIdx.x < 80) ((v4u*)(ws + (blockIdx.x < 16 ? 0 : WS_M - 65536)))[blockIdx.x * 256 + threadIdx.x] = z;
}

extern "C" void kernel_launch(void* const* d_in, const int* in_sizes, int n_in, void* d_out, int out_size, void* d_ws, size_t ws_size, hipStream_t stream) {
    Params p;
    p.x = (const u16*)d_in[0]; p.mem0 = (const u16*)d_in[1]; p.wq = (const u16*)d_in[2]; p.wk = (const u16*)d_in[3]; p.wv = (const u16*)d_in[4]; p.wo = (const u16*)d_in[5];
    p.wg = (const u16*)d_in[8]; p.wu = (const u16*)d_in[9]; p.wd = (const u16*)d_in[10];
    p.out = (u16*)d_out;
    p.ws = (u8*)(((uintptr_t)d_ws + (2u * MB - 1)) & ~(uintptr_t)(2u * MB - 1));
    (void)hipFuncSetAttribute((const void*)k_main, hipFuncAttributeMaxDynamicSharedMemorySize, LDS_BYTES);
    hipLaunchKernelGGL(k_zero, dim3(257), dim3(256), 0, stream, p);
    hipLaunchKernelGGL(k_main, dim3(256), dim3(256), LDS_BYTES, stream, p);
}
```

```cpp
#define V2 0
#pragma clang diagnostic ignored "-Wint-to-pointer-cast"
#pragma clang diagnostic ignored "-Winline-asm"
#include <hip/hip_runtime.h>
#include <hip/hip_bf16.h>
#include <stdint.h>

#define LAS __attribute__((address_space(3)))
#define GAS __attribute__((address_space(1)))
typedef unsigned u32;
typedef unsigned char u8;
typedef unsigned short u16;
typedef unsigned long long u64;
typedef u32 v2u __attribute__((ext_vector_type(2)));
typedef u32 v4u __attribute__((ext_vector_type(4)));
typedef float f32x4 __attribute__((ext_vector_type(4)));
typedef __bf16 bf16x2 __attribute__((ext_vector_type(2)));
typedef __amdgpu_buffer_rsrc_t rsrc_t;

constexpr int NSEG = 64, DM = 1024, NH = 32, FF = 3584;
constexpr int MB = 1 << 20;
constexpr int WS_CT = 0;
constexpr int WS_M = 262144;
constexpr int WS_O = 524288, WS_V = 1048576;
constexpr int WS_DBG = 4 * MB;
constexpr int WS_XF = 8 * MB;
constexpr int WS_HF = 32 * MB;
constexpr int WS_GT = 16 * MB;
constexpr int WS_TK = 64 * MB;
constexpr int WS_END = 192 * MB;
__device__ __forceinline__ int farx(int off) { return off ^ ((off >> 8) & 0x2000); }
constexpr int CT_PAR = 0, CT_TMO = 64, CT_TICK = 256;
constexpr int CT_LN = 1024;
constexpr int CT_PLAN = 1600;
constexpr int CT_WD = 1536;
constexpr int CT_P1 = 2048;
constexpr int CT_G1 = 4096;
constexpr int CT_AT = 3072;
constexpr int CT_S = 8192;
#define NQ0 16
#define RGX 0
#define SCRAMBLE 0
#define CODEPF 0
#define ZAUX 16
#define ZR 8
#define WARM 1
#define NWO 4
#define PMAP 1
#define S64_HEAD (18 + 10 * 256)
#define S64_R0 116
constexpr int OOB = 0x7ffffff0;
constexpr int AUX_SC1 = 16;

struct Params {
    const u16 *x, *mem0, *wq, *wk, *wv, *wo, *wg, *wu, *wd;
    u16* out;
    u8* ws;
};
__device__ __forceinline__ u32 pack2(float a, float b) { bf16x2 v = {(__bf16)a, (__bf16)b}; return __builtin_bit_cast(u32, v); }
__device__ __forceinline__ float bflo(u32 d) { return __builtin_bit_cast(float, d << 16); }
__device__ __forceinline__ float bfhi(u32 d) { return __builtin_bit_cast(float, d & 0xFFFF0000u); }
__device__ __forceinline__ float rbf(float a) { return (float)(__bf16)a; }
__device__ __forceinline__ void st128s(v4u v, rsrc_t rs, int voff) { __builtin_amdgcn_raw_buffer_store_b128(v, rs, voff, 0, AUX_SC1); }
__device__ __forceinline__ void st32s(u32 v, rsrc_t rs, int voff) { __builtin_amdgcn_raw_buffer_store_b32(v, rs, voff, 0, AUX_SC1); }
__device__ __forceinline__ u32 ld32s(rsrc_t rs, int voff) { return __builtin_amdgcn_raw_buffer_load_b32(rs, voff, 0, AUX_SC1); }
__device__ __forceinline__ u32 xcc_id() { return (u32)__builtin_amdgcn_s_getreg(20 | (3 << 11)) & 0xfu; }
__device__ __forceinline__ int rfl(int x) { return __builtin_amdgcn_readfirstlane(x); }
__device__ __forceinline__ v4u mksrd(const void* p, u32 bytes) {
    const u64 a = (u64)p;
    return v4u{(u32)rfl((u32)a), (u32)rfl((u32)(a >> 32) & 0xFFFFu), (u32)rfl(bytes), 0x00020000u};
}
#define CFENCE() asm volatile("" ::: "memory")
#define WAIT_V0() asm volatile("s_waitcnt vmcnt(0)" ::: "memory")
#define BAR() do { asm volatile("s_waitcnt lgkmcnt(0)" ::: "memory"); __builtin_amdgcn_s_barrier(); asm volatile("" ::: "memory"); } while (0)
#define RLXA __ATOMIC_RELAXED, __HIP_MEMORY_SCOPE_AGENT
__device__ __forceinline__ int amap(int off, int cls) { return ((off >> 13) << 14) + (off & 8191) + (cls & 8192); }

constexpr int WS_WC = 208 * MB;
constexpr int WS_WG = 216 * MB;
constexpr int WS_WD = 192 * MB;
__device__ __forceinline__ const u16* wmat(const Params& p, int m) { return m == 0 ? p.wq : (m == 1 ? p.wk : (m == 2 ? p.wv : p.wo)); }
__device__ __forceinline__ int slowpos(const void* b) { return (3 - (int)((u64)b >> 7)) & 7; }
__device__ __forceinline__ int slowpos2(const void* b) { return (63 - (int)((u64)b >> 7)) & 63; }
__device__ __forceinline__ const u16* colsrc(const Params& p, int m, int j) {
    const u16* b = wmat(p, m);
    if (WARM && m < 3 && j == slowpos2(b)) return (const u16*)(p.ws + WS_WC) + (5 + m) * 64;
    return (WARM && (j & 7) == slowpos(b)) ? (const u16*)(p.ws + WS_WC) + (j >> 3) * 512 + (m == 3 ? 4 : m) * 64 : b + j * 64;
}
__device__ __forceinline__ const u16* colsrc2(const Params& p, int m, int j) {
    const u16* b = m ? p.wu : p.wg;
    return (WARM > 1 && (j & 7) == slowpos(b)) ? (const u16*)(p.ws + WS_WG) + (j >> 3) * 512 + m * 64 : b + j * 64;
}
#define ASM_P1_QK \
"s_nop 7\n" \
"s_nop 7\n" \
"s_lshr_b32 s52, s48, 1\n" \
"s_lshl_b32 s52, s52, 13\n" \
"s_and_b32 s53, s48, 1\n" \
"s_lshl_b32 s53, s53, 10\n" \
"s_add_u32 s52, s52, s53\n" \
"s_mov_b32 s54, 0\n" \
"s_add_u32 m0, s52, 0\n" \
"s_nop 0\n" \
"buffer_load_dwordx4 v1, s[40:43], s54 offen nt lds\n" \
"s_add_u32 s54, s54, s44\n" \
"s_add_u32 m0, s52, 2048\n" \
"s_nop 0\n" \
"buffer_load_dwordx4 v1, s[40:43], s54 offen nt lds\n" \
"s_add_u32 s54, s54, s44\n" \
"s_add_u32 m0, s52, 4096\n" \
"s_nop 0\n" \
"buffer_load_dwordx4 v1, s[40:43], s54 offen nt lds\n" \
"s_add_u32 s54, s54, s44\n" \
"s_add_u32 m0, s52, 6144\n" \
"s_nop 0\n" \
"buffer_load_dwordx4 v1, s[40:43], s54 offen nt lds\n" \
"s_add_u32 s54, s54, s44\n" \
"s_add_u32 m0, s52, 16384\n" \
"s_nop 0\n" \
"buffer_load_dwordx4 v1, s[40:43], s54 offen nt lds\n" \
"s_add_u32 s54, s54, s44\n" \
"s_add_u32 m0, s52, 18432\n" \
"s_nop 0\n" \
"buffer_load_dwordx4 v1, s[40:43], s54 offen nt lds\n" \
"s_add_u32 s54, s54, s44\n" \
"s_add_u32 m0, s52, 20480\n" \
"s_nop 0\n" \
"buffer_load_dwordx4 v1, s[40:43], s54 offen nt lds\n" \
"s_add_u32 s54, s54, s44\n" \
"s_add_u32 m0, s52, 22528\n" \
"s_nop 0\n" \
"buffer_load_dwordx4 v1, s[40:43], s54 offen nt lds\n" \
"s_add_u32 s54, s54, s44\n" \
"s_add_u32 m0, s52, 32768\n" \
"s_nop 0\n" \
"buffer_load_dwordx4 v1, s[40:43], s54 offen nt lds\n" \
"s_add_u32 s54, s54, s44\n" \
"s_add_u32 m0, s52, 34816\n" \
"s_nop 0\n" \
"buffer_load_dwordx4 v1, s[40:43], s54 offen nt lds\n" \
"s_add_u32 s54, s54, s44\n" \
"s_add_u32 m0, s52, 36864\n" \
"s_nop 0\n" \
"buffer_load_dwordx4 v1, s[40:43], s54 offen nt lds\n" \
"s_add_u32 s54, s54, s44\n" \
"s_add_u32 m0, s52, 38912\n" \
"s_nop 0\n" \
"buffer_load_dwordx4 v1, s[40:43], s54 offen nt lds\n" \
"s_add_u32 s54, s54, s44\n" \
"s_add_u32 m0, s52, 49152\n" \
"s_nop 0\n" \
"buffer_load_dwordx4 v1, s[40:43], s54 offen nt lds\n" \
"s_add_u32 s54, s54, s44\n" \
"s_add_u32 m0, s52, 51200\n" \
"s_nop 0\n" \
"buffer_load_dwordx4 v1, s[40:43], s54 offen nt lds\n" \
"s_add_u32 s54, s54, s44\n" \
"s_add_u32 m0, s52, 53248\n" \
"s_nop 0\n" \
"buffer_load_dwordx4 v1, s[40:43], s54 offen nt lds\n" \
"s_add_u32 s54, s54, s44\n" \
"s_add_u32 m0, s52, 55296\n" \
"s_nop 0\n" \
"buffer_load_dwordx4 v1, s[40:43], s54 offen nt lds\n" \
"s_add_u32 s54, s54, s44\n" \
"s_add_u32 m0, s52, 65536\n" \
"s_nop 0\n" \
"buffer_load_dwordx4 v1, s[40:43], s54 offen nt lds\n" \
"s_add_u32 s54, s54, s44\n" \
"s_add_u32 m0, s52, 67584\n" \
"s_nop 0\n" \
"buffer_load_dwordx4 v1, s[40:43], s54 offen nt lds\n" \
"s_add_u32 s54, s54, s44\n" \
"s_add_u32 m0, s52, 69632\n" \
"s_nop 0\n" \
"buffer_load_dwordx4 v1, s[40:43], s54 offen nt lds\n" \
"s_add_u32 s54, s54, s44\n" \
"s_add_u32 m0, s52, 71680\n" \
"s_nop 0\n" \
"buffer_load_dwordx4 v1, s[40:43], s54 offen nt lds\n" \
"s_add_u32 s54, s54, s44\n" \
"s_add_u32 m0, s52, 81920\n" \
"s_nop 0\n" \
"buffer_load_dwordx4 v1, s[40:43], s54 offen nt lds\n" \
"s_add_u32 s54, s54, s44\n" \
"s_add_u32 m0, s52, 83968\n" \
"s_nop 0\n" \
"buffer_load_dwordx4 v1, s[40:43], s54 offen nt lds\n" \
"s_add_u32 s54, s54, s44\n" \
"s_add_u32 m0, s52, 86016\n" \
"s_nop 0\n" \
"buffer_load_dwordx4 v1, s[40:43], s54 offen nt lds\n" \
"s_add_u32 s54, s54, s44\n" \
"s_add_u32 m0, s52, 88064\n" \
"s_nop 0\n" \
"buffer_load_dwordx4 v1, s[40:43], s54 offen nt lds\n" \
"s_add_u32 s54, s54, s44\n" \
"s_add_u32 m0, s52, 98304\n" \
"s_nop 0\n" \
"buffer_load_dwordx4 v1, s[40:43], s54 offen nt lds\n" \
"s_add_u32 s54, s54, s44\n" \
"s_add_u32 m0, s52, 100352\n" \
"s_nop 0\n" \
"buffer_load_dwordx4 v1, s[40:43], s54 offen nt lds\n" \
"s_add_u32 s54, s54, s44\n" \
"s_add_u32 m0, s52, 102400\n" \
"s_nop 0\n" \
"buffer_load_dwordx4 v1, s[40:43], s54 offen nt lds\n" \
"s_add_u32 s54, s54, s44\n" \
"s_add_u32 m0, s52, 104448\n" \
"s_nop 0\n" \
"buffer_load_dwordx4 v1, s[40:43], s54 offen nt lds\n" \
"s_add_u32 s54, s54, s44\n" \
"s_add_u32 m0, s52, 114688\n" \
"s_nop 0\n" \
"buffer_load_dwordx4 v1, s[40:43], s54 offen nt lds\n" \
"s_add_u32 s54, s54, s44\n" \
"s_add_u32 m0, s52, 116736\n" \
"s_nop 0\n" \
"buffer_load_dwordx4 v1, s[40:43], s54 offen nt lds\n" \
"s_add_u32 s54, s54, s44\n" \
"s_add_u32 m0, s52, 118784\n" \
"s_nop 0\n" \
"buffer_load_dwordx4 v1, s[40:43], s54 offen nt lds\n" \
"s_add_u32 s54, s54, s44\n" \
"s_add_u32 m0, s52, 120832\n" \
"s_nop 0\n" \
"buffer_load_dwordx4 v1, s[40:43], s54 offen nt lds\n" \
"s_add_u32 s54, s54, s44\n" \
"v_add_u32 v8, 0x10000, v2\n" \
"v_add_u32 v9, 0x10000, v3\n" \
"s_waitcnt vmcnt(28)\n" \
"s_barrier\n" \
"ds_read_b64_tr_b16 a[0:1], v2 offset:0\n" \
"ds_read_b64_tr_b16 a[2:3], v2 offset:512\n" \
"ds_read_b64_tr_b16 a[128:129], v3 offset:0\n" \
"ds_read_b64_tr_b16 a[130:131], v3 offset:512\n" \
"ds_read_b64_tr_b16 a[4:5], v2 offset:4096\n" \
"ds_read_b64_tr_b16 a[6:7], v2 offset:4608\n" \
"ds_read_b64_tr_b16 a[132:133], v3 offset:4096\n" \
"ds_read_b64_tr_b16 a[134:135], v3 offset:4608\n" \
"s_waitcnt lgkmcnt(0)\n" \
"s_barrier\n" \
"s_add_u32 m0, s52, 0\n" \
"s_nop 0\n" \
"buffer_load_dwordx4 v1, s[40:43], s54 offen nt lds\n" \
"s_add_u32 s54, s54, s44\n" \
"s_add_u32 m0, s52, 2048\n" \
"s_nop 0\n" \
"buffer_load_dwordx4 v1, s[40:43], s54 offen nt lds\n" \
"s_add_u32 s54, s54, s44\n" \
"s_add_u32 m0, s52, 4096\n" \
"s_nop 0\n" \
"buffer_load_dwordx4 v1, s[40:43], s54 offen nt lds\n" \
"s_add_u32 s54, s54, s44\n" \
"s_add_u32 m0, s52, 6144\n" \
"s_nop 0\n" \
"buffer_load_dwordx4 v1, s[40:43], s54 offen nt lds\n" \
"s_add_u32 s54, s54, s44\n" \
"s_waitcnt vmcnt(28)\n" \
"s_barrier\n" \
"ds_read_b64_tr_b16 a[8:9], v2 offset:16384\n" \
"ds_read_b64_tr_b16 a[10:11], v2 offset:16896\n" \
"ds_read_b64_tr_b16 a[136:137], v3 offset:16384\n" \
"ds_read_b64_tr_b16 a[138:139], v3 offset:16896\n" \
"ds_read_b64_tr_b16 a[12:13], v2 offset:20480\n" \
"ds_read_b64_tr_b16 a[14:15], v2 offset:20992\n" \
"ds_read_b64_tr_b16 a[140:141], v3 offset:20480\n" \
"ds_read_b64_tr_b16 a[142:143], v3 offset:20992\n" \
"s_waitcnt lgkmcnt(0)\n" \
"s_barrier\n" \
"s_add_u32 m0, s52, 16384\n" \
"s_nop 0\n" \
"buffer_load_dwordx4 v1, s[40:43], s54 offen nt lds\n" \
"s_add_u32 s54, s54, s44\n" \
"s_add_u32 m0, s52, 18432\n" \
"s_nop 0\n" \
"buffer_load_dwordx4 v1, s[40:43], s54 offen nt lds\n" \
"s_add_u32 s54, s54, s44\n" \
"s_add_u32 m0, s52, 20480\n" \
"s_nop 0\n" \
"buffer_load_dwordx4 v1, s[40:43], s54 offen nt lds\n" \
"s_add_u32 s54, s54, s44\n" \
"s_add_u32 m0, s52, 22528\n" \
"s_nop 0\n" \
"buffer_load_dwordx4 v1, s[40:43], s54 offen nt lds\n" \
"s_add_u32 s54, s54, s44\n" \
"s_waitcnt vmcnt(28)\n" \
"s_barrier\n" \
"ds_read_b64_tr_b16 a[16:17], v2 offset:32768\n" \
"ds_read_b64_tr_b16 a[18:19], v2 offset:33280\n" \
"ds_read_b64_tr_b16 a[144:145], v3 offset:32768\n" \
"ds_read_b64_tr_b16 a[146:147], v3 offset:33280\n" \
"ds_read_b64_tr_b16 a[20:21], v2 offset:36864\n" \
"ds_read_b64_tr_b16 a[22:23], v2 offset:37376\n" \
"ds_read_b64_tr_b16 a[148:149], v3 offset:36864\n" \
"ds_read_b64_tr_b16 a[150:151], v3 offset:37376\n" \
"s_waitcnt lgkmcnt(0)\n" \
"s_barrier\n" \
"s_add_u32 m0, s52, 32768\n" \
"s_nop 0\n" \
"buffer_load_dwordx4 v1, s[40:43], s54 offen nt lds\n" \
"s_add_u32 s54, s54, s44\n" \
"s_add_u32 m0, s52, 34816\n" \
"s_nop 0\n" \
"buffer_load_dwordx4 v1, s[40:43], s54 offen nt lds\n" \
"s_add_u32 s54, s54, s44\n" \
"s_add_u32 m0, s52, 36864\n" \
"s_nop 0\n" \
"buffer_load_dwordx4 v1, s[40:43], s54 offen nt lds\n" \
"s_add_u32 s54, s54, s44\n" \
"s_add_u32 m0, s52, 38912\n" \
"s_nop 0\n" \
"buffer_load_dwordx4 v1, s[40:43], s54 offen nt lds\n" \
"s_add_u32 s54, s54, s44\n" \
"s_waitcnt vmcnt(28)\n" \
"s_barrier\n" \
"ds_read_b64_tr_b16 a[24:25], v2 offset:49152\n" \
"ds_read_b64_tr_b16 a[26:27], v2 offset:49664\n" \
"ds_read_b64_tr_b16 a[152:153], v3 offset:49152\n" \
"ds_read_b64_tr_b16 a[154:155], v3 offset:49664\n" \
"ds_read_b64_tr_b16 a[28:29], v2 offset:53248\n" \
"ds_read_b64_tr_b16 a[30:31], v2 offset:53760\n" \
"ds_read_b64_tr_b16 a[156:157], v3 offset:53248\n" \
"ds_read_b64_tr_b16 a[158:159], v3 offset:53760\n" \
"s_waitcnt lgkmcnt(0)\n" \
"s_barrier\n" \
"s_add_u32 m0, s52, 49152\n" \
"s_nop 0\n" \
"buffer_load_dwordx4 v1, s[40:43], s54 offen nt lds\n" \
"s_add_u32 s54, s54, s44\n" \
"s_add_u32 m0, s52, 51200\n" \
"s_nop 0\n" \
"buffer_load_dwordx4 v1, s[40:43], s54 offen nt lds\n" \
"s_add_u32 s54, s54, s44\n" \
"s_add_u32 m0, s52, 53248\n" \
"s_nop 0\n" \
"buffer_load_dwordx4 v1, s[40:43], s54 offen nt lds\n" \
"s_add_u32 s54, s54, s44\n" \
"s_add_u32 m0, s52, 55296\n" \
"s_nop 0\n" \
"buffer_load_dwordx4 v1, s[40:43], s54 offen nt lds\n" \
"s_add_u32 s54, s54, s44\n" \
"s_waitcnt vmcnt(28)\n" \
"s_barrier\n" \
"ds_read_b64_tr_b16 a[32:33], v8 offset:0\n" \
"ds_read_b64_tr_b16 a[34:35], v8 offset:512\n" \
"ds_read_b64_tr_b16 a[160:161], v9 offset:0\n" \
"ds_read_b64_tr_b16 a[162:163], v9 offset:512\n" \
"ds_read_b64_tr_b16 a[36:37], v8 offset:4096\n" \
"ds_read_b64_tr_b16 a[38:39], v8 offset:4608\n" \
"ds_read_b64_tr_b16 a[164:165], v9 offset:4096\n" \
"ds_read_b64_tr_b16 a[166:167], v9 offset:4608\n" \
"s_waitcnt lgkmcnt(0)\n" \
"s_barrier\n" \
"s_add_u32 m0, s52, 65536\n" \
"s_nop 0\n" \
"buffer_load_dwordx4 v1, s[40:43], s54 offen nt lds\n" \
"s_add_u32 s54, s54, s44\n" \
"s_add_u32 m0, s52, 67584\n" \
"s_nop 0\n" \
"buffer_load_dwordx4 v1, s[40:43], s54 offen nt lds\n" \
"s_add_u32 s54, s54, s44\n" \
"s_add_u32 m0, s52, 69632\n" \
"s_nop 0\n" \
"buffer_load_dwordx4 v1, s[40:43], s54 offen nt lds\n" \
"s_add_u32 s54, s54, s44\n" \
"s_add_u32 m0, s52, 71680\n" \
"s_nop 0\n" \
"buffer_load_dwordx4 v1, s[40:43], s54 offen nt lds\n" \
"s_add_u32 s54, s54, s44\n" \
"s_waitcnt vmcnt(28)\n" \
"s_barrier\n" \
"ds_read_b64_tr_b16 a[40:41], v8 offset:16384\n" \
"ds_read_b64_tr_b16 a[42:43], v8 offset:16896\n" \
"ds_read_b64_tr_b16 a[168:169], v9 offset:16384\n" \
"ds_read_b64_tr_b16 a[170:171], v9 offset:16896\n" \
"ds_read_b64_tr_b16 a[44:45], v8 offset:20480\n" \
"ds_read_b64_tr_b16 a[46:47], v8 offset:20992\n" \
"ds_read_b64_tr_b16 a[172:173], v9 offset:20480\n" \
"ds_read_b64_tr_b16 a[174:175], v9 offset:20992\n" \
"s_waitcnt lgkmcnt(0)\n" \
"s_barrier\n" \
"s_add_u32 m0, s52, 81920\n" \
"s_nop 0\n" \
"buffer_load_dwordx4 v1, s[40:43], s54 offen nt lds\n" \
"s_add_u32 s54, s54, s44\n" \
"s_add_u32 m0, s52, 83968\n" \
"s_nop 0\n" \
"buffer_load_dwordx4 v1, s[40:43], s54 offen nt lds\n" \
"s_add_u32 s54, s54, s44\n" \
"s_add_u32 m0, s52, 86016\n" \
"s_nop 0\n" \
"buffer_load_dwordx4 v1, s[40:43], s54 offen nt lds\n" \
"s_add_u32 s54, s54, s44\n" \
"s_add_u32 m0, s52, 88064\n" \
"s_nop 0\n" \
"buffer_load_dwordx4 v1, s[40:43], s54 offen nt lds\n" \
"s_add_u32 s54, s54, s44\n" \
"s_waitcnt vmcnt(28)\n" \
"s_barrier\n" \
"ds_read_b64_tr_b16 a[48:49], v8 offset:32768\n" \
"ds_read_b64_tr_b16 a[50:51], v8 offset:33280\n" \
"ds_read_b64_tr_b16 a[176:177], v9 offset:32768\n" \
"ds_read_b64_tr_b16 a[178:179], v9 offset:33280\n" \
"ds_read_b64_tr_b16 a[52:53], v8 offset:36864\n" \
"ds_read_b64_tr_b16 a[54:55], v8 offset:37376\n" \
"ds_read_b64_tr_b16 a[180:181], v9 offset:36864\n" \
"ds_read_b64_tr_b16 a[182:183], v9 offset:37376\n" \
"s_waitcnt lgkmcnt(0)\n" \
"s_barrier\n" \
"s_add_u32 m0, s52, 98304\n" \
"s_nop 0\n" \
"buffer_load_dwordx4 v1, s[40:43], s54 offen nt lds\n" \
"s_add_u32 s54, s54, s44\n" \
"s_add_u32 m0, s52, 100352\n" \
"s_nop 0\n" \
"buffer_load_dwordx4 v1, s[40:43], s54 offen nt lds\n" \
"s_add_u32 s54, s54, s44\n" \
"s_add_u32 m0, s52, 102400\n" \
"s_nop 0\n" \
"buffer_load_dwordx4 v1, s[40:43], s54 offen nt lds\n" \
"s_add_u32 s54, s54, s44\n" \
"s_add_u32 m0, s52, 104448\n" \
"s_nop 0\n" \
"buffer_load_dwordx4 v1, s[40:43], s54 offen nt lds\n" \
"s_add_u32 s54, s54, s44\n" \
"s_waitcnt vmcnt(28)\n" \
"s_barrier\n" \
"ds_read_b64_tr_b16 a[56:57], v8 offset:49152\n" \
"ds_read_b64_tr_b16 a[58:59], v8 offset:49664\n" \
"ds_read_b64_tr_b16 a[184:185], v9 offset:49152\n" \
"ds_read_b64_tr_b16 a[186:187], v9 offset:49664\n" \
"ds_read_b64_tr_b16 a[60:61], v8 offset:53248\n" \
"ds_read_b64_tr_b16 a[62:63], v8 offset:53760\n" \
"ds_read_b64_tr_b16 a[188:189], v9 offset:53248\n" \
"ds_read_b64_tr_b16 a[190:191], v9 offset:53760\n" \
"s_waitcnt lgkmcnt(0)\n" \
"s_barrier\n" \
"s_add_u32 m0, s52, 114688\n" \
"s_nop 0\n" \
"buffer_load_dwordx4 v1, s[40:43], s54 offen nt lds\n" \
"s_add_u32 s54, s54, s44\n" \
"s_add_u32 m0, s52, 116736\n" \
"s_nop 0\n" \
"buffer_load_dwordx4 v1, s[40:43], s54 offen nt lds\n" \
"s_add_u32 s54, s54, s44\n" \
"s_add_u32 m0, s52, 118784\n" \
"s_nop 0\n" \
"buffer_load_dwordx4 v1, s[40:43], s54 offen nt lds\n" \
"s_add_u32 s54, s54, s44\n" \
"s_add_u32 m0, s52, 120832\n" \
"s_nop 0\n" \
"buffer_load_dwordx4 v1, s[40:43], s54 offen nt lds\n" \
"s_add_u32 s54, s54, s44\n" \
"s_waitcnt vmcnt(28)\n" \
"s_barrier\n" \
"ds_read_b64_tr_b16 a[64:65], v2 offset:0\n" \
"ds_read_b64_tr_b16 a[66:67], v2 offset:512\n" \
"ds_read_b64_tr_b16 a[192:193], v3 offset:0\n" \
"ds_read_b64_tr_b16 a[194:195], v3 offset:512\n" \
"ds_read_b64_tr_b16 a[68:69], v2 offset:4096\n" \
"ds_read_b64_tr_b16 a[70:71], v2 offset:4608\n" \
"ds_read_b64_tr_b16 a[196:197], v3 offset:4096\n" \
"ds_read_b64_tr_b16 a[198:199], v3 offset:4608\n" \
"s_waitcnt vmcnt(24)\n" \
"s_barrier\n" \
"ds_read_b64_tr_b16 a[72:73], v2 offset:16384\n" \
"ds_read_b64_tr_b16 a[74:75], v2 offset:16896\n" \
"ds_read_b64_tr_b16 a[200:201], v3 offset:16384\n" \
"ds_read_b64_tr_b16 a[202:203], v3 offset:16896\n" \
"ds_read_b64_tr_b16 a[76:77], v2 offset:20480\n" \
"ds_read_b64_tr_b16 a[78:79], v2 offset:20992\n" \
"ds_read_b64_tr_b16 a[204:205], v3 offset:20480\n" \
"ds_read_b64_tr_b16 a[206:207], v3 offset:20992\n" \
"s_waitcnt vmcnt(20)\n" \
"s_barrier\n" \
"ds_read_b64_tr_b16 a[80:81], v2 offset:32768\n" \
"ds_read_b64_tr_b16 a[82:83], v2 offset:33280\n" \
"ds_read_b64_tr_b16 a[208:209], v3 offset:32768\n" \
"ds_read_b64_tr_b16 a[210:211], v3 offset:33280\n" \
"ds_read_b64_tr_b16 a[84:85], v2 offset:36864\n" \
"ds_read_b64_tr_b16 a[86:87], v2 offset:37376\n" \
"ds_read_b64_tr_b16 a[212:213], v3 offset:36864\n" \
"ds_read_b64_tr_b16 a[214:215], v3 offset:37376\n" \
"s_waitcnt vmcnt(16)\n" \
"s_barrier\n" \
"ds_read_b64_tr_b16 a[88:89], v2 offset:49152\n" \
"ds_read_b64_tr_b16 a[90:91], v2 offset:49664\n" \
"ds_read_b64_tr_b16 a[216:217], v3 offset:49152\n" \
"ds_read_b64_tr_b16 a[218:219], v3 offset:49664\n" \
"ds_read_b64_tr_b16 a[92:93], v2 offset:53248\n" \
"ds_read_b64_tr_b16 a[94:95], v2 offset:53760\n" \
"ds_read_b64_tr_b16 a[220:221], v3 offset:53248\n" \
"ds_read_b64_tr_b16 a[222:223], v3 offset:53760\n" \
"s_waitcnt vmcnt(12)\n" \
"s_barrier\n" \
"ds_read_b64_tr_b16 a[96:97], v8 offset:0\n" \
"ds_read_b64_tr_b16 a[98:99], v8 offset:512\n" \
"ds_read_b64_tr_b16 a[224:225], v9 offset:0\n" \
"ds_read_b64_tr_b16 a[226:227], v9 offset:512\n" \
"ds_read_b64_tr_b16 a[100:101], v8 offset:4096\n" \
"ds_read_b64_tr_b16 a[102:103], v8 offset:4608\n" \
"ds_read_b64_tr_b16 a[228:229], v9 offset:4096\n" \
"ds_read_b64_tr_b16 a[230:231], v9 offset:4608\n" \
"s_waitcnt vmcnt(8)\n" \
"s_barrier\n" \
"ds_read_b64_tr_b16 a[104:105], v8 offset:16384\n" \
"ds_read_b64_tr_b16 a[106:107], v8 offset:16896\n" \
"ds_read_b64_tr_b16 a[232:233], v9 offset:16384\n" \
"ds_read_b64_tr_b16 a[234:235], v9 offset:16896\n" \
"ds_read_b64_tr_b16 a[108:109], v8 offset:20480\n" \
"ds_read_b64_tr_b16 a[110:111], v8 offset:20992\n" \
"ds_read_b64_tr_b16 a[236:237], v9 offset:20480\n" \
"ds_read_b64_tr_b16 a[238:239], v9 offset:20992\n" \
"s_waitcnt vmcnt(4)\n" \
"s_barrier\n" \
"ds_read_b64_tr_b16 a[112:113], v8 offset:32768\n" \
"ds_read_b64_tr_b16 a[114:115], v8 offset:33280\n" \
"ds_read_b64_tr_b16 a[240:241], v9 offset:32768\n" \
"ds_read_b64_tr_b16 a[242:243], v9 offset:33280\n" \
"ds_read_b64_tr_b16 a[116:117], v8 offset:36864\n" \
"ds_read_b64_tr_b16 a[118:119], v8 offset:37376\n" \
"ds_read_b64_tr_b16 a[244:245], v9 offset:36864\n" \
"ds_read_b64_tr_b16 a[246:247], v9 offset:37376\n" \
"s_waitcnt vmcnt(0)\n" \
"s_barrier\n" \
"ds_read_b64_tr_b16 a[120:121], v8 offset:49152\n" \
"ds_read_b64_tr_b16 a[122:123], v8 offset:49664\n" \
"ds_read_b64_tr_b16 a[248:249], v9 offset:49152\n" \
"ds_read_b64_tr_b16 a[250:251], v9 offset:49664\n" \
"ds_read_b64_tr_b16 a[124:125], v8 offset:53248\n" \
"ds_read_b64_tr_b16 a[126:127], v8 offset:53760\n" \
"ds_read_b64_tr_b16 a[252:253], v9 offset:53248\n" \
"ds_read_b64_tr_b16 a[254:255], v9 offset:53760\n" \
"s_waitcnt lgkmcnt(0)\n" \
"s_barrier\n" \
"v_mov_b32 v14, 4096\n" \
"s_mov_b32 s59, s46\n" \
"s_add_u32 s63, s59, 0\n" \
"s_min_u32 s63, s63, 63\n" \
"s_lshr_b32 s63, s63, 3\n" \
"s_lshl_b32 s63, s63, 7\n" \
"s_branch .Lp1qk_l0_%=\n" \
".Lp1qk_l0k_%=:\n" \
"s_add_u32 s63, s59, 1\n" \
"s_min_u32 s63, s63, 63\n" \
"s_lshr_b32 s63, s63, 3\n" \
"s_lshl_b32 s63, s63, 7\n" \
"s_branch .Lp1qk_l1_%=\n" \
".Lp1qk_l1k_%=:\n" \
"s_lshl_b32 s55, s48, 13\n" \
"s_lshl_b32 s56, s46, 17\n" \
"s_add_u32 s56, s56, s45\n" \
"s_lshl_b32 s60, s47, 17\n" \
"s_sub_u32 s60, s60, 0x20000\n" \
"s_add_u32 s60, s60, s45\n" \
"s_mov_b32 s57, 0\n" \
"s_mov_b32 s58, 0\n" \
"v_mov_b32 v7, v5\n" \
"v_mov_b32 v5, 0x7ffffff0\n" \
"v_mov_b32 v10, v0\n" \
"v_mov_b32 v11, v6\n" \
"v_mov_b32 v6, 0x7ffffff0\n" \
"s_mov_b32 s59, s46\n" \
"s_min_u32 s61, s56, s60\n" \
"s_lshr_b32 s65, s61, 8\n" \
"s_and_b32 s65, s65, 0x2000\n" \
"s_xor_b32 s61, s61, s65\n" \
"s_add_u32 m0, s55, 0\n" \
"s_nop 0\n" \
"buffer_load_dwordx4 v4, s[36:39], s61 offen offset:0 sc1 lds\n" \
"buffer_load_dwordx4 v4, s[36:39], s61 offen offset:1024 sc1 lds\n" \
"buffer_load_dwordx4 v4, s[36:39], s61 offen offset:2048 sc1 lds\n" \
"buffer_load_dwordx4 v4, s[36:39], s61 offen offset:3072 sc1 lds\n" \
"s_add_u32 m0, s55, 4096\n" \
"s_add_u32 s61, s61, 4096\n" \
"buffer_load_dwordx4 v4, s[36:39], s61 offen offset:0 sc1 lds\n" \
"buffer_load_dwordx4 v4, s[36:39], s61 offen offset:1024 sc1 lds\n" \
"buffer_load_dwordx4 v4, s[36:39], s61 offen offset:2048 sc1 lds\n" \
"buffer_load_dwordx4 v4, s[36:39], s61 offen offset:3072 sc1 lds\n" \
"s_add_u32 s61, s61, 28672\n" \
"s_add_u32 s56, s56, 0x20000\n" \
"s_add_u32 m0, s55, 32768\n" \
"s_nop 0\n" \
"buffer_load_dwordx4 v4, s[36:39], s61 offen offset:0 sc1 lds\n" \
"buffer_load_dwordx4 v4, s[36:39], s61 offen offset:1024 sc1 lds\n" \
"buffer_load_dwordx4 v4, s[36:39], s61 offen offset:2048 sc1 lds\n" \
"buffer_load_dwordx4 v4, s[36:39], s61 offen offset:3072 sc1 lds\n" \
"s_add_u32 m0, s55, 36864\n" \
"s_add_u32 s61, s61, 4096\n" \
"buffer_load_dwordx4 v4, s[36:39], s61 offen offset:0 sc1 lds\n" \
"buffer_load_dwordx4 v4, s[36:39], s61 offen offset:1024 sc1 lds\n" \
"buffer_load_dwordx4 v4, s[36:39], s61 offen offset:2048 sc1 lds\n" \
"buffer_load_dwordx4 v4, s[36:39], s61 offen offset:3072 sc1 lds\n" \
"s_mov_b32 s70, 0\n" \
"s_mov_b32 s72, 0\n" \
"s_waitcnt vmcnt(0) lgkmcnt(0)\n" \
"s_barrier\n" \
"v_add_u32 v12, 0x10000, v10\n" \
"ds_read_b128 v[16:19], v10 offset:0\n" \
"ds_read_b128 v[20:23], v10 offset:16384\n" \
"ds_read_b128 v[24:27], v10 offset:1024\n" \
"ds_read_b128 v[28:31], v10 offset:17408\n" \
"ds_read_b128 v[32:35], v10 offset:2048\n" \
"ds_read_b128 v[36:39], v10 offset:18432\n" \
"s_waitcnt lgkmcnt(4)\n" \
"s_nop 7\n" \
"s_nop 7\n" \
".p2align 6\n" \
".Lp1qk_loop_%=:\n" \
"v_mfma_f32_16x16x32_bf16 v[48:51], a[0:3], v[16:19], 0\n" \
"s_min_u32 s61, s56, s60\n" \
"s_lshr_b32 s65, s61, 8\n" \
"s_and_b32 s65, s65, 0x2000\n" \
"s_xor_b32 s61, s61, s65\n" \
"s_add_u32 m0, s55, 65536\n" \
"s_nop 0\n" \
"buffer_load_dwordx4 v4, s[36:39], s61 offen offset:0 sc1 lds\n" \
"v_mov_b32_e64 v13, s59\n" \
"v_mfma_f32_16x16x32_bf16 v[52:55], a[0:3], v[20:23], 0\n" \
"ds_read_b128 v[40:43], v10 offset:3072\n" \
"s_nop 0\n" \
"v_mfma_f32_16x16x32_bf16 v[56:59], a[128:131], v[16:19], 0\n" \
"ds_read_b128 v[44:47], v10 offset:19456\n" \
"s_nop 0\n" \
"v_mfma_f32_16x16x32_bf16 v[60:63], a[128:131], v[20:23], 0\n" \
"s_waitcnt lgkmcnt(4)\n" \
"v_mul_f32 v64, s51, v64\n" \
"v_mul_f32_e64 v65, s51, v65\n" \
"v_mfma_f32_16x16x32_bf16 v[48:51], a[4:7], v[24:27], v[48:51]\n" \
"buffer_load_dwordx4 v4, s[36:39], s61 offen offset:1024 sc1 lds\n" \
"v_mfma_f32_16x16x32_bf16 v[52:55], a[4:7], v[28:31], v[52:55]\n" \
"ds_read_b128 v[16:19], v10 offset:4096\n" \
"v_mul_f32_e64 v66, s51, v66\n" \
"v_mfma_f32_16x16x32_bf16 v[56:59], a[132:135], v[24:27], v[56:59]\n" \
"ds_read_b128 v[20:23], v10 offset:20480\n" \
"v_mul_f32_e64 v67, s51, v67\n" \
"v_mfma_f32_16x16x32_bf16 v[60:63], a[132:135], v[28:31], v[60:63]\n" \
"s_waitcnt lgkmcnt(4)\n" \
"v_mul_f32 v72, s51, v72\n" \
"v_mul_f32_e64 v73, s51, v73\n" \
"v_mfma_f32_16x16x32_bf16 v[48:51], a[8:11], v[32:35], v[48:51]\n" \
"s_add_u32 s63, s59, 2\n" \
"s_min_u32 s63, s63, 63\n" \
"v_mfma_f32_16x16x32_bf16 v[52:55], a[8:11], v[36:39], v[52:55]\n" \
"ds_read_b128 v[24:27], v10 offset:5120\n" \
"s_lshr_b32 s63, s63, 3\n" \
"s_lshl_b32 s63, s63, 7\n" \
"v_mul_f32_e64 v74, s51, v74\n" \
"v_mfma_f32_16x16x32_bf16 v[56:59], a[136:139], v[32:35], v[56:59]\n" \
"ds_read_b128 v[28:31], v10 offset:21504\n" \
"v_mul_f32_e64 v75, s51, v75\n" \
"v_mfma_f32_16x16x32_bf16 v[60:63], a[136:139], v[36:39], v[60:63]\n" \
"s_waitcnt lgkmcnt(4)\n" \
"v_cvt_pk_bf16_f32 v80, v64, v65\n" \
"v_cvt_pk_bf16_f32 v81, v66, v67\n" \
"v_mfma_f32_16x16x32_bf16 v[48:51], a[12:15], v[40:43], v[48:51]\n" \
"buffer_load_dwordx4 v4, s[36:39], s61 offen offset:2048 sc1 lds\n" \
"v_mfma_f32_16x16x32_bf16 v[52:55], a[12:15], v[44:47], v[52:55]\n" \
"ds_read_b128 v[32:35], v10 offset:6144\n" \
"v_cvt_pk_bf16_f32 v82, v72, v73\n" \
"v_mfma_f32_16x16x32_bf16 v[56:59], a[140:143], v[40:43], v[56:59]\n" \
"ds_read_b128 v[36:39], v10 offset:22528\n" \
"v_cvt_pk_bf16_f32 v83, v74, v75\n" \
"v_mfma_f32_16x16x32_bf16 v[60:63], a[140:143], v[44:47], v[60:63]\n" \
"s_waitcnt lgkmcnt(4)\n" \
"buffer_store_dwordx4 v[80:83], v5, s[36:39], s57 offen sc1\n" \
"v_mul_f32_e64 v68, s51, v68\n" \
"v_mfma_f32_16x16x32_bf16 v[48:51], a[16:19], v[16:19], v[48:51]\n" \
"buffer_load_dwordx4 v4, s[36:39], s61 offen offset:3072 sc1 lds\n" \
"v_mfma_f32_16x16x32_bf16 v[52:55], a[16:19], v[20:23], v[52:55]\n" \
"ds_read_b128 v[40:43], v10 offset:7168\n" \
"v_mul_f32_e64 v69, s51, v69\n" \
"v_mfma_f32_16x16x32_bf16 v[56:59], a[144:147], v[16:19], v[56:59]\n" \
"ds_read_b128 v[44:47], v10 offset:23552\n" \
"v_mul_f32_e64 v70, s51, v70\n" \
"v_mfma_f32_16x16x32_bf16 v[60:63], a[144:147], v[20:23], v[60:63]\n" \
"s_waitcnt lgkmcnt(4)\n" \
"v_mul_f32 v71, s51, v71\n" \
"v_mul_f32_e64 v76, s51, v76\n" \
"v_mfma_f32_16x16x32_bf16 v[48:51], a[20:23], v[24:27], v[48:51]\n" \
"buffer_load_dword v15, v14, s[36:39], s63 offen sc1\n" \
"v_mfma_f32_16x16x32_bf16 v[52:55], a[20:23], v[28:31], v[52:55]\n" \
"ds_read_b128 v[16:19], v10 offset:8192\n" \
"v_mul_f32_e64 v77, s51, v77\n" \
"v_mfma_f32_16x16x32_bf16 v[56:59], a[148:151], v[24:27], v[56:59]\n" \
"ds_read_b128 v[20:23], v10 offset:24576\n" \
"v_mul_f32_e64 v78, s51, v78\n" \
"v_mfma_f32_16x16x32_bf16 v[60:63], a[148:151], v[28:31], v[60:63]\n" \
"s_waitcnt lgkmcnt(4)\n" \
"v_mul_f32 v79, s51, v79\n" \
"v_cvt_pk_bf16_f32 v84, v68, v69\n" \
"v_mfma_f32_16x16x32_bf16 v[48:51], a[24:27], v[32:35], v[48:51]\n" \
"s_add_u32 m0, s55, 69632\n" \
"s_add_u32 s61, s61, 4096\n" \
"buffer_load_dwordx4 v4, s[36:39], s61 offen offset:0 sc1 lds\n" \
"v_mfma_f32_16x16x32_bf16 v[52:55], a[24:27], v[36:39], v[52:55]\n" \
"ds_read_b128 v[24:27], v10 offset:9216\n" \
"v_cvt_pk_bf16_f32 v85, v70, v71\n" \
"v_mfma_f32_16x16x32_bf16 v[56:59], a[152:155], v[32:35], v[56:59]\n" \
"ds_read_b128 v[28:31], v10 offset:25600\n" \
"v_cvt_pk_bf16_f32 v86, v76, v77\n" \
"v_mfma_f32_16x16x32_bf16 v[60:63], a[152:155], v[36:39], v[60:63]\n" \
"s_waitcnt lgkmcnt(4)\n" \
"v_cvt_pk_bf16_f32 v87, v78, v79\n" \
"buffer_store_dwordx4 v[84:87], v5, s[36:39], s58 offen sc1\n" \
"v_mfma_f32_16x16x32_bf16 v[48:51], a[28:31], v[40:43], v[48:51]\n" \
"buffer_load_dwordx4 v4, s[36:39], s61 offen offset:1024 sc1 lds\n" \
"v_mfma_f32_16x16x32_bf16 v[52:55], a[28:31], v[44:47], v[52:55]\n" \
"ds_read_b128 v[32:35], v10 offset:10240\n" \
"v_mfma_f32_16x16x32_bf16 v[56:59], a[156:159], v[40:43], v[56:59]\n" \
"ds_read_b128 v[36:39], v10 offset:26624\n" \
"v_mfma_f32_16x16x32_bf16 v[60:63], a[156:159], v[44:47], v[60:63]\n" \
"s_waitcnt lgkmcnt(4)\n" \
"v_mfma_f32_16x16x32_bf16 v[48:51], a[32:35], v[16:19], v[48:51]\n" \
"v_mfma_f32_16x16x32_bf16 v[52:55], a[32:35], v[20:23], v[52:55]\n" \
"ds_read_b128 v[40:43], v10 offset:11264\n" \
"v_mfma_f32_16x16x32_bf16 v[56:59], a[160:163], v[16:19], v[56:59]\n" \
"ds_read_b128 v[44:47], v10 offset:27648\n" \
"v_mfma_f32_16x16x32_bf16 v[60:63], a[160:163], v[20:23], v[60:63]\n" \
"s_waitcnt lgkmcnt(4)\n" \
"v_mfma_f32_16x16x32_bf16 v[48:51], a[36:39], v[24:27], v[48:51]\n" \
"buffer_load_dwordx4 v4, s[36:39], s61 offen offset:2048 sc1 lds\n" \
"v_mfma_f32_16x16x32_bf16 v[52:55], a[36:39], v[28:31], v[52:55]\n" \
"ds_read_b128 v[16:19], v10 offset:12288\n" \
"v_mfma_f32_16x16x32_bf16 v[56:59], a[164:167], v[24:27], v[56:59]\n" \
"ds_read_b128 v[20:23], v10 offset:28672\n" \
"v_mfma_f32_16x16x32_bf16 v[60:63], a[164:167], v[28:31], v[60:63]\n" \
"s_waitcnt lgkmcnt(4)\n" \
"v_mfma_f32_16x16x32_bf16 v[48:51], a[40:43], v[32:35], v[48:51]\n" \
"buffer_load_dwordx4 v4, s[36:39], s61 offen offset:3072 sc1 lds\n" \
"v_mfma_f32_16x16x32_bf16 v[52:55], a[40:43], v[36:39], v[52:55]\n" \
"ds_read_b128 v[24:27], v10 offset:13312\n" \
"v_mfma_f32_16x16x32_bf16 v[56:59], a[168:171], v[32:35], v[56:59]\n" \
"ds_read_b128 v[28:31], v10 offset:29696\n" \
"v_mfma_f32_16x16x32_bf16 v[60:63], a[168:171], v[36:39], v[60:63]\n" \
"s_waitcnt lgkmcnt(4)\n" \
"v_mfma_f32_16x16x32_bf16 v[48:51], a[44:47], v[40:43], v[48:51]\n" \
"v_mfma_f32_16x16x32_bf16 v[52:55], a[44:47], v[44:47], v[52:55]\n" \
"ds_read_b128 v[32:35], v10 offset:14336\n" \
"v_mfma_f32_16x16x32_bf16 v[56:59], a[172:175], v[40:43], v[56:59]\n" \
"ds_read_b128 v[36:39], v10 offset:30720\n" \
"v_mfma_f32_16x16x32_bf16 v[60:63], a[172:175], v[44:47], v[60:63]\n" \
"s_waitcnt lgkmcnt(4)\n" \
"v_mfma_f32_16x16x32_bf16 v[48:51], a[48:51], v[16:19], v[48:51]\n" \
"v_mfma_f32_16x16x32_bf16 v[52:55], a[48:51], v[20:23], v[52:55]\n" \
"ds_read_b128 v[40:43], v10 offset:15360\n" \
"v_mfma_f32_16x16x32_bf16 v[56:59], a[176:179], v[16:19], v[56:59]\n" \
"ds_read_b128 v[44:47], v10 offset:31744\n" \
"v_mfma_f32_16x16x32_bf16 v[60:63], a[176:179], v[20:23], v[60:63]\n" \
"s_waitcnt lgkmcnt(4)\n" \
"v_mfma_f32_16x16x32_bf16 v[48:51], a[52:55], v[24:27], v[48:51]\n" \
"s_waitcnt vmcnt(12)\n" \
"s_barrier\n" \
"v_mfma_f32_16x16x32_bf16 v[52:55], a[52:55], v[28:31], v[52:55]\n" \
"ds_read_b128 v[16:19], v10 offset:32768\n" \
"v_mfma_f32_16x16x32_bf16 v[56:59], a[180:183], v[24:27], v[56:59]\n" \
"ds_read_b128 v[20:23], v10 offset:49152\n" \
"v_mfma_f32_16x16x32_bf16 v[60:63], a[180:183], v[28:31], v[60:63]\n" \
"s_waitcnt lgkmcnt(4)\n" \
"v_mfma_f32_16x16x32_bf16 v[48:51], a[56:59], v[32:35], v[48:51]\n" \
"v_mfma_f32_16x16x32_bf16 v[52:55], a[56:59], v[36:39], v[52:55]\n" \
"ds_read_b128 v[24:27], v10 offset:33792\n" \
"v_mfma_f32_16x16x32_bf16 v[56:59], a[184:187], v[32:35], v[56:59]\n" \
"ds_read_b128 v[28:31], v10 offset:50176\n" \
"v_mfma_f32_16x16x32_bf16 v[60:63], a[184:187], v[36:39], v[60:63]\n" \
"s_waitcnt lgkmcnt(4)\n" \
"v_mfma_f32_16x16x32_bf16 v[48:51], a[60:63], v[40:43], v[48:51]\n" \
"v_mfma_f32_16x16x32_bf16 v[52:55], a[60:63], v[44:47], v[52:55]\n" \
"ds_read_b128 v[32:35], v10 offset:34816\n" \
"v_mfma_f32_16x16x32_bf16 v[56:59], a[188:191], v[40:43], v[56:59]\n" \
"ds_read_b128 v[36:39], v10 offset:51200\n" \
"v_mfma_f32_16x16x32_bf16 v[60:63], a[188:191], v[44:47], v[60:63]\n" \
"s_waitcnt lgkmcnt(4)\n" \
"v_mfma_f32_16x16x32_bf16 v[48:51], a[64:67], v[16:19], v[48:51]\n" \
"s_add_u32 s61, s61, 28672\n" \
"s_add_u32 s56, s56, 0x20000\n" \
"s_add_u32 m0, s55, 98304\n" \
"s_nop 0\n" \
"buffer_load_dwordx4 v4, s[36:39], s61 offen offset:0 sc1 lds\n" \
"v_mfma_f32_16x16x32_bf16 v[52:55], a[64:67], v[20:23], v[52:55]\n" \
"ds_read_b128 v[40:43], v10 offset:35840\n" \
"v_mfma_f32_16x16x32_bf16 v[56:59], a[192:195], v[16:19], v[56:59]\n" \
"ds_read_b128 v[44:47], v10 offset:52224\n" \
"v_mfma_f32_16x16x32_bf16 v[60:63], a[192:195], v[20:23], v[60:63]\n" \
"s_waitcnt lgkmcnt(4)\n" \
"v_mfma_f32_16x16x32_bf16 v[48:51], a[68:71], v[24:27], v[48:51]\n" \
"buffer_load_dwordx4 v4, s[36:39], s61 offen offset:1024 sc1 lds\n" \
"v_mfma_f32_16x16x32_bf16 v[52:55], a[68:71], v[28:31], v[52:55]\n" \
"ds_read_b128 v[16:19], v10 offset:36864\n" \
"v_mfma_f32_16x16x32_bf16 v[56:59], a[196:199], v[24:27], v[56:59]\n" \
"ds_read_b128 v[20:23], v10 offset:53248\n" \
"v_mfma_f32_16x16x32_bf16 v[60:63], a[196:199], v[28:31], v[60:63]\n" \
"s_waitcnt lgkmcnt(4)\n" \
"v_mfma_f32_16x16x32_bf16 v[48:51], a[72:75], v[32:35], v[48:51]\n" \
"v_mfma_f32_16x16x32_bf16 v[52:55], a[72:75], v[36:39], v[52:55]\n" \
"ds_read_b128 v[24:27], v10 offset:37888\n" \
"v_mfma_f32_16x16x32_bf16 v[56:59], a[200:203], v[32:35], v[56:59]\n" \
"ds_read_b128 v[28:31], v10 offset:54272\n" \
"v_mfma_f32_16x16x32_bf16 v[60:63], a[200:203], v[36:39], v[60:63]\n" \
"s_waitcnt lgkmcnt(4)\n" \
"v_mfma_f32_16x16x32_bf16 v[48:51], a[76:79], v[40:43], v[48:51]\n" \
"buffer_load_dwordx4 v4, s[36:39], s61 offen offset:2048 sc1 lds\n" \
"v_mfma_f32_16x16x32_bf16 v[52:55], a[76:79], v[44:47], v[52:55]\n" \
"ds_read_b128 v[32:35], v10 offset:38912\n" \
"v_mfma_f32_16x16x32_bf16 v[56:59], a[204:207], v[40:43], v[56:59]\n" \
"ds_read_b128 v[36:39], v10 offset:55296\n" \
"v_mfma_f32_16x16x32_bf16 v[60:63], a[204:207], v[44:47], v[60:63]\n" \
"s_waitcnt lgkmcnt(4)\n" \
"v_mfma_f32_16x16x32_bf16 v[48:51], a[80:83], v[16:19], v[48:51]\n" \
"buffer_load_dwordx4 v4, s[36:39], s61 offen offset:3072 sc1 lds\n" \
"v_mfma_f32_16x16x32_bf16 v[52:55], a[80:83], v[20:23], v[52:55]\n" \
"ds_read_b128 v[40:43], v10 offset:39936\n" \
"v_mfma_f32_16x16x32_bf16 v[56:59], a[208:211], v[16:19], v[56:59]\n" \
"ds_read_b128 v[44:47], v10 offset:56320\n" \
"v_mfma_f32_16x16x32_bf16 v[60:63], a[208:211], v[20:23], v[60:63]\n" \
"s_waitcnt lgkmcnt(4)\n" \
"v_mfma_f32_16x16x32_bf16 v[48:51], a[84:87], v[24:27], v[48:51]\n" \
"v_mfma_f32_16x16x32_bf16 v[52:55], a[84:87], v[28:31], v[52:55]\n" \
"ds_read_b128 v[16:19], v10 offset:40960\n" \
"v_mfma_f32_16x16x32_bf16 v[56:59], a[212:215], v[24:27], v[56:59]\n" \
"ds_read_b128 v[20:23], v10 offset:57344\n" \
"v_mfma_f32_16x16x32_bf16 v[60:63], a[212:215], v[28:31], v[60:63]\n" \
"s_waitcnt lgkmcnt(4)\n" \
"v_mfma_f32_16x16x32_bf16 v[48:51], a[88:91], v[32:35], v[48:51]\n" \
"s_add_u32 m0, s55, 102400\n" \
"s_add_u32 s61, s61, 4096\n" \
"buffer_load_dwordx4 v4, s[36:39], s61 offen offset:0 sc1 lds\n" \
"v_mfma_f32_16x16x32_bf16 v[52:55], a[88:91], v[36:39], v[52:55]\n" \
"ds_read_b128 v[24:27], v10 offset:41984\n" \
"v_mfma_f32_16x16x32_bf16 v[56:59], a[216:219], v[32:35], v[56:59]\n" \
"ds_read_b128 v[28:31], v10 offset:58368\n" \
"v_mfma_f32_16x16x32_bf16 v[60:63], a[216:219], v[36:39], v[60:63]\n" \
"s_waitcnt lgkmcnt(4)\n" \
"v_mfma_f32_16x16x32_bf16 v[48:51], a[92:95], v[40:43], v[48:51]\n" \
"buffer_load_dwordx4 v4, s[36:39], s61 offen offset:1024 sc1 lds\n" \
"v_mfma_f32_16x16x32_bf16 v[52:55], a[92:95], v[44:47], v[52:55]\n" \
"ds_read_b128 v[32:35], v10 offset:43008\n" \
"v_mfma_f32_16x16x32_bf16 v[56:59], a[220:223], v[40:43], v[56:59]\n" \
"ds_read_b128 v[36:39], v10 offset:59392\n" \
"v_mfma_f32_16x16x32_bf16 v[60:63], a[220:223], v[44:47], v[60:63]\n" \
"s_waitcnt lgkmcnt(4)\n" \
"v_mfma_f32_16x16x32_bf16 v[48:51], a[96:99], v[16:19], v[48:51]\n" \
"v_mfma_f32_16x16x32_bf16 v[52:55], a[96:99], v[20:23], v[52:55]\n" \
"ds_read_b128 v[40:43], v10 offset:44032\n" \
"v_mfma_f32_16x16x32_bf16 v[56:59], a[224:227], v[16:19], v[56:59]\n" \
"ds_read_b128 v[44:47], v10 offset:60416\n" \
"v_mfma_f32_16x16x32_bf16 v[60:63], a[224:227], v[20:23], v[60:63]\n" \
"s_waitcnt lgkmcnt(4)\n" \
"v_mfma_f32_16x16x32_bf16 v[48:51], a[100:103], v[24:27], v[48:51]\n" \
"buffer_load_dwordx4 v4, s[36:39], s61 offen offset:2048 sc1 lds\n" \
"v_mfma_f32_16x16x32_bf16 v[52:55], a[100:103], v[28:31], v[52:55]\n" \
"ds_read_b128 v[16:19], v10 offset:45056\n" \
"v_mfma_f32_16x16x32_bf16 v[56:59], a[228:231], v[24:27], v[56:59]\n" \
"ds_read_b128 v[20:23], v10 offset:61440\n" \
"v_mfma_f32_16x16x32_bf16 v[60:63], a[228:231], v[28:31], v[60:63]\n" \
"s_waitcnt lgkmcnt(4)\n" \
"v_mfma_f32_16x16x32_bf16 v[48:51], a[104:107], v[32:35], v[48:51]\n" \
"buffer_load_dwordx4 v4, s[36:39], s61 offen offset:3072 sc1 lds\n" \
"v_mfma_f32_16x16x32_bf16 v[52:55], a[104:107], v[36:39], v[52:55]\n" \
"ds_read_b128 v[24:27], v10 offset:46080\n" \
"v_mfma_f32_16x16x32_bf16 v[56:59], a[232:235], v[32:35], v[56:59]\n" \
"ds_read_b128 v[28:31], v10 offset:62464\n" \
"v_mfma_f32_16x16x32_bf16 v[60:63], a[232:235], v[36:39], v[60:63]\n" \
"s_waitcnt lgkmcnt(4)\n" \
"v_mfma_f32_16x16x32_bf16 v[48:51], a[108:111], v[40:43], v[48:51]\n" \
"s_lshl_b32 s66, s59, s49\n" \
"s_lshr_b32 s65, s66, 8\n" \
"v_mfma_f32_16x16x32_bf16 v[52:55], a[108:111], v[44:47], v[52:55]\n" \
"ds_read_b128 v[32:35], v10 offset:47104\n" \
"v_mfma_f32_16x16x32_bf16 v[56:59], a[236:239], v[40:43], v[56:59]\n" \
"ds_read_b128 v[36:39], v10 offset:63488\n" \
"v_mfma_f32_16x16x32_bf16 v[60:63], a[236:239], v[44:47], v[60:63]\n" \
"s_waitcnt lgkmcnt(4)\n" \
"s_and_b32 s65, s65, 0x2000\n" \
"s_xor_b32 s66, s66, s65\n" \
"v_mfma_f32_16x16x32_bf16 v[48:51], a[112:115], v[16:19], v[48:51]\n" \
"v_mfma_f32_16x16x32_bf16 v[52:55], a[112:115], v[20:23], v[52:55]\n" \
"ds_read_b128 v[40:43], v10 offset:48128\n" \
"v_mfma_f32_16x16x32_bf16 v[56:59], a[240:243], v[16:19], v[56:59]\n" \
"ds_read_b128 v[44:47], v10 offset:64512\n" \
"v_mfma_f32_16x16x32_bf16 v[60:63], a[240:243], v[20:23], v[60:63]\n" \
"s_waitcnt lgkmcnt(4)\n" \
"v_mfma_f32_16x16x32_bf16 v[48:51], a[116:119], v[24:27], v[48:51]\n" \
"s_waitcnt vmcnt(8)\n" \
"s_barrier\n" \
"v_mfma_f32_16x16x32_bf16 v[52:55], a[116:119], v[28:31], v[52:55]\n" \
"ds_read_b128 v[16:19], v12 offset:0\n" \
"v_mfma_f32_16x16x32_bf16 v[56:59], a[244:247], v[24:27], v[56:59]\n" \
"ds_read_b128 v[20:23], v12 offset:16384\n" \
"v_mfma_f32_16x16x32_bf16 v[60:63], a[244:247], v[28:31], v[60:63]\n" \
"s_waitcnt lgkmcnt(4)\n" \
"buffer_store_dword v13, v6, s[36:39], 0 offen sc1\n" \
"v_mfma_f32_16x16x32_bf16 v[48:51], a[120:123], v[32:35], v[48:51]\n" \
"v_cmp_gt_u32_e64 vcc, 16, v15\n" \
"v_mfma_f32_16x16x32_bf16 v[52:55], a[120:123], v[36:39], v[52:55]\n" \
"ds_read_b128 v[24:27], v12 offset:1024\n" \
"v_mfma_f32_16x16x32_bf16 v[56:59], a[248:251], v[32:35], v[56:59]\n" \
"ds_read_b128 v[28:31], v12 offset:17408\n" \
"v_mfma_f32_16x16x32_bf16 v[60:63], a[248:251], v[36:39], v[60:63]\n" \
"s_waitcnt lgkmcnt(4)\n" \
"v_mfma_f32_16x16x32_bf16 v[48:51], a[124:127], v[40:43], v[48:51]\n" \
"s_cbranch_vccnz .Lp1qk_m0_%=\n" \
".Lp1qk_m0k_%=:\n" \
"v_mfma_f32_16x16x32_bf16 v[52:55], a[124:127], v[44:47], v[52:55]\n" \
"ds_read_b128 v[32:35], v12 offset:2048\n" \
"v_mfma_f32_16x16x32_bf16 v[56:59], a[252:255], v[40:43], v[56:59]\n" \
"ds_read_b128 v[36:39], v12 offset:18432\n" \
"v_mfma_f32_16x16x32_bf16 v[60:63], a[252:255], v[44:47], v[60:63]\n" \
"s_waitcnt lgkmcnt(4)\n" \
"s_xor_b32 s57, s66, s64\n" \
"s_add_u32 s58, s57, s50\n" \
"s_add_u32 s59, s59, 1\n" \
"v_mov_b32_e64 v5, v7\n" \
"v_mov_b32_e64 v6, v11\n" \
"s_cmp_ge_u32 s59, s47\n" \
"s_nop 0\n" \
"s_cbranch_scc1 .Lp1qk_out0_%=\n" \
"v_mfma_f32_16x16x32_bf16 v[64:67], a[0:3], v[16:19], 0\n" \
"s_min_u32 s61, s56, s60\n" \
"s_lshr_b32 s65, s61, 8\n" \
"s_and_b32 s65, s65, 0x2000\n" \
"s_xor_b32 s61, s61, s65\n" \
"s_add_u32 m0, s55, 0\n" \
"s_nop 0\n" \
"buffer_load_dwordx4 v4, s[36:39], s61 offen offset:0 sc1 lds\n" \
"v_mov_b32_e64 v13, s59\n" \
"v_mfma_f32_16x16x32_bf16 v[68:71], a[0:3], v[20:23], 0\n" \
"ds_read_b128 v[40:43], v12 offset:3072\n" \
"s_nop 0\n" \
"v_mfma_f32_16x16x32_bf16 v[72:75], a[128:131], v[16:19], 0\n" \
"ds_read_b128 v[44:47], v12 offset:19456\n" \
"s_nop 0\n" \
"v_mfma_f32_16x16x32_bf16 v[76:79], a[128:131], v[20:23], 0\n" \
"s_waitcnt lgkmcnt(4)\n" \
"v_mul_f32 v48, s51, v48\n" \
"v_mul_f32_e64 v49, s51, v49\n" \
"v_mfma_f32_16x16x32_bf16 v[64:67], a[4:7], v[24:27], v[64:67]\n" \
"buffer_load_dwordx4 v4, s[36:39], s61 offen offset:1024 sc1 lds\n" \
"v_mfma_f32_16x16x32_bf16 v[68:71], a[4:7], v[28:31], v[68:71]\n" \
"ds_read_b128 v[16:19], v12 offset:4096\n" \
"v_mul_f32_e64 v50, s51, v50\n" \
"v_mfma_f32_16x16x32_bf16 v[72:75], a[132:135], v[24:27], v[72:75]\n" \
"ds_read_b128 v[20:23], v12 offset:20480\n" \
"v_mul_f32_e64 v51, s51, v51\n" \
"v_mfma_f32_16x16x32_bf16 v[76:79], a[132:135], v[28:31], v[76:79]\n" \
"s_waitcnt lgkmcnt(4)\n" \
"v_mul_f32 v56, s51, v56\n" \
"v_mul_f32_e64 v57, s51, v57\n" \
"v_mfma_f32_16x16x32_bf16 v[64:67], a[8:11], v[32:35], v[64:67]\n" \
"s_add_u32 s63, s59, 2\n" \
"s_min_u32 s63, s63, 63\n" \
"v_mfma_f32_16x16x32_bf16 v[68:71], a[8:11], v[36:39], v[68:71]\n" \
"ds_read_b128 v[24:27], v12 offset:5120\n" \
"s_lshr_b32 s63, s63, 3\n" \
"s_lshl_b32 s63, s63, 7\n" \
"v_mul_f32_e64 v58, s51, v58\n" \
"v_mfma_f32_16x16x32_bf16 v[72:75], a[136:139], v[32:35], v[72:75]\n" \
"ds_read_b128 v[28:31], v12 offset:21504\n" \
"v_mul_f32_e64 v59, s51, v59\n" \
"v_mfma_f32_16x16x32_bf16 v[76:79], a[136:139], v[36:39], v[76:79]\n" \
"s_waitcnt lgkmcnt(4)\n" \
"v_cvt_pk_bf16_f32 v80, v48, v49\n" \
"v_cvt_pk_bf16_f32 v81, v50, v51\n" \
"v_mfma_f32_16x16x32_bf16 v[64:67], a[12:15], v[40:43], v[64:67]\n" \
"buffer_load_dwordx4 v4, s[36:39], s61 offen offset:2048 sc1 lds\n" \
"v_mfma_f32_16x16x32_bf16 v[68:71], a[12:15], v[44:47], v[68:71]\n" \
"ds_read_b128 v[32:35], v12 offset:6144\n" \
"v_cvt_pk_bf16_f32 v82, v56, v57\n" \
"v_mfma_f32_16x16x32_bf16 v[72:75], a[140:143], v[40:43], v[72:75]\n" \
"ds_read_b128 v[36:39], v12 offset:22528\n" \
"v_cvt_pk_bf16_f32 v83, v58, v59\n" \
"v_mfma_f32_16x16x32_bf16 v[76:79], a[140:143], v[44:47], v[76:79]\n" \
"s_waitcnt lgkmcnt(4)\n" \
"buffer_store_dwordx4 v[80:83], v5, s[36:39], s57 offen sc1\n" \
"v_mul_f32_e64 v52, s51, v52\n" \
"v_mfma_f32_16x16x32_bf16 v[64:67], a[16:19], v[16:19], v[64:67]\n" \
"buffer_load_dwordx4 v4, s[36:39], s61 offen offset:3072 sc1 lds\n" \
"v_mfma_f32_16x16x32_bf16 v[68:71], a[16:19], v[20:23], v[68:71]\n" \
"ds_read_b128 v[40:43], v12 offset:7168\n" \
"v_mul_f32_e64 v53, s51, v53\n" \
"v_mfma_f32_16x16x32_bf16 v[72:75], a[144:147], v[16:19], v[72:75]\n" \
"ds_read_b128 v[44:47], v12 offset:23552\n" \
"v_mul_f32_e64 v54, s51, v54\n" \
"v_mfma_f32_16x16x32_bf16 v[76:79], a[144:147], v[20:23], v[76:79]\n" \
"s_waitcnt lgkmcnt(4)\n" \
"v_mul_f32 v55, s51, v55\n" \
"v_mul_f32_e64 v60, s51, v60\n" \
"v_mfma_f32_16x16x32_bf16 v[64:67], a[20:23], v[24:27], v[64:67]\n" \
"buffer_load_dword v15, v14, s[36:39], s63 offen sc1\n" \
"v_mfma_f32_16x16x32_bf16 v[68:71], a[20:23], v[28:31], v[68:71]\n" \
"ds_read_b128 v[16:19], v12 offset:8192\n" \
"v_mul_f32_e64 v61, s51, v61\n" \
"v_mfma_f32_16x16x32_bf16 v[72:75], a[148:151], v[24:27], v[72:75]\n" \
"ds_read_b128 v[20:23], v12 offset:24576\n" \
"v_mul_f32_e64 v62, s51, v62\n" \
"v_mfma_f32_16x16x32_bf16 v[76:79], a[148:151], v[28:31], v[76:79]\n" \
"s_waitcnt lgkmcnt(4)\n" \
"v_mul_f32 v63, s51, v63\n" \
"v_cvt_pk_bf16_f32 v84, v52, v53\n" \
"v_mfma_f32_16x16x32_bf16 v[64:67], a[24:27], v[32:35], v[64:67]\n" \
"s_add_u32 m0, s55, 4096\n" \
"s_add_u32 s61, s61, 4096\n" \
"buffer_load_dwordx4 v4, s[36:39], s61 offen offset:0 sc1 lds\n" \
"v_mfma_f32_16x16x32_bf16 v[68:71], a[24:27], v[36:39], v[68:71]\n" \
"ds_read_b128 v[24:27], v12 offset:9216\n" \
"v_cvt_pk_bf16_f32 v85, v54, v55\n" \
"v_mfma_f32_16x16x32_bf16 v[72:75], a[152:155], v[32:35], v[72:75]\n" \
"ds_read_b128 v[28:31], v12 offset:25600\n" \
"v_cvt_pk_bf16_f32 v86, v60, v61\n" \
"v_mfma_f32_16x16x32_bf16 v[76:79], a[152:155], v[36:39], v[76:79]\n" \
"s_waitcnt lgkmcnt(4)\n" \
"v_cvt_pk_bf16_f32 v87, v62, v63\n" \
"buffer_store_dwordx4 v[84:87], v5, s[36:39], s58 offen sc1\n" \
"v_mfma_f32_16x16x32_bf16 v[64:67], a[28:31], v[40:43], v[64:67]\n" \
"buffer_load_dwordx4 v4, s[36:39], s61 offen offset:1024 sc1 lds\n" \
"v_mfma_f32_16x16x32_bf16 v[68:71], a[28:31], v[44:47], v[68:71]\n" \
"ds_read_b128 v[32:35], v12 offset:10240\n" \
"v_mfma_f32_16x16x32_bf16 v[72:75], a[156:159], v[40:43], v[72:75]\n" \
"ds_read_b128 v[36:39], v12 offset:26624\n" \
"v_mfma_f32_16x16x32_bf16 v[76:79], a[156:159], v[44:47], v[76:79]\n" \
"s_waitcnt lgkmcnt(4)\n" \
"v_mfma_f32_16x16x32_bf16 v[64:67], a[32:35], v[16:19], v[64:67]\n" \
"v_mfma_f32_16x16x32_bf16 v[68:71], a[32:35], v[20:23], v[68:71]\n" \
"ds_read_b128 v[40:43], v12 offset:11264\n" \
"v_mfma_f32_16x16x32_bf16 v[72:75], a[160:163], v[16:19], v[72:75]\n" \
"ds_read_b128 v[44:47], v12 offset:27648\n" \
"v_mfma_f32_16x16x32_bf16 v[76:79], a[160:163], v[20:23], v[76:79]\n" \
"s_waitcnt lgkmcnt(4)\n" \
"v_mfma_f32_16x16x32_bf16 v[64:67], a[36:39], v[24:27], v[64:67]\n" \
"buffer_load_dwordx4 v4, s[36:39], s61 offen offset:2048 sc1 lds\n" \
"v_mfma_f32_16x16x32_bf16 v[68:71], a[36:39], v[28:31], v[68:71]\n" \
"ds_read_b128 v[16:19], v12 offset:12288\n" \
"v_mfma_f32_16x16x32_bf16 v[72:75], a[164:167], v[24:27], v[72:75]\n" \
"ds_read_b128 v[20:23], v12 offset:28672\n" \
"v_mfma_f32_16x16x32_bf16 v[76:79], a[164:167], v[28:31], v[76:79]\n" \
"s_waitcnt lgkmcnt(4)\n" \
"v_mfma_f32_16x16x32_bf16 v[64:67], a[40:43], v[32:35], v[64:67]\n" \
"buffer_load_dwordx4 v4, s[36:39], s61 offen offset:3072 sc1 lds\n" \
"v_mfma_f32_16x16x32_bf16 v[68:71], a[40:43], v[36:39], v[68:71]\n" \
"ds_read_b128 v[24:27], v12 offset:13312\n" \
"v_mfma_f32_16x16x32_bf16 v[72:75], a[168:171], v[32:35], v[72:75]\n" \
"ds_read_b128 v[28:31], v12 offset:29696\n" \
"v_mfma_f32_16x16x32_bf16 v[76:79], a[168:171], v[36:39], v[76:79]\n" \
"s_waitcnt lgkmcnt(4)\n" \
"v_mfma_f32_16x16x32_bf16 v[64:67], a[44:47], v[40:43], v[64:67]\n" \
"v_mfma_f32_16x16x32_bf16 v[68:71], a[44:47], v[44:47], v[68:71]\n" \
"ds_read_b128 v[32:35], v12 offset:14336\n" \
"v_mfma_f32_16x16x32_bf16 v[72:75], a[172:175], v[40:43], v[72:75]\n" \
"ds_read_b128 v[36:39], v12 offset:30720\n" \
"v_mfma_f32_16x16x32_bf16 v[76:79], a[172:175], v[44:47], v[76:79]\n" \
"s_waitcnt lgkmcnt(4)\n" \
"v_mfma_f32_16x16x32_bf16 v[64:67], a[48:51], v[16:19], v[64:67]\n" \
"v_mfma_f32_16x16x32_bf16 v[68:71], a[48:51], v[20:23], v[68:71]\n" \
"ds_read_b128 v[40:43], v12 offset:15360\n" \
"v_mfma_f32_16x16x32_bf16 v[72:75], a[176:179], v[16:19], v[72:75]\n" \
"ds_read_b128 v[44:47], v12 offset:31744\n" \
"v_mfma_f32_16x16x32_bf16 v[76:79], a[176:179], v[20:23], v[76:79]\n" \
"s_waitcnt lgkmcnt(4)\n" \
"v_mfma_f32_16x16x32_bf16 v[64:67], a[52:55], v[24:27], v[64:67]\n" \
"s_waitcnt vmcnt(12)\n" \
"s_barrier\n" \
"v_mfma_f32_16x16x32_bf16 v[68:71], a[52:55], v[28:31], v[68:71]\n" \
"ds_read_b128 v[16:19], v12 offset:32768\n" \
"v_mfma_f32_16x16x32_bf16 v[72:75], a[180:183], v[24:27], v[72:75]\n" \
"ds_read_b128 v[20:23], v12 offset:49152\n" \
"v_mfma_f32_16x16x32_bf16 v[76:79], a[180:183], v[28:31], v[76:79]\n" \
"s_waitcnt lgkmcnt(4)\n" \
"v_mfma_f32_16x16x32_bf16 v[64:67], a[56:59], v[32:35], v[64:67]\n" \
"v_mfma_f32_16x16x32_bf16 v[68:71], a[56:59], v[36:39], v[68:71]\n" \
"ds_read_b128 v[24:27], v12 offset:33792\n" \
"v_mfma_f32_16x16x32_bf16 v[72:75], a[184:187], v[32:35], v[72:75]\n" \
"ds_read_b128 v[28:31], v12 offset:50176\n" \
"v_mfma_f32_16x16x32_bf16 v[76:79], a[184:187], v[36:39], v[76:79]\n" \
"s_waitcnt lgkmcnt(4)\n" \
"v_mfma_f32_16x16x32_bf16 v[64:67], a[60:63], v[40:43], v[64:67]\n" \
"v_mfma_f32_16x16x32_bf16 v[68:71], a[60:63], v[44:47], v[68:71]\n" \
"ds_read_b128 v[32:35], v12 offset:34816\n" \
"v_mfma_f32_16x16x32_bf16 v[72:75], a[188:191], v[40:43], v[72:75]\n" \
"ds_read_b128 v[36:39], v12 offset:51200\n" \
"v_mfma_f32_16x16x32_bf16 v[76:79], a[188:191], v[44:47], v[76:79]\n" \
"s_waitcnt lgkmcnt(4)\n" \
"v_mfma_f32_16x16x32_bf16 v[64:67], a[64:67], v[16:19], v[64:67]\n" \
"s_add_u32 s61, s61, 28672\n" \
"s_add_u32 s56, s56, 0x20000\n" \
"s_add_u32 m0, s55, 32768\n" \
"s_nop 0\n" \
"buffer_load_dwordx4 v4, s[36:39], s61 offen offset:0 sc1 lds\n" \
"v_mfma_f32_16x16x32_bf16 v[68:71], a[64:67], v[20:23], v[68:71]\n" \
"ds_read_b128 v[40:43], v12 offset:35840\n" \
"v_mfma_f32_16x16x32_bf16 v[72:75], a[192:195], v[16:19], v[72:75]\n" \
"ds_read_b128 v[44:47], v12 offset:52224\n" \
"v_mfma_f32_16x16x32_bf16 v[76:79], a[192:195], v[20:23], v[76:79]\n" \
"s_waitcnt lgkmcnt(4)\n" \
"v_mfma_f32_16x16x32_bf16 v[64:67], a[68:71], v[24:27], v[64:67]\n" \
"buffer_load_dwordx4 v4, s[36:39], s61 offen offset:1024 sc1 lds\n" \
"v_mfma_f32_16x16x32_bf16 v[68:71], a[68:71], v[28:31], v[68:71]\n" \
"ds_read_b128 v[16:19], v12 offset:36864\n" \
"v_mfma_f32_16x16x32_bf16 v[72:75], a[196:199], v[24:27], v[72:75]\n" \
"ds_read_b128 v[20:23], v12 offset:53248\n" \
"v_mfma_f32_16x16x32_bf16 v[76:79], a[196:199], v[28:31], v[76:79]\n" \
"s_waitcnt lgkmcnt(4)\n" \
"v_mfma_f32_16x16x32_bf16 v[64:67], a[72:75], v[32:35], v[64:67]\n" \
"v_mfma_f32_16x16x32_bf16 v[68:71], a[72:75], v[36:39], v[68:71]\n" \
"ds_read_b128 v[24:27], v12 offset:37888\n" \
"v_mfma_f32_16x16x32_bf16 v[72:75], a[200:203], v[32:35], v[72:75]\n" \
"ds_read_b128 v[28:31], v12 offset:54272\n" \
"v_mfma_f32_16x16x32_bf16 v[76:79], a[200:203], v[36:39], v[76:79]\n" \
"s_waitcnt lgkmcnt(4)\n" \
"v_mfma_f32_16x16x32_bf16 v[64:67], a[76:79], v[40:43], v[64:67]\n" \
"buffer_load_dwordx4 v4, s[36:39], s61 offen offset:2048 sc1 lds\n" \
"v_mfma_f32_16x16x32_bf16 v[68:71], a[76:79], v[44:47], v[68:71]\n" \
"ds_read_b128 v[32:35], v12 offset:38912\n" \
"v_mfma_f32_16x16x32_bf16 v[72:75], a[204:207], v[40:43], v[72:75]\n" \
"ds_read_b128 v[36:39], v12 offset:55296\n" \
"v_mfma_f32_16x16x32_bf16 v[76:79], a[204:207], v[44:47], v[76:79]\n" \
"s_waitcnt lgkmcnt(4)\n" \
"v_mfma_f32_16x16x32_bf16 v[64:67], a[80:83], v[16:19], v[64:67]\n" \
"buffer_load_dwordx4 v4, s[36:39], s61 offen offset:3072 sc1 lds\n" \
"v_mfma_f32_16x16x32_bf16 v[68:71], a[80:83], v[20:23], v[68:71]\n" \
"ds_read_b128 v[40:43], v12 offset:39936\n" \
"v_mfma_f32_16x16x32_bf16 v[72:75], a[208:211], v[16:19], v[72:75]\n" \
"ds_read_b128 v[44:47], v12 offset:56320\n" \
"v_mfma_f32_16x16x32_bf16 v[76:79], a[208:211], v[20:23], v[76:79]\n" \
"s_waitcnt lgkmcnt(4)\n" \
"v_mfma_f32_16x16x32_bf16 v[64:67], a[84:87], v[24:27], v[64:67]\n" \
"v_mfma_f32_16x16x32_bf16 v[68:71], a[84:87], v[28:31], v[68:71]\n" \
"ds_read_b128 v[16:19], v12 offset:40960\n" \
"v_mfma_f32_16x16x32_bf16 v[72:75], a[212:215], v[24:27], v[72:75]\n" \
"ds_read_b128 v[20:23], v12 offset:57344\n" \
"v_mfma_f32_16x16x32_bf16 v[76:79], a[212:215], v[28:31], v[76:79]\n" \
"s_waitcnt lgkmcnt(4)\n" \
"v_mfma_f32_16x16x32_bf16 v[64:67], a[88:91], v[32:35], v[64:67]\n" \
"s_add_u32 m0, s55, 36864\n" \
"s_add_u32 s61, s61, 4096\n" \
"buffer_load_dwordx4 v4, s[36:39], s61 offen offset:0 sc1 lds\n" \
"v_mfma_f32_16x16x32_bf16 v[68:71], a[88:91], v[36:39], v[68:71]\n" \
"ds_read_b128 v[24:27], v12 offset:41984\n" \
"v_mfma_f32_16x16x32_bf16 v[72:75], a[216:219], v[32:35], v[72:75]\n" \
"ds_read_b128 v[28:31], v12 offset:58368\n" \
"v_mfma_f32_16x16x32_bf16 v[76:79], a[216:219], v[36:39], v[76:79]\n" \
"s_waitcnt lgkmcnt(4)\n" \
"v_mfma_f32_16x16x32_bf16 v[64:67], a[92:95], v[40:43], v[64:67]\n" \
"buffer_load_dwordx4 v4, s[36:39], s61 offen offset:1024 sc1 lds\n" \
"v_mfma_f32_16x16x32_bf16 v[68:71], a[92:95], v[44:47], v[68:71]\n" \
"ds_read_b128 v[32:35], v12 offset:43008\n" \
"v_mfma_f32_16x16x32_bf16 v[72:75], a[220:223], v[40:43], v[72:75]\n" \
"ds_read_b128 v[36:39], v12 offset:59392\n" \
"v_mfma_f32_16x16x32_bf16 v[76:79], a[220:223], v[44:47], v[76:79]\n" \
"s_waitcnt lgkmcnt(4)\n" \
"v_mfma_f32_16x16x32_bf16 v[64:67], a[96:99], v[16:19], v[64:67]\n" \
"v_mfma_f32_16x16x32_bf16 v[68:71], a[96:99], v[20:23], v[68:71]\n" \
"ds_read_b128 v[40:43], v12 offset:44032\n" \
"v_mfma_f32_16x16x32_bf16 v[72:75], a[224:227], v[16:19], v[72:75]\n" \
"ds_read_b128 v[44:47], v12 offset:60416\n" \
"v_mfma_f32_16x16x32_bf16 v[76:79], a[224:227], v[20:23], v[76:79]\n" \
"s_waitcnt lgkmcnt(4)\n" \
"v_mfma_f32_16x16x32_bf16 v[64:67], a[100:103], v[24:27], v[64:67]\n" \
"buffer_load_dwordx4 v4, s[36:39], s61 offen offset:2048 sc1 lds\n" \
"v_mfma_f32_16x16x32_bf16 v[68:71], a[100:103], v[28:31], v[68:71]\n" \
"ds_read_b128 v[16:19], v12 offset:45056\n" \
"v_mfma_f32_16x16x32_bf16 v[72:75], a[228:231], v[24:27], v[72:75]\n" \
"ds_read_b128 v[20:23], v12 offset:61440\n" \
"v_mfma_f32_16x16x32_bf16 v[76:79], a[228:231], v[28:31], v[76:79]\n" \
"s_waitcnt lgkmcnt(4)\n" \
"v_mfma_f32_16x16x32_bf16 v[64:67], a[104:107], v[32:35], v[64:67]\n" \
"buffer_load_dwordx4 v4, s[36:39], s61 offen offset:3072 sc1 lds\n" \
"v_mfma_f32_16x16x32_bf16 v[68:71], a[104:107], v[36:39], v[68:71]\n" \
"ds_read_b128 v[24:27], v12 offset:46080\n" \
"v_mfma_f32_16x16x32_bf16 v[72:75], a[232:235], v[32:35], v[72:75]\n" \
"ds_read_b128 v[28:31], v12 offset:62464\n" \
"v_mfma_f32_16x16x32_bf16 v[76:79], a[232:235], v[36:39], v[76:79]\n" \
"s_waitcnt lgkmcnt(4)\n" \
"v_mfma_f32_16x16x32_bf16 v[64:67], a[108:111], v[40:43], v[64:67]\n" \
"s_lshl_b32 s66, s59, s49\n" \
"s_lshr_b32 s65, s66, 8\n" \
"v_mfma_f32_16x16x32_bf16 v[68:71], a[108:111], v[44:47], v[68:71]\n" \
"ds_read_b128 v[32:35], v12 offset:47104\n" \
"v_mfma_f32_16x16x32_bf16 v[72:75], a[236:239], v[40:43], v[72:75]\n" \
"ds_read_b128 v[36:39], v12 offset:63488\n" \
"v_mfma_f32_16x16x32_bf16 v[76:79], a[236:239], v[44:47], v[76:79]\n" \
"s_waitcnt lgkmcnt(4)\n" \
"s_and_b32 s65, s65, 0x2000\n" \
"s_xor_b32 s66, s66, s65\n" \
"v_mfma_f32_16x16x32_bf16 v[64:67], a[112:115], v[16:19], v[64:67]\n" \
"v_mfma_f32_16x16x32_bf16 v[68:71], a[112:115], v[20:23], v[68:71]\n" \
"ds_read_b128 v[40:43], v12 offset:48128\n" \
"v_mfma_f32_16x16x32_bf16 v[72:75], a[240:243], v[16:19], v[72:75]\n" \
"ds_read_b128 v[44:47], v12 offset:64512\n" \
"v_mfma_f32_16x16x32_bf16 v[76:79], a[240:243], v[20:23], v[76:79]\n" \
"s_waitcnt lgkmcnt(4)\n" \
"v_mfma_f32_16x16x32_bf16 v[64:67], a[116:119], v[24:27], v[64:67]\n" \
"s_waitcnt vmcnt(8)\n" \
"s_barrier\n" \
"v_mfma_f32_16x16x32_bf16 v[68:71], a[116:119], v[28:31], v[68:71]\n" \
"ds_read_b128 v[16:19], v10 offset:0\n" \
"v_mfma_f32_16x16x32_bf16 v[72:75], a[244:247], v[24:27], v[72:75]\n" \
"ds_read_b128 v[20:23], v10 offset:16384\n" \
"v_mfma_f32_16x16x32_bf16 v[76:79], a[244:247], v[28:31], v[76:79]\n" \
"s_waitcnt lgkmcnt(4)\n" \
"buffer_store_dword v13, v6, s[36:39], 0 offen sc1\n" \
"v_mfma_f32_16x16x32_bf16 v[64:67], a[120:123], v[32:35], v[64:67]\n" \
"v_cmp_gt_u32_e64 vcc, 16, v15\n" \
"v_mfma_f32_16x16x32_bf16 v[68:71], a[120:123], v[36:39], v[68:71]\n" \
"ds_read_b128 v[24:27], v10 offset:1024\n" \
"v_mfma_f32_16x16x32_bf16 v[72:75], a[248:251], v[32:35], v[72:75]\n" \
"ds_read_b128 v[28:31], v10 offset:17408\n" \
"v_mfma_f32_16x16x32_bf16 v[76:79], a[248:251], v[36:39], v[76:79]\n" \
"s_waitcnt lgkmcnt(4)\n" \
"v_mfma_f32_16x16x32_bf16 v[64:67], a[124:127], v[40:43], v[64:67]\n" \
"s_cbranch_vccnz .Lp1qk_m1_%=\n" \
".Lp1qk_m1k_%=:\n" \
"v_mfma_f32_16x16x32_bf16 v[68:71], a[124:127], v[44:47], v[68:71]\n" \
"ds_read_b128 v[32:35], v10 offset:2048\n" \
"v_mfma_f32_16x16x32_bf16 v[72:75], a[252:255], v[40:43], v[72:75]\n" \
"ds_read_b128 v[36:39], v10 offset:18432\n" \
"v_mfma_f32_16x16x32_bf16 v[76:79], a[252:255], v[44:47], v[76:79]\n" \
"s_waitcnt lgkmcnt(4)\n" \
"s_xor_b32 s57, s66, s64\n" \
"s_add_u32 s58, s57, s50\n" \
"s_add_u32 s59, s59, 1\n" \
"v_mov_b32_e64 v5, v7\n" \
"v_mov_b32_e64 v6, v11\n" \
"s_cmp_ge_u32 s59, s47\n" \
"s_nop 0\n" \
"s_cbranch_scc0 .Lp1qk_loop_%=\n" \
".Lp1qk_out1_%=:\n" \
"s_nop 7\n" \
"s_nop 7\n" \
"v_mul_f32 v64, s51, v64\n" \
"v_mul_f32 v65, s51, v65\n" \
"v_mul_f32 v66, s51, v66\n" \
"v_mul_f32 v67, s51, v67\n" \
"v_mul_f32 v72, s51, v72\n" \
"v_mul_f32 v73, s51, v73\n" \
"v_mul_f32 v74, s51, v74\n" \
"v_mul_f32 v75, s51, v75\n" \
"v_cvt_pk_bf16_f32 v80, v64, v65\n" \
"v_cvt_pk_bf16_f32 v81, v66, v67\n" \
"v_cvt_pk_bf16_f32 v82, v72, v73\n" \
"v_cvt_pk_bf16_f32 v83, v74, v75\n" \
"buffer_store_dwordx4 v[80:83], v5, s[36:39], s57 offen sc1\n" \
"v_mul_f32 v68, s51, v68\n" \
"v_mul_f32 v69, s51, v69\n" \
"v_mul_f32 v70, s51, v70\n" \
"v_mul_f32 v71, s51, v71\n" \
"v_mul_f32 v76, s51, v76\n" \
"v_mul_f32 v77, s51, v77\n" \
"v_mul_f32 v78, s51, v78\n" \
"v_mul_f32 v79, s51, v79\n" \
"v_cvt_pk_bf16_f32 v84, v68, v69\n" \
"v_cvt_pk_bf16_f32 v85, v70, v71\n" \
"v_cvt_pk_bf16_f32 v86, v76, v77\n" \
"v_cvt_pk_bf16_f32 v87, v78, v79\n" \
"buffer_store_dwordx4 v[84:87], v5, s[36:39], s58 offen sc1\n" \
"s_branch .Lp1qk_end_%=\n" \
".Lp1qk_out0_%=:\n" \
"s_nop 7\n" \
"s_nop 7\n" \
"v_mul_f32 v48, s51, v48\n" \
"v_mul_f32 v49, s51, v49\n" \
"v_mul_f32 v50, s51, v50\n" \
"v_mul_f32 v51, s51, v51\n" \
"v_mul_f32 v56, s51, v56\n" \
"v_mul_f32 v57, s51, v57\n" \
"v_mul_f32 v58, s51, v58\n" \
"v_mul_f32 v59, s51, v59\n" \
"v_cvt_pk_bf16_f32 v80, v48, v49\n" \
"v_cvt_pk_bf16_f32 v81, v50, v51\n" \
"v_cvt_pk_bf16_f32 v82, v56, v57\n" \
"v_cvt_pk_bf16_f32 v83, v58, v59\n" \
"buffer_store_dwordx4 v[80:83], v5, s[36:39], s57 offen sc1\n" \
"v_mul_f32 v52, s51, v52\n" \
"v_mul_f32 v53, s51, v53\n" \
"v_mul_f32 v54, s51, v54\n" \
"v_mul_f32 v55, s51, v55\n" \
"v_mul_f32 v60, s51, v60\n" \
"v_mul_f32 v61, s51, v61\n" \
"v_mul_f32 v62, s51, v62\n" \
"v_mul_f32 v63, s51, v63\n" \
"v_cvt_pk_bf16_f32 v84, v52, v53\n" \
"v_cvt_pk_bf16_f32 v85, v54, v55\n" \
"v_cvt_pk_bf16_f32 v86, v60, v61\n" \
"v_cvt_pk_bf16_f32 v87, v62, v63\n" \
"buffer_store_dwordx4 v[84:87], v5, s[36:39], s58 offen sc1\n" \
".Lp1qk_end_%=:\n" \
"s_waitcnt vmcnt(0) lgkmcnt(0)\n" \
"s_barrier\n" \
"v_mov_b32 v13, s59\n" \
"buffer_store_dword v13, v6, s[36:39], 0 offen sc1\n" \
"s_waitcnt vmcnt(0) lgkmcnt(0)\n" \
"s_branch .Lp1qk_x_%=\n" \
".Lp1qk_l0_%=:\n" \
"s_nop 7\n" \
"s_nop 7\n" \
"s_waitcnt vmcnt(0) lgkmcnt(0)\n" \
"s_mov_b32 s62, 0\n" \
".Lp1qk_l0a_%=:\n" \
"buffer_load_dword v15, v14, s[36:39], s63 offen sc1\n" \
"s_waitcnt vmcnt(0)\n" \
"v_cmp_gt_u32 vcc, 16, v15\n" \
"s_nop 4\n" \
"s_cbranch_vccz .Lp1qk_l0k_%=\n" \
"s_sleep 4\n" \
"s_add_u32 s62, s62, 1\n" \
"s_cmp_lt_u32 s62, 0x10000\n" \
"s_cbranch_scc1 .Lp1qk_l0a_%=\n" \
"v_mov_b32 v15, 256\n" \
"buffer_store_dword v15, v15, s[36:39], 0 offen sc1\n" \
"s_waitcnt vmcnt(0) lgkmcnt(0)\n" \
"s_branch .Lp1qk_l0k_%=\n" \
".Lp1qk_l1_%=:\n" \
"s_nop 7\n" \
"s_nop 7\n" \
"s_waitcnt vmcnt(0) lgkmcnt(0)\n" \
"s_mov_b32 s62, 0\n" \
".Lp1qk_l1a_%=:\n" \
"buffer_load_dword v15, v14, s[36:39], s63 offen sc1\n" \
"s_waitcnt vmcnt(0)\n" \
"v_cmp_gt_u32 vcc, 16, v15\n" \
"s_nop 4\n" \
"s_cbranch_vccz .Lp1qk_l1k_%=\n" \
"s_sleep 4\n" \
"s_add_u32 s62, s62, 1\n" \
"s_cmp_lt_u32 s62, 0x10000\n" \
"s_cbranch_scc1 .Lp1qk_l1a_%=\n" \
"v_mov_b32 v15, 256\n" \
"buffer_store_dword v15, v15, s[36:39], 0 offen sc1\n" \
"s_waitcnt vmcnt(0) lgkmcnt(0)\n" \
"s_branch .Lp1qk_l1k_%=\n" \
".Lp1qk_m0_%=:\n" \
"s_nop 7\n" \
"s_nop 7\n" \
"s_waitcnt vmcnt(0) lgkmcnt(0)\n" \
"s_mov_b32 s62, 0\n" \
".Lp1qk_m0a_%=:\n" \
"buffer_load_dword v15, v14, s[36:39], s63 offen sc1\n" \
"s_waitcnt vmcnt(0)\n" \
"v_cmp_gt_u32 vcc, 16, v15\n" \
"s_nop 4\n" \
"s_cbranch_vccz .Lp1qk_m0k_%=\n" \
"s_sleep 4\n" \
"s_add_u32 s62, s62, 1\n" \
"s_cmp_lt_u32 s62, 0x10000\n" \
"s_cbranch_scc1 .Lp1qk_m0a_%=\n" \
"v_mov_b32 v15, 256\n" \
"buffer_store_dword v15, v15, s[36:39], 0 offen sc1\n" \
"s_waitcnt vmcnt(0) lgkmcnt(0)\n" \
"s_branch .Lp1qk_m0k_%=\n" \
".Lp1qk_m1_%=:\n" \
"s_nop 7\n" \
"s_nop 7\n" \
"s_waitcnt vmcnt(0) lgkmcnt(0)\n" \
"s_mov_b32 s62, 0\n" \
".Lp1qk_m1a_%=:\n" \
"buffer_load_dword v15, v14, s[36:39], s63 offen sc1\n" \
"s_waitcnt vmcnt(0)\n" \
"v_cmp_gt_u32 vcc, 16, v15\n" \
"s_nop 4\n" \
"s_cbranch_vccz .Lp1qk_m1k_%=\n" \
"s_sleep 4\n" \
"s_add_u32 s62, s62, 1\n" \
"s_cmp_lt_u32 s62, 0x10000\n" \
"s_cbranch_scc1 .Lp1qk_m1a_%=\n" \
"v_mov_b32 v15, 256\n" \
"buffer_store_dword v15, v15, s[36:39], 0 offen sc1\n" \
"s_waitcnt vmcnt(0) lgkmcnt(0)\n" \
"s_branch .Lp1qk_m1k_%=\n" \
".Lp1qk_x_%=:\n"
#define ASM_P1_V \
"s_nop 7\n" \
"s_nop 7\n" \
"s_lshr_b32 s52, s48, 1\n" \
"s_lshl_b32 s52, s52, 13\n" \
"s_and_b32 s53, s48, 1\n" \
"s_lshl_b32 s53, s53, 10\n" \
"s_add_u32 s52, s52, s53\n" \
"s_mov_b32 s54, 0\n" \
"s_add_u32 m0, s52, 0\n" \
"s_nop 0\n" \
"buffer_load_dwordx4 v1, s[40:43], s54 offen nt lds\n" \
"s_add_u32 s54, s54, s44\n" \
"s_add_u32 m0, s52, 2048\n" \
"s_nop 0\n" \
"buffer_load_dwordx4 v1, s[40:43], s54 offen nt lds\n" \
"s_add_u32 s54, s54, s44\n" \
"s_add_u32 m0, s52, 4096\n" \
"s_nop 0\n" \
"buffer_load_dwordx4 v1, s[40:43], s54 offen nt lds\n" \
"s_add_u32 s54, s54, s44\n" \
"s_add_u32 m0, s52, 6144\n" \
"s_nop 0\n" \
"buffer_load_dwordx4 v1, s[40:43], s54 offen nt lds\n" \
"s_add_u32 s54, s54, s44\n" \
"s_add_u32 m0, s52, 16384\n" \
"s_nop 0\n" \
"buffer_load_dwordx4 v1, s[40:43], s54 offen nt lds\n" \
"s_add_u32 s54, s54, s44\n" \
"s_add_u32 m0, s52, 18432\n" \
"s_nop 0\n" \
"buffer_load_dwordx4 v1, s[40:43], s54 offen nt lds\n" \
"s_add_u32 s54, s54, s44\n" \
"s_add_u32 m0, s52, 20480\n" \
"s_nop 0\n" \
"buffer_load_dwordx4 v1, s[40:43], s54 offen nt lds\n" \
"s_add_u32 s54, s54, s44\n" \
"s_add_u32 m0, s52, 22528\n" \
"s_nop 0\n" \
"buffer_load_dwordx4 v1, s[40:43], s54 offen nt lds\n" \
"s_add_u32 s54, s54, s44\n" \
"s_add_u32 m0, s52, 32768\n" \
"s_nop 0\n" \
"buffer_load_dwordx4 v1, s[40:43], s54 offen nt lds\n" \
"s_add_u32 s54, s54, s44\n" \
"s_add_u32 m0, s52, 34816\n" \
"s_nop 0\n" \
"buffer_load_dwordx4 v1, s[40:43], s54 offen nt lds\n" \
"s_add_u32 s54, s54, s44\n" \
"s_add_u32 m0, s52, 36864\n" \
"s_nop 0\n" \
"buffer_load_dwordx4 v1, s[40:43], s54 offen nt lds\n" \
"s_add_u32 s54, s54, s44\n" \
"s_add_u32 m0, s52, 38912\n" \
"s_nop 0\n" \
"buffer_load_dwordx4 v1, s[40:43], s54 offen nt lds\n" \
"s_add_u32 s54, s54, s44\n" \
"s_add_u32 m0, s52, 49152\n" \
"s_nop 0\n" \
"buffer_load_dwordx4 v1, s[40:43], s54 offen nt lds\n" \
"s_add_u32 s54, s54, s44\n" \
"s_add_u32 m0, s52, 51200\n" \
"s_nop 0\n" \
"buffer_load_dwordx4 v1, s[40:43], s54 offen nt lds\n" \
"s_add_u32 s54, s54, s44\n" \
"s_add_u32 m0, s52, 53248\n" \
"s_nop 0\n" \
"buffer_load_dwordx4 v1, s[40:43], s54 offen nt lds\n" \
"s_add_u32 s54, s54, s44\n" \
"s_add_u32 m0, s52, 55296\n" \
"s_nop 0\n" \
"buffer_load_dwordx4 v1, s[40:43], s54 offen nt lds\n" \
"s_add_u32 s54, s54, s44\n" \
"s_add_u32 m0, s52, 65536\n" \
"s_nop 0\n" \
"buffer_load_dwordx4 v1, s[40:43], s54 offen nt lds\n" \
"s_add_u32 s54, s54, s44\n" \
"s_add_u32 m0, s52, 67584\n" \
"s_nop 0\n" \
"buffer_load_dwordx4 v1, s[40:43], s54 offen nt lds\n" \
"s_add_u32 s54, s54, s44\n" \
"s_add_u32 m0, s52, 69632\n" \
"s_nop 0\n" \
"buffer_load_dwordx4 v1, s[40:43], s54 offen nt lds\n" \
"s_add_u32 s54, s54, s44\n" \
"s_add_u32 m0, s52, 71680\n" \
"s_nop 0\n" \
"buffer_load_dwordx4 v1, s[40:43], s54 offen nt lds\n" \
"s_add_u32 s54, s54, s44\n" \
"s_add_u32 m0, s52, 81920\n" \
"s_nop 0\n" \
"buffer_load_dwordx4 v1, s[40:43], s54 offen nt lds\n" \
"s_add_u32 s54, s54, s44\n" \
"s_add_u32 m0, s52, 83968\n" \
"s_nop 0\n" \
"buffer_load_dwordx4 v1, s[40:43], s54 offen nt lds\n" \
"s_add_u32 s54, s54, s44\n" \
"s_add_u32 m0, s52, 86016\n" \
"s_nop 0\n" \
"buffer_load_dwordx4 v1, s[40:43], s54 offen nt lds\n" \
"s_add_u32 s54, s54, s44\n" \
"s_add_u32 m0, s52, 88064\n" \
"s_nop 0\n" \
"buffer_load_dwordx4 v1, s[40:43], s54 offen nt lds\n" \
"s_add_u32 s54, s54, s44\n" \
"s_add_u32 m0, s52, 98304\n" \
"s_nop 0\n" \
"buffer_load_dwordx4 v1, s[40:43], s54 offen nt lds\n" \
"s_add_u32 s54, s54, s44\n" \
"s_add_u32 m0, s52, 100352\n" \
"s_nop 0\n" \
"buffer_load_dwordx4 v1, s[40:43], s54 offen nt lds\n" \
"s_add_u32 s54, s54, s44\n" \
"s_add_u32 m0, s52, 102400\n" \
"s_nop 0\n" \
"buffer_load_dwordx4 v1, s[40:43], s54 offen nt lds\n" \
"s_add_u32 s54, s54, s44\n" \
"s_add_u32 m0, s52, 104448\n" \
"s_nop 0\n" \
"buffer_load_dwordx4 v1, s[40:43], s54 offen nt lds\n" \
"s_add_u32 s54, s54, s44\n" \
"s_add_u32 m0, s52, 114688\n" \
"s_nop 0\n" \
"buffer_load_dwordx4 v1, s[40:43], s54 offen nt lds\n" \
"s_add_u32 s54, s54, s44\n" \
"s_add_u32 m0, s52, 116736\n" \
"s_nop 0\n" \
"buffer_load_dwordx4 v1, s[40:43], s54 offen nt lds\n" \
"s_add_u32 s54, s54, s44\n" \
"s_add_u32 m0, s52, 118784\n" \
"s_nop 0\n" \
"buffer_load_dwordx4 v1, s[40:43], s54 offen nt lds\n" \
"s_add_u32 s54, s54, s44\n" \
"s_add_u32 m0, s52, 120832\n" \
"s_nop 0\n" \
"buffer_load_dwordx4 v1, s[40:43], s54 offen nt lds\n" \
"s_add_u32 s54, s54, s44\n" \
"v_add_u32 v8, 0x10000, v2\n" \
"v_add_u32 v9, 0x10000, v3\n" \
"s_waitcnt vmcnt(28)\n" \
"s_barrier\n" \
"ds_read_b64_tr_b16 a[0:1], v2 offset:0\n" \
"ds_read_b64_tr_b16 a[2:3], v2 offset:512\n" \
"ds_read_b64_tr_b16 a[128:129], v3 offset:0\n" \
"ds_read_b64_tr_b16 a[130:131], v3 offset:512\n" \
"ds_read_b64_tr_b16 a[4:5], v2 offset:4096\n" \
"ds_read_b64_tr_b16 a[6:7], v2 offset:4608\n" \
"ds_read_b64_tr_b16 a[132:133], v3 offset:4096\n" \
"ds_read_b64_tr_b16 a[134:135], v3 offset:4608\n" \
"s_waitcnt lgkmcnt(0)\n" \
"s_barrier\n" \
"s_add_u32 m0, s52, 0\n" \
"s_nop 0\n" \
"buffer_load_dwordx4 v1, s[40:43], s54 offen nt lds\n" \
"s_add_u32 s54, s54, s44\n" \
"s_add_u32 m0, s52, 2048\n" \
"s_nop 0\n" \
"buffer_load_dwordx4 v1, s[40:43], s54 offen nt lds\n" \
"s_add_u32 s54, s54, s44\n" \
"s_add_u32 m0, s52, 4096\n" \
"s_nop 0\n" \
"buffer_load_dwordx4 v1, s[40:43], s54 offen nt lds\n" \
"s_add_u32 s54, s54, s44\n" \
"s_add_u32 m0, s52, 6144\n" \
"s_nop 0\n" \
"buffer_load_dwordx4 v1, s[40:43], s54 offen nt lds\n" \
"s_add_u32 s54, s54, s44\n" \
"s_waitcnt vmcnt(28)\n" \
"s_barrier\n" \
"ds_read_b64_tr_b16 a[8:9], v2 offset:16384\n" \
"ds_read_b64_tr_b16 a[10:11], v2 offset:16896\n" \
"ds_read_b64_tr_b16 a[136:137], v3 offset:16384\n" \
"ds_read_b64_tr_b16 a[138:139], v3 offset:16896\n" \
"ds_read_b64_tr_b16 a[12:13], v2 offset:20480\n" \
"ds_read_b64_tr_b16 a[14:15], v2 offset:20992\n" \
"ds_read_b64_tr_b16 a[140:141], v3 offset:20480\n" \
"ds_read_b64_tr_b16 a[142:143], v3 offset:20992\n" \
"s_waitcnt lgkmcnt(0)\n" \
"s_barrier\n" \
"s_add_u32 m0, s52, 16384\n" \
"s_nop 0\n" \
"buffer_load_dwordx4 v1, s[40:43], s54 offen nt lds\n" \
"s_add_u32 s54, s54, s44\n" \
"s_add_u32 m0, s52, 18432\n" \
"s_nop 0\n" \
"buffer_load_dwordx4 v1, s[40:43], s54 offen nt lds\n" \
"s_add_u32 s54, s54, s44\n" \
"s_add_u32 m0, s52, 20480\n" \
"s_nop 0\n" \
"buffer_load_dwordx4 v1, s[40:43], s54 offen nt lds\n" \
"s_add_u32 s54, s54, s44\n" \
"s_add_u32 m0, s52, 22528\n" \
"s_nop 0\n" \
"buffer_load_dwordx4 v1, s[40:43], s54 offen nt lds\n" \
"s_add_u32 s54, s54, s44\n" \
"s_waitcnt vmcnt(28)\n" \
"s_barrier\n" \
"ds_read_b64_tr_b16 a[16:17], v2 offset:32768\n" \
"ds_read_b64_tr_b16 a[18:19], v2 offset:33280\n" \
"ds_read_b64_tr_b16 a[144:145], v3 offset:32768\n" \
"ds_read_b64_tr_b16 a[146:147], v3 offset:33280\n" \
"ds_read_b64_tr_b16 a[20:21], v2 offset:36864\n" \
"ds_read_b64_tr_b16 a[22:23], v2 offset:37376\n" \
"ds_read_b64_tr_b16 a[148:149], v3 offset:36864\n" \
"ds_read_b64_tr_b16 a[150:151], v3 offset:37376\n" \
"s_waitcnt lgkmcnt(0)\n" \
"s_barrier\n" \
"s_add_u32 m0, s52, 32768\n" \
"s_nop 0\n" \
"buffer_load_dwordx4 v1, s[40:43], s54 offen nt lds\n" \
"s_add_u32 s54, s54, s44\n" \
"s_add_u32 m0, s52, 34816\n" \
"s_nop 0\n" \
"buffer_load_dwordx4 v1, s[40:43], s54 offen nt lds\n" \
"s_add_u32 s54, s54, s44\n" \
"s_add_u32 m0, s52, 36864\n" \
"s_nop 0\n" \
"buffer_load_dwordx4 v1, s[40:43], s54 offen nt lds\n" \
"s_add_u32 s54, s54, s44\n" \
"s_add_u32 m0, s52, 38912\n" \
"s_nop 0\n" \
"buffer_load_dwordx4 v1, s[40:43], s54 offen nt lds\n" \
"s_add_u32 s54, s54, s44\n" \
"s_waitcnt vmcnt(28)\n" \
"s_barrier\n" \
"ds_read_b64_tr_b16 a[24:25], v2 offset:49152\n" \
"ds_read_b64_tr_b16 a[26:27], v2 offset:49664\n" \
"ds_read_b64_tr_b16 a[152:153], v3 offset:49152\n" \
"ds_read_b64_tr_b16 a[154:155], v3 offset:49664\n" \
"ds_read_b64_tr_b16 a[28:29], v2 offset:53248\n" \
"ds_read_b64_tr_b16 a[30:31], v2 offset:53760\n" \
"ds_read_b64_tr_b16 a[156:157], v3 offset:53248\n" \
"ds_read_b64_tr_b16 a[158:159], v3 offset:53760\n" \
"s_waitcnt lgkmcnt(0)\n" \
"s_barrier\n" \
"s_add_u32 m0, s52, 49152\n" \
"s_nop 0\n" \
"buffer_load_dwordx4 v1, s[40:43], s54 offen nt lds\n" \
"s_add_u32 s54, s54, s44\n" \
"s_add_u32 m0, s52, 51200\n" \
"s_nop 0\n" \
"buffer_load_dwordx4 v1, s[40:43], s54 offen nt lds\n" \
"s_add_u32 s54, s54, s44\n" \
"s_add_u32 m0, s52, 53248\n" \
"s_nop 0\n" \
"buffer_load_dwordx4 v1, s[40:43], s54 offen nt lds\n" \
"s_add_u32 s54, s54, s44\n" \
"s_add_u32 m0, s52, 55296\n" \
"s_nop 0\n" \
"buffer_load_dwordx4 v1, s[40:43], s54 offen nt lds\n" \
"s_add_u32 s54, s54, s44\n" \
"s_waitcnt vmcnt(28)\n" \
"s_barrier\n" \
"ds_read_b64_tr_b16 a[32:33], v8 offset:0\n" \
"ds_read_b64_tr_b16 a[34:35], v8 offset:512\n" \
"ds_read_b64_tr_b16 a[160:161], v9 offset:0\n" \
"ds_read_b64_tr_b16 a[162:163], v9 offset:512\n" \
"ds_read_b64_tr_b16 a[36:37], v8 offset:4096\n" \
"ds_read_b64_tr_b16 a[38:39], v8 offset:4608\n" \
"ds_read_b64_tr_b16 a[164:165], v9 offset:4096\n" \
"ds_read_b64_tr_b16 a[166:167], v9 offset:4608\n" \
"s_waitcnt lgkmcnt(0)\n" \
"s_barrier\n" \
"s_add_u32 m0, s52, 65536\n" \
"s_nop 0\n" \
"buffer_load_dwordx4 v1, s[40:43], s54 offen nt lds\n" \
"s_add_u32 s54, s54, s44\n" \
"s_add_u32 m0, s52, 67584\n" \
"s_nop 0\n" \
"buffer_load_dwordx4 v1, s[40:43], s54 offen nt lds\n" \
"s_add_u32 s54, s54, s44\n" \
"s_add_u32 m0, s52, 69632\n" \
"s_nop 0\n" \
"buffer_load_dwordx4 v1, s[40:43], s54 offen nt lds\n" \
"s_add_u32 s54, s54, s44\n" \
"s_add_u32 m0, s52, 71680\n" \
"s_nop 0\n" \
"buffer_load_dwordx4 v1, s[40:43], s54 offen nt lds\n" \
"s_add_u32 s54, s54, s44\n" \
"s_waitcnt vmcnt(28)\n" \
"s_barrier\n" \
"ds_read_b64_tr_b16 a[40:41], v8 offset:16384\n" \
"ds_read_b64_tr_b16 a[42:43], v8 offset:16896\n" \
"ds_read_b64_tr_b16 a[168:169], v9 offset:16384\n" \
"ds_read_b64_tr_b16 a[170:171], v9 offset:16896\n" \
"ds_read_b64_tr_b16 a[44:45], v8 offset:20480\n" \
"ds_read_b64_tr_b16 a[46:47], v8 offset:20992\n" \
"ds_read_b64_tr_b16 a[172:173], v9 offset:20480\n" \
"ds_read_b64_tr_b16 a[174:175], v9 offset:20992\n" \
"s_waitcnt lgkmcnt(0)\n" \
"s_barrier\n" \
"s_add_u32 m0, s52, 81920\n" \
"s_nop 0\n" \
"buffer_load_dwordx4 v1, s[40:43], s54 offen nt lds\n" \
"s_add_u32 s54, s54, s44\n" \
"s_add_u32 m0, s52, 83968\n" \
"s_nop 0\n" \
"buffer_load_dwordx4 v1, s[40:43], s54 offen nt lds\n" \
"s_add_u32 s54, s54, s44\n" \
"s_add_u32 m0, s52, 86016\n" \
"s_nop 0\n" \
"buffer_load_dwordx4 v1, s[40:43], s54 offen nt lds\n" \
"s_add_u32 s54, s54, s44\n" \
"s_add_u32 m0, s52, 88064\n" \
"s_nop 0\n" \
"buffer_load_dwordx4 v1, s[40:43], s54 offen nt lds\n" \
"s_add_u32 s54, s54, s44\n" \
"s_waitcnt vmcnt(28)\n" \
"s_barrier\n" \
"ds_read_b64_tr_b16 a[48:49], v8 offset:32768\n" \
"ds_read_b64_tr_b16 a[50:51], v8 offset:33280\n" \
"ds_read_b64_tr_b16 a[176:177], v9 offset:32768\n" \
"ds_read_b64_tr_b16 a[178:179], v9 offset:33280\n" \
"ds_read_b64_tr_b16 a[52:53], v8 offset:36864\n" \
"ds_read_b64_tr_b16 a[54:55], v8 offset:37376\n" \
"ds_read_b64_tr_b16 a[180:181], v9 offset:36864\n" \
"ds_read_b64_tr_b16 a[182:183], v9 offset:37376\n" \
"s_waitcnt lgkmcnt(0)\n" \
"s_barrier\n" \
"s_add_u32 m0, s52, 98304\n" \
"s_nop 0\n" \
"buffer_load_dwordx4 v1, s[40:43], s54 offen nt lds\n" \
"s_add_u32 s54, s54, s44\n" \
"s_add_u32 m0, s52, 100352\n" \
"s_nop 0\n" \
"buffer_load_dwordx4 v1, s[40:43], s54 offen nt lds\n" \
"s_add_u32 s54, s54, s44\n" \
"s_add_u32 m0, s52, 102400\n" \
"s_nop 0\n" \
"buffer_load_dwordx4 v1, s[40:43], s54 offen nt lds\n" \
"s_add_u32 s54, s54, s44\n" \
"s_add_u32 m0, s52, 104448\n" \
"s_nop 0\n" \
"buffer_load_dwordx4 v1, s[40:43], s54 offen nt lds\n" \
"s_add_u32 s54, s54, s44\n" \
"s_waitcnt vmcnt(28)\n" \
"s_barrier\n" \
"ds_read_b64_tr_b16 a[56:57], v8 offset:49152\n" \
"ds_read_b64_tr_b16 a[58:59], v8 offset:49664\n" \
"ds_read_b64_tr_b16 a[184:185], v9 offset:49152\n" \
"ds_read_b64_tr_b16 a[186:187], v9 offset:49664\n" \
"ds_read_b64_tr_b16 a[60:61], v8 offset:53248\n" \
"ds_read_b64_tr_b16 a[62:63], v8 offset:53760\n" \
"ds_read_b64_tr_b16 a[188:189], v9 offset:53248\n" \
"ds_read_b64_tr_b16 a[190:191], v9 offset:53760\n" \
"s_waitcnt lgkmcnt(0)\n" \
"s_barrier\n" \
"s_add_u32 m0, s52, 114688\n" \
"s_nop 0\n" \
"buffer_load_dwordx4 v1, s[40:43], s54 offen nt lds\n" \
"s_add_u32 s54, s54, s44\n" \
"s_add_u32 m0, s52, 116736\n" \
"s_nop 0\n" \
"buffer_load_dwordx4 v1, s[40:43], s54 offen nt lds\n" \
"s_add_u32 s54, s54, s44\n" \
"s_add_u32 m0, s52, 118784\n" \
"s_nop 0\n" \
"buffer_load_dwordx4 v1, s[40:43], s54 offen nt lds\n" \
"s_add_u32 s54, s54, s44\n" \
"s_add_u32 m0, s52, 120832\n" \
"s_nop 0\n" \
"buffer_load_dwordx4 v1, s[40:43], s54 offen nt lds\n" \
"s_add_u32 s54, s54, s44\n" \
"s_waitcnt vmcnt(28)\n" \
"s_barrier\n" \
"ds_read_b64_tr_b16 a[64:65], v2 offset:0\n" \
"ds_read_b64_tr_b16 a[66:67], v2 offset:512\n" \
"ds_read_b64_tr_b16 a[192:193], v3 offset:0\n" \
"ds_read_b64_tr_b16 a[194:195], v3 offset:512\n" \
"ds_read_b64_tr_b16 a[68:69], v2 offset:4096\n" \
"ds_read_b64_tr_b16 a[70:71], v2 offset:4608\n" \
"ds_read_b64_tr_b16 a[196:197], v3 offset:4096\n" \
"ds_read_b64_tr_b16 a[198:199], v3 offset:4608\n" \
"s_waitcnt vmcnt(24)\n" \
"s_barrier\n" \
"ds_read_b64_tr_b16 a[72:73], v2 offset:16384\n" \
"ds_read_b64_tr_b16 a[74:75], v2 offset:16896\n" \
"ds_read_b64_tr_b16 a[200:201], v3 offset:16384\n" \
"ds_read_b64_tr_b16 a[202:203], v3 offset:16896\n" \
"ds_read_b64_tr_b16 a[76:77], v2 offset:20480\n" \
"ds_read_b64_tr_b16 a[78:79], v2 offset:20992\n" \
"ds_read_b64_tr_b16 a[204:205], v3 offset:20480\n" \
"ds_read_b64_tr_b16 a[206:207], v3 offset:20992\n" \
"s_waitcnt vmcnt(20)\n" \
"s_barrier\n" \
"ds_read_b64_tr_b16 a[80:81], v2 offset:32768\n" \
"ds_read_b64_tr_b16 a[82:83], v2 offset:33280\n" \
"ds_read_b64_tr_b16 a[208:209], v3 offset:32768\n" \
"ds_read_b64_tr_b16 a[210:211], v3 offset:33280\n" \
"ds_read_b64_tr_b16 a[84:85], v2 offset:36864\n" \
"ds_read_b64_tr_b16 a[86:87], v2 offset:37376\n" \
"ds_read_b64_tr_b16 a[212:213], v3 offset:36864\n" \
"ds_read_b64_tr_b16 a[214:215], v3 offset:37376\n" \
"s_waitcnt vmcnt(16)\n" \
"s_barrier\n" \
"ds_read_b64_tr_b16 a[88:89], v2 offset:49152\n" \
"ds_read_b64_tr_b16 a[90:91], v2 offset:49664\n" \
"ds_read_b64_tr_b16 a[216:217], v3 offset:49152\n" \
"ds_read_b64_tr_b16 a[218:219], v3 offset:49664\n" \
"ds_read_b64_tr_b16 a[92:93], v2 offset:53248\n" \
"ds_read_b64_tr_b16 a[94:95], v2 offset:53760\n" \
"ds_read_b64_tr_b16 a[220:221], v3 offset:53248\n" \
"ds_read_b64_tr_b16 a[222:223], v3 offset:53760\n" \
"s_waitcnt vmcnt(12)\n" \
"s_barrier\n" \
"ds_read_b64_tr_b16 a[96:97], v8 offset:0\n" \
"ds_read_b64_tr_b16 a[98:99], v8 offset:512\n" \
"ds_read_b64_tr_b16 a[224:225], v9 offset:0\n" \
"ds_read_b64_tr_b16 a[226:227], v9 offset:512\n" \
"ds_read_b64_tr_b16 a[100:101], v8 offset:4096\n" \
"ds_read_b64_tr_b16 a[102:103], v8 offset:4608\n" \
"ds_read_b64_tr_b16 a[228:229], v9 offset:4096\n" \
"ds_read_b64_tr_b16 a[230:231], v9 offset:4608\n" \
"s_waitcnt vmcnt(8)\n" \
"s_barrier\n" \
"ds_read_b64_tr_b16 a[104:105], v8 offset:16384\n" \
"ds_read_b64_tr_b16 a[106:107], v8 offset:16896\n" \
"ds_read_b64_tr_b16 a[232:233], v9 offset:16384\n" \
"ds_read_b64_tr_b16 a[234:235], v9 offset:16896\n" \
"ds_read_b64_tr_b16 a[108:109], v8 offset:20480\n" \
"ds_read_b64_tr_b16 a[110:111], v8 offset:20992\n" \
"ds_read_b64_tr_b16 a[236:237], v9 offset:20480\n" \
"ds_read_b64_tr_b16 a[238:239], v9 offset:20992\n" \
"s_waitcnt vmcnt(4)\n" \
"s_barrier\n" \
"ds_read_b64_tr_b16 a[112:113], v8 offset:32768\n" \
"ds_read_b64_tr_b16 a[114:115], v8 offset:33280\n" \
"ds_read_b64_tr_b16 a[240:241], v9 offset:32768\n" \
"ds_read_b64_tr_b16 a[242:243], v9 offset:33280\n" \
"ds_read_b64_tr_b16 a[116:117], v8 offset:36864\n" \
"ds_read_b64_tr_b16 a[118:119], v8 offset:37376\n" \
"ds_read_b64_tr_b16 a[244:245], v9 offset:36864\n" \
"ds_read_b64_tr_b16 a[246:247], v9 offset:37376\n" \
"s_waitcnt vmcnt(0)\n" \
"s_barrier\n" \
"ds_read_b64_tr_b16 a[120:121], v8 offset:49152\n" \
"ds_read_b64_tr_b16 a[122:123], v8 offset:49664\n" \
"ds_read_b64_tr_b16 a[248:249], v9 offset:49152\n" \
"ds_read_b64_tr_b16 a[250:251], v9 offset:49664\n" \
"ds_read_b64_tr_b16 a[124:125], v8 offset:53248\n" \
"ds_read_b64_tr_b16 a[126:127], v8 offset:53760\n" \
"ds_read_b64_tr_b16 a[252:253], v9 offset:53248\n" \
"ds_read_b64_tr_b16 a[254:255], v9 offset:53760\n" \
"s_waitcnt lgkmcnt(0)\n" \
"s_barrier\n" \
"v_mov_b32 v14, 4096\n" \
"s_mov_b32 s59, s46\n" \
"s_add_u32 s63, s59, 0\n" \
"s_min_u32 s63, s63, 63\n" \
"s_lshr_b32 s63, s63, 3\n" \
"s_lshl_b32 s63, s63, 7\n" \
"s_branch .Lp1v_l0_%=\n" \
".Lp1v_l0k_%=:\n" \
"s_add_u32 s63, s59, 1\n" \
"s_min_u32 s63, s63, 63\n" \
"s_lshr_b32 s63, s63, 3\n" \
"s_lshl_b32 s63, s63, 7\n" \
"s_branch .Lp1v_l1_%=\n" \
".Lp1v_l1k_%=:\n" \
"s_lshl_b32 s55, s48, 13\n" \
"s_lshl_b32 s56, s46, 17\n" \
"s_add_u32 s56, s56, s45\n" \
"s_lshl_b32 s60, s47, 17\n" \
"s_sub_u32 s60, s60, 0x20000\n" \
"s_add_u32 s60, s60, s45\n" \
"s_mov_b32 s57, 0\n" \
"s_mov_b32 s58, 0\n" \
"v_mov_b32 v7, v5\n" \
"v_mov_b32 v5, 0x7ffffff0\n" \
"v_mov_b32 v10, v0\n" \
"v_mov_b32 v11, v6\n" \
"v_mov_b32 v6, 0x7ffffff0\n" \
"s_mov_b32 s59, s46\n" \
"s_min_u32 s61, s56, s60\n" \
"s_lshr_b32 s65, s61, 8\n" \
"s_and_b32 s65, s65, 0x2000\n" \
"s_xor_b32 s61, s61, s65\n" \
"s_add_u32 m0, s55, 0\n" \
"s_nop 0\n" \
"buffer_load_dwordx4 v4, s[36:39], s61 offen offset:0 sc1 lds\n" \
"buffer_load_dwordx4 v4, s[36:39], s61 offen offset:1024 sc1 lds\n" \
"buffer_load_dwordx4 v4, s[36:39], s61 offen offset:2048 sc1 lds\n" \
"buffer_load_dwordx4 v4, s[36:39], s61 offen offset:3072 sc1 lds\n" \
"s_add_u32 m0, s55, 4096\n" \
"s_add_u32 s61, s61, 4096\n" \
"buffer_load_dwordx4 v4, s[36:39], s61 offen offset:0 sc1 lds\n" \
"buffer_load_dwordx4 v4, s[36:39], s61 offen offset:1024 sc1 lds\n" \
"buffer_load_dwordx4 v4, s[36:39], s61 offen offset:2048 sc1 lds\n" \
"buffer_load_dwordx4 v4, s[36:39], s61 offen offset:3072 sc1 lds\n" \
"s_add_u32 s61, s61, 28672\n" \
"s_add_u32 s56, s56, 0x20000\n" \
"s_add_u32 m0, s55, 32768\n" \
"s_nop 0\n" \
"buffer_load_dwordx4 v4, s[36:39], s61 offen offset:0 sc1 lds\n" \
"buffer_load_dwordx4 v4, s[36:39], s61 offen offset:1024 sc1 lds\n" \
"buffer_load_dwordx4 v4, s[36:39], s61 offen offset:2048 sc1 lds\n" \
"buffer_load_dwordx4 v4, s[36:39], s61 offen offset:3072 sc1 lds\n" \
"s_add_u32 m0, s55, 36864\n" \
"s_add_u32 s61, s61, 4096\n" \
"buffer_load_dwordx4 v4, s[36:39], s61 offen offset:0 sc1 lds\n" \
"buffer_load_dwordx4 v4, s[36:39], s61 offen offset:1024 sc1 lds\n" \
"buffer_load_dwordx4 v4, s[36:39], s61 offen offset:2048 sc1 lds\n" \
"buffer_load_dwordx4 v4, s[36:39], s61 offen offset:3072 sc1 lds\n" \
"s_mov_b32 s70, 0\n" \
"s_mov_b32 s72, 0\n" \
"s_waitcnt vmcnt(0) lgkmcnt(0)\n" \
"s_barrier\n" \
"v_add_u32 v12, 0x10000, v10\n" \
"ds_read_b128 v[16:19], v10 offset:0\n" \
"ds_read_b128 v[20:23], v10 offset:16384\n" \
"ds_read_b128 v[24:27], v10 offset:1024\n" \
"ds_read_b128 v[28:31], v10 offset:17408\n" \
"ds_read_b128 v[32:35], v10 offset:2048\n" \
"ds_read_b128 v[36:39], v10 offset:18432\n" \
"s_waitcnt lgkmcnt(4)\n" \
"s_nop 7\n" \
"s_nop 7\n" \
".p2align 6\n" \
".Lp1v_loop_%=:\n" \
"v_mfma_f32_16x16x32_bf16 v[48:51], v[16:19], a[0:3], 0\n" \
"s_min_u32 s61, s56, s60\n" \
"s_lshr_b32 s65, s61, 8\n" \
"s_and_b32 s65, s65, 0x2000\n" \
"s_xor_b32 s61, s61, s65\n" \
"s_add_u32 m0, s55, 65536\n" \
"s_nop 0\n" \
"buffer_load_dwordx4 v4, s[36:39], s61 offen offset:0 sc1 lds\n" \
"v_mov_b32_e64 v13, s59\n" \
"v_mfma_f32_16x16x32_bf16 v[52:55], v[20:23], a[0:3], 0\n" \
"ds_read_b128 v[40:43], v10 offset:3072\n" \
"s_nop 0\n" \
"v_mfma_f32_16x16x32_bf16 v[56:59], v[16:19], a[128:131], 0\n" \
"ds_read_b128 v[44:47], v10 offset:19456\n" \
"s_nop 0\n" \
"v_mfma_f32_16x16x32_bf16 v[60:63], v[20:23], a[128:131], 0\n" \
"s_waitcnt lgkmcnt(4)\n" \
"v_cvt_pk_bf16_f32 v80, v64, v65\n" \
"v_cvt_pk_bf16_f32 v81, v66, v67\n" \
"v_mfma_f32_16x16x32_bf16 v[48:51], v[24:27], a[4:7], v[48:51]\n" \
"buffer_load_dwordx4 v4, s[36:39], s61 offen offset:1024 sc1 lds\n" \
"v_mfma_f32_16x16x32_bf16 v[52:55], v[28:31], a[4:7], v[52:55]\n" \
"ds_read_b128 v[16:19], v10 offset:4096\n" \
"v_cvt_pk_bf16_f32 v82, v68, v69\n" \
"v_mfma_f32_16x16x32_bf16 v[56:59], v[24:27], a[132:135], v[56:59]\n" \
"ds_read_b128 v[20:23], v10 offset:20480\n" \
"v_cvt_pk_bf16_f32 v83, v70, v71\n" \
"v_mfma_f32_16x16x32_bf16 v[60:63], v[28:31], a[132:135], v[60:63]\n" \
"s_waitcnt lgkmcnt(4)\n" \
"buffer_store_dwordx4 v[80:83], v5, s[36:39], s57 offen sc1\n" \
"v_cvt_pk_bf16_f32 v84, v72, v73\n" \
"v_mfma_f32_16x16x32_bf16 v[48:51], v[32:35], a[8:11], v[48:51]\n" \
"s_add_u32 s63, s59, 2\n" \
"s_min_u32 s63, s63, 63\n" \
"v_mfma_f32_16x16x32_bf16 v[52:55], v[36:39], a[8:11], v[52:55]\n" \
"ds_read_b128 v[24:27], v10 offset:5120\n" \
"s_lshr_b32 s63, s63, 3\n" \
"s_lshl_b32 s63, s63, 7\n" \
"v_cvt_pk_bf16_f32 v85, v74, v75\n" \
"v_mfma_f32_16x16x32_bf16 v[56:59], v[32:35], a[136:139], v[56:59]\n" \
"ds_read_b128 v[28:31], v10 offset:21504\n" \
"v_cvt_pk_bf16_f32 v86, v76, v77\n" \
"v_mfma_f32_16x16x32_bf16 v[60:63], v[36:39], a[136:139], v[60:63]\n" \
"s_waitcnt lgkmcnt(4)\n" \
"v_cvt_pk_bf16_f32 v87, v78, v79\n" \
"buffer_store_dwordx4 v[84:87], v5, s[36:39], s58 offen sc1\n" \
"v_mfma_f32_16x16x32_bf16 v[48:51], v[40:43], a[12:15], v[48:51]\n" \
"buffer_load_dwordx4 v4, s[36:39], s61 offen offset:2048 sc1 lds\n" \
"v_mfma_f32_16x16x32_bf16 v[52:55], v[44:47], a[12:15], v[52:55]\n" \
"ds_read_b128 v[32:35], v10 offset:6144\n" \
"v_mfma_f32_16x16x32_bf16 v[56:59], v[40:43], a[140:143], v[56:59]\n" \
"ds_read_b128 v[36:39], v10 offset:22528\n" \
"v_mfma_f32_16x16x32_bf16 v[60:63], v[44:47], a[140:143], v[60:63]\n" \
"s_waitcnt lgkmcnt(4)\n" \
"v_mfma_f32_16x16x32_bf16 v[48:51], v[16:19], a[16:19], v[48:51]\n" \
"buffer_load_dwordx4 v4, s[36:39], s61 offen offset:3072 sc1 lds\n" \
"v_mfma_f32_16x16x32_bf16 v[52:55], v[20:23], a[16:19], v[52:55]\n" \
"ds_read_b128 v[40:43], v10 offset:7168\n" \
"v_mfma_f32_16x16x32_bf16 v[56:59], v[16:19], a[144:147], v[56:59]\n" \
"ds_read_b128 v[44:47], v10 offset:23552\n" \
"v_mfma_f32_16x16x32_bf16 v[60:63], v[20:23], a[144:147], v[60:63]\n" \
"s_waitcnt lgkmcnt(4)\n" \
"v_mfma_f32_16x16x32_bf16 v[48:51], v[24:27], a[20:23], v[48:51]\n" \
"buffer_load_dword v15, v14, s[36:39], s63 offen sc1\n" \
"v_mfma_f32_16x16x32_bf16 v[52:55], v[28:31], a[20:23], v[52:55]\n" \
"ds_read_b128 v[16:19], v10 offset:8192\n" \
"v_mfma_f32_16x16x32_bf16 v[56:59], v[24:27], a[148:151], v[56:59]\n" \
"ds_read_b128 v[20:23], v10 offset:24576\n" \
"v_mfma_f32_16x16x32_bf16 v[60:63], v[28:31], a[148:151], v[60:63]\n" \
"s_waitcnt lgkmcnt(4)\n" \
"v_mfma_f32_16x16x32_bf16 v[48:51], v[32:35], a[24:27], v[48:51]\n" \
"s_add_u32 m0, s55, 69632\n" \
"s_add_u32 s61, s61, 4096\n" \
"buffer_load_dwordx4 v4, s[36:39], s61 offen offset:0 sc1 lds\n" \
"v_mfma_f32_16x16x32_bf16 v[52:55], v[36:39], a[24:27], v[52:55]\n" \
"ds_read_b128 v[24:27], v10 offset:9216\n" \
"v_mfma_f32_16x16x32_bf16 v[56:59], v[32:35], a[152:155], v[56:59]\n" \
"ds_read_b128 v[28:31], v10 offset:25600\n" \
"v_mfma_f32_16x16x32_bf16 v[60:63], v[36:39], a[152:155], v[60:63]\n" \
"s_waitcnt lgkmcnt(4)\n" \
"v_mfma_f32_16x16x32_bf16 v[48:51], v[40:43], a[28:31], v[48:51]\n" \
"buffer_load_dwordx4 v4, s[36:39], s61 offen offset:1024 sc1 lds\n" \
"v_mfma_f32_16x16x32_bf16 v[52:55], v[44:47], a[28:31], v[52:55]\n" \
"ds_read_b128 v[32:35], v10 offset:10240\n" \
"v_mfma_f32_16x16x32_bf16 v[56:59], v[40:43], a[156:159], v[56:59]\n" \
"ds_read_b128 v[36:39], v10 offset:26624\n" \
"v_mfma_f32_16x16x32_bf16 v[60:63], v[44:47], a[156:159], v[60:63]\n" \
"s_waitcnt lgkmcnt(4)\n" \
"v_mfma_f32_16x16x32_bf16 v[48:51], v[16:19], a[32:35], v[48:51]\n" \
"v_mfma_f32_16x16x32_bf16 v[52:55], v[20:23], a[32:35], v[52:55]\n" \
"ds_read_b128 v[40:43], v10 offset:11264\n" \
"v_mfma_f32_16x16x32_bf16 v[56:59], v[16:19], a[160:163], v[56:59]\n" \
"ds_read_b128 v[44:47], v10 offset:27648\n" \
"v_mfma_f32_16x16x32_bf16 v[60:63], v[20:23], a[160:163], v[60:63]\n" \
"s_waitcnt lgkmcnt(4)\n" \
"v_mfma_f32_16x16x32_bf16 v[48:51], v[24:27], a[36:39], v[48:51]\n" \
"buffer_load_dwordx4 v4, s[36:39], s61 offen offset:2048 sc1 lds\n" \
"v_mfma_f32_16x16x32_bf16 v[52:55], v[28:31], a[36:39], v[52:55]\n" \
"ds_read_b128 v[16:19], v10 offset:12288\n" \
"v_mfma_f32_16x16x32_bf16 v[56:59], v[24:27], a[164:167], v[56:59]\n" \
"ds_read_b128 v[20:23], v10 offset:28672\n" \
"v_mfma_f32_16x16x32_bf16 v[60:63], v[28:31], a[164:167], v[60:63]\n" \
"s_waitcnt lgkmcnt(4)\n" \
"v_mfma_f32_16x16x32_bf16 v[48:51], v[32:35], a[40:43], v[48:51]\n" \
"buffer_load_dwordx4 v4, s[36:39], s61 offen offset:3072 sc1 lds\n" \
"v_mfma_f32_16x16x32_bf16 v[52:55], v[36:39], a[40:43], v[52:55]\n" \
"ds_read_b128 v[24:27], v10 offset:13312\n" \
"v_mfma_f32_16x16x32_bf16 v[56:59], v[32:35], a[168:171], v[56:59]\n" \
"ds_read_b128 v[28:31], v10 offset:29696\n" \
"v_mfma_f32_16x16x32_bf16 v[60:63], v[36:39], a[168:171], v[60:63]\n" \
"s_waitcnt lgkmcnt(4)\n" \
"v_mfma_f32_16x16x32_bf16 v[48:51], v[40:43], a[44:47], v[48:51]\n" \
"v_mfma_f32_16x16x32_bf16 v[52:55], v[44:47], a[44:47], v[52:55]\n" \
"ds_read_b128 v[32:35], v10 offset:14336\n" \
"v_mfma_f32_16x16x32_bf16 v[56:59], v[40:43], a[172:175], v[56:59]\n" \
"ds_read_b128 v[36:39], v10 offset:30720\n" \
"v_mfma_f32_16x16x32_bf16 v[60:63], v[44:47], a[172:175], v[60:63]\n" \
"s_waitcnt lgkmcnt(4)\n" \
"v_mfma_f32_16x16x32_bf16 v[48:51], v[16:19], a[48:51], v[48:51]\n" \
"v_mfma_f32_16x16x32_bf16 v[52:55], v[20:23], a[48:51], v[52:55]\n" \
"ds_read_b128 v[40:43], v10 offset:15360\n" \
"v_mfma_f32_16x16x32_bf16 v[56:59], v[16:19], a[176:179], v[56:59]\n" \
"ds_read_b128 v[44:47], v10 offset:31744\n" \
"v_mfma_f32_16x16x32_bf16 v[60:63], v[20:23], a[176:179], v[60:63]\n" \
"s_waitcnt lgkmcnt(4)\n" \
"v_mfma_f32_16x16x32_bf16 v[48:51], v[24:27], a[52:55], v[48:51]\n" \
"s_waitcnt vmcnt(12)\n" \
"s_barrier\n" \
"v_mfma_f32_16x16x32_bf16 v[52:55], v[28:31], a[52:55], v[52:55]\n" \
"ds_read_b128 v[16:19], v10 offset:32768\n" \
"v_mfma_f32_16x16x32_bf16 v[56:59], v[24:27], a[180:183], v[56:59]\n" \
"ds_read_b128 v[20:23], v10 offset:49152\n" \
"v_mfma_f32_16x16x32_bf16 v[60:63], v[28:31], a[180:183], v[60:63]\n" \
"s_waitcnt lgkmcnt(4)\n" \
"v_mfma_f32_16x16x32_bf16 v[48:51], v[32:35], a[56:59], v[48:51]\n" \
"v_mfma_f32_16x16x32_bf16 v[52:55], v[36:39], a[56:59], v[52:55]\n" \
"ds_read_b128 v[24:27], v10 offset:33792\n" \
"v_mfma_f32_16x16x32_bf16 v[56:59], v[32:35], a[184:187], v[56:59]\n" \
"ds_read_b128 v[28:31], v10 offset:50176\n" \
"v_mfma_f32_16x16x32_bf16 v[60:63], v[36:39], a[184:187], v[60:63]\n" \
"s_waitcnt lgkmcnt(4)\n" \
"v_mfma_f32_16x16x32_bf16 v[48:51], v[40:43], a[60:63], v[48:51]\n" \
"v_mfma_f32_16x16x32_bf16 v[52:55], v[44:47], a[60:63], v[52:55]\n" \
"ds_read_b128 v[32:35], v10 offset:34816\n" \
"v_mfma_f32_16x16x32_bf16 v[56:59], v[40:43], a[188:191], v[56:59]\n" \
"ds_read_b128 v[36:39], v10 offset:51200\n" \
"v_mfma_f32_16x16x32_bf16 v[60:63], v[44:47], a[188:191], v[60:63]\n" \
"s_waitcnt lgkmcnt(4)\n" \
"v_mfma_f32_16x16x32_bf16 v[48:51], v[16:19], a[64:67], v[48:51]\n" \
"s_add_u32 s61, s61, 28672\n" \
"s_add_u32 s56, s56, 0x20000\n" \
"s_add_u32 m0, s55, 98304\n" \
"s_nop 0\n" \
"buffer_load_dwordx4 v4, s[36:39], s61 offen offset:0 sc1 lds\n" \
"v_mfma_f32_16x16x32_bf16 v[52:55], v[20:23], a[64:67], v[52:55]\n" \
"ds_read_b128 v[40:43], v10 offset:35840\n" \
"v_mfma_f32_16x16x32_bf16 v[56:59], v[16:19], a[192:195], v[56:59]\n" \
"ds_read_b128 v[44:47], v10 offset:52224\n" \
"v_mfma_f32_16x16x32_bf16 v[60:63], v[20:23], a[192:195], v[60:63]\n" \
"s_waitcnt lgkmcnt(4)\n" \
"v_mfma_f32_16x16x32_bf16 v[48:51], v[24:27], a[68:71], v[48:51]\n" \
"buffer_load_dwordx4 v4, s[36:39], s61 offen offset:1024 sc1 lds\n" \
"v_mfma_f32_16x16x32_bf16 v[52:55], v[28:31], a[68:71], v[52:55]\n" \
"ds_read_b128 v[16:19], v10 offset:36864\n" \
"v_mfma_f32_16x16x32_bf16 v[56:59], v[24:27], a[196:199], v[56:59]\n" \
"ds_read_b128 v[20:23], v10 offset:53248\n" \
"v_mfma_f32_16x16x32_bf16 v[60:63], v[28:31], a[196:199], v[60:63]\n" \
"s_waitcnt lgkmcnt(4)\n" \
"v_mfma_f32_16x16x32_bf16 v[48:51], v[32:35], a[72:75], v[48:51]\n" \
"v_mfma_f32_16x16x32_bf16 v[52:55], v[36:39], a[72:75], v[52:55]\n" \
"ds_read_b128 v[24:27], v10 offset:37888\n" \
"v_mfma_f32_16x16x32_bf16 v[56:59], v[32:35], a[200:203], v[56:59]\n" \
"ds_read_b128 v[28:31], v10 offset:54272\n" \
"v_mfma_f32_16x16x32_bf16 v[60:63], v[36:39], a[200:203], v[60:63]\n" \
"s_waitcnt lgkmcnt(4)\n" \
"v_mfma_f32_16x16x32_bf16 v[48:51], v[40:43], a[76:79], v[48:51]\n" \
"buffer_load_dwordx4 v4, s[36:39], s61 offen offset:2048 sc1 lds\n" \
"v_mfma_f32_16x16x32_bf16 v[52:55], v[44:47], a[76:79], v[52:55]\n" \
"ds_read_b128 v[32:35], v10 offset:38912\n" \
"v_mfma_f32_16x16x32_bf16 v[56:59], v[40:43], a[204:207], v[56:59]\n" \
"ds_read_b128 v[36:39], v10 offset:55296\n" \
"v_mfma_f32_16x16x32_bf16 v[60:63], v[44:47], a[204:207], v[60:63]\n" \
"s_waitcnt lgkmcnt(4)\n" \
"v_mfma_f32_16x16x32_bf16 v[48:51], v[16:19], a[80:83], v[48:51]\n" \
"buffer_load_dwordx4 v4, s[36:39], s61 offen offset:3072 sc1 lds\n" \
"v_mfma_f32_16x16x32_bf16 v[52:55], v[20:23], a[80:83], v[52:55]\n" \
"ds_read_b128 v[40:43], v10 offset:39936\n" \
"v_mfma_f32_16x16x32_bf16 v[56:59], v[16:19], a[208:211], v[56:59]\n" \
"ds_read_b128 v[44:47], v10 offset:56320\n" \
"v_mfma_f32_16x16x32_bf16 v[60:63], v[20:23], a[208:211], v[60:63]\n" \
"s_waitcnt lgkmcnt(4)\n" \
"v_mfma_f32_16x16x32_bf16 v[48:51], v[24:27], a[84:87], v[48:51]\n" \
"v_mfma_f32_16x16x32_bf16 v[52:55], v[28:31], a[84:87], v[52:55]\n" \
"ds_read_b128 v[16:19], v10 offset:40960\n" \
"v_mfma_f32_16x16x32_bf16 v[56:59], v[24:27], a[212:215], v[56:59]\n" \
"ds_read_b128 v[20:23], v10 offset:57344\n" \
"v_mfma_f32_16x16x32_bf16 v[60:63], v[28:31], a[212:215], v[60:63]\n" \
"s_waitcnt lgkmcnt(4)\n" \
"v_mfma_f32_16x16x32_bf16 v[48:51], v[32:35], a[88:91], v[48:51]\n" \
"s_add_u32 m0, s55, 102400\n" \
"s_add_u32 s61, s61, 4096\n" \
"buffer_load_dwordx4 v4, s[36:39], s61 offen offset:0 sc1 lds\n" \
"v_mfma_f32_16x16x32_bf16 v[52:55], v[36:39], a[88:91], v[52:55]\n" \
"ds_read_b128 v[24:27], v10 offset:41984\n" \
"v_mfma_f32_16x16x32_bf16 v[56:59], v[32:35], a[216:219], v[56:59]\n" \
"ds_read_b128 v[28:31], v10 offset:58368\n" \
"v_mfma_f32_16x16x32_bf16 v[60:63], v[36:39], a[216:219], v[60:63]\n" \
"s_waitcnt lgkmcnt(4)\n" \
"v_mfma_f32_16x16x32_bf16 v[48:51], v[40:43], a[92:95], v[48:51]\n" \
"buffer_load_dwordx4 v4, s[36:39], s61 offen offset:1024 sc1 lds\n" \
"v_mfma_f32_16x16x32_bf16 v[52:55], v[44:47], a[92:95], v[52:55]\n" \
"ds_read_b128 v[32:35], v10 offset:43008\n" \
"v_mfma_f32_16x16x32_bf16 v[56:59], v[40:43], a[220:223], v[56:59]\n" \
"ds_read_b128 v[36:39], v10 offset:59392\n" \
"v_mfma_f32_16x16x32_bf16 v[60:63], v[44:47], a[220:223], v[60:63]\n" \
"s_waitcnt lgkmcnt(4)\n" \
"v_mfma_f32_16x16x32_bf16 v[48:51], v[16:19], a[96:99], v[48:51]\n" \
"v_mfma_f32_16x16x32_bf16 v[52:55], v[20:23], a[96:99], v[52:55]\n" \
"ds_read_b128 v[40:43], v10 offset:44032\n" \
"v_mfma_f32_16x16x32_bf16 v[56:59], v[16:19], a[224:227], v[56:59]\n" \
"ds_read_b128 v[44:47], v10 offset:60416\n" \
"v_mfma_f32_16x16x32_bf16 v[60:63], v[20:23], a[224:227], v[60:63]\n" \
"s_waitcnt lgkmcnt(4)\n" \
"v_mfma_f32_16x16x32_bf16 v[48:51], v[24:27], a[100:103], v[48:51]\n" \
"buffer_load_dwordx4 v4, s[36:39], s61 offen offset:2048 sc1 lds\n" \
"v_mfma_f32_16x16x32_bf16 v[52:55], v[28:31], a[100:103], v[52:55]\n" \
"ds_read_b128 v[16:19], v10 offset:45056\n" \
"v_mfma_f32_16x16x32_bf16 v[56:59], v[24:27], a[228:231], v[56:59]\n" \
"ds_read_b128 v[20:23], v10 offset:61440\n" \
"v_mfma_f32_16x16x32_bf16 v[60:63], v[28:31], a[228:231], v[60:63]\n" \
"s_waitcnt lgkmcnt(4)\n" \
"v_mfma_f32_16x16x32_bf16 v[48:51], v[32:35], a[104:107], v[48:51]\n" \
"buffer_load_dwordx4 v4, s[36:39], s61 offen offset:3072 sc1 lds\n" \
"v_mfma_f32_16x16x32_bf16 v[52:55], v[36:39], a[104:107], v[52:55]\n" \
"ds_read_b128 v[24:27], v10 offset:46080\n" \
"v_mfma_f32_16x16x32_bf16 v[56:59], v[32:35], a[232:235], v[56:59]\n" \
"ds_read_b128 v[28:31], v10 offset:62464\n" \
"v_mfma_f32_16x16x32_bf16 v[60:63], v[36:39], a[232:235], v[60:63]\n" \
"s_waitcnt lgkmcnt(4)\n" \
"v_mfma_f32_16x16x32_bf16 v[48:51], v[40:43], a[108:111], v[48:51]\n" \
"s_lshl_b32 s66, s59, s49\n" \
"s_lshr_b32 s65, s66, 8\n" \
"v_mfma_f32_16x16x32_bf16 v[52:55], v[44:47], a[108:111], v[52:55]\n" \
"ds_read_b128 v[32:35], v10 offset:47104\n" \
"v_mfma_f32_16x16x32_bf16 v[56:59], v[40:43], a[236:239], v[56:59]\n" \
"ds_read_b128 v[36:39], v10 offset:63488\n" \
"v_mfma_f32_16x16x32_bf16 v[60:63], v[44:47], a[236:239], v[60:63]\n" \
"s_waitcnt lgkmcnt(4)\n" \
"s_and_b32 s65, s65, 0x2000\n" \
"s_xor_b32 s66, s66, s65\n" \
"v_mfma_f32_16x16x32_bf16 v[48:51], v[16:19], a[112:115], v[48:51]\n" \
"v_mfma_f32_16x16x32_bf16 v[52:55], v[20:23], a[112:115], v[52:55]\n" \
"ds_read_b128 v[40:43], v10 offset:48128\n" \
"v_mfma_f32_16x16x32_bf16 v[56:59], v[16:19], a[240:243], v[56:59]\n" \
"ds_read_b128 v[44:47], v10 offset:64512\n" \
"v_mfma_f32_16x16x32_bf16 v[60:63], v[20:23], a[240:243], v[60:63]\n" \
"s_waitcnt lgkmcnt(4)\n" \
"v_mfma_f32_16x16x32_bf16 v[48:51], v[24:27], a[116:119], v[48:51]\n" \
"s_waitcnt vmcnt(8)\n" \
"s_barrier\n" \
"v_mfma_f32_16x16x32_bf16 v[52:55], v[28:31], a[116:119], v[52:55]\n" \
"ds_read_b128 v[16:19], v12 offset:0\n" \
"v_mfma_f32_16x16x32_bf16 v[56:59], v[24:27], a[244:247], v[56:59]\n" \
"ds_read_b128 v[20:23], v12 offset:16384\n" \
"v_mfma_f32_16x16x32_bf16 v[60:63], v[28:31], a[244:247], v[60:63]\n" \
"s_waitcnt lgkmcnt(4)\n" \
"buffer_store_dword v13, v6, s[36:39], 0 offen sc1\n" \
"v_mfma_f32_16x16x32_bf16 v[48:51], v[32:35], a[120:123], v[48:51]\n" \
"v_cmp_gt_u32_e64 vcc, 16, v15\n" \
"v_mfma_f32_16x16x32_bf16 v[52:55], v[36:39], a[120:123], v[52:55]\n" \
"ds_read_b128 v[24:27], v12 offset:1024\n" \
"v_mfma_f32_16x16x32_bf16 v[56:59], v[32:35], a[248:251], v[56:59]\n" \
"ds_read_b128 v[28:31], v12 offset:17408\n" \
"v_mfma_f32_16x16x32_bf16 v[60:63], v[36:39], a[248:251], v[60:63]\n" \
"s_waitcnt lgkmcnt(4)\n" \
"v_mfma_f32_16x16x32_bf16 v[48:51], v[40:43], a[124:127], v[48:51]\n" \
"s_nop 0\n" \
"s_cbranch_vccnz .Lp1v_m0_%=\n" \
".Lp1v_m0k_%=:\n" \
"v_mfma_f32_16x16x32_bf16 v[52:55], v[44:47], a[124:127], v[52:55]\n" \
"ds_read_b128 v[32:35], v12 offset:2048\n" \
"v_mfma_f32_16x16x32_bf16 v[56:59], v[40:43], a[252:255], v[56:59]\n" \
"ds_read_b128 v[36:39], v12 offset:18432\n" \
"v_mfma_f32_16x16x32_bf16 v[60:63], v[44:47], a[252:255], v[60:63]\n" \
"s_waitcnt lgkmcnt(4)\n" \
"s_xor_b32 s57, s66, s64\n" \
"s_add_u32 s58, s57, s50\n" \
"s_add_u32 s59, s59, 1\n" \
"v_mov_b32 v5, v7\n" \
"v_mov_b32 v6, v11\n" \
"s_cmp_ge_u32 s59, s47\n" \
"s_cbranch_scc1 .Lp1v_out0_%=\n" \
"v_mfma_f32_16x16x32_bf16 v[64:67], v[16:19], a[0:3], 0\n" \
"s_min_u32 s61, s56, s60\n" \
"s_lshr_b32 s65, s61, 8\n" \
"s_and_b32 s65, s65, 0x2000\n" \
"s_xor_b32 s61, s61, s65\n" \
"s_add_u32 m0, s55, 0\n" \
"s_nop 0\n" \
"buffer_load_dwordx4 v4, s[36:39], s61 offen offset:0 sc1 lds\n" \
"v_mov_b32_e64 v13, s59\n" \
"v_mfma_f32_16x16x32_bf16 v[68:71], v[20:23], a[0:3], 0\n" \
"ds_read_b128 v[40:43], v12 offset:3072\n" \
"s_nop 0\n" \
"v_mfma_f32_16x16x32_bf16 v[72:75], v[16:19], a[128:131], 0\n" \
"ds_read_b128 v[44:47], v12 offset:19456\n" \
"s_nop 0\n" \
"v_mfma_f32_16x16x32_bf16 v[76:79], v[20:23], a[128:131], 0\n" \
"s_waitcnt lgkmcnt(4)\n" \
"v_cvt_pk_bf16_f32 v80, v48, v49\n" \
"v_cvt_pk_bf16_f32 v81, v50, v51\n" \
"v_mfma_f32_16x16x32_bf16 v[64:67], v[24:27], a[4:7], v[64:67]\n" \
"buffer_load_dwordx4 v4, s[36:39], s61 offen offset:1024 sc1 lds\n" \
"v_mfma_f32_16x16x32_bf16 v[68:71], v[28:31], a[4:7], v[68:71]\n" \
"ds_read_b128 v[16:19], v12 offset:4096\n" \
"v_cvt_pk_bf16_f32 v82, v52, v53\n" \
"v_mfma_f32_16x16x32_bf16 v[72:75], v[24:27], a[132:135], v[72:75]\n" \
"ds_read_b128 v[20:23], v12 offset:20480\n" \
"v_cvt_pk_bf16_f32 v83, v54, v55\n" \
"v_mfma_f32_16x16x32_bf16 v[76:79], v[28:31], a[132:135], v[76:79]\n" \
"s_waitcnt lgkmcnt(4)\n" \
"buffer_store_dwordx4 v[80:83], v5, s[36:39], s57 offen sc1\n" \
"v_cvt_pk_bf16_f32 v84, v56, v57\n" \
"v_mfma_f32_16x16x32_bf16 v[64:67], v[32:35], a[8:11], v[64:67]\n" \
"s_add_u32 s63, s59, 2\n" \
"s_min_u32 s63, s63, 63\n" \
"v_mfma_f32_16x16x32_bf16 v[68:71], v[36:39], a[8:11], v[68:71]\n" \
"ds_read_b128 v[24:27], v12 offset:5120\n" \
"s_lshr_b32 s63, s63, 3\n" \
"s_lshl_b32 s63, s63, 7\n" \
"v_cvt_pk_bf16_f32 v85, v58, v59\n" \
"v_mfma_f32_16x16x32_bf16 v[72:75], v[32:35], a[136:139], v[72:75]\n" \
"ds_read_b128 v[28:31], v12 offset:21504\n" \
"v_cvt_pk_bf16_f32 v86, v60, v61\n" \
"v_mfma_f32_16x16x32_bf16 v[76:79], v[36:39], a[136:139], v[76:79]\n" \
"s_waitcnt lgkmcnt(4)\n" \
"v_cvt_pk_bf16_f32 v87, v62, v63\n" \
"buffer_store_dwordx4 v[84:87], v5, s[36:39], s58 offen sc1\n" \
"v_mfma_f32_16x16x32_bf16 v[64:67], v[40:43], a[12:15], v[64:67]\n" \
"buffer_load_dwordx4 v4, s[36:39], s61 offen offset:2048 sc1 lds\n" \
"v_mfma_f32_16x16x32_bf16 v[68:71], v[44:47], a[12:15], v[68:71]\n" \
"ds_read_b128 v[32:35], v12 offset:6144\n" \
"v_mfma_f32_16x16x32_bf16 v[72:75], v[40:43], a[140:143], v[72:75]\n" \
"ds_read_b128 v[36:39], v12 offset:22528\n" \
"v_mfma_f32_16x16x32_bf16 v[76:79], v[44:47], a[140:143], v[76:79]\n" \
"s_waitcnt lgkmcnt(4)\n" \
"v_mfma_f32_16x16x32_bf16 v[64:67], v[16:19], a[16:19], v[64:67]\n" \
"buffer_load_dwordx4 v4, s[36:39], s61 offen offset:3072 sc1 lds\n" \
"v_mfma_f32_16x16x32_bf16 v[68:71], v[20:23], a[16:19], v[68:71]\n" \
"ds_read_b128 v[40:43], v12 offset:7168\n" \
"v_mfma_f32_16x16x32_bf16 v[72:75], v[16:19], a[144:147], v[72:75]\n" \
"ds_read_b128 v[44:47], v12 offset:23552\n" \
"v_mfma_f32_16x16x32_bf16 v[76:79], v[20:23], a[144:147], v[76:79]\n" \
"s_waitcnt lgkmcnt(4)\n" \
"v_mfma_f32_16x16x32_bf16 v[64:67], v[24:27], a[20:23], v[64:67]\n" \
"buffer_load_dword v15, v14, s[36:39], s63 offen sc1\n" \
"v_mfma_f32_16x16x32_bf16 v[68:71], v[28:31], a[20:23], v[68:71]\n" \
"ds_read_b128 v[16:19], v12 offset:8192\n" \
"v_mfma_f32_16x16x32_bf16 v[72:75], v[24:27], a[148:151], v[72:75]\n" \
"ds_read_b128 v[20:23], v12 offset:24576\n" \
"v_mfma_f32_16x16x32_bf16 v[76:79], v[28:31], a[148:151], v[76:79]\n" \
"s_waitcnt lgkmcnt(4)\n" \
"v_mfma_f32_16x16x32_bf16 v[64:67], v[32:35], a[24:27], v[64:67]\n" \
"s_add_u32 m0, s55, 4096\n" \
"s_add_u32 s61, s61, 4096\n" \
"buffer_load_dwordx4 v4, s[36:39], s61 offen offset:0 sc1 lds\n" \
"v_mfma_f32_16x16x32_bf16 v[68:71], v[36:39], a[24:27], v[68:71]\n" \
"ds_read_b128 v[24:27], v12 offset:9216\n" \
"v_mfma_f32_16x16x32_bf16 v[72:75], v[32:35], a[152:155], v[72:75]\n" \
"ds_read_b128 v[28:31], v12 offset:25600\n" \
"v_mfma_f32_16x16x32_bf16 v[76:79], v[36:39], a[152:155], v[76:79]\n" \
"s_waitcnt lgkmcnt(4)\n" \
"v_mfma_f32_16x16x32_bf16 v[64:67], v[40:43], a[28:31], v[64:67]\n" \
"buffer_load_dwordx4 v4, s[36:39], s61 offen offset:1024 sc1 lds\n" \
"v_mfma_f32_16x16x32_bf16 v[68:71], v[44:47], a[28:31], v[68:71]\n" \
"ds_read_b128 v[32:35], v12 offset:10240\n" \
"v_mfma_f32_16x16x32_bf16 v[72:75], v[40:43], a[156:159], v[72:75]\n" \
"ds_read_b128 v[36:39], v12 offset:26624\n" \
"v_mfma_f32_16x16x32_bf16 v[76:79], v[44:47], a[156:159], v[76:79]\n" \
"s_waitcnt lgkmcnt(4)\n" \
"v_mfma_f32_16x16x32_bf16 v[64:67], v[16:19], a[32:35], v[64:67]\n" \
"v_mfma_f32_16x16x32_bf16 v[68:71], v[20:23], a[32:35], v[68:71]\n" \
"ds_read_b128 v[40:43], v12 offset:11264\n" \
"v_mfma_f32_16x16x32_bf16 v[72:75], v[16:19], a[160:163], v[72:75]\n" \
"ds_read_b128 v[44:47], v12 offset:27648\n" \
"v_mfma_f32_16x16x32_bf16 v[76:79], v[20:23], a[160:163], v[76:79]\n" \
"s_waitcnt lgkmcnt(4)\n" \
"v_mfma_f32_16x16x32_bf16 v[64:67], v[24:27], a[36:39], v[64:67]\n" \
"buffer_load_dwordx4 v4, s[36:39], s61 offen offset:2048 sc1 lds\n" \
"v_mfma_f32_16x16x32_bf16 v[68:71], v[28:31], a[36:39], v[68:71]\n" \
"ds_read_b128 v[16:19], v12 offset:12288\n" \
"v_mfma_f32_16x16x32_bf16 v[72:75], v[24:27], a[164:167], v[72:75]\n" \
"ds_read_b128 v[20:23], v12 offset:28672\n" \
"v_mfma_f32_16x16x32_bf16 v[76:79], v[28:31], a[164:167], v[76:79]\n" \
"s_waitcnt lgkmcnt(4)\n" \
"v_mfma_f32_16x16x32_bf16 v[64:67], v[32:35], a[40:43], v[64:67]\n" \
"buffer_load_dwordx4 v4, s[36:39], s61 offen offset:3072 sc1 lds\n" \
"v_mfma_f32_16x16x32_bf16 v[68:71], v[36:39], a[40:43], v[68:71]\n" \
"ds_read_b128 v[24:27], v12 offset:13312\n" \
"v_mfma_f32_16x16x32_bf16 v[72:75], v[32:35], a[168:171], v[72:75]\n" \
"ds_read_b128 v[28:31], v12 offset:29696\n" \
"v_mfma_f32_16x16x32_bf16 v[76:79], v[36:39], a[168:171], v[76:79]\n" \
"s_waitcnt lgkmcnt(4)\n" \
"v_mfma_f32_16x16x32_bf16 v[64:67], v[40:43], a[44:47], v[64:67]\n" \
"v_mfma_f32_16x16x32_bf16 v[68:71], v[44:47], a[44:47], v[68:71]\n" \
"ds_read_b128 v[32:35], v12 offset:14336\n" \
"v_mfma_f32_16x16x32_bf16 v[72:75], v[40:43], a[172:175], v[72:75]\n" \
"ds_read_b128 v[36:39], v12 offset:30720\n" \
"v_mfma_f32_16x16x32_bf16 v[76:79], v[44:47], a[172:175], v[76:79]\n" \
"s_waitcnt lgkmcnt(4)\n" \
"v_mfma_f32_16x16x32_bf16 v[64:67], v[16:19], a[48:51], v[64:67]\n" \
"v_mfma_f32_16x16x32_bf16 v[68:71], v[20:23], a[48:51], v[68:71]\n" \
"ds_read_b128 v[40:43], v12 offset:15360\n" \
"v_mfma_f32_16x16x32_bf16 v[72:75], v[16:19], a[176:179], v[72:75]\n" \
"ds_read_b128 v[44:47], v12 offset:31744\n" \
"v_mfma_f32_16x16x32_bf16 v[76:79], v[20:23], a[176:179], v[76:79]\n" \
"s_waitcnt lgkmcnt(4)\n" \
"v_mfma_f32_16x16x32_bf16 v[64:67], v[24:27], a[52:55], v[64:67]\n" \
"s_waitcnt vmcnt(12)\n" \
"s_barrier\n" \
"v_mfma_f32_16x16x32_bf16 v[68:71], v[28:31], a[52:55], v[68:71]\n" \
"ds_read_b128 v[16:19], v12 offset:32768\n" \
"v_mfma_f32_16x16x32_bf16 v[72:75], v[24:27], a[180:183], v[72:75]\n" \
"ds_read_b128 v[20:23], v12 offset:49152\n" \
"v_mfma_f32_16x16x32_bf16 v[76:79], v[28:31], a[180:183], v[76:79]\n" \
"s_waitcnt lgkmcnt(4)\n" \
"v_mfma_f32_16x16x32_bf16 v[64:67], v[32:35], a[56:59], v[64:67]\n" \
"v_mfma_f32_16x16x32_bf16 v[68:71], v[36:39], a[56:59], v[68:71]\n" \
"ds_read_b128 v[24:27], v12 offset:33792\n" \
"v_mfma_f32_16x16x32_bf16 v[72:75], v[32:35], a[184:187], v[72:75]\n" \
"ds_read_b128 v[28:31], v12 offset:50176\n" \
"v_mfma_f32_16x16x32_bf16 v[76:79], v[36:39], a[184:187], v[76:79]\n" \
"s_waitcnt lgkmcnt(4)\n" \
"v_mfma_f32_16x16x32_bf16 v[64:67], v[40:43], a[60:63], v[64:67]\n" \
"v_mfma_f32_16x16x32_bf16 v[68:71], v[44:47], a[60:63], v[68:71]\n" \
"ds_read_b128 v[32:35], v12 offset:34816\n" \
"v_mfma_f32_16x16x32_bf16 v[72:75], v[40:43], a[188:191], v[72:75]\n" \
"ds_read_b128 v[36:39], v12 offset:51200\n" \
"v_mfma_f32_16x16x32_bf16 v[76:79], v[44:47], a[188:191], v[76:79]\n" \
"s_waitcnt lgkmcnt(4)\n" \
"v_mfma_f32_16x16x32_bf16 v[64:67], v[16:19], a[64:67], v[64:67]\n" \
"s_add_u32 s61, s61, 28672\n" \
"s_add_u32 s56, s56, 0x20000\n" \
"s_add_u32 m0, s55, 32768\n" \
"s_nop 0\n" \
"buffer_load_dwordx4 v4, s[36:39], s61 offen offset:0 sc1 lds\n" \
"v_mfma_f32_16x16x32_bf16 v[68:71], v[20:23], a[64:67], v[68:71]\n" \
"ds_read_b128 v[40:43], v12 offset:35840\n" \
"v_mfma_f32_16x16x32_bf16 v[72:75], v[16:19], a[192:195], v[72:75]\n" \
"ds_read_b128 v[44:47], v12 offset:52224\n" \
"v_mfma_f32_16x16x32_bf16 v[76:79], v[20:23], a[192:195], v[76:79]\n" \
"s_waitcnt lgkmcnt(4)\n" \
"v_mfma_f32_16x16x32_bf16 v[64:67], v[24:27], a[68:71], v[64:67]\n" \
"buffer_load_dwordx4 v4, s[36:39], s61 offen offset:1024 sc1 lds\n" \
"v_mfma_f32_16x16x32_bf16 v[68:71], v[28:31], a[68:71], v[68:71]\n" \
"ds_read_b128 v[16:19], v12 offset:36864\n" \
"v_mfma_f32_16x16x32_bf16 v[72:75], v[24:27], a[196:199], v[72:75]\n" \
"ds_read_b128 v[20:23], v12 offset:53248\n" \
"v_mfma_f32_16x16x32_bf16 v[76:79], v[28:31], a[196:199], v[76:79]\n" \
"s_waitcnt lgkmcnt(4)\n" \
"v_mfma_f32_16x16x32_bf16 v[64:67], v[32:35], a[72:75], v[64:67]\n" \
"v_mfma_f32_16x16x32_bf16 v[68:71], v[36:39], a[72:75], v[68:71]\n" \
"ds_read_b128 v[24:27], v12 offset:37888\n" \
"v_mfma_f32_16x16x32_bf16 v[72:75], v[32:35], a[200:203], v[72:75]\n" \
"ds_read_b128 v[28:31], v12 offset:54272\n" \
"v_mfma_f32_16x16x32_bf16 v[76:79], v[36:39], a[200:203], v[76:79]\n" \
"s_waitcnt lgkmcnt(4)\n" \
"v_mfma_f32_16x16x32_bf16 v[64:67], v[40:43], a[76:79], v[64:67]\n" \
"buffer_load_dwordx4 v4, s[36:39], s61 offen offset:2048 sc1 lds\n" \
"v_mfma_f32_16x16x32_bf16 v[68:71], v[44:47], a[76:79], v[68:71]\n" \
"ds_read_b128 v[32:35], v12 offset:38912\n" \
"v_mfma_f32_16x16x32_bf16 v[72:75], v[40:43], a[204:207], v[72:75]\n" \
"ds_read_b128 v[36:39], v12 offset:55296\n" \
"v_mfma_f32_16x16x32_bf16 v[76:79], v[44:47], a[204:207], v[76:79]\n" \
"s_waitcnt lgkmcnt(4)\n" \
"v_mfma_f32_16x16x32_bf16 v[64:67], v[16:19], a[80:83], v[64:67]\n" \
"buffer_load_dwordx4 v4, s[36:39], s61 offen offset:3072 sc1 lds\n" \
"v_mfma_f32_16x16x32_bf16 v[68:71], v[20:23], a[80:83], v[68:71]\n" \
"ds_read_b128 v[40:43], v12 offset:39936\n" \
"v_mfma_f32_16x16x32_bf16 v[72:75], v[16:19], a[208:211], v[72:75]\n" \
"ds_read_b128 v[44:47], v12 offset:56320\n" \
"v_mfma_f32_16x16x32_bf16 v[76:79], v[20:23], a[208:211], v[76:79]\n" \
"s_waitcnt lgkmcnt(4)\n" \
"v_mfma_f32_16x16x32_bf16 v[64:67], v[24:27], a[84:87], v[64:67]\n" \
"v_mfma_f32_16x16x32_bf16 v[68:71], v[28:31], a[84:87], v[68:71]\n" \
"ds_read_b128 v[16:19], v12 offset:40960\n" \
"v_mfma_f32_16x16x32_bf16 v[72:75], v[24:27], a[212:215], v[72:75]\n" \
"ds_read_b128 v[20:23], v12 offset:57344\n" \
"v_mfma_f32_16x16x32_bf16 v[76:79], v[28:31], a[212:215], v[76:79]\n" \
"s_waitcnt lgkmcnt(4)\n" \
"v_mfma_f32_16x16x32_bf16 v[64:67], v[32:35], a[88:91], v[64:67]\n" \
"s_add_u32 m0, s55, 36864\n" \
"s_add_u32 s61, s61, 4096\n" \
"buffer_load_dwordx4 v4, s[36:39], s61 offen offset:0 sc1 lds\n" \
"v_mfma_f32_16x16x32_bf16 v[68:71], v[36:39], a[88:91], v[68:71]\n" \
"ds_read_b128 v[24:27], v12 offset:41984\n" \
"v_mfma_f32_16x16x32_bf16 v[72:75], v[32:35], a[216:219], v[72:75]\n" \
"ds_read_b128 v[28:31], v12 offset:58368\n" \
"v_mfma_f32_16x16x32_bf16 v[76:79], v[36:39], a[216:219], v[76:79]\n" \
"s_waitcnt lgkmcnt(4)\n" \
"v_mfma_f32_16x16x32_bf16 v[64:67], v[40:43], a[92:95], v[64:67]\n" \
"buffer_load_dwordx4 v4, s[36:39], s61 offen offset:1024 sc1 lds\n" \
"v_mfma_f32_16x16x32_bf16 v[68:71], v[44:47], a[92:95], v[68:71]\n" \
"ds_read_b128 v[32:35], v12 offset:43008\n" \
"v_mfma_f32_16x16x32_bf16 v[72:75], v[40:43], a[220:223], v[72:75]\n" \
"ds_read_b128 v[36:39], v12 offset:59392\n" \
"v_mfma_f32_16x16x32_bf16 v[76:79], v[44:47], a[220:223], v[76:79]\n" \
"s_waitcnt lgkmcnt(4)\n" \
"v_mfma_f32_16x16x32_bf16 v[64:67], v[16:19], a[96:99], v[64:67]\n" \
"v_mfma_f32_16x16x32_bf16 v[68:71], v[20:23], a[96:99], v[68:71]\n" \
"ds_read_b128 v[40:43], v12 offset:44032\n" \
"v_mfma_f32_16x16x32_bf16 v[72:75], v[16:19], a[224:227], v[72:75]\n" \
"ds_read_b128 v[44:47], v12 offset:60416\n" \
"v_mfma_f32_16x16x32_bf16 v[76:79], v[20:23], a[224:227], v[76:79]\n" \
"s_waitcnt lgkmcnt(4)\n" \
"v_mfma_f32_16x16x32_bf16 v[64:67], v[24:27], a[100:103], v[64:67]\n" \
"buffer_load_dwordx4 v4, s[36:39], s61 offen offset:2048 sc1 lds\n" \
"v_mfma_f32_16x16x32_bf16 v[68:71], v[28:31], a[100:103], v[68:71]\n" \
"ds_read_b128 v[16:19], v12 offset:45056\n" \
"v_mfma_f32_16x16x32_bf16 v[72:75], v[24:27], a[228:231], v[72:75]\n" \
"ds_read_b128 v[20:23], v12 offset:61440\n" \
"v_mfma_f32_16x16x32_bf16 v[76:79], v[28:31], a[228:231], v[76:79]\n" \
"s_waitcnt lgkmcnt(4)\n" \
"v_mfma_f32_16x16x32_bf16 v[64:67], v[32:35], a[104:107], v[64:67]\n" \
"buffer_load_dwordx4 v4, s[36:39], s61 offen offset:3072 sc1 lds\n" \
"v_mfma_f32_16x16x32_bf16 v[68:71], v[36:39], a[104:107], v[68:71]\n" \
"ds_read_b128 v[24:27], v12 offset:46080\n" \
"v_mfma_f32_16x16x32_bf16 v[72:75], v[32:35], a[232:235], v[72:75]\n" \
"ds_read_b128 v[28:31], v12 offset:62464\n" \
"v_mfma_f32_16x16x32_bf16 v[76:79], v[36:39], a[232:235], v[76:79]\n" \
"s_waitcnt lgkmcnt(4)\n" \
"v_mfma_f32_16x16x32_bf16 v[64:67], v[40:43], a[108:111], v[64:67]\n" \
"s_lshl_b32 s66, s59, s49\n" \
"s_lshr_b32 s65, s66, 8\n" \
"v_mfma_f32_16x16x32_bf16 v[68:71], v[44:47], a[108:111], v[68:71]\n" \
"ds_read_b128 v[32:35], v12 offset:47104\n" \
"v_mfma_f32_16x16x32_bf16 v[72:75], v[40:43], a[236:239], v[72:75]\n" \
"ds_read_b128 v[36:39], v12 offset:63488\n" \
"v_mfma_f32_16x16x32_bf16 v[76:79], v[44:47], a[236:239], v[76:79]\n" \
"s_waitcnt lgkmcnt(4)\n" \
"s_and_b32 s65, s65, 0x2000\n" \
"s_xor_b32 s66, s66, s65\n" \
"v_mfma_f32_16x16x32_bf16 v[64:67], v[16:19], a[112:115], v[64:67]\n" \
"v_mfma_f32_16x16x32_bf16 v[68:71], v[20:23], a[112:115], v[68:71]\n" \
"ds_read_b128 v[40:43], v12 offset:48128\n" \
"v_mfma_f32_16x16x32_bf16 v[72:75], v[16:19], a[240:243], v[72:75]\n" \
"ds_read_b128 v[44:47], v12 offset:64512\n" \
"v_mfma_f32_16x16x32_bf16 v[76:79], v[20:23], a[240:243], v[76:79]\n" \
"s_waitcnt lgkmcnt(4)\n" \
"v_mfma_f32_16x16x32_bf16 v[64:67], v[24:27], a[116:119], v[64:67]\n" \
"s_waitcnt vmcnt(8)\n" \
"s_barrier\n" \
"v_mfma_f32_16x16x32_bf16 v[68:71], v[28:31], a[116:119], v[68:71]\n" \
"ds_read_b128 v[16:19], v10 offset:0\n" \
"v_mfma_f32_16x16x32_bf16 v[72:75], v[24:27], a[244:247], v[72:75]\n" \
"ds_read_b128 v[20:23], v10 offset:16384\n" \
"v_mfma_f32_16x16x32_bf16 v[76:79], v[28:31], a[244:247], v[76:79]\n" \
"s_waitcnt lgkmcnt(4)\n" \
"buffer_store_dword v13, v6, s[36:39], 0 offen sc1\n" \
"v_mfma_f32_16x16x32_bf16 v[64:67], v[32:35], a[120:123], v[64:67]\n" \
"v_cmp_gt_u32_e64 vcc, 16, v15\n" \
"v_mfma_f32_16x16x32_bf16 v[68:71], v[36:39], a[120:123], v[68:71]\n" \
"ds_read_b128 v[24:27], v10 offset:1024\n" \
"v_mfma_f32_16x16x32_bf16 v[72:75], v[32:35], a[248:251], v[72:75]\n" \
"ds_read_b128 v[28:31], v10 offset:17408\n" \
"v_mfma_f32_16x16x32_bf16 v[76:79], v[36:39], a[248:251], v[76:79]\n" \
"s_waitcnt lgkmcnt(4)\n" \
"v_mfma_f32_16x16x32_bf16 v[64:67], v[40:43], a[124:127], v[64:67]\n" \
"s_cbranch_vccnz .Lp1v_m1_%=\n" \
".Lp1v_m1k_%=:\n" \
"v_mfma_f32_16x16x32_bf16 v[68:71], v[44:47], a[124:127], v[68:71]\n" \
"ds_read_b128 v[32:35], v10 offset:2048\n" \
"v_mfma_f32_16x16x32_bf16 v[72:75], v[40:43], a[252:255], v[72:75]\n" \
"ds_read_b128 v[36:39], v10 offset:18432\n" \
"v_mfma_f32_16x16x32_bf16 v[76:79], v[44:47], a[252:255], v[76:79]\n" \
"s_waitcnt lgkmcnt(4)\n" \
"s_xor_b32 s57, s66, s64\n" \
"s_add_u32 s58, s57, s50\n" \
"s_add_u32 s59, s59, 1\n" \
"v_mov_b32 v5, v7\n" \
"v_mov_b32 v6, v11\n" \
"s_cmp_ge_u32 s59, s47\n" \
"s_cbranch_scc0 .Lp1v_loop_%=\n" \
".Lp1v_out1_%=:\n" \
"s_nop 7\n" \
"s_nop 7\n" \
"v_cvt_pk_bf16_f32 v80, v64, v65\n" \
"v_cvt_pk_bf16_f32 v81, v66, v67\n" \
"v_cvt_pk_bf16_f32 v82, v68, v69\n" \
"v_cvt_pk_bf16_f32 v83, v70, v71\n" \
"buffer_store_dwordx4 v[80:83], v5, s[36:39], s57 offen sc1\n" \
"v_cvt_pk_bf16_f32 v84, v72, v73\n" \
"v_cvt_pk_bf16_f32 v85, v74, v75\n" \
"v_cvt_pk_bf16_f32 v86, v76, v77\n" \
"v_cvt_pk_bf16_f32 v87, v78, v79\n" \
"buffer_store_dwordx4 v[84:87], v5, s[36:39], s58 offen sc1\n" \
"s_branch .Lp1v_end_%=\n" \
".Lp1v_out0_%=:\n" \
"s_nop 7\n" \
"s_nop 7\n" \
"v_cvt_pk_bf16_f32 v80, v48, v49\n" \
"v_cvt_pk_bf16_f32 v81, v50, v51\n" \
"v_cvt_pk_bf16_f32 v82, v52, v53\n" \
"v_cvt_pk_bf16_f32 v83, v54, v55\n" \
"buffer_store_dwordx4 v[80:83], v5, s[36:39], s57 offen sc1\n" \
"v_cvt_pk_bf16_f32 v84, v56, v57\n" \
"v_cvt_pk_bf16_f32 v85, v58, v59\n" \
"v_cvt_pk_bf16_f32 v86, v60, v61\n" \
"v_cvt_pk_bf16_f32 v87, v62, v63\n" \
"buffer_store_dwordx4 v[84:87], v5, s[36:39], s58 offen sc1\n" \
".Lp1v_end_%=:\n" \
"s_waitcnt vmcnt(0) lgkmcnt(0)\n" \
"s_barrier\n" \
"v_mov_b32 v13, s59\n" \
"buffer_store_dword v13, v6, s[36:39], 0 offen sc1\n" \
"s_waitcnt vmcnt(0) lgkmcnt(0)\n" \
"s_branch .Lp1v_x_%=\n" \
".Lp1v_l0_%=:\n" \
"s_nop 7\n" \
"s_nop 7\n" \
"s_waitcnt vmcnt(0) lgkmcnt(0)\n" \
"s_mov_b32 s62, 0\n" \
".Lp1v_l0a_%=:\n" \
"buffer_load_dword v15, v14, s[36:39], s63 offen sc1\n" \
"s_waitcnt vmcnt(0)\n" \
"v_cmp_gt_u32 vcc, 16, v15\n" \
"s_nop 4\n" \
"s_cbranch_vccz .Lp1v_l0k_%=\n" \
"s_sleep 4\n" \
"s_add_u32 s62, s62, 1\n" \
"s_cmp_lt_u32 s62, 0x10000\n" \
"s_cbranch_scc1 .Lp1v_l0a_%=\n" \
"v_mov_b32 v15, 256\n" \
"buffer_store_dword v15, v15, s[36:39], 0 offen sc1\n" \
"s_waitcnt vmcnt(0) lgkmcnt(0)\n" \
"s_branch .Lp1v_l0k_%=\n" \
".Lp1v_l1_%=:\n" \
"s_nop 7\n" \
"s_nop 7\n" \
"s_waitcnt vmcnt(0) lgkmcnt(0)\n" \
"s_mov_b32 s62, 0\n" \
".Lp1v_l1a_%=:\n" \
"buffer_load_dword v15, v14, s[36:39], s63 offen sc1\n" \
"s_waitcnt vmcnt(0)\n" \
"v_cmp_gt_u32 vcc, 16, v15\n" \
"s_nop 4\n" \
"s_cbranch_vccz .Lp1v_l1k_%=\n" \
"s_sleep 4\n" \
"s_add_u32 s62, s62, 1\n" \
"s_cmp_lt_u32 s62, 0x10000\n" \
"s_cbranch_scc1 .Lp1v_l1a_%=\n" \
"v_mov_b32 v15, 256\n" \
"buffer_store_dword v15, v15, s[36:39], 0 offen sc1\n" \
"s_waitcnt vmcnt(0) lgkmcnt(0)\n" \
"s_branch .Lp1v_l1k_%=\n" \
".Lp1v_m0_%=:\n" \
"s_nop 7\n" \
"s_nop 7\n" \
"s_waitcnt vmcnt(0) lgkmcnt(0)\n" \
"s_mov_b32 s62, 0\n" \
".Lp1v_m0a_%=:\n" \
"buffer_load_dword v15, v14, s[36:39], s63 offen sc1\n" \
"s_waitcnt vmcnt(0)\n" \
"v_cmp_gt_u32 vcc, 16, v15\n" \
"s_nop 4\n" \
"s_cbranch_vccz .Lp1v_m0k_%=\n" \
"s_sleep 4\n" \
"s_add_u32 s62, s62, 1\n" \
"s_cmp_lt_u32 s62, 0x10000\n" \
"s_cbranch_scc1 .Lp1v_m0a_%=\n" \
"v_mov_b32 v15, 256\n" \
"buffer_store_dword v15, v15, s[36:39], 0 offen sc1\n" \
"s_waitcnt vmcnt(0) lgkmcnt(0)\n" \
"s_branch .Lp1v_m0k_%=\n" \
".Lp1v_m1_%=:\n" \
"s_nop 7\n" \
"s_nop 7\n" \
"s_waitcnt vmcnt(0) lgkmcnt(0)\n" \
"s_mov_b32 s62, 0\n" \
".Lp1v_m1a_%=:\n" \
"buffer_load_dword v15, v14, s[36:39], s63 offen sc1\n" \
"s_waitcnt vmcnt(0)\n" \
"v_cmp_gt_u32 vcc, 16, v15\n" \
"s_nop 4\n" \
"s_cbranch_vccz .Lp1v_m1k_%=\n" \
"s_sleep 4\n" \
"s_add_u32 s62, s62, 1\n" \
"s_cmp_lt_u32 s62, 0x10000\n" \
"s_cbranch_scc1 .Lp1v_m1a_%=\n" \
"v_mov_b32 v15, 256\n" \
"buffer_store_dword v15, v15, s[36:39], 0 offen sc1\n" \
"s_waitcnt vmcnt(0) lgkmcnt(0)\n" \
"s_branch .Lp1v_m1k_%=\n" \
".Lp1v_x_%=:\n"
#define ASM_P1_G1 \
"s_nop 7\n" \
"s_nop 7\n" \
"s_lshr_b32 s52, s48, 1\n" \
"s_lshl_b32 s52, s52, 13\n" \
"s_and_b32 s53, s48, 1\n" \
"s_lshl_b32 s53, s53, 10\n" \
"s_add_u32 s52, s52, s53\n" \
"s_mov_b32 s54, 0\n" \
"s_add_u32 m0, s52, 0\n" \
"s_nop 0\n" \
"buffer_load_dwordx4 v1, s[40:43], s54 offen nt lds\n" \
"s_add_u32 s54, s54, s44\n" \
"s_add_u32 m0, s52, 2048\n" \
"s_nop 0\n" \
"buffer_load_dwordx4 v1, s[40:43], s54 offen nt lds\n" \
"s_add_u32 s54, s54, s44\n" \
"s_add_u32 m0, s52, 4096\n" \
"s_nop 0\n" \
"buffer_load_dwordx4 v1, s[40:43], s54 offen nt lds\n" \
"s_add_u32 s54, s54, s44\n" \
"s_add_u32 m0, s52, 6144\n" \
"s_nop 0\n" \
"buffer_load_dwordx4 v1, s[40:43], s54 offen nt lds\n" \
"s_add_u32 s54, s54, s44\n" \
"s_add_u32 m0, s52, 16384\n" \
"s_nop 0\n" \
"buffer_load_dwordx4 v1, s[40:43], s54 offen nt lds\n" \
"s_add_u32 s54, s54, s44\n" \
"s_add_u32 m0, s52, 18432\n" \
"s_nop 0\n" \
"buffer_load_dwordx4 v1, s[40:43], s54 offen nt lds\n" \
"s_add_u32 s54, s54, s44\n" \
"s_add_u32 m0, s52, 20480\n" \
"s_nop 0\n" \
"buffer_load_dwordx4 v1, s[40:43], s54 offen nt lds\n" \
"s_add_u32 s54, s54, s44\n" \
"s_add_u32 m0, s52, 22528\n" \
"s_nop 0\n" \
"buffer_load_dwordx4 v1, s[40:43], s54 offen nt lds\n" \
"s_add_u32 s54, s54, s44\n" \
"s_add_u32 m0, s52, 32768\n" \
"s_nop 0\n" \
"buffer_load_dwordx4 v1, s[40:43], s54 offen nt lds\n" \
"s_add_u32 s54, s54, s44\n" \
"s_add_u32 m0, s52, 34816\n" \
"s_nop 0\n" \
"buffer_load_dwordx4 v1, s[40:43], s54 offen nt lds\n" \
"s_add_u32 s54, s54, s44\n" \
"s_add_u32 m0, s52, 36864\n" \
"s_nop 0\n" \
"buffer_load_dwordx4 v1, s[40:43], s54 offen nt lds\n" \
"s_add_u32 s54, s54, s44\n" \
"s_add_u32 m0, s52, 38912\n" \
"s_nop 0\n" \
"buffer_load_dwordx4 v1, s[40:43], s54 offen nt lds\n" \
"s_add_u32 s54, s54, s44\n" \
"s_add_u32 m0, s52, 49152\n" \
"s_nop 0\n" \
"buffer_load_dwordx4 v1, s[40:43], s54 offen nt lds\n" \
"s_add_u32 s54, s54, s44\n" \
"s_add_u32 m0, s52, 51200\n" \
"s_nop 0\n" \
"buffer_load_dwordx4 v1, s[40:43], s54 offen nt lds\n" \
"s_add_u32 s54, s54, s44\n" \
"s_add_u32 m0, s52, 53248\n" \
"s_nop 0\n" \
"buffer_load_dwordx4 v1, s[40:43], s54 offen nt lds\n" \
"s_add_u32 s54, s54, s44\n" \
"s_add_u32 m0, s52, 55296\n" \
"s_nop 0\n" \
"buffer_load_dwordx4 v1, s[40:43], s54 offen nt lds\n" \
"s_add_u32 s54, s54, s44\n" \
"s_add_u32 m0, s52, 65536\n" \
"s_nop 0\n" \
"buffer_load_dwordx4 v1, s[40:43], s54 offen nt lds\n" \
"s_add_u32 s54, s54, s44\n" \
"s_add_u32 m0, s52, 67584\n" \
"s_nop 0\n" \
"buffer_load_dwordx4 v1, s[40:43], s54 offen nt lds\n" \
"s_add_u32 s54, s54, s44\n" \
"s_add_u32 m0, s52, 69632\n" \
"s_nop 0\n" \
"buffer_load_dwordx4 v1, s[40:43], s54 offen nt lds\n" \
"s_add_u32 s54, s54, s44\n" \
"s_add_u32 m0, s52, 71680\n" \
"s_nop 0\n" \
"buffer_load_dwordx4 v1, s[40:43], s54 offen nt lds\n" \
"s_add_u32 s54, s54, s44\n" \
"s_add_u32 m0, s52, 81920\n" \
"s_nop 0\n" \
"buffer_load_dwordx4 v1, s[40:43], s54 offen nt lds\n" \
"s_add_u32 s54, s54, s44\n" \
"s_add_u32 m0, s52, 83968\n" \
"s_nop 0\n" \
"buffer_load_dwordx4 v1, s[40:43], s54 offen nt lds\n" \
"s_add_u32 s54, s54, s44\n" \
"s_add_u32 m0, s52, 86016\n" \
"s_nop 0\n" \
"buffer_load_dwordx4 v1, s[40:43], s54 offen nt lds\n" \
"s_add_u32 s54, s54, s44\n" \
"s_add_u32 m0, s52, 88064\n" \
"s_nop 0\n" \
"buffer_load_dwordx4 v1, s[40:43], s54 offen nt lds\n" \
"s_add_u32 s54, s54, s44\n" \
"s_add_u32 m0, s52, 98304\n" \
"s_nop 0\n" \
"buffer_load_dwordx4 v1, s[40:43], s54 offen nt lds\n" \
"s_add_u32 s54, s54, s44\n" \
"s_add_u32 m0, s52, 100352\n" \
"s_nop 0\n" \
"buffer_load_dwordx4 v1, s[40:43], s54 offen nt lds\n" \
"s_add_u32 s54, s54, s44\n" \
"s_add_u32 m0, s52, 102400\n" \
"s_nop 0\n" \
"buffer_load_dwordx4 v1, s[40:43], s54 offen nt lds\n" \
"s_add_u32 s54, s54, s44\n" \
"s_add_u32 m0, s52, 104448\n" \
"s_nop 0\n" \
"buffer_load_dwordx4 v1, s[40:43], s54 offen nt lds\n" \
"s_add_u32 s54, s54, s44\n" \
"s_add_u32 m0, s52, 114688\n" \
"s_nop 0\n" \
"buffer_load_dwordx4 v1, s[40:43], s54 offen nt lds\n" \
"s_add_u32 s54, s54, s44\n" \
"s_add_u32 m0, s52, 116736\n" \
"s_nop 0\n" \
"buffer_load_dwordx4 v1, s[40:43], s54 offen nt lds\n" \
"s_add_u32 s54, s54, s44\n" \
"s_add_u32 m0, s52, 118784\n" \
"s_nop 0\n" \
"buffer_load_dwordx4 v1, s[40:43], s54 offen nt lds\n" \
"s_add_u32 s54, s54, s44\n" \
"s_add_u32 m0, s52, 120832\n" \
"s_nop 0\n" \
"buffer_load_dwordx4 v1, s[40:43], s54 offen nt lds\n" \
"s_add_u32 s54, s54, s44\n" \
"v_add_u32 v8, 0x10000, v2\n" \
"v_add_u32 v9, 0x10000, v3\n" \
"s_waitcnt vmcnt(28)\n" \
"s_barrier\n" \
"ds_read_b64_tr_b16 a[0:1], v2 offset:0\n" \
"ds_read_b64_tr_b16 a[2:3], v2 offset:512\n" \
"ds_read_b64_tr_b16 a[128:129], v3 offset:0\n" \
"ds_read_b64_tr_b16 a[130:131], v3 offset:512\n" \
"ds_read_b64_tr_b16 a[4:5], v2 offset:4096\n" \
"ds_read_b64_tr_b16 a[6:7], v2 offset:4608\n" \
"ds_read_b64_tr_b16 a[132:133], v3 offset:4096\n" \
"ds_read_b64_tr_b16 a[134:135], v3 offset:4608\n" \
"s_waitcnt lgkmcnt(0)\n" \
"s_barrier\n" \
"s_add_u32 m0, s52, 0\n" \
"s_nop 0\n" \
"buffer_load_dwordx4 v1, s[40:43], s54 offen nt lds\n" \
"s_add_u32 s54, s54, s44\n" \
"s_add_u32 m0, s52, 2048\n" \
"s_nop 0\n" \
"buffer_load_dwordx4 v1, s[40:43], s54 offen nt lds\n" \
"s_add_u32 s54, s54, s44\n" \
"s_add_u32 m0, s52, 4096\n" \
"s_nop 0\n" \
"buffer_load_dwordx4 v1, s[40:43], s54 offen nt lds\n" \
"s_add_u32 s54, s54, s44\n" \
"s_add_u32 m0, s52, 6144\n" \
"s_nop 0\n" \
"buffer_load_dwordx4 v1, s[40:43], s54 offen nt lds\n" \
"s_add_u32 s54, s54, s44\n" \
"s_waitcnt vmcnt(28)\n" \
"s_barrier\n" \
"ds_read_b64_tr_b16 a[8:9], v2 offset:16384\n" \
"ds_read_b64_tr_b16 a[10:11], v2 offset:16896\n" \
"ds_read_b64_tr_b16 a[136:137], v3 offset:16384\n" \
"ds_read_b64_tr_b16 a[138:139], v3 offset:16896\n" \
"ds_read_b64_tr_b16 a[12:13], v2 offset:20480\n" \
"ds_read_b64_tr_b16 a[14:15], v2 offset:20992\n" \
"ds_read_b64_tr_b16 a[140:141], v3 offset:20480\n" \
"ds_read_b64_tr_b16 a[142:143], v3 offset:20992\n" \
"s_waitcnt lgkmcnt(0)\n" \
"s_barrier\n" \
"s_add_u32 m0, s52, 16384\n" \
"s_nop 0\n" \
"buffer_load_dwordx4 v1, s[40:43], s54 offen nt lds\n" \
"s_add_u32 s54, s54, s44\n" \
"s_add_u32 m0, s52, 18432\n" \
"s_nop 0\n" \
"buffer_load_dwordx4 v1, s[40:43], s54 offen nt lds\n" \
"s_add_u32 s54, s54, s44\n" \
"s_add_u32 m0, s52, 20480\n" \
"s_nop 0\n" \
"buffer_load_dwordx4 v1, s[40:43], s54 offen nt lds\n" \
"s_add_u32 s54, s54, s44\n" \
"s_add_u32 m0, s52, 22528\n" \
"s_nop 0\n" \
"buffer_load_dwordx4 v1, s[40:43], s54 offen nt lds\n" \
"s_add_u32 s54, s54, s44\n" \
"s_waitcnt vmcnt(28)\n" \
"s_barrier\n" \
"ds_read_b64_tr_b16 a[16:17], v2 offset:32768\n" \
"ds_read_b64_tr_b16 a[18:19], v2 offset:33280\n" \
"ds_read_b64_tr_b16 a[144:145], v3 offset:32768\n" \
"ds_read_b64_tr_b16 a[146:147], v3 offset:33280\n" \
"ds_read_b64_tr_b16 a[20:21], v2 offset:36864\n" \
"ds_read_b64_tr_b16 a[22:23], v2 offset:37376\n" \
"ds_read_b64_tr_b16 a[148:149], v3 offset:36864\n" \
"ds_read_b64_tr_b16 a[150:151], v3 offset:37376\n" \
"s_waitcnt lgkmcnt(0)\n" \
"s_barrier\n" \
"s_add_u32 m0, s52, 32768\n" \
"s_nop 0\n" \
"buffer_load_dwordx4 v1, s[40:43], s54 offen nt lds\n" \
"s_add_u32 s54, s54, s44\n" \
"s_add_u32 m0, s52, 34816\n" \
"s_nop 0\n" \
"buffer_load_dwordx4 v1, s[40:43], s54 offen nt lds\n" \
"s_add_u32 s54, s54, s44\n" \
"s_add_u32 m0, s52, 36864\n" \
"s_nop 0\n" \
"buffer_load_dwordx4 v1, s[40:43], s54 offen nt lds\n" \
"s_add_u32 s54, s54, s44\n" \
"s_add_u32 m0, s52, 38912\n" \
"s_nop 0\n" \
"buffer_load_dwordx4 v1, s[40:43], s54 offen nt lds\n" \
"s_add_u32 s54, s54, s44\n" \
"s_waitcnt vmcnt(28)\n" \
"s_barrier\n" \
"ds_read_b64_tr_b16 a[24:25], v2 offset:49152\n" \
"ds_read_b64_tr_b16 a[26:27], v2 offset:49664\n" \
"ds_read_b64_tr_b16 a[152:153], v3 offset:49152\n" \
"ds_read_b64_tr_b16 a[154:155], v3 offset:49664\n" \
"ds_read_b64_tr_b16 a[28:29], v2 offset:53248\n" \
"ds_read_b64_tr_b16 a[30:31], v2 offset:53760\n" \
"ds_read_b64_tr_b16 a[156:157], v3 offset:53248\n" \
"ds_read_b64_tr_b16 a[158:159], v3 offset:53760\n" \
"s_waitcnt lgkmcnt(0)\n" \
"s_barrier\n" \
"s_add_u32 m0, s52, 49152\n" \
"s_nop 0\n" \
"buffer_load_dwordx4 v1, s[40:43], s54 offen nt lds\n" \
"s_add_u32 s54, s54, s44\n" \
"s_add_u32 m0, s52, 51200\n" \
"s_nop 0\n" \
"buffer_load_dwordx4 v1, s[40:43], s54 offen nt lds\n" \
"s_add_u32 s54, s54, s44\n" \
"s_add_u32 m0, s52, 53248\n" \
"s_nop 0\n" \
"buffer_load_dwordx4 v1, s[40:43], s54 offen nt lds\n" \
"s_add_u32 s54, s54, s44\n" \
"s_add_u32 m0, s52, 55296\n" \
"s_nop 0\n" \
"buffer_load_dwordx4 v1, s[40:43], s54 offen nt lds\n" \
"s_add_u32 s54, s54, s44\n" \
"s_waitcnt vmcnt(28)\n" \
"s_barrier\n" \
"ds_read_b64_tr_b16 a[32:33], v8 offset:0\n" \
"ds_read_b64_tr_b16 a[34:35], v8 offset:512\n" \
"ds_read_b64_tr_b16 a[160:161], v9 offset:0\n" \
"ds_read_b64_tr_b16 a[162:163], v9 offset:512\n" \
"ds_read_b64_tr_b16 a[36:37], v8 offset:4096\n" \
"ds_read_b64_tr_b16 a[38:39], v8 offset:4608\n" \
"ds_read_b64_tr_b16 a[164:165], v9 offset:4096\n" \
"ds_read_b64_tr_b16 a[166:167], v9 offset:4608\n" \
"s_waitcnt lgkmcnt(0)\n" \
"s_barrier\n" \
"s_add_u32 m0, s52, 65536\n" \
"s_nop 0\n" \
"buffer_load_dwordx4 v1, s[40:43], s54 offen nt lds\n" \
"s_add_u32 s54, s54, s44\n" \
"s_add_u32 m0, s52, 67584\n" \
"s_nop 0\n" \
"buffer_load_dwordx4 v1, s[40:43], s54 offen nt lds\n" \
"s_add_u32 s54, s54, s44\n" \
"s_add_u32 m0, s52, 69632\n" \
"s_nop 0\n" \
"buffer_load_dwordx4 v1, s[40:43], s54 offen nt lds\n" \
"s_add_u32 s54, s54, s44\n" \
"s_add_u32 m0, s52, 71680\n" \
"s_nop 0\n" \
"buffer_load_dwordx4 v1, s[40:43], s54 offen nt lds\n" \
"s_add_u32 s54, s54, s44\n" \
"s_waitcnt vmcnt(28)\n" \
"s_barrier\n" \
"ds_read_b64_tr_b16 a[40:41], v8 offset:16384\n" \
"ds_read_b64_tr_b16 a[42:43], v8 offset:16896\n" \
"ds_read_b64_tr_b16 a[168:169], v9 offset:16384\n" \
"ds_read_b64_tr_b16 a[170:171], v9 offset:16896\n" \
"ds_read_b64_tr_b16 a[44:45], v8 offset:20480\n" \
"ds_read_b64_tr_b16 a[46:47], v8 offset:20992\n" \
"ds_read_b64_tr_b16 a[172:173], v9 offset:20480\n" \
"ds_read_b64_tr_b16 a[174:175], v9 offset:20992\n" \
"s_waitcnt lgkmcnt(0)\n" \
"s_barrier\n" \
"s_add_u32 m0, s52, 81920\n" \
"s_nop 0\n" \
"buffer_load_dwordx4 v1, s[40:43], s54 offen nt lds\n" \
"s_add_u32 s54, s54, s44\n" \
"s_add_u32 m0, s52, 83968\n" \
"s_nop 0\n" \
"buffer_load_dwordx4 v1, s[40:43], s54 offen nt lds\n" \
"s_add_u32 s54, s54, s44\n" \
"s_add_u32 m0, s52, 86016\n" \
"s_nop 0\n" \
"buffer_load_dwordx4 v1, s[40:43], s54 offen nt lds\n" \
"s_add_u32 s54, s54, s44\n" \
"s_add_u32 m0, s52, 88064\n" \
"s_nop 0\n" \
"buffer_load_dwordx4 v1, s[40:43], s54 offen nt lds\n" \
"s_add_u32 s54, s54, s44\n" \
"s_waitcnt vmcnt(28)\n" \
"s_barrier\n" \
"ds_read_b64_tr_b16 a[48:49], v8 offset:32768\n" \
"ds_read_b64_tr_b16 a[50:51], v8 offset:33280\n" \
"ds_read_b64_tr_b16 a[176:177], v9 offset:32768\n" \
"ds_read_b64_tr_b16 a[178:179], v9 offset:33280\n" \
"ds_read_b64_tr_b16 a[52:53], v8 offset:36864\n" \
"ds_read_b64_tr_b16 a[54:55], v8 offset:37376\n" \
"ds_read_b64_tr_b16 a[180:181], v9 offset:36864\n" \
"ds_read_b64_tr_b16 a[182:183], v9 offset:37376\n" \
"s_waitcnt lgkmcnt(0)\n" \
"s_barrier\n" \
"s_add_u32 m0, s52, 98304\n" \
"s_nop 0\n" \
"buffer_load_dwordx4 v1, s[40:43], s54 offen nt lds\n" \
"s_add_u32 s54, s54, s44\n" \
"s_add_u32 m0, s52, 100352\n" \
"s_nop 0\n" \
"buffer_load_dwordx4 v1, s[40:43], s54 offen nt lds\n" \
"s_add_u32 s54, s54, s44\n" \
"s_add_u32 m0, s52, 102400\n" \
"s_nop 0\n" \
"buffer_load_dwordx4 v1, s[40:43], s54 offen nt lds\n" \
"s_add_u32 s54, s54, s44\n" \
"s_add_u32 m0, s52, 104448\n" \
"s_nop 0\n" \
"buffer_load_dwordx4 v1, s[40:43], s54 offen nt lds\n" \
"s_add_u32 s54, s54, s44\n" \
"s_waitcnt vmcnt(28)\n" \
"s_barrier\n" \
"ds_read_b64_tr_b16 a[56:57], v8 offset:49152\n" \
"ds_read_b64_tr_b16 a[58:59], v8 offset:49664\n" \
"ds_read_b64_tr_b16 a[184:185], v9 offset:49152\n" \
"ds_read_b64_tr_b16 a[186:187], v9 offset:49664\n" \
"ds_read_b64_tr_b16 a[60:61], v8 offset:53248\n" \
"ds_read_b64_tr_b16 a[62:63], v8 offset:53760\n" \
"ds_read_b64_tr_b16 a[188:189], v9 offset:53248\n" \
"ds_read_b64_tr_b16 a[190:191], v9 offset:53760\n" \
"s_waitcnt lgkmcnt(0)\n" \
"s_barrier\n" \
"s_add_u32 m0, s52, 114688\n" \
"s_nop 0\n" \
"buffer_load_dwordx4 v1, s[40:43], s54 offen nt lds\n" \
"s_add_u32 s54, s54, s44\n" \
"s_add_u32 m0, s52, 116736\n" \
"s_nop 0\n" \
"buffer_load_dwordx4 v1, s[40:43], s54 offen nt lds\n" \
"s_add_u32 s54, s54, s44\n" \
"s_add_u32 m0, s52, 118784\n" \
"s_nop 0\n" \
"buffer_load_dwordx4 v1, s[40:43], s54 offen nt lds\n" \
"s_add_u32 s54, s54, s44\n" \
"s_add_u32 m0, s52, 120832\n" \
"s_nop 0\n" \
"buffer_load_dwordx4 v1, s[40:43], s54 offen nt lds\n" \
"s_add_u32 s54, s54, s44\n" \
"s_waitcnt vmcnt(28)\n" \
"s_barrier\n" \
"ds_read_b64_tr_b16 a[64:65], v2 offset:0\n" \
"ds_read_b64_tr_b16 a[66:67], v2 offset:512\n" \
"ds_read_b64_tr_b16 a[192:193], v3 offset:0\n" \
"ds_read_b64_tr_b16 a[194:195], v3 offset:512\n" \
"ds_read_b64_tr_b16 a[68:69], v2 offset:4096\n" \
"ds_read_b64_tr_b16 a[70:71], v2 offset:4608\n" \
"ds_read_b64_tr_b16 a[196:197], v3 offset:4096\n" \
"ds_read_b64_tr_b16 a[198:199], v3 offset:4608\n" \
"s_waitcnt vmcnt(24)\n" \
"s_barrier\n" \
"ds_read_b64_tr_b16 a[72:73], v2 offset:16384\n" \
"ds_read_b64_tr_b16 a[74:75], v2 offset:16896\n" \
"ds_read_b64_tr_b16 a[200:201], v3 offset:16384\n" \
"ds_read_b64_tr_b16 a[202:203], v3 offset:16896\n" \
"ds_read_b64_tr_b16 a[76:77], v2 offset:20480\n" \
"ds_read_b64_tr_b16 a[78:79], v2 offset:20992\n" \
"ds_read_b64_tr_b16 a[204:205], v3 offset:20480\n" \
"ds_read_b64_tr_b16 a[206:207], v3 offset:20992\n" \
"s_waitcnt vmcnt(20)\n" \
"s_barrier\n" \
"ds_read_b64_tr_b16 a[80:81], v2 offset:32768\n" \
"ds_read_b64_tr_b16 a[82:83], v2 offset:33280\n" \
"ds_read_b64_tr_b16 a[208:209], v3 offset:32768\n" \
"ds_read_b64_tr_b16 a[210:211], v3 offset:33280\n" \
"ds_read_b64_tr_b16 a[84:85], v2 offset:36864\n" \
"ds_read_b64_tr_b16 a[86:87], v2 offset:37376\n" \
"ds_read_b64_tr_b16 a[212:213], v3 offset:36864\n" \
"ds_read_b64_tr_b16 a[214:215], v3 offset:37376\n" \
"s_waitcnt vmcnt(16)\n" \
"s_barrier\n" \
"ds_read_b64_tr_b16 a[88:89], v2 offset:49152\n" \
"ds_read_b64_tr_b16 a[90:91], v2 offset:49664\n" \
"ds_read_b64_tr_b16 a[216:217], v3 offset:49152\n" \
"ds_read_b64_tr_b16 a[218:219], v3 offset:49664\n" \
"ds_read_b64_tr_b16 a[92:93], v2 offset:53248\n" \
"ds_read_b64_tr_b16 a[94:95], v2 offset:53760\n" \
"ds_read_b64_tr_b16 a[220:221], v3 offset:53248\n" \
"ds_read_b64_tr_b16 a[222:223], v3 offset:53760\n" \
"s_waitcnt vmcnt(12)\n" \
"s_barrier\n" \
"ds_read_b64_tr_b16 a[96:97], v8 offset:0\n" \
"ds_read_b64_tr_b16 a[98:99], v8 offset:512\n" \
"ds_read_b64_tr_b16 a[224:225], v9 offset:0\n" \
"ds_read_b64_tr_b16 a[226:227], v9 offset:512\n" \
"ds_read_b64_tr_b16 a[100:101], v8 offset:4096\n" \
"ds_read_b64_tr_b16 a[102:103], v8 offset:4608\n" \
"ds_read_b64_tr_b16 a[228:229], v9 offset:4096\n" \
"ds_read_b64_tr_b16 a[230:231], v9 offset:4608\n" \
"s_waitcnt vmcnt(8)\n" \
"s_barrier\n" \
"ds_read_b64_tr_b16 a[104:105], v8 offset:16384\n" \
"ds_read_b64_tr_b16 a[106:107], v8 offset:16896\n" \
"ds_read_b64_tr_b16 a[232:233], v9 offset:16384\n" \
"ds_read_b64_tr_b16 a[234:235], v9 offset:16896\n" \
"ds_read_b64_tr_b16 a[108:109], v8 offset:20480\n" \
"ds_read_b64_tr_b16 a[110:111], v8 offset:20992\n" \
"ds_read_b64_tr_b16 a[236:237], v9 offset:20480\n" \
"ds_read_b64_tr_b16 a[238:239], v9 offset:20992\n" \
"s_waitcnt vmcnt(4)\n" \
"s_barrier\n" \
"ds_read_b64_tr_b16 a[112:113], v8 offset:32768\n" \
"ds_read_b64_tr_b16 a[114:115], v8 offset:33280\n" \
"ds_read_b64_tr_b16 a[240:241], v9 offset:32768\n" \
"ds_read_b64_tr_b16 a[242:243], v9 offset:33280\n" \
"ds_read_b64_tr_b16 a[116:117], v8 offset:36864\n" \
"ds_read_b64_tr_b16 a[118:119], v8 offset:37376\n" \
"ds_read_b64_tr_b16 a[244:245], v9 offset:36864\n" \
"ds_read_b64_tr_b16 a[246:247], v9 offset:37376\n" \
"s_waitcnt vmcnt(0)\n" \
"s_barrier\n" \
"ds_read_b64_tr_b16 a[120:121], v8 offset:49152\n" \
"ds_read_b64_tr_b16 a[122:123], v8 offset:49664\n" \
"ds_read_b64_tr_b16 a[248:249], v9 offset:49152\n" \
"ds_read_b64_tr_b16 a[250:251], v9 offset:49664\n" \
"ds_read_b64_tr_b16 a[124:125], v8 offset:53248\n" \
"ds_read_b64_tr_b16 a[126:127], v8 offset:53760\n" \
"ds_read_b64_tr_b16 a[252:253], v9 offset:53248\n" \
"ds_read_b64_tr_b16 a[254:255], v9 offset:53760\n" \
"s_waitcnt lgkmcnt(0)\n" \
"s_barrier\n" \
"v_mov_b32 v14, 4096\n" \
"s_mov_b32 s59, s46\n" \
"s_add_u32 s63, s59, 0\n" \
"s_min_u32 s63, s63, 63\n" \
"s_lshr_b32 s63, s63, 3\n" \
"s_lshl_b32 s63, s63, 7\n" \
"s_branch .Lp1g1_l0_%=\n" \
".Lp1g1_l0k_%=:\n" \
"s_add_u32 s63, s59, 1\n" \
"s_min_u32 s63, s63, 63\n" \
"s_lshr_b32 s63, s63, 3\n" \
"s_lshl_b32 s63, s63, 7\n" \
"s_branch .Lp1g1_l1_%=\n" \
".Lp1g1_l1k_%=:\n" \
"s_lshl_b32 s55, s48, 13\n" \
"s_lshl_b32 s56, s46, 17\n" \
"s_add_u32 s56, s56, s45\n" \
"s_lshl_b32 s60, s47, 17\n" \
"s_sub_u32 s60, s60, 0x20000\n" \
"s_add_u32 s60, s60, s45\n" \
"s_mov_b32 s57, 0\n" \
"s_mov_b32 s58, 0\n" \
"v_mov_b32 v7, v5\n" \
"v_mov_b32 v5, 0x7ffffff0\n" \
"v_mov_b32 v10, v0\n" \
"v_mov_b32 v11, v6\n" \
"v_mov_b32 v6, 0x7ffffff0\n" \
"s_mov_b32 s59, s46\n" \
"s_min_u32 s61, s56, s60\n" \
"s_lshr_b32 s65, s61, 8\n" \
"s_and_b32 s65, s65, 0x2000\n" \
"s_xor_b32 s61, s61, s65\n" \
"s_add_u32 m0, s55, 0\n" \
"s_nop 0\n" \
"buffer_load_dwordx4 v4, s[36:39], s61 offen offset:0 sc1 lds\n" \
"buffer_load_dwordx4 v4, s[36:39], s61 offen offset:1024 sc1 lds\n" \
"buffer_load_dwordx4 v4, s[36:39], s61 offen offset:2048 sc1 lds\n" \
"buffer_load_dwordx4 v4, s[36:39], s61 offen offset:3072 sc1 lds\n" \
"s_add_u32 m0, s55, 4096\n" \
"s_add_u32 s61, s61, 4096\n" \
"buffer_load_dwordx4 v4, s[36:39], s61 offen offset:0 sc1 lds\n" \
"buffer_load_dwordx4 v4, s[36:39], s61 offen offset:1024 sc1 lds\n" \
"buffer_load_dwordx4 v4, s[36:39], s61 offen offset:2048 sc1 lds\n" \
"buffer_load_dwordx4 v4, s[36:39], s61 offen offset:3072 sc1 lds\n" \
"s_add_u32 s61, s61, 28672\n" \
"s_add_u32 s56, s56, 0x20000\n" \
"s_add_u32 m0, s55, 32768\n" \
"s_nop 0\n" \
"buffer_load_dwordx4 v4, s[36:39], s61 offen offset:0 sc1 lds\n" \
"buffer_load_dwordx4 v4, s[36:39], s61 offen offset:1024 sc1 lds\n" \
"buffer_load_dwordx4 v4, s[36:39], s61 offen offset:2048 sc1 lds\n" \
"buffer_load_dwordx4 v4, s[36:39], s61 offen offset:3072 sc1 lds\n" \
"s_add_u32 m0, s55, 36864\n" \
"s_add_u32 s61, s61, 4096\n" \
"buffer_load_dwordx4 v4, s[36:39], s61 offen offset:0 sc1 lds\n" \
"buffer_load_dwordx4 v4, s[36:39], s61 offen offset:1024 sc1 lds\n" \
"buffer_load_dwordx4 v4, s[36:39], s61 offen offset:2048 sc1 lds\n" \
"buffer_load_dwordx4 v4, s[36:39], s61 offen offset:3072 sc1 lds\n" \
"s_mov_b32 s70, 0\n" \
"s_mov_b32 s72, 0\n" \
"s_waitcnt vmcnt(0) lgkmcnt(0)\n" \
"s_barrier\n" \
"v_add_u32 v12, 0x10000, v10\n" \
"ds_read_b128 v[16:19], v10 offset:0\n" \
"ds_read_b128 v[20:23], v10 offset:16384\n" \
"ds_read_b128 v[24:27], v10 offset:1024\n" \
"ds_read_b128 v[28:31], v10 offset:17408\n" \
"ds_read_b128 v[32:35], v10 offset:2048\n" \
"ds_read_b128 v[36:39], v10 offset:18432\n" \
"s_waitcnt lgkmcnt(4)\n" \
"s_nop 7\n" \
"s_nop 7\n" \
".p2align 6\n" \
".Lp1g1_loop_%=:\n" \
"v_mfma_f32_16x16x32_bf16 v[48:51], a[0:3], v[16:19], 0\n" \
"s_min_u32 s61, s56, s60\n" \
"s_lshr_b32 s65, s61, 8\n" \
"s_and_b32 s65, s65, 0x2000\n" \
"s_xor_b32 s61, s61, s65\n" \
"s_add_u32 m0, s55, 65536\n" \
"s_nop 0\n" \
"buffer_load_dwordx4 v4, s[36:39], s61 offen offset:0 sc1 lds\n" \
"v_add_u32_e64 v13, -1, s59\n" \
"v_mfma_f32_16x16x32_bf16 v[52:55], a[0:3], v[20:23], 0\n" \
"ds_read_b128 v[40:43], v10 offset:3072\n" \
"s_nop 0\n" \
"v_mfma_f32_16x16x32_bf16 v[56:59], a[128:131], v[16:19], 0\n" \
"ds_read_b128 v[44:47], v10 offset:19456\n" \
"s_nop 0\n" \
"v_mfma_f32_16x16x32_bf16 v[60:63], a[128:131], v[20:23], 0\n" \
"s_waitcnt lgkmcnt(4)\n" \
"v_mul_f32 v64, 0xbfb8aa3b, v64\n" \
"v_mul_f32 v65, 0xbfb8aa3b, v65\n" \
"v_mfma_f32_16x16x32_bf16 v[48:51], a[4:7], v[24:27], v[48:51]\n" \
"buffer_load_dwordx4 v4, s[36:39], s61 offen offset:1024 sc1 lds\n" \
"v_mfma_f32_16x16x32_bf16 v[52:55], a[4:7], v[28:31], v[52:55]\n" \
"ds_read_b128 v[16:19], v10 offset:4096\n" \
"v_mul_f32 v66, 0xbfb8aa3b, v66\n" \
"v_mfma_f32_16x16x32_bf16 v[56:59], a[132:135], v[24:27], v[56:59]\n" \
"ds_read_b128 v[20:23], v10 offset:20480\n" \
"v_mul_f32 v67, 0xbfb8aa3b, v67\n" \
"v_mfma_f32_16x16x32_bf16 v[60:63], a[132:135], v[28:31], v[60:63]\n" \
"s_waitcnt lgkmcnt(4)\n" \
"v_exp_f32_e64 v64, v64\n" \
"v_mfma_f32_16x16x32_bf16 v[48:51], a[8:11], v[32:35], v[48:51]\n" \
"s_add_u32 s63, s59, 2\n" \
"s_min_u32 s63, s63, 63\n" \
"v_mfma_f32_16x16x32_bf16 v[52:55], a[8:11], v[36:39], v[52:55]\n" \
"ds_read_b128 v[24:27], v10 offset:5120\n" \
"s_lshr_b32 s63, s63, 3\n" \
"s_lshl_b32 s63, s63, 7\n" \
"v_mfma_f32_16x16x32_bf16 v[56:59], a[136:139], v[32:35], v[56:59]\n" \
"ds_read_b128 v[28:31], v10 offset:21504\n" \
"v_mfma_f32_16x16x32_bf16 v[60:63], a[136:139], v[36:39], v[60:63]\n" \
"s_waitcnt lgkmcnt(4)\n" \
"v_exp_f32 v65, v65\n" \
"v_mfma_f32_16x16x32_bf16 v[48:51], a[12:15], v[40:43], v[48:51]\n" \
"buffer_load_dwordx4 v4, s[36:39], s61 offen offset:2048 sc1 lds\n" \
"v_mfma_f32_16x16x32_bf16 v[52:55], a[12:15], v[44:47], v[52:55]\n" \
"ds_read_b128 v[32:35], v10 offset:6144\n" \
"v_mfma_f32_16x16x32_bf16 v[56:59], a[140:143], v[40:43], v[56:59]\n" \
"ds_read_b128 v[36:39], v10 offset:22528\n" \
"v_mfma_f32_16x16x32_bf16 v[60:63], a[140:143], v[44:47], v[60:63]\n" \
"s_waitcnt lgkmcnt(4)\n" \
"v_exp_f32 v66, v66\n" \
"v_mfma_f32_16x16x32_bf16 v[48:51], a[16:19], v[16:19], v[48:51]\n" \
"buffer_load_dwordx4 v4, s[36:39], s61 offen offset:3072 sc1 lds\n" \
"v_mfma_f32_16x16x32_bf16 v[52:55], a[16:19], v[20:23], v[52:55]\n" \
"ds_read_b128 v[40:43], v10 offset:7168\n" \
"v_mfma_f32_16x16x32_bf16 v[56:59], a[144:147], v[16:19], v[56:59]\n" \
"ds_read_b128 v[44:47], v10 offset:23552\n" \
"v_mfma_f32_16x16x32_bf16 v[60:63], a[144:147], v[20:23], v[60:63]\n" \
"s_waitcnt lgkmcnt(4)\n" \
"v_exp_f32 v67, v67\n" \
"v_mfma_f32_16x16x32_bf16 v[48:51], a[20:23], v[24:27], v[48:51]\n" \
"buffer_load_dword v15, v14, s[36:39], s63 offen sc1\n" \
"v_mfma_f32_16x16x32_bf16 v[52:55], a[20:23], v[28:31], v[52:55]\n" \
"ds_read_b128 v[16:19], v10 offset:8192\n" \
"v_add_f32_e64 v64, 1.0, v64\n" \
"v_mfma_f32_16x16x32_bf16 v[56:59], a[148:151], v[24:27], v[56:59]\n" \
"ds_read_b128 v[20:23], v10 offset:24576\n" \
"v_add_f32_e64 v65, 1.0, v65\n" \
"v_mfma_f32_16x16x32_bf16 v[60:63], a[148:151], v[28:31], v[60:63]\n" \
"s_waitcnt lgkmcnt(4)\n" \
"v_add_f32 v66, 1.0, v66\n" \
"v_add_f32_e64 v67, 1.0, v67\n" \
"v_mfma_f32_16x16x32_bf16 v[48:51], a[24:27], v[32:35], v[48:51]\n" \
"s_add_u32 m0, s55, 69632\n" \
"s_add_u32 s61, s61, 4096\n" \
"buffer_load_dwordx4 v4, s[36:39], s61 offen offset:0 sc1 lds\n" \
"v_mfma_f32_16x16x32_bf16 v[52:55], a[24:27], v[36:39], v[52:55]\n" \
"ds_read_b128 v[24:27], v10 offset:9216\n" \
"v_mfma_f32_16x16x32_bf16 v[56:59], a[152:155], v[32:35], v[56:59]\n" \
"ds_read_b128 v[28:31], v10 offset:25600\n" \
"v_mfma_f32_16x16x32_bf16 v[60:63], a[152:155], v[36:39], v[60:63]\n" \
"s_waitcnt lgkmcnt(4)\n" \
"v_rcp_f32 v64, v64\n" \
"v_mfma_f32_16x16x32_bf16 v[48:51], a[28:31], v[40:43], v[48:51]\n" \
"buffer_load_dwordx4 v4, s[36:39], s61 offen offset:1024 sc1 lds\n" \
"v_mfma_f32_16x16x32_bf16 v[52:55], a[28:31], v[44:47], v[52:55]\n" \
"ds_read_b128 v[32:35], v10 offset:10240\n" \
"v_mfma_f32_16x16x32_bf16 v[56:59], a[156:159], v[40:43], v[56:59]\n" \
"ds_read_b128 v[36:39], v10 offset:26624\n" \
"v_mfma_f32_16x16x32_bf16 v[60:63], a[156:159], v[44:47], v[60:63]\n" \
"s_waitcnt lgkmcnt(4)\n" \
"v_rcp_f32 v65, v65\n" \
"v_mfma_f32_16x16x32_bf16 v[48:51], a[32:35], v[16:19], v[48:51]\n" \
"v_rcp_f32_e64 v66, v66\n" \
"v_mfma_f32_16x16x32_bf16 v[52:55], a[32:35], v[20:23], v[52:55]\n" \
"ds_read_b128 v[40:43], v10 offset:11264\n" \
"v_mfma_f32_16x16x32_bf16 v[56:59], a[160:163], v[16:19], v[56:59]\n" \
"ds_read_b128 v[44:47], v10 offset:27648\n" \
"v_mfma_f32_16x16x32_bf16 v[60:63], a[160:163], v[20:23], v[60:63]\n" \
"s_waitcnt lgkmcnt(4)\n" \
"v_rcp_f32 v67, v67\n" \
"v_mfma_f32_16x16x32_bf16 v[48:51], a[36:39], v[24:27], v[48:51]\n" \
"buffer_load_dwordx4 v4, s[36:39], s61 offen offset:2048 sc1 lds\n" \
"v_mfma_f32_16x16x32_bf16 v[52:55], a[36:39], v[28:31], v[52:55]\n" \
"ds_read_b128 v[16:19], v10 offset:12288\n" \
"v_mul_f32_e64 v64, v64, v72\n" \
"v_mfma_f32_16x16x32_bf16 v[56:59], a[164:167], v[24:27], v[56:59]\n" \
"ds_read_b128 v[20:23], v10 offset:28672\n" \
"v_mul_f32_e64 v65, v65, v73\n" \
"v_mfma_f32_16x16x32_bf16 v[60:63], a[164:167], v[28:31], v[60:63]\n" \
"s_waitcnt lgkmcnt(4)\n" \
"v_mul_f32 v66, v66, v74\n" \
"v_mul_f32_e64 v67, v67, v75\n" \
"v_mfma_f32_16x16x32_bf16 v[48:51], a[40:43], v[32:35], v[48:51]\n" \
"buffer_load_dwordx4 v4, s[36:39], s61 offen offset:3072 sc1 lds\n" \
"v_mfma_f32_16x16x32_bf16 v[52:55], a[40:43], v[36:39], v[52:55]\n" \
"ds_read_b128 v[24:27], v10 offset:13312\n" \
"v_cvt_pk_bf16_f32 v80, v64, v65\n" \
"v_mfma_f32_16x16x32_bf16 v[56:59], a[168:171], v[32:35], v[56:59]\n" \
"ds_read_b128 v[28:31], v10 offset:29696\n" \
"v_cvt_pk_bf16_f32 v81, v66, v67\n" \
"v_mfma_f32_16x16x32_bf16 v[60:63], a[168:171], v[36:39], v[60:63]\n" \
"s_waitcnt lgkmcnt(4)\n" \
"v_mul_f32 v68, 0xbfb8aa3b, v68\n" \
"v_mul_f32 v69, 0xbfb8aa3b, v69\n" \
"v_mfma_f32_16x16x32_bf16 v[48:51], a[44:47], v[40:43], v[48:51]\n" \
"v_mul_f32 v70, 0xbfb8aa3b, v70\n" \
"v_mul_f32 v71, 0xbfb8aa3b, v71\n" \
"v_mfma_f32_16x16x32_bf16 v[52:55], a[44:47], v[44:47], v[52:55]\n" \
"ds_read_b128 v[32:35], v10 offset:14336\n" \
"v_mfma_f32_16x16x32_bf16 v[56:59], a[172:175], v[40:43], v[56:59]\n" \
"ds_read_b128 v[36:39], v10 offset:30720\n" \
"v_mfma_f32_16x16x32_bf16 v[60:63], a[172:175], v[44:47], v[60:63]\n" \
"s_waitcnt lgkmcnt(4)\n" \
"v_exp_f32_e64 v68, v68\n" \
"v_mfma_f32_16x16x32_bf16 v[48:51], a[48:51], v[16:19], v[48:51]\n" \
"v_exp_f32_e64 v69, v69\n" \
"v_mfma_f32_16x16x32_bf16 v[52:55], a[48:51], v[20:23], v[52:55]\n" \
"ds_read_b128 v[40:43], v10 offset:15360\n" \
"v_mfma_f32_16x16x32_bf16 v[56:59], a[176:179], v[16:19], v[56:59]\n" \
"ds_read_b128 v[44:47], v10 offset:31744\n" \
"v_mfma_f32_16x16x32_bf16 v[60:63], a[176:179], v[20:23], v[60:63]\n" \
"s_waitcnt lgkmcnt(4)\n" \
"v_exp_f32 v70, v70\n" \
"v_mfma_f32_16x16x32_bf16 v[48:51], a[52:55], v[24:27], v[48:51]\n" \
"s_waitcnt vmcnt(9)\n" \
"s_barrier\n" \
"v_mfma_f32_16x16x32_bf16 v[52:55], a[52:55], v[28:31], v[52:55]\n" \
"ds_read_b128 v[16:19], v10 offset:32768\n" \
"v_mfma_f32_16x16x32_bf16 v[56:59], a[180:183], v[24:27], v[56:59]\n" \
"ds_read_b128 v[20:23], v10 offset:49152\n" \
"v_mfma_f32_16x16x32_bf16 v[60:63], a[180:183], v[28:31], v[60:63]\n" \
"s_waitcnt lgkmcnt(4)\n" \
"buffer_store_dword v13, v6, s[36:39], 0 offen sc1\n" \
"v_exp_f32 v71, v71\n" \
"v_mfma_f32_16x16x32_bf16 v[48:51], a[56:59], v[32:35], v[48:51]\n" \
"v_add_f32 v68, 1.0, v68\n" \
"v_add_f32 v69, 1.0, v69\n" \
"v_mfma_f32_16x16x32_bf16 v[52:55], a[56:59], v[36:39], v[52:55]\n" \
"ds_read_b128 v[24:27], v10 offset:33792\n" \
"v_add_f32_e64 v70, 1.0, v70\n" \
"v_mfma_f32_16x16x32_bf16 v[56:59], a[184:187], v[32:35], v[56:59]\n" \
"ds_read_b128 v[28:31], v10 offset:50176\n" \
"v_add_f32_e64 v71, 1.0, v71\n" \
"v_mfma_f32_16x16x32_bf16 v[60:63], a[184:187], v[36:39], v[60:63]\n" \
"s_waitcnt lgkmcnt(4)\n" \
"v_rcp_f32 v68, v68\n" \
"v_mfma_f32_16x16x32_bf16 v[48:51], a[60:63], v[40:43], v[48:51]\n" \
"v_rcp_f32_e64 v69, v69\n" \
"v_mfma_f32_16x16x32_bf16 v[52:55], a[60:63], v[44:47], v[52:55]\n" \
"ds_read_b128 v[32:35], v10 offset:34816\n" \
"v_mfma_f32_16x16x32_bf16 v[56:59], a[188:191], v[40:43], v[56:59]\n" \
"ds_read_b128 v[36:39], v10 offset:51200\n" \
"v_mfma_f32_16x16x32_bf16 v[60:63], a[188:191], v[44:47], v[60:63]\n" \
"s_waitcnt lgkmcnt(4)\n" \
"v_rcp_f32 v70, v70\n" \
"v_mfma_f32_16x16x32_bf16 v[48:51], a[64:67], v[16:19], v[48:51]\n" \
"s_add_u32 s61, s61, 28672\n" \
"s_add_u32 s56, s56, 0x20000\n" \
"s_add_u32 m0, s55, 98304\n" \
"s_nop 0\n" \
"buffer_load_dwordx4 v4, s[36:39], s61 offen offset:0 sc1 lds\n" \
"v_mfma_f32_16x16x32_bf16 v[52:55], a[64:67], v[20:23], v[52:55]\n" \
"ds_read_b128 v[40:43], v10 offset:35840\n" \
"v_mfma_f32_16x16x32_bf16 v[56:59], a[192:195], v[16:19], v[56:59]\n" \
"ds_read_b128 v[44:47], v10 offset:52224\n" \
"v_mfma_f32_16x16x32_bf16 v[60:63], a[192:195], v[20:23], v[60:63]\n" \
"s_waitcnt lgkmcnt(4)\n" \
"v_rcp_f32_e64 v71, v71\n" \
"v_mfma_f32_16x16x32_bf16 v[48:51], a[68:71], v[24:27], v[48:51]\n" \
"buffer_load_dwordx4 v4, s[36:39], s61 offen offset:1024 sc1 lds\n" \
"v_mfma_f32_16x16x32_bf16 v[52:55], a[68:71], v[28:31], v[52:55]\n" \
"ds_read_b128 v[16:19], v10 offset:36864\n" \
"v_mul_f32_e64 v68, v68, v76\n" \
"v_mfma_f32_16x16x32_bf16 v[56:59], a[196:199], v[24:27], v[56:59]\n" \
"ds_read_b128 v[20:23], v10 offset:53248\n" \
"v_mul_f32_e64 v69, v69, v77\n" \
"v_mfma_f32_16x16x32_bf16 v[60:63], a[196:199], v[28:31], v[60:63]\n" \
"s_waitcnt lgkmcnt(4)\n" \
"v_mul_f32 v70, v70, v78\n" \
"v_mul_f32_e64 v71, v71, v79\n" \
"v_mfma_f32_16x16x32_bf16 v[48:51], a[72:75], v[32:35], v[48:51]\n" \
"v_cvt_pk_bf16_f32 v82, v68, v69\n" \
"v_cvt_pk_bf16_f32 v83, v70, v71\n" \
"v_mfma_f32_16x16x32_bf16 v[52:55], a[72:75], v[36:39], v[52:55]\n" \
"ds_read_b128 v[24:27], v10 offset:37888\n" \
"s_nop 0\n" \
"v_mfma_f32_16x16x32_bf16 v[56:59], a[200:203], v[32:35], v[56:59]\n" \
"ds_read_b128 v[28:31], v10 offset:54272\n" \
"s_nop 0\n" \
"v_mfma_f32_16x16x32_bf16 v[60:63], a[200:203], v[36:39], v[60:63]\n" \
"s_waitcnt lgkmcnt(4)\n" \
"v_permlane16_swap_b32 v80, v82\n" \
"v_permlane16_swap_b32 v81, v83\n" \
"v_mfma_f32_16x16x32_bf16 v[48:51], a[76:79], v[40:43], v[48:51]\n" \
"buffer_load_dwordx4 v4, s[36:39], s61 offen offset:2048 sc1 lds\n" \
"v_mfma_f32_16x16x32_bf16 v[52:55], a[76:79], v[44:47], v[52:55]\n" \
"ds_read_b128 v[32:35], v10 offset:38912\n" \
"buffer_store_dwordx4 v[80:83], v5, s[36:39], s57 offen sc1\n" \
"v_mfma_f32_16x16x32_bf16 v[56:59], a[204:207], v[40:43], v[56:59]\n" \
"ds_read_b128 v[36:39], v10 offset:55296\n" \
"v_mfma_f32_16x16x32_bf16 v[60:63], a[204:207], v[44:47], v[60:63]\n" \
"s_waitcnt lgkmcnt(4)\n" \
"v_mfma_f32_16x16x32_bf16 v[48:51], a[80:83], v[16:19], v[48:51]\n" \
"buffer_load_dwordx4 v4, s[36:39], s61 offen offset:3072 sc1 lds\n" \
"v_mfma_f32_16x16x32_bf16 v[52:55], a[80:83], v[20:23], v[52:55]\n" \
"ds_read_b128 v[40:43], v10 offset:39936\n" \
"v_mfma_f32_16x16x32_bf16 v[56:59], a[208:211], v[16:19], v[56:59]\n" \
"ds_read_b128 v[44:47], v10 offset:56320\n" \
"v_mfma_f32_16x16x32_bf16 v[60:63], a[208:211], v[20:23], v[60:63]\n" \
"s_waitcnt lgkmcnt(4)\n" \
"v_mfma_f32_16x16x32_bf16 v[48:51], a[84:87], v[24:27], v[48:51]\n" \
"v_mfma_f32_16x16x32_bf16 v[52:55], a[84:87], v[28:31], v[52:55]\n" \
"ds_read_b128 v[16:19], v10 offset:40960\n" \
"v_mfma_f32_16x16x32_bf16 v[56:59], a[212:215], v[24:27], v[56:59]\n" \
"ds_read_b128 v[20:23], v10 offset:57344\n" \
"v_mfma_f32_16x16x32_bf16 v[60:63], a[212:215], v[28:31], v[60:63]\n" \
"s_waitcnt lgkmcnt(4)\n" \
"v_mfma_f32_16x16x32_bf16 v[48:51], a[88:91], v[32:35], v[48:51]\n" \
"s_add_u32 m0, s55, 102400\n" \
"s_add_u32 s61, s61, 4096\n" \
"buffer_load_dwordx4 v4, s[36:39], s61 offen offset:0 sc1 lds\n" \
"v_mfma_f32_16x16x32_bf16 v[52:55], a[88:91], v[36:39], v[52:55]\n" \
"ds_read_b128 v[24:27], v10 offset:41984\n" \
"v_mfma_f32_16x16x32_bf16 v[56:59], a[216:219], v[32:35], v[56:59]\n" \
"ds_read_b128 v[28:31], v10 offset:58368\n" \
"v_mfma_f32_16x16x32_bf16 v[60:63], a[216:219], v[36:39], v[60:63]\n" \
"s_waitcnt lgkmcnt(4)\n" \
"v_mfma_f32_16x16x32_bf16 v[48:51], a[92:95], v[40:43], v[48:51]\n" \
"buffer_load_dwordx4 v4, s[36:39], s61 offen offset:1024 sc1 lds\n" \
"v_mfma_f32_16x16x32_bf16 v[52:55], a[92:95], v[44:47], v[52:55]\n" \
"ds_read_b128 v[32:35], v10 offset:43008\n" \
"v_mfma_f32_16x16x32_bf16 v[56:59], a[220:223], v[40:43], v[56:59]\n" \
"ds_read_b128 v[36:39], v10 offset:59392\n" \
"v_mfma_f32_16x16x32_bf16 v[60:63], a[220:223], v[44:47], v[60:63]\n" \
"s_waitcnt lgkmcnt(4)\n" \
"v_mfma_f32_16x16x32_bf16 v[48:51], a[96:99], v[16:19], v[48:51]\n" \
"v_mfma_f32_16x16x32_bf16 v[52:55], a[96:99], v[20:23], v[52:55]\n" \
"ds_read_b128 v[40:43], v10 offset:44032\n" \
"v_mfma_f32_16x16x32_bf16 v[56:59], a[224:227], v[16:19], v[56:59]\n" \
"ds_read_b128 v[44:47], v10 offset:60416\n" \
"v_mfma_f32_16x16x32_bf16 v[60:63], a[224:227], v[20:23], v[60:63]\n" \
"s_waitcnt lgkmcnt(4)\n" \
"v_mfma_f32_16x16x32_bf16 v[48:51], a[100:103], v[24:27], v[48:51]\n" \
"buffer_load_dwordx4 v4, s[36:39], s61 offen offset:2048 sc1 lds\n" \
"v_mfma_f32_16x16x32_bf16 v[52:55], a[100:103], v[28:31], v[52:55]\n" \
"ds_read_b128 v[16:19], v10 offset:45056\n" \
"v_mfma_f32_16x16x32_bf16 v[56:59], a[228:231], v[24:27], v[56:59]\n" \
"ds_read_b128 v[20:23], v10 offset:61440\n" \
"v_mfma_f32_16x16x32_bf16 v[60:63], a[228:231], v[28:31], v[60:63]\n" \
"s_waitcnt lgkmcnt(4)\n" \
"v_mfma_f32_16x16x32_bf16 v[48:51], a[104:107], v[32:35], v[48:51]\n" \
"buffer_load_dwordx4 v4, s[36:39], s61 offen offset:3072 sc1 lds\n" \
"v_mfma_f32_16x16x32_bf16 v[52:55], a[104:107], v[36:39], v[52:55]\n" \
"ds_read_b128 v[24:27], v10 offset:46080\n" \
"v_mfma_f32_16x16x32_bf16 v[56:59], a[232:235], v[32:35], v[56:59]\n" \
"ds_read_b128 v[28:31], v10 offset:62464\n" \
"v_mfma_f32_16x16x32_bf16 v[60:63], a[232:235], v[36:39], v[60:63]\n" \
"s_waitcnt lgkmcnt(4)\n" \
"v_mfma_f32_16x16x32_bf16 v[48:51], a[108:111], v[40:43], v[48:51]\n" \
"s_lshl_b32 s66, s59, s49\n" \
"s_lshr_b32 s65, s66, 8\n" \
"v_mfma_f32_16x16x32_bf16 v[52:55], a[108:111], v[44:47], v[52:55]\n" \
"ds_read_b128 v[32:35], v10 offset:47104\n" \
"v_mfma_f32_16x16x32_bf16 v[56:59], a[236:239], v[40:43], v[56:59]\n" \
"ds_read_b128 v[36:39], v10 offset:63488\n" \
"v_mfma_f32_16x16x32_bf16 v[60:63], a[236:239], v[44:47], v[60:63]\n" \
"s_waitcnt lgkmcnt(4)\n" \
"s_and_b32 s65, s65, 0x2000\n" \
"s_xor_b32 s66, s66, s65\n" \
"v_mfma_f32_16x16x32_bf16 v[48:51], a[112:115], v[16:19], v[48:51]\n" \
"v_mfma_f32_16x16x32_bf16 v[52:55], a[112:115], v[20:23], v[52:55]\n" \
"ds_read_b128 v[40:43], v10 offset:48128\n" \
"v_mfma_f32_16x16x32_bf16 v[56:59], a[240:243], v[16:19], v[56:59]\n" \
"ds_read_b128 v[44:47], v10 offset:64512\n" \
"v_mfma_f32_16x16x32_bf16 v[60:63], a[240:243], v[20:23], v[60:63]\n" \
"s_waitcnt lgkmcnt(4)\n" \
"v_mfma_f32_16x16x32_bf16 v[48:51], a[116:119], v[24:27], v[48:51]\n" \
"s_waitcnt vmcnt(10)\n" \
"s_barrier\n" \
"v_mfma_f32_16x16x32_bf16 v[52:55], a[116:119], v[28:31], v[52:55]\n" \
"ds_read_b128 v[16:19], v12 offset:0\n" \
"v_mfma_f32_16x16x32_bf16 v[56:59], a[244:247], v[24:27], v[56:59]\n" \
"ds_read_b128 v[20:23], v12 offset:16384\n" \
"v_mfma_f32_16x16x32_bf16 v[60:63], a[244:247], v[28:31], v[60:63]\n" \
"s_waitcnt lgkmcnt(4)\n" \
"v_mfma_f32_16x16x32_bf16 v[48:51], a[120:123], v[32:35], v[48:51]\n" \
"v_cmp_gt_u32 vcc, 16, v15\n" \
"v_mfma_f32_16x16x32_bf16 v[52:55], a[120:123], v[36:39], v[52:55]\n" \
"ds_read_b128 v[24:27], v12 offset:1024\n" \
"v_mfma_f32_16x16x32_bf16 v[56:59], a[248:251], v[32:35], v[56:59]\n" \
"ds_read_b128 v[28:31], v12 offset:17408\n" \
"v_mfma_f32_16x16x32_bf16 v[60:63], a[248:251], v[36:39], v[60:63]\n" \
"s_waitcnt lgkmcnt(4)\n" \
"v_mfma_f32_16x16x32_bf16 v[48:51], a[124:127], v[40:43], v[48:51]\n" \
"s_cbranch_vccnz .Lp1g1_m0_%=\n" \
".Lp1g1_m0k_%=:\n" \
"v_mfma_f32_16x16x32_bf16 v[52:55], a[124:127], v[44:47], v[52:55]\n" \
"ds_read_b128 v[32:35], v12 offset:2048\n" \
"v_mfma_f32_16x16x32_bf16 v[56:59], a[252:255], v[40:43], v[56:59]\n" \
"ds_read_b128 v[36:39], v12 offset:18432\n" \
"v_mfma_f32_16x16x32_bf16 v[60:63], a[252:255], v[44:47], v[60:63]\n" \
"s_waitcnt lgkmcnt(4)\n" \
"s_xor_b32 s57, s66, s64\n" \
"s_add_u32 s58, s57, s50\n" \
"s_add_u32 s59, s59, 1\n" \
"v_mov_b32 v5, v7\n" \
"v_mov_b32 v6, v11\n" \
"s_cmp_ge_u32 s59, s47\n" \
"s_cbranch_scc1 .Lp1g1_out0_%=\n" \
"v_mfma_f32_16x16x32_bf16 v[64:67], a[0:3], v[16:19], 0\n" \
"s_min_u32 s61, s56, s60\n" \
"s_lshr_b32 s65, s61, 8\n" \
"s_and_b32 s65, s65, 0x2000\n" \
"s_xor_b32 s61, s61, s65\n" \
"s_add_u32 m0, s55, 0\n" \
"s_nop 0\n" \
"buffer_load_dwordx4 v4, s[36:39], s61 offen offset:0 sc1 lds\n" \
"v_add_u32_e64 v13, -1, s59\n" \
"v_mfma_f32_16x16x32_bf16 v[68:71], a[0:3], v[20:23], 0\n" \
"ds_read_b128 v[40:43], v12 offset:3072\n" \
"s_nop 0\n" \
"v_mfma_f32_16x16x32_bf16 v[72:75], a[128:131], v[16:19], 0\n" \
"ds_read_b128 v[44:47], v12 offset:19456\n" \
"s_nop 0\n" \
"v_mfma_f32_16x16x32_bf16 v[76:79], a[128:131], v[20:23], 0\n" \
"s_waitcnt lgkmcnt(4)\n" \
"v_mul_f32 v48, 0xbfb8aa3b, v48\n" \
"v_mul_f32 v49, 0xbfb8aa3b, v49\n" \
"v_mfma_f32_16x16x32_bf16 v[64:67], a[4:7], v[24:27], v[64:67]\n" \
"buffer_load_dwordx4 v4, s[36:39], s61 offen offset:1024 sc1 lds\n" \
"v_mfma_f32_16x16x32_bf16 v[68:71], a[4:7], v[28:31], v[68:71]\n" \
"ds_read_b128 v[16:19], v12 offset:4096\n" \
"v_mul_f32 v50, 0xbfb8aa3b, v50\n" \
"v_mfma_f32_16x16x32_bf16 v[72:75], a[132:135], v[24:27], v[72:75]\n" \
"ds_read_b128 v[20:23], v12 offset:20480\n" \
"v_mul_f32 v51, 0xbfb8aa3b, v51\n" \
"v_mfma_f32_16x16x32_bf16 v[76:79], a[132:135], v[28:31], v[76:79]\n" \
"s_waitcnt lgkmcnt(4)\n" \
"v_exp_f32 v48, v48\n" \
"v_mfma_f32_16x16x32_bf16 v[64:67], a[8:11], v[32:35], v[64:67]\n" \
"s_add_u32 s63, s59, 2\n" \
"s_min_u32 s63, s63, 63\n" \
"v_mfma_f32_16x16x32_bf16 v[68:71], a[8:11], v[36:39], v[68:71]\n" \
"ds_read_b128 v[24:27], v12 offset:5120\n" \
"s_lshr_b32 s63, s63, 3\n" \
"s_lshl_b32 s63, s63, 7\n" \
"v_mfma_f32_16x16x32_bf16 v[72:75], a[136:139], v[32:35], v[72:75]\n" \
"ds_read_b128 v[28:31], v12 offset:21504\n" \
"v_mfma_f32_16x16x32_bf16 v[76:79], a[136:139], v[36:39], v[76:79]\n" \
"s_waitcnt lgkmcnt(4)\n" \
"v_exp_f32 v49, v49\n" \
"v_mfma_f32_16x16x32_bf16 v[64:67], a[12:15], v[40:43], v[64:67]\n" \
"buffer_load_dwordx4 v4, s[36:39], s61 offen offset:2048 sc1 lds\n" \
"v_mfma_f32_16x16x32_bf16 v[68:71], a[12:15], v[44:47], v[68:71]\n" \
"ds_read_b128 v[32:35], v12 offset:6144\n" \
"v_mfma_f32_16x16x32_bf16 v[72:75], a[140:143], v[40:43], v[72:75]\n" \
"ds_read_b128 v[36:39], v12 offset:22528\n" \
"v_mfma_f32_16x16x32_bf16 v[76:79], a[140:143], v[44:47], v[76:79]\n" \
"s_waitcnt lgkmcnt(4)\n" \
"v_exp_f32 v50, v50\n" \
"v_mfma_f32_16x16x32_bf16 v[64:67], a[16:19], v[16:19], v[64:67]\n" \
"buffer_load_dwordx4 v4, s[36:39], s61 offen offset:3072 sc1 lds\n" \
"v_mfma_f32_16x16x32_bf16 v[68:71], a[16:19], v[20:23], v[68:71]\n" \
"ds_read_b128 v[40:43], v12 offset:7168\n" \
"v_mfma_f32_16x16x32_bf16 v[72:75], a[144:147], v[16:19], v[72:75]\n" \
"ds_read_b128 v[44:47], v12 offset:23552\n" \
"v_mfma_f32_16x16x32_bf16 v[76:79], a[144:147], v[20:23], v[76:79]\n" \
"s_waitcnt lgkmcnt(4)\n" \
"v_exp_f32 v51, v51\n" \
"v_mfma_f32_16x16x32_bf16 v[64:67], a[20:23], v[24:27], v[64:67]\n" \
"buffer_load_dword v15, v14, s[36:39], s63 offen sc1\n" \
"v_mfma_f32_16x16x32_bf16 v[68:71], a[20:23], v[28:31], v[68:71]\n" \
"ds_read_b128 v[16:19], v12 offset:8192\n" \
"v_add_f32_e64 v48, 1.0, v48\n" \
"v_mfma_f32_16x16x32_bf16 v[72:75], a[148:151], v[24:27], v[72:75]\n" \
"ds_read_b128 v[20:23], v12 offset:24576\n" \
"v_add_f32_e64 v49, 1.0, v49\n" \
"v_mfma_f32_16x16x32_bf16 v[76:79], a[148:151], v[28:31], v[76:79]\n" \
"s_waitcnt lgkmcnt(4)\n" \
"v_add_f32 v50, 1.0, v50\n" \
"v_add_f32_e64 v51, 1.0, v51\n" \
"v_mfma_f32_16x16x32_bf16 v[64:67], a[24:27], v[32:35], v[64:67]\n" \
"s_add_u32 m0, s55, 4096\n" \
"s_add_u32 s61, s61, 4096\n" \
"buffer_load_dwordx4 v4, s[36:39], s61 offen offset:0 sc1 lds\n" \
"v_mfma_f32_16x16x32_bf16 v[68:71], a[24:27], v[36:39], v[68:71]\n" \
"ds_read_b128 v[24:27], v12 offset:9216\n" \
"v_mfma_f32_16x16x32_bf16 v[72:75], a[152:155], v[32:35], v[72:75]\n" \
"ds_read_b128 v[28:31], v12 offset:25600\n" \
"v_mfma_f32_16x16x32_bf16 v[76:79], a[152:155], v[36:39], v[76:79]\n" \
"s_waitcnt lgkmcnt(4)\n" \
"v_rcp_f32 v48, v48\n" \
"v_mfma_f32_16x16x32_bf16 v[64:67], a[28:31], v[40:43], v[64:67]\n" \
"buffer_load_dwordx4 v4, s[36:39], s61 offen offset:1024 sc1 lds\n" \
"v_mfma_f32_16x16x32_bf16 v[68:71], a[28:31], v[44:47], v[68:71]\n" \
"ds_read_b128 v[32:35], v12 offset:10240\n" \
"v_mfma_f32_16x16x32_bf16 v[72:75], a[156:159], v[40:43], v[72:75]\n" \
"ds_read_b128 v[36:39], v12 offset:26624\n" \
"v_mfma_f32_16x16x32_bf16 v[76:79], a[156:159], v[44:47], v[76:79]\n" \
"s_waitcnt lgkmcnt(4)\n" \
"v_rcp_f32 v49, v49\n" \
"v_mfma_f32_16x16x32_bf16 v[64:67], a[32:35], v[16:19], v[64:67]\n" \
"v_rcp_f32_e64 v50, v50\n" \
"v_mfma_f32_16x16x32_bf16 v[68:71], a[32:35], v[20:23], v[68:71]\n" \
"ds_read_b128 v[40:43], v12 offset:11264\n" \
"v_mfma_f32_16x16x32_bf16 v[72:75], a[160:163], v[16:19], v[72:75]\n" \
"ds_read_b128 v[44:47], v12 offset:27648\n" \
"v_mfma_f32_16x16x32_bf16 v[76:79], a[160:163], v[20:23], v[76:79]\n" \
"s_waitcnt lgkmcnt(4)\n" \
"v_rcp_f32 v51, v51\n" \
"v_mfma_f32_16x16x32_bf16 v[64:67], a[36:39], v[24:27], v[64:67]\n" \
"buffer_load_dwordx4 v4, s[36:39], s61 offen offset:2048 sc1 lds\n" \
"v_mfma_f32_16x16x32_bf16 v[68:71], a[36:39], v[28:31], v[68:71]\n" \
"ds_read_b128 v[16:19], v12 offset:12288\n" \
"v_mul_f32_e64 v48, v48, v56\n" \
"v_mfma_f32_16x16x32_bf16 v[72:75], a[164:167], v[24:27], v[72:75]\n" \
"ds_read_b128 v[20:23], v12 offset:28672\n" \
"v_mul_f32_e64 v49, v49, v57\n" \
"v_mfma_f32_16x16x32_bf16 v[76:79], a[164:167], v[28:31], v[76:79]\n" \
"s_waitcnt lgkmcnt(4)\n" \
"v_mul_f32 v50, v50, v58\n" \
"v_mul_f32_e64 v51, v51, v59\n" \
"v_mfma_f32_16x16x32_bf16 v[64:67], a[40:43], v[32:35], v[64:67]\n" \
"buffer_load_dwordx4 v4, s[36:39], s61 offen offset:3072 sc1 lds\n" \
"v_mfma_f32_16x16x32_bf16 v[68:71], a[40:43], v[36:39], v[68:71]\n" \
"ds_read_b128 v[24:27], v12 offset:13312\n" \
"v_cvt_pk_bf16_f32 v80, v48, v49\n" \
"v_mfma_f32_16x16x32_bf16 v[72:75], a[168:171], v[32:35], v[72:75]\n" \
"ds_read_b128 v[28:31], v12 offset:29696\n" \
"v_cvt_pk_bf16_f32 v81, v50, v51\n" \
"v_mfma_f32_16x16x32_bf16 v[76:79], a[168:171], v[36:39], v[76:79]\n" \
"s_waitcnt lgkmcnt(4)\n" \
"v_mul_f32 v52, 0xbfb8aa3b, v52\n" \
"v_mul_f32 v53, 0xbfb8aa3b, v53\n" \
"v_mfma_f32_16x16x32_bf16 v[64:67], a[44:47], v[40:43], v[64:67]\n" \
"v_mul_f32 v54, 0xbfb8aa3b, v54\n" \
"v_mul_f32 v55, 0xbfb8aa3b, v55\n" \
"v_mfma_f32_16x16x32_bf16 v[68:71], a[44:47], v[44:47], v[68:71]\n" \
"ds_read_b128 v[32:35], v12 offset:14336\n" \
"v_mfma_f32_16x16x32_bf16 v[72:75], a[172:175], v[40:43], v[72:75]\n" \
"ds_read_b128 v[36:39], v12 offset:30720\n" \
"v_mfma_f32_16x16x32_bf16 v[76:79], a[172:175], v[44:47], v[76:79]\n" \
"s_waitcnt lgkmcnt(4)\n" \
"v_exp_f32_e64 v52, v52\n" \
"v_mfma_f32_16x16x32_bf16 v[64:67], a[48:51], v[16:19], v[64:67]\n" \
"v_exp_f32_e64 v53, v53\n" \
"v_mfma_f32_16x16x32_bf16 v[68:71], a[48:51], v[20:23], v[68:71]\n" \
"ds_read_b128 v[40:43], v12 offset:15360\n" \
"v_mfma_f32_16x16x32_bf16 v[72:75], a[176:179], v[16:19], v[72:75]\n" \
"ds_read_b128 v[44:47], v12 offset:31744\n" \
"v_mfma_f32_16x16x32_bf16 v[76:79], a[176:179], v[20:23], v[76:79]\n" \
"s_waitcnt lgkmcnt(4)\n" \
"v_exp_f32 v54, v54\n" \
"v_mfma_f32_16x16x32_bf16 v[64:67], a[52:55], v[24:27], v[64:67]\n" \
"s_waitcnt vmcnt(9)\n" \
"s_barrier\n" \
"v_mfma_f32_16x16x32_bf16 v[68:71], a[52:55], v[28:31], v[68:71]\n" \
"ds_read_b128 v[16:19], v12 offset:32768\n" \
"v_mfma_f32_16x16x32_bf16 v[72:75], a[180:183], v[24:27], v[72:75]\n" \
"ds_read_b128 v[20:23], v12 offset:49152\n" \
"v_mfma_f32_16x16x32_bf16 v[76:79], a[180:183], v[28:31], v[76:79]\n" \
"s_waitcnt lgkmcnt(4)\n" \
"buffer_store_dword v13, v6, s[36:39], 0 offen sc1\n" \
"v_exp_f32 v55, v55\n" \
"v_mfma_f32_16x16x32_bf16 v[64:67], a[56:59], v[32:35], v[64:67]\n" \
"v_add_f32 v52, 1.0, v52\n" \
"v_add_f32 v53, 1.0, v53\n" \
"v_mfma_f32_16x16x32_bf16 v[68:71], a[56:59], v[36:39], v[68:71]\n" \
"ds_read_b128 v[24:27], v12 offset:33792\n" \
"v_add_f32_e64 v54, 1.0, v54\n" \
"v_mfma_f32_16x16x32_bf16 v[72:75], a[184:187], v[32:35], v[72:75]\n" \
"ds_read_b128 v[28:31], v12 offset:50176\n" \
"v_add_f32_e64 v55, 1.0, v55\n" \
"v_mfma_f32_16x16x32_bf16 v[76:79], a[184:187], v[36:39], v[76:79]\n" \
"s_waitcnt lgkmcnt(4)\n" \
"v_rcp_f32 v52, v52\n" \
"v_mfma_f32_16x16x32_bf16 v[64:67], a[60:63], v[40:43], v[64:67]\n" \
"v_rcp_f32_e64 v53, v53\n" \
"v_mfma_f32_16x16x32_bf16 v[68:71], a[60:63], v[44:47], v[68:71]\n" \
"ds_read_b128 v[32:35], v12 offset:34816\n" \
"v_mfma_f32_16x16x32_bf16 v[72:75], a[188:191], v[40:43], v[72:75]\n" \
"ds_read_b128 v[36:39], v12 offset:51200\n" \
"v_mfma_f32_16x16x32_bf16 v[76:79], a[188:191], v[44:47], v[76:79]\n" \
"s_waitcnt lgkmcnt(4)\n" \
"v_rcp_f32 v54, v54\n" \
"v_mfma_f32_16x16x32_bf16 v[64:67], a[64:67], v[16:19], v[64:67]\n" \
"s_add_u32 s61, s61, 28672\n" \
"s_add_u32 s56, s56, 0x20000\n" \
"s_add_u32 m0, s55, 32768\n" \
"s_nop 0\n" \
"buffer_load_dwordx4 v4, s[36:39], s61 offen offset:0 sc1 lds\n" \
"v_mfma_f32_16x16x32_bf16 v[68:71], a[64:67], v[20:23], v[68:71]\n" \
"ds_read_b128 v[40:43], v12 offset:35840\n" \
"v_mfma_f32_16x16x32_bf16 v[72:75], a[192:195], v[16:19], v[72:75]\n" \
"ds_read_b128 v[44:47], v12 offset:52224\n" \
"v_mfma_f32_16x16x32_bf16 v[76:79], a[192:195], v[20:23], v[76:79]\n" \
"s_waitcnt lgkmcnt(4)\n" \
"v_rcp_f32_e64 v55, v55\n" \
"v_mfma_f32_16x16x32_bf16 v[64:67], a[68:71], v[24:27], v[64:67]\n" \
"buffer_load_dwordx4 v4, s[36:39], s61 offen offset:1024 sc1 lds\n" \
"v_mfma_f32_16x16x32_bf16 v[68:71], a[68:71], v[28:31], v[68:71]\n" \
"ds_read_b128 v[16:19], v12 offset:36864\n" \
"v_mul_f32_e64 v52, v52, v60\n" \
"v_mfma_f32_16x16x32_bf16 v[72:75], a[196:199], v[24:27], v[72:75]\n" \
"ds_read_b128 v[20:23], v12 offset:53248\n" \
"v_mul_f32_e64 v53, v53, v61\n" \
"v_mfma_f32_16x16x32_bf16 v[76:79], a[196:199], v[28:31], v[76:79]\n" \
"s_waitcnt lgkmcnt(4)\n" \
"v_mul_f32 v54, v54, v62\n" \
"v_mul_f32_e64 v55, v55, v63\n" \
"v_mfma_f32_16x16x32_bf16 v[64:67], a[72:75], v[32:35], v[64:67]\n" \
"v_cvt_pk_bf16_f32 v82, v52, v53\n" \
"v_cvt_pk_bf16_f32 v83, v54, v55\n" \
"v_mfma_f32_16x16x32_bf16 v[68:71], a[72:75], v[36:39], v[68:71]\n" \
"ds_read_b128 v[24:27], v12 offset:37888\n" \
"s_nop 0\n" \
"v_mfma_f32_16x16x32_bf16 v[72:75], a[200:203], v[32:35], v[72:75]\n" \
"ds_read_b128 v[28:31], v12 offset:54272\n" \
"s_nop 0\n" \
"v_mfma_f32_16x16x32_bf16 v[76:79], a[200:203], v[36:39], v[76:79]\n" \
"s_waitcnt lgkmcnt(4)\n" \
"v_permlane16_swap_b32 v80, v82\n" \
"v_permlane16_swap_b32 v81, v83\n" \
"v_mfma_f32_16x16x32_bf16 v[64:67], a[76:79], v[40:43], v[64:67]\n" \
"buffer_load_dwordx4 v4, s[36:39], s61 offen offset:2048 sc1 lds\n" \
"v_mfma_f32_16x16x32_bf16 v[68:71], a[76:79], v[44:47], v[68:71]\n" \
"ds_read_b128 v[32:35], v12 offset:38912\n" \
"buffer_store_dwordx4 v[80:83], v5, s[36:39], s57 offen sc1\n" \
"v_mfma_f32_16x16x32_bf16 v[72:75], a[204:207], v[40:43], v[72:75]\n" \
"ds_read_b128 v[36:39], v12 offset:55296\n" \
"v_mfma_f32_16x16x32_bf16 v[76:79], a[204:207], v[44:47], v[76:79]\n" \
"s_waitcnt lgkmcnt(4)\n" \
"v_mfma_f32_16x16x32_bf16 v[64:67], a[80:83], v[16:19], v[64:67]\n" \
"buffer_load_dwordx4 v4, s[36:39], s61 offen offset:3072 sc1 lds\n" \
"v_mfma_f32_16x16x32_bf16 v[68:71], a[80:83], v[20:23], v[68:71]\n" \
"ds_read_b128 v[40:43], v12 offset:39936\n" \
"v_mfma_f32_16x16x32_bf16 v[72:75], a[208:211], v[16:19], v[72:75]\n" \
"ds_read_b128 v[44:47], v12 offset:56320\n" \
"v_mfma_f32_16x16x32_bf16 v[76:79], a[208:211], v[20:23], v[76:79]\n" \
"s_waitcnt lgkmcnt(4)\n" \
"v_mfma_f32_16x16x32_bf16 v[64:67], a[84:87], v[24:27], v[64:67]\n" \
"v_mfma_f32_16x16x32_bf16 v[68:71], a[84:87], v[28:31], v[68:71]\n" \
"ds_read_b128 v[16:19], v12 offset:40960\n" \
"v_mfma_f32_16x16x32_bf16 v[72:75], a[212:215], v[24:27], v[72:75]\n" \
"ds_read_b128 v[20:23], v12 offset:57344\n" \
"v_mfma_f32_16x16x32_bf16 v[76:79], a[212:215], v[28:31], v[76:79]\n" \
"s_waitcnt lgkmcnt(4)\n" \
"v_mfma_f32_16x16x32_bf16 v[64:67], a[88:91], v[32:35], v[64:67]\n" \
"s_add_u32 m0, s55, 36864\n" \
"s_add_u32 s61, s61, 4096\n" \
"buffer_load_dwordx4 v4, s[36:39], s61 offen offset:0 sc1 lds\n" \
"v_mfma_f32_16x16x32_bf16 v[68:71], a[88:91], v[36:39], v[68:71]\n" \
"ds_read_b128 v[24:27], v12 offset:41984\n" \
"v_mfma_f32_16x16x32_bf16 v[72:75], a[216:219], v[32:35], v[72:75]\n" \
"ds_read_b128 v[28:31], v12 offset:58368\n" \
"v_mfma_f32_16x16x32_bf16 v[76:79], a[216:219], v[36:39], v[76:79]\n" \
"s_waitcnt lgkmcnt(4)\n" \
"v_mfma_f32_16x16x32_bf16 v[64:67], a[92:95], v[40:43], v[64:67]\n" \
"buffer_load_dwordx4 v4, s[36:39], s61 offen offset:1024 sc1 lds\n" \
"v_mfma_f32_16x16x32_bf16 v[68:71], a[92:95], v[44:47], v[68:71]\n" \
"ds_read_b128 v[32:35], v12 offset:43008\n" \
"v_mfma_f32_16x16x32_bf16 v[72:75], a[220:223], v[40:43], v[72:75]\n" \
"ds_read_b128 v[36:39], v12 offset:59392\n" \
"v_mfma_f32_16x16x32_bf16 v[76:79], a[220:223], v[44:47], v[76:79]\n" \
"s_waitcnt lgkmcnt(4)\n" \
"v_mfma_f32_16x16x32_bf16 v[64:67], a[96:99], v[16:19], v[64:67]\n" \
"v_mfma_f32_16x16x32_bf16 v[68:71], a[96:99], v[20:23], v[68:71]\n" \
"ds_read_b128 v[40:43], v12 offset:44032\n" \
"v_mfma_f32_16x16x32_bf16 v[72:75], a[224:227], v[16:19], v[72:75]\n" \
"ds_read_b128 v[44:47], v12 offset:60416\n" \
"v_mfma_f32_16x16x32_bf16 v[76:79], a[224:227], v[20:23], v[76:79]\n" \
"s_waitcnt lgkmcnt(4)\n" \
"v_mfma_f32_16x16x32_bf16 v[64:67], a[100:103], v[24:27], v[64:67]\n" \
"buffer_load_dwordx4 v4, s[36:39], s61 offen offset:2048 sc1 lds\n" \
"v_mfma_f32_16x16x32_bf16 v[68:71], a[100:103], v[28:31], v[68:71]\n" \
"ds_read_b128 v[16:19], v12 offset:45056\n" \
"v_mfma_f32_16x16x32_bf16 v[72:75], a[228:231], v[24:27], v[72:75]\n" \
"ds_read_b128 v[20:23], v12 offset:61440\n" \
"v_mfma_f32_16x16x32_bf16 v[76:79], a[228:231], v[28:31], v[76:79]\n" \
"s_waitcnt lgkmcnt(4)\n" \
"v_mfma_f32_16x16x32_bf16 v[64:67], a[104:107], v[32:35], v[64:67]\n" \
"buffer_load_dwordx4 v4, s[36:39], s61 offen offset:3072 sc1 lds\n" \
"v_mfma_f32_16x16x32_bf16 v[68:71], a[104:107], v[36:39], v[68:71]\n" \
"ds_read_b128 v[24:27], v12 offset:46080\n" \
"v_mfma_f32_16x16x32_bf16 v[72:75], a[232:235], v[32:35], v[72:75]\n" \
"ds_read_b128 v[28:31], v12 offset:62464\n" \
"v_mfma_f32_16x16x32_bf16 v[76:79], a[232:235], v[36:39], v[76:79]\n" \
"s_waitcnt lgkmcnt(4)\n" \
"v_mfma_f32_16x16x32_bf16 v[64:67], a[108:111], v[40:43], v[64:67]\n" \
"s_lshl_b32 s66, s59, s49\n" \
"s_lshr_b32 s65, s66, 8\n" \
"v_mfma_f32_16x16x32_bf16 v[68:71], a[108:111], v[44:47], v[68:71]\n" \
"ds_read_b128 v[32:35], v12 offset:47104\n" \
"v_mfma_f32_16x16x32_bf16 v[72:75], a[236:239], v[40:43], v[72:75]\n" \
"ds_read_b128 v[36:39], v12 offset:63488\n" \
"v_mfma_f32_16x16x32_bf16 v[76:79], a[236:239], v[44:47], v[76:79]\n" \
"s_waitcnt lgkmcnt(4)\n" \
"s_and_b32 s65, s65, 0x2000\n" \
"s_xor_b32 s66, s66, s65\n" \
"v_mfma_f32_16x16x32_bf16 v[64:67], a[112:115], v[16:19], v[64:67]\n" \
"v_mfma_f32_16x16x32_bf16 v[68:71], a[112:115], v[20:23], v[68:71]\n" \
"ds_read_b128 v[40:43], v12 offset:48128\n" \
"v_mfma_f32_16x16x32_bf16 v[72:75], a[240:243], v[16:19], v[72:75]\n" \
"ds_read_b128 v[44:47], v12 offset:64512\n" \
"v_mfma_f32_16x16x32_bf16 v[76:79], a[240:243], v[20:23], v[76:79]\n" \
"s_waitcnt lgkmcnt(4)\n" \
"v_mfma_f32_16x16x32_bf16 v[64:67], a[116:119], v[24:27], v[64:67]\n" \
"s_waitcnt vmcnt(10)\n" \
"s_barrier\n" \
"v_mfma_f32_16x16x32_bf16 v[68:71], a[116:119], v[28:31], v[68:71]\n" \
"ds_read_b128 v[16:19], v10 offset:0\n" \
"v_mfma_f32_16x16x32_bf16 v[72:75], a[244:247], v[24:27], v[72:75]\n" \
"ds_read_b128 v[20:23], v10 offset:16384\n" \
"v_mfma_f32_16x16x32_bf16 v[76:79], a[244:247], v[28:31], v[76:79]\n" \
"s_waitcnt lgkmcnt(4)\n" \
"v_mfma_f32_16x16x32_bf16 v[64:67], a[120:123], v[32:35], v[64:67]\n" \
"v_cmp_gt_u32 vcc, 16, v15\n" \
"v_mfma_f32_16x16x32_bf16 v[68:71], a[120:123], v[36:39], v[68:71]\n" \
"ds_read_b128 v[24:27], v10 offset:1024\n" \
"v_mfma_f32_16x16x32_bf16 v[72:75], a[248:251], v[32:35], v[72:75]\n" \
"ds_read_b128 v[28:31], v10 offset:17408\n" \
"v_mfma_f32_16x16x32_bf16 v[76:79], a[248:251], v[36:39], v[76:79]\n" \
"s_waitcnt lgkmcnt(4)\n" \
"v_mfma_f32_16x16x32_bf16 v[64:67], a[124:127], v[40:43], v[64:67]\n" \
"s_cbranch_vccnz .Lp1g1_m1_%=\n" \
".Lp1g1_m1k_%=:\n" \
"v_mfma_f32_16x16x32_bf16 v[68:71], a[124:127], v[44:47], v[68:71]\n" \
"ds_read_b128 v[32:35], v10 offset:2048\n" \
"v_mfma_f32_16x16x32_bf16 v[72:75], a[252:255], v[40:43], v[72:75]\n" \
"ds_read_b128 v[36:39], v10 offset:18432\n" \
"v_mfma_f32_16x16x32_bf16 v[76:79], a[252:255], v[44:47], v[76:79]\n" \
"s_waitcnt lgkmcnt(4)\n" \
"s_xor_b32 s57, s66, s64\n" \
"s_add_u32 s58, s57, s50\n" \
"s_add_u32 s59, s59, 1\n" \
"v_mov_b32_e64 v5, v7\n" \
"v_mov_b32_e64 v6, v11\n" \
"s_cmp_ge_u32 s59, s47\n" \
"s_nop 0\n" \
"s_cbranch_scc0 .Lp1g1_loop_%=\n" \
".Lp1g1_out1_%=:\n" \
"s_nop 7\n" \
"s_nop 7\n" \
"v_mul_f32 v64, 0xbfb8aa3b, v64\n" \
"v_mul_f32 v65, 0xbfb8aa3b, v65\n" \
"v_mul_f32 v66, 0xbfb8aa3b, v66\n" \
"v_mul_f32 v67, 0xbfb8aa3b, v67\n" \
"v_exp_f32 v64, v64\n" \
"v_exp_f32 v65, v65\n" \
"v_exp_f32 v66, v66\n" \
"v_exp_f32 v67, v67\n" \
"v_add_f32 v64, 1.0, v64\n" \
"v_add_f32 v65, 1.0, v65\n" \
"v_add_f32 v66, 1.0, v66\n" \
"v_add_f32 v67, 1.0, v67\n" \
"v_rcp_f32 v64, v64\n" \
"v_rcp_f32 v65, v65\n" \
"v_rcp_f32 v66, v66\n" \
"v_rcp_f32 v67, v67\n" \
"v_mul_f32 v64, v64, v72\n" \
"v_mul_f32 v65, v65, v73\n" \
"v_mul_f32 v66, v66, v74\n" \
"v_mul_f32 v67, v67, v75\n" \
"v_cvt_pk_bf16_f32 v80, v64, v65\n" \
"v_cvt_pk_bf16_f32 v81, v66, v67\n" \
"v_mul_f32 v68, 0xbfb8aa3b, v68\n" \
"v_mul_f32 v69, 0xbfb8aa3b, v69\n" \
"v_mul_f32 v70, 0xbfb8aa3b, v70\n" \
"v_mul_f32 v71, 0xbfb8aa3b, v71\n" \
"v_exp_f32 v68, v68\n" \
"v_exp_f32 v69, v69\n" \
"v_exp_f32 v70, v70\n" \
"v_exp_f32 v71, v71\n" \
"v_add_f32 v68, 1.0, v68\n" \
"v_add_f32 v69, 1.0, v69\n" \
"v_add_f32 v70, 1.0, v70\n" \
"v_add_f32 v71, 1.0, v71\n" \
"v_rcp_f32 v68, v68\n" \
"v_rcp_f32 v69, v69\n" \
"v_rcp_f32 v70, v70\n" \
"v_rcp_f32 v71, v71\n" \
"v_mul_f32 v68, v68, v76\n" \
"v_mul_f32 v69, v69, v77\n" \
"v_mul_f32 v70, v70, v78\n" \
"v_mul_f32 v71, v71, v79\n" \
"v_cvt_pk_bf16_f32 v82, v68, v69\n" \
"v_cvt_pk_bf16_f32 v83, v70, v71\n" \
"s_nop 0\n" \
"s_nop 0\n" \
"v_permlane16_swap_b32 v80, v82\n" \
"v_permlane16_swap_b32 v81, v83\n" \
"buffer_store_dwordx4 v[80:83], v5, s[36:39], s57 offen sc1\n" \
"s_branch .Lp1g1_end_%=\n" \
".Lp1g1_out0_%=:\n" \
"s_nop 7\n" \
"s_nop 7\n" \
"v_mul_f32 v48, 0xbfb8aa3b, v48\n" \
"v_mul_f32 v49, 0xbfb8aa3b, v49\n" \
"v_mul_f32 v50, 0xbfb8aa3b, v50\n" \
"v_mul_f32 v51, 0xbfb8aa3b, v51\n" \
"v_exp_f32 v48, v48\n" \
"v_exp_f32 v49, v49\n" \
"v_exp_f32 v50, v50\n" \
"v_exp_f32 v51, v51\n" \
"v_add_f32 v48, 1.0, v48\n" \
"v_add_f32 v49, 1.0, v49\n" \
"v_add_f32 v50, 1.0, v50\n" \
"v_add_f32 v51, 1.0, v51\n" \
"v_rcp_f32 v48, v48\n" \
"v_rcp_f32 v49, v49\n" \
"v_rcp_f32 v50, v50\n" \
"v_rcp_f32 v51, v51\n" \
"v_mul_f32 v48, v48, v56\n" \
"v_mul_f32 v49, v49, v57\n" \
"v_mul_f32 v50, v50, v58\n" \
"v_mul_f32 v51, v51, v59\n" \
"v_cvt_pk_bf16_f32 v80, v48, v49\n" \
"v_cvt_pk_bf16_f32 v81, v50, v51\n" \
"v_mul_f32 v52, 0xbfb8aa3b, v52\n" \
"v_mul_f32 v53, 0xbfb8aa3b, v53\n" \
"v_mul_f32 v54, 0xbfb8aa3b, v54\n" \
"v_mul_f32 v55, 0xbfb8aa3b, v55\n" \
"v_exp_f32 v52, v52\n" \
"v_exp_f32 v53, v53\n" \
"v_exp_f32 v54, v54\n" \
"v_exp_f32 v55, v55\n" \
"v_add_f32 v52, 1.0, v52\n" \
"v_add_f32 v53, 1.0, v53\n" \
"v_add_f32 v54, 1.0, v54\n" \
"v_add_f32 v55, 1.0, v55\n" \
"v_rcp_f32 v52, v52\n" \
"v_rcp_f32 v53, v53\n" \
"v_rcp_f32 v54, v54\n" \
"v_rcp_f32 v55, v55\n" \
"v_mul_f32 v52, v52, v60\n" \
"v_mul_f32 v53, v53, v61\n" \
"v_mul_f32 v54, v54, v62\n" \
"v_mul_f32 v55, v55, v63\n" \
"v_cvt_pk_bf16_f32 v82, v52, v53\n" \
"v_cvt_pk_bf16_f32 v83, v54, v55\n" \
"s_nop 0\n" \
"s_nop 0\n" \
"v_permlane16_swap_b32 v80, v82\n" \
"v_permlane16_swap_b32 v81, v83\n" \
"buffer_store_dwordx4 v[80:83], v5, s[36:39], s57 offen sc1\n" \
".Lp1g1_end_%=:\n" \
"s_waitcnt vmcnt(0) lgkmcnt(0)\n" \
"s_barrier\n" \
"v_mov_b32 v13, s59\n" \
"buffer_store_dword v13, v6, s[36:39], 0 offen sc1\n" \
"s_waitcnt vmcnt(0) lgkmcnt(0)\n" \
"s_branch .Lp1g1_x_%=\n" \
".Lp1g1_l0_%=:\n" \
"s_nop 7\n" \
"s_nop 7\n" \
"s_waitcnt vmcnt(0) lgkmcnt(0)\n" \
"s_mov_b32 s62, 0\n" \
".Lp1g1_l0a_%=:\n" \
"buffer_load_dword v15, v14, s[36:39], s63 offen sc1\n" \
"s_waitcnt vmcnt(0)\n" \
"v_cmp_gt_u32 vcc, 16, v15\n" \
"s_nop 4\n" \
"s_cbranch_vccz .Lp1g1_l0k_%=\n" \
"s_sleep 4\n" \
"s_add_u32 s62, s62, 1\n" \
"s_cmp_lt_u32 s62, 0x10000\n" \
"s_cbranch_scc1 .Lp1g1_l0a_%=\n" \
"v_mov_b32 v15, 256\n" \
"buffer_store_dword v15, v15, s[36:39], 0 offen sc1\n" \
"s_waitcnt vmcnt(0) lgkmcnt(0)\n" \
"s_branch .Lp1g1_l0k_%=\n" \
".Lp1g1_l1_%=:\n" \
"s_nop 7\n" \
"s_nop 7\n" \
"s_waitcnt vmcnt(0) lgkmcnt(0)\n" \
"s_mov_b32 s62, 0\n" \
".Lp1g1_l1a_%=:\n" \
"buffer_load_dword v15, v14, s[36:39], s63 offen sc1\n" \
"s_waitcnt vmcnt(0)\n" \
"v_cmp_gt_u32 vcc, 16, v15\n" \
"s_nop 4\n" \
"s_cbranch_vccz .Lp1g1_l1k_%=\n" \
"s_sleep 4\n" \
"s_add_u32 s62, s62, 1\n" \
"s_cmp_lt_u32 s62, 0x10000\n" \
"s_cbranch_scc1 .Lp1g1_l1a_%=\n" \
"v_mov_b32 v15, 256\n" \
"buffer_store_dword v15, v15, s[36:39], 0 offen sc1\n" \
"s_waitcnt vmcnt(0) lgkmcnt(0)\n" \
"s_branch .Lp1g1_l1k_%=\n" \
".Lp1g1_m0_%=:\n" \
"s_nop 7\n" \
"s_nop 7\n" \
"s_waitcnt vmcnt(0) lgkmcnt(0)\n" \
"s_mov_b32 s62, 0\n" \
".Lp1g1_m0a_%=:\n" \
"buffer_load_dword v15, v14, s[36:39], s63 offen sc1\n" \
"s_waitcnt vmcnt(0)\n" \
"v_cmp_gt_u32 vcc, 16, v15\n" \
"s_nop 4\n" \
"s_cbranch_vccz .Lp1g1_m0k_%=\n" \
"s_sleep 4\n" \
"s_add_u32 s62, s62, 1\n" \
"s_cmp_lt_u32 s62, 0x10000\n" \
"s_cbranch_scc1 .Lp1g1_m0a_%=\n" \
"v_mov_b32 v15, 256\n" \
"buffer_store_dword v15, v15, s[36:39], 0 offen sc1\n" \
"s_waitcnt vmcnt(0) lgkmcnt(0)\n" \
"s_branch .Lp1g1_m0k_%=\n" \
".Lp1g1_m1_%=:\n" \
"s_nop 7\n" \
"s_nop 7\n" \
"s_waitcnt vmcnt(0) lgkmcnt(0)\n" \
"s_mov_b32 s62, 0\n" \
".Lp1g1_m1a_%=:\n" \
"buffer_load_dword v15, v14, s[36:39], s63 offen sc1\n" \
"s_waitcnt vmcnt(0)\n" \
"v_cmp_gt_u32 vcc, 16, v15\n" \
"s_nop 4\n" \
"s_cbranch_vccz .Lp1g1_m1k_%=\n" \
"s_sleep 4\n" \
"s_add_u32 s62, s62, 1\n" \
"s_cmp_lt_u32 s62, 0x10000\n" \
"s_cbranch_scc1 .Lp1g1_m1a_%=\n" \
"v_mov_b32 v15, 256\n" \
"buffer_store_dword v15, v15, s[36:39], 0 offen sc1\n" \
"s_waitcnt vmcnt(0) lgkmcnt(0)\n" \
"s_branch .Lp1g1_m1k_%=\n" \
".Lp1g1_x_%=:\n"
#define CLOB_P1 \
"a0", "a1", "a2", "a3", "a4", "a5", "a6", "a7", "a8", "a9", "a10", "a11", "a12", "a13", "a14", "a15", "a16", "a17", "a18", "a19", "a20", "a21", "a22", "a23", "a24", "a25", "a26", "a27", "a28", "a29", "a30", "a31", "a32", "a33", "a34", "a35", "a36", "a37", "a38", "a39", "a40", "a41", "a42", "a43", "a44", "a45", "a46", "a47", "a48", "a49", "a50", "a51", "a52", "a53", "a54", "a55", "a56", "a57", "a58", "a59", "a60", "a61", "a62", "a63", "a64", "a65", "a66", "a67", "a68", "a69", "a70", "a71", "a72", "a73", "a74", "a75", "a76", "a77", "a78", "a79", "a80", "a81", "a82", "a83", "a84", "a85", "a86", "a87", "a88", "a89", "a90", "a91", "a92", "a93", "a94", "a95", "a96", "a97", "a98", "a99", "a100", "a101", "a102", "a103", "a104", "a105", "a106", "a107", "a108", "a109", "a110", "a111", "a112", "a113", "a114", "a115", "a116", "a117", "a118", "a119", "a120", "a121", "a122", "a123", "a124", "a125", "a126", "a127", "a128", "a129", "a130", "a131", "a132", "a133", "a134", "a135", "a136", "a137", "a138", "a139", "a140", "a141", "a142", "a143", "a144", "a145", "a146", "a147", "a148", "a149", "a150", "a151", "a152", "a153", "a154", "a155", "a156", "a157", "a158", "a159", "a160", "a161", "a162", "a163", "a164", "a165", "a166", "a167", "a168", "a169", "a170", "a171", "a172", "a173", "a174", "a175", "a176", "a177", "a178", "a179", "a180", "a181", "a182", "a183", "a184", "a185", "a186", "a187", "a188", "a189", "a190", "a191", "a192", "a193", "a194", "a195", "a196", "a197", "a198", "a199", "a200", "a201", "a202", "a203", "a204", "a205", "a206", "a207", "a208", "a209", "a210", "a211", "a212", "a213", "a214", "a215", "a216", "a217", "a218", "a219", "a220", "a221", "a222", "a223", "a224", "a225", "a226", "a227", "a228", "a229", "a230", "a231", "a232", "a233", "a234", "a235", "a236", "a237", "a238", "a239", "a240", "a241", "a242", "a243", "a244", "a245", "a246", "a247", "a248", "a249", "a250", "a251", "a252", "a253", "a254", "a255", "s52", "s53", "s54", "s55", "s56", "s57", "s58", "s59", "s60", "s61", "s62", "s63", "s65", "s66", "v7", "v8", "v9", "v10", "v11", "v12", "v13", "v14", "v15", "v16", "v17", "v18", "v19", "v20", "v21", "v22", "v23", "v24", "v25", "v26", "v27", "v28", "v29", "v30", "v31", "v32", "v33", "v34", "v35", "v36", "v37", "v38", "v39", "v40", "v41", "v42", "v43", "v44", "v45", "v46", "v47", "v48", "v49", "v50", "v51", "v52", "v53", "v54", "v55", "v56", "v57", "v58", "v59", "v60", "v61", "v62", "v63", "v64", "v65", "v66", "v67", "v68", "v69", "v70", "v71", "v72", "v73", "v74", "v75", "v76", "v77", "v78", "v79", "v80", "v81", "v82", "v83", "v84", "v85", "v86", "v87", "vcc", "scc", "memory"
#define ASM_QK \
"s_nop 7\n" \
"s_nop 7\n" \
"v_add_u32 v36, 0x10000, v34\n" \
"v_add_u32 v38, 0x20000, v34\n" \
"v_add_u32 v37, 0x10000, v35\n" \
"v_add_u32 v39, 0x20000, v35\n" \
"s_lshl_b32 s62, s48, 12\n" \
"s_add_u32 m0, s62, 147456\n" \
"s_xor_b32 s54, s15, 524288\n" \
"buffer_load_dwordx4 v32, s[40:43], s54 offen nt lds\n" \
"s_add_u32 m0, s62, 148480\n" \
"s_xor_b32 s54, s15, 524288\n" \
"buffer_load_dwordx4 v33, s[40:43], s54 offen nt lds\n" \
"s_add_u32 m0, s62, 149504\n" \
"s_xor_b32 s54, s15, 655360\n" \
"buffer_load_dwordx4 v32, s[40:43], s54 offen nt lds\n" \
"s_add_u32 m0, s62, 150528\n" \
"s_xor_b32 s54, s15, 655360\n" \
"buffer_load_dwordx4 v33, s[40:43], s54 offen nt lds\n" \
"s_add_u32 m0, s62, 131072\n" \
"s_xor_b32 s54, s15, 786432\n" \
"buffer_load_dwordx4 v32, s[40:43], s54 offen nt lds\n" \
"s_add_u32 m0, s62, 132096\n" \
"s_xor_b32 s54, s15, 786432\n" \
"buffer_load_dwordx4 v33, s[40:43], s54 offen nt lds\n" \
"s_add_u32 m0, s62, 133120\n" \
"s_xor_b32 s54, s15, 917504\n" \
"buffer_load_dwordx4 v32, s[40:43], s54 offen nt lds\n" \
"s_add_u32 m0, s62, 134144\n" \
"s_xor_b32 s54, s15, 917504\n" \
"buffer_load_dwordx4 v33, s[40:43], s54 offen nt lds\n" \
"s_add_u32 m0, s62, 114688\n" \
"s_xor_b32 s54, s15, 1572864\n" \
"buffer_load_dwordx4 v32, s[40:43], s54 offen nt lds\n" \
"s_add_u32 m0, s62, 115712\n" \
"s_xor_b32 s54, s15, 1572864\n" \
"buffer_load_dwordx4 v33, s[40:43], s54 offen nt lds\n" \
"s_add_u32 m0, s62, 116736\n" \
"s_xor_b32 s54, s15, 1703936\n" \
"buffer_load_dwordx4 v32, s[40:43], s54 offen nt lds\n" \
"s_add_u32 m0, s62, 117760\n" \
"s_xor_b32 s54, s15, 1703936\n" \
"buffer_load_dwordx4 v33, s[40:43], s54 offen nt lds\n" \
"s_add_u32 m0, s62, 98304\n" \
"s_xor_b32 s54, s15, 1835008\n" \
"buffer_load_dwordx4 v32, s[40:43], s54 offen nt lds\n" \
"s_add_u32 m0, s62, 99328\n" \
"s_xor_b32 s54, s15, 1835008\n" \
"buffer_load_dwordx4 v33, s[40:43], s54 offen nt lds\n" \
"s_add_u32 m0, s62, 100352\n" \
"s_xor_b32 s54, s15, 1966080\n" \
"buffer_load_dwordx4 v32, s[40:43], s54 offen nt lds\n" \
"s_add_u32 m0, s62, 101376\n" \
"s_xor_b32 s54, s15, 1966080\n" \
"buffer_load_dwordx4 v33, s[40:43], s54 offen nt lds\n" \
"s_add_u32 m0, s62, 81920\n" \
"s_xor_b32 s54, s15, 2621440\n" \
"buffer_load_dwordx4 v32, s[40:43], s54 offen nt lds\n" \
"s_add_u32 m0, s62, 82944\n" \
"s_xor_b32 s54, s15, 2621440\n" \
"buffer_load_dwordx4 v33, s[40:43], s54 offen nt lds\n" \
"s_add_u32 m0, s62, 83968\n" \
"s_xor_b32 s54, s15, 2752512\n" \
"buffer_load_dwordx4 v32, s[40:43], s54 offen nt lds\n" \
"s_add_u32 m0, s62, 84992\n" \
"s_xor_b32 s54, s15, 2752512\n" \
"buffer_load_dwordx4 v33, s[40:43], s54 offen nt lds\n" \
"s_add_u32 m0, s62, 65536\n" \
"s_xor_b32 s54, s15, 2883584\n" \
"buffer_load_dwordx4 v32, s[40:43], s54 offen nt lds\n" \
"s_add_u32 m0, s62, 66560\n" \
"s_xor_b32 s54, s15, 2883584\n" \
"buffer_load_dwordx4 v33, s[40:43], s54 offen nt lds\n" \
"s_add_u32 m0, s62, 67584\n" \
"s_xor_b32 s54, s15, 3014656\n" \
"buffer_load_dwordx4 v32, s[40:43], s54 offen nt lds\n" \
"s_add_u32 m0, s62, 68608\n" \
"s_xor_b32 s54, s15, 3014656\n" \
"buffer_load_dwordx4 v33, s[40:43], s54 offen nt lds\n" \
"s_add_u32 m0, s62, 49152\n" \
"s_xor_b32 s54, s15, 3670016\n" \
"buffer_load_dwordx4 v32, s[40:43], s54 offen nt lds\n" \
"s_add_u32 m0, s62, 50176\n" \
"s_xor_b32 s54, s15, 3670016\n" \
"buffer_load_dwordx4 v33, s[40:43], s54 offen nt lds\n" \
"s_add_u32 m0, s62, 51200\n" \
"s_xor_b32 s54, s15, 3801088\n" \
"buffer_load_dwordx4 v32, s[40:43], s54 offen nt lds\n" \
"s_add_u32 m0, s62, 52224\n" \
"s_xor_b32 s54, s15, 3801088\n" \
"buffer_load_dwordx4 v33, s[40:43], s54 offen nt lds\n" \
"s_add_u32 m0, s62, 32768\n" \
"s_xor_b32 s54, s15, 3932160\n" \
"buffer_load_dwordx4 v32, s[40:43], s54 offen nt lds\n" \
"s_add_u32 m0, s62, 33792\n" \
"s_xor_b32 s54, s15, 3932160\n" \
"buffer_load_dwordx4 v33, s[40:43], s54 offen nt lds\n" \
"s_add_u32 m0, s62, 34816\n" \
"s_xor_b32 s54, s15, 4063232\n" \
"buffer_load_dwordx4 v32, s[40:43], s54 offen nt lds\n" \
"s_add_u32 m0, s62, 35840\n" \
"s_xor_b32 s54, s15, 4063232\n" \
"buffer_load_dwordx4 v33, s[40:43], s54 offen nt lds\n" \
"s_add_u32 m0, s62, 16384\n" \
"s_xor_b32 s54, s15, 4718592\n" \
"buffer_load_dwordx4 v32, s[40:43], s54 offen nt lds\n" \
"s_add_u32 m0, s62, 17408\n" \
"s_xor_b32 s54, s15, 4718592\n" \
"buffer_load_dwordx4 v33, s[40:43], s54 offen nt lds\n" \
"s_add_u32 m0, s62, 18432\n" \
"s_xor_b32 s54, s15, 4849664\n" \
"buffer_load_dwordx4 v32, s[40:43], s54 offen nt lds\n" \
"s_add_u32 m0, s62, 19456\n" \
"s_xor_b32 s54, s15, 4849664\n" \
"buffer_load_dwordx4 v33, s[40:43], s54 offen nt lds\n" \
"s_add_u32 m0, s62, 0\n" \
"s_xor_b32 s54, s15, 4980736\n" \
"buffer_load_dwordx4 v32, s[40:43], s54 offen nt lds\n" \
"s_add_u32 m0, s62, 1024\n" \
"s_xor_b32 s54, s15, 4980736\n" \
"buffer_load_dwordx4 v33, s[40:43], s54 offen nt lds\n" \
"s_add_u32 m0, s62, 2048\n" \
"s_xor_b32 s54, s15, 5111808\n" \
"buffer_load_dwordx4 v32, s[40:43], s54 offen nt lds\n" \
"s_add_u32 m0, s62, 3072\n" \
"s_xor_b32 s54, s15, 5111808\n" \
"buffer_load_dwordx4 v33, s[40:43], s54 offen nt lds\n" \
"s_waitcnt vmcnt(32)\n" \
"s_barrier\n" \
"ds_read_b64_tr_b16 a[16:17], v38 offset:16384\n" \
"ds_read_b64_tr_b16 a[18:19], v38 offset:16896\n" \
"ds_read_b64_tr_b16 a[24:25], v39 offset:16384\n" \
"ds_read_b64_tr_b16 a[26:27], v39 offset:16896\n" \
"ds_read_b64_tr_b16 v[112:113], v38 offset:24576\n" \
"ds_read_b64_tr_b16 v[114:115], v38 offset:25088\n" \
"ds_read_b64_tr_b16 v[120:121], v39 offset:24576\n" \
"ds_read_b64_tr_b16 v[122:123], v39 offset:25088\n" \
"ds_read_b64_tr_b16 a[20:21], v38 offset:0\n" \
"ds_read_b64_tr_b16 a[22:23], v38 offset:512\n" \
"ds_read_b64_tr_b16 a[28:29], v39 offset:0\n" \
"ds_read_b64_tr_b16 a[30:31], v39 offset:512\n" \
"ds_read_b64_tr_b16 v[116:117], v38 offset:8192\n" \
"ds_read_b64_tr_b16 v[118:119], v38 offset:8704\n" \
"ds_read_b64_tr_b16 v[124:125], v39 offset:8192\n" \
"ds_read_b64_tr_b16 v[126:127], v39 offset:8704\n" \
"s_waitcnt lgkmcnt(0)\n" \
"s_barrier\n" \
"s_add_u32 m0, s62, 147456\n" \
"s_xor_b32 s54, s15, 5767168\n" \
"buffer_load_dwordx4 v32, s[40:43], s54 offen nt lds\n" \
"s_add_u32 m0, s62, 148480\n" \
"s_xor_b32 s54, s15, 5767168\n" \
"buffer_load_dwordx4 v33, s[40:43], s54 offen nt lds\n" \
"s_add_u32 m0, s62, 149504\n" \
"s_xor_b32 s54, s15, 5898240\n" \
"buffer_load_dwordx4 v32, s[40:43], s54 offen nt lds\n" \
"s_add_u32 m0, s62, 150528\n" \
"s_xor_b32 s54, s15, 5898240\n" \
"buffer_load_dwordx4 v33, s[40:43], s54 offen nt lds\n" \
"s_add_u32 m0, s62, 131072\n" \
"s_xor_b32 s54, s15, 6029312\n" \
"buffer_load_dwordx4 v32, s[40:43], s54 offen nt lds\n" \
"s_add_u32 m0, s62, 132096\n" \
"s_xor_b32 s54, s15, 6029312\n" \
"buffer_load_dwordx4 v33, s[40:43], s54 offen nt lds\n" \
"s_add_u32 m0, s62, 133120\n" \
"s_xor_b32 s54, s15, 6160384\n" \
"buffer_load_dwordx4 v32, s[40:43], s54 offen nt lds\n" \
"s_add_u32 m0, s62, 134144\n" \
"s_xor_b32 s54, s15, 6160384\n" \
"buffer_load_dwordx4 v33, s[40:43], s54 offen nt lds\n" \
"s_waitcnt vmcnt(32)\n" \
"s_barrier\n" \
"ds_read_b64_tr_b16 a[48:49], v36 offset:49152\n" \
"ds_read_b64_tr_b16 a[50:51], v36 offset:49664\n" \
"ds_read_b64_tr_b16 a[56:57], v37 offset:49152\n" \
"ds_read_b64_tr_b16 a[58:59], v37 offset:49664\n" \
"ds_read_b64_tr_b16 v[128:129], v36 offset:57344\n" \
"ds_read_b64_tr_b16 v[130:131], v36 offset:57856\n" \
"ds_read_b64_tr_b16 v[136:137], v37 offset:57344\n" \
"ds_read_b64_tr_b16 v[138:139], v37 offset:57856\n" \
"ds_read_b64_tr_b16 a[52:53], v36 offset:32768\n" \
"ds_read_b64_tr_b16 a[54:55], v36 offset:33280\n" \
"ds_read_b64_tr_b16 a[60:61], v37 offset:32768\n" \
"ds_read_b64_tr_b16 a[62:63], v37 offset:33280\n" \
"ds_read_b64_tr_b16 v[132:133], v36 offset:40960\n" \
"ds_read_b64_tr_b16 v[134:135], v36 offset:41472\n" \
"ds_read_b64_tr_b16 v[140:141], v37 offset:40960\n" \
"ds_read_b64_tr_b16 v[142:143], v37 offset:41472\n" \
"s_waitcnt lgkmcnt(0)\n" \
"s_barrier\n" \
"s_add_u32 m0, s62, 114688\n" \
"s_xor_b32 s54, s15, 6815744\n" \
"buffer_load_dwordx4 v32, s[40:43], s54 offen nt lds\n" \
"s_add_u32 m0, s62, 115712\n" \
"s_xor_b32 s54, s15, 6815744\n" \
"buffer_load_dwordx4 v33, s[40:43], s54 offen nt lds\n" \
"s_add_u32 m0, s62, 116736\n" \
"s_xor_b32 s54, s15, 6946816\n" \
"buffer_load_dwordx4 v32, s[40:43], s54 offen nt lds\n" \
"s_add_u32 m0, s62, 117760\n" \
"s_xor_b32 s54, s15, 6946816\n" \
"buffer_load_dwordx4 v33, s[40:43], s54 offen nt lds\n" \
"s_add_u32 m0, s62, 98304\n" \
"s_xor_b32 s54, s15, 7077888\n" \
"buffer_load_dwordx4 v32, s[40:43], s54 offen nt lds\n" \
"s_add_u32 m0, s62, 99328\n" \
"s_xor_b32 s54, s15, 7077888\n" \
"buffer_load_dwordx4 v33, s[40:43], s54 offen nt lds\n" \
"s_add_u32 m0, s62, 100352\n" \
"s_xor_b32 s54, s15, 7208960\n" \
"buffer_load_dwordx4 v32, s[40:43], s54 offen nt lds\n" \
"s_add_u32 m0, s62, 101376\n" \
"s_xor_b32 s54, s15, 7208960\n" \
"buffer_load_dwordx4 v33, s[40:43], s54 offen nt lds\n" \
"s_waitcnt vmcnt(32)\n" \
"s_barrier\n" \
"ds_read_b64_tr_b16 a[80:81], v36 offset:16384\n" \
"ds_read_b64_tr_b16 a[82:83], v36 offset:16896\n" \
"ds_read_b64_tr_b16 a[88:89], v37 offset:16384\n" \
"ds_read_b64_tr_b16 a[90:91], v37 offset:16896\n" \
"ds_read_b64_tr_b16 v[144:145], v36 offset:24576\n" \
"ds_read_b64_tr_b16 v[146:147], v36 offset:25088\n" \
"ds_read_b64_tr_b16 v[152:153], v37 offset:24576\n" \
"ds_read_b64_tr_b16 v[154:155], v37 offset:25088\n" \
"ds_read_b64_tr_b16 a[84:85], v36 offset:0\n" \
"ds_read_b64_tr_b16 a[86:87], v36 offset:512\n" \
"ds_read_b64_tr_b16 a[92:93], v37 offset:0\n" \
"ds_read_b64_tr_b16 a[94:95], v37 offset:512\n" \
"ds_read_b64_tr_b16 v[148:149], v36 offset:8192\n" \
"ds_read_b64_tr_b16 v[150:151], v36 offset:8704\n" \
"ds_read_b64_tr_b16 v[156:157], v37 offset:8192\n" \
"ds_read_b64_tr_b16 v[158:159], v37 offset:8704\n" \
"s_waitcnt lgkmcnt(0)\n" \
"s_barrier\n" \
"s_add_u32 m0, s62, 81920\n" \
"s_xor_b32 s54, s15, 7340032\n" \
"buffer_load_dwordx4 v32, s[40:43], s54 offen nt lds\n" \
"s_add_u32 m0, s62, 82944\n" \
"s_xor_b32 s54, s15, 7340032\n" \
"buffer_load_dwordx4 v33, s[40:43], s54 offen nt lds\n" \
"s_add_u32 m0, s62, 83968\n" \
"s_xor_b32 s54, s15, 7471104\n" \
"buffer_load_dwordx4 v32, s[40:43], s54 offen nt lds\n" \
"s_add_u32 m0, s62, 84992\n" \
"s_xor_b32 s54, s15, 7471104\n" \
"buffer_load_dwordx4 v33, s[40:43], s54 offen nt lds\n" \
"s_add_u32 m0, s62, 65536\n" \
"s_xor_b32 s54, s15, 7602176\n" \
"buffer_load_dwordx4 v32, s[40:43], s54 offen nt lds\n" \
"s_add_u32 m0, s62, 66560\n" \
"s_xor_b32 s54, s15, 7602176\n" \
"buffer_load_dwordx4 v33, s[40:43], s54 offen nt lds\n" \
"s_add_u32 m0, s62, 67584\n" \
"s_xor_b32 s54, s15, 7733248\n" \
"buffer_load_dwordx4 v32, s[40:43], s54 offen nt lds\n" \
"s_add_u32 m0, s62, 68608\n" \
"s_xor_b32 s54, s15, 7733248\n" \
"buffer_load_dwordx4 v33, s[40:43], s54 offen nt lds\n" \
"s_waitcnt vmcnt(32)\n" \
"s_barrier\n" \
"ds_read_b64_tr_b16 a[112:113], v34 offset:49152\n" \
"ds_read_b64_tr_b16 a[114:115], v34 offset:49664\n" \
"ds_read_b64_tr_b16 a[120:121], v35 offset:49152\n" \
"ds_read_b64_tr_b16 a[122:123], v35 offset:49664\n" \
"ds_read_b64_tr_b16 v[160:161], v34 offset:57344\n" \
"ds_read_b64_tr_b16 v[162:163], v34 offset:57856\n" \
"ds_read_b64_tr_b16 v[168:169], v35 offset:57344\n" \
"ds_read_b64_tr_b16 v[170:171], v35 offset:57856\n" \
"ds_read_b64_tr_b16 a[116:117], v34 offset:32768\n" \
"ds_read_b64_tr_b16 a[118:119], v34 offset:33280\n" \
"ds_read_b64_tr_b16 a[124:125], v35 offset:32768\n" \
"ds_read_b64_tr_b16 a[126:127], v35 offset:33280\n" \
"ds_read_b64_tr_b16 v[164:165], v34 offset:40960\n" \
"ds_read_b64_tr_b16 v[166:167], v34 offset:41472\n" \
"ds_read_b64_tr_b16 v[172:173], v35 offset:40960\n" \
"ds_read_b64_tr_b16 v[174:175], v35 offset:41472\n" \
"s_waitcnt lgkmcnt(0)\n" \
"s_barrier\n" \
"s_add_u32 m0, s62, 49152\n" \
"s_xor_b32 s54, s15, 7864320\n" \
"buffer_load_dwordx4 v32, s[40:43], s54 offen nt lds\n" \
"s_add_u32 m0, s62, 50176\n" \
"s_xor_b32 s54, s15, 7864320\n" \
"buffer_load_dwordx4 v33, s[40:43], s54 offen nt lds\n" \
"s_add_u32 m0, s62, 51200\n" \
"s_xor_b32 s54, s15, 7995392\n" \
"buffer_load_dwordx4 v32, s[40:43], s54 offen nt lds\n" \
"s_add_u32 m0, s62, 52224\n" \
"s_xor_b32 s54, s15, 7995392\n" \
"buffer_load_dwordx4 v33, s[40:43], s54 offen nt lds\n" \
"s_add_u32 m0, s62, 32768\n" \
"s_xor_b32 s54, s15, 8126464\n" \
"buffer_load_dwordx4 v32, s[40:43], s54 offen nt lds\n" \
"s_add_u32 m0, s62, 33792\n" \
"s_xor_b32 s54, s15, 8126464\n" \
"buffer_load_dwordx4 v33, s[40:43], s54 offen nt lds\n" \
"s_add_u32 m0, s62, 34816\n" \
"s_xor_b32 s54, s15, 8257536\n" \
"buffer_load_dwordx4 v32, s[40:43], s54 offen nt lds\n" \
"s_add_u32 m0, s62, 35840\n" \
"s_xor_b32 s54, s15, 8257536\n" \
"buffer_load_dwordx4 v33, s[40:43], s54 offen nt lds\n" \
"s_waitcnt vmcnt(32)\n" \
"s_barrier\n" \
"ds_read_b64_tr_b16 a[144:145], v34 offset:16384\n" \
"ds_read_b64_tr_b16 a[146:147], v34 offset:16896\n" \
"ds_read_b64_tr_b16 a[152:153], v35 offset:16384\n" \
"ds_read_b64_tr_b16 a[154:155], v35 offset:16896\n" \
"ds_read_b64_tr_b16 v[176:177], v34 offset:24576\n" \
"ds_read_b64_tr_b16 v[178:179], v34 offset:25088\n" \
"ds_read_b64_tr_b16 v[184:185], v35 offset:24576\n" \
"ds_read_b64_tr_b16 v[186:187], v35 offset:25088\n" \
"ds_read_b64_tr_b16 a[148:149], v34 offset:0\n" \
"ds_read_b64_tr_b16 a[150:151], v34 offset:512\n" \
"ds_read_b64_tr_b16 a[156:157], v35 offset:0\n" \
"ds_read_b64_tr_b16 a[158:159], v35 offset:512\n" \
"ds_read_b64_tr_b16 v[180:181], v34 offset:8192\n" \
"ds_read_b64_tr_b16 v[182:183], v34 offset:8704\n" \
"ds_read_b64_tr_b16 v[188:189], v35 offset:8192\n" \
"ds_read_b64_tr_b16 v[190:191], v35 offset:8704\n" \
"s_waitcnt lgkmcnt(0)\n" \
"s_barrier\n" \
"s_add_u32 m0, s62, 16384\n" \
"s_xor_b32 s54, s15, 0\n" \
"buffer_load_dwordx4 v32, s[40:43], s54 offen nt lds\n" \
"s_add_u32 m0, s62, 17408\n" \
"s_xor_b32 s54, s15, 0\n" \
"buffer_load_dwordx4 v33, s[40:43], s54 offen nt lds\n" \
"s_add_u32 m0, s62, 18432\n" \
"s_xor_b32 s54, s15, 131072\n" \
"buffer_load_dwordx4 v32, s[40:43], s54 offen nt lds\n" \
"s_add_u32 m0, s62, 19456\n" \
"s_xor_b32 s54, s15, 131072\n" \
"buffer_load_dwordx4 v33, s[40:43], s54 offen nt lds\n" \
"s_add_u32 m0, s62, 0\n" \
"s_xor_b32 s54, s15, 262144\n" \
"buffer_load_dwordx4 v32, s[40:43], s54 offen nt lds\n" \
"s_add_u32 m0, s62, 1024\n" \
"s_xor_b32 s54, s15, 262144\n" \
"buffer_load_dwordx4 v33, s[40:43], s54 offen nt lds\n" \
"s_add_u32 m0, s62, 2048\n" \
"s_xor_b32 s54, s15, 393216\n" \
"buffer_load_dwordx4 v32, s[40:43], s54 offen nt lds\n" \
"s_add_u32 m0, s62, 3072\n" \
"s_xor_b32 s54, s15, 393216\n" \
"buffer_load_dwordx4 v33, s[40:43], s54 offen nt lds\n" \
"s_waitcnt vmcnt(32)\n" \
"s_barrier\n" \
"ds_read_b64_tr_b16 a[176:177], v38 offset:16384\n" \
"ds_read_b64_tr_b16 a[178:179], v38 offset:16896\n" \
"ds_read_b64_tr_b16 a[184:185], v39 offset:16384\n" \
"ds_read_b64_tr_b16 a[186:187], v39 offset:16896\n" \
"ds_read_b64_tr_b16 v[192:193], v38 offset:24576\n" \
"ds_read_b64_tr_b16 v[194:195], v38 offset:25088\n" \
"ds_read_b64_tr_b16 v[200:201], v39 offset:24576\n" \
"ds_read_b64_tr_b16 v[202:203], v39 offset:25088\n" \
"ds_read_b64_tr_b16 a[180:181], v38 offset:0\n" \
"ds_read_b64_tr_b16 a[182:183], v38 offset:512\n" \
"ds_read_b64_tr_b16 a[188:189], v39 offset:0\n" \
"ds_read_b64_tr_b16 a[190:191], v39 offset:512\n" \
"ds_read_b64_tr_b16 v[196:197], v38 offset:8192\n" \
"ds_read_b64_tr_b16 v[198:199], v38 offset:8704\n" \
"ds_read_b64_tr_b16 v[204:205], v39 offset:8192\n" \
"ds_read_b64_tr_b16 v[206:207], v39 offset:8704\n" \
"s_waitcnt lgkmcnt(0)\n" \
"s_barrier\n" \
"s_add_u32 m0, s62, 147456\n" \
"s_xor_b32 s54, s15, 1048576\n" \
"buffer_load_dwordx4 v32, s[40:43], s54 offen nt lds\n" \
"s_add_u32 m0, s62, 148480\n" \
"s_xor_b32 s54, s15, 1048576\n" \
"buffer_load_dwordx4 v33, s[40:43], s54 offen nt lds\n" \
"s_add_u32 m0, s62, 149504\n" \
"s_xor_b32 s54, s15, 1179648\n" \
"buffer_load_dwordx4 v32, s[40:43], s54 offen nt lds\n" \
"s_add_u32 m0, s62, 150528\n" \
"s_xor_b32 s54, s15, 1179648\n" \
"buffer_load_dwordx4 v33, s[40:43], s54 offen nt lds\n" \
"s_add_u32 m0, s62, 131072\n" \
"s_xor_b32 s54, s15, 1310720\n" \
"buffer_load_dwordx4 v32, s[40:43], s54 offen nt lds\n" \
"s_add_u32 m0, s62, 132096\n" \
"s_xor_b32 s54, s15, 1310720\n" \
"buffer_load_dwordx4 v33, s[40:43], s54 offen nt lds\n" \
"s_add_u32 m0, s62, 133120\n" \
"s_xor_b32 s54, s15, 1441792\n" \
"buffer_load_dwordx4 v32, s[40:43], s54 offen nt lds\n" \
"s_add_u32 m0, s62, 134144\n" \
"s_xor_b32 s54, s15, 1441792\n" \
"buffer_load_dwordx4 v33, s[40:43], s54 offen nt lds\n" \
"s_waitcnt vmcnt(32)\n" \
"s_barrier\n" \
"ds_read_b64_tr_b16 a[208:209], v36 offset:49152\n" \
"ds_read_b64_tr_b16 a[210:211], v36 offset:49664\n" \
"ds_read_b64_tr_b16 a[216:217], v37 offset:49152\n" \
"ds_read_b64_tr_b16 a[218:219], v37 offset:49664\n" \
"ds_read_b64_tr_b16 v[208:209], v36 offset:57344\n" \
"ds_read_b64_tr_b16 v[210:211], v36 offset:57856\n" \
"ds_read_b64_tr_b16 v[216:217], v37 offset:57344\n" \
"ds_read_b64_tr_b16 v[218:219], v37 offset:57856\n" \
"ds_read_b64_tr_b16 a[212:213], v36 offset:32768\n" \
"ds_read_b64_tr_b16 a[214:215], v36 offset:33280\n" \
"ds_read_b64_tr_b16 a[220:221], v37 offset:32768\n" \
"ds_read_b64_tr_b16 a[222:223], v37 offset:33280\n" \
"ds_read_b64_tr_b16 v[212:213], v36 offset:40960\n" \
"ds_read_b64_tr_b16 v[214:215], v36 offset:41472\n" \
"ds_read_b64_tr_b16 v[220:221], v37 offset:40960\n" \
"ds_read_b64_tr_b16 v[222:223], v37 offset:41472\n" \
"s_waitcnt lgkmcnt(0)\n" \
"s_barrier\n" \
"s_add_u32 m0, s62, 114688\n" \
"s_xor_b32 s54, s15, 2097152\n" \
"buffer_load_dwordx4 v32, s[40:43], s54 offen nt lds\n" \
"s_add_u32 m0, s62, 115712\n" \
"s_xor_b32 s54, s15, 2097152\n" \
"buffer_load_dwordx4 v33, s[40:43], s54 offen nt lds\n" \
"s_add_u32 m0, s62, 116736\n" \
"s_xor_b32 s54, s15, 2228224\n" \
"buffer_load_dwordx4 v32, s[40:43], s54 offen nt lds\n" \
"s_add_u32 m0, s62, 117760\n" \
"s_xor_b32 s54, s15, 2228224\n" \
"buffer_load_dwordx4 v33, s[40:43], s54 offen nt lds\n" \
"s_add_u32 m0, s62, 98304\n" \
"s_xor_b32 s54, s15, 2359296\n" \
"buffer_load_dwordx4 v32, s[40:43], s54 offen nt lds\n" \
"s_add_u32 m0, s62, 99328\n" \
"s_xor_b32 s54, s15, 2359296\n" \
"buffer_load_dwordx4 v33, s[40:43], s54 offen nt lds\n" \
"s_add_u32 m0, s62, 100352\n" \
"s_xor_b32 s54, s15, 2490368\n" \
"buffer_load_dwordx4 v32, s[40:43], s54 offen nt lds\n" \
"s_add_u32 m0, s62, 101376\n" \
"s_xor_b32 s54, s15, 2490368\n" \
"buffer_load_dwordx4 v33, s[40:43], s54 offen nt lds\n" \
"s_waitcnt vmcnt(32)\n" \
"s_barrier\n" \
"ds_read_b64_tr_b16 a[224:225], v36 offset:16384\n" \
"ds_read_b64_tr_b16 a[226:227], v36 offset:16896\n" \
"ds_read_b64_tr_b16 a[232:233], v37 offset:16384\n" \
"ds_read_b64_tr_b16 a[234:235], v37 offset:16896\n" \
"ds_read_b64_tr_b16 v[224:225], v36 offset:24576\n" \
"ds_read_b64_tr_b16 v[226:227], v36 offset:25088\n" \
"ds_read_b64_tr_b16 v[232:233], v37 offset:24576\n" \
"ds_read_b64_tr_b16 v[234:235], v37 offset:25088\n" \
"ds_read_b64_tr_b16 a[228:229], v36 offset:0\n" \
"ds_read_b64_tr_b16 a[230:231], v36 offset:512\n" \
"ds_read_b64_tr_b16 a[236:237], v37 offset:0\n" \
"ds_read_b64_tr_b16 a[238:239], v37 offset:512\n" \
"ds_read_b64_tr_b16 v[228:229], v36 offset:8192\n" \
"ds_read_b64_tr_b16 v[230:231], v36 offset:8704\n" \
"ds_read_b64_tr_b16 v[236:237], v37 offset:8192\n" \
"ds_read_b64_tr_b16 v[238:239], v37 offset:8704\n" \
"s_waitcnt lgkmcnt(0)\n" \
"s_barrier\n" \
"s_add_u32 m0, s62, 81920\n" \
"s_xor_b32 s54, s15, 3145728\n" \
"buffer_load_dwordx4 v32, s[40:43], s54 offen nt lds\n" \
"s_add_u32 m0, s62, 82944\n" \
"s_xor_b32 s54, s15, 3145728\n" \
"buffer_load_dwordx4 v33, s[40:43], s54 offen nt lds\n" \
"s_add_u32 m0, s62, 83968\n" \
"s_xor_b32 s54, s15, 3276800\n" \
"buffer_load_dwordx4 v32, s[40:43], s54 offen nt lds\n" \
"s_add_u32 m0, s62, 84992\n" \
"s_xor_b32 s54, s15, 3276800\n" \
"buffer_load_dwordx4 v33, s[40:43], s54 offen nt lds\n" \
"s_add_u32 m0, s62, 65536\n" \
"s_xor_b32 s54, s15, 3407872\n" \
"buffer_load_dwordx4 v32, s[40:43], s54 offen nt lds\n" \
"s_add_u32 m0, s62, 66560\n" \
"s_xor_b32 s54, s15, 3407872\n" \
"buffer_load_dwordx4 v33, s[40:43], s54 offen nt lds\n" \
"s_add_u32 m0, s62, 67584\n" \
"s_xor_b32 s54, s15, 3538944\n" \
"buffer_load_dwordx4 v32, s[40:43], s54 offen nt lds\n" \
"s_add_u32 m0, s62, 68608\n" \
"s_xor_b32 s54, s15, 3538944\n" \
"buffer_load_dwordx4 v33, s[40:43], s54 offen nt lds\n" \
"s_waitcnt vmcnt(32)\n" \
"s_barrier\n" \
"ds_read_b64_tr_b16 a[240:241], v34 offset:49152\n" \
"ds_read_b64_tr_b16 a[242:243], v34 offset:49664\n" \
"ds_read_b64_tr_b16 a[248:249], v35 offset:49152\n" \
"ds_read_b64_tr_b16 a[250:251], v35 offset:49664\n" \
"ds_read_b64_tr_b16 v[240:241], v34 offset:57344\n" \
"ds_read_b64_tr_b16 v[242:243], v34 offset:57856\n" \
"ds_read_b64_tr_b16 v[248:249], v35 offset:57344\n" \
"ds_read_b64_tr_b16 v[250:251], v35 offset:57856\n" \
"ds_read_b64_tr_b16 a[244:245], v34 offset:32768\n" \
"ds_read_b64_tr_b16 a[246:247], v34 offset:33280\n" \
"ds_read_b64_tr_b16 a[252:253], v35 offset:32768\n" \
"ds_read_b64_tr_b16 a[254:255], v35 offset:33280\n" \
"ds_read_b64_tr_b16 v[244:245], v34 offset:40960\n" \
"ds_read_b64_tr_b16 v[246:247], v34 offset:41472\n" \
"ds_read_b64_tr_b16 v[252:253], v35 offset:40960\n" \
"ds_read_b64_tr_b16 v[254:255], v35 offset:41472\n" \
"s_waitcnt lgkmcnt(0)\n" \
"s_barrier\n" \
"s_add_u32 m0, s62, 49152\n" \
"s_xor_b32 s54, s15, 4194304\n" \
"buffer_load_dwordx4 v32, s[40:43], s54 offen nt lds\n" \
"s_add_u32 m0, s62, 50176\n" \
"s_xor_b32 s54, s15, 4194304\n" \
"buffer_load_dwordx4 v33, s[40:43], s54 offen nt lds\n" \
"s_add_u32 m0, s62, 51200\n" \
"s_xor_b32 s54, s15, 4325376\n" \
"buffer_load_dwordx4 v32, s[40:43], s54 offen nt lds\n" \
"s_add_u32 m0, s62, 52224\n" \
"s_xor_b32 s54, s15, 4325376\n" \
"buffer_load_dwordx4 v33, s[40:43], s54 offen nt lds\n" \
"s_add_u32 m0, s62, 32768\n" \
"s_xor_b32 s54, s15, 4456448\n" \
"buffer_load_dwordx4 v32, s[40:43], s54 offen nt lds\n" \
"s_add_u32 m0, s62, 33792\n" \
"s_xor_b32 s54, s15, 4456448\n" \
"buffer_load_dwordx4 v33, s[40:43], s54 offen nt lds\n" \
"s_add_u32 m0, s62, 34816\n" \
"s_xor_b32 s54, s15, 4587520\n" \
"buffer_load_dwordx4 v32, s[40:43], s54 offen nt lds\n" \
"s_add_u32 m0, s62, 35840\n" \
"s_xor_b32 s54, s15, 4587520\n" \
"buffer_load_dwordx4 v33, s[40:43], s54 offen nt lds\n" \
"s_waitcnt vmcnt(32)\n" \
"s_barrier\n" \
"ds_read_b64_tr_b16 a[0:1], v34 offset:16384\n" \
"ds_read_b64_tr_b16 a[2:3], v34 offset:16896\n" \
"ds_read_b64_tr_b16 a[8:9], v35 offset:16384\n" \
"ds_read_b64_tr_b16 a[10:11], v35 offset:16896\n" \
"ds_read_b64_tr_b16 v[48:49], v34 offset:24576\n" \
"ds_read_b64_tr_b16 v[50:51], v34 offset:25088\n" \
"ds_read_b64_tr_b16 v[56:57], v35 offset:24576\n" \
"ds_read_b64_tr_b16 v[58:59], v35 offset:25088\n" \
"ds_read_b64_tr_b16 a[4:5], v34 offset:0\n" \
"ds_read_b64_tr_b16 a[6:7], v34 offset:512\n" \
"ds_read_b64_tr_b16 a[12:13], v35 offset:0\n" \
"ds_read_b64_tr_b16 a[14:15], v35 offset:512\n" \
"ds_read_b64_tr_b16 v[52:53], v34 offset:8192\n" \
"ds_read_b64_tr_b16 v[54:55], v34 offset:8704\n" \
"ds_read_b64_tr_b16 v[60:61], v35 offset:8192\n" \
"ds_read_b64_tr_b16 v[62:63], v35 offset:8704\n" \
"s_waitcnt lgkmcnt(0)\n" \
"s_barrier\n" \
"s_add_u32 m0, s62, 16384\n" \
"s_xor_b32 s54, s15, 5242880\n" \
"buffer_load_dwordx4 v32, s[40:43], s54 offen nt lds\n" \
"s_add_u32 m0, s62, 17408\n" \
"s_xor_b32 s54, s15, 5242880\n" \
"buffer_load_dwordx4 v33, s[40:43], s54 offen nt lds\n" \
"s_add_u32 m0, s62, 18432\n" \
"s_xor_b32 s54, s15, 5373952\n" \
"buffer_load_dwordx4 v32, s[40:43], s54 offen nt lds\n" \
"s_add_u32 m0, s62, 19456\n" \
"s_xor_b32 s54, s15, 5373952\n" \
"buffer_load_dwordx4 v33, s[40:43], s54 offen nt lds\n" \
"s_add_u32 m0, s62, 0\n" \
"s_xor_b32 s54, s15, 5505024\n" \
"buffer_load_dwordx4 v32, s[40:43], s54 offen nt lds\n" \
"s_add_u32 m0, s62, 1024\n" \
"s_xor_b32 s54, s15, 5505024\n" \
"buffer_load_dwordx4 v33, s[40:43], s54 offen nt lds\n" \
"s_add_u32 m0, s62, 2048\n" \
"s_xor_b32 s54, s15, 5636096\n" \
"buffer_load_dwordx4 v32, s[40:43], s54 offen nt lds\n" \
"s_add_u32 m0, s62, 3072\n" \
"s_xor_b32 s54, s15, 5636096\n" \
"buffer_load_dwordx4 v33, s[40:43], s54 offen nt lds\n" \
"s_waitcnt vmcnt(32)\n" \
"s_barrier\n" \
"ds_read_b64_tr_b16 a[32:33], v38 offset:16384\n" \
"ds_read_b64_tr_b16 a[34:35], v38 offset:16896\n" \
"ds_read_b64_tr_b16 a[40:41], v39 offset:16384\n" \
"ds_read_b64_tr_b16 a[42:43], v39 offset:16896\n" \
"ds_read_b64_tr_b16 v[64:65], v38 offset:24576\n" \
"ds_read_b64_tr_b16 v[66:67], v38 offset:25088\n" \
"ds_read_b64_tr_b16 v[72:73], v39 offset:24576\n" \
"ds_read_b64_tr_b16 v[74:75], v39 offset:25088\n" \
"ds_read_b64_tr_b16 a[36:37], v38 offset:0\n" \
"ds_read_b64_tr_b16 a[38:39], v38 offset:512\n" \
"ds_read_b64_tr_b16 a[44:45], v39 offset:0\n" \
"ds_read_b64_tr_b16 a[46:47], v39 offset:512\n" \
"ds_read_b64_tr_b16 v[68:69], v38 offset:8192\n" \
"ds_read_b64_tr_b16 v[70:71], v38 offset:8704\n" \
"ds_read_b64_tr_b16 v[76:77], v39 offset:8192\n" \
"ds_read_b64_tr_b16 v[78:79], v39 offset:8704\n" \
"s_waitcnt lgkmcnt(0)\n" \
"s_barrier\n" \
"s_add_u32 m0, s62, 147456\n" \
"s_xor_b32 s54, s15, 6291456\n" \
"buffer_load_dwordx4 v32, s[40:43], s54 offen nt lds\n" \
"s_add_u32 m0, s62, 148480\n" \
"s_xor_b32 s54, s15, 6291456\n" \
"buffer_load_dwordx4 v33, s[40:43], s54 offen nt lds\n" \
"s_add_u32 m0, s62, 149504\n" \
"s_xor_b32 s54, s15, 6422528\n" \
"buffer_load_dwordx4 v32, s[40:43], s54 offen nt lds\n" \
"s_add_u32 m0, s62, 150528\n" \
"s_xor_b32 s54, s15, 6422528\n" \
"buffer_load_dwordx4 v33, s[40:43], s54 offen nt lds\n" \
"s_add_u32 m0, s62, 131072\n" \
"s_xor_b32 s54, s15, 6553600\n" \
"buffer_load_dwordx4 v32, s[40:43], s54 offen nt lds\n" \
"s_add_u32 m0, s62, 132096\n" \
"s_xor_b32 s54, s15, 6553600\n" \
"buffer_load_dwordx4 v33, s[40:43], s54 offen nt lds\n" \
"s_add_u32 m0, s62, 133120\n" \
"s_xor_b32 s54, s15, 6684672\n" \
"buffer_load_dwordx4 v32, s[40:43], s54 offen nt lds\n" \
"s_add_u32 m0, s62, 134144\n" \
"s_xor_b32 s54, s15, 6684672\n" \
"buffer_load_dwordx4 v33, s[40:43], s54 offen nt lds\n" \
"s_waitcnt vmcnt(32)\n" \
"s_barrier\n" \
"ds_read_b64_tr_b16 a[64:65], v36 offset:49152\n" \
"ds_read_b64_tr_b16 a[66:67], v36 offset:49664\n" \
"ds_read_b64_tr_b16 a[72:73], v37 offset:49152\n" \
"ds_read_b64_tr_b16 a[74:75], v37 offset:49664\n" \
"ds_read_b64_tr_b16 v[80:81], v36 offset:57344\n" \
"ds_read_b64_tr_b16 v[82:83], v36 offset:57856\n" \
"ds_read_b64_tr_b16 v[88:89], v37 offset:57344\n" \
"ds_read_b64_tr_b16 v[90:91], v37 offset:57856\n" \
"ds_read_b64_tr_b16 a[68:69], v36 offset:32768\n" \
"ds_read_b64_tr_b16 a[70:71], v36 offset:33280\n" \
"ds_read_b64_tr_b16 a[76:77], v37 offset:32768\n" \
"ds_read_b64_tr_b16 a[78:79], v37 offset:33280\n" \
"ds_read_b64_tr_b16 v[84:85], v36 offset:40960\n" \
"ds_read_b64_tr_b16 v[86:87], v36 offset:41472\n" \
"ds_read_b64_tr_b16 v[92:93], v37 offset:40960\n" \
"ds_read_b64_tr_b16 v[94:95], v37 offset:41472\n" \
"s_waitcnt lgkmcnt(0)\n" \
"s_barrier\n" \
"ds_write_b128 v2, v[48:51] offset:32768\n" \
"ds_write_b128 v2, v[52:55] offset:33792\n" \
"ds_write_b128 v2, v[56:59] offset:34816\n" \
"ds_write_b128 v2, v[60:63] offset:35840\n" \
"s_waitcnt vmcnt(24)\n" \
"s_barrier\n" \
"ds_read_b64_tr_b16 a[96:97], v36 offset:16384\n" \
"ds_read_b64_tr_b16 a[98:99], v36 offset:16896\n" \
"ds_read_b64_tr_b16 a[104:105], v37 offset:16384\n" \
"ds_read_b64_tr_b16 a[106:107], v37 offset:16896\n" \
"ds_read_b64_tr_b16 v[96:97], v36 offset:24576\n" \
"ds_read_b64_tr_b16 v[98:99], v36 offset:25088\n" \
"ds_read_b64_tr_b16 v[104:105], v37 offset:24576\n" \
"ds_read_b64_tr_b16 v[106:107], v37 offset:25088\n" \
"ds_read_b64_tr_b16 a[100:101], v36 offset:0\n" \
"ds_read_b64_tr_b16 a[102:103], v36 offset:512\n" \
"ds_read_b64_tr_b16 a[108:109], v37 offset:0\n" \
"ds_read_b64_tr_b16 a[110:111], v37 offset:512\n" \
"ds_read_b64_tr_b16 v[100:101], v36 offset:8192\n" \
"ds_read_b64_tr_b16 v[102:103], v36 offset:8704\n" \
"ds_read_b64_tr_b16 v[108:109], v37 offset:8192\n" \
"ds_read_b64_tr_b16 v[110:111], v37 offset:8704\n" \
"s_waitcnt lgkmcnt(0)\n" \
"s_barrier\n" \
"ds_write_b128 v2, v[64:67] offset:0\n" \
"ds_write_b128 v2, v[68:71] offset:1024\n" \
"ds_write_b128 v2, v[72:75] offset:2048\n" \
"ds_write_b128 v2, v[76:79] offset:3072\n" \
"ds_write_b128 v2, v[80:83] offset:16384\n" \
"ds_write_b128 v2, v[84:87] offset:17408\n" \
"ds_write_b128 v2, v[88:91] offset:18432\n" \
"ds_write_b128 v2, v[92:95] offset:19456\n" \
"s_waitcnt vmcnt(16)\n" \
"s_barrier\n" \
"ds_read_b64_tr_b16 a[128:129], v34 offset:49152\n" \
"ds_read_b64_tr_b16 a[130:131], v34 offset:49664\n" \
"ds_read_b64_tr_b16 a[136:137], v35 offset:49152\n" \
"ds_read_b64_tr_b16 a[138:139], v35 offset:49664\n" \
"ds_read_b64_tr_b16 v[48:49], v34 offset:57344\n" \
"ds_read_b64_tr_b16 v[50:51], v34 offset:57856\n" \
"ds_read_b64_tr_b16 v[56:57], v35 offset:57344\n" \
"ds_read_b64_tr_b16 v[58:59], v35 offset:57856\n" \
"ds_read_b64_tr_b16 a[132:133], v34 offset:32768\n" \
"ds_read_b64_tr_b16 a[134:135], v34 offset:33280\n" \
"ds_read_b64_tr_b16 a[140:141], v35 offset:32768\n" \
"ds_read_b64_tr_b16 a[142:143], v35 offset:33280\n" \
"ds_read_b64_tr_b16 v[52:53], v34 offset:40960\n" \
"ds_read_b64_tr_b16 v[54:55], v34 offset:41472\n" \
"ds_read_b64_tr_b16 v[60:61], v35 offset:40960\n" \
"ds_read_b64_tr_b16 v[62:63], v35 offset:41472\n" \
"s_waitcnt lgkmcnt(0)\n" \
"s_barrier\n" \
"ds_write_b128 v1, v[96:99] offset:32768\n" \
"ds_write_b128 v1, v[100:103] offset:33792\n" \
"ds_write_b128 v1, v[104:107] offset:34816\n" \
"ds_write_b128 v1, v[108:111] offset:35840\n" \
"ds_write_b128 v1, v[48:51] offset:49152\n" \
"ds_write_b128 v1, v[52:55] offset:50176\n" \
"ds_write_b128 v1, v[56:59] offset:51200\n" \
"ds_write_b128 v1, v[60:63] offset:52224\n" \
"s_waitcnt vmcnt(8)\n" \
"s_barrier\n" \
"ds_read_b64_tr_b16 a[160:161], v34 offset:16384\n" \
"ds_read_b64_tr_b16 a[162:163], v34 offset:16896\n" \
"ds_read_b64_tr_b16 a[168:169], v35 offset:16384\n" \
"ds_read_b64_tr_b16 a[170:171], v35 offset:16896\n" \
"ds_read_b64_tr_b16 v[64:65], v34 offset:24576\n" \
"ds_read_b64_tr_b16 v[66:67], v34 offset:25088\n" \
"ds_read_b64_tr_b16 v[72:73], v35 offset:24576\n" \
"ds_read_b64_tr_b16 v[74:75], v35 offset:25088\n" \
"ds_read_b64_tr_b16 a[164:165], v34 offset:0\n" \
"ds_read_b64_tr_b16 a[166:167], v34 offset:512\n" \
"ds_read_b64_tr_b16 a[172:173], v35 offset:0\n" \
"ds_read_b64_tr_b16 a[174:175], v35 offset:512\n" \
"ds_read_b64_tr_b16 v[68:69], v34 offset:8192\n" \
"ds_read_b64_tr_b16 v[70:71], v34 offset:8704\n" \
"ds_read_b64_tr_b16 v[76:77], v35 offset:8192\n" \
"ds_read_b64_tr_b16 v[78:79], v35 offset:8704\n" \
"s_waitcnt lgkmcnt(0)\n" \
"s_barrier\n" \
"ds_write_b128 v1, v[64:67] offset:0\n" \
"ds_write_b128 v1, v[68:71] offset:1024\n" \
"ds_write_b128 v1, v[72:75] offset:2048\n" \
"ds_write_b128 v1, v[76:79] offset:3072\n" \
"s_waitcnt vmcnt(0)\n" \
"s_barrier\n" \
"ds_read_b64_tr_b16 a[192:193], v38 offset:16384\n" \
"ds_read_b64_tr_b16 a[194:195], v38 offset:16896\n" \
"ds_read_b64_tr_b16 a[200:201], v39 offset:16384\n" \
"ds_read_b64_tr_b16 a[202:203], v39 offset:16896\n" \
"ds_read_b64_tr_b16 v[80:81], v38 offset:24576\n" \
"ds_read_b64_tr_b16 v[82:83], v38 offset:25088\n" \
"ds_read_b64_tr_b16 v[88:89], v39 offset:24576\n" \
"ds_read_b64_tr_b16 v[90:91], v39 offset:25088\n" \
"ds_read_b64_tr_b16 a[196:197], v38 offset:0\n" \
"ds_read_b64_tr_b16 a[198:199], v38 offset:512\n" \
"ds_read_b64_tr_b16 a[204:205], v39 offset:0\n" \
"ds_read_b64_tr_b16 a[206:207], v39 offset:512\n" \
"ds_read_b64_tr_b16 v[84:85], v38 offset:8192\n" \
"ds_read_b64_tr_b16 v[86:87], v38 offset:8704\n" \
"ds_read_b64_tr_b16 v[92:93], v39 offset:8192\n" \
"ds_read_b64_tr_b16 v[94:95], v39 offset:8704\n" \
"s_waitcnt lgkmcnt(0)\n" \
"s_barrier\n" \
"ds_write_b128 v1, v[80:83] offset:16384\n" \
"ds_write_b128 v1, v[84:87] offset:17408\n" \
"ds_write_b128 v1, v[88:91] offset:18432\n" \
"ds_write_b128 v1, v[92:95] offset:19456\n" \
"s_waitcnt vmcnt(0) lgkmcnt(0)\n" \
"s_barrier\n" \
"s_getpc_b64 s[18:19]\n" \
".Lq1_pc_%=:\n" \
"v_mbcnt_lo_u32_b32 v40, -1, 0\n" \
"v_mbcnt_hi_u32_b32 v40, -1, v40\n" \
"v_lshl_add_u32 v40, s48, 6, v40\n" \
"v_lshlrev_b32 v40, 6, v40\n" \
"v_min_u32 v40, .Lq_fin_%=-.Lq1_pc_%=, v40\n" \
"global_load_dword v41, v40, s[18:19]\n" \
"s_lshl_b32 s62, s48, 11\n" \
"s_mov_b32 s21, s12\n" \
"s_add_u32 s12, s63, s62\n" \
"s_mov_b32 s49, 0\n" \
"s_mov_b32 s52, 0\n" \
"s_mov_b32 s81, 0\n" \
"s_mov_b32 s88, 0\n" \
"s_mov_b32 s95, 0x7ffffff0\n" \
"s_mov_b32 s98, 0x3e0293ee\n" \
"s_mov_b32 s99, 0x3e0293ee\n" \
"v_mov_b32 v20, 0x3f803f80\n" \
"v_mov_b32 v21, 0x3f803f80\n" \
"v_mov_b32 v22, 0x3f803f80\n" \
"v_mov_b32 v23, 0x3f803f80\n" \
"v_mov_b32 v24, 0\n" \
"v_mov_b32 v25, 0\n" \
"v_mov_b32 v26, 0\n" \
"v_mov_b32 v27, 0\n" \
"v_mov_b32 v28, 0\n" \
"v_mov_b32 v29, 0\n" \
"v_mov_b32 v104, 0\n" \
"v_mov_b32 v105, 0\n" \
"v_mov_b32 v106, 0\n" \
"v_mov_b32 v107, 0\n" \
"v_mov_b32 v108, 0\n" \
"v_mov_b32 v109, 0\n" \
"v_mov_b32 v110, 0\n" \
"v_mov_b32 v111, 0\n" \
"v_cmp_le_i32_e64 s[64:65], 0, v14\n" \
"v_cmp_le_i32_e64 s[66:67], 1, v14\n" \
"v_cmp_le_i32_e64 s[68:69], 2, v14\n" \
"v_cmp_le_i32_e64 s[70:71], 3, v14\n" \
"v_cmp_le_i32_e64 s[72:73], 16, v14\n" \
"v_cmp_le_i32_e64 s[74:75], 17, v14\n" \
"v_cmp_le_i32_e64 s[76:77], 18, v14\n" \
"v_cmp_le_i32_e64 s[78:79], 19, v14\n" \
"s_mov_b32 s82, -1\n" \
"s_mov_b32 s83, 0\n" \
"s_mov_b32 s6, 0x33333333\n" \
"s_mov_b32 s7, 0x33333333\n" \
"s_mov_b32 s8, 0x53800000\n" \
"s_mov_b32 s41, s10\n" \
"s_mov_b32 s22, 0xfffffc00\n" \
"s_mov_b32 s23, -1\n" \
"s_mul_i32 s32, s46, 1\n" \
"s_mov_b32 s33, s32\n" \
"s_mov_b32 s34, s46\n" \
"s_add_u32 s35, s17, s13\n" \
"s_mov_b32 s10, s14\n" \
"s_mov_b32 s14, s21\n" \
"s_lshl_b32 s42, s48, 10\n" \
"s_add_u32 s42, s42, 114688\n" \
"s_and_b32 s43, s48, 1\n" \
"s_lshl_b32 s43, s43, 12\n" \
"s_mov_b32 s54, 0\n" \
"buffer_store_dwordx4 v[24:27], v7, s[36:39], s54 offen sc1\n" \
"s_mov_b32 s54, 131072\n" \
"buffer_store_dwordx4 v[24:27], v7, s[36:39], s54 offen sc1\n" \
"s_add_u32 s54, s94, 65536\n" \
"buffer_store_dword v24, v10, s[36:39], s54 offen sc1\n" \
"s_mov_b32 s54, 262144\n" \
"buffer_store_dwordx4 v[24:27], v7, s[36:39], s54 offen sc1\n" \
"s_add_u32 s54, s94, 131072\n" \
"buffer_store_dword v24, v10, s[36:39], s54 offen sc1\n" \
"s_mov_b32 s54, 393216\n" \
"buffer_store_dwordx4 v[24:27], v7, s[36:39], s54 offen sc1\n" \
"s_add_u32 s54, s94, 196608\n" \
"buffer_store_dword v24, v10, s[36:39], s54 offen sc1\n" \
"s_cmp_eq_u32 s48, 0\n" \
"s_cselect_b32 s9, 7, 0\n" \
"buffer_load_dword v30, v15, s[36:39], s80 offen sc1\n" \
"s_waitcnt vmcnt(0)\n" \
"v_cmp_ge_u32_e64 s[96:97], s81, v30\n" \
"s_nop 4\n" \
"s_and_b32 s96, s96, s9\n" \
"s_cbranch_scc1 .Lq_f0_cold_%=\n" \
".Lq_f0_ok_%=:\n" \
"s_barrier\n" \
"s_add_u32 m0, s62, 131072\n" \
"s_sub_u32 s15, s12, 114688\n" \
"buffer_load_dwordx4 v3, s[36:39], s15 offen offset:0 sc1 lds\n" \
"buffer_load_dwordx4 v3, s[36:39], s15 offen offset:1024 sc1 lds\n" \
"s_add_u32 s54, s49, 0\n" \
"s_and_b32 s54, s54, 3\n" \
"s_mul_i32 s58, s54, 131072\n" \
"s_add_u32 s54, s49, 1\n" \
"s_and_b32 s54, s54, 3\n" \
"s_mul_i32 s59, s54, 131072\n" \
"s_add_u32 s54, s49, 2\n" \
"s_and_b32 s54, s54, 3\n" \
"s_mul_i32 s60, s54, 131072\n" \
"s_add_u32 s54, s49, 3\n" \
"s_and_b32 s54, s54, 3\n" \
"s_mul_i32 s61, s54, 131072\n" \
"s_mov_b32 s53, 0\n" \
"s_cmp_lg_u32 s48, 0\n" \
"s_cbranch_scc1 .Lq_go0x_%=\n" \
".Lq_go0_%=:\n" \
"buffer_load_dword v13, v0, s[36:39], 0 offen sc1\n" \
"s_waitcnt vmcnt(0)\n" \
"v_cmp_eq_u32 vcc, 0, v13\n" \
"s_add_u32 s53, s53, 1\n" \
"s_cmp_lt_u32 s53, 32768\n" \
"s_cselect_b64 s[54:55], -1, 0\n" \
"s_and_b64 vcc, vcc, s[54:55]\n" \
"s_cbranch_vccz .Lq_go0x_%=\n" \
"s_sleep 4\n" \
"s_branch .Lq_go0_%=\n" \
".Lq_go0x_%=:\n" \
"s_lshr_b32 s53, s58, 1\n" \
"s_add_u32 s92, s53, s94\n" \
"s_add_u32 s55, s53, s41\n" \
"s_xor_b32 s89, s55, 0x4800\n" \
"s_xor_b32 s90, s55, 0x800\n" \
"s_xor_b32 s91, s55, 0x4000\n" \
"s_lshr_b32 s93, s59, 1\n" \
"s_add_u32 s93, s93, s94\n" \
"s_add_u32 s81, s49, 2\n" \
"s_min_u32 s81, s81, 63\n" \
"ds_read_b128 v[32:35], v2 offset:32768\n" \
"ds_read_b128 v[36:39], v2 offset:33792\n" \
"ds_read_b128 v[40:43], v2 offset:34816\n" \
"ds_read_b128 v[44:47], v2 offset:35840\n" \
"ds_read_b128 v[48:51], v2 offset:0\n" \
"ds_read_b128 v[52:55], v2 offset:1024\n" \
"ds_read_b128 v[56:59], v2 offset:2048\n" \
"ds_read_b128 v[60:63], v2 offset:3072\n" \
"v_mov_b64 v[80:81], 0\n" \
"v_mov_b64 v[82:83], 0\n" \
"v_mov_b64 v[84:85], 0\n" \
"v_mov_b64 v[86:87], 0\n" \
"v_mov_b64 v[88:89], 0\n" \
"v_mov_b64 v[90:91], 0\n" \
"v_mov_b64 v[92:93], 0\n" \
"v_mov_b64 v[94:95], 0\n" \
"v_mov_b32 v13, -1\n" \
"v_add_u32 v14, s62, v3\n" \
"v_xor_b32 v31, 0x400, v11\n" \
"s_max_u32 s53, s49, 1\n" \
"s_sub_u32 s53, s53, 1\n" \
"v_mov_b32 v30, s53\n" \
"s_mov_b32 m0, s42\n" \
"s_memrealtime s[56:57]\n" \
"s_waitcnt lgkmcnt(0)\n" \
"s_sub_u32 s50, s56, 20\n" \
"s_waitcnt vmcnt(0) lgkmcnt(0)\n" \
"s_nop 7\n" \
"s_nop 7\n" \
".p2align 6\n" \
".Lq_loop_%=:\n" \
"s_getpc_b64 s[18:19]\n" \
".Lq_wp_%=:\n" \
"s_add_u32 s18, s18, .Lq_ws_%=-.Lq_wp_%=\n" \
"s_addc_u32 s19, s19, 0\n" \
"s_sub_u32 s21, s50, 5\n" \
"s_nop 0\n" \
".Lq_w_%=:\n" \
"s_memrealtime s[56:57]\n" \
"s_waitcnt lgkmcnt(0)\n" \
"s_sub_u32 s54, s56, s21\n" \
"s_cmp_gt_u32 s54, s22\n" \
"s_cbranch_scc1 .Lq_w_%=\n" \
"s_min_u32 s21, s54, 3\n" \
"s_lshl2_add_u32 s18, s21, s18\n" \
"s_setpc_b64 s[18:19]\n" \
"s_nop 0\n" \
".Lq_ws_%=:\n" \
"s_nop 4\n" \
"s_nop 5\n" \
"s_nop 4\n" \
".Lq_w4_%=:\n" \
"s_barrier\n" \
".Lq_go_%=:\n" \
"buffer_load_dwordx4 v4, s[36:39], s55 offen sc1 lds\n" \
"s_memtime s[0:1]\n" \
"s_cmp_lt_u32 s54, 8\n" \
"s_cselect_b32 s51, s50, s56\n" \
"s_cselect_b32 s23, s23, s49\n" \
"s_cselect_b32 s53, 0, s14\n" \
"s_add_u32 s53, s53, s34\n" \
"v_mov_b32 v17, s35\n" \
"s_nop 7\n" \
"s_nop 7\n" \
"s_add_u32 m0, s42, 4096\n" \
"s_nop 0\n" \
"buffer_load_dwordx4 v4, s[36:39], s89 offen sc1 lds\n" \
"s_nop 7\n" \
"s_nop 7\n" \
"s_nop 7\n" \
"s_add_u32 m0, s42, 8192\n" \
"s_nop 0\n" \
"buffer_load_dwordx4 v4, s[36:39], s90 offen sc1 lds\n" \
"s_add_u32 m0, s42, 12288\n" \
"s_nop 0\n" \
"buffer_load_dwordx4 v4, s[36:39], s91 offen sc1 lds\n" \
"s_add_u32 m0, s62, 147456\n" \
"s_sub_u32 s15, s12, 81920\n" \
"buffer_load_dwordx4 v3, s[36:39], s15 offen offset:0 sc1 lds\n" \
"buffer_load_dwordx4 v3, s[36:39], s15 offen offset:1024 sc1 lds\n" \
"s_add_u32 m0, s62, 139264\n" \
"s_sub_u32 s15, s12, 98304\n" \
"buffer_load_dwordx4 v3, s[36:39], s15 offen offset:0 sc1 lds\n" \
"buffer_load_dwordx4 v3, s[36:39], s15 offen offset:1024 sc1 lds\n" \
"s_waitcnt lgkmcnt(0)\n" \
"s_waitcnt vmcnt(7)\n" \
"s_barrier\n" \
"s_memtime s[2:3]\n" \
"ds_read_b128 v[64:67], v3 offset:0\n" \
"s_nop 7\n" \
"ds_read_b128 v[68:71], v3 offset:1024\n" \
"s_waitcnt lgkmcnt(1)\n" \
"v_smfmac_f32_16x16x64_bf16 v[80:83], v[64:67], a[0:7], v12\n" \
"ds_read_b128 v[72:75], v3 offset:2048\n" \
"v_min3_u32 v13, v13, v64, v66\n" \
"v_smfmac_f32_16x16x64_bf16 v[84:87], v[64:67], a[8:15], v12\n" \
"ds_read_b128 v[76:79], v3 offset:3072\n" \
"v_smfmac_f32_16x16x64_bf16 v[88:91], v[64:67], v[32:39], v12\n" \
"v_smfmac_f32_16x16x64_bf16 v[92:95], v[64:67], v[40:47], v12\n" \
"s_waitcnt lgkmcnt(2)\n" \
"v_smfmac_f32_16x16x64_bf16 v[80:83], v[68:71], a[16:23], v12\n" \
"v_min3_u32 v13, v13, v68, v70\n" \
"v_smfmac_f32_16x16x64_bf16 v[84:87], v[68:71], a[24:31], v12\n" \
"s_waitcnt lgkmcnt(0)\n" \
"ds_read_b128 v[32:35], v2 offset:16384\n" \
"v_min3_u32 v13, v13, v72, v74\n" \
"v_smfmac_f32_16x16x64_bf16 v[88:91], v[68:71], v[112:119], v12\n" \
"ds_read_b128 v[36:39], v2 offset:17408\n" \
"v_min3_u32 v13, v13, v76, v78\n" \
"v_smfmac_f32_16x16x64_bf16 v[92:95], v[68:71], v[120:127], v12\n" \
"s_waitcnt vmcnt(6)\n" \
"s_barrier\n" \
"ds_read_b128 v[64:67], v3 offset:4096\n" \
"v_smfmac_f32_16x16x64_bf16 v[80:83], v[72:75], a[32:39], v12\n" \
"ds_read_b128 v[68:71], v3 offset:5120\n" \
"v_cmp_eq_u32 vcc, 0, v13\n" \
"v_smfmac_f32_16x16x64_bf16 v[84:87], v[72:75], a[40:47], v12\n" \
"ds_read_b128 v[40:43], v2 offset:18432\n" \
"s_sub_u32 s2, s2, s0\n" \
"v_smfmac_f32_16x16x64_bf16 v[88:91], v[72:75], v[48:55], v12\n" \
"ds_read_b128 v[44:47], v2 offset:19456\n" \
"s_mul_i32 s2, s2, 23\n" \
"v_smfmac_f32_16x16x64_bf16 v[92:95], v[72:75], v[56:63], v12\n" \
"ds_read_b128 v[72:75], v3 offset:6144\n" \
"s_lshr_b32 s2, s2, 9\n" \
"v_smfmac_f32_16x16x64_bf16 v[80:83], v[76:79], a[48:55], v12\n" \
"s_cbranch_vccnz .Lq_retry_%=\n" \
"s_add_u32 s3, s51, s2\n" \
"v_smfmac_f32_16x16x64_bf16 v[84:87], v[76:79], a[56:63], v12\n" \
"ds_read_b128 v[48:51], v1 offset:32768\n" \
"s_add_u32 s53, s53, s3\n" \
"v_smfmac_f32_16x16x64_bf16 v[88:91], v[76:79], v[128:135], v12\n" \
"ds_read_b128 v[52:55], v1 offset:33792\n" \
"s_max_u32 s53, s53, 1\n" \
"v_mov_b32 v16, s53\n" \
"v_smfmac_f32_16x16x64_bf16 v[92:95], v[76:79], v[136:143], v12\n" \
"ds_read_b128 v[76:79], v3 offset:7168\n" \
"s_waitcnt lgkmcnt(7)\n" \
"v_smfmac_f32_16x16x64_bf16 v[80:83], v[64:67], a[64:71], v12\n" \
"buffer_store_dwordx2 v[16:17], v10, s[36:39], s92 offen sc1\n" \
"v_min3_u32 v13, v13, v64, v66\n" \
"v_smfmac_f32_16x16x64_bf16 v[84:87], v[64:67], a[72:79], v12\n" \
"ds_read_b128 v[56:59], v1 offset:34816\n" \
"v_smfmac_f32_16x16x64_bf16 v[88:91], v[64:67], v[32:39], v12\n" \
"ds_read_b128 v[60:63], v1 offset:35840\n" \
"s_waitcnt lgkmcnt(6)\n" \
"v_smfmac_f32_16x16x64_bf16 v[92:95], v[64:67], v[40:47], v12\n" \
"v_smfmac_f32_16x16x64_bf16 v[80:83], v[68:71], a[80:87], v12\n" \
"v_min3_u32 v13, v13, v68, v70\n" \
"v_smfmac_f32_16x16x64_bf16 v[84:87], v[68:71], a[88:95], v12\n" \
"ds_read_b128 v[32:35], v1 offset:49152\n" \
"v_smfmac_f32_16x16x64_bf16 v[88:91], v[68:71], v[144:151], v12\n" \
"ds_read_b128 v[36:39], v1 offset:50176\n" \
"v_smfmac_f32_16x16x64_bf16 v[92:95], v[68:71], v[152:159], v12\n" \
"s_waitcnt vmcnt(5)\n" \
"s_barrier\n" \
"ds_read_b128 v[64:67], v3 offset:8192\n" \
"s_waitcnt lgkmcnt(8)\n" \
"v_smfmac_f32_16x16x64_bf16 v[80:83], v[72:75], a[96:103], v12\n" \
"ds_read_b128 v[68:71], v3 offset:9216\n" \
"v_min3_u32 v13, v13, v72, v74\n" \
"v_smfmac_f32_16x16x64_bf16 v[84:87], v[72:75], a[104:111], v12\n" \
"ds_read_b128 v[40:43], v1 offset:51200\n" \
"s_waitcnt lgkmcnt(8)\n" \
"v_smfmac_f32_16x16x64_bf16 v[88:91], v[72:75], v[48:55], v12\n" \
"ds_read_b128 v[44:47], v1 offset:52224\n" \
"s_waitcnt lgkmcnt(6)\n" \
"v_smfmac_f32_16x16x64_bf16 v[92:95], v[72:75], v[56:63], v12\n" \
"ds_read_b128 v[72:75], v3 offset:10240\n" \
"v_smfmac_f32_16x16x64_bf16 v[80:83], v[76:79], a[112:119], v12\n" \
"v_min3_u32 v13, v13, v76, v78\n" \
"v_smfmac_f32_16x16x64_bf16 v[84:87], v[76:79], a[120:127], v12\n" \
"ds_read_b128 v[48:51], v1 offset:0\n" \
"v_smfmac_f32_16x16x64_bf16 v[88:91], v[76:79], v[160:167], v12\n" \
"ds_read_b128 v[52:55], v1 offset:1024\n" \
"v_smfmac_f32_16x16x64_bf16 v[92:95], v[76:79], v[168:175], v12\n" \
"ds_read_b128 v[76:79], v3 offset:11264\n" \
"s_waitcnt lgkmcnt(7)\n" \
"v_smfmac_f32_16x16x64_bf16 v[80:83], v[64:67], a[128:135], v12\n" \
"buffer_store_dwordx4 v[96:99], v11, s[36:39], s95 offen sc1\n" \
"v_min3_u32 v13, v13, v64, v66\n" \
"v_smfmac_f32_16x16x64_bf16 v[84:87], v[64:67], a[136:143], v12\n" \
"ds_read_b128 v[56:59], v1 offset:2048\n" \
"v_smfmac_f32_16x16x64_bf16 v[88:91], v[64:67], v[32:39], v12\n" \
"ds_read_b128 v[60:63], v1 offset:3072\n" \
"s_waitcnt lgkmcnt(6)\n" \
"v_smfmac_f32_16x16x64_bf16 v[92:95], v[64:67], v[40:47], v12\n" \
"ds_read_b128 v[64:67], v3 offset:12288\n" \
"v_smfmac_f32_16x16x64_bf16 v[80:83], v[68:71], a[144:151], v12\n" \
"v_min3_u32 v13, v13, v68, v70\n" \
"v_smfmac_f32_16x16x64_bf16 v[84:87], v[68:71], a[152:159], v12\n" \
"ds_read_b128 v[32:35], v1 offset:16384\n" \
"v_smfmac_f32_16x16x64_bf16 v[88:91], v[68:71], v[176:183], v12\n" \
"ds_read_b128 v[36:39], v1 offset:17408\n" \
"v_smfmac_f32_16x16x64_bf16 v[92:95], v[68:71], v[184:191], v12\n" \
"ds_read_b128 v[68:71], v3 offset:13312\n" \
"s_waitcnt lgkmcnt(9)\n" \
"v_smfmac_f32_16x16x64_bf16 v[80:83], v[72:75], a[160:167], v12\n" \
"v_min3_u32 v13, v13, v72, v74\n" \
"v_smfmac_f32_16x16x64_bf16 v[84:87], v[72:75], a[168:175], v12\n" \
"ds_read_b128 v[40:43], v1 offset:18432\n" \
"s_waitcnt lgkmcnt(8)\n" \
"v_smfmac_f32_16x16x64_bf16 v[88:91], v[72:75], v[48:55], v12\n" \
"ds_read_b128 v[44:47], v1 offset:19456\n" \
"s_waitcnt lgkmcnt(6)\n" \
"v_smfmac_f32_16x16x64_bf16 v[92:95], v[72:75], v[56:63], v12\n" \
"ds_read_b128 v[72:75], v3 offset:14336\n" \
"v_smfmac_f32_16x16x64_bf16 v[80:83], v[76:79], a[176:183], v12\n" \
"buffer_store_dwordx4 v[100:103], v31, s[36:39], s95 offen sc1\n" \
"v_min3_u32 v13, v13, v76, v78\n" \
"v_smfmac_f32_16x16x64_bf16 v[84:87], v[76:79], a[184:191], v12\n" \
"ds_read_b128 v[48:51], v3 offset:20480\n" \
"v_smfmac_f32_16x16x64_bf16 v[88:91], v[76:79], v[192:199], v12\n" \
"ds_read_b128 v[52:55], v3 offset:21504\n" \
"v_smfmac_f32_16x16x64_bf16 v[92:95], v[76:79], v[200:207], v12\n" \
"ds_read_b128 v[76:79], v3 offset:15360\n" \
"s_waitcnt lgkmcnt(9)\n" \
"v_smfmac_f32_16x16x64_bf16 v[80:83], v[64:67], a[192:199], v12\n" \
"v_min3_u32 v13, v13, v64, v66\n" \
"v_smfmac_f32_16x16x64_bf16 v[84:87], v[64:67], a[200:207], v12\n" \
"ds_read_b128 v[56:59], v3 offset:22528\n" \
"s_waitcnt lgkmcnt(8)\n" \
"v_smfmac_f32_16x16x64_bf16 v[88:91], v[64:67], v[32:39], v12\n" \
"ds_read_b128 v[60:63], v3 offset:23552\n" \
"s_waitcnt lgkmcnt(6)\n" \
"v_smfmac_f32_16x16x64_bf16 v[92:95], v[64:67], v[40:47], v12\n" \
"v_smfmac_f32_16x16x64_bf16 v[80:83], v[68:71], a[208:215], v12\n" \
"v_min3_u32 v13, v13, v68, v70\n" \
"v_smfmac_f32_16x16x64_bf16 v[84:87], v[68:71], a[216:223], v12\n" \
"ds_read_b128 v[32:35], v3 offset:16384\n" \
"v_smfmac_f32_16x16x64_bf16 v[88:91], v[68:71], v[208:215], v12\n" \
"ds_read_b128 v[36:39], v3 offset:17408\n" \
"v_smfmac_f32_16x16x64_bf16 v[92:95], v[68:71], v[216:223], v12\n" \
"s_waitcnt lgkmcnt(7)\n" \
"v_smfmac_f32_16x16x64_bf16 v[80:83], v[72:75], a[224:231], v12\n" \
"v_min3_u32 v13, v13, v72, v74\n" \
"v_smfmac_f32_16x16x64_bf16 v[84:87], v[72:75], a[232:239], v12\n" \
"ds_read_b128 v[40:43], v3 offset:18432\n" \
"s_waitcnt lgkmcnt(5)\n" \
"v_smfmac_f32_16x16x64_bf16 v[80:83], v[76:79], a[240:247], v12\n" \
"ds_read_b128 v[44:47], v3 offset:19456\n" \
"v_min3_u32 v13, v13, v76, v78\n" \
"v_smfmac_f32_16x16x64_bf16 v[84:87], v[76:79], a[248:255], v12\n" \
"v_cmp_eq_u32 vcc, 0, v13\n" \
"v_smfmac_f32_16x16x64_bf16 v[88:91], v[72:75], v[224:231], v12\n" \
"v_add_f32_e64 v80, v80, v82\n" \
"v_smfmac_f32_16x16x64_bf16 v[92:95], v[72:75], v[232:239], v12\n" \
"v_add_f32_e64 v81, v81, v83\n" \
"v_smfmac_f32_16x16x64_bf16 v[88:91], v[76:79], v[240:247], v12\n" \
"s_cbranch_vccnz .Lq_retry_%=\n" \
"v_add_f32 v84, v84, v86\n" \
"v_smfmac_f32_16x16x64_bf16 v[92:95], v[76:79], v[248:255], v12\n" \
"v_add_f32_e64 v85, v85, v87\n" \
"v_pk_mul_f32 v[80:81], v[80:81], s[98:99]\n" \
"v_pk_mul_f32 v[84:85], v[84:85], s[98:99]\n" \
"v_cvt_pk_bf16_f32 v80, v80, v81\n" \
"v_cvt_pk_bf16_f32 v84, v84, v85\n" \
"v_pk_add_f32 v[88:89], v[88:89], v[90:91]\n" \
"v_cvt_pk_bf16_f32 v81, v88, v89\n" \
"s_nop 0\n" \
"v_pk_add_f32 v[92:93], v[92:93], v[94:95]\n" \
"v_cvt_pk_bf16_f32 v85, v92, v93\n" \
"ds_write2_b64 v5, v[80:81], v[84:85] offset1:64\n" \
"buffer_load_dword v104, v8, s[36:39], s58 offen offset:0 sc1\n" \
"buffer_load_dword v106, v8, s[36:39], s58 offen offset:256 sc1\n" \
"s_cmp_eq_u32 s49, 0\n" \
"s_nop 0\n" \
"s_nop 0\n" \
"s_cbranch_scc1 .Lq_first_%=\n" \
".Lq_first2_%=:\n" \
"s_cmp_eq_u32 s49, 63\n" \
"s_waitcnt vmcnt(5)\n" \
"s_waitcnt lgkmcnt(0)\n" \
"s_barrier\n" \
"ds_read_b64_tr_b16 v[64:65], v6 offset:0\n" \
"ds_read_b64_tr_b16 v[66:67], v6 offset:512\n" \
"ds_read_b64_tr_b16 v[68:69], v6 offset:1024\n" \
"ds_read_b64_tr_b16 v[70:71], v6 offset:1536\n" \
"ds_read_b64_tr_b16 v[72:73], v6 offset:2048\n" \
"ds_read_b64_tr_b16 v[74:75], v6 offset:2560\n" \
"s_cbranch_scc1 .Lq_last_%=\n" \
"s_waitcnt lgkmcnt(4)\n" \
"v_mfma_f32_16x16x32_bf16 v[88:91], v[64:67], v[64:67], 0\n" \
"ds_read_b64_tr_b16 v[76:77], v6 offset:3072\n" \
"v_mfma_f32_16x16x32_bf16 v[80:83], v[32:35], v[64:67], 0\n" \
"ds_read_b64_tr_b16 v[78:79], v6 offset:3584\n" \
"s_waitcnt lgkmcnt(4)\n" \
"v_mfma_f32_16x16x32_bf16 v[88:91], v[68:71], v[68:71], v[88:91]\n" \
"ds_read_b128 v[96:99], v14 offset:32768\n" \
"s_waitcnt lgkmcnt(3)\n" \
"v_mfma_f32_16x16x32_bf16 v[88:91], v[72:75], v[72:75], v[88:91]\n" \
"ds_read_b128 v[100:103], v14 offset:33792\n" \
"v_mfma_f32_16x16x32_bf16 v[80:83], v[36:39], v[68:71], v[80:83]\n" \
"buffer_store_dword v30, v10, s[36:39], s87 offen sc1\n" \
"s_waitcnt lgkmcnt(2)\n" \
"v_mfma_f32_16x16x32_bf16 v[88:91], v[76:79], v[76:79], v[88:91]\n" \
"buffer_load_dword v30, v15, s[36:39], s80 offen sc1\n" \
"v_mfma_f32_16x16x32_bf16 v[80:83], v[40:43], v[72:75], v[80:83]\n" \
"v_mfma_f32_16x16x32_bf16 v[80:83], v[44:47], v[76:79], v[80:83]\n" \
"v_max_f32 v13, v90, v91\n" \
"v_max_f32 v14, v90, v91\n" \
"v_mfma_f32_16x16x32_bf16 v[84:87], v[48:51], v[64:67], 0\n" \
"s_nop 0\n" \
"v_permlane16_swap_b32 v13, v14\n" \
"v_mfma_f32_16x16x32_bf16 v[84:87], v[52:55], v[68:71], v[84:87]\n" \
"v_max_f32 v13, v13, v14\n" \
"v_max_f32 v14, v13, v14\n" \
"v_mfma_f32_16x16x32_bf16 v[84:87], v[56:59], v[72:75], v[84:87]\n" \
"s_nop 0\n" \
"v_permlane32_swap_b32 v13, v14\n" \
"v_mfma_f32_16x16x32_bf16 v[84:87], v[60:63], v[76:79], v[84:87]\n" \
"v_max_f32 v14, v13, v14\n" \
"v_sub_f32 v80, v80, v14\n" \
"v_sub_f32 v81, v81, v14\n" \
"v_exp_f32 v80, v80\n" \
"v_pk_add_f32 v[82:83], v[82:83], v[14:15] op_sel_hi:[1,0] neg_lo:[0,1] neg_hi:[0,1]\n" \
"v_exp_f32 v81, v81\n" \
"v_exp_f32 v82, v82\n" \
"v_exp_f32_e64 v83, v83\n" \
"v_pk_add_f32 v[84:85], v[84:85], v[14:15] op_sel_hi:[1,0] neg_lo:[0,1] neg_hi:[0,1]\n" \
"v_pk_add_f32 v[86:87], v[86:87], v[14:15] op_sel_hi:[1,0] neg_lo:[0,1] neg_hi:[0,1]\n" \
"v_pk_add_f32 v[90:91], v[90:91], v[14:15] op_sel_hi:[1,0] neg_lo:[0,1] neg_hi:[0,1]\n" \
"v_exp_f32 v84, v84\n" \
"v_exp_f32 v85, v85\n" \
"v_exp_f32 v86, v86\n" \
"v_exp_f32 v87, v87\n" \
"v_cvt_pk_bf16_f32 v80, v80, v81\n" \
"v_cvt_pk_bf16_f32 v81, v82, v83\n" \
"v_cvt_pk_bf16_f32 v82, v84, v85\n" \
"v_cvt_pk_bf16_f32 v83, v86, v87\n" \
"v_exp_f32 v90, v90\n" \
"s_waitcnt lgkmcnt(1)\n" \
"v_mfma_f32_16x16x32_bf16 v[84:87], v[96:99], v[80:83], 0\n" \
"v_exp_f32 v91, v91\n" \
"s_waitcnt lgkmcnt(0)\n" \
"v_mfma_f32_16x16x32_bf16 v[92:95], v[100:103], v[80:83], 0\n" \
"v_cvt_pk_bf16_f32 v26, v90, v91\n" \
"v_mfma_f32_16x16x32_bf16 v[16:19], v[20:23], v[80:83], 0\n" \
"s_nop 0\n" \
"v_mfma_f32_16x16x32_bf16 v[16:19], v[20:23], v[26:29], v[16:19]\n" \
"s_waitcnt vmcnt(2)\n" \
"v_mfma_f32_16x16x32_bf16 v[84:87], v[104:107], v[26:29], v[84:87]\n" \
"v_mfma_f32_16x16x32_bf16 v[92:95], v[104:107], v[24:27], v[92:95]\n" \
"v_min_u32 v13, v104, v106\n" \
"v_cmp_eq_u32 vcc, 0, v13\n" \
"v_rcp_f32_e64 v14, v16\n" \
"v_cmp_nlt_f32_e64 s[26:27], v16, s8\n" \
".Lq_vr2_%=:\n" \
"v_pk_mul_f32 v[84:85], v[84:85], v[14:15] op_sel_hi:[1,0]\n" \
"v_pk_mul_f32 v[86:87], v[86:87], v[14:15] op_sel_hi:[1,0]\n" \
"v_cvt_pk_bf16_f32 v88, v84, v85\n" \
"v_cvt_pk_bf16_f32 v89, v86, v87\n" \
"v_max_u32 v88, 1, v88\n" \
"s_and_b64 s[26:27], s[26:27], s[6:7]\n" \
"s_or_b64 vcc, vcc, s[26:27]\n" \
"s_cbranch_vccnz .Lq_vr_%=\n" \
"v_pk_mul_f32 v[92:93], v[92:93], v[14:15] op_sel_hi:[1,0]\n" \
"v_pk_mul_f32 v[94:95], v[94:95], v[14:15] op_sel_hi:[1,0]\n" \
"v_cvt_pk_bf16_f32 v90, v92, v93\n" \
"v_cvt_pk_bf16_f32 v91, v94, v95\n" \
"v_max_u32_e64 v90, 1, v90\n" \
"buffer_store_dwordx4 v[88:91], v7, s[36:39], s58 offen sc1\n" \
".Lq_tok_%=:\n" \
"s_memrealtime s[24:25]\n" \
"v_add_u32_e64 v14, s43, v3\n" \
"ds_read_b128 v[96:99], v14 offset:24576\n" \
"ds_read_b128 v[100:103], v14 offset:25600\n" \
"ds_read_b128 v[92:95], v14 offset:26624\n" \
"s_memrealtime s[26:27]\n" \
"ds_read_b128 v[16:19], v14 offset:27648\n" \
"v_xor_b32 v0, 512, v8\n" \
"s_nop 2\n" \
"s_memrealtime s[28:29]\n" \
"s_nop 4\n" \
"s_memrealtime s[30:31]\n" \
"s_waitcnt lgkmcnt(0)\n" \
"v_mfma_f32_16x16x32_bf16 v[88:91], v[64:67], v[96:99], 0\n" \
"s_sub_u32 s13, s12, s62\n" \
"s_add_u32 s54, s49, 1\n" \
"s_min_u32 s54, s54, 63\n" \
"s_lshl_b32 s54, s54, 16\n" \
"v_mfma_f32_16x16x32_bf16 v[80:83], v[32:35], v[96:99], 0\n" \
"s_lshr_b32 s53, s54, 8\n" \
"s_and_b32 s53, s53, 0x2000\n" \
"s_xor_b32 s54, s54, s53\n" \
"buffer_load_dword v108, v0, s[36:39], s58 offen offset:0 sc1\n" \
"v_mfma_f32_16x16x32_bf16 v[88:91], v[68:71], v[100:103], v[88:91]\n" \
"s_xor_b32 s12, s54, s63\n" \
"s_add_u32 s12, s12, s62\n" \
"buffer_load_dword v110, v0, s[36:39], s58 offen offset:256 sc1\n" \
"v_mfma_f32_16x16x32_bf16 v[88:91], v[72:75], v[92:95], v[88:91]\n" \
"s_add_u32 m0, s62, 131072\n" \
"s_sub_u32 s15, s12, 114688\n" \
"buffer_load_dwordx4 v3, s[36:39], s15 offen offset:0 sc1 lds\n" \
"v_mfma_f32_16x16x32_bf16 v[80:83], v[36:39], v[100:103], v[80:83]\n" \
"buffer_load_dwordx4 v3, s[36:39], s15 offen offset:1024 sc1 lds\n" \
"v_mfma_f32_16x16x32_bf16 v[88:91], v[76:79], v[16:19], v[88:91]\n" \
"s_add_u32 s25, s24, 3\n" \
"s_cmp_eq_u32 s26, s24\n" \
"s_subb_u32 s25, s25, 0\n" \
"v_mfma_f32_16x16x32_bf16 v[80:83], v[40:43], v[92:95], v[80:83]\n" \
"s_cmp_eq_u32 s28, s24\n" \
"s_subb_u32 s25, s25, 0\n" \
"v_mfma_f32_16x16x32_bf16 v[80:83], v[44:47], v[16:19], v[80:83]\n" \
"s_cmp_eq_u32 s30, s24\n" \
"s_subb_u32 s25, s25, 0\n" \
"s_max_u32 s16, s25, 1\n" \
"v_add_u32 v14, s62, v3\n" \
"v_mfma_f32_16x16x32_bf16 v[84:87], v[48:51], v[96:99], 0\n" \
"s_sub_u32 s24, s25, s3\n" \
"s_min_u32 s24, s24, 255\n" \
"v_xor_b32 v0, 0x800, v14\n" \
"v_mfma_f32_16x16x32_bf16 v[84:87], v[52:55], v[100:103], v[84:87]\n" \
"ds_read_b128 v[32:35], v14 offset:32768\n" \
"ds_read_b128 v[36:39], v14 offset:33792\n" \
"s_cmp_eq_u32 s23, s49\n" \
"s_cselect_b32 s24, s34, s24\n" \
"v_mov_b32 v13, 0xceabdc6e\n" \
"v_mfma_f32_16x16x32_bf16 v[84:87], v[56:59], v[92:95], v[84:87]\n" \
"ds_read_b128 v[40:43], v0 offset:32768\n" \
"ds_read_b128 v[44:47], v0 offset:33792\n" \
"s_min_u32 s34, s24, s32\n" \
"s_max_u32 s25, s24, s32\n" \
"v_mfma_f32_16x16x32_bf16 v[84:87], v[60:63], v[16:19], v[84:87]\n" \
"s_min_u32 s25, s25, s33\n" \
"s_max_u32 s34, s34, s25\n" \
"s_mov_b32 s33, s32\n" \
"s_mov_b32 s32, s24\n" \
"v_cndmask_b32_e64 v80, v13, v80, s[64:65]\n" \
"v_cndmask_b32_e64 v81, v13, v81, s[66:67]\n" \
"v_cndmask_b32_e64 v82, v13, v82, s[68:69]\n" \
"v_cndmask_b32_e64 v83, v13, v83, s[70:71]\n" \
"s_waitcnt vmcnt(5)\n" \
"v_cmp_ge_u32_e64 s[96:97], s81, v30\n" \
"v_readlane_b32 s35, v30, 3\n" \
"v_readlane_b32 s53, v30, 4\n" \
"v_cndmask_b32_e64 v84, v13, v84, s[72:73]\n" \
"v_cndmask_b32_e64 v85, v13, v85, s[74:75]\n" \
"v_cndmask_b32_e64 v86, v13, v86, s[76:77]\n" \
"v_cndmask_b32_e64 v87, v13, v87, s[78:79]\n" \
"s_max_u32 s35, s35, s53\n" \
"s_cmp_eq_u32 s35, 0\n" \
"s_cselect_b32 s35, s17, s35\n" \
"s_and_b32 s96, s96, s9\n" \
"s_cbranch_scc1 .Lq_f_cold_%=\n" \
".Lq_f_ok_%=:\n" \
"s_lshr_b32 s11, s58, 1\n" \
"s_mov_b32 s54, s58\n" \
"s_mov_b32 s58, s59\n" \
"s_mov_b32 s59, s60\n" \
"s_mov_b32 s60, s61\n" \
"s_mov_b32 s61, s54\n" \
"buffer_load_dwordx2 v[64:65], v9, s[36:39], s11 offen sc1\n" \
"v_max3_f32 v13, v90, v91, v80\n" \
"v_max3_f32 v14, v81, v82, v83\n" \
"v_max3_f32 v14, v14, v84, v85\n" \
"v_max3_f32 v13, v13, v86, v87\n" \
"v_max_f32 v13, v13, v14\n" \
"v_max_f32 v14, v13, v14\n" \
"s_nop 1\n" \
"v_permlane16_swap_b32 v13, v14\n" \
"v_max_f32 v13, v13, v14\n" \
"v_max_f32 v14, v13, v14\n" \
"s_nop 1\n" \
"v_permlane32_swap_b32 v13, v14\n" \
"v_max_f32 v14, v13, v14\n" \
"v_pk_add_f32 v[80:81], v[80:81], v[14:15] op_sel_hi:[1,0] neg_lo:[0,1] neg_hi:[0,1]\n" \
"v_pk_add_f32 v[82:83], v[82:83], v[14:15] op_sel_hi:[1,0] neg_lo:[0,1] neg_hi:[0,1]\n" \
"v_pk_add_f32 v[84:85], v[84:85], v[14:15] op_sel_hi:[1,0] neg_lo:[0,1] neg_hi:[0,1]\n" \
"v_pk_add_f32 v[86:87], v[86:87], v[14:15] op_sel_hi:[1,0] neg_lo:[0,1] neg_hi:[0,1]\n" \
"v_pk_add_f32 v[90:91], v[90:91], v[14:15] op_sel_hi:[1,0] neg_lo:[0,1] neg_hi:[0,1]\n" \
"v_exp_f32 v80, v80\n" \
"v_exp_f32 v81, v81\n" \
"v_exp_f32 v82, v82\n" \
"v_exp_f32 v83, v83\n" \
"v_exp_f32 v84, v84\n" \
"v_exp_f32 v85, v85\n" \
"v_exp_f32 v86, v86\n" \
"v_exp_f32 v87, v87\n" \
"v_cvt_pk_bf16_f32 v80, v80, v81\n" \
"v_cvt_pk_bf16_f32 v81, v82, v83\n" \
"v_cvt_pk_bf16_f32 v82, v84, v85\n" \
"v_cvt_pk_bf16_f32 v83, v86, v87\n" \
"v_exp_f32 v90, v90\n" \
"s_waitcnt lgkmcnt(3)\n" \
"v_mfma_f32_16x16x32_bf16 v[48:51], v[32:35], v[80:83], 0\n" \
"v_exp_f32 v91, v91\n" \
"s_waitcnt lgkmcnt(2)\n" \
"v_mfma_f32_16x16x32_bf16 v[52:55], v[36:39], v[80:83], 0\n" \
"v_cvt_pk_bf16_f32 v26, v90, v91\n" \
"s_waitcnt lgkmcnt(1)\n" \
"v_mfma_f32_16x16x32_bf16 v[56:59], v[40:43], v[80:83], 0\n" \
"s_waitcnt lgkmcnt(0)\n" \
"v_mfma_f32_16x16x32_bf16 v[60:63], v[44:47], v[80:83], 0\n" \
"v_mfma_f32_16x16x32_bf16 v[92:95], v[20:23], v[80:83], 0\n" \
"s_waitcnt vmcnt(4)\n" \
"v_min3_u32 v13, v104, v106, v108\n" \
"s_waitcnt vmcnt(3)\n" \
"v_min3_u32 v13, v13, v110, v110\n" \
"v_cmp_eq_u32_e64 vcc, 0, v13\n" \
"s_nop 0\n" \
"s_nop 0\n" \
"s_cbranch_vccnz .Lq_v2_cold_%=\n" \
".Lq_v2_ok_%=:\n" \
"v_mfma_f32_16x16x32_bf16 v[92:95], v[20:23], v[26:29], v[92:95]\n" \
"v_mfma_f32_16x16x32_bf16 v[48:51], v[104:107], v[26:29], v[48:51]\n" \
"ds_read_b128 v[32:35], v2 offset:32768\n" \
"s_mov_b32 s95, s13\n" \
"s_add_u32 s49, s49, 1\n" \
"v_mfma_f32_16x16x32_bf16 v[52:55], v[104:107], v[24:27], v[52:55]\n" \
"ds_read_b128 v[36:39], v2 offset:33792\n" \
"s_lshr_b32 s53, s58, 1\n" \
"s_add_u32 s92, s53, s94\n" \
"v_mfma_f32_16x16x32_bf16 v[56:59], v[108:111], v[26:29], v[56:59]\n" \
"ds_read_b128 v[40:43], v2 offset:34816\n" \
"s_add_u32 s55, s53, s41\n" \
"s_xor_b32 s89, s55, 0x4800\n" \
"v_mfma_f32_16x16x32_bf16 v[60:63], v[108:111], v[24:27], v[60:63]\n" \
"ds_read_b128 v[44:47], v2 offset:35840\n" \
"s_xor_b32 s90, s55, 0x800\n" \
"s_xor_b32 s91, s55, 0x4000\n" \
"s_lshr_b32 s93, s59, 1\n" \
"s_add_u32 s93, s93, s94\n" \
"s_add_u32 s81, s49, 2\n" \
"s_min_u32 s81, s81, 63\n" \
"s_mov_b32 s14, s10\n" \
"v_rcp_f32 v14, v92\n" \
"s_nop 0\n" \
"v_pk_mul_f32 v[48:49], v[48:49], v[14:15] op_sel_hi:[1,0]\n" \
"v_pk_mul_f32 v[50:51], v[50:51], v[14:15] op_sel_hi:[1,0]\n" \
"v_pk_mul_f32 v[52:53], v[52:53], v[14:15] op_sel_hi:[1,0]\n" \
"v_pk_mul_f32 v[54:55], v[54:55], v[14:15] op_sel_hi:[1,0]\n" \
"v_cvt_pk_bf16_f32 v96, v48, v49\n" \
"v_cvt_pk_bf16_f32 v97, v50, v51\n" \
"v_cvt_pk_bf16_f32 v98, v52, v53\n" \
"v_cvt_pk_bf16_f32 v99, v54, v55\n" \
"ds_read_b128 v[48:51], v2 offset:0\n" \
"ds_read_b128 v[52:55], v2 offset:1024\n" \
"v_pk_mul_f32 v[56:57], v[56:57], v[14:15] op_sel_hi:[1,0]\n" \
"v_pk_mul_f32 v[58:59], v[58:59], v[14:15] op_sel_hi:[1,0]\n" \
"v_pk_mul_f32 v[60:61], v[60:61], v[14:15] op_sel_hi:[1,0]\n" \
"v_pk_mul_f32 v[62:63], v[62:63], v[14:15] op_sel_hi:[1,0]\n" \
"v_cvt_pk_bf16_f32 v100, v56, v57\n" \
"v_cvt_pk_bf16_f32 v101, v58, v59\n" \
"v_cvt_pk_bf16_f32 v102, v60, v61\n" \
"v_cvt_pk_bf16_f32 v103, v62, v63\n" \
"ds_read_b128 v[56:59], v2 offset:2048\n" \
"ds_read_b128 v[60:63], v2 offset:3072\n" \
"s_cmp_eq_u32 s49, 64\n" \
"s_nop 0\n" \
"s_nop 0\n" \
"s_cbranch_scc1 .Lq_end_%=\n" \
"v_mov_b64 v[80:81], 0\n" \
"v_mov_b64 v[82:83], 0\n" \
"v_mov_b64 v[84:85], 0\n" \
"v_mov_b64 v[86:87], 0\n" \
"v_mov_b64 v[88:89], 0\n" \
"v_mov_b64 v[90:91], 0\n" \
"v_mov_b64 v[92:93], 0\n" \
"v_mov_b64 v[94:95], 0\n" \
"v_mov_b32 v13, -1\n" \
"v_add_u32_e64 v14, s62, v3\n" \
"v_xor_b32 v31, 0x400, v11\n" \
"s_max_u32 s53, s49, 1\n" \
"s_sub_u32 s53, s53, 1\n" \
"v_mov_b32 v30, s53\n" \
"s_mov_b32 m0, s42\n" \
"buffer_store_dwordx4 v[80:83], v7, s[36:39], s59 offen sc1\n" \
"buffer_store_dword v24, v10, s[36:39], s93 offen sc1\n" \
"s_waitcnt lgkmcnt(0)\n" \
"s_mov_b32 s54, 0\n" \
"s_cmp_lg_u32 s48, 0\n" \
"s_cbranch_scc1 .Lq_w4_%=\n" \
"s_waitcnt vmcnt(2)\n" \
"v_cmp_eq_u32_e64 vcc, 0, v64\n" \
"s_nop 0\n" \
"s_and_b64 vcc, vcc, s[82:83]\n" \
"s_nop 0\n" \
"s_cbranch_vccnz .Lq_h_cold_%=\n" \
".Lq_h_ok_%=:\n" \
"v_subrev_u32 v64, s50, v64\n" \
"v_max_i32_dpp v65, v65, v65 row_ror:8 row_mask:0xf bank_mask:0xf\n" \
"s_nop 0\n" \
"v_max_i32_dpp v64, v64, v64 row_ror:8 row_mask:0xf bank_mask:0xf\n" \
"v_max_i32_dpp v65, v65, v65 row_ror:4 row_mask:0xf bank_mask:0xf\n" \
"s_nop 0\n" \
"v_max_i32_dpp v64, v64, v64 row_ror:4 row_mask:0xf bank_mask:0xf\n" \
"v_max_i32_dpp v65, v65, v65 row_ror:2 row_mask:0xf bank_mask:0xf\n" \
"s_nop 0\n" \
"v_max_i32_dpp v64, v64, v64 row_ror:2 row_mask:0xf bank_mask:0xf\n" \
"v_max_i32_dpp v65, v65, v65 row_ror:1 row_mask:0xf bank_mask:0xf\n" \
"s_nop 0\n" \
"v_max_i32_dpp v64, v64, v64 row_ror:1 row_mask:0xf bank_mask:0xf\n" \
"v_readlane_b32 s53, v65, 0\n" \
"v_readlane_b32 s24, v65, 16\n" \
"v_readlane_b32 s54, v64, 0\n" \
"v_readlane_b32 s40, v64, 16\n" \
"s_max_i32 s24, s24, s53\n" \
"s_max_i32 s40, s40, s54\n" \
"s_and_b32 s53, s46, 0xffff\n" \
"s_add_u32 s54, s53, 30\n" \
"s_cmp_gt_i32 s40, s54\n" \
"s_cselect_b32 s54, 0x10000, 0\n" \
"s_add_u32 s46, s46, s54\n" \
"s_sub_u32 s46, s46, s53\n" \
"s_min_i32 s53, s53, s40\n" \
"s_add_u32 s46, s46, s53\n" \
"s_cmp_lt_u32 s49, 5\n" \
"s_cselect_b32 s46, 0x7fff, s46\n" \
"s_lshr_b32 s53, s46, 17\n" \
"s_lshl_b32 s53, s53, 1\n" \
"s_min_u32 s53, s53, 4\n" \
"s_add_u32 s40, s40, s50\n" \
"s_add_u32 s40, s40, s44\n" \
"s_add_u32 s40, s40, s53\n" \
"s_add_u32 s50, s24, s40\n" \
"s_add_u32 s50, s50, s45\n" \
"s_add_u32 s50, s50, s53\n" \
"s_branch .Lq_loop_%=\n" \
".Lq_f0_cold_%=:\n" \
"s_nop 7\n" \
"s_nop 7\n" \
"s_waitcnt vmcnt(0) lgkmcnt(0)\n" \
"s_mov_b32 s53, 0\n" \
".Lq_f0_again_%=:\n" \
"s_cmp_lg_u32 s52, 0\n" \
"s_cbranch_scc1 .Lq_f0_ok_%=\n" \
"buffer_load_dword v30, v15, s[36:39], s80 offen sc1\n" \
"s_waitcnt vmcnt(0)\n" \
"v_cmp_ge_u32_e64 s[96:97], s81, v30\n" \
"s_nop 4\n" \
"s_and_b32 s96, s96, s9\n" \
"s_cbranch_scc0 .Lq_f0_ok_%=\n" \
"s_sleep 3\n" \
"s_add_u32 s53, s53, 1\n" \
"s_cmp_lt_u32 s53, 32768\n" \
"s_cbranch_scc1 .Lq_f0_again_%=\n" \
"s_mov_b32 s52, 1\n" \
"v_mov_b32 v30, 256\n" \
"v_mov_b32 v31, 1\n" \
"buffer_store_dword v31, v30, s[36:39], 0 offen sc1\n" \
"s_waitcnt vmcnt(0) lgkmcnt(0)\n" \
"s_branch .Lq_f0_ok_%=\n" \
".Lq_f1_cold_%=:\n" \
"s_nop 7\n" \
"s_nop 7\n" \
"s_waitcnt vmcnt(0) lgkmcnt(0)\n" \
"s_mov_b32 s53, 0\n" \
".Lq_f1_again_%=:\n" \
"s_cmp_lg_u32 s52, 0\n" \
"s_cbranch_scc1 .Lq_f1_ok_%=\n" \
"buffer_load_dword v30, v15, s[36:39], s80 offen sc1\n" \
"s_waitcnt vmcnt(0)\n" \
"v_cmp_ge_u32_e64 s[96:97], s81, v30\n" \
"s_nop 4\n" \
"s_and_b32 s96, s96, s9\n" \
"s_cbranch_scc0 .Lq_f1_ok_%=\n" \
"s_sleep 3\n" \
"s_add_u32 s53, s53, 1\n" \
"s_cmp_lt_u32 s53, 32768\n" \
"s_cbranch_scc1 .Lq_f1_again_%=\n" \
"s_mov_b32 s52, 1\n" \
"v_mov_b32 v30, 256\n" \
"v_mov_b32 v31, 1\n" \
"buffer_store_dword v31, v30, s[36:39], 0 offen sc1\n" \
"s_waitcnt vmcnt(0) lgkmcnt(0)\n" \
"s_branch .Lq_f1_ok_%=\n" \
".Lq_first_%=:\n" \
"s_mov_b32 s81, 1\n" \
"s_mov_b32 s96, -1\n" \
"s_and_b32 s96, s96, s9\n" \
"s_cbranch_scc1 .Lq_f1_cold_%=\n" \
".Lq_f1_ok_%=:\n" \
"s_mov_b32 s81, 2\n" \
"v_mov_b32 v30, 0\n" \
"s_branch .Lq_first2_%=\n" \
".Lq_last_%=:\n" \
"ds_read_b64_tr_b16 v[76:77], v6 offset:3072\n" \
"ds_read_b64_tr_b16 v[78:79], v6 offset:3584\n" \
"ds_read_b128 v[96:99], v14 offset:32768\n" \
"ds_read_b128 v[100:103], v14 offset:33792\n" \
"buffer_store_dword v30, v10, s[36:39], s87 offen sc1\n" \
"buffer_load_dword v30, v15, s[36:39], s80 offen sc1\n" \
"s_waitcnt vmcnt(0) lgkmcnt(0)\n" \
"s_barrier\n" \
"v_mov_b32 v13, 63\n" \
"buffer_store_dword v13, v10, s[36:39], s87 offen sc1\n" \
"s_nop 7\n" \
"s_nop 7\n" \
"s_branch .Lq_tok_%=\n" \
".Lq_vr_%=:\n" \
"s_nop 7\n" \
"s_nop 7\n" \
"s_waitcnt vmcnt(0) lgkmcnt(0)\n" \
"v_min3_u32 v13, v104, v106, v106\n" \
"v_cmp_eq_u32 vcc, 0, v13\n" \
"s_nop 0\n" \
"s_cbranch_vccnz .Lq_v_cold_%=\n" \
".Lq_v_ok_%=:\n" \
"s_mov_b32 s23, s49\n" \
"s_cmp_eq_u64 s[26:27], 0\n" \
"s_cbranch_scc1 .Lq_vr3_%=\n" \
"v_mfma_f32_16x16x32_bf16 v[88:91], v[64:67], v[64:67], 0\n" \
"v_mfma_f32_16x16x32_bf16 v[80:83], v[32:35], v[64:67], 0\n" \
"v_mfma_f32_16x16x32_bf16 v[88:91], v[68:71], v[68:71], v[88:91]\n" \
"v_mfma_f32_16x16x32_bf16 v[88:91], v[72:75], v[72:75], v[88:91]\n" \
"v_mfma_f32_16x16x32_bf16 v[80:83], v[36:39], v[68:71], v[80:83]\n" \
"v_mfma_f32_16x16x32_bf16 v[88:91], v[76:79], v[76:79], v[88:91]\n" \
"v_mfma_f32_16x16x32_bf16 v[80:83], v[40:43], v[72:75], v[80:83]\n" \
"v_mfma_f32_16x16x32_bf16 v[80:83], v[44:47], v[76:79], v[80:83]\n" \
"v_mfma_f32_16x16x32_bf16 v[84:87], v[48:51], v[64:67], 0\n" \
"v_mfma_f32_16x16x32_bf16 v[84:87], v[52:55], v[68:71], v[84:87]\n" \
"v_mfma_f32_16x16x32_bf16 v[84:87], v[56:59], v[72:75], v[84:87]\n" \
"v_max3_f32 v13, v90, v91, v80\n" \
"v_mfma_f32_16x16x32_bf16 v[84:87], v[60:63], v[76:79], v[84:87]\n" \
"v_max3_f32 v14, v81, v82, v83\n" \
"s_nop 6\n" \
"v_max3_f32 v14, v14, v84, v85\n" \
"v_max3_f32 v13, v13, v86, v87\n" \
"v_max_f32 v13, v13, v14\n" \
"v_max_f32 v14, v13, v14\n" \
"s_nop 1\n" \
"v_permlane16_swap_b32 v13, v14\n" \
"v_max_f32 v13, v13, v14\n" \
"v_max_f32 v14, v13, v14\n" \
"s_nop 1\n" \
"v_permlane32_swap_b32 v13, v14\n" \
"v_max_f32 v14, v13, v14\n" \
"v_pk_add_f32 v[80:81], v[80:81], v[14:15] op_sel_hi:[1,0] neg_lo:[0,1] neg_hi:[0,1]\n" \
"v_pk_add_f32 v[82:83], v[82:83], v[14:15] op_sel_hi:[1,0] neg_lo:[0,1] neg_hi:[0,1]\n" \
"v_pk_add_f32 v[84:85], v[84:85], v[14:15] op_sel_hi:[1,0] neg_lo:[0,1] neg_hi:[0,1]\n" \
"v_pk_add_f32 v[86:87], v[86:87], v[14:15] op_sel_hi:[1,0] neg_lo:[0,1] neg_hi:[0,1]\n" \
"v_pk_add_f32 v[90:91], v[90:91], v[14:15] op_sel_hi:[1,0] neg_lo:[0,1] neg_hi:[0,1]\n" \
"v_exp_f32 v80, v80\n" \
"v_exp_f32 v81, v81\n" \
"v_exp_f32 v82, v82\n" \
"v_exp_f32 v83, v83\n" \
"v_exp_f32 v84, v84\n" \
"v_exp_f32 v85, v85\n" \
"v_exp_f32 v86, v86\n" \
"v_exp_f32 v87, v87\n" \
"v_cvt_pk_bf16_f32 v80, v80, v81\n" \
"v_cvt_pk_bf16_f32 v81, v82, v83\n" \
"v_cvt_pk_bf16_f32 v82, v84, v85\n" \
"v_cvt_pk_bf16_f32 v83, v86, v87\n" \
"v_exp_f32 v90, v90\n" \
"s_nop 0\n" \
"v_mfma_f32_16x16x32_bf16 v[84:87], v[96:99], v[80:83], 0\n" \
"v_exp_f32 v91, v91\n" \
"v_mfma_f32_16x16x32_bf16 v[92:95], v[100:103], v[80:83], 0\n" \
"v_cvt_pk_bf16_f32 v26, v90, v91\n" \
"v_mfma_f32_16x16x32_bf16 v[16:19], v[20:23], v[80:83], 0\n" \
".Lq_vr3_%=:\n" \
"s_nop 7\n" \
"s_nop 7\n" \
"v_mfma_f32_16x16x32_bf16 v[84:87], v[96:99], v[80:83], 0\n" \
"v_mfma_f32_16x16x32_bf16 v[92:95], v[100:103], v[80:83], 0\n" \
"v_mfma_f32_16x16x32_bf16 v[16:19], v[20:23], v[80:83], 0\n" \
"v_mfma_f32_16x16x32_bf16 v[16:19], v[20:23], v[26:29], v[16:19]\n" \
"v_mfma_f32_16x16x32_bf16 v[84:87], v[104:107], v[26:29], v[84:87]\n" \
"v_mfma_f32_16x16x32_bf16 v[92:95], v[104:107], v[24:27], v[92:95]\n" \
"v_rcp_f32 v14, v16\n" \
"s_mov_b64 vcc, 0\n" \
"s_mov_b64 s[26:27], 0\n" \
"s_nop 7\n" \
"s_nop 7\n" \
"s_branch .Lq_vr2_%=\n" \
".Lq_v_cold_%=:\n" \
"s_nop 7\n" \
"s_nop 7\n" \
"s_waitcnt vmcnt(0) lgkmcnt(0)\n" \
"s_mov_b32 s53, 0\n" \
".Lq_v_again_%=:\n" \
"s_cmp_lg_u32 s52, 0\n" \
"s_cbranch_scc1 .Lq_v_ok_%=\n" \
"buffer_load_dword v104, v8, s[36:39], s58 offen offset:0 sc1\n" \
"buffer_load_dword v106, v8, s[36:39], s58 offen offset:256 sc1\n" \
"s_waitcnt vmcnt(0)\n" \
"v_min3_u32 v13, v104, v106, v106\n" \
"v_cmp_eq_u32 vcc, 0, v13\n" \
"s_nop 4\n" \
"s_cbranch_vccz .Lq_v_ok_%=\n" \
"s_add_u32 s53, s53, 1\n" \
"s_cmp_lt_u32 s53, 32768\n" \
"s_cbranch_scc1 .Lq_v_again_%=\n" \
"s_mov_b32 s52, 1\n" \
"v_mov_b32 v13, 256\n" \
"v_mov_b32 v14, 1\n" \
"buffer_store_dword v14, v13, s[36:39], 0 offen sc1\n" \
"s_waitcnt vmcnt(0) lgkmcnt(0)\n" \
"s_branch .Lq_v_ok_%=\n" \
".Lq_f_cold_%=:\n" \
"s_nop 7\n" \
"s_nop 7\n" \
"s_waitcnt vmcnt(0) lgkmcnt(0)\n" \
"s_mov_b32 s53, 0\n" \
".Lq_f_again_%=:\n" \
"s_cmp_lg_u32 s52, 0\n" \
"s_cbranch_scc1 .Lq_f_ok_%=\n" \
"buffer_load_dword v30, v15, s[36:39], s80 offen sc1\n" \
"s_waitcnt vmcnt(0)\n" \
"v_cmp_ge_u32_e64 s[96:97], s81, v30\n" \
"s_nop 4\n" \
"s_and_b32 s96, s96, s9\n" \
"s_cbranch_scc0 .Lq_f_ok_%=\n" \
"s_sleep 3\n" \
"s_add_u32 s53, s53, 1\n" \
"s_cmp_lt_u32 s53, 32768\n" \
"s_cbranch_scc1 .Lq_f_again_%=\n" \
"s_mov_b32 s52, 1\n" \
"v_mov_b32 v30, 256\n" \
"v_mov_b32 v31, 1\n" \
"buffer_store_dword v31, v30, s[36:39], 0 offen sc1\n" \
"s_waitcnt vmcnt(0) lgkmcnt(0)\n" \
"s_branch .Lq_f_ok_%=\n" \
".Lq_v2_cold_%=:\n" \
"s_nop 7\n" \
"s_nop 7\n" \
"s_waitcnt vmcnt(0) lgkmcnt(0)\n" \
"s_mov_b32 s53, 0\n" \
".Lq_v2_again_%=:\n" \
"s_cmp_lg_u32 s52, 0\n" \
"s_cbranch_scc1 .Lq_v2_ok_%=\n" \
"s_sleep 1\n" \
"v_xor_b32 v31, 512, v8\n" \
"buffer_load_dword v104, v8, s[36:39], s61 offen offset:0 sc1\n" \
"buffer_load_dword v106, v8, s[36:39], s61 offen offset:256 sc1\n" \
"buffer_load_dword v108, v31, s[36:39], s61 offen offset:0 sc1\n" \
"buffer_load_dword v110, v31, s[36:39], s61 offen offset:256 sc1\n" \
"s_waitcnt vmcnt(1)\n" \
"v_min3_u32 v13, v104, v106, v108\n" \
"s_waitcnt vmcnt(0)\n" \
"v_min3_u32 v13, v13, v110, v110\n" \
"v_cmp_eq_u32 vcc, 0, v13\n" \
"s_nop 4\n" \
"s_cbranch_vccz .Lq_v2_ok_%=\n" \
"s_add_u32 s53, s53, 1\n" \
"s_cmp_lt_u32 s53, 32768\n" \
"s_cbranch_scc1 .Lq_v2_again_%=\n" \
"s_mov_b32 s52, 1\n" \
"v_mov_b32 v13, 256\n" \
"v_mov_b32 v14, 1\n" \
"buffer_store_dword v14, v13, s[36:39], 0 offen sc1\n" \
"s_waitcnt vmcnt(0) lgkmcnt(0)\n" \
"s_branch .Lq_v2_ok_%=\n" \
".Lq_h_cold_%=:\n" \
"s_nop 7\n" \
"s_nop 7\n" \
"s_waitcnt vmcnt(0) lgkmcnt(0)\n" \
"s_mov_b32 s53, 0\n" \
".Lq_h_again_%=:\n" \
"s_cmp_lg_u32 s52, 0\n" \
"s_cbranch_scc1 .Lq_h_ok_%=\n" \
"s_sleep 1\n" \
"buffer_load_dwordx2 v[64:65], v9, s[36:39], s11 offen sc1\n" \
"s_waitcnt vmcnt(0)\n" \
"v_cmp_eq_u32 vcc, 0, v64\n" \
"s_nop 0\n" \
"s_and_b64 vcc, vcc, s[82:83]\n" \
"s_nop 4\n" \
"s_cbranch_vccz .Lq_h_ok_%=\n" \
"s_add_u32 s53, s53, 1\n" \
"s_cmp_lt_u32 s53, 32768\n" \
"s_cbranch_scc1 .Lq_h_again_%=\n" \
"s_mov_b32 s52, 1\n" \
"v_mov_b32 v18, 256\n" \
"v_mov_b32 v19, 1\n" \
"buffer_store_dword v19, v18, s[36:39], 0 offen sc1\n" \
"s_waitcnt vmcnt(0) lgkmcnt(0)\n" \
"s_branch .Lq_h_ok_%=\n" \
".Lq_retry_%=:\n" \
"s_nop 7\n" \
"s_nop 7\n" \
"s_waitcnt vmcnt(0) lgkmcnt(0)\n" \
"ds_read_b128 v[32:35], v2 offset:32768\n" \
"ds_read_b128 v[36:39], v2 offset:33792\n" \
"ds_read_b128 v[40:43], v2 offset:34816\n" \
"ds_read_b128 v[44:47], v2 offset:35840\n" \
"ds_read_b128 v[48:51], v2 offset:0\n" \
"ds_read_b128 v[52:55], v2 offset:1024\n" \
"ds_read_b128 v[56:59], v2 offset:2048\n" \
"ds_read_b128 v[60:63], v2 offset:3072\n" \
"v_mov_b64 v[80:81], 0\n" \
"v_mov_b64 v[82:83], 0\n" \
"v_mov_b64 v[84:85], 0\n" \
"v_mov_b64 v[86:87], 0\n" \
"v_mov_b64 v[88:89], 0\n" \
"v_mov_b64 v[90:91], 0\n" \
"v_mov_b64 v[92:93], 0\n" \
"v_mov_b64 v[94:95], 0\n" \
"v_mov_b32 v13, -1\n" \
"v_add_u32 v14, s62, v3\n" \
"v_xor_b32 v31, 0x400, v11\n" \
"s_max_u32 s53, s49, 1\n" \
"s_sub_u32 s53, s53, 1\n" \
"v_mov_b32 v30, s53\n" \
"s_mov_b32 m0, s42\n" \
"s_memrealtime s[56:57]\n" \
"s_mov_b32 s54, 100\n" \
"s_waitcnt vmcnt(0) lgkmcnt(0)\n" \
"s_barrier\n" \
"s_add_u32 s88, s88, 1\n" \
"s_cmp_lt_u32 s88, 32768\n" \
"s_cbranch_scc1 .Lq_r1_%=\n" \
"s_mov_b32 s52, 1\n" \
"v_mov_b32 v13, 256\n" \
"v_mov_b32 v14, 1\n" \
"buffer_store_dword v14, v13, s[36:39], 0 offen sc1\n" \
"s_waitcnt vmcnt(0) lgkmcnt(0)\n" \
"s_endpgm\n" \
".Lq_r1_%=:\n" \
"s_cmp_lg_u32 s49, 0\n" \
"s_cbranch_scc1 .Lq_r2_%=\n" \
"s_sleep 8\n" \
".Lq_r2_%=:\n" \
"s_branch .Lq_go_%=\n" \
".Lq_end_%=:\n" \
"s_nop 7\n" \
"s_nop 7\n" \
"v_xor_b32 v31, 0x400, v11\n" \
"buffer_store_dwordx4 v[96:99], v11, s[36:39], s95 offen sc1\n" \
"buffer_store_dwordx4 v[100:103], v31, s[36:39], s95 offen sc1\n" \
"s_waitcnt vmcnt(0) lgkmcnt(0)\n" \
"s_barrier\n" \
"v_mov_b32 v31, 64\n" \
"buffer_store_dword v31, v10, s[36:39], s87 offen sc1\n" \
"s_waitcnt vmcnt(0) lgkmcnt(0)\n" \
".Lq_fin_%=:\n" \
"s_endpgm\n"
#define ASM_VO \
"s_nop 7\n" \
"s_nop 7\n" \
"buffer_load_dword v20, v13, s[68:71], 0 offen offset:0\n" \
"buffer_load_dword v21, v13, s[68:71], 0 offen offset:8\n" \
"buffer_load_dword v22, v13, s[68:71], 0 offen offset:16\n" \
"buffer_load_dword v23, v13, s[68:71], 0 offen offset:24\n" \
"v_mov_b32 v16, 0\n" \
"v_mov_b32 v17, 0\n" \
"v_mov_b32 v18, 0\n" \
"v_mov_b32 v19, 0\n" \
"s_mov_b32 s54, 0\n" \
"buffer_store_dword v16, v7, s[36:39], s54 offen sc1\n" \
"s_mov_b32 s54, 131072\n" \
"buffer_store_dword v16, v7, s[36:39], s54 offen sc1\n" \
"s_mov_b32 s54, 65536\n" \
"buffer_store_dwordx4 v[16:19], v8, s[36:39], s54 offen sc1\n" \
"s_mov_b32 s54, 262144\n" \
"buffer_store_dword v16, v7, s[36:39], s54 offen sc1\n" \
"s_mov_b32 s54, 131072\n" \
"buffer_store_dwordx4 v[16:19], v8, s[36:39], s54 offen sc1\n" \
"s_mov_b32 s54, 393216\n" \
"buffer_store_dword v16, v7, s[36:39], s54 offen sc1\n" \
"s_mov_b32 s54, 196608\n" \
"buffer_store_dwordx4 v[16:19], v8, s[36:39], s54 offen sc1\n" \
"s_mul_i32 s62, s48, 40960\n" \
"v_add_u32 v32, 40960, v3\n" \
"v_add_u32 v33, 81920, v3\n" \
"v_add_u32 v34, 122880, v3\n" \
"s_add_u32 m0, s62, 0\n" \
"s_mov_b32 s54, 0\n" \
"buffer_load_dwordx4 v1, s[40:43], s54 offen nt lds\n" \
"s_add_u32 m0, s62, 1024\n" \
"s_mov_b32 s54, 65536\n" \
"buffer_load_dwordx4 v1, s[40:43], s54 offen nt lds\n" \
"s_add_u32 m0, s62, 2048\n" \
"s_mov_b32 s54, 131072\n" \
"buffer_load_dwordx4 v1, s[40:43], s54 offen nt lds\n" \
"s_add_u32 m0, s62, 3072\n" \
"s_mov_b32 s54, 196608\n" \
"buffer_load_dwordx4 v1, s[40:43], s54 offen nt lds\n" \
"s_add_u32 m0, s62, 4096\n" \
"s_mov_b32 s54, 262144\n" \
"buffer_load_dwordx4 v1, s[40:43], s54 offen nt lds\n" \
"s_add_u32 m0, s62, 5120\n" \
"s_mov_b32 s54, 327680\n" \
"buffer_load_dwordx4 v1, s[40:43], s54 offen nt lds\n" \
"s_add_u32 m0, s62, 6144\n" \
"s_mov_b32 s54, 393216\n" \
"buffer_load_dwordx4 v1, s[40:43], s54 offen nt lds\n" \
"s_add_u32 m0, s62, 7168\n" \
"s_mov_b32 s54, 458752\n" \
"buffer_load_dwordx4 v1, s[40:43], s54 offen nt lds\n" \
"s_add_u32 m0, s62, 8192\n" \
"s_mov_b32 s54, 524288\n" \
"buffer_load_dwordx4 v1, s[40:43], s54 offen nt lds\n" \
"s_add_u32 m0, s62, 9216\n" \
"s_mov_b32 s54, 589824\n" \
"buffer_load_dwordx4 v1, s[40:43], s54 offen nt lds\n" \
"s_add_u32 m0, s62, 10240\n" \
"s_mov_b32 s54, 655360\n" \
"buffer_load_dwordx4 v1, s[40:43], s54 offen nt lds\n" \
"s_add_u32 m0, s62, 11264\n" \
"s_mov_b32 s54, 720896\n" \
"buffer_load_dwordx4 v1, s[40:43], s54 offen nt lds\n" \
"s_add_u32 m0, s62, 12288\n" \
"s_mov_b32 s54, 786432\n" \
"buffer_load_dwordx4 v1, s[40:43], s54 offen nt lds\n" \
"s_add_u32 m0, s62, 13312\n" \
"s_mov_b32 s54, 851968\n" \
"buffer_load_dwordx4 v1, s[40:43], s54 offen nt lds\n" \
"s_add_u32 m0, s62, 14336\n" \
"s_mov_b32 s54, 917504\n" \
"buffer_load_dwordx4 v1, s[40:43], s54 offen nt lds\n" \
"s_add_u32 m0, s62, 15360\n" \
"s_mov_b32 s54, 983040\n" \
"buffer_load_dwordx4 v1, s[40:43], s54 offen nt lds\n" \
"s_add_u32 m0, s62, 16384\n" \
"s_mov_b32 s54, 1048576\n" \
"buffer_load_dwordx4 v1, s[40:43], s54 offen nt lds\n" \
"s_add_u32 m0, s62, 17408\n" \
"s_mov_b32 s54, 1114112\n" \
"buffer_load_dwordx4 v1, s[40:43], s54 offen nt lds\n" \
"s_add_u32 m0, s62, 18432\n" \
"s_mov_b32 s54, 1179648\n" \
"buffer_load_dwordx4 v1, s[40:43], s54 offen nt lds\n" \
"s_add_u32 m0, s62, 19456\n" \
"s_mov_b32 s54, 1245184\n" \
"buffer_load_dwordx4 v1, s[40:43], s54 offen nt lds\n" \
"s_add_u32 m0, s62, 20480\n" \
"s_mov_b32 s54, 1310720\n" \
"buffer_load_dwordx4 v1, s[40:43], s54 offen nt lds\n" \
"s_add_u32 m0, s62, 21504\n" \
"s_mov_b32 s54, 1376256\n" \
"buffer_load_dwordx4 v1, s[40:43], s54 offen nt lds\n" \
"s_add_u32 m0, s62, 22528\n" \
"s_mov_b32 s54, 1441792\n" \
"buffer_load_dwordx4 v1, s[40:43], s54 offen nt lds\n" \
"s_add_u32 m0, s62, 23552\n" \
"s_mov_b32 s54, 1507328\n" \
"buffer_load_dwordx4 v1, s[40:43], s54 offen nt lds\n" \
"s_add_u32 m0, s62, 24576\n" \
"s_mov_b32 s54, 1572864\n" \
"buffer_load_dwordx4 v1, s[40:43], s54 offen nt lds\n" \
"s_add_u32 m0, s62, 25600\n" \
"s_mov_b32 s54, 1638400\n" \
"buffer_load_dwordx4 v1, s[40:43], s54 offen nt lds\n" \
"s_add_u32 m0, s62, 26624\n" \
"s_mov_b32 s54, 1703936\n" \
"buffer_load_dwordx4 v1, s[40:43], s54 offen nt lds\n" \
"s_add_u32 m0, s62, 27648\n" \
"s_mov_b32 s54, 1769472\n" \
"buffer_load_dwordx4 v1, s[40:43], s54 offen nt lds\n" \
"s_add_u32 m0, s62, 28672\n" \
"s_mov_b32 s54, 1835008\n" \
"buffer_load_dwordx4 v1, s[40:43], s54 offen nt lds\n" \
"s_add_u32 m0, s62, 29696\n" \
"s_mov_b32 s54, 1900544\n" \
"buffer_load_dwordx4 v1, s[40:43], s54 offen nt lds\n" \
"s_add_u32 m0, s62, 30720\n" \
"s_mov_b32 s54, 1966080\n" \
"buffer_load_dwordx4 v1, s[40:43], s54 offen nt lds\n" \
"s_add_u32 m0, s62, 31744\n" \
"s_mov_b32 s54, 2031616\n" \
"buffer_load_dwordx4 v1, s[40:43], s54 offen nt lds\n" \
"s_add_u32 m0, s62, 32768\n" \
"s_mov_b32 s54, 0\n" \
"buffer_load_dwordx4 v2, s[64:67], s54 offen lds\n" \
"s_add_u32 m0, s62, 33792\n" \
"s_mov_b32 s54, 32768\n" \
"buffer_load_dwordx4 v2, s[64:67], s54 offen lds\n" \
"s_add_u32 m0, s62, 34816\n" \
"s_mov_b32 s54, 65536\n" \
"buffer_load_dwordx4 v2, s[64:67], s54 offen lds\n" \
"s_add_u32 m0, s62, 35840\n" \
"s_mov_b32 s54, 98304\n" \
"buffer_load_dwordx4 v2, s[64:67], s54 offen lds\n" \
"s_add_u32 m0, s62, 36864\n" \
"s_mov_b32 s54, 131072\n" \
"buffer_load_dwordx4 v2, s[64:67], s54 offen lds\n" \
"s_add_u32 m0, s62, 37888\n" \
"s_mov_b32 s54, 163840\n" \
"buffer_load_dwordx4 v2, s[64:67], s54 offen lds\n" \
"s_add_u32 m0, s62, 38912\n" \
"s_mov_b32 s54, 196608\n" \
"buffer_load_dwordx4 v2, s[64:67], s54 offen lds\n" \
"s_add_u32 m0, s62, 39936\n" \
"s_mov_b32 s54, 229376\n" \
"buffer_load_dwordx4 v2, s[64:67], s54 offen lds\n" \
"s_waitcnt vmcnt(32)\n" \
"s_barrier\n" \
"ds_read_b64_tr_b16 v[128:129], v3 offset:0\n" \
"ds_read_b64_tr_b16 v[130:131], v3 offset:512\n" \
"ds_read_b64_tr_b16 v[132:133], v3 offset:4096\n" \
"ds_read_b64_tr_b16 v[134:135], v3 offset:4608\n" \
"ds_read_b64_tr_b16 v[160:161], v32 offset:0\n" \
"ds_read_b64_tr_b16 v[162:163], v32 offset:512\n" \
"ds_read_b64_tr_b16 v[164:165], v32 offset:4096\n" \
"ds_read_b64_tr_b16 v[166:167], v32 offset:4608\n" \
"ds_read_b64_tr_b16 v[192:193], v33 offset:0\n" \
"ds_read_b64_tr_b16 v[194:195], v33 offset:512\n" \
"ds_read_b64_tr_b16 v[196:197], v33 offset:4096\n" \
"ds_read_b64_tr_b16 v[198:199], v33 offset:4608\n" \
"ds_read_b64_tr_b16 v[224:225], v34 offset:0\n" \
"ds_read_b64_tr_b16 v[226:227], v34 offset:512\n" \
"ds_read_b64_tr_b16 v[228:229], v34 offset:4096\n" \
"ds_read_b64_tr_b16 v[230:231], v34 offset:4608\n" \
"s_waitcnt lgkmcnt(0)\n" \
"s_barrier\n" \
"s_add_u32 m0, s62, 0\n" \
"s_mov_b32 s54, 262144\n" \
"buffer_load_dwordx4 v2, s[64:67], s54 offen lds\n" \
"s_add_u32 m0, s62, 1024\n" \
"s_mov_b32 s54, 294912\n" \
"buffer_load_dwordx4 v2, s[64:67], s54 offen lds\n" \
"s_add_u32 m0, s62, 2048\n" \
"s_mov_b32 s54, 327680\n" \
"buffer_load_dwordx4 v2, s[64:67], s54 offen lds\n" \
"s_add_u32 m0, s62, 3072\n" \
"s_mov_b32 s54, 360448\n" \
"buffer_load_dwordx4 v2, s[64:67], s54 offen lds\n" \
"s_add_u32 m0, s62, 4096\n" \
"s_mov_b32 s54, 393216\n" \
"buffer_load_dwordx4 v2, s[64:67], s54 offen lds\n" \
"s_add_u32 m0, s62, 5120\n" \
"s_mov_b32 s54, 425984\n" \
"buffer_load_dwordx4 v2, s[64:67], s54 offen lds\n" \
"s_add_u32 m0, s62, 6144\n" \
"s_mov_b32 s54, 458752\n" \
"buffer_load_dwordx4 v2, s[64:67], s54 offen lds\n" \
"s_add_u32 m0, s62, 7168\n" \
"s_mov_b32 s54, 491520\n" \
"buffer_load_dwordx4 v2, s[64:67], s54 offen lds\n" \
"s_waitcnt vmcnt(32)\n" \
"s_barrier\n" \
"ds_read_b64_tr_b16 v[136:137], v3 offset:8192\n" \
"ds_read_b64_tr_b16 v[138:139], v3 offset:8704\n" \
"ds_read_b64_tr_b16 v[140:141], v3 offset:12288\n" \
"ds_read_b64_tr_b16 v[142:143], v3 offset:12800\n" \
"ds_read_b64_tr_b16 v[168:169], v32 offset:8192\n" \
"ds_read_b64_tr_b16 v[170:171], v32 offset:8704\n" \
"ds_read_b64_tr_b16 v[172:173], v32 offset:12288\n" \
"ds_read_b64_tr_b16 v[174:175], v32 offset:12800\n" \
"ds_read_b64_tr_b16 v[200:201], v33 offset:8192\n" \
"ds_read_b64_tr_b16 v[202:203], v33 offset:8704\n" \
"ds_read_b64_tr_b16 v[204:205], v33 offset:12288\n" \
"ds_read_b64_tr_b16 v[206:207], v33 offset:12800\n" \
"ds_read_b64_tr_b16 v[232:233], v34 offset:8192\n" \
"ds_read_b64_tr_b16 v[234:235], v34 offset:8704\n" \
"ds_read_b64_tr_b16 v[236:237], v34 offset:12288\n" \
"ds_read_b64_tr_b16 v[238:239], v34 offset:12800\n" \
"s_waitcnt lgkmcnt(0)\n" \
"s_barrier\n" \
"s_add_u32 m0, s62, 8192\n" \
"s_mov_b32 s54, 524288\n" \
"buffer_load_dwordx4 v2, s[64:67], s54 offen lds\n" \
"s_add_u32 m0, s62, 9216\n" \
"s_mov_b32 s54, 557056\n" \
"buffer_load_dwordx4 v2, s[64:67], s54 offen lds\n" \
"s_add_u32 m0, s62, 10240\n" \
"s_mov_b32 s54, 589824\n" \
"buffer_load_dwordx4 v2, s[64:67], s54 offen lds\n" \
"s_add_u32 m0, s62, 11264\n" \
"s_mov_b32 s54, 622592\n" \
"buffer_load_dwordx4 v2, s[64:67], s54 offen lds\n" \
"s_add_u32 m0, s62, 12288\n" \
"s_mov_b32 s54, 655360\n" \
"buffer_load_dwordx4 v2, s[64:67], s54 offen lds\n" \
"s_add_u32 m0, s62, 13312\n" \
"s_mov_b32 s54, 688128\n" \
"buffer_load_dwordx4 v2, s[64:67], s54 offen lds\n" \
"s_add_u32 m0, s62, 14336\n" \
"s_mov_b32 s54, 720896\n" \
"buffer_load_dwordx4 v2, s[64:67], s54 offen lds\n" \
"s_add_u32 m0, s62, 15360\n" \
"s_mov_b32 s54, 753664\n" \
"buffer_load_dwordx4 v2, s[64:67], s54 offen lds\n" \
"s_waitcnt vmcnt(32)\n" \
"s_barrier\n" \
"ds_read_b64_tr_b16 v[144:145], v3 offset:16384\n" \
"ds_read_b64_tr_b16 v[146:147], v3 offset:16896\n" \
"ds_read_b64_tr_b16 v[148:149], v3 offset:20480\n" \
"ds_read_b64_tr_b16 v[150:151], v3 offset:20992\n" \
"ds_read_b64_tr_b16 v[176:177], v32 offset:16384\n" \
"ds_read_b64_tr_b16 v[178:179], v32 offset:16896\n" \
"ds_read_b64_tr_b16 v[180:181], v32 offset:20480\n" \
"ds_read_b64_tr_b16 v[182:183], v32 offset:20992\n" \
"ds_read_b64_tr_b16 v[208:209], v33 offset:16384\n" \
"ds_read_b64_tr_b16 v[210:211], v33 offset:16896\n" \
"ds_read_b64_tr_b16 v[212:213], v33 offset:20480\n" \
"ds_read_b64_tr_b16 v[214:215], v33 offset:20992\n" \
"ds_read_b64_tr_b16 v[240:241], v34 offset:16384\n" \
"ds_read_b64_tr_b16 v[242:243], v34 offset:16896\n" \
"ds_read_b64_tr_b16 v[244:245], v34 offset:20480\n" \
"ds_read_b64_tr_b16 v[246:247], v34 offset:20992\n" \
"s_waitcnt lgkmcnt(0)\n" \
"s_barrier\n" \
"s_add_u32 m0, s62, 16384\n" \
"s_mov_b32 s54, 786432\n" \
"buffer_load_dwordx4 v2, s[64:67], s54 offen lds\n" \
"s_add_u32 m0, s62, 17408\n" \
"s_mov_b32 s54, 819200\n" \
"buffer_load_dwordx4 v2, s[64:67], s54 offen lds\n" \
"s_add_u32 m0, s62, 18432\n" \
"s_mov_b32 s54, 851968\n" \
"buffer_load_dwordx4 v2, s[64:67], s54 offen lds\n" \
"s_add_u32 m0, s62, 19456\n" \
"s_mov_b32 s54, 884736\n" \
"buffer_load_dwordx4 v2, s[64:67], s54 offen lds\n" \
"s_add_u32 m0, s62, 20480\n" \
"s_mov_b32 s54, 917504\n" \
"buffer_load_dwordx4 v2, s[64:67], s54 offen lds\n" \
"s_add_u32 m0, s62, 21504\n" \
"s_mov_b32 s54, 950272\n" \
"buffer_load_dwordx4 v2, s[64:67], s54 offen lds\n" \
"s_add_u32 m0, s62, 22528\n" \
"s_mov_b32 s54, 983040\n" \
"buffer_load_dwordx4 v2, s[64:67], s54 offen lds\n" \
"s_add_u32 m0, s62, 23552\n" \
"s_mov_b32 s54, 1015808\n" \
"buffer_load_dwordx4 v2, s[64:67], s54 offen lds\n" \
"s_waitcnt vmcnt(32)\n" \
"s_barrier\n" \
"ds_read_b64_tr_b16 v[152:153], v3 offset:24576\n" \
"ds_read_b64_tr_b16 v[154:155], v3 offset:25088\n" \
"ds_read_b64_tr_b16 v[156:157], v3 offset:28672\n" \
"ds_read_b64_tr_b16 v[158:159], v3 offset:29184\n" \
"ds_read_b64_tr_b16 v[184:185], v32 offset:24576\n" \
"ds_read_b64_tr_b16 v[186:187], v32 offset:25088\n" \
"ds_read_b64_tr_b16 v[188:189], v32 offset:28672\n" \
"ds_read_b64_tr_b16 v[190:191], v32 offset:29184\n" \
"ds_read_b64_tr_b16 v[216:217], v33 offset:24576\n" \
"ds_read_b64_tr_b16 v[218:219], v33 offset:25088\n" \
"ds_read_b64_tr_b16 v[220:221], v33 offset:28672\n" \
"ds_read_b64_tr_b16 v[222:223], v33 offset:29184\n" \
"ds_read_b64_tr_b16 v[248:249], v34 offset:24576\n" \
"ds_read_b64_tr_b16 v[250:251], v34 offset:25088\n" \
"ds_read_b64_tr_b16 v[252:253], v34 offset:28672\n" \
"ds_read_b64_tr_b16 v[254:255], v34 offset:29184\n" \
"s_waitcnt lgkmcnt(0)\n" \
"s_barrier\n" \
"s_add_u32 m0, s62, 24576\n" \
"s_mov_b32 s54, 1048576\n" \
"buffer_load_dwordx4 v2, s[64:67], s54 offen lds\n" \
"s_add_u32 m0, s62, 25600\n" \
"s_mov_b32 s54, 1081344\n" \
"buffer_load_dwordx4 v2, s[64:67], s54 offen lds\n" \
"s_add_u32 m0, s62, 26624\n" \
"s_mov_b32 s54, 1114112\n" \
"buffer_load_dwordx4 v2, s[64:67], s54 offen lds\n" \
"s_add_u32 m0, s62, 27648\n" \
"s_mov_b32 s54, 1146880\n" \
"buffer_load_dwordx4 v2, s[64:67], s54 offen lds\n" \
"s_add_u32 m0, s62, 28672\n" \
"s_mov_b32 s54, 1179648\n" \
"buffer_load_dwordx4 v2, s[64:67], s54 offen lds\n" \
"s_add_u32 m0, s62, 29696\n" \
"s_mov_b32 s54, 1212416\n" \
"buffer_load_dwordx4 v2, s[64:67], s54 offen lds\n" \
"s_add_u32 m0, s62, 30720\n" \
"s_mov_b32 s54, 1245184\n" \
"buffer_load_dwordx4 v2, s[64:67], s54 offen lds\n" \
"s_add_u32 m0, s62, 31744\n" \
"s_mov_b32 s54, 1277952\n" \
"buffer_load_dwordx4 v2, s[64:67], s54 offen lds\n" \
"s_waitcnt vmcnt(36)\n" \
"ds_read_b64_tr_b16 a[0:1], v4 offset:32768\n" \
"ds_read_b64_tr_b16 a[2:3], v4 offset:32896\n" \
"ds_read_b64_tr_b16 a[4:5], v4 offset:34816\n" \
"ds_read_b64_tr_b16 a[6:7], v4 offset:34944\n" \
"ds_read_b64_tr_b16 a[8:9], v4 offset:32800\n" \
"ds_read_b64_tr_b16 a[10:11], v4 offset:32928\n" \
"ds_read_b64_tr_b16 a[12:13], v4 offset:34848\n" \
"ds_read_b64_tr_b16 a[14:15], v4 offset:34976\n" \
"s_waitcnt lgkmcnt(0)\n" \
"s_add_u32 m0, s62, 32768\n" \
"s_mov_b32 s54, 1310720\n" \
"buffer_load_dwordx4 v2, s[64:67], s54 offen lds\n" \
"s_add_u32 m0, s62, 33792\n" \
"s_mov_b32 s54, 1343488\n" \
"buffer_load_dwordx4 v2, s[64:67], s54 offen lds\n" \
"s_add_u32 m0, s62, 34816\n" \
"s_mov_b32 s54, 1376256\n" \
"buffer_load_dwordx4 v2, s[64:67], s54 offen lds\n" \
"s_add_u32 m0, s62, 35840\n" \
"s_mov_b32 s54, 1409024\n" \
"buffer_load_dwordx4 v2, s[64:67], s54 offen lds\n" \
"s_waitcnt vmcnt(36)\n" \
"ds_read_b64_tr_b16 a[16:17], v4 offset:36864\n" \
"ds_read_b64_tr_b16 a[18:19], v4 offset:36992\n" \
"ds_read_b64_tr_b16 a[20:21], v4 offset:38912\n" \
"ds_read_b64_tr_b16 a[22:23], v4 offset:39040\n" \
"ds_read_b64_tr_b16 a[24:25], v4 offset:36896\n" \
"ds_read_b64_tr_b16 a[26:27], v4 offset:37024\n" \
"ds_read_b64_tr_b16 a[28:29], v4 offset:38944\n" \
"ds_read_b64_tr_b16 a[30:31], v4 offset:39072\n" \
"s_waitcnt lgkmcnt(0)\n" \
"s_add_u32 m0, s62, 36864\n" \
"s_mov_b32 s54, 1441792\n" \
"buffer_load_dwordx4 v2, s[64:67], s54 offen lds\n" \
"s_add_u32 m0, s62, 37888\n" \
"s_mov_b32 s54, 1474560\n" \
"buffer_load_dwordx4 v2, s[64:67], s54 offen lds\n" \
"s_add_u32 m0, s62, 38912\n" \
"s_mov_b32 s54, 1507328\n" \
"buffer_load_dwordx4 v2, s[64:67], s54 offen lds\n" \
"s_add_u32 m0, s62, 39936\n" \
"s_mov_b32 s54, 1540096\n" \
"buffer_load_dwordx4 v2, s[64:67], s54 offen lds\n" \
"s_waitcnt vmcnt(36)\n" \
"ds_read_b64_tr_b16 a[32:33], v4 offset:0\n" \
"ds_read_b64_tr_b16 a[34:35], v4 offset:128\n" \
"ds_read_b64_tr_b16 a[36:37], v4 offset:2048\n" \
"ds_read_b64_tr_b16 a[38:39], v4 offset:2176\n" \
"ds_read_b64_tr_b16 a[40:41], v4 offset:32\n" \
"ds_read_b64_tr_b16 a[42:43], v4 offset:160\n" \
"ds_read_b64_tr_b16 a[44:45], v4 offset:2080\n" \
"ds_read_b64_tr_b16 a[46:47], v4 offset:2208\n" \
"s_waitcnt lgkmcnt(0)\n" \
"s_add_u32 m0, s62, 0\n" \
"s_mov_b32 s54, 1572864\n" \
"buffer_load_dwordx4 v2, s[64:67], s54 offen lds\n" \
"s_add_u32 m0, s62, 1024\n" \
"s_mov_b32 s54, 1605632\n" \
"buffer_load_dwordx4 v2, s[64:67], s54 offen lds\n" \
"s_add_u32 m0, s62, 2048\n" \
"s_mov_b32 s54, 1638400\n" \
"buffer_load_dwordx4 v2, s[64:67], s54 offen lds\n" \
"s_add_u32 m0, s62, 3072\n" \
"s_mov_b32 s54, 1671168\n" \
"buffer_load_dwordx4 v2, s[64:67], s54 offen lds\n" \
"s_waitcnt vmcnt(36)\n" \
"ds_read_b64_tr_b16 a[48:49], v4 offset:4096\n" \
"ds_read_b64_tr_b16 a[50:51], v4 offset:4224\n" \
"ds_read_b64_tr_b16 a[52:53], v4 offset:6144\n" \
"ds_read_b64_tr_b16 a[54:55], v4 offset:6272\n" \
"ds_read_b64_tr_b16 a[56:57], v4 offset:4128\n" \
"ds_read_b64_tr_b16 a[58:59], v4 offset:4256\n" \
"ds_read_b64_tr_b16 a[60:61], v4 offset:6176\n" \
"ds_read_b64_tr_b16 a[62:63], v4 offset:6304\n" \
"s_waitcnt lgkmcnt(0)\n" \
"s_add_u32 m0, s62, 4096\n" \
"s_mov_b32 s54, 1703936\n" \
"buffer_load_dwordx4 v2, s[64:67], s54 offen lds\n" \
"s_add_u32 m0, s62, 5120\n" \
"s_mov_b32 s54, 1736704\n" \
"buffer_load_dwordx4 v2, s[64:67], s54 offen lds\n" \
"s_add_u32 m0, s62, 6144\n" \
"s_mov_b32 s54, 1769472\n" \
"buffer_load_dwordx4 v2, s[64:67], s54 offen lds\n" \
"s_add_u32 m0, s62, 7168\n" \
"s_mov_b32 s54, 1802240\n" \
"buffer_load_dwordx4 v2, s[64:67], s54 offen lds\n" \
"s_waitcnt vmcnt(36)\n" \
"ds_read_b64_tr_b16 a[64:65], v4 offset:8192\n" \
"ds_read_b64_tr_b16 a[66:67], v4 offset:8320\n" \
"ds_read_b64_tr_b16 a[68:69], v4 offset:10240\n" \
"ds_read_b64_tr_b16 a[70:71], v4 offset:10368\n" \
"ds_read_b64_tr_b16 a[72:73], v4 offset:8224\n" \
"ds_read_b64_tr_b16 a[74:75], v4 offset:8352\n" \
"ds_read_b64_tr_b16 a[76:77], v4 offset:10272\n" \
"ds_read_b64_tr_b16 a[78:79], v4 offset:10400\n" \
"s_waitcnt lgkmcnt(0)\n" \
"s_add_u32 m0, s62, 8192\n" \
"s_mov_b32 s54, 1835008\n" \
"buffer_load_dwordx4 v2, s[64:67], s54 offen lds\n" \
"s_add_u32 m0, s62, 9216\n" \
"s_mov_b32 s54, 1867776\n" \
"buffer_load_dwordx4 v2, s[64:67], s54 offen lds\n" \
"s_add_u32 m0, s62, 10240\n" \
"s_mov_b32 s54, 1900544\n" \
"buffer_load_dwordx4 v2, s[64:67], s54 offen lds\n" \
"s_add_u32 m0, s62, 11264\n" \
"s_mov_b32 s54, 1933312\n" \
"buffer_load_dwordx4 v2, s[64:67], s54 offen lds\n" \
"s_waitcnt vmcnt(36)\n" \
"ds_read_b64_tr_b16 a[80:81], v4 offset:12288\n" \
"ds_read_b64_tr_b16 a[82:83], v4 offset:12416\n" \
"ds_read_b64_tr_b16 a[84:85], v4 offset:14336\n" \
"ds_read_b64_tr_b16 a[86:87], v4 offset:14464\n" \
"ds_read_b64_tr_b16 a[88:89], v4 offset:12320\n" \
"ds_read_b64_tr_b16 a[90:91], v4 offset:12448\n" \
"ds_read_b64_tr_b16 a[92:93], v4 offset:14368\n" \
"ds_read_b64_tr_b16 a[94:95], v4 offset:14496\n" \
"s_waitcnt lgkmcnt(0)\n" \
"s_add_u32 m0, s62, 12288\n" \
"s_mov_b32 s54, 1966080\n" \
"buffer_load_dwordx4 v2, s[64:67], s54 offen lds\n" \
"s_add_u32 m0, s62, 13312\n" \
"s_mov_b32 s54, 1998848\n" \
"buffer_load_dwordx4 v2, s[64:67], s54 offen lds\n" \
"s_add_u32 m0, s62, 14336\n" \
"s_mov_b32 s54, 2031616\n" \
"buffer_load_dwordx4 v2, s[64:67], s54 offen lds\n" \
"s_add_u32 m0, s62, 15360\n" \
"s_mov_b32 s54, 2064384\n" \
"buffer_load_dwordx4 v2, s[64:67], s54 offen lds\n" \
"s_waitcnt vmcnt(36)\n" \
"ds_read_b64_tr_b16 a[96:97], v4 offset:16384\n" \
"ds_read_b64_tr_b16 a[98:99], v4 offset:16512\n" \
"ds_read_b64_tr_b16 a[100:101], v4 offset:18432\n" \
"ds_read_b64_tr_b16 a[102:103], v4 offset:18560\n" \
"ds_read_b64_tr_b16 a[104:105], v4 offset:16416\n" \
"ds_read_b64_tr_b16 a[106:107], v4 offset:16544\n" \
"ds_read_b64_tr_b16 a[108:109], v4 offset:18464\n" \
"ds_read_b64_tr_b16 a[110:111], v4 offset:18592\n" \
"s_waitcnt vmcnt(32)\n" \
"ds_read_b64_tr_b16 a[112:113], v4 offset:20480\n" \
"ds_read_b64_tr_b16 a[114:115], v4 offset:20608\n" \
"ds_read_b64_tr_b16 a[116:117], v4 offset:22528\n" \
"ds_read_b64_tr_b16 a[118:119], v4 offset:22656\n" \
"ds_read_b64_tr_b16 a[120:121], v4 offset:20512\n" \
"ds_read_b64_tr_b16 a[122:123], v4 offset:20640\n" \
"ds_read_b64_tr_b16 a[124:125], v4 offset:22560\n" \
"ds_read_b64_tr_b16 a[126:127], v4 offset:22688\n" \
"s_waitcnt vmcnt(28)\n" \
"ds_read_b64_tr_b16 a[128:129], v4 offset:24576\n" \
"ds_read_b64_tr_b16 a[130:131], v4 offset:24704\n" \
"ds_read_b64_tr_b16 a[132:133], v4 offset:26624\n" \
"ds_read_b64_tr_b16 a[134:135], v4 offset:26752\n" \
"ds_read_b64_tr_b16 a[136:137], v4 offset:24608\n" \
"ds_read_b64_tr_b16 a[138:139], v4 offset:24736\n" \
"ds_read_b64_tr_b16 a[140:141], v4 offset:26656\n" \
"ds_read_b64_tr_b16 a[142:143], v4 offset:26784\n" \
"s_waitcnt vmcnt(24)\n" \
"ds_read_b64_tr_b16 a[144:145], v4 offset:28672\n" \
"ds_read_b64_tr_b16 a[146:147], v4 offset:28800\n" \
"ds_read_b64_tr_b16 a[148:149], v4 offset:30720\n" \
"ds_read_b64_tr_b16 a[150:151], v4 offset:30848\n" \
"ds_read_b64_tr_b16 a[152:153], v4 offset:28704\n" \
"ds_read_b64_tr_b16 a[154:155], v4 offset:28832\n" \
"ds_read_b64_tr_b16 a[156:157], v4 offset:30752\n" \
"ds_read_b64_tr_b16 a[158:159], v4 offset:30880\n" \
"s_waitcnt vmcnt(20)\n" \
"ds_read_b64_tr_b16 a[160:161], v4 offset:32768\n" \
"ds_read_b64_tr_b16 a[162:163], v4 offset:32896\n" \
"ds_read_b64_tr_b16 a[164:165], v4 offset:34816\n" \
"ds_read_b64_tr_b16 a[166:167], v4 offset:34944\n" \
"ds_read_b64_tr_b16 a[168:169], v4 offset:32800\n" \
"ds_read_b64_tr_b16 a[170:171], v4 offset:32928\n" \
"ds_read_b64_tr_b16 a[172:173], v4 offset:34848\n" \
"ds_read_b64_tr_b16 a[174:175], v4 offset:34976\n" \
"s_waitcnt vmcnt(16)\n" \
"ds_read_b64_tr_b16 a[176:177], v4 offset:36864\n" \
"ds_read_b64_tr_b16 a[178:179], v4 offset:36992\n" \
"ds_read_b64_tr_b16 a[180:181], v4 offset:38912\n" \
"ds_read_b64_tr_b16 a[182:183], v4 offset:39040\n" \
"ds_read_b64_tr_b16 a[184:185], v4 offset:36896\n" \
"ds_read_b64_tr_b16 a[186:187], v4 offset:37024\n" \
"ds_read_b64_tr_b16 a[188:189], v4 offset:38944\n" \
"ds_read_b64_tr_b16 a[190:191], v4 offset:39072\n" \
"s_waitcnt vmcnt(12)\n" \
"ds_read_b64_tr_b16 a[192:193], v4 offset:0\n" \
"ds_read_b64_tr_b16 a[194:195], v4 offset:128\n" \
"ds_read_b64_tr_b16 a[196:197], v4 offset:2048\n" \
"ds_read_b64_tr_b16 a[198:199], v4 offset:2176\n" \
"ds_read_b64_tr_b16 a[200:201], v4 offset:32\n" \
"ds_read_b64_tr_b16 a[202:203], v4 offset:160\n" \
"ds_read_b64_tr_b16 a[204:205], v4 offset:2080\n" \
"ds_read_b64_tr_b16 a[206:207], v4 offset:2208\n" \
"s_waitcnt vmcnt(8)\n" \
"ds_read_b64_tr_b16 a[208:209], v4 offset:4096\n" \
"ds_read_b64_tr_b16 a[210:211], v4 offset:4224\n" \
"ds_read_b64_tr_b16 a[212:213], v4 offset:6144\n" \
"ds_read_b64_tr_b16 a[214:215], v4 offset:6272\n" \
"ds_read_b64_tr_b16 a[216:217], v4 offset:4128\n" \
"ds_read_b64_tr_b16 a[218:219], v4 offset:4256\n" \
"ds_read_b64_tr_b16 a[220:221], v4 offset:6176\n" \
"ds_read_b64_tr_b16 a[222:223], v4 offset:6304\n" \
"s_waitcnt vmcnt(4)\n" \
"ds_read_b64_tr_b16 a[224:225], v4 offset:8192\n" \
"ds_read_b64_tr_b16 a[226:227], v4 offset:8320\n" \
"ds_read_b64_tr_b16 a[228:229], v4 offset:10240\n" \
"ds_read_b64_tr_b16 a[230:231], v4 offset:10368\n" \
"ds_read_b64_tr_b16 a[232:233], v4 offset:8224\n" \
"ds_read_b64_tr_b16 a[234:235], v4 offset:8352\n" \
"ds_read_b64_tr_b16 a[236:237], v4 offset:10272\n" \
"ds_read_b64_tr_b16 a[238:239], v4 offset:10400\n" \
"s_waitcnt vmcnt(0)\n" \
"ds_read_b64_tr_b16 a[240:241], v4 offset:12288\n" \
"ds_read_b64_tr_b16 a[242:243], v4 offset:12416\n" \
"ds_read_b64_tr_b16 a[244:245], v4 offset:14336\n" \
"ds_read_b64_tr_b16 a[246:247], v4 offset:14464\n" \
"ds_read_b64_tr_b16 a[248:249], v4 offset:12320\n" \
"ds_read_b64_tr_b16 a[250:251], v4 offset:12448\n" \
"ds_read_b64_tr_b16 a[252:253], v4 offset:14368\n" \
"ds_read_b64_tr_b16 a[254:255], v4 offset:14496\n" \
"s_waitcnt vmcnt(0) lgkmcnt(0)\n" \
"s_barrier\n" \
"s_getpc_b64 s[18:19]\n" \
".Lv1_pc_%=:\n" \
"v_mbcnt_lo_u32_b32 v40, -1, 0\n" \
"v_mbcnt_hi_u32_b32 v40, -1, v40\n" \
"v_lshl_add_u32 v40, s48, 6, v40\n" \
"v_lshlrev_b32 v40, 6, v40\n" \
"v_min_u32 v40, .Lv_fin_%=-.Lv1_pc_%=, v40\n" \
"global_load_dword v41, v40, s[18:19]\n" \
"s_mov_b32 s49, 0\n" \
"s_mov_b32 s52, 0\n" \
"s_mov_b32 s88, 0\n" \
"s_mov_b32 s76, 0\n" \
"s_mov_b32 s96, 0\n" \
"s_mov_b32 s22, 0xfffffc00\n" \
"s_mov_b32 s23, -1\n" \
"s_mul_i32 s32, s17, 1\n" \
"s_mov_b32 s33, s32\n" \
"s_mov_b32 s34, s17\n" \
"v_max_u32 v20, 1, v20\n" \
"v_max_u32 v21, 1, v21\n" \
"v_max_u32 v22, 1, v22\n" \
"v_max_u32 v23, 1, v23\n" \
"buffer_store_dwordx4 v[20:23], v8, s[36:39], 0 offen sc1\n" \
"s_mov_b32 s72, 0\n" \
"s_mov_b32 s73, -1\n" \
"v_lshlrev_b32 v31, 1, v0\n" \
"v_add_u32 v31, 16384, v31\n" \
"s_lshl_b32 s79, s48, 10\n" \
"s_add_u32 s79, s79, 0\n" \
"v_xor_b32 v30, 0x1000, v6\n" \
"s_mov_b32 s53, 0\n" \
"s_cmp_lg_u32 s48, 0\n" \
"s_cbranch_scc1 .Lv_go0x_%=\n" \
".Lv_go0_%=:\n" \
"buffer_load_dword v13, v10, s[36:39], 0 offen sc1\n" \
"s_waitcnt vmcnt(0)\n" \
"v_cmp_eq_u32 vcc, 0, v13\n" \
"s_add_u32 s53, s53, 1\n" \
"s_cmp_lt_u32 s53, 32768\n" \
"s_cselect_b64 s[54:55], -1, 0\n" \
"s_and_b64 vcc, vcc, s[54:55]\n" \
"s_cbranch_vccz .Lv_go0x_%=\n" \
"s_sleep 4\n" \
"s_branch .Lv_go0_%=\n" \
".Lv_go0x_%=:\n" \
"s_memrealtime s[56:57]\n" \
"s_waitcnt lgkmcnt(0)\n" \
"s_sub_u32 s50, s56, 20\n" \
"s_waitcnt vmcnt(0) lgkmcnt(0)\n" \
"s_add_u32 s54, s49, 0\n" \
"s_and_b32 s54, s54, 3\n" \
"s_mul_i32 s58, s54, 131072\n" \
"s_add_u32 s54, s49, 1\n" \
"s_and_b32 s54, s54, 3\n" \
"s_mul_i32 s59, s54, 131072\n" \
"s_add_u32 s54, s49, 2\n" \
"s_and_b32 s54, s54, 3\n" \
"s_mul_i32 s60, s54, 131072\n" \
"s_add_u32 s54, s49, 3\n" \
"s_and_b32 s54, s54, 3\n" \
"s_mul_i32 s61, s54, 131072\n" \
"s_mov_b32 s77, 0\n" \
"s_add_u32 s78, s77, s63\n" \
"s_xor_b32 s89, s78, 0x4800\n" \
"s_xor_b32 s90, s78, 0x800\n" \
"s_xor_b32 s91, s78, 0x4000\n" \
"s_mov_b32 m0, s79\n" \
"s_add_u32 s51, s50, s46\n" \
"v_mov_b64 v[80:81], 0\n" \
"v_mov_b64 v[82:83], 0\n" \
"v_mov_b32 v13, -1\n" \
"s_mov_b32 s23, -1\n" \
"s_cmp_eq_u32 s48, 0\n" \
"s_cselect_b32 s80, -1, 0\n" \
"s_mov_b32 s81, 0\n" \
"s_nop 7\n" \
"s_nop 7\n" \
".p2align 6\n" \
".Lv_loop_%=:\n" \
"s_getpc_b64 s[18:19]\n" \
".Lv_wp_%=:\n" \
"s_add_u32 s18, s18, .Lv_ws_%=-.Lv_wp_%=\n" \
"s_addc_u32 s19, s19, 0\n" \
"s_sub_u32 s21, s51, 5\n" \
"s_nop 0\n" \
".Lv_w_%=:\n" \
"s_memrealtime s[56:57]\n" \
"s_waitcnt lgkmcnt(0)\n" \
"s_sub_u32 s54, s56, s21\n" \
"s_cmp_gt_u32 s54, s22\n" \
"s_cbranch_scc1 .Lv_w_%=\n" \
"s_min_u32 s21, s54, 3\n" \
"s_lshl2_add_u32 s18, s21, s18\n" \
"s_setpc_b64 s[18:19]\n" \
"s_nop 0\n" \
".Lv_ws_%=:\n" \
"s_nop 4\n" \
"s_nop 5\n" \
"s_nop 4\n" \
".Lv_w4_%=:\n" \
"s_barrier\n" \
".Lv_go_%=:\n" \
"buffer_load_dwordx4 v5, s[36:39], s78 offen sc1 lds\n" \
"s_nop 7\n" \
"s_add_u32 m0, s79, 4096\n" \
"s_nop 0\n" \
"buffer_load_dwordx4 v5, s[36:39], s89 offen sc1 lds\n" \
"s_nop 7\n" \
"s_add_u32 m0, s79, 8192\n" \
"s_nop 0\n" \
"buffer_load_dwordx4 v5, s[36:39], s90 offen sc1 lds\n" \
"s_nop 7\n" \
"s_add_u32 m0, s79, 12288\n" \
"s_nop 0\n" \
"buffer_load_dwordx4 v5, s[36:39], s91 offen sc1 lds\n" \
".Lv_x_%=:\n" \
"s_waitcnt vmcnt(2)\n" \
"s_barrier\n" \
"ds_read_b128 v[32:35], v0 offset:0\n" \
"ds_read_b128 v[36:39], v0 offset:1024\n" \
"ds_read_b128 v[40:43], v0 offset:2048\n" \
"ds_read_b128 v[44:47], v0 offset:3072\n" \
"ds_read_b128 v[48:51], v0 offset:4096\n" \
"s_waitcnt lgkmcnt(4)\n" \
"v_smfmac_f32_16x16x64_bf16 v[80:83], v[32:35], v[128:135], v12\n" \
"ds_read_b128 v[52:55], v0 offset:5120\n" \
"s_waitcnt lgkmcnt(4)\n" \
"v_min3_u32 v13, v13, v32, v34\n" \
"v_smfmac_f32_16x16x64_bf16 v[80:83], v[36:39], v[136:143], v12\n" \
"ds_read_b128 v[56:59], v0 offset:6144\n" \
"s_waitcnt lgkmcnt(4)\n" \
"v_min3_u32 v13, v13, v36, v38\n" \
"v_smfmac_f32_16x16x64_bf16 v[80:83], v[40:43], v[144:151], v12\n" \
"ds_read_b128 v[60:63], v0 offset:7168\n" \
"s_waitcnt lgkmcnt(4)\n" \
"v_min3_u32 v13, v13, v40, v42\n" \
"v_smfmac_f32_16x16x64_bf16 v[80:83], v[44:47], v[152:159], v12\n" \
"s_waitcnt vmcnt(0)\n" \
"s_barrier\n" \
"ds_read_b128 v[32:35], v0 offset:8192\n" \
"s_waitcnt lgkmcnt(4)\n" \
"v_min3_u32 v13, v13, v44, v46\n" \
"v_smfmac_f32_16x16x64_bf16 v[80:83], v[48:51], v[160:167], v12\n" \
"ds_read_b128 v[36:39], v0 offset:9216\n" \
"s_waitcnt lgkmcnt(4)\n" \
"v_min3_u32 v13, v13, v48, v50\n" \
"v_cmp_eq_u32_e64 vcc, 0, v13\n" \
"v_smfmac_f32_16x16x64_bf16 v[80:83], v[52:55], v[168:175], v12\n" \
"ds_read_b128 v[40:43], v0 offset:10240\n" \
"s_waitcnt lgkmcnt(4)\n" \
"v_min3_u32 v13, v13, v52, v54\n" \
"v_smfmac_f32_16x16x64_bf16 v[80:83], v[56:59], v[176:183], v12\n" \
"ds_read_b128 v[44:47], v0 offset:11264\n" \
"s_waitcnt lgkmcnt(4)\n" \
"v_min3_u32 v13, v13, v56, v58\n" \
"v_smfmac_f32_16x16x64_bf16 v[80:83], v[60:63], v[184:191], v12\n" \
"ds_read_b128 v[48:51], v0 offset:12288\n" \
"s_waitcnt lgkmcnt(4)\n" \
"s_nop 0\n" \
"s_cbranch_vccnz .Lv_mretry_%=\n" \
"v_min3_u32 v13, v13, v60, v62\n" \
"v_smfmac_f32_16x16x64_bf16 v[80:83], v[32:35], v[192:199], v12\n" \
"ds_read_b128 v[52:55], v0 offset:13312\n" \
"s_waitcnt lgkmcnt(4)\n" \
"v_min3_u32 v13, v13, v32, v34\n" \
"v_smfmac_f32_16x16x64_bf16 v[80:83], v[36:39], v[200:207], v12\n" \
"ds_read_b128 v[56:59], v0 offset:14336\n" \
"s_waitcnt lgkmcnt(4)\n" \
"v_min3_u32 v13, v13, v36, v38\n" \
"v_smfmac_f32_16x16x64_bf16 v[80:83], v[40:43], v[208:215], v12\n" \
"ds_read_b128 v[60:63], v0 offset:15360\n" \
"s_waitcnt lgkmcnt(4)\n" \
"v_min3_u32 v13, v13, v40, v42\n" \
"v_smfmac_f32_16x16x64_bf16 v[80:83], v[44:47], v[216:223], v12\n" \
"s_waitcnt lgkmcnt(3)\n" \
"v_min3_u32 v13, v13, v44, v46\n" \
"v_smfmac_f32_16x16x64_bf16 v[80:83], v[48:51], v[224:231], v12\n" \
"s_waitcnt lgkmcnt(2)\n" \
"v_min3_u32 v13, v13, v48, v50\n" \
"v_smfmac_f32_16x16x64_bf16 v[80:83], v[52:55], v[232:239], v12\n" \
"s_waitcnt lgkmcnt(1)\n" \
"v_min3_u32 v13, v13, v52, v54\n" \
"v_smfmac_f32_16x16x64_bf16 v[80:83], v[56:59], v[240:247], v12\n" \
"s_waitcnt lgkmcnt(0)\n" \
"v_min3_u32 v13, v13, v56, v58\n" \
"v_smfmac_f32_16x16x64_bf16 v[80:83], v[60:63], v[248:255], v12\n" \
"v_min3_u32 v13, v13, v60, v62\n" \
"v_cmp_eq_u32 vcc, 0, v13\n" \
"s_nop 5\n" \
"v_pk_add_f32 v[80:81], v[80:81], v[82:83]\n" \
"s_cbranch_vccnz .Lv_mretry_%=\n" \
"v_cvt_pk_bf16_f32 v84, v80, v81\n" \
"v_max_u32 v84, 1, v84\n" \
"buffer_store_dword v84, v7, s[36:39], s58 offen sc1\n" \
"s_mov_b32 s75, s77\n" \
"s_lshr_b32 s77, s59, 1\n" \
"buffer_load_dwordx2 v[64:65], v9, s[36:39], s75 offen sc1\n" \
"s_sub_u32 s53, s49, 1\n" \
"s_add_u32 s25, s24, 3\n" \
"s_cmp_eq_u32 s26, s24\n" \
"s_subb_u32 s25, s25, 0\n" \
"s_cmp_eq_u32 s28, s24\n" \
"s_subb_u32 s25, s25, 0\n" \
"s_cmp_eq_u32 s30, s24\n" \
"s_subb_u32 s25, s25, 0\n" \
"s_max_u32 s16, s25, 1\n" \
"s_sub_u32 s24, s25, s55\n" \
"s_min_u32 s24, s24, 255\n" \
"s_cmp_eq_u32 s23, s53\n" \
"s_cselect_b32 s24, s34, s24\n" \
"s_min_u32 s34, s24, s32\n" \
"s_max_u32 s25, s24, s32\n" \
"s_min_u32 s25, s25, s33\n" \
"s_max_u32 s34, s34, s25\n" \
"s_mov_b32 s33, s32\n" \
"s_mov_b32 s32, s24\n" \
"s_cmp_eq_u32 s49, 63\n" \
"s_cbranch_scc1 .Lv_end_%=\n" \
"v_mov_b64 v[80:81], 0\n" \
"v_mov_b64 v[82:83], 0\n" \
"v_mov_b64 v[84:85], 0\n" \
"v_mov_b64 v[86:87], 0\n" \
"v_mov_b64 v[88:89], 0\n" \
"v_mov_b64 v[90:91], 0\n" \
"v_mov_b64 v[92:93], 0\n" \
"v_mov_b64 v[94:95], 0\n" \
"buffer_store_dword v16, v7, s[36:39], s60 offen sc1\n" \
"s_lshr_b32 s54, s61, 1\n" \
"buffer_store_dwordx4 v[16:19], v8, s[36:39], s54 offen sc1\n" \
"s_sub_u32 s54, s34, s74\n" \
"v_mov_b32_e64 v13, s54\n" \
"buffer_store_dword v13, v28, s[36:39], 0 offen sc1\n" \
"v_mov_b32_e64 v13, -1\n" \
"s_mov_b32 s54, 0\n" \
"s_cmp_lg_u32 s48, 0\n" \
"s_cbranch_scc1 .Lv_wo4_%=\n" \
"s_waitcnt vmcnt(3)\n" \
"v_cmp_eq_u32_e64 vcc, 0, v64\n" \
"s_nop 0\n" \
"s_and_b64 vcc, vcc, s[80:81]\n" \
"s_cbranch_vccnz .Lv_h_cold_%=\n" \
".Lv_h_ok_%=:\n" \
"v_subrev_u32 v64, s50, v64\n" \
"v_max_i32_dpp v65, v65, v65 row_ror:8 row_mask:0xf bank_mask:0xf\n" \
"s_nop 0\n" \
"v_max_i32_dpp v64, v64, v64 row_ror:8 row_mask:0xf bank_mask:0xf\n" \
"v_max_i32_dpp v65, v65, v65 row_ror:4 row_mask:0xf bank_mask:0xf\n" \
"s_nop 0\n" \
"v_max_i32_dpp v64, v64, v64 row_ror:4 row_mask:0xf bank_mask:0xf\n" \
"v_max_i32_dpp v65, v65, v65 row_ror:2 row_mask:0xf bank_mask:0xf\n" \
"s_nop 0\n" \
"v_max_i32_dpp v64, v64, v64 row_ror:2 row_mask:0xf bank_mask:0xf\n" \
"v_max_i32_dpp v65, v65, v65 row_ror:1 row_mask:0xf bank_mask:0xf\n" \
"s_nop 0\n" \
"v_max_i32_dpp v64, v64, v64 row_ror:1 row_mask:0xf bank_mask:0xf\n" \
"v_readlane_b32 s53, v65, 0\n" \
"v_readlane_b32 s24, v65, 16\n" \
"v_readlane_b32 s54, v64, 0\n" \
"v_readlane_b32 s55, v64, 16\n" \
"s_max_i32 s24, s24, s53\n" \
"s_max_i32 s55, s55, s54\n" \
"s_and_b32 s53, s96, 0xffff\n" \
"s_add_u32 s54, s53, 30\n" \
"s_cmp_gt_i32 s55, s54\n" \
"s_cselect_b32 s54, 0x10000, 0\n" \
"s_add_u32 s96, s96, s54\n" \
"s_sub_u32 s96, s96, s53\n" \
"s_min_i32 s53, s53, s55\n" \
"s_add_u32 s96, s96, s53\n" \
"s_cmp_lt_u32 s49, 4\n" \
"s_cselect_b32 s96, 0x7fff, s96\n" \
"s_lshr_b32 s53, s96, 17\n" \
"s_lshl_b32 s53, s53, 1\n" \
"s_min_u32 s53, s53, 4\n" \
"s_add_u32 s55, s55, s50\n" \
"s_add_u32 s55, s55, s44\n" \
"s_add_u32 s55, s55, s53\n" \
"s_add_u32 s50, s24, s55\n" \
"s_add_u32 s50, s50, s45\n" \
"s_add_u32 s50, s50, s53\n" \
"s_getpc_b64 s[18:19]\n" \
".Lv_wop_%=:\n" \
"s_add_u32 s18, s18, .Lv_wos_%=-.Lv_wop_%=\n" \
"s_addc_u32 s19, s19, 0\n" \
"s_sub_u32 s21, s55, 5\n" \
"s_nop 0\n" \
"s_nop 0\n" \
"s_nop 0\n" \
".Lv_wo_%=:\n" \
"s_memrealtime s[56:57]\n" \
"s_waitcnt lgkmcnt(0)\n" \
"s_sub_u32 s54, s56, s21\n" \
"s_cmp_gt_u32 s54, s22\n" \
"s_cbranch_scc1 .Lv_wo_%=\n" \
"s_min_u32 s21, s54, 3\n" \
"s_lshl2_add_u32 s18, s21, s18\n" \
"s_setpc_b64 s[18:19]\n" \
"s_nop 0\n" \
".Lv_wos_%=:\n" \
"s_nop 4\n" \
"s_nop 5\n" \
"s_nop 4\n" \
".Lv_wo4_%=:\n" \
"s_barrier\n" \
".Lv_o_%=:\n" \
"buffer_load_dwordx4 v[32:35], v6, s[36:39], s58 offen offset:0 sc1\n" \
"s_cmp_lt_u32 s54, 8\n" \
"s_cselect_b32 s23, s23, s49\n" \
"s_nop 8\n" \
"buffer_load_dwordx4 v[36:39], v6, s[36:39], s58 offen offset:1024 sc1\n" \
"s_nop 10\n" \
"buffer_load_dwordx4 v[40:43], v6, s[36:39], s58 offen offset:2048 sc1\n" \
"s_nop 10\n" \
"buffer_load_dwordx4 v[44:47], v6, s[36:39], s58 offen offset:3072 sc1\n" \
"s_nop 10\n" \
"buffer_load_dwordx4 v[48:51], v30, s[36:39], s58 offen offset:0 sc1\n" \
"s_nop 10\n" \
"buffer_load_dwordx4 v[52:55], v30, s[36:39], s58 offen offset:1024 sc1\n" \
"s_nop 10\n" \
"buffer_load_dwordx4 v[56:59], v30, s[36:39], s58 offen offset:2048 sc1\n" \
"s_nop 10\n" \
"buffer_load_dwordx4 v[60:63], v30, s[36:39], s58 offen offset:3072 sc1\n" \
".Lv_y_%=:\n" \
"s_waitcnt vmcnt(7)\n" \
"v_smfmac_f32_16x16x64_bf16 v[80:83], v[32:35], a[0:7], v12\n" \
"v_min3_u32 v13, v13, v32, v34\n" \
"v_smfmac_f32_16x16x64_bf16 v[84:87], v[32:35], a[8:15], v12\n" \
"v_smfmac_f32_16x16x64_bf16 v[88:91], v[32:35], a[16:23], v12\n" \
"v_smfmac_f32_16x16x64_bf16 v[92:95], v[32:35], a[24:31], v12\n" \
"s_waitcnt vmcnt(6)\n" \
"v_smfmac_f32_16x16x64_bf16 v[80:83], v[36:39], a[32:39], v12\n" \
"v_min3_u32 v13, v13, v36, v38\n" \
"v_smfmac_f32_16x16x64_bf16 v[84:87], v[36:39], a[40:47], v12\n" \
"v_cmp_eq_u32 vcc, 0, v13\n" \
"v_smfmac_f32_16x16x64_bf16 v[88:91], v[36:39], a[48:55], v12\n" \
"v_smfmac_f32_16x16x64_bf16 v[92:95], v[36:39], a[56:63], v12\n" \
"s_cbranch_vccnz .Lv_retry_%=\n" \
"s_waitcnt vmcnt(5)\n" \
"v_smfmac_f32_16x16x64_bf16 v[80:83], v[40:43], a[64:71], v12\n" \
"v_min3_u32 v13, v13, v40, v42\n" \
"v_smfmac_f32_16x16x64_bf16 v[84:87], v[40:43], a[72:79], v12\n" \
"v_smfmac_f32_16x16x64_bf16 v[88:91], v[40:43], a[80:87], v12\n" \
"v_smfmac_f32_16x16x64_bf16 v[92:95], v[40:43], a[88:95], v12\n" \
"s_waitcnt vmcnt(4)\n" \
"v_smfmac_f32_16x16x64_bf16 v[80:83], v[44:47], a[96:103], v12\n" \
"v_min3_u32 v13, v13, v44, v46\n" \
"v_smfmac_f32_16x16x64_bf16 v[84:87], v[44:47], a[104:111], v12\n" \
"v_smfmac_f32_16x16x64_bf16 v[88:91], v[44:47], a[112:119], v12\n" \
"v_smfmac_f32_16x16x64_bf16 v[92:95], v[44:47], a[120:127], v12\n" \
"s_waitcnt vmcnt(3)\n" \
"v_smfmac_f32_16x16x64_bf16 v[80:83], v[48:51], a[128:135], v12\n" \
"v_min3_u32 v13, v13, v48, v50\n" \
"v_smfmac_f32_16x16x64_bf16 v[84:87], v[48:51], a[136:143], v12\n" \
"v_smfmac_f32_16x16x64_bf16 v[88:91], v[48:51], a[144:151], v12\n" \
"v_smfmac_f32_16x16x64_bf16 v[92:95], v[48:51], a[152:159], v12\n" \
"s_waitcnt vmcnt(2)\n" \
"v_smfmac_f32_16x16x64_bf16 v[80:83], v[52:55], a[160:167], v12\n" \
"v_min3_u32 v13, v13, v52, v54\n" \
"v_smfmac_f32_16x16x64_bf16 v[84:87], v[52:55], a[168:175], v12\n" \
"v_smfmac_f32_16x16x64_bf16 v[88:91], v[52:55], a[176:183], v12\n" \
"v_smfmac_f32_16x16x64_bf16 v[92:95], v[52:55], a[184:191], v12\n" \
"s_waitcnt vmcnt(1)\n" \
"v_smfmac_f32_16x16x64_bf16 v[80:83], v[56:59], a[192:199], v12\n" \
"v_min3_u32 v13, v13, v56, v58\n" \
"v_smfmac_f32_16x16x64_bf16 v[84:87], v[56:59], a[200:207], v12\n" \
"v_smfmac_f32_16x16x64_bf16 v[88:91], v[56:59], a[208:215], v12\n" \
"v_smfmac_f32_16x16x64_bf16 v[92:95], v[56:59], a[216:223], v12\n" \
"s_waitcnt vmcnt(0)\n" \
"v_smfmac_f32_16x16x64_bf16 v[80:83], v[60:63], a[224:231], v12\n" \
"v_min3_u32 v13, v13, v60, v62\n" \
"v_smfmac_f32_16x16x64_bf16 v[84:87], v[60:63], a[232:239], v12\n" \
"v_cmp_eq_u32 vcc, 0, v13\n" \
"v_smfmac_f32_16x16x64_bf16 v[88:91], v[60:63], a[240:247], v12\n" \
"v_smfmac_f32_16x16x64_bf16 v[92:95], v[60:63], a[248:255], v12\n" \
"s_cbranch_vccnz .Lv_retry_%=\n" \
".Lv_y2_%=:\n" \
"v_pk_add_f32 v[80:81], v[80:81], v[82:83]\n" \
"v_pk_add_f32 v[84:85], v[84:85], v[86:87]\n" \
"s_nop 0\n" \
"v_pk_add_f32 v[88:89], v[88:89], v[90:91]\n" \
"s_nop 2\n" \
"v_pk_add_f32 v[92:93], v[92:93], v[94:95]\n" \
"v_cndmask_b32_e64 v80, v80, v88, s[72:73]\n" \
"v_cndmask_b32_e64 v81, v81, v89, s[72:73]\n" \
"v_cndmask_b32_e64 v84, v84, v92, s[72:73]\n" \
"v_cndmask_b32_e64 v85, v85, v93, s[72:73]\n" \
"s_nop 0\n" \
"v_permlane32_swap_b32 v80, v84\n" \
"v_permlane32_swap_b32 v81, v85\n" \
"v_pk_add_f32 v[80:81], v[80:81], v[84:85]\n" \
"ds_write2_b32 v15, v80, v81 offset1:32\n" \
"s_cmp_lg_u32 s48, 0\n" \
"s_waitcnt lgkmcnt(0)\n" \
"s_barrier\n" \
"s_nop 0\n" \
"s_nop 0\n" \
"s_nop 0\n" \
"s_cbranch_scc1 .Lv_skip_%=\n" \
"ds_read_b128 v[64:67], v31\n" \
"ds_read_b128 v[68:71], v31 offset:16\n" \
"s_waitcnt lgkmcnt(1)\n" \
"v_pk_add_f32 v[64:65], v[64:65], v[66:67]\n" \
"s_waitcnt lgkmcnt(0)\n" \
"v_pk_add_f32 v[68:69], v[68:69], v[70:71]\n" \
"v_pk_add_f32 v[64:65], v[64:65], v[68:69]\n" \
"v_cvt_pk_bf16_f32 v72, v64, v65\n" \
"v_max_u32 v72, 1, v72\n" \
"s_nop 1\n" \
"v_mov_b32_dpp v73, v72 quad_perm:[1,1,1,1] row_mask:0xf bank_mask:0xf\n" \
"v_mov_b32_dpp v74, v72 quad_perm:[2,2,2,2] row_mask:0xf bank_mask:0xf\n" \
"v_mov_b32_dpp v75, v72 quad_perm:[3,3,3,3] row_mask:0xf bank_mask:0xf\n" \
".Lv_skip_%=:\n" \
"buffer_store_dwordx4 v[72:75], v8, s[36:39], s77 offen sc1\n" \
"s_memrealtime s[24:25]\n" \
"s_mov_b32 s54, s58\n" \
"s_mov_b32 s58, s59\n" \
"s_mov_b32 s59, s60\n" \
"s_mov_b32 s60, s61\n" \
"s_mov_b32 s61, s54\n" \
"s_memrealtime s[26:27]\n" \
"s_add_u32 s78, s77, s63\n" \
"s_xor_b32 s89, s78, 0x4800\n" \
"s_xor_b32 s90, s78, 0x800\n" \
"s_xor_b32 s91, s78, 0x4000\n" \
"s_mov_b32 m0, s79\n" \
"s_memrealtime s[28:29]\n" \
"s_add_u32 s51, s50, s46\n" \
"v_mov_b64 v[80:81], 0\n" \
"v_mov_b64 v[82:83], 0\n" \
"v_mov_b32 v13, -1\n" \
"s_memrealtime s[30:31]\n" \
"s_add_u32 s49, s49, 1\n" \
"s_waitcnt lgkmcnt(0)\n" \
"s_cmp_lg_u32 s48, 0\n" \
"s_nop 0\n" \
"s_nop 0\n" \
"s_nop 0\n" \
"s_cbranch_scc1 .Lv_w4_%=\n" \
"s_branch .Lv_loop_%=\n" \
".Lv_h_cold_%=:\n" \
"s_nop 7\n" \
"s_nop 7\n" \
"s_waitcnt vmcnt(0) lgkmcnt(0)\n" \
"s_mov_b32 s53, 0\n" \
".Lv_h_again_%=:\n" \
"s_cmp_lg_u32 s52, 0\n" \
"s_cbranch_scc1 .Lv_h_ok_%=\n" \
"s_sleep 1\n" \
"buffer_load_dwordx2 v[64:65], v9, s[36:39], s75 offen sc1\n" \
"s_waitcnt vmcnt(0)\n" \
"v_cmp_eq_u32 vcc, 0, v64\n" \
"s_nop 0\n" \
"s_and_b64 vcc, vcc, s[80:81]\n" \
"s_nop 4\n" \
"s_cbranch_vccz .Lv_h_ok_%=\n" \
"s_add_u32 s53, s53, 1\n" \
"s_cmp_lt_u32 s53, 32768\n" \
"s_cbranch_scc1 .Lv_h_again_%=\n" \
"s_mov_b32 s52, 1\n" \
"v_mov_b32 v10, 256\n" \
"v_mov_b32 v11, 1\n" \
"buffer_store_dword v11, v10, s[36:39], 0 offen sc1\n" \
"s_waitcnt vmcnt(0) lgkmcnt(0)\n" \
"s_branch .Lv_h_ok_%=\n" \
".Lv_retry_%=:\n" \
"s_nop 7\n" \
"s_nop 7\n" \
"s_cmp_lg_u32 s52, 0\n" \
"s_cbranch_scc1 .Lv_dead_%=\n" \
"v_mov_b64 v[80:81], 0\n" \
"v_mov_b64 v[82:83], 0\n" \
"v_mov_b64 v[84:85], 0\n" \
"v_mov_b64 v[86:87], 0\n" \
"v_mov_b64 v[88:89], 0\n" \
"v_mov_b64 v[90:91], 0\n" \
"v_mov_b64 v[92:93], 0\n" \
"v_mov_b64 v[94:95], 0\n" \
"v_mov_b32 v13, -1\n" \
"s_mov_b32 s54, 100\n" \
"s_add_u32 s76, s76, 1\n" \
"s_cmp_lt_u32 s76, 32768\n" \
"s_cbranch_scc1 .Lv_o_%=\n" \
"s_mov_b32 s52, 1\n" \
"v_mov_b32 v13, 256\n" \
"v_mov_b32 v14, 1\n" \
"buffer_store_dword v14, v13, s[36:39], 0 offen sc1\n" \
".Lv_dead_%=:\n" \
"s_waitcnt vmcnt(0) lgkmcnt(0)\n" \
"s_branch .Lv_y2_%=\n" \
".Lv_mretry_%=:\n" \
"s_nop 7\n" \
"s_nop 7\n" \
"v_mov_b64 v[80:81], 0\n" \
"v_mov_b64 v[82:83], 0\n" \
"v_mov_b32 v13, -1\n" \
"s_mov_b32 m0, s79\n" \
"s_waitcnt vmcnt(0) lgkmcnt(0)\n" \
"s_barrier\n" \
"s_add_u32 s88, s88, 1\n" \
"s_cmp_lt_u32 s88, 32768\n" \
"s_cbranch_scc1 .Lv_r1_%=\n" \
"s_mov_b32 s52, 1\n" \
"v_mov_b32 v13, 256\n" \
"v_mov_b32 v14, 1\n" \
"buffer_store_dword v14, v13, s[36:39], 0 offen sc1\n" \
"s_waitcnt vmcnt(0) lgkmcnt(0)\n" \
"s_endpgm\n" \
".Lv_r1_%=:\n" \
"s_cmp_lg_u32 s49, 0\n" \
"s_cbranch_scc1 .Lv_r2_%=\n" \
"s_sleep 8\n" \
".Lv_r2_%=:\n" \
"s_branch .Lv_go_%=\n" \
".Lv_end_%=:\n" \
"s_nop 7\n" \
"s_nop 7\n" \
"s_waitcnt vmcnt(0) lgkmcnt(0)\n" \
"s_waitcnt vmcnt(0) lgkmcnt(0)\n" \
".Lv_fin_%=:\n" \
"s_endpgm\n"
#define ASM_S64_WO \
"s_nop 7\n" \
"s_nop 7\n" \
"s_lshl_b32 s52, s48, 15\n" \
"s_lshl_b32 s53, s48, 13\n" \
"s_add_u32 s53, s53, 98304\n" \
"s_sub_u32 s54, s53, s52\n" \
"v_add_u32 v9, s54, v1\n" \
"s_add_u32 m0, s52, 0\n" \
"s_mov_b32 s54, 0\n" \
"buffer_load_dwordx4 v0, s[40:43], s54 offen sc1 lds\n" \
"s_add_u32 m0, s52, 1024\n" \
"s_mov_b32 s54, 16384\n" \
"buffer_load_dwordx4 v0, s[40:43], s54 offen sc1 lds\n" \
"s_add_u32 m0, s52, 2048\n" \
"s_mov_b32 s54, 32768\n" \
"buffer_load_dwordx4 v0, s[40:43], s54 offen sc1 lds\n" \
"s_add_u32 m0, s52, 3072\n" \
"s_mov_b32 s54, 49152\n" \
"buffer_load_dwordx4 v0, s[40:43], s54 offen sc1 lds\n" \
"s_mov_b32 s54, s99\n" \
".Lswo_z0_%=:\n" \
"s_sleep 1\n" \
"s_sub_u32 s54, s54, 1\n" \
"s_cbranch_scc0 .Lswo_z0_%=\n" \
"s_add_u32 m0, s52, 4096\n" \
"s_mov_b32 s54, 65536\n" \
"buffer_load_dwordx4 v0, s[40:43], s54 offen sc1 lds\n" \
"s_add_u32 m0, s52, 5120\n" \
"s_mov_b32 s54, 81920\n" \
"buffer_load_dwordx4 v0, s[40:43], s54 offen sc1 lds\n" \
"s_add_u32 m0, s52, 6144\n" \
"s_mov_b32 s54, 98304\n" \
"buffer_load_dwordx4 v0, s[40:43], s54 offen sc1 lds\n" \
"s_add_u32 m0, s52, 7168\n" \
"s_mov_b32 s54, 114688\n" \
"buffer_load_dwordx4 v0, s[40:43], s54 offen sc1 lds\n" \
"s_mov_b32 s54, s99\n" \
".Lswo_z1_%=:\n" \
"s_sleep 1\n" \
"s_sub_u32 s54, s54, 1\n" \
"s_cbranch_scc0 .Lswo_z1_%=\n" \
"s_add_u32 m0, s52, 8192\n" \
"s_mov_b32 s54, 131072\n" \
"buffer_load_dwordx4 v0, s[40:43], s54 offen sc1 lds\n" \
"s_add_u32 m0, s52, 9216\n" \
"s_mov_b32 s54, 147456\n" \
"buffer_load_dwordx4 v0, s[40:43], s54 offen sc1 lds\n" \
"s_add_u32 m0, s52, 10240\n" \
"s_mov_b32 s54, 163840\n" \
"buffer_load_dwordx4 v0, s[40:43], s54 offen sc1 lds\n" \
"s_add_u32 m0, s52, 11264\n" \
"s_mov_b32 s54, 180224\n" \
"buffer_load_dwordx4 v0, s[40:43], s54 offen sc1 lds\n" \
"s_mov_b32 s54, s99\n" \
".Lswo_z2_%=:\n" \
"s_sleep 1\n" \
"s_sub_u32 s54, s54, 1\n" \
"s_cbranch_scc0 .Lswo_z2_%=\n" \
"s_add_u32 m0, s52, 12288\n" \
"s_mov_b32 s54, 196608\n" \
"buffer_load_dwordx4 v0, s[40:43], s54 offen sc1 lds\n" \
"s_add_u32 m0, s52, 13312\n" \
"s_mov_b32 s54, 212992\n" \
"buffer_load_dwordx4 v0, s[40:43], s54 offen sc1 lds\n" \
"s_add_u32 m0, s52, 14336\n" \
"s_mov_b32 s54, 229376\n" \
"buffer_load_dwordx4 v0, s[40:43], s54 offen sc1 lds\n" \
"s_add_u32 m0, s52, 15360\n" \
"s_mov_b32 s54, 245760\n" \
"buffer_load_dwordx4 v0, s[40:43], s54 offen sc1 lds\n" \
"s_mov_b32 s54, s99\n" \
".Lswo_z3_%=:\n" \
"s_sleep 1\n" \
"s_sub_u32 s54, s54, 1\n" \
"s_cbranch_scc0 .Lswo_z3_%=\n" \
"s_add_u32 m0, s52, 16384\n" \
"s_mov_b32 s54, 262144\n" \
"buffer_load_dwordx4 v0, s[40:43], s54 offen sc1 lds\n" \
"s_add_u32 m0, s52, 17408\n" \
"s_mov_b32 s54, 278528\n" \
"buffer_load_dwordx4 v0, s[40:43], s54 offen sc1 lds\n" \
"s_add_u32 m0, s52, 18432\n" \
"s_mov_b32 s54, 294912\n" \
"buffer_load_dwordx4 v0, s[40:43], s54 offen sc1 lds\n" \
"s_add_u32 m0, s52, 19456\n" \
"s_mov_b32 s54, 311296\n" \
"buffer_load_dwordx4 v0, s[40:43], s54 offen sc1 lds\n" \
"s_mov_b32 s54, s99\n" \
".Lswo_z4_%=:\n" \
"s_sleep 1\n" \
"s_sub_u32 s54, s54, 1\n" \
"s_cbranch_scc0 .Lswo_z4_%=\n" \
"s_add_u32 m0, s52, 20480\n" \
"s_mov_b32 s54, 327680\n" \
"buffer_load_dwordx4 v0, s[40:43], s54 offen sc1 lds\n" \
"s_add_u32 m0, s52, 21504\n" \
"s_mov_b32 s54, 344064\n" \
"buffer_load_dwordx4 v0, s[40:43], s54 offen sc1 lds\n" \
"s_add_u32 m0, s52, 22528\n" \
"s_mov_b32 s54, 360448\n" \
"buffer_load_dwordx4 v0, s[40:43], s54 offen sc1 lds\n" \
"s_add_u32 m0, s52, 23552\n" \
"s_mov_b32 s54, 376832\n" \
"buffer_load_dwordx4 v0, s[40:43], s54 offen sc1 lds\n" \
"s_mov_b32 s54, s99\n" \
".Lswo_z5_%=:\n" \
"s_sleep 1\n" \
"s_sub_u32 s54, s54, 1\n" \
"s_cbranch_scc0 .Lswo_z5_%=\n" \
"s_add_u32 m0, s52, 24576\n" \
"s_mov_b32 s54, 393216\n" \
"buffer_load_dwordx4 v0, s[40:43], s54 offen sc1 lds\n" \
"s_add_u32 m0, s52, 25600\n" \
"s_mov_b32 s54, 409600\n" \
"buffer_load_dwordx4 v0, s[40:43], s54 offen sc1 lds\n" \
"s_add_u32 m0, s52, 26624\n" \
"s_mov_b32 s54, 425984\n" \
"buffer_load_dwordx4 v0, s[40:43], s54 offen sc1 lds\n" \
"s_add_u32 m0, s52, 27648\n" \
"s_mov_b32 s54, 442368\n" \
"buffer_load_dwordx4 v0, s[40:43], s54 offen sc1 lds\n" \
"s_mov_b32 s54, s99\n" \
".Lswo_z6_%=:\n" \
"s_sleep 1\n" \
"s_sub_u32 s54, s54, 1\n" \
"s_cbranch_scc0 .Lswo_z6_%=\n" \
"s_add_u32 m0, s52, 28672\n" \
"s_mov_b32 s54, 458752\n" \
"buffer_load_dwordx4 v0, s[40:43], s54 offen sc1 lds\n" \
"s_add_u32 m0, s52, 29696\n" \
"s_mov_b32 s54, 475136\n" \
"buffer_load_dwordx4 v0, s[40:43], s54 offen sc1 lds\n" \
"s_add_u32 m0, s52, 30720\n" \
"s_mov_b32 s54, 491520\n" \
"buffer_load_dwordx4 v0, s[40:43], s54 offen sc1 lds\n" \
"s_add_u32 m0, s52, 31744\n" \
"s_mov_b32 s54, 507904\n" \
"buffer_load_dwordx4 v0, s[40:43], s54 offen sc1 lds\n" \
"s_mov_b32 s54, s99\n" \
".Lswo_z7_%=:\n" \
"s_sleep 1\n" \
"s_sub_u32 s54, s54, 1\n" \
"s_cbranch_scc0 .Lswo_z7_%=\n" \
"s_add_u32 m0, s53, 32768\n" \
"s_mov_b32 s54, 524288\n" \
"buffer_load_dwordx4 v0, s[40:43], s54 offen sc1 lds\n" \
"s_add_u32 m0, s53, 33792\n" \
"s_mov_b32 s54, 540672\n" \
"buffer_load_dwordx4 v0, s[40:43], s54 offen sc1 lds\n" \
"s_add_u32 m0, s53, 34816\n" \
"s_mov_b32 s54, 557056\n" \
"buffer_load_dwordx4 v0, s[40:43], s54 offen sc1 lds\n" \
"s_add_u32 m0, s53, 35840\n" \
"s_mov_b32 s54, 573440\n" \
"buffer_load_dwordx4 v0, s[40:43], s54 offen sc1 lds\n" \
"s_mov_b32 s54, s99\n" \
".Lswo_z8_%=:\n" \
"s_sleep 1\n" \
"s_sub_u32 s54, s54, 1\n" \
"s_cbranch_scc0 .Lswo_z8_%=\n" \
"s_add_u32 m0, s53, 36864\n" \
"s_mov_b32 s54, 589824\n" \
"buffer_load_dwordx4 v0, s[40:43], s54 offen sc1 lds\n" \
"s_add_u32 m0, s53, 37888\n" \
"s_mov_b32 s54, 606208\n" \
"buffer_load_dwordx4 v0, s[40:43], s54 offen sc1 lds\n" \
"s_add_u32 m0, s53, 38912\n" \
"s_mov_b32 s54, 622592\n" \
"buffer_load_dwordx4 v0, s[40:43], s54 offen sc1 lds\n" \
"s_add_u32 m0, s53, 39936\n" \
"s_mov_b32 s54, 638976\n" \
"buffer_load_dwordx4 v0, s[40:43], s54 offen sc1 lds\n" \
"s_mov_b32 s54, s99\n" \
".Lswo_z9_%=:\n" \
"s_sleep 1\n" \
"s_sub_u32 s54, s54, 1\n" \
"s_cbranch_scc0 .Lswo_z9_%=\n" \
"s_waitcnt vmcnt(36)\n" \
"ds_read_b64_tr_b16 a[0:1], v1 offset:0\n" \
"ds_read_b64_tr_b16 a[2:3], v1 offset:2048\n" \
"ds_read_b64_tr_b16 a[128:129], v1 offset:32\n" \
"ds_read_b64_tr_b16 a[130:131], v1 offset:2080\n" \
"ds_read_b64_tr_b16 v[128:129], v1 offset:64\n" \
"ds_read_b64_tr_b16 v[130:131], v1 offset:2112\n" \
"ds_read_b64_tr_b16 v[16:17], v1 offset:96\n" \
"ds_read_b64_tr_b16 v[18:19], v1 offset:2144\n" \
"s_waitcnt lgkmcnt(0)\n" \
"s_add_u32 m0, s52, 0\n" \
"s_mov_b32 s54, 655360\n" \
"buffer_load_dwordx4 v0, s[40:43], s54 offen sc1 lds\n" \
"s_add_u32 m0, s52, 1024\n" \
"s_mov_b32 s54, 671744\n" \
"buffer_load_dwordx4 v0, s[40:43], s54 offen sc1 lds\n" \
"s_add_u32 m0, s52, 2048\n" \
"s_mov_b32 s54, 688128\n" \
"buffer_load_dwordx4 v0, s[40:43], s54 offen sc1 lds\n" \
"s_add_u32 m0, s52, 3072\n" \
"s_mov_b32 s54, 704512\n" \
"buffer_load_dwordx4 v0, s[40:43], s54 offen sc1 lds\n" \
"s_mov_b32 s54, s99\n" \
".Lswo_z10_%=:\n" \
"s_sleep 1\n" \
"s_sub_u32 s54, s54, 1\n" \
"s_cbranch_scc0 .Lswo_z10_%=\n" \
"s_waitcnt vmcnt(36)\n" \
"ds_read_b64_tr_b16 a[4:5], v1 offset:4096\n" \
"ds_read_b64_tr_b16 a[6:7], v1 offset:6144\n" \
"ds_read_b64_tr_b16 a[132:133], v1 offset:4128\n" \
"ds_read_b64_tr_b16 a[134:135], v1 offset:6176\n" \
"ds_read_b64_tr_b16 v[132:133], v1 offset:4160\n" \
"ds_read_b64_tr_b16 v[134:135], v1 offset:6208\n" \
"ds_read_b64_tr_b16 v[20:21], v1 offset:4192\n" \
"ds_read_b64_tr_b16 v[22:23], v1 offset:6240\n" \
"s_waitcnt lgkmcnt(0)\n" \
"s_add_u32 m0, s52, 4096\n" \
"s_mov_b32 s54, 720896\n" \
"buffer_load_dwordx4 v0, s[40:43], s54 offen sc1 lds\n" \
"s_add_u32 m0, s52, 5120\n" \
"s_mov_b32 s54, 737280\n" \
"buffer_load_dwordx4 v0, s[40:43], s54 offen sc1 lds\n" \
"s_add_u32 m0, s52, 6144\n" \
"s_mov_b32 s54, 753664\n" \
"buffer_load_dwordx4 v0, s[40:43], s54 offen sc1 lds\n" \
"s_add_u32 m0, s52, 7168\n" \
"s_mov_b32 s54, 770048\n" \
"buffer_load_dwordx4 v0, s[40:43], s54 offen sc1 lds\n" \
"s_mov_b32 s54, s99\n" \
".Lswo_z11_%=:\n" \
"s_sleep 1\n" \
"s_sub_u32 s54, s54, 1\n" \
"s_cbranch_scc0 .Lswo_z11_%=\n" \
"s_waitcnt vmcnt(36)\n" \
"ds_read_b64_tr_b16 a[8:9], v1 offset:8192\n" \
"ds_read_b64_tr_b16 a[10:11], v1 offset:10240\n" \
"ds_read_b64_tr_b16 a[136:137], v1 offset:8224\n" \
"ds_read_b64_tr_b16 a[138:139], v1 offset:10272\n" \
"ds_read_b64_tr_b16 v[136:137], v1 offset:8256\n" \
"ds_read_b64_tr_b16 v[138:139], v1 offset:10304\n" \
"ds_read_b64_tr_b16 v[24:25], v1 offset:8288\n" \
"ds_read_b64_tr_b16 v[26:27], v1 offset:10336\n" \
"s_waitcnt lgkmcnt(0)\n" \
"s_add_u32 m0, s52, 8192\n" \
"s_mov_b32 s54, 786432\n" \
"buffer_load_dwordx4 v0, s[40:43], s54 offen sc1 lds\n" \
"s_add_u32 m0, s52, 9216\n" \
"s_mov_b32 s54, 802816\n" \
"buffer_load_dwordx4 v0, s[40:43], s54 offen sc1 lds\n" \
"s_add_u32 m0, s52, 10240\n" \
"s_mov_b32 s54, 819200\n" \
"buffer_load_dwordx4 v0, s[40:43], s54 offen sc1 lds\n" \
"s_add_u32 m0, s52, 11264\n" \
"s_mov_b32 s54, 835584\n" \
"buffer_load_dwordx4 v0, s[40:43], s54 offen sc1 lds\n" \
"s_mov_b32 s54, s99\n" \
".Lswo_z12_%=:\n" \
"s_sleep 1\n" \
"s_sub_u32 s54, s54, 1\n" \
"s_cbranch_scc0 .Lswo_z12_%=\n" \
"s_waitcnt vmcnt(36)\n" \
"ds_read_b64_tr_b16 a[12:13], v1 offset:12288\n" \
"ds_read_b64_tr_b16 a[14:15], v1 offset:14336\n" \
"ds_read_b64_tr_b16 a[140:141], v1 offset:12320\n" \
"ds_read_b64_tr_b16 a[142:143], v1 offset:14368\n" \
"ds_read_b64_tr_b16 v[140:141], v1 offset:12352\n" \
"ds_read_b64_tr_b16 v[142:143], v1 offset:14400\n" \
"ds_read_b64_tr_b16 v[28:29], v1 offset:12384\n" \
"ds_read_b64_tr_b16 v[30:31], v1 offset:14432\n" \
"s_waitcnt lgkmcnt(0)\n" \
"s_add_u32 m0, s52, 12288\n" \
"s_mov_b32 s54, 851968\n" \
"buffer_load_dwordx4 v0, s[40:43], s54 offen sc1 lds\n" \
"s_add_u32 m0, s52, 13312\n" \
"s_mov_b32 s54, 868352\n" \
"buffer_load_dwordx4 v0, s[40:43], s54 offen sc1 lds\n" \
"s_add_u32 m0, s52, 14336\n" \
"s_mov_b32 s54, 884736\n" \
"buffer_load_dwordx4 v0, s[40:43], s54 offen sc1 lds\n" \
"s_add_u32 m0, s52, 15360\n" \
"s_mov_b32 s54, 901120\n" \
"buffer_load_dwordx4 v0, s[40:43], s54 offen sc1 lds\n" \
"s_mov_b32 s54, s99\n" \
".Lswo_z13_%=:\n" \
"s_sleep 1\n" \
"s_sub_u32 s54, s54, 1\n" \
"s_cbranch_scc0 .Lswo_z13_%=\n" \
"s_waitcnt vmcnt(36)\n" \
"ds_read_b64_tr_b16 a[16:17], v1 offset:16384\n" \
"ds_read_b64_tr_b16 a[18:19], v1 offset:18432\n" \
"ds_read_b64_tr_b16 a[144:145], v1 offset:16416\n" \
"ds_read_b64_tr_b16 a[146:147], v1 offset:18464\n" \
"ds_read_b64_tr_b16 v[144:145], v1 offset:16448\n" \
"ds_read_b64_tr_b16 v[146:147], v1 offset:18496\n" \
"ds_read_b64_tr_b16 v[32:33], v1 offset:16480\n" \
"ds_read_b64_tr_b16 v[34:35], v1 offset:18528\n" \
"s_waitcnt lgkmcnt(0)\n" \
"s_add_u32 m0, s52, 16384\n" \
"s_mov_b32 s54, 917504\n" \
"buffer_load_dwordx4 v0, s[40:43], s54 offen sc1 lds\n" \
"s_add_u32 m0, s52, 17408\n" \
"s_mov_b32 s54, 933888\n" \
"buffer_load_dwordx4 v0, s[40:43], s54 offen sc1 lds\n" \
"s_add_u32 m0, s52, 18432\n" \
"s_mov_b32 s54, 950272\n" \
"buffer_load_dwordx4 v0, s[40:43], s54 offen sc1 lds\n" \
"s_add_u32 m0, s52, 19456\n" \
"s_mov_b32 s54, 966656\n" \
"buffer_load_dwordx4 v0, s[40:43], s54 offen sc1 lds\n" \
"s_mov_b32 s54, s99\n" \
".Lswo_z14_%=:\n" \
"s_sleep 1\n" \
"s_sub_u32 s54, s54, 1\n" \
"s_cbranch_scc0 .Lswo_z14_%=\n" \
"s_waitcnt vmcnt(36)\n" \
"ds_read_b64_tr_b16 a[20:21], v1 offset:20480\n" \
"ds_read_b64_tr_b16 a[22:23], v1 offset:22528\n" \
"ds_read_b64_tr_b16 a[148:149], v1 offset:20512\n" \
"ds_read_b64_tr_b16 a[150:151], v1 offset:22560\n" \
"ds_read_b64_tr_b16 v[148:149], v1 offset:20544\n" \
"ds_read_b64_tr_b16 v[150:151], v1 offset:22592\n" \
"ds_read_b64_tr_b16 v[36:37], v1 offset:20576\n" \
"ds_read_b64_tr_b16 v[38:39], v1 offset:22624\n" \
"s_waitcnt lgkmcnt(0)\n" \
"s_add_u32 m0, s52, 20480\n" \
"s_mov_b32 s54, 983040\n" \
"buffer_load_dwordx4 v0, s[40:43], s54 offen sc1 lds\n" \
"s_add_u32 m0, s52, 21504\n" \
"s_mov_b32 s54, 999424\n" \
"buffer_load_dwordx4 v0, s[40:43], s54 offen sc1 lds\n" \
"s_add_u32 m0, s52, 22528\n" \
"s_mov_b32 s54, 1015808\n" \
"buffer_load_dwordx4 v0, s[40:43], s54 offen sc1 lds\n" \
"s_add_u32 m0, s52, 23552\n" \
"s_mov_b32 s54, 1032192\n" \
"buffer_load_dwordx4 v0, s[40:43], s54 offen sc1 lds\n" \
"s_mov_b32 s54, s99\n" \
".Lswo_z15_%=:\n" \
"s_sleep 1\n" \
"s_sub_u32 s54, s54, 1\n" \
"s_cbranch_scc0 .Lswo_z15_%=\n" \
"s_waitcnt vmcnt(36)\n" \
"ds_read_b64_tr_b16 a[24:25], v1 offset:24576\n" \
"ds_read_b64_tr_b16 a[26:27], v1 offset:26624\n" \
"ds_read_b64_tr_b16 a[152:153], v1 offset:24608\n" \
"ds_read_b64_tr_b16 a[154:155], v1 offset:26656\n" \
"ds_read_b64_tr_b16 v[152:153], v1 offset:24640\n" \
"ds_read_b64_tr_b16 v[154:155], v1 offset:26688\n" \
"ds_read_b64_tr_b16 v[40:41], v1 offset:24672\n" \
"ds_read_b64_tr_b16 v[42:43], v1 offset:26720\n" \
"s_waitcnt lgkmcnt(0)\n" \
"s_add_u32 m0, s52, 24576\n" \
"s_mov_b32 s54, 1048576\n" \
"buffer_load_dwordx4 v0, s[40:43], s54 offen sc1 lds\n" \
"s_add_u32 m0, s52, 25600\n" \
"s_mov_b32 s54, 1064960\n" \
"buffer_load_dwordx4 v0, s[40:43], s54 offen sc1 lds\n" \
"s_add_u32 m0, s52, 26624\n" \
"s_mov_b32 s54, 1081344\n" \
"buffer_load_dwordx4 v0, s[40:43], s54 offen sc1 lds\n" \
"s_add_u32 m0, s52, 27648\n" \
"s_mov_b32 s54, 1097728\n" \
"buffer_load_dwordx4 v0, s[40:43], s54 offen sc1 lds\n" \
"s_mov_b32 s54, s99\n" \
".Lswo_z16_%=:\n" \
"s_sleep 1\n" \
"s_sub_u32 s54, s54, 1\n" \
"s_cbranch_scc0 .Lswo_z16_%=\n" \
"s_waitcnt vmcnt(36)\n" \
"ds_read_b64_tr_b16 a[28:29], v1 offset:28672\n" \
"ds_read_b64_tr_b16 a[30:31], v1 offset:30720\n" \
"ds_read_b64_tr_b16 a[156:157], v1 offset:28704\n" \
"ds_read_b64_tr_b16 a[158:159], v1 offset:30752\n" \
"ds_read_b64_tr_b16 v[156:157], v1 offset:28736\n" \
"ds_read_b64_tr_b16 v[158:159], v1 offset:30784\n" \
"ds_read_b64_tr_b16 v[44:45], v1 offset:28768\n" \
"ds_read_b64_tr_b16 v[46:47], v1 offset:30816\n" \
"s_waitcnt lgkmcnt(0)\n" \
"s_add_u32 m0, s52, 28672\n" \
"s_mov_b32 s54, 1114112\n" \
"buffer_load_dwordx4 v0, s[40:43], s54 offen sc1 lds\n" \
"s_add_u32 m0, s52, 29696\n" \
"s_mov_b32 s54, 1130496\n" \
"buffer_load_dwordx4 v0, s[40:43], s54 offen sc1 lds\n" \
"s_add_u32 m0, s52, 30720\n" \
"s_mov_b32 s54, 1146880\n" \
"buffer_load_dwordx4 v0, s[40:43], s54 offen sc1 lds\n" \
"s_add_u32 m0, s52, 31744\n" \
"s_mov_b32 s54, 1163264\n" \
"buffer_load_dwordx4 v0, s[40:43], s54 offen sc1 lds\n" \
"s_mov_b32 s54, s99\n" \
".Lswo_z17_%=:\n" \
"s_sleep 1\n" \
"s_sub_u32 s54, s54, 1\n" \
"s_cbranch_scc0 .Lswo_z17_%=\n" \
"s_waitcnt vmcnt(36)\n" \
"ds_read_b64_tr_b16 a[32:33], v9 offset:32768\n" \
"ds_read_b64_tr_b16 a[34:35], v9 offset:34816\n" \
"ds_read_b64_tr_b16 a[160:161], v9 offset:32800\n" \
"ds_read_b64_tr_b16 a[162:163], v9 offset:34848\n" \
"ds_read_b64_tr_b16 v[160:161], v9 offset:32832\n" \
"ds_read_b64_tr_b16 v[162:163], v9 offset:34880\n" \
"ds_read_b64_tr_b16 v[48:49], v9 offset:32864\n" \
"ds_read_b64_tr_b16 v[50:51], v9 offset:34912\n" \
"s_waitcnt lgkmcnt(0)\n" \
"s_add_u32 m0, s53, 32768\n" \
"s_mov_b32 s54, 1179648\n" \
"buffer_load_dwordx4 v0, s[40:43], s54 offen sc1 lds\n" \
"s_add_u32 m0, s53, 33792\n" \
"s_mov_b32 s54, 1196032\n" \
"buffer_load_dwordx4 v0, s[40:43], s54 offen sc1 lds\n" \
"s_add_u32 m0, s53, 34816\n" \
"s_mov_b32 s54, 1212416\n" \
"buffer_load_dwordx4 v0, s[40:43], s54 offen sc1 lds\n" \
"s_add_u32 m0, s53, 35840\n" \
"s_mov_b32 s54, 1228800\n" \
"buffer_load_dwordx4 v0, s[40:43], s54 offen sc1 lds\n" \
"s_mov_b32 s54, s99\n" \
".Lswo_z18_%=:\n" \
"s_sleep 1\n" \
"s_sub_u32 s54, s54, 1\n" \
"s_cbranch_scc0 .Lswo_z18_%=\n" \
"s_waitcnt vmcnt(36)\n" \
"ds_read_b64_tr_b16 a[36:37], v9 offset:36864\n" \
"ds_read_b64_tr_b16 a[38:39], v9 offset:38912\n" \
"ds_read_b64_tr_b16 a[164:165], v9 offset:36896\n" \
"ds_read_b64_tr_b16 a[166:167], v9 offset:38944\n" \
"ds_read_b64_tr_b16 v[164:165], v9 offset:36928\n" \
"ds_read_b64_tr_b16 v[166:167], v9 offset:38976\n" \
"ds_read_b64_tr_b16 v[52:53], v9 offset:36960\n" \
"ds_read_b64_tr_b16 v[54:55], v9 offset:39008\n" \
"s_waitcnt lgkmcnt(0)\n" \
"s_add_u32 m0, s53, 36864\n" \
"s_mov_b32 s54, 1245184\n" \
"buffer_load_dwordx4 v0, s[40:43], s54 offen sc1 lds\n" \
"s_add_u32 m0, s53, 37888\n" \
"s_mov_b32 s54, 1261568\n" \
"buffer_load_dwordx4 v0, s[40:43], s54 offen sc1 lds\n" \
"s_add_u32 m0, s53, 38912\n" \
"s_mov_b32 s54, 1277952\n" \
"buffer_load_dwordx4 v0, s[40:43], s54 offen sc1 lds\n" \
"s_add_u32 m0, s53, 39936\n" \
"s_mov_b32 s54, 1294336\n" \
"buffer_load_dwordx4 v0, s[40:43], s54 offen sc1 lds\n" \
"s_mov_b32 s54, s99\n" \
".Lswo_z19_%=:\n" \
"s_sleep 1\n" \
"s_sub_u32 s54, s54, 1\n" \
"s_cbranch_scc0 .Lswo_z19_%=\n" \
"s_waitcnt vmcnt(36)\n" \
"ds_read_b64_tr_b16 a[40:41], v1 offset:0\n" \
"ds_read_b64_tr_b16 a[42:43], v1 offset:2048\n" \
"ds_read_b64_tr_b16 a[168:169], v1 offset:32\n" \
"ds_read_b64_tr_b16 a[170:171], v1 offset:2080\n" \
"ds_read_b64_tr_b16 v[168:169], v1 offset:64\n" \
"ds_read_b64_tr_b16 v[170:171], v1 offset:2112\n" \
"ds_read_b64_tr_b16 v[56:57], v1 offset:96\n" \
"ds_read_b64_tr_b16 v[58:59], v1 offset:2144\n" \
"s_waitcnt lgkmcnt(0)\n" \
"s_add_u32 m0, s52, 0\n" \
"s_mov_b32 s54, 1310720\n" \
"buffer_load_dwordx4 v0, s[40:43], s54 offen sc1 lds\n" \
"s_add_u32 m0, s52, 1024\n" \
"s_mov_b32 s54, 1327104\n" \
"buffer_load_dwordx4 v0, s[40:43], s54 offen sc1 lds\n" \
"s_add_u32 m0, s52, 2048\n" \
"s_mov_b32 s54, 1343488\n" \
"buffer_load_dwordx4 v0, s[40:43], s54 offen sc1 lds\n" \
"s_add_u32 m0, s52, 3072\n" \
"s_mov_b32 s54, 1359872\n" \
"buffer_load_dwordx4 v0, s[40:43], s54 offen sc1 lds\n" \
"s_mov_b32 s54, s99\n" \
".Lswo_z20_%=:\n" \
"s_sleep 1\n" \
"s_sub_u32 s54, s54, 1\n" \
"s_cbranch_scc0 .Lswo_z20_%=\n" \
"s_waitcnt vmcnt(36)\n" \
"ds_read_b64_tr_b16 a[44:45], v1 offset:4096\n" \
"ds_read_b64_tr_b16 a[46:47], v1 offset:6144\n" \
"ds_read_b64_tr_b16 a[172:173], v1 offset:4128\n" \
"ds_read_b64_tr_b16 a[174:175], v1 offset:6176\n" \
"ds_read_b64_tr_b16 v[172:173], v1 offset:4160\n" \
"ds_read_b64_tr_b16 v[174:175], v1 offset:6208\n" \
"ds_read_b64_tr_b16 v[60:61], v1 offset:4192\n" \
"ds_read_b64_tr_b16 v[62:63], v1 offset:6240\n" \
"s_waitcnt lgkmcnt(0)\n" \
"s_add_u32 m0, s52, 4096\n" \
"s_mov_b32 s54, 1376256\n" \
"buffer_load_dwordx4 v0, s[40:43], s54 offen sc1 lds\n" \
"s_add_u32 m0, s52, 5120\n" \
"s_mov_b32 s54, 1392640\n" \
"buffer_load_dwordx4 v0, s[40:43], s54 offen sc1 lds\n" \
"s_add_u32 m0, s52, 6144\n" \
"s_mov_b32 s54, 1409024\n" \
"buffer_load_dwordx4 v0, s[40:43], s54 offen sc1 lds\n" \
"s_add_u32 m0, s52, 7168\n" \
"s_mov_b32 s54, 1425408\n" \
"buffer_load_dwordx4 v0, s[40:43], s54 offen sc1 lds\n" \
"s_mov_b32 s54, s99\n" \
".Lswo_z21_%=:\n" \
"s_sleep 1\n" \
"s_sub_u32 s54, s54, 1\n" \
"s_cbranch_scc0 .Lswo_z21_%=\n" \
"s_waitcnt vmcnt(36)\n" \
"ds_read_b64_tr_b16 a[48:49], v1 offset:8192\n" \
"ds_read_b64_tr_b16 a[50:51], v1 offset:10240\n" \
"ds_read_b64_tr_b16 a[176:177], v1 offset:8224\n" \
"ds_read_b64_tr_b16 a[178:179], v1 offset:10272\n" \
"ds_read_b64_tr_b16 v[176:177], v1 offset:8256\n" \
"ds_read_b64_tr_b16 v[178:179], v1 offset:10304\n" \
"ds_read_b64_tr_b16 v[64:65], v1 offset:8288\n" \
"ds_read_b64_tr_b16 v[66:67], v1 offset:10336\n" \
"s_waitcnt lgkmcnt(0)\n" \
"s_add_u32 m0, s52, 8192\n" \
"s_mov_b32 s54, 1441792\n" \
"buffer_load_dwordx4 v0, s[40:43], s54 offen sc1 lds\n" \
"s_add_u32 m0, s52, 9216\n" \
"s_mov_b32 s54, 1458176\n" \
"buffer_load_dwordx4 v0, s[40:43], s54 offen sc1 lds\n" \
"s_add_u32 m0, s52, 10240\n" \
"s_mov_b32 s54, 1474560\n" \
"buffer_load_dwordx4 v0, s[40:43], s54 offen sc1 lds\n" \
"s_add_u32 m0, s52, 11264\n" \
"s_mov_b32 s54, 1490944\n" \
"buffer_load_dwordx4 v0, s[40:43], s54 offen sc1 lds\n" \
"s_mov_b32 s54, s99\n" \
".Lswo_z22_%=:\n" \
"s_sleep 1\n" \
"s_sub_u32 s54, s54, 1\n" \
"s_cbranch_scc0 .Lswo_z22_%=\n" \
"s_waitcnt vmcnt(36)\n" \
"ds_read_b64_tr_b16 a[52:53], v1 offset:12288\n" \
"ds_read_b64_tr_b16 a[54:55], v1 offset:14336\n" \
"ds_read_b64_tr_b16 a[180:181], v1 offset:12320\n" \
"ds_read_b64_tr_b16 a[182:183], v1 offset:14368\n" \
"ds_read_b64_tr_b16 v[180:181], v1 offset:12352\n" \
"ds_read_b64_tr_b16 v[182:183], v1 offset:14400\n" \
"ds_read_b64_tr_b16 v[68:69], v1 offset:12384\n" \
"ds_read_b64_tr_b16 v[70:71], v1 offset:14432\n" \
"s_waitcnt lgkmcnt(0)\n" \
"s_add_u32 m0, s52, 12288\n" \
"s_mov_b32 s54, 1507328\n" \
"buffer_load_dwordx4 v0, s[40:43], s54 offen sc1 lds\n" \
"s_add_u32 m0, s52, 13312\n" \
"s_mov_b32 s54, 1523712\n" \
"buffer_load_dwordx4 v0, s[40:43], s54 offen sc1 lds\n" \
"s_add_u32 m0, s52, 14336\n" \
"s_mov_b32 s54, 1540096\n" \
"buffer_load_dwordx4 v0, s[40:43], s54 offen sc1 lds\n" \
"s_add_u32 m0, s52, 15360\n" \
"s_mov_b32 s54, 1556480\n" \
"buffer_load_dwordx4 v0, s[40:43], s54 offen sc1 lds\n" \
"s_mov_b32 s54, s99\n" \
".Lswo_z23_%=:\n" \
"s_sleep 1\n" \
"s_sub_u32 s54, s54, 1\n" \
"s_cbranch_scc0 .Lswo_z23_%=\n" \
"s_waitcnt vmcnt(36)\n" \
"ds_read_b64_tr_b16 a[56:57], v1 offset:16384\n" \
"ds_read_b64_tr_b16 a[58:59], v1 offset:18432\n" \
"ds_read_b64_tr_b16 a[184:185], v1 offset:16416\n" \
"ds_read_b64_tr_b16 a[186:187], v1 offset:18464\n" \
"ds_read_b64_tr_b16 v[184:185], v1 offset:16448\n" \
"ds_read_b64_tr_b16 v[186:187], v1 offset:18496\n" \
"ds_read_b64_tr_b16 v[72:73], v1 offset:16480\n" \
"ds_read_b64_tr_b16 v[74:75], v1 offset:18528\n" \
"s_waitcnt lgkmcnt(0)\n" \
"s_add_u32 m0, s52, 16384\n" \
"s_mov_b32 s54, 1572864\n" \
"buffer_load_dwordx4 v0, s[40:43], s54 offen sc1 lds\n" \
"s_add_u32 m0, s52, 17408\n" \
"s_mov_b32 s54, 1589248\n" \
"buffer_load_dwordx4 v0, s[40:43], s54 offen sc1 lds\n" \
"s_add_u32 m0, s52, 18432\n" \
"s_mov_b32 s54, 1605632\n" \
"buffer_load_dwordx4 v0, s[40:43], s54 offen sc1 lds\n" \
"s_add_u32 m0, s52, 19456\n" \
"s_mov_b32 s54, 1622016\n" \
"buffer_load_dwordx4 v0, s[40:43], s54 offen sc1 lds\n" \
"s_mov_b32 s54, s99\n" \
".Lswo_z24_%=:\n" \
"s_sleep 1\n" \
"s_sub_u32 s54, s54, 1\n" \
"s_cbranch_scc0 .Lswo_z24_%=\n" \
"s_waitcnt vmcnt(36)\n" \
"ds_read_b64_tr_b16 a[60:61], v1 offset:20480\n" \
"ds_read_b64_tr_b16 a[62:63], v1 offset:22528\n" \
"ds_read_b64_tr_b16 a[188:189], v1 offset:20512\n" \
"ds_read_b64_tr_b16 a[190:191], v1 offset:22560\n" \
"ds_read_b64_tr_b16 v[188:189], v1 offset:20544\n" \
"ds_read_b64_tr_b16 v[190:191], v1 offset:22592\n" \
"ds_read_b64_tr_b16 v[76:77], v1 offset:20576\n" \
"ds_read_b64_tr_b16 v[78:79], v1 offset:22624\n" \
"s_waitcnt lgkmcnt(0)\n" \
"s_add_u32 m0, s52, 20480\n" \
"s_mov_b32 s54, 1638400\n" \
"buffer_load_dwordx4 v0, s[40:43], s54 offen sc1 lds\n" \
"s_add_u32 m0, s52, 21504\n" \
"s_mov_b32 s54, 1654784\n" \
"buffer_load_dwordx4 v0, s[40:43], s54 offen sc1 lds\n" \
"s_add_u32 m0, s52, 22528\n" \
"s_mov_b32 s54, 1671168\n" \
"buffer_load_dwordx4 v0, s[40:43], s54 offen sc1 lds\n" \
"s_add_u32 m0, s52, 23552\n" \
"s_mov_b32 s54, 1687552\n" \
"buffer_load_dwordx4 v0, s[40:43], s54 offen sc1 lds\n" \
"s_mov_b32 s54, s99\n" \
".Lswo_z25_%=:\n" \
"s_sleep 1\n" \
"s_sub_u32 s54, s54, 1\n" \
"s_cbranch_scc0 .Lswo_z25_%=\n" \
"s_waitcnt vmcnt(36)\n" \
"ds_read_b64_tr_b16 a[64:65], v1 offset:24576\n" \
"ds_read_b64_tr_b16 a[66:67], v1 offset:26624\n" \
"ds_read_b64_tr_b16 a[192:193], v1 offset:24608\n" \
"ds_read_b64_tr_b16 a[194:195], v1 offset:26656\n" \
"ds_read_b64_tr_b16 v[192:193], v1 offset:24640\n" \
"ds_read_b64_tr_b16 v[194:195], v1 offset:26688\n" \
"ds_read_b64_tr_b16 v[80:81], v1 offset:24672\n" \
"ds_read_b64_tr_b16 v[82:83], v1 offset:26720\n" \
"s_waitcnt lgkmcnt(0)\n" \
"s_add_u32 m0, s52, 24576\n" \
"s_mov_b32 s54, 1703936\n" \
"buffer_load_dwordx4 v0, s[40:43], s54 offen sc1 lds\n" \
"s_add_u32 m0, s52, 25600\n" \
"s_mov_b32 s54, 1720320\n" \
"buffer_load_dwordx4 v0, s[40:43], s54 offen sc1 lds\n" \
"s_add_u32 m0, s52, 26624\n" \
"s_mov_b32 s54, 1736704\n" \
"buffer_load_dwordx4 v0, s[40:43], s54 offen sc1 lds\n" \
"s_add_u32 m0, s52, 27648\n" \
"s_mov_b32 s54, 1753088\n" \
"buffer_load_dwordx4 v0, s[40:43], s54 offen sc1 lds\n" \
"s_mov_b32 s54, s99\n" \
".Lswo_z26_%=:\n" \
"s_sleep 1\n" \
"s_sub_u32 s54, s54, 1\n" \
"s_cbranch_scc0 .Lswo_z26_%=\n" \
"s_waitcnt vmcnt(36)\n" \
"ds_read_b64_tr_b16 a[68:69], v1 offset:28672\n" \
"ds_read_b64_tr_b16 a[70:71], v1 offset:30720\n" \
"ds_read_b64_tr_b16 a[196:197], v1 offset:28704\n" \
"ds_read_b64_tr_b16 a[198:199], v1 offset:30752\n" \
"ds_read_b64_tr_b16 v[196:197], v1 offset:28736\n" \
"ds_read_b64_tr_b16 v[198:199], v1 offset:30784\n" \
"ds_read_b64_tr_b16 v[84:85], v1 offset:28768\n" \
"ds_read_b64_tr_b16 v[86:87], v1 offset:30816\n" \
"s_waitcnt lgkmcnt(0)\n" \
"s_add_u32 m0, s52, 28672\n" \
"s_mov_b32 s54, 1769472\n" \
"buffer_load_dwordx4 v0, s[40:43], s54 offen sc1 lds\n" \
"s_add_u32 m0, s52, 29696\n" \
"s_mov_b32 s54, 1785856\n" \
"buffer_load_dwordx4 v0, s[40:43], s54 offen sc1 lds\n" \
"s_add_u32 m0, s52, 30720\n" \
"s_mov_b32 s54, 1802240\n" \
"buffer_load_dwordx4 v0, s[40:43], s54 offen sc1 lds\n" \
"s_add_u32 m0, s52, 31744\n" \
"s_mov_b32 s54, 1818624\n" \
"buffer_load_dwordx4 v0, s[40:43], s54 offen sc1 lds\n" \
"s_mov_b32 s54, s99\n" \
".Lswo_z27_%=:\n" \
"s_sleep 1\n" \
"s_sub_u32 s54, s54, 1\n" \
"s_cbranch_scc0 .Lswo_z27_%=\n" \
"s_waitcnt vmcnt(36)\n" \
"ds_read_b64_tr_b16 a[72:73], v9 offset:32768\n" \
"ds_read_b64_tr_b16 a[74:75], v9 offset:34816\n" \
"ds_read_b64_tr_b16 a[200:201], v9 offset:32800\n" \
"ds_read_b64_tr_b16 a[202:203], v9 offset:34848\n" \
"ds_read_b64_tr_b16 v[200:201], v9 offset:32832\n" \
"ds_read_b64_tr_b16 v[202:203], v9 offset:34880\n" \
"ds_read_b64_tr_b16 v[88:89], v9 offset:32864\n" \
"ds_read_b64_tr_b16 v[90:91], v9 offset:34912\n" \
"s_waitcnt lgkmcnt(0)\n" \
"s_add_u32 m0, s53, 32768\n" \
"s_mov_b32 s54, 1835008\n" \
"buffer_load_dwordx4 v0, s[40:43], s54 offen sc1 lds\n" \
"s_add_u32 m0, s53, 33792\n" \
"s_mov_b32 s54, 1851392\n" \
"buffer_load_dwordx4 v0, s[40:43], s54 offen sc1 lds\n" \
"s_add_u32 m0, s53, 34816\n" \
"s_mov_b32 s54, 1867776\n" \
"buffer_load_dwordx4 v0, s[40:43], s54 offen sc1 lds\n" \
"s_add_u32 m0, s53, 35840\n" \
"s_mov_b32 s54, 1884160\n" \
"buffer_load_dwordx4 v0, s[40:43], s54 offen sc1 lds\n" \
"s_mov_b32 s54, s99\n" \
".Lswo_z28_%=:\n" \
"s_sleep 1\n" \
"s_sub_u32 s54, s54, 1\n" \
"s_cbranch_scc0 .Lswo_z28_%=\n" \
"s_waitcnt vmcnt(36)\n" \
"ds_read_b64_tr_b16 a[76:77], v9 offset:36864\n" \
"ds_read_b64_tr_b16 a[78:79], v9 offset:38912\n" \
"ds_read_b64_tr_b16 a[204:205], v9 offset:36896\n" \
"ds_read_b64_tr_b16 a[206:207], v9 offset:38944\n" \
"ds_read_b64_tr_b16 v[204:205], v9 offset:36928\n" \
"ds_read_b64_tr_b16 v[206:207], v9 offset:38976\n" \
"ds_read_b64_tr_b16 v[92:93], v9 offset:36960\n" \
"ds_read_b64_tr_b16 v[94:95], v9 offset:39008\n" \
"s_waitcnt lgkmcnt(0)\n" \
"s_add_u32 m0, s53, 36864\n" \
"s_mov_b32 s54, 1900544\n" \
"buffer_load_dwordx4 v0, s[40:43], s54 offen sc1 lds\n" \
"s_add_u32 m0, s53, 37888\n" \
"s_mov_b32 s54, 1916928\n" \
"buffer_load_dwordx4 v0, s[40:43], s54 offen sc1 lds\n" \
"s_add_u32 m0, s53, 38912\n" \
"s_mov_b32 s54, 1933312\n" \
"buffer_load_dwordx4 v0, s[40:43], s54 offen sc1 lds\n" \
"s_add_u32 m0, s53, 39936\n" \
"s_mov_b32 s54, 1949696\n" \
"buffer_load_dwordx4 v0, s[40:43], s54 offen sc1 lds\n" \
"s_mov_b32 s54, s99\n" \
".Lswo_z29_%=:\n" \
"s_sleep 1\n" \
"s_sub_u32 s54, s54, 1\n" \
"s_cbranch_scc0 .Lswo_z29_%=\n" \
"s_waitcnt vmcnt(36)\n" \
"ds_read_b64_tr_b16 a[80:81], v1 offset:0\n" \
"ds_read_b64_tr_b16 a[82:83], v1 offset:2048\n" \
"ds_read_b64_tr_b16 a[208:209], v1 offset:32\n" \
"ds_read_b64_tr_b16 a[210:211], v1 offset:2080\n" \
"ds_read_b64_tr_b16 v[208:209], v1 offset:64\n" \
"ds_read_b64_tr_b16 v[210:211], v1 offset:2112\n" \
"ds_read_b64_tr_b16 v[96:97], v1 offset:96\n" \
"ds_read_b64_tr_b16 v[98:99], v1 offset:2144\n" \
"s_waitcnt lgkmcnt(0)\n" \
"s_add_u32 m0, s52, 0\n" \
"s_mov_b32 s54, 1966080\n" \
"buffer_load_dwordx4 v0, s[40:43], s54 offen sc1 lds\n" \
"s_add_u32 m0, s52, 1024\n" \
"s_mov_b32 s54, 1982464\n" \
"buffer_load_dwordx4 v0, s[40:43], s54 offen sc1 lds\n" \
"s_add_u32 m0, s52, 2048\n" \
"s_mov_b32 s54, 1998848\n" \
"buffer_load_dwordx4 v0, s[40:43], s54 offen sc1 lds\n" \
"s_add_u32 m0, s52, 3072\n" \
"s_mov_b32 s54, 2015232\n" \
"buffer_load_dwordx4 v0, s[40:43], s54 offen sc1 lds\n" \
"s_mov_b32 s54, s99\n" \
".Lswo_z30_%=:\n" \
"s_sleep 1\n" \
"s_sub_u32 s54, s54, 1\n" \
"s_cbranch_scc0 .Lswo_z30_%=\n" \
"s_waitcnt vmcnt(36)\n" \
"ds_read_b64_tr_b16 a[84:85], v1 offset:4096\n" \
"ds_read_b64_tr_b16 a[86:87], v1 offset:6144\n" \
"ds_read_b64_tr_b16 a[212:213], v1 offset:4128\n" \
"ds_read_b64_tr_b16 a[214:215], v1 offset:6176\n" \
"ds_read_b64_tr_b16 v[212:213], v1 offset:4160\n" \
"ds_read_b64_tr_b16 v[214:215], v1 offset:6208\n" \
"ds_read_b64_tr_b16 v[100:101], v1 offset:4192\n" \
"ds_read_b64_tr_b16 v[102:103], v1 offset:6240\n" \
"s_waitcnt lgkmcnt(0)\n" \
"s_add_u32 m0, s52, 4096\n" \
"s_mov_b32 s54, 2031616\n" \
"buffer_load_dwordx4 v0, s[40:43], s54 offen sc1 lds\n" \
"s_add_u32 m0, s52, 5120\n" \
"s_mov_b32 s54, 2048000\n" \
"buffer_load_dwordx4 v0, s[40:43], s54 offen sc1 lds\n" \
"s_add_u32 m0, s52, 6144\n" \
"s_mov_b32 s54, 2064384\n" \
"buffer_load_dwordx4 v0, s[40:43], s54 offen sc1 lds\n" \
"s_add_u32 m0, s52, 7168\n" \
"s_mov_b32 s54, 2080768\n" \
"buffer_load_dwordx4 v0, s[40:43], s54 offen sc1 lds\n" \
"s_mov_b32 s54, s99\n" \
".Lswo_z31_%=:\n" \
"s_sleep 1\n" \
"s_sub_u32 s54, s54, 1\n" \
"s_cbranch_scc0 .Lswo_z31_%=\n" \
"s_waitcnt vmcnt(36)\n" \
"ds_read_b64_tr_b16 a[88:89], v1 offset:8192\n" \
"ds_read_b64_tr_b16 a[90:91], v1 offset:10240\n" \
"ds_read_b64_tr_b16 a[216:217], v1 offset:8224\n" \
"ds_read_b64_tr_b16 a[218:219], v1 offset:10272\n" \
"ds_read_b64_tr_b16 v[216:217], v1 offset:8256\n" \
"ds_read_b64_tr_b16 v[218:219], v1 offset:10304\n" \
"ds_read_b64_tr_b16 v[104:105], v1 offset:8288\n" \
"ds_read_b64_tr_b16 v[106:107], v1 offset:10336\n" \
"s_waitcnt lgkmcnt(0)\n" \
"ds_write_b128 v2, v[16:19] offset:8192\n" \
"ds_write_b128 v2, v[20:23] offset:9216\n" \
"ds_write_b128 v2, v[24:27] offset:10240\n" \
"ds_write_b128 v2, v[28:31] offset:11264\n" \
"s_waitcnt vmcnt(32)\n" \
"ds_read_b64_tr_b16 a[92:93], v1 offset:12288\n" \
"ds_read_b64_tr_b16 a[94:95], v1 offset:14336\n" \
"ds_read_b64_tr_b16 a[220:221], v1 offset:12320\n" \
"ds_read_b64_tr_b16 a[222:223], v1 offset:14368\n" \
"ds_read_b64_tr_b16 v[220:221], v1 offset:12352\n" \
"ds_read_b64_tr_b16 v[222:223], v1 offset:14400\n" \
"ds_read_b64_tr_b16 v[108:109], v1 offset:12384\n" \
"ds_read_b64_tr_b16 v[110:111], v1 offset:14432\n" \
"s_waitcnt lgkmcnt(0)\n" \
"ds_write_b128 v2, v[32:35] offset:12288\n" \
"ds_write_b128 v2, v[36:39] offset:13312\n" \
"ds_write_b128 v2, v[40:43] offset:14336\n" \
"ds_write_b128 v2, v[44:47] offset:15360\n" \
"s_waitcnt vmcnt(28)\n" \
"ds_read_b64_tr_b16 a[96:97], v1 offset:16384\n" \
"ds_read_b64_tr_b16 a[98:99], v1 offset:18432\n" \
"ds_read_b64_tr_b16 a[224:225], v1 offset:16416\n" \
"ds_read_b64_tr_b16 a[226:227], v1 offset:18464\n" \
"ds_read_b64_tr_b16 v[224:225], v1 offset:16448\n" \
"ds_read_b64_tr_b16 v[226:227], v1 offset:18496\n" \
"ds_read_b64_tr_b16 v[16:17], v1 offset:16480\n" \
"ds_read_b64_tr_b16 v[18:19], v1 offset:18528\n" \
"s_waitcnt lgkmcnt(0)\n" \
"ds_write_b128 v2, v[48:51] offset:16384\n" \
"ds_write_b128 v2, v[52:55] offset:17408\n" \
"ds_write_b128 v2, v[56:59] offset:18432\n" \
"ds_write_b128 v2, v[60:63] offset:19456\n" \
"s_waitcnt vmcnt(24)\n" \
"ds_read_b64_tr_b16 a[100:101], v1 offset:20480\n" \
"ds_read_b64_tr_b16 a[102:103], v1 offset:22528\n" \
"ds_read_b64_tr_b16 a[228:229], v1 offset:20512\n" \
"ds_read_b64_tr_b16 a[230:231], v1 offset:22560\n" \
"ds_read_b64_tr_b16 v[228:229], v1 offset:20544\n" \
"ds_read_b64_tr_b16 v[230:231], v1 offset:22592\n" \
"ds_read_b64_tr_b16 v[20:21], v1 offset:20576\n" \
"ds_read_b64_tr_b16 v[22:23], v1 offset:22624\n" \
"s_waitcnt lgkmcnt(0)\n" \
"ds_write_b128 v2, v[64:67] offset:20480\n" \
"ds_write_b128 v2, v[68:71] offset:21504\n" \
"ds_write_b128 v2, v[72:75] offset:22528\n" \
"ds_write_b128 v2, v[76:79] offset:23552\n" \
"s_waitcnt vmcnt(20)\n" \
"ds_read_b64_tr_b16 a[104:105], v1 offset:24576\n" \
"ds_read_b64_tr_b16 a[106:107], v1 offset:26624\n" \
"ds_read_b64_tr_b16 a[232:233], v1 offset:24608\n" \
"ds_read_b64_tr_b16 a[234:235], v1 offset:26656\n" \
"ds_read_b64_tr_b16 v[232:233], v1 offset:24640\n" \
"ds_read_b64_tr_b16 v[234:235], v1 offset:26688\n" \
"ds_read_b64_tr_b16 v[24:25], v1 offset:24672\n" \
"ds_read_b64_tr_b16 v[26:27], v1 offset:26720\n" \
"s_waitcnt lgkmcnt(0)\n" \
"ds_write_b128 v2, v[80:83] offset:24576\n" \
"ds_write_b128 v2, v[84:87] offset:25600\n" \
"ds_write_b128 v2, v[88:91] offset:26624\n" \
"ds_write_b128 v2, v[92:95] offset:27648\n" \
"s_waitcnt vmcnt(16)\n" \
"ds_read_b64_tr_b16 a[108:109], v1 offset:28672\n" \
"ds_read_b64_tr_b16 a[110:111], v1 offset:30720\n" \
"ds_read_b64_tr_b16 a[236:237], v1 offset:28704\n" \
"ds_read_b64_tr_b16 a[238:239], v1 offset:30752\n" \
"ds_read_b64_tr_b16 v[236:237], v1 offset:28736\n" \
"ds_read_b64_tr_b16 v[238:239], v1 offset:30784\n" \
"ds_read_b64_tr_b16 v[28:29], v1 offset:28768\n" \
"ds_read_b64_tr_b16 v[30:31], v1 offset:30816\n" \
"s_waitcnt lgkmcnt(0)\n" \
"ds_write_b128 v2, v[96:99] offset:28672\n" \
"ds_write_b128 v2, v[100:103] offset:29696\n" \
"ds_write_b128 v2, v[104:107] offset:30720\n" \
"ds_write_b128 v2, v[108:111] offset:31744\n" \
"s_waitcnt vmcnt(12)\n" \
"ds_read_b64_tr_b16 a[112:113], v9 offset:32768\n" \
"ds_read_b64_tr_b16 a[114:115], v9 offset:34816\n" \
"ds_read_b64_tr_b16 a[240:241], v9 offset:32800\n" \
"ds_read_b64_tr_b16 a[242:243], v9 offset:34848\n" \
"ds_read_b64_tr_b16 v[240:241], v9 offset:32832\n" \
"ds_read_b64_tr_b16 v[242:243], v9 offset:34880\n" \
"ds_read_b64_tr_b16 v[32:33], v9 offset:32864\n" \
"ds_read_b64_tr_b16 v[34:35], v9 offset:34912\n" \
"s_waitcnt lgkmcnt(0)\n" \
"s_waitcnt vmcnt(8)\n" \
"ds_read_b64_tr_b16 a[116:117], v9 offset:36864\n" \
"ds_read_b64_tr_b16 a[118:119], v9 offset:38912\n" \
"ds_read_b64_tr_b16 a[244:245], v9 offset:36896\n" \
"ds_read_b64_tr_b16 a[246:247], v9 offset:38944\n" \
"ds_read_b64_tr_b16 v[244:245], v9 offset:36928\n" \
"ds_read_b64_tr_b16 v[246:247], v9 offset:38976\n" \
"ds_read_b64_tr_b16 v[36:37], v9 offset:36960\n" \
"ds_read_b64_tr_b16 v[38:39], v9 offset:39008\n" \
"s_waitcnt lgkmcnt(0)\n" \
"s_waitcnt vmcnt(4)\n" \
"ds_read_b64_tr_b16 a[120:121], v1 offset:0\n" \
"ds_read_b64_tr_b16 a[122:123], v1 offset:2048\n" \
"ds_read_b64_tr_b16 a[248:249], v1 offset:32\n" \
"ds_read_b64_tr_b16 a[250:251], v1 offset:2080\n" \
"ds_read_b64_tr_b16 v[248:249], v1 offset:64\n" \
"ds_read_b64_tr_b16 v[250:251], v1 offset:2112\n" \
"ds_read_b64_tr_b16 v[40:41], v1 offset:96\n" \
"ds_read_b64_tr_b16 v[42:43], v1 offset:2144\n" \
"s_waitcnt lgkmcnt(0)\n" \
"ds_write_b128 v2, v[16:19] offset:0\n" \
"ds_write_b128 v2, v[20:23] offset:1024\n" \
"ds_write_b128 v2, v[24:27] offset:2048\n" \
"ds_write_b128 v2, v[28:31] offset:3072\n" \
"s_waitcnt vmcnt(0)\n" \
"ds_read_b64_tr_b16 a[124:125], v1 offset:4096\n" \
"ds_read_b64_tr_b16 a[126:127], v1 offset:6144\n" \
"ds_read_b64_tr_b16 a[252:253], v1 offset:4128\n" \
"ds_read_b64_tr_b16 a[254:255], v1 offset:6176\n" \
"ds_read_b64_tr_b16 v[252:253], v1 offset:4160\n" \
"ds_read_b64_tr_b16 v[254:255], v1 offset:6208\n" \
"ds_read_b64_tr_b16 v[44:45], v1 offset:4192\n" \
"ds_read_b64_tr_b16 v[46:47], v1 offset:6240\n" \
"s_waitcnt lgkmcnt(0)\n" \
"ds_write_b128 v2, v[32:35] offset:4096\n" \
"ds_write_b128 v2, v[36:39] offset:5120\n" \
"ds_write_b128 v2, v[40:43] offset:6144\n" \
"ds_write_b128 v2, v[44:47] offset:7168\n" \
"s_waitcnt vmcnt(0) lgkmcnt(0)\n" \
"s_barrier\n" \
"s_add_u32 s86, s85, 0\n" \
"s_add_u32 s87, s85, 4259840\n" \
"s_add_u32 s88, s85, 8519680\n" \
"s_add_u32 s89, s85, 12779520\n" \
"s_add_u32 s90, s85, 17039360\n" \
"s_add_u32 s91, s85, 21299200\n" \
"s_add_u32 s92, s85, 25559040\n" \
"s_add_u32 s93, s85, 29818880\n" \
"s_mov_b32 s55, 0\n" \
"s_mov_b32 s60, 0\n" \
"s_mov_b32 s79, s95\n" \
"s_mov_b32 s98, 0x70000000\n" \
".Lswo_prime_%=:\n" \
"s_lshr_b32 s59, s44, 1\n" \
"s_add_u32 s59, s59, 1\n" \
"s_lshr_b32 s56, s44, 1\n" \
"s_lshl_b32 s56, s56, 16\n" \
"s_lshr_b32 s58, s56, 8\n" \
"s_and_b32 s58, s58, 0x2000\n" \
"s_xor_b32 s56, s56, s58\n" \
"s_and_b32 s58, s44, 1\n" \
"s_lshl_b32 s58, s58, 12\n" \
"s_or_b32 s56, s56, s58\n" \
"s_lshl_b32 s54, s44, 16\n" \
"s_add_u32 s54, s54, s78\n" \
"s_lshr_b32 s58, s54, 8\n" \
"s_and_b32 s58, s58, 0x2000\n" \
"s_xor_b32 s54, s54, s58\n" \
"s_add_u32 s54, s54, 8388608\n" \
"s_lshl_b32 s77, s44, 15\n" \
"s_or_b32 s77, s77, s98\n" \
"s_or_b32 s54, s54, s98\n" \
"buffer_load_dwordx2 v[14:15], v6, s[36:39], s54 offen sc1\n" \
"buffer_load_dwordx2 v[12:13], v5, s[64:67], s77 offen\n" \
"buffer_load_dword v11, v4, s[36:39], 0 offen sc1\n" \
"s_waitcnt vmcnt(0)\n" \
"v_cmp_gt_u32_e64 s[62:63], s59, v11\n" \
"s_nop 4\n" \
"s_cmp_lg_u64 s[62:63], 0\n" \
"s_cbranch_scc1 .Lswo_w_%=\n" \
".Lswo_wk_%=:\n" \
"s_mov_b32 s76, 0x7ffffff0\n" \
"s_mov_b32 s81, 1\n" \
"s_mov_b32 s84, 0\n" \
"s_waitcnt vmcnt(0) lgkmcnt(0)\n" \
"s_xor_b32 s57, s56, s86\n" \
"buffer_load_dwordx4 v[16:19], v3, s[36:39], s57 offen offset:0 sc1\n" \
"buffer_load_dwordx4 v[20:23], v3, s[36:39], s57 offen offset:1024 sc1\n" \
"buffer_load_dwordx4 v[24:27], v3, s[36:39], s57 offen offset:2048 sc1\n" \
"buffer_load_dwordx4 v[28:31], v3, s[36:39], s57 offen offset:3072 sc1\n" \
"s_xor_b32 s57, s56, s87\n" \
"buffer_load_dwordx4 v[32:35], v3, s[36:39], s57 offen offset:0 sc1\n" \
"buffer_load_dwordx4 v[36:39], v3, s[36:39], s57 offen offset:1024 sc1\n" \
"buffer_load_dwordx4 v[40:43], v3, s[36:39], s57 offen offset:2048 sc1\n" \
"buffer_load_dwordx4 v[44:47], v3, s[36:39], s57 offen offset:3072 sc1\n" \
"s_xor_b32 s57, s56, s88\n" \
"buffer_load_dwordx4 v[48:51], v3, s[36:39], s57 offen offset:0 sc1\n" \
"buffer_load_dwordx4 v[52:55], v3, s[36:39], s57 offen offset:1024 sc1\n" \
"buffer_load_dwordx4 v[56:59], v3, s[36:39], s57 offen offset:2048 sc1\n" \
"buffer_load_dwordx4 v[60:63], v3, s[36:39], s57 offen offset:3072 sc1\n" \
"s_xor_b32 s57, s56, s89\n" \
"buffer_load_dwordx4 v[64:67], v3, s[36:39], s57 offen offset:0 sc1\n" \
"buffer_load_dwordx4 v[68:71], v3, s[36:39], s57 offen offset:1024 sc1\n" \
"buffer_load_dwordx4 v[72:75], v3, s[36:39], s57 offen offset:2048 sc1\n" \
"buffer_load_dwordx4 v[76:79], v3, s[36:39], s57 offen offset:3072 sc1\n" \
"ds_read_b128 v[80:83], v2 offset:8192\n" \
"ds_read_b128 v[84:87], v2 offset:9216\n" \
"ds_read_b128 v[88:91], v2 offset:10240\n" \
"s_waitcnt lgkmcnt(0)\n" \
"v_mov_b32 v9, 0x7ffffff0\n" \
"buffer_store_dword v9, v9, s[36:39], 0 offen\n" \
"s_waitcnt vmcnt(16)\n" \
"s_nop 7\n" \
"s_nop 7\n" \
".p2align 6\n" \
".Lswo_loop_%=:\n" \
"v_mfma_f32_16x16x32_bf16 v[96:99], a[0:3], v[16:19], 0\n" \
"ds_write_b128 v7, v[112:115] offset:0\n" \
"v_mfma_f32_16x16x32_bf16 v[100:103], a[128:131], v[16:19], 0\n" \
"ds_read_b128 v[92:95], v2 offset:11264\n" \
"v_xor_b32_e64 v7, 64, v7\n" \
"v_mfma_f32_16x16x32_bf16 v[104:107], v[128:131], v[16:19], 0\n" \
"s_waitcnt vmcnt(15) lgkmcnt(4)\n" \
"v_mfma_f32_16x16x32_bf16 v[108:111], v[80:83], v[16:19], 0\n" \
"s_xor_b32 s57, s56, s90\n" \
"buffer_load_dwordx4 v[16:19], v3, s[36:39], s57 offen offset:0 sc1\n" \
"v_mfma_f32_16x16x32_bf16 v[96:99], a[4:7], v[20:23], v[96:99]\n" \
"ds_write_b128 v7, v[116:119] offset:1024\n" \
"v_mfma_f32_16x16x32_bf16 v[100:103], a[132:135], v[20:23], v[100:103]\n" \
"ds_read_b128 v[80:83], v2 offset:12288\n" \
"s_cmp_lt_i32 s44, s50\n" \
"v_xor_b32 v7, 192, v7\n" \
"v_mfma_f32_16x16x32_bf16 v[104:107], v[132:135], v[20:23], v[104:107]\n" \
"s_waitcnt vmcnt(15) lgkmcnt(5)\n" \
"s_cselect_b32 s82, s45, s47\n" \
"v_mfma_f32_16x16x32_bf16 v[108:111], v[84:87], v[20:23], v[108:111]\n" \
"buffer_load_dwordx4 v[20:23], v3, s[36:39], s57 offen offset:1024 sc1\n" \
"v_mfma_f32_16x16x32_bf16 v[96:99], a[8:11], v[24:27], v[96:99]\n" \
"ds_write_b128 v7, v[120:123] offset:2048\n" \
"v_mfma_f32_16x16x32_bf16 v[100:103], a[136:139], v[24:27], v[100:103]\n" \
"ds_read_b128 v[84:87], v2 offset:13312\n" \
"s_add_u32 s82, s82, s44\n" \
"s_cmp_lt_u32 s82, 128\n" \
"v_xor_b32_e64 v7, 64, v7\n" \
"v_mfma_f32_16x16x32_bf16 v[104:107], v[136:139], v[24:27], v[104:107]\n" \
"s_waitcnt vmcnt(15) lgkmcnt(6)\n" \
"s_cselect_b32 s82, s82, s94\n" \
"v_mfma_f32_16x16x32_bf16 v[108:111], v[88:91], v[24:27], v[108:111]\n" \
"buffer_load_dwordx4 v[24:27], v3, s[36:39], s57 offen offset:2048 sc1\n" \
"v_mfma_f32_16x16x32_bf16 v[96:99], a[12:15], v[28:31], v[96:99]\n" \
"ds_write_b128 v7, v[124:127] offset:3072\n" \
"v_mfma_f32_16x16x32_bf16 v[100:103], a[140:143], v[28:31], v[100:103]\n" \
"ds_read_b128 v[88:91], v2 offset:14336\n" \
"s_lshr_b32 s83, s82, 1\n" \
"v_xor_b32 v7, 192, v7\n" \
"v_mfma_f32_16x16x32_bf16 v[104:107], v[140:143], v[28:31], v[104:107]\n" \
"s_waitcnt vmcnt(15) lgkmcnt(6)\n" \
"s_lshl_b32 s83, s83, 16\n" \
"v_mfma_f32_16x16x32_bf16 v[108:111], v[92:95], v[28:31], v[108:111]\n" \
"buffer_load_dwordx4 v[28:31], v3, s[36:39], s57 offen offset:3072 sc1\n" \
"v_mfma_f32_16x16x32_bf16 v[96:99], a[16:19], v[32:35], v[96:99]\n" \
"v_mfma_f32_16x16x32_bf16 v[100:103], a[144:147], v[32:35], v[100:103]\n" \
"ds_read_b128 v[92:95], v2 offset:15360\n" \
"s_lshr_b32 s58, s83, 8\n" \
"v_mfma_f32_16x16x32_bf16 v[104:107], v[144:147], v[32:35], v[104:107]\n" \
"s_waitcnt vmcnt(15) lgkmcnt(5)\n" \
"s_and_b32 s58, s58, 0x2000\n" \
"v_mfma_f32_16x16x32_bf16 v[108:111], v[80:83], v[32:35], v[108:111]\n" \
"s_xor_b32 s57, s56, s91\n" \
"buffer_load_dwordx4 v[32:35], v3, s[36:39], s57 offen offset:0 sc1\n" \
"v_mfma_f32_16x16x32_bf16 v[96:99], a[20:23], v[36:39], v[96:99]\n" \
"s_waitcnt lgkmcnt(2)\n" \
"s_cmp_lg_u32 s81, 0\n" \
"s_cbranch_scc1 .Lswo_nb_%=\n" \
"s_barrier\n" \
".Lswo_nb_%=:\n" \
"v_mfma_f32_16x16x32_bf16 v[100:103], a[148:151], v[36:39], v[100:103]\n" \
"ds_read_b128 v[80:83], v2 offset:16384\n" \
"s_xor_b32 s83, s83, s58\n" \
"v_xor_b32 v7, s84, v7\n" \
"v_mfma_f32_16x16x32_bf16 v[104:107], v[148:151], v[36:39], v[104:107]\n" \
"s_waitcnt vmcnt(15)\n" \
"s_and_b32 s58, s82, 1\n" \
"v_mfma_f32_16x16x32_bf16 v[108:111], v[84:87], v[36:39], v[108:111]\n" \
"buffer_load_dwordx4 v[36:39], v3, s[36:39], s57 offen offset:1024 sc1\n" \
"v_mfma_f32_16x16x32_bf16 v[96:99], a[24:27], v[40:43], v[96:99]\n" \
"ds_read_b128 v[112:115], v8 offset:0\n" \
"v_mfma_f32_16x16x32_bf16 v[100:103], a[152:155], v[40:43], v[100:103]\n" \
"ds_read_b128 v[84:87], v2 offset:17408\n" \
"s_lshl_b32 s58, s58, 12\n" \
"v_mfma_f32_16x16x32_bf16 v[104:107], v[152:155], v[40:43], v[104:107]\n" \
"s_waitcnt vmcnt(15) lgkmcnt(4)\n" \
"s_or_b32 s83, s83, s58\n" \
"v_mfma_f32_16x16x32_bf16 v[108:111], v[88:91], v[40:43], v[108:111]\n" \
"buffer_load_dwordx4 v[40:43], v3, s[36:39], s57 offen offset:2048 sc1\n" \
"v_mfma_f32_16x16x32_bf16 v[96:99], a[28:31], v[44:47], v[96:99]\n" \
"ds_read_b128 v[116:119], v8 offset:4096\n" \
"v_mfma_f32_16x16x32_bf16 v[100:103], a[156:159], v[44:47], v[100:103]\n" \
"ds_read_b128 v[88:91], v2 offset:18432\n" \
"v_mfma_f32_16x16x32_bf16 v[104:107], v[156:159], v[44:47], v[104:107]\n" \
"s_waitcnt vmcnt(15) lgkmcnt(5)\n" \
"v_mfma_f32_16x16x32_bf16 v[108:111], v[92:95], v[44:47], v[108:111]\n" \
"buffer_load_dwordx4 v[44:47], v3, s[36:39], s57 offen offset:3072 sc1\n" \
"v_mfma_f32_16x16x32_bf16 v[96:99], a[32:35], v[48:51], v[96:99]\n" \
"ds_read_b128 v[120:123], v8 offset:8192\n" \
"s_lshr_b32 s59, s82, 1\n" \
"s_add_u32 s59, s59, 1\n" \
"v_mfma_f32_16x16x32_bf16 v[100:103], a[160:163], v[48:51], v[100:103]\n" \
"ds_read_b128 v[92:95], v2 offset:19456\n" \
"v_mfma_f32_16x16x32_bf16 v[104:107], v[160:163], v[48:51], v[104:107]\n" \
"s_waitcnt vmcnt(15) lgkmcnt(6)\n" \
"v_mfma_f32_16x16x32_bf16 v[108:111], v[80:83], v[48:51], v[108:111]\n" \
"s_xor_b32 s57, s56, s92\n" \
"buffer_load_dwordx4 v[48:51], v3, s[36:39], s57 offen offset:0 sc1\n" \
"v_mfma_f32_16x16x32_bf16 v[96:99], a[36:39], v[52:55], v[96:99]\n" \
"ds_read_b128 v[124:127], v8 offset:12288\n" \
"v_mfma_f32_16x16x32_bf16 v[100:103], a[164:167], v[52:55], v[100:103]\n" \
"ds_read_b128 v[80:83], v2 offset:20480\n" \
"v_mfma_f32_16x16x32_bf16 v[104:107], v[164:167], v[52:55], v[104:107]\n" \
"s_waitcnt vmcnt(15) lgkmcnt(6)\n" \
"v_mfma_f32_16x16x32_bf16 v[108:111], v[84:87], v[52:55], v[108:111]\n" \
"buffer_load_dwordx4 v[52:55], v3, s[36:39], s57 offen offset:1024 sc1\n" \
"v_mfma_f32_16x16x32_bf16 v[96:99], a[40:43], v[56:59], v[96:99]\n" \
"v_mfma_f32_16x16x32_bf16 v[100:103], a[168:171], v[56:59], v[100:103]\n" \
"ds_read_b128 v[84:87], v2 offset:21504\n" \
"v_mfma_f32_16x16x32_bf16 v[104:107], v[168:171], v[56:59], v[104:107]\n" \
"s_waitcnt vmcnt(15) lgkmcnt(5)\n" \
"v_add_f32_e64 v112, v112, v116\n" \
"v_mfma_f32_16x16x32_bf16 v[108:111], v[88:91], v[56:59], v[108:111]\n" \
"buffer_load_dwordx4 v[56:59], v3, s[36:39], s57 offen offset:2048 sc1\n" \
"v_add_f32_e64 v113, v113, v117\n" \
"v_mfma_f32_16x16x32_bf16 v[96:99], a[44:47], v[60:63], v[96:99]\n" \
"v_add_f32_e64 v114, v114, v118\n" \
"v_mfma_f32_16x16x32_bf16 v[100:103], a[172:175], v[60:63], v[100:103]\n" \
"ds_read_b128 v[88:91], v2 offset:22528\n" \
"v_add_f32_e64 v115, v115, v119\n" \
"v_mfma_f32_16x16x32_bf16 v[104:107], v[172:175], v[60:63], v[104:107]\n" \
"s_waitcnt vmcnt(15) lgkmcnt(4)\n" \
"s_waitcnt lgkmcnt(3)\n" \
"v_add_f32_e64 v120, v120, v124\n" \
"v_mfma_f32_16x16x32_bf16 v[108:111], v[92:95], v[60:63], v[108:111]\n" \
"buffer_load_dwordx4 v[60:63], v3, s[36:39], s57 offen offset:3072 sc1\n" \
"v_add_f32_e64 v121, v121, v125\n" \
"v_mfma_f32_16x16x32_bf16 v[96:99], a[48:51], v[64:67], v[96:99]\n" \
"v_add_f32_e64 v122, v122, v126\n" \
"v_mfma_f32_16x16x32_bf16 v[100:103], a[176:179], v[64:67], v[100:103]\n" \
"ds_read_b128 v[92:95], v2 offset:23552\n" \
"v_add_f32_e64 v123, v123, v127\n" \
"v_mfma_f32_16x16x32_bf16 v[104:107], v[176:179], v[64:67], v[104:107]\n" \
"s_waitcnt vmcnt(14) lgkmcnt(3)\n" \
"v_add_f32 v112, v112, v120\n" \
"v_mfma_f32_16x16x32_bf16 v[108:111], v[80:83], v[64:67], v[108:111]\n" \
"s_xor_b32 s57, s56, s93\n" \
"buffer_load_dwordx4 v[64:67], v3, s[36:39], s57 offen offset:0 sc1\n" \
"v_mfma_f32_16x16x32_bf16 v[96:99], a[52:55], v[68:71], v[96:99]\n" \
"v_cmp_gt_u32_e64 s[62:63], s59, v11\n" \
"v_mfma_f32_16x16x32_bf16 v[100:103], a[180:183], v[68:71], v[100:103]\n" \
"ds_read_b128 v[80:83], v2 offset:24576\n" \
"v_add_f32 v113, v113, v121\n" \
"v_mfma_f32_16x16x32_bf16 v[104:107], v[180:183], v[68:71], v[104:107]\n" \
"s_waitcnt vmcnt(14) lgkmcnt(3)\n" \
"v_add_f32 v114, v114, v122\n" \
"v_mfma_f32_16x16x32_bf16 v[108:111], v[84:87], v[68:71], v[108:111]\n" \
"buffer_load_dwordx4 v[68:71], v3, s[36:39], s57 offen offset:1024 sc1\n" \
"v_add_f32_e64 v115, v115, v123\n" \
"v_mfma_f32_16x16x32_bf16 v[96:99], a[56:59], v[72:75], v[96:99]\n" \
"s_add_u32 s54, s55, 1\n" \
"s_cmp_lt_u32 s54, s79\n" \
"v_mfma_f32_16x16x32_bf16 v[100:103], a[184:187], v[72:75], v[100:103]\n" \
"ds_read_b128 v[84:87], v2 offset:25600\n" \
"s_cselect_b64 s[62:63], s[62:63], -1\n" \
"v_mfma_f32_16x16x32_bf16 v[104:107], v[184:187], v[72:75], v[104:107]\n" \
"s_waitcnt vmcnt(14) lgkmcnt(3)\n" \
"v_xor_b32_e64 v8, s84, v8\n" \
"v_mfma_f32_16x16x32_bf16 v[108:111], v[88:91], v[72:75], v[108:111]\n" \
"buffer_load_dwordx4 v[72:75], v3, s[36:39], s57 offen offset:2048 sc1\n" \
"v_cvt_pk_bf16_f32 v0, v112, v113\n" \
"v_mfma_f32_16x16x32_bf16 v[96:99], a[60:63], v[76:79], v[96:99]\n" \
"s_cmp_lg_u64 s[62:63], 0\n" \
"s_cselect_b32 s81, 1, 0\n" \
"v_mfma_f32_16x16x32_bf16 v[100:103], a[188:191], v[76:79], v[100:103]\n" \
"ds_read_b128 v[88:91], v2 offset:26624\n" \
"s_cselect_b32 s83, 0x70000000, s83\n" \
"v_mfma_f32_16x16x32_bf16 v[104:107], v[188:191], v[76:79], v[104:107]\n" \
"s_waitcnt vmcnt(14) lgkmcnt(3)\n" \
"v_cvt_pk_bf16_f32 v1, v114, v115\n" \
"v_mfma_f32_16x16x32_bf16 v[108:111], v[92:95], v[76:79], v[108:111]\n" \
"buffer_load_dwordx4 v[76:79], v3, s[36:39], s57 offen offset:3072 sc1\n" \
"v_mfma_f32_16x16x32_bf16 v[96:99], a[64:67], v[16:19], v[96:99]\n" \
"buffer_store_dwordx2 v[0:1], v6, s[36:39], s76 offen sc1\n" \
"v_mfma_f32_16x16x32_bf16 v[100:103], a[192:195], v[16:19], v[100:103]\n" \
"ds_read_b128 v[92:95], v2 offset:27648\n" \
"v_mfma_f32_16x16x32_bf16 v[104:107], v[192:195], v[16:19], v[104:107]\n" \
"s_waitcnt vmcnt(15) lgkmcnt(3)\n" \
"s_mov_b32 s56, s83\n" \
"v_mfma_f32_16x16x32_bf16 v[108:111], v[80:83], v[16:19], v[108:111]\n" \
"s_xor_b32 s57, s56, s86\n" \
"buffer_load_dwordx4 v[16:19], v3, s[36:39], s57 offen offset:0 sc1\n" \
"v_mfma_f32_16x16x32_bf16 v[96:99], a[68:71], v[20:23], v[96:99]\n" \
"v_mfma_f32_16x16x32_bf16 v[100:103], a[196:199], v[20:23], v[100:103]\n" \
"ds_read_b128 v[80:83], v2 offset:28672\n" \
"v_mfma_f32_16x16x32_bf16 v[104:107], v[196:199], v[20:23], v[104:107]\n" \
"s_waitcnt vmcnt(15) lgkmcnt(3)\n" \
"v_mfma_f32_16x16x32_bf16 v[108:111], v[84:87], v[20:23], v[108:111]\n" \
"buffer_load_dwordx4 v[20:23], v3, s[36:39], s57 offen offset:1024 sc1\n" \
"v_mfma_f32_16x16x32_bf16 v[96:99], a[72:75], v[24:27], v[96:99]\n" \
"v_mfma_f32_16x16x32_bf16 v[100:103], a[200:203], v[24:27], v[100:103]\n" \
"ds_read_b128 v[84:87], v2 offset:29696\n" \
"v_mfma_f32_16x16x32_bf16 v[104:107], v[200:203], v[24:27], v[104:107]\n" \
"s_waitcnt vmcnt(15) lgkmcnt(3)\n" \
"v_mfma_f32_16x16x32_bf16 v[108:111], v[88:91], v[24:27], v[108:111]\n" \
"buffer_load_dwordx4 v[24:27], v3, s[36:39], s57 offen offset:2048 sc1\n" \
"v_mfma_f32_16x16x32_bf16 v[96:99], a[76:79], v[28:31], v[96:99]\n" \
"v_mfma_f32_16x16x32_bf16 v[100:103], a[204:207], v[28:31], v[100:103]\n" \
"ds_read_b128 v[88:91], v2 offset:30720\n" \
"v_mfma_f32_16x16x32_bf16 v[104:107], v[204:207], v[28:31], v[104:107]\n" \
"s_waitcnt vmcnt(15) lgkmcnt(3)\n" \
"v_mfma_f32_16x16x32_bf16 v[108:111], v[92:95], v[28:31], v[108:111]\n" \
"buffer_load_dwordx4 v[28:31], v3, s[36:39], s57 offen offset:3072 sc1\n" \
"v_mfma_f32_16x16x32_bf16 v[96:99], a[80:83], v[32:35], v[96:99]\n" \
"v_mfma_f32_16x16x32_bf16 v[100:103], a[208:211], v[32:35], v[100:103]\n" \
"ds_read_b128 v[92:95], v2 offset:31744\n" \
"v_mfma_f32_16x16x32_bf16 v[104:107], v[208:211], v[32:35], v[104:107]\n" \
"s_waitcnt vmcnt(15) lgkmcnt(3)\n" \
"v_mfma_f32_16x16x32_bf16 v[108:111], v[80:83], v[32:35], v[108:111]\n" \
"s_xor_b32 s57, s56, s87\n" \
"buffer_load_dwordx4 v[32:35], v3, s[36:39], s57 offen offset:0 sc1\n" \
"v_mfma_f32_16x16x32_bf16 v[96:99], a[84:87], v[36:39], v[96:99]\n" \
"v_mfma_f32_16x16x32_bf16 v[100:103], a[212:215], v[36:39], v[100:103]\n" \
"ds_read_b128 v[80:83], v2 offset:0\n" \
"v_mfma_f32_16x16x32_bf16 v[104:107], v[212:215], v[36:39], v[104:107]\n" \
"s_waitcnt vmcnt(15) lgkmcnt(3)\n" \
"v_mfma_f32_16x16x32_bf16 v[108:111], v[84:87], v[36:39], v[108:111]\n" \
"buffer_load_dwordx4 v[36:39], v3, s[36:39], s57 offen offset:1024 sc1\n" \
"v_mfma_f32_16x16x32_bf16 v[96:99], a[88:91], v[40:43], v[96:99]\n" \
"v_mfma_f32_16x16x32_bf16 v[100:103], a[216:219], v[40:43], v[100:103]\n" \
"ds_read_b128 v[84:87], v2 offset:1024\n" \
"v_mfma_f32_16x16x32_bf16 v[104:107], v[216:219], v[40:43], v[104:107]\n" \
"s_waitcnt vmcnt(15) lgkmcnt(3)\n" \
"v_mfma_f32_16x16x32_bf16 v[108:111], v[88:91], v[40:43], v[108:111]\n" \
"buffer_load_dwordx4 v[40:43], v3, s[36:39], s57 offen offset:2048 sc1\n" \
"v_mfma_f32_16x16x32_bf16 v[96:99], a[92:95], v[44:47], v[96:99]\n" \
"v_mfma_f32_16x16x32_bf16 v[100:103], a[220:223], v[44:47], v[100:103]\n" \
"ds_read_b128 v[88:91], v2 offset:2048\n" \
"v_mfma_f32_16x16x32_bf16 v[104:107], v[220:223], v[44:47], v[104:107]\n" \
"s_waitcnt vmcnt(15) lgkmcnt(3)\n" \
"v_mfma_f32_16x16x32_bf16 v[108:111], v[92:95], v[44:47], v[108:111]\n" \
"buffer_load_dwordx4 v[44:47], v3, s[36:39], s57 offen offset:3072 sc1\n" \
"v_mfma_f32_16x16x32_bf16 v[96:99], a[96:99], v[48:51], v[96:99]\n" \
"v_mfma_f32_16x16x32_bf16 v[100:103], a[224:227], v[48:51], v[100:103]\n" \
"ds_read_b128 v[92:95], v2 offset:3072\n" \
"v_mfma_f32_16x16x32_bf16 v[104:107], v[224:227], v[48:51], v[104:107]\n" \
"s_waitcnt vmcnt(15) lgkmcnt(3)\n" \
"v_mfma_f32_16x16x32_bf16 v[108:111], v[80:83], v[48:51], v[108:111]\n" \
"s_xor_b32 s57, s56, s88\n" \
"buffer_load_dwordx4 v[48:51], v3, s[36:39], s57 offen offset:0 sc1\n" \
"v_mfma_f32_16x16x32_bf16 v[96:99], a[100:103], v[52:55], v[96:99]\n" \
"v_mfma_f32_16x16x32_bf16 v[100:103], a[228:231], v[52:55], v[100:103]\n" \
"ds_read_b128 v[80:83], v2 offset:4096\n" \
"v_mfma_f32_16x16x32_bf16 v[104:107], v[228:231], v[52:55], v[104:107]\n" \
"s_waitcnt vmcnt(15) lgkmcnt(3)\n" \
"v_mfma_f32_16x16x32_bf16 v[108:111], v[84:87], v[52:55], v[108:111]\n" \
"buffer_load_dwordx4 v[52:55], v3, s[36:39], s57 offen offset:1024 sc1\n" \
"v_mfma_f32_16x16x32_bf16 v[96:99], a[104:107], v[56:59], v[96:99]\n" \
"v_mfma_f32_16x16x32_bf16 v[100:103], a[232:235], v[56:59], v[100:103]\n" \
"ds_read_b128 v[84:87], v2 offset:5120\n" \
"s_lshl_b32 s80, s44, 16\n" \
"v_mfma_f32_16x16x32_bf16 v[104:107], v[232:235], v[56:59], v[104:107]\n" \
"s_waitcnt vmcnt(15) lgkmcnt(3)\n" \
"s_add_u32 s80, s80, s78\n" \
"v_mfma_f32_16x16x32_bf16 v[108:111], v[88:91], v[56:59], v[108:111]\n" \
"buffer_load_dwordx4 v[56:59], v3, s[36:39], s57 offen offset:2048 sc1\n" \
"v_mfma_f32_16x16x32_bf16 v[96:99], a[108:111], v[60:63], v[96:99]\n" \
"v_mfma_f32_16x16x32_bf16 v[100:103], a[236:239], v[60:63], v[100:103]\n" \
"ds_read_b128 v[88:91], v2 offset:6144\n" \
"s_lshr_b32 s58, s80, 8\n" \
"v_mfma_f32_16x16x32_bf16 v[104:107], v[236:239], v[60:63], v[104:107]\n" \
"s_waitcnt vmcnt(15) lgkmcnt(3)\n" \
"s_and_b32 s58, s58, 0x2000\n" \
"v_mfma_f32_16x16x32_bf16 v[108:111], v[92:95], v[60:63], v[108:111]\n" \
"buffer_load_dwordx4 v[60:63], v3, s[36:39], s57 offen offset:3072 sc1\n" \
"v_mfma_f32_16x16x32_bf16 v[96:99], a[112:115], v[64:67], v[96:99]\n" \
"v_mfma_f32_16x16x32_bf16 v[100:103], a[240:243], v[64:67], v[100:103]\n" \
"ds_read_b128 v[92:95], v2 offset:7168\n" \
"s_xor_b32 s80, s80, s58\n" \
"v_mfma_f32_16x16x32_bf16 v[104:107], v[240:243], v[64:67], v[104:107]\n" \
"s_waitcnt vmcnt(15) lgkmcnt(3)\n" \
"v_mfma_f32_16x16x32_bf16 v[108:111], v[80:83], v[64:67], v[108:111]\n" \
"s_xor_b32 s57, s56, s89\n" \
"buffer_load_dwordx4 v[64:67], v3, s[36:39], s57 offen offset:0 sc1\n" \
"v_mfma_f32_16x16x32_bf16 v[96:99], a[116:119], v[68:71], v[96:99]\n" \
"buffer_load_dword v11, v4, s[36:39], 0 offen sc1\n" \
"v_mfma_f32_16x16x32_bf16 v[100:103], a[244:247], v[68:71], v[100:103]\n" \
"ds_read_b128 v[80:83], v2 offset:8192\n" \
"v_mfma_f32_16x16x32_bf16 v[104:107], v[244:247], v[68:71], v[104:107]\n" \
"s_waitcnt vmcnt(16) lgkmcnt(3)\n" \
"v_mfma_f32_16x16x32_bf16 v[108:111], v[84:87], v[68:71], v[108:111]\n" \
"buffer_load_dwordx4 v[68:71], v3, s[36:39], s57 offen offset:1024 sc1\n" \
"v_mfma_f32_16x16x32_bf16 v[96:99], a[120:123], v[72:75], v[96:99]\n" \
"s_add_u32 s55, s55, 1\n" \
"s_mov_b32 s84, 0x4000\n" \
"v_mfma_f32_16x16x32_bf16 v[100:103], a[248:251], v[72:75], v[100:103]\n" \
"ds_read_b128 v[84:87], v2 offset:9216\n" \
"v_mfma_f32_16x16x32_bf16 v[104:107], v[248:251], v[72:75], v[104:107]\n" \
"s_waitcnt vmcnt(16) lgkmcnt(3)\n" \
"v_mfma_f32_16x16x32_bf16 v[108:111], v[88:91], v[72:75], v[108:111]\n" \
"buffer_load_dwordx4 v[72:75], v3, s[36:39], s57 offen offset:2048 sc1\n" \
"v_mfma_f32_16x16x32_bf16 v[112:115], a[124:127], v[76:79], v[96:99]\n" \
"s_mov_b32 s76, s80\n" \
"v_mfma_f32_16x16x32_bf16 v[116:119], a[252:255], v[76:79], v[100:103]\n" \
"ds_read_b128 v[88:91], v2 offset:10240\n" \
"s_mov_b32 s44, s82\n" \
"v_mfma_f32_16x16x32_bf16 v[120:123], v[252:255], v[76:79], v[104:107]\n" \
"s_waitcnt vmcnt(15) lgkmcnt(3)\n" \
"v_mfma_f32_16x16x32_bf16 v[124:127], v[92:95], v[76:79], v[108:111]\n" \
"buffer_load_dwordx4 v[76:79], v3, s[36:39], s57 offen offset:3072 sc1\n" \
"s_cmp_eq_u32 s81, 0\n" \
"s_cbranch_scc1 .Lswo_loop_%=\n" \
"s_nop 7\n" \
"s_nop 7\n" \
"s_waitcnt vmcnt(0) lgkmcnt(0)\n" \
"ds_write_b128 v7, v[112:115] offset:0\n" \
"v_xor_b32 v7, 64, v7\n" \
"ds_write_b128 v7, v[116:119] offset:1024\n" \
"v_xor_b32 v7, 192, v7\n" \
"ds_write_b128 v7, v[120:123] offset:2048\n" \
"v_xor_b32 v7, 64, v7\n" \
"ds_write_b128 v7, v[124:127] offset:3072\n" \
"v_xor_b32 v7, 192, v7\n" \
"s_waitcnt lgkmcnt(0)\n" \
"s_barrier\n" \
"ds_read_b128 v[112:115], v8 offset:0\n" \
"ds_read_b128 v[116:119], v8 offset:4096\n" \
"ds_read_b128 v[120:123], v8 offset:8192\n" \
"ds_read_b128 v[124:127], v8 offset:12288\n" \
"s_waitcnt lgkmcnt(2)\n" \
"v_add_f32 v112, v112, v116\n" \
"v_add_f32 v113, v113, v117\n" \
"v_add_f32 v114, v114, v118\n" \
"v_add_f32 v115, v115, v119\n" \
"s_waitcnt lgkmcnt(0)\n" \
"v_add_f32 v120, v120, v124\n" \
"v_add_f32 v121, v121, v125\n" \
"v_add_f32 v122, v122, v126\n" \
"v_add_f32 v123, v123, v127\n" \
"v_add_f32 v112, v112, v120\n" \
"v_add_f32 v113, v113, v121\n" \
"v_add_f32 v114, v114, v122\n" \
"v_add_f32 v115, v115, v123\n" \
"v_xor_b32 v8, s84, v8\n" \
"v_cvt_pk_bf16_f32 v0, v112, v113\n" \
"v_cvt_pk_bf16_f32 v1, v114, v115\n" \
"v_xor_b32 v7, 0x4000, v7\n" \
"s_cmp_eq_u32 s98, 0\n" \
"s_cbranch_scc0 .Lswo_f_%=\n" \
"v_lshlrev_b32 v112, 16, v0\n" \
"v_and_b32 v113, 0xffff0000, v0\n" \
"v_lshlrev_b32 v114, 16, v1\n" \
"v_and_b32 v115, 0xffff0000, v1\n" \
"v_lshlrev_b32 v116, 16, v14\n" \
"v_and_b32 v117, 0xffff0000, v14\n" \
"v_lshlrev_b32 v118, 16, v15\n" \
"v_and_b32 v119, 0xffff0000, v15\n" \
"v_lshlrev_b32 v120, 16, v12\n" \
"v_and_b32 v121, 0xffff0000, v12\n" \
"v_lshlrev_b32 v122, 16, v13\n" \
"v_and_b32 v123, 0xffff0000, v13\n" \
"v_fmac_f32 v120, v112, v116\n" \
"v_fmac_f32 v121, v113, v117\n" \
"v_fmac_f32 v122, v114, v118\n" \
"v_fmac_f32 v123, v115, v119\n" \
"v_cvt_pk_bf16_f32 v12, v120, v121\n" \
"v_cvt_pk_bf16_f32 v13, v122, v123\n" \
"buffer_store_dwordx2 v[12:13], v5, s[68:71], s77 offen\n" \
"s_branch .Lswo_g_%=\n" \
".Lswo_f_%=:\n" \
"buffer_store_dwordx2 v[0:1], v6, s[36:39], s76 offen sc1\n" \
"s_cmp_lg_u32 s55, s95\n" \
"s_cbranch_scc1 .Lswo_g_%=\n" \
"s_waitcnt vmcnt(0) lgkmcnt(0)\n" \
"v_mov_b32 v9, 1\n" \
"s_mov_b64 exec, 1\n" \
"buffer_atomic_add v9, off, s[36:39], s49 offset:0\n" \
"s_mov_b64 exec, -1\n" \
"s_mov_b32 s79, 0\n" \
"s_mov_b32 s98, 0\n" \
"s_branch .Lswo_o_%=\n" \
".Lswo_ok_%=:\n" \
".Lswo_g_%=:\n" \
"s_cmp_lt_u32 s55, s46\n" \
"s_cbranch_scc1 .Lswo_prime_%=\n" \
"s_branch .Lswo_end_%=\n" \
".Lswo_w_%=:\n" \
"s_nop 7\n" \
"s_nop 7\n" \
"s_waitcnt vmcnt(0) lgkmcnt(0)\n" \
"s_mov_b32 s61, 0\n" \
".Lswo_wa_%=:\n" \
"s_cmp_lg_u32 s60, 0\n" \
"s_cbranch_scc1 .Lswo_wk_%=\n" \
"buffer_load_dword v11, v4, s[36:39], 0 offen sc1\n" \
"s_waitcnt vmcnt(0)\n" \
"v_cmp_gt_u32_e64 s[62:63], s59, v11\n" \
"s_nop 4\n" \
"s_cmp_eq_u64 s[62:63], 0\n" \
"s_cbranch_scc1 .Lswo_wk_%=\n" \
"s_sleep 1\n" \
"s_add_u32 s61, s61, 1\n" \
"s_cmp_lt_u32 s61, 65536\n" \
"s_cbranch_scc1 .Lswo_wa_%=\n" \
"s_mov_b32 s60, 1\n" \
"v_mov_b32 v11, 256\n" \
"v_mov_b32 v9, 1\n" \
"buffer_store_dword v9, v11, s[36:39], 0 offen sc1\n" \
"s_waitcnt vmcnt(0) lgkmcnt(0)\n" \
"s_branch .Lswo_wk_%=\n" \
".Lswo_o_%=:\n" \
"s_nop 7\n" \
"s_nop 7\n" \
"s_waitcnt vmcnt(0) lgkmcnt(0)\n" \
"s_mov_b32 s61, 0\n" \
".Lswo_oa_%=:\n" \
"s_cmp_lg_u32 s60, 0\n" \
"s_cbranch_scc1 .Lswo_ok_%=\n" \
"buffer_load_dword v9, off, s[36:39], s49 offset:64 sc1\n" \
"s_sub_u32 s54, 10, s47\n" \
"s_waitcnt vmcnt(0)\n" \
"v_cmp_gt_u32 vcc, s54, v9\n" \
"s_nop 4\n" \
"s_cbranch_vccz .Lswo_ok_%=\n" \
"s_sleep 1\n" \
"s_add_u32 s61, s61, 1\n" \
"s_cmp_lt_u32 s61, 65536\n" \
"s_cbranch_scc1 .Lswo_oa_%=\n" \
"s_mov_b32 s60, 1\n" \
"v_mov_b32 v11, 256\n" \
"v_mov_b32 v9, 1\n" \
"buffer_store_dword v9, v11, s[36:39], 0 offen sc1\n" \
"s_waitcnt vmcnt(0) lgkmcnt(0)\n" \
"s_branch .Lswo_ok_%=\n" \
".Lswo_end_%=:\n" \
"s_endpgm\n"
#define ASM_S64_G2 \
"s_nop 7\n" \
"s_nop 7\n" \
"s_lshl_b32 s52, s48, 15\n" \
"s_lshl_b32 s53, s48, 13\n" \
"s_add_u32 s53, s53, 98304\n" \
"s_sub_u32 s54, s53, s52\n" \
"v_add_u32 v9, s54, v1\n" \
"s_add_u32 m0, s52, 0\n" \
"s_add_u32 s54, s97, 0\n" \
"s_lshr_b32 s55, s54, 8\n" \
"s_and_b32 s55, s55, 0x2000\n" \
"s_xor_b32 s54, s54, s55\n" \
"buffer_load_dwordx4 v0, s[40:43], s54 offen sc1 lds\n" \
"s_add_u32 m0, s52, 1024\n" \
"s_add_u32 s54, s97, 32768\n" \
"s_lshr_b32 s55, s54, 8\n" \
"s_and_b32 s55, s55, 0x2000\n" \
"s_xor_b32 s54, s54, s55\n" \
"buffer_load_dwordx4 v0, s[40:43], s54 offen sc1 lds\n" \
"s_add_u32 m0, s52, 2048\n" \
"s_add_u32 s54, s97, 65536\n" \
"s_lshr_b32 s55, s54, 8\n" \
"s_and_b32 s55, s55, 0x2000\n" \
"s_xor_b32 s54, s54, s55\n" \
"buffer_load_dwordx4 v0, s[40:43], s54 offen sc1 lds\n" \
"s_add_u32 m0, s52, 3072\n" \
"s_add_u32 s54, s97, 98304\n" \
"s_lshr_b32 s55, s54, 8\n" \
"s_and_b32 s55, s55, 0x2000\n" \
"s_xor_b32 s54, s54, s55\n" \
"buffer_load_dwordx4 v0, s[40:43], s54 offen sc1 lds\n" \
"s_mov_b32 s54, s99\n" \
".Lsg2_z0_%=:\n" \
"s_sleep 1\n" \
"s_sub_u32 s54, s54, 1\n" \
"s_cbranch_scc0 .Lsg2_z0_%=\n" \
"s_add_u32 m0, s52, 4096\n" \
"s_add_u32 s54, s97, 131072\n" \
"s_lshr_b32 s55, s54, 8\n" \
"s_and_b32 s55, s55, 0x2000\n" \
"s_xor_b32 s54, s54, s55\n" \
"buffer_load_dwordx4 v0, s[40:43], s54 offen sc1 lds\n" \
"s_add_u32 m0, s52, 5120\n" \
"s_add_u32 s54, s97, 163840\n" \
"s_lshr_b32 s55, s54, 8\n" \
"s_and_b32 s55, s55, 0x2000\n" \
"s_xor_b32 s54, s54, s55\n" \
"buffer_load_dwordx4 v0, s[40:43], s54 offen sc1 lds\n" \
"s_add_u32 m0, s52, 6144\n" \
"s_add_u32 s54, s97, 196608\n" \
"s_lshr_b32 s55, s54, 8\n" \
"s_and_b32 s55, s55, 0x2000\n" \
"s_xor_b32 s54, s54, s55\n" \
"buffer_load_dwordx4 v0, s[40:43], s54 offen sc1 lds\n" \
"s_add_u32 m0, s52, 7168\n" \
"s_add_u32 s54, s97, 229376\n" \
"s_lshr_b32 s55, s54, 8\n" \
"s_and_b32 s55, s55, 0x2000\n" \
"s_xor_b32 s54, s54, s55\n" \
"buffer_load_dwordx4 v0, s[40:43], s54 offen sc1 lds\n" \
"s_mov_b32 s54, s99\n" \
".Lsg2_z1_%=:\n" \
"s_sleep 1\n" \
"s_sub_u32 s54, s54, 1\n" \
"s_cbranch_scc0 .Lsg2_z1_%=\n" \
"s_add_u32 m0, s52, 8192\n" \
"s_add_u32 s54, s97, 262144\n" \
"s_lshr_b32 s55, s54, 8\n" \
"s_and_b32 s55, s55, 0x2000\n" \
"s_xor_b32 s54, s54, s55\n" \
"buffer_load_dwordx4 v0, s[40:43], s54 offen sc1 lds\n" \
"s_add_u32 m0, s52, 9216\n" \
"s_add_u32 s54, s97, 294912\n" \
"s_lshr_b32 s55, s54, 8\n" \
"s_and_b32 s55, s55, 0x2000\n" \
"s_xor_b32 s54, s54, s55\n" \
"buffer_load_dwordx4 v0, s[40:43], s54 offen sc1 lds\n" \
"s_add_u32 m0, s52, 10240\n" \
"s_add_u32 s54, s97, 327680\n" \
"s_lshr_b32 s55, s54, 8\n" \
"s_and_b32 s55, s55, 0x2000\n" \
"s_xor_b32 s54, s54, s55\n" \
"buffer_load_dwordx4 v0, s[40:43], s54 offen sc1 lds\n" \
"s_add_u32 m0, s52, 11264\n" \
"s_add_u32 s54, s97, 360448\n" \
"s_lshr_b32 s55, s54, 8\n" \
"s_and_b32 s55, s55, 0x2000\n" \
"s_xor_b32 s54, s54, s55\n" \
"buffer_load_dwordx4 v0, s[40:43], s54 offen sc1 lds\n" \
"s_mov_b32 s54, s99\n" \
".Lsg2_z2_%=:\n" \
"s_sleep 1\n" \
"s_sub_u32 s54, s54, 1\n" \
"s_cbranch_scc0 .Lsg2_z2_%=\n" \
"s_add_u32 m0, s52, 12288\n" \
"s_add_u32 s54, s97, 393216\n" \
"s_lshr_b32 s55, s54, 8\n" \
"s_and_b32 s55, s55, 0x2000\n" \
"s_xor_b32 s54, s54, s55\n" \
"buffer_load_dwordx4 v0, s[40:43], s54 offen sc1 lds\n" \
"s_add_u32 m0, s52, 13312\n" \
"s_add_u32 s54, s97, 425984\n" \
"s_lshr_b32 s55, s54, 8\n" \
"s_and_b32 s55, s55, 0x2000\n" \
"s_xor_b32 s54, s54, s55\n" \
"buffer_load_dwordx4 v0, s[40:43], s54 offen sc1 lds\n" \
"s_add_u32 m0, s52, 14336\n" \
"s_add_u32 s54, s97, 458752\n" \
"s_lshr_b32 s55, s54, 8\n" \
"s_and_b32 s55, s55, 0x2000\n" \
"s_xor_b32 s54, s54, s55\n" \
"buffer_load_dwordx4 v0, s[40:43], s54 offen sc1 lds\n" \
"s_add_u32 m0, s52, 15360\n" \
"s_add_u32 s54, s97, 491520\n" \
"s_lshr_b32 s55, s54, 8\n" \
"s_and_b32 s55, s55, 0x2000\n" \
"s_xor_b32 s54, s54, s55\n" \
"buffer_load_dwordx4 v0, s[40:43], s54 offen sc1 lds\n" \
"s_mov_b32 s54, s99\n" \
".Lsg2_z3_%=:\n" \
"s_sleep 1\n" \
"s_sub_u32 s54, s54, 1\n" \
"s_cbranch_scc0 .Lsg2_z3_%=\n" \
"s_add_u32 m0, s52, 16384\n" \
"s_add_u32 s54, s97, 524288\n" \
"s_lshr_b32 s55, s54, 8\n" \
"s_and_b32 s55, s55, 0x2000\n" \
"s_xor_b32 s54, s54, s55\n" \
"buffer_load_dwordx4 v0, s[40:43], s54 offen sc1 lds\n" \
"s_add_u32 m0, s52, 17408\n" \
"s_add_u32 s54, s97, 557056\n" \
"s_lshr_b32 s55, s54, 8\n" \
"s_and_b32 s55, s55, 0x2000\n" \
"s_xor_b32 s54, s54, s55\n" \
"buffer_load_dwordx4 v0, s[40:43], s54 offen sc1 lds\n" \
"s_add_u32 m0, s52, 18432\n" \
"s_add_u32 s54, s97, 589824\n" \
"s_lshr_b32 s55, s54, 8\n" \
"s_and_b32 s55, s55, 0x2000\n" \
"s_xor_b32 s54, s54, s55\n" \
"buffer_load_dwordx4 v0, s[40:43], s54 offen sc1 lds\n" \
"s_add_u32 m0, s52, 19456\n" \
"s_add_u32 s54, s97, 622592\n" \
"s_lshr_b32 s55, s54, 8\n" \
"s_and_b32 s55, s55, 0x2000\n" \
"s_xor_b32 s54, s54, s55\n" \
"buffer_load_dwordx4 v0, s[40:43], s54 offen sc1 lds\n" \
"s_mov_b32 s54, s99\n" \
".Lsg2_z4_%=:\n" \
"s_sleep 1\n" \
"s_sub_u32 s54, s54, 1\n" \
"s_cbranch_scc0 .Lsg2_z4_%=\n" \
"s_add_u32 m0, s52, 20480\n" \
"s_add_u32 s54, s97, 655360\n" \
"s_lshr_b32 s55, s54, 8\n" \
"s_and_b32 s55, s55, 0x2000\n" \
"s_xor_b32 s54, s54, s55\n" \
"buffer_load_dwordx4 v0, s[40:43], s54 offen sc1 lds\n" \
"s_add_u32 m0, s52, 21504\n" \
"s_add_u32 s54, s97, 688128\n" \
"s_lshr_b32 s55, s54, 8\n" \
"s_and_b32 s55, s55, 0x2000\n" \
"s_xor_b32 s54, s54, s55\n" \
"buffer_load_dwordx4 v0, s[40:43], s54 offen sc1 lds\n" \
"s_add_u32 m0, s52, 22528\n" \
"s_add_u32 s54, s97, 720896\n" \
"s_lshr_b32 s55, s54, 8\n" \
"s_and_b32 s55, s55, 0x2000\n" \
"s_xor_b32 s54, s54, s55\n" \
"buffer_load_dwordx4 v0, s[40:43], s54 offen sc1 lds\n" \
"s_add_u32 m0, s52, 23552\n" \
"s_add_u32 s54, s97, 753664\n" \
"s_lshr_b32 s55, s54, 8\n" \
"s_and_b32 s55, s55, 0x2000\n" \
"s_xor_b32 s54, s54, s55\n" \
"buffer_load_dwordx4 v0, s[40:43], s54 offen sc1 lds\n" \
"s_mov_b32 s54, s99\n" \
".Lsg2_z5_%=:\n" \
"s_sleep 1\n" \
"s_sub_u32 s54, s54, 1\n" \
"s_cbranch_scc0 .Lsg2_z5_%=\n" \
"s_add_u32 m0, s52, 24576\n" \
"s_add_u32 s54, s97, 786432\n" \
"s_lshr_b32 s55, s54, 8\n" \
"s_and_b32 s55, s55, 0x2000\n" \
"s_xor_b32 s54, s54, s55\n" \
"buffer_load_dwordx4 v0, s[40:43], s54 offen sc1 lds\n" \
"s_add_u32 m0, s52, 25600\n" \
"s_add_u32 s54, s97, 819200\n" \
"s_lshr_b32 s55, s54, 8\n" \
"s_and_b32 s55, s55, 0x2000\n" \
"s_xor_b32 s54, s54, s55\n" \
"buffer_load_dwordx4 v0, s[40:43], s54 offen sc1 lds\n" \
"s_add_u32 m0, s52, 26624\n" \
"s_add_u32 s54, s97, 851968\n" \
"s_lshr_b32 s55, s54, 8\n" \
"s_and_b32 s55, s55, 0x2000\n" \
"s_xor_b32 s54, s54, s55\n" \
"buffer_load_dwordx4 v0, s[40:43], s54 offen sc1 lds\n" \
"s_add_u32 m0, s52, 27648\n" \
"s_add_u32 s54, s97, 884736\n" \
"s_lshr_b32 s55, s54, 8\n" \
"s_and_b32 s55, s55, 0x2000\n" \
"s_xor_b32 s54, s54, s55\n" \
"buffer_load_dwordx4 v0, s[40:43], s54 offen sc1 lds\n" \
"s_mov_b32 s54, s99\n" \
".Lsg2_z6_%=:\n" \
"s_sleep 1\n" \
"s_sub_u32 s54, s54, 1\n" \
"s_cbranch_scc0 .Lsg2_z6_%=\n" \
"s_add_u32 m0, s52, 28672\n" \
"s_add_u32 s54, s97, 917504\n" \
"s_lshr_b32 s55, s54, 8\n" \
"s_and_b32 s55, s55, 0x2000\n" \
"s_xor_b32 s54, s54, s55\n" \
"buffer_load_dwordx4 v0, s[40:43], s54 offen sc1 lds\n" \
"s_add_u32 m0, s52, 29696\n" \
"s_add_u32 s54, s97, 950272\n" \
"s_lshr_b32 s55, s54, 8\n" \
"s_and_b32 s55, s55, 0x2000\n" \
"s_xor_b32 s54, s54, s55\n" \
"buffer_load_dwordx4 v0, s[40:43], s54 offen sc1 lds\n" \
"s_add_u32 m0, s52, 30720\n" \
"s_add_u32 s54, s97, 983040\n" \
"s_lshr_b32 s55, s54, 8\n" \
"s_and_b32 s55, s55, 0x2000\n" \
"s_xor_b32 s54, s54, s55\n" \
"buffer_load_dwordx4 v0, s[40:43], s54 offen sc1 lds\n" \
"s_add_u32 m0, s52, 31744\n" \
"s_add_u32 s54, s97, 1015808\n" \
"s_lshr_b32 s55, s54, 8\n" \
"s_and_b32 s55, s55, 0x2000\n" \
"s_xor_b32 s54, s54, s55\n" \
"buffer_load_dwordx4 v0, s[40:43], s54 offen sc1 lds\n" \
"s_mov_b32 s54, s99\n" \
".Lsg2_z7_%=:\n" \
"s_sleep 1\n" \
"s_sub_u32 s54, s54, 1\n" \
"s_cbranch_scc0 .Lsg2_z7_%=\n" \
"s_add_u32 m0, s53, 32768\n" \
"s_add_u32 s54, s97, 1048576\n" \
"s_lshr_b32 s55, s54, 8\n" \
"s_and_b32 s55, s55, 0x2000\n" \
"s_xor_b32 s54, s54, s55\n" \
"buffer_load_dwordx4 v0, s[40:43], s54 offen sc1 lds\n" \
"s_add_u32 m0, s53, 33792\n" \
"s_add_u32 s54, s97, 1081344\n" \
"s_lshr_b32 s55, s54, 8\n" \
"s_and_b32 s55, s55, 0x2000\n" \
"s_xor_b32 s54, s54, s55\n" \
"buffer_load_dwordx4 v0, s[40:43], s54 offen sc1 lds\n" \
"s_add_u32 m0, s53, 34816\n" \
"s_add_u32 s54, s97, 1114112\n" \
"s_lshr_b32 s55, s54, 8\n" \
"s_and_b32 s55, s55, 0x2000\n" \
"s_xor_b32 s54, s54, s55\n" \
"buffer_load_dwordx4 v0, s[40:43], s54 offen sc1 lds\n" \
"s_add_u32 m0, s53, 35840\n" \
"s_add_u32 s54, s97, 1146880\n" \
"s_lshr_b32 s55, s54, 8\n" \
"s_and_b32 s55, s55, 0x2000\n" \
"s_xor_b32 s54, s54, s55\n" \
"buffer_load_dwordx4 v0, s[40:43], s54 offen sc1 lds\n" \
"s_mov_b32 s54, s99\n" \
".Lsg2_z8_%=:\n" \
"s_sleep 1\n" \
"s_sub_u32 s54, s54, 1\n" \
"s_cbranch_scc0 .Lsg2_z8_%=\n" \
"s_add_u32 m0, s53, 36864\n" \
"s_add_u32 s54, s97, 1179648\n" \
"s_lshr_b32 s55, s54, 8\n" \
"s_and_b32 s55, s55, 0x2000\n" \
"s_xor_b32 s54, s54, s55\n" \
"buffer_load_dwordx4 v0, s[40:43], s54 offen sc1 lds\n" \
"s_add_u32 m0, s53, 37888\n" \
"s_add_u32 s54, s97, 1212416\n" \
"s_lshr_b32 s55, s54, 8\n" \
"s_and_b32 s55, s55, 0x2000\n" \
"s_xor_b32 s54, s54, s55\n" \
"buffer_load_dwordx4 v0, s[40:43], s54 offen sc1 lds\n" \
"s_add_u32 m0, s53, 38912\n" \
"s_add_u32 s54, s97, 1245184\n" \
"s_lshr_b32 s55, s54, 8\n" \
"s_and_b32 s55, s55, 0x2000\n" \
"s_xor_b32 s54, s54, s55\n" \
"buffer_load_dwordx4 v0, s[40:43], s54 offen sc1 lds\n" \
"s_add_u32 m0, s53, 39936\n" \
"s_add_u32 s54, s97, 1277952\n" \
"s_lshr_b32 s55, s54, 8\n" \
"s_and_b32 s55, s55, 0x2000\n" \
"s_xor_b32 s54, s54, s55\n" \
"buffer_load_dwordx4 v0, s[40:43], s54 offen sc1 lds\n" \
"s_mov_b32 s54, s99\n" \
".Lsg2_z9_%=:\n" \
"s_sleep 1\n" \
"s_sub_u32 s54, s54, 1\n" \
"s_cbranch_scc0 .Lsg2_z9_%=\n" \
"s_waitcnt vmcnt(36)\n" \
"ds_read_b64_tr_b16 a[0:1], v1 offset:0\n" \
"ds_read_b64_tr_b16 a[2:3], v1 offset:512\n" \
"ds_read_b64_tr_b16 a[128:129], v1 offset:32\n" \
"ds_read_b64_tr_b16 a[130:131], v1 offset:544\n" \
"ds_read_b64_tr_b16 v[128:129], v1 offset:64\n" \
"ds_read_b64_tr_b16 v[130:131], v1 offset:576\n" \
"ds_read_b64_tr_b16 v[16:17], v1 offset:96\n" \
"ds_read_b64_tr_b16 v[18:19], v1 offset:608\n" \
"s_waitcnt lgkmcnt(0)\n" \
"s_add_u32 m0, s52, 0\n" \
"s_add_u32 s54, s97, 1310720\n" \
"s_lshr_b32 s55, s54, 8\n" \
"s_and_b32 s55, s55, 0x2000\n" \
"s_xor_b32 s54, s54, s55\n" \
"buffer_load_dwordx4 v0, s[40:43], s54 offen sc1 lds\n" \
"s_add_u32 m0, s52, 1024\n" \
"s_add_u32 s54, s97, 1343488\n" \
"s_lshr_b32 s55, s54, 8\n" \
"s_and_b32 s55, s55, 0x2000\n" \
"s_xor_b32 s54, s54, s55\n" \
"buffer_load_dwordx4 v0, s[40:43], s54 offen sc1 lds\n" \
"s_add_u32 m0, s52, 2048\n" \
"s_add_u32 s54, s97, 1376256\n" \
"s_lshr_b32 s55, s54, 8\n" \
"s_and_b32 s55, s55, 0x2000\n" \
"s_xor_b32 s54, s54, s55\n" \
"buffer_load_dwordx4 v0, s[40:43], s54 offen sc1 lds\n" \
"s_add_u32 m0, s52, 3072\n" \
"s_add_u32 s54, s97, 1409024\n" \
"s_lshr_b32 s55, s54, 8\n" \
"s_and_b32 s55, s55, 0x2000\n" \
"s_xor_b32 s54, s54, s55\n" \
"buffer_load_dwordx4 v0, s[40:43], s54 offen sc1 lds\n" \
"s_mov_b32 s54, s99\n" \
".Lsg2_z10_%=:\n" \
"s_sleep 1\n" \
"s_sub_u32 s54, s54, 1\n" \
"s_cbranch_scc0 .Lsg2_z10_%=\n" \
"s_waitcnt vmcnt(36)\n" \
"ds_read_b64_tr_b16 a[4:5], v1 offset:4096\n" \
"ds_read_b64_tr_b16 a[6:7], v1 offset:4608\n" \
"ds_read_b64_tr_b16 a[132:133], v1 offset:4128\n" \
"ds_read_b64_tr_b16 a[134:135], v1 offset:4640\n" \
"ds_read_b64_tr_b16 v[132:133], v1 offset:4160\n" \
"ds_read_b64_tr_b16 v[134:135], v1 offset:4672\n" \
"ds_read_b64_tr_b16 v[20:21], v1 offset:4192\n" \
"ds_read_b64_tr_b16 v[22:23], v1 offset:4704\n" \
"s_waitcnt lgkmcnt(0)\n" \
"s_add_u32 m0, s52, 4096\n" \
"s_add_u32 s54, s97, 1441792\n" \
"s_lshr_b32 s55, s54, 8\n" \
"s_and_b32 s55, s55, 0x2000\n" \
"s_xor_b32 s54, s54, s55\n" \
"buffer_load_dwordx4 v0, s[40:43], s54 offen sc1 lds\n" \
"s_add_u32 m0, s52, 5120\n" \
"s_add_u32 s54, s97, 1474560\n" \
"s_lshr_b32 s55, s54, 8\n" \
"s_and_b32 s55, s55, 0x2000\n" \
"s_xor_b32 s54, s54, s55\n" \
"buffer_load_dwordx4 v0, s[40:43], s54 offen sc1 lds\n" \
"s_add_u32 m0, s52, 6144\n" \
"s_add_u32 s54, s97, 1507328\n" \
"s_lshr_b32 s55, s54, 8\n" \
"s_and_b32 s55, s55, 0x2000\n" \
"s_xor_b32 s54, s54, s55\n" \
"buffer_load_dwordx4 v0, s[40:43], s54 offen sc1 lds\n" \
"s_add_u32 m0, s52, 7168\n" \
"s_add_u32 s54, s97, 1540096\n" \
"s_lshr_b32 s55, s54, 8\n" \
"s_and_b32 s55, s55, 0x2000\n" \
"s_xor_b32 s54, s54, s55\n" \
"buffer_load_dwordx4 v0, s[40:43], s54 offen sc1 lds\n" \
"s_mov_b32 s54, s99\n" \
".Lsg2_z11_%=:\n" \
"s_sleep 1\n" \
"s_sub_u32 s54, s54, 1\n" \
"s_cbranch_scc0 .Lsg2_z11_%=\n" \
"s_waitcnt vmcnt(36)\n" \
"ds_read_b64_tr_b16 a[8:9], v1 offset:8192\n" \
"ds_read_b64_tr_b16 a[10:11], v1 offset:8704\n" \
"ds_read_b64_tr_b16 a[136:137], v1 offset:8224\n" \
"ds_read_b64_tr_b16 a[138:139], v1 offset:8736\n" \
"ds_read_b64_tr_b16 v[136:137], v1 offset:8256\n" \
"ds_read_b64_tr_b16 v[138:139], v1 offset:8768\n" \
"ds_read_b64_tr_b16 v[24:25], v1 offset:8288\n" \
"ds_read_b64_tr_b16 v[26:27], v1 offset:8800\n" \
"s_waitcnt lgkmcnt(0)\n" \
"s_add_u32 m0, s52, 8192\n" \
"s_add_u32 s54, s97, 1572864\n" \
"s_lshr_b32 s55, s54, 8\n" \
"s_and_b32 s55, s55, 0x2000\n" \
"s_xor_b32 s54, s54, s55\n" \
"buffer_load_dwordx4 v0, s[40:43], s54 offen sc1 lds\n" \
"s_add_u32 m0, s52, 9216\n" \
"s_add_u32 s54, s97, 1605632\n" \
"s_lshr_b32 s55, s54, 8\n" \
"s_and_b32 s55, s55, 0x2000\n" \
"s_xor_b32 s54, s54, s55\n" \
"buffer_load_dwordx4 v0, s[40:43], s54 offen sc1 lds\n" \
"s_add_u32 m0, s52, 10240\n" \
"s_add_u32 s54, s97, 1638400\n" \
"s_lshr_b32 s55, s54, 8\n" \
"s_and_b32 s55, s55, 0x2000\n" \
"s_xor_b32 s54, s54, s55\n" \
"buffer_load_dwordx4 v0, s[40:43], s54 offen sc1 lds\n" \
"s_add_u32 m0, s52, 11264\n" \
"s_add_u32 s54, s97, 1671168\n" \
"s_lshr_b32 s55, s54, 8\n" \
"s_and_b32 s55, s55, 0x2000\n" \
"s_xor_b32 s54, s54, s55\n" \
"buffer_load_dwordx4 v0, s[40:43], s54 offen sc1 lds\n" \
"s_mov_b32 s54, s99\n" \
".Lsg2_z12_%=:\n" \
"s_sleep 1\n" \
"s_sub_u32 s54, s54, 1\n" \
"s_cbranch_scc0 .Lsg2_z12_%=\n" \
"s_waitcnt vmcnt(36)\n" \
"ds_read_b64_tr_b16 a[12:13], v1 offset:12288\n" \
"ds_read_b64_tr_b16 a[14:15], v1 offset:12800\n" \
"ds_read_b64_tr_b16 a[140:141], v1 offset:12320\n" \
"ds_read_b64_tr_b16 a[142:143], v1 offset:12832\n" \
"ds_read_b64_tr_b16 v[140:141], v1 offset:12352\n" \
"ds_read_b64_tr_b16 v[142:143], v1 offset:12864\n" \
"ds_read_b64_tr_b16 v[28:29], v1 offset:12384\n" \
"ds_read_b64_tr_b16 v[30:31], v1 offset:12896\n" \
"s_waitcnt lgkmcnt(0)\n" \
"s_add_u32 m0, s52, 12288\n" \
"s_add_u32 s54, s97, 1703936\n" \
"s_lshr_b32 s55, s54, 8\n" \
"s_and_b32 s55, s55, 0x2000\n" \
"s_xor_b32 s54, s54, s55\n" \
"buffer_load_dwordx4 v0, s[40:43], s54 offen sc1 lds\n" \
"s_add_u32 m0, s52, 13312\n" \
"s_add_u32 s54, s97, 1736704\n" \
"s_lshr_b32 s55, s54, 8\n" \
"s_and_b32 s55, s55, 0x2000\n" \
"s_xor_b32 s54, s54, s55\n" \
"buffer_load_dwordx4 v0, s[40:43], s54 offen sc1 lds\n" \
"s_add_u32 m0, s52, 14336\n" \
"s_add_u32 s54, s97, 1769472\n" \
"s_lshr_b32 s55, s54, 8\n" \
"s_and_b32 s55, s55, 0x2000\n" \
"s_xor_b32 s54, s54, s55\n" \
"buffer_load_dwordx4 v0, s[40:43], s54 offen sc1 lds\n" \
"s_add_u32 m0, s52, 15360\n" \
"s_add_u32 s54, s97, 1802240\n" \
"s_lshr_b32 s55, s54, 8\n" \
"s_and_b32 s55, s55, 0x2000\n" \
"s_xor_b32 s54, s54, s55\n" \
"buffer_load_dwordx4 v0, s[40:43], s54 offen sc1 lds\n" \
"s_mov_b32 s54, s99\n" \
".Lsg2_z13_%=:\n" \
"s_sleep 1\n" \
"s_sub_u32 s54, s54, 1\n" \
"s_cbranch_scc0 .Lsg2_z13_%=\n" \
"s_waitcnt vmcnt(36)\n" \
"ds_read_b64_tr_b16 a[16:17], v1 offset:16384\n" \
"ds_read_b64_tr_b16 a[18:19], v1 offset:16896\n" \
"ds_read_b64_tr_b16 a[144:145], v1 offset:16416\n" \
"ds_read_b64_tr_b16 a[146:147], v1 offset:16928\n" \
"ds_read_b64_tr_b16 v[144:145], v1 offset:16448\n" \
"ds_read_b64_tr_b16 v[146:147], v1 offset:16960\n" \
"ds_read_b64_tr_b16 v[32:33], v1 offset:16480\n" \
"ds_read_b64_tr_b16 v[34:35], v1 offset:16992\n" \
"s_waitcnt lgkmcnt(0)\n" \
"s_add_u32 m0, s52, 16384\n" \
"s_add_u32 s54, s97, 1835008\n" \
"s_lshr_b32 s55, s54, 8\n" \
"s_and_b32 s55, s55, 0x2000\n" \
"s_xor_b32 s54, s54, s55\n" \
"buffer_load_dwordx4 v0, s[40:43], s54 offen sc1 lds\n" \
"s_add_u32 m0, s52, 17408\n" \
"s_add_u32 s54, s97, 1867776\n" \
"s_lshr_b32 s55, s54, 8\n" \
"s_and_b32 s55, s55, 0x2000\n" \
"s_xor_b32 s54, s54, s55\n" \
"buffer_load_dwordx4 v0, s[40:43], s54 offen sc1 lds\n" \
"s_add_u32 m0, s52, 18432\n" \
"s_add_u32 s54, s97, 1900544\n" \
"s_lshr_b32 s55, s54, 8\n" \
"s_and_b32 s55, s55, 0x2000\n" \
"s_xor_b32 s54, s54, s55\n" \
"buffer_load_dwordx4 v0, s[40:43], s54 offen sc1 lds\n" \
"s_add_u32 m0, s52, 19456\n" \
"s_add_u32 s54, s97, 1933312\n" \
"s_lshr_b32 s55, s54, 8\n" \
"s_and_b32 s55, s55, 0x2000\n" \
"s_xor_b32 s54, s54, s55\n" \
"buffer_load_dwordx4 v0, s[40:43], s54 offen sc1 lds\n" \
"s_mov_b32 s54, s99\n" \
".Lsg2_z14_%=:\n" \
"s_sleep 1\n" \
"s_sub_u32 s54, s54, 1\n" \
"s_cbranch_scc0 .Lsg2_z14_%=\n" \
"s_waitcnt vmcnt(36)\n" \
"ds_read_b64_tr_b16 a[20:21], v1 offset:20480\n" \
"ds_read_b64_tr_b16 a[22:23], v1 offset:20992\n" \
"ds_read_b64_tr_b16 a[148:149], v1 offset:20512\n" \
"ds_read_b64_tr_b16 a[150:151], v1 offset:21024\n" \
"ds_read_b64_tr_b16 v[148:149], v1 offset:20544\n" \
"ds_read_b64_tr_b16 v[150:151], v1 offset:21056\n" \
"ds_read_b64_tr_b16 v[36:37], v1 offset:20576\n" \
"ds_read_b64_tr_b16 v[38:39], v1 offset:21088\n" \
"s_waitcnt lgkmcnt(0)\n" \
"s_add_u32 m0, s52, 20480\n" \
"s_add_u32 s54, s97, 1966080\n" \
"s_lshr_b32 s55, s54, 8\n" \
"s_and_b32 s55, s55, 0x2000\n" \
"s_xor_b32 s54, s54, s55\n" \
"buffer_load_dwordx4 v0, s[40:43], s54 offen sc1 lds\n" \
"s_add_u32 m0, s52, 21504\n" \
"s_add_u32 s54, s97, 1998848\n" \
"s_lshr_b32 s55, s54, 8\n" \
"s_and_b32 s55, s55, 0x2000\n" \
"s_xor_b32 s54, s54, s55\n" \
"buffer_load_dwordx4 v0, s[40:43], s54 offen sc1 lds\n" \
"s_add_u32 m0, s52, 22528\n" \
"s_add_u32 s54, s97, 2031616\n" \
"s_lshr_b32 s55, s54, 8\n" \
"s_and_b32 s55, s55, 0x2000\n" \
"s_xor_b32 s54, s54, s55\n" \
"buffer_load_dwordx4 v0, s[40:43], s54 offen sc1 lds\n" \
"s_add_u32 m0, s52, 23552\n" \
"s_add_u32 s54, s97, 2064384\n" \
"s_lshr_b32 s55, s54, 8\n" \
"s_and_b32 s55, s55, 0x2000\n" \
"s_xor_b32 s54, s54, s55\n" \
"buffer_load_dwordx4 v0, s[40:43], s54 offen sc1 lds\n" \
"s_mov_b32 s54, s99\n" \
".Lsg2_z15_%=:\n" \
"s_sleep 1\n" \
"s_sub_u32 s54, s54, 1\n" \
"s_cbranch_scc0 .Lsg2_z15_%=\n" \
"s_waitcnt vmcnt(36)\n" \
"ds_read_b64_tr_b16 a[24:25], v1 offset:24576\n" \
"ds_read_b64_tr_b16 a[26:27], v1 offset:25088\n" \
"ds_read_b64_tr_b16 a[152:153], v1 offset:24608\n" \
"ds_read_b64_tr_b16 a[154:155], v1 offset:25120\n" \
"ds_read_b64_tr_b16 v[152:153], v1 offset:24640\n" \
"ds_read_b64_tr_b16 v[154:155], v1 offset:25152\n" \
"ds_read_b64_tr_b16 v[40:41], v1 offset:24672\n" \
"ds_read_b64_tr_b16 v[42:43], v1 offset:25184\n" \
"s_waitcnt lgkmcnt(0)\n" \
"s_add_u32 m0, s52, 24576\n" \
"s_add_u32 s54, s97, 2097152\n" \
"s_lshr_b32 s55, s54, 8\n" \
"s_and_b32 s55, s55, 0x2000\n" \
"s_xor_b32 s54, s54, s55\n" \
"buffer_load_dwordx4 v0, s[40:43], s54 offen sc1 lds\n" \
"s_add_u32 m0, s52, 25600\n" \
"s_add_u32 s54, s97, 2129920\n" \
"s_lshr_b32 s55, s54, 8\n" \
"s_and_b32 s55, s55, 0x2000\n" \
"s_xor_b32 s54, s54, s55\n" \
"buffer_load_dwordx4 v0, s[40:43], s54 offen sc1 lds\n" \
"s_add_u32 m0, s52, 26624\n" \
"s_add_u32 s54, s97, 2162688\n" \
"s_lshr_b32 s55, s54, 8\n" \
"s_and_b32 s55, s55, 0x2000\n" \
"s_xor_b32 s54, s54, s55\n" \
"buffer_load_dwordx4 v0, s[40:43], s54 offen sc1 lds\n" \
"s_add_u32 m0, s52, 27648\n" \
"s_add_u32 s54, s97, 2195456\n" \
"s_lshr_b32 s55, s54, 8\n" \
"s_and_b32 s55, s55, 0x2000\n" \
"s_xor_b32 s54, s54, s55\n" \
"buffer_load_dwordx4 v0, s[40:43], s54 offen sc1 lds\n" \
"s_mov_b32 s54, s99\n" \
".Lsg2_z16_%=:\n" \
"s_sleep 1\n" \
"s_sub_u32 s54, s54, 1\n" \
"s_cbranch_scc0 .Lsg2_z16_%=\n" \
"s_waitcnt vmcnt(36)\n" \
"ds_read_b64_tr_b16 a[28:29], v1 offset:28672\n" \
"ds_read_b64_tr_b16 a[30:31], v1 offset:29184\n" \
"ds_read_b64_tr_b16 a[156:157], v1 offset:28704\n" \
"ds_read_b64_tr_b16 a[158:159], v1 offset:29216\n" \
"ds_read_b64_tr_b16 v[156:157], v1 offset:28736\n" \
"ds_read_b64_tr_b16 v[158:159], v1 offset:29248\n" \
"ds_read_b64_tr_b16 v[44:45], v1 offset:28768\n" \
"ds_read_b64_tr_b16 v[46:47], v1 offset:29280\n" \
"s_waitcnt lgkmcnt(0)\n" \
"s_add_u32 m0, s52, 28672\n" \
"s_add_u32 s54, s97, 2228224\n" \
"s_lshr_b32 s55, s54, 8\n" \
"s_and_b32 s55, s55, 0x2000\n" \
"s_xor_b32 s54, s54, s55\n" \
"buffer_load_dwordx4 v0, s[40:43], s54 offen sc1 lds\n" \
"s_add_u32 m0, s52, 29696\n" \
"s_add_u32 s54, s97, 2260992\n" \
"s_lshr_b32 s55, s54, 8\n" \
"s_and_b32 s55, s55, 0x2000\n" \
"s_xor_b32 s54, s54, s55\n" \
"buffer_load_dwordx4 v0, s[40:43], s54 offen sc1 lds\n" \
"s_add_u32 m0, s52, 30720\n" \
"s_add_u32 s54, s97, 2293760\n" \
"s_lshr_b32 s55, s54, 8\n" \
"s_and_b32 s55, s55, 0x2000\n" \
"s_xor_b32 s54, s54, s55\n" \
"buffer_load_dwordx4 v0, s[40:43], s54 offen sc1 lds\n" \
"s_add_u32 m0, s52, 31744\n" \
"s_add_u32 s54, s97, 2326528\n" \
"s_lshr_b32 s55, s54, 8\n" \
"s_and_b32 s55, s55, 0x2000\n" \
"s_xor_b32 s54, s54, s55\n" \
"buffer_load_dwordx4 v0, s[40:43], s54 offen sc1 lds\n" \
"s_mov_b32 s54, s99\n" \
".Lsg2_z17_%=:\n" \
"s_sleep 1\n" \
"s_sub_u32 s54, s54, 1\n" \
"s_cbranch_scc0 .Lsg2_z17_%=\n" \
"s_waitcnt vmcnt(36)\n" \
"ds_read_b64_tr_b16 a[32:33], v9 offset:32768\n" \
"ds_read_b64_tr_b16 a[34:35], v9 offset:33280\n" \
"ds_read_b64_tr_b16 a[160:161], v9 offset:32800\n" \
"ds_read_b64_tr_b16 a[162:163], v9 offset:33312\n" \
"ds_read_b64_tr_b16 v[160:161], v9 offset:32832\n" \
"ds_read_b64_tr_b16 v[162:163], v9 offset:33344\n" \
"ds_read_b64_tr_b16 v[48:49], v9 offset:32864\n" \
"ds_read_b64_tr_b16 v[50:51], v9 offset:33376\n" \
"s_waitcnt lgkmcnt(0)\n" \
"s_add_u32 m0, s53, 32768\n" \
"s_add_u32 s54, s97, 2359296\n" \
"s_lshr_b32 s55, s54, 8\n" \
"s_and_b32 s55, s55, 0x2000\n" \
"s_xor_b32 s54, s54, s55\n" \
"buffer_load_dwordx4 v0, s[40:43], s54 offen sc1 lds\n" \
"s_add_u32 m0, s53, 33792\n" \
"s_add_u32 s54, s97, 2392064\n" \
"s_lshr_b32 s55, s54, 8\n" \
"s_and_b32 s55, s55, 0x2000\n" \
"s_xor_b32 s54, s54, s55\n" \
"buffer_load_dwordx4 v0, s[40:43], s54 offen sc1 lds\n" \
"s_add_u32 m0, s53, 34816\n" \
"s_add_u32 s54, s97, 2424832\n" \
"s_lshr_b32 s55, s54, 8\n" \
"s_and_b32 s55, s55, 0x2000\n" \
"s_xor_b32 s54, s54, s55\n" \
"buffer_load_dwordx4 v0, s[40:43], s54 offen sc1 lds\n" \
"s_add_u32 m0, s53, 35840\n" \
"s_add_u32 s54, s97, 2457600\n" \
"s_lshr_b32 s55, s54, 8\n" \
"s_and_b32 s55, s55, 0x2000\n" \
"s_xor_b32 s54, s54, s55\n" \
"buffer_load_dwordx4 v0, s[40:43], s54 offen sc1 lds\n" \
"s_mov_b32 s54, s99\n" \
".Lsg2_z18_%=:\n" \
"s_sleep 1\n" \
"s_sub_u32 s54, s54, 1\n" \
"s_cbranch_scc0 .Lsg2_z18_%=\n" \
"s_waitcnt vmcnt(36)\n" \
"ds_read_b64_tr_b16 a[36:37], v9 offset:36864\n" \
"ds_read_b64_tr_b16 a[38:39], v9 offset:37376\n" \
"ds_read_b64_tr_b16 a[164:165], v9 offset:36896\n" \
"ds_read_b64_tr_b16 a[166:167], v9 offset:37408\n" \
"ds_read_b64_tr_b16 v[164:165], v9 offset:36928\n" \
"ds_read_b64_tr_b16 v[166:167], v9 offset:37440\n" \
"ds_read_b64_tr_b16 v[52:53], v9 offset:36960\n" \
"ds_read_b64_tr_b16 v[54:55], v9 offset:37472\n" \
"s_waitcnt lgkmcnt(0)\n" \
"s_add_u32 m0, s53, 36864\n" \
"s_add_u32 s54, s97, 2490368\n" \
"s_lshr_b32 s55, s54, 8\n" \
"s_and_b32 s55, s55, 0x2000\n" \
"s_xor_b32 s54, s54, s55\n" \
"buffer_load_dwordx4 v0, s[40:43], s54 offen sc1 lds\n" \
"s_add_u32 m0, s53, 37888\n" \
"s_add_u32 s54, s97, 2523136\n" \
"s_lshr_b32 s55, s54, 8\n" \
"s_and_b32 s55, s55, 0x2000\n" \
"s_xor_b32 s54, s54, s55\n" \
"buffer_load_dwordx4 v0, s[40:43], s54 offen sc1 lds\n" \
"s_add_u32 m0, s53, 38912\n" \
"s_add_u32 s54, s97, 2555904\n" \
"s_lshr_b32 s55, s54, 8\n" \
"s_and_b32 s55, s55, 0x2000\n" \
"s_xor_b32 s54, s54, s55\n" \
"buffer_load_dwordx4 v0, s[40:43], s54 offen sc1 lds\n" \
"s_add_u32 m0, s53, 39936\n" \
"s_add_u32 s54, s97, 2588672\n" \
"s_lshr_b32 s55, s54, 8\n" \
"s_and_b32 s55, s55, 0x2000\n" \
"s_xor_b32 s54, s54, s55\n" \
"buffer_load_dwordx4 v0, s[40:43], s54 offen sc1 lds\n" \
"s_mov_b32 s54, s99\n" \
".Lsg2_z19_%=:\n" \
"s_sleep 1\n" \
"s_sub_u32 s54, s54, 1\n" \
"s_cbranch_scc0 .Lsg2_z19_%=\n" \
"s_waitcnt vmcnt(36)\n" \
"ds_read_b64_tr_b16 a[40:41], v1 offset:0\n" \
"ds_read_b64_tr_b16 a[42:43], v1 offset:512\n" \
"ds_read_b64_tr_b16 a[168:169], v1 offset:32\n" \
"ds_read_b64_tr_b16 a[170:171], v1 offset:544\n" \
"ds_read_b64_tr_b16 v[168:169], v1 offset:64\n" \
"ds_read_b64_tr_b16 v[170:171], v1 offset:576\n" \
"ds_read_b64_tr_b16 v[56:57], v1 offset:96\n" \
"ds_read_b64_tr_b16 v[58:59], v1 offset:608\n" \
"s_waitcnt lgkmcnt(0)\n" \
"s_add_u32 m0, s52, 0\n" \
"s_add_u32 s54, s97, 2621440\n" \
"s_lshr_b32 s55, s54, 8\n" \
"s_and_b32 s55, s55, 0x2000\n" \
"s_xor_b32 s54, s54, s55\n" \
"buffer_load_dwordx4 v0, s[40:43], s54 offen sc1 lds\n" \
"s_add_u32 m0, s52, 1024\n" \
"s_add_u32 s54, s97, 2654208\n" \
"s_lshr_b32 s55, s54, 8\n" \
"s_and_b32 s55, s55, 0x2000\n" \
"s_xor_b32 s54, s54, s55\n" \
"buffer_load_dwordx4 v0, s[40:43], s54 offen sc1 lds\n" \
"s_add_u32 m0, s52, 2048\n" \
"s_add_u32 s54, s97, 2686976\n" \
"s_lshr_b32 s55, s54, 8\n" \
"s_and_b32 s55, s55, 0x2000\n" \
"s_xor_b32 s54, s54, s55\n" \
"buffer_load_dwordx4 v0, s[40:43], s54 offen sc1 lds\n" \
"s_add_u32 m0, s52, 3072\n" \
"s_add_u32 s54, s97, 2719744\n" \
"s_lshr_b32 s55, s54, 8\n" \
"s_and_b32 s55, s55, 0x2000\n" \
"s_xor_b32 s54, s54, s55\n" \
"buffer_load_dwordx4 v0, s[40:43], s54 offen sc1 lds\n" \
"s_mov_b32 s54, s99\n" \
".Lsg2_z20_%=:\n" \
"s_sleep 1\n" \
"s_sub_u32 s54, s54, 1\n" \
"s_cbranch_scc0 .Lsg2_z20_%=\n" \
"s_waitcnt vmcnt(36)\n" \
"ds_read_b64_tr_b16 a[44:45], v1 offset:4096\n" \
"ds_read_b64_tr_b16 a[46:47], v1 offset:4608\n" \
"ds_read_b64_tr_b16 a[172:173], v1 offset:4128\n" \
"ds_read_b64_tr_b16 a[174:175], v1 offset:4640\n" \
"ds_read_b64_tr_b16 v[172:173], v1 offset:4160\n" \
"ds_read_b64_tr_b16 v[174:175], v1 offset:4672\n" \
"ds_read_b64_tr_b16 v[60:61], v1 offset:4192\n" \
"ds_read_b64_tr_b16 v[62:63], v1 offset:4704\n" \
"s_waitcnt lgkmcnt(0)\n" \
"s_add_u32 m0, s52, 4096\n" \
"s_add_u32 s54, s97, 2752512\n" \
"s_lshr_b32 s55, s54, 8\n" \
"s_and_b32 s55, s55, 0x2000\n" \
"s_xor_b32 s54, s54, s55\n" \
"buffer_load_dwordx4 v0, s[40:43], s54 offen sc1 lds\n" \
"s_add_u32 m0, s52, 5120\n" \
"s_add_u32 s54, s97, 2785280\n" \
"s_lshr_b32 s55, s54, 8\n" \
"s_and_b32 s55, s55, 0x2000\n" \
"s_xor_b32 s54, s54, s55\n" \
"buffer_load_dwordx4 v0, s[40:43], s54 offen sc1 lds\n" \
"s_add_u32 m0, s52, 6144\n" \
"s_add_u32 s54, s97, 2818048\n" \
"s_lshr_b32 s55, s54, 8\n" \
"s_and_b32 s55, s55, 0x2000\n" \
"s_xor_b32 s54, s54, s55\n" \
"buffer_load_dwordx4 v0, s[40:43], s54 offen sc1 lds\n" \
"s_add_u32 m0, s52, 7168\n" \
"s_add_u32 s54, s97, 2850816\n" \
"s_lshr_b32 s55, s54, 8\n" \
"s_and_b32 s55, s55, 0x2000\n" \
"s_xor_b32 s54, s54, s55\n" \
"buffer_load_dwordx4 v0, s[40:43], s54 offen sc1 lds\n" \
"s_mov_b32 s54, s99\n" \
".Lsg2_z21_%=:\n" \
"s_sleep 1\n" \
"s_sub_u32 s54, s54, 1\n" \
"s_cbranch_scc0 .Lsg2_z21_%=\n" \
"s_waitcnt vmcnt(36)\n" \
"ds_read_b64_tr_b16 a[48:49], v1 offset:8192\n" \
"ds_read_b64_tr_b16 a[50:51], v1 offset:8704\n" \
"ds_read_b64_tr_b16 a[176:177], v1 offset:8224\n" \
"ds_read_b64_tr_b16 a[178:179], v1 offset:8736\n" \
"ds_read_b64_tr_b16 v[176:177], v1 offset:8256\n" \
"ds_read_b64_tr_b16 v[178:179], v1 offset:8768\n" \
"ds_read_b64_tr_b16 v[64:65], v1 offset:8288\n" \
"ds_read_b64_tr_b16 v[66:67], v1 offset:8800\n" \
"s_waitcnt lgkmcnt(0)\n" \
"s_add_u32 m0, s52, 8192\n" \
"s_add_u32 s54, s97, 2883584\n" \
"s_lshr_b32 s55, s54, 8\n" \
"s_and_b32 s55, s55, 0x2000\n" \
"s_xor_b32 s54, s54, s55\n" \
"buffer_load_dwordx4 v0, s[40:43], s54 offen sc1 lds\n" \
"s_add_u32 m0, s52, 9216\n" \
"s_add_u32 s54, s97, 2916352\n" \
"s_lshr_b32 s55, s54, 8\n" \
"s_and_b32 s55, s55, 0x2000\n" \
"s_xor_b32 s54, s54, s55\n" \
"buffer_load_dwordx4 v0, s[40:43], s54 offen sc1 lds\n" \
"s_add_u32 m0, s52, 10240\n" \
"s_add_u32 s54, s97, 2949120\n" \
"s_lshr_b32 s55, s54, 8\n" \
"s_and_b32 s55, s55, 0x2000\n" \
"s_xor_b32 s54, s54, s55\n" \
"buffer_load_dwordx4 v0, s[40:43], s54 offen sc1 lds\n" \
"s_add_u32 m0, s52, 11264\n" \
"s_add_u32 s54, s97, 2981888\n" \
"s_lshr_b32 s55, s54, 8\n" \
"s_and_b32 s55, s55, 0x2000\n" \
"s_xor_b32 s54, s54, s55\n" \
"buffer_load_dwordx4 v0, s[40:43], s54 offen sc1 lds\n" \
"s_mov_b32 s54, s99\n" \
".Lsg2_z22_%=:\n" \
"s_sleep 1\n" \
"s_sub_u32 s54, s54, 1\n" \
"s_cbranch_scc0 .Lsg2_z22_%=\n" \
"s_waitcnt vmcnt(36)\n" \
"ds_read_b64_tr_b16 a[52:53], v1 offset:12288\n" \
"ds_read_b64_tr_b16 a[54:55], v1 offset:12800\n" \
"ds_read_b64_tr_b16 a[180:181], v1 offset:12320\n" \
"ds_read_b64_tr_b16 a[182:183], v1 offset:12832\n" \
"ds_read_b64_tr_b16 v[180:181], v1 offset:12352\n" \
"ds_read_b64_tr_b16 v[182:183], v1 offset:12864\n" \
"ds_read_b64_tr_b16 v[68:69], v1 offset:12384\n" \
"ds_read_b64_tr_b16 v[70:71], v1 offset:12896\n" \
"s_waitcnt lgkmcnt(0)\n" \
"s_add_u32 m0, s52, 12288\n" \
"s_add_u32 s54, s97, 3014656\n" \
"s_lshr_b32 s55, s54, 8\n" \
"s_and_b32 s55, s55, 0x2000\n" \
"s_xor_b32 s54, s54, s55\n" \
"buffer_load_dwordx4 v0, s[40:43], s54 offen sc1 lds\n" \
"s_add_u32 m0, s52, 13312\n" \
"s_add_u32 s54, s97, 3047424\n" \
"s_lshr_b32 s55, s54, 8\n" \
"s_and_b32 s55, s55, 0x2000\n" \
"s_xor_b32 s54, s54, s55\n" \
"buffer_load_dwordx4 v0, s[40:43], s54 offen sc1 lds\n" \
"s_add_u32 m0, s52, 14336\n" \
"s_add_u32 s54, s97, 3080192\n" \
"s_lshr_b32 s55, s54, 8\n" \
"s_and_b32 s55, s55, 0x2000\n" \
"s_xor_b32 s54, s54, s55\n" \
"buffer_load_dwordx4 v0, s[40:43], s54 offen sc1 lds\n" \
"s_add_u32 m0, s52, 15360\n" \
"s_add_u32 s54, s97, 3112960\n" \
"s_lshr_b32 s55, s54, 8\n" \
"s_and_b32 s55, s55, 0x2000\n" \
"s_xor_b32 s54, s54, s55\n" \
"buffer_load_dwordx4 v0, s[40:43], s54 offen sc1 lds\n" \
"s_mov_b32 s54, s99\n" \
".Lsg2_z23_%=:\n" \
"s_sleep 1\n" \
"s_sub_u32 s54, s54, 1\n" \
"s_cbranch_scc0 .Lsg2_z23_%=\n" \
"s_waitcnt vmcnt(36)\n" \
"ds_read_b64_tr_b16 a[56:57], v1 offset:16384\n" \
"ds_read_b64_tr_b16 a[58:59], v1 offset:16896\n" \
"ds_read_b64_tr_b16 a[184:185], v1 offset:16416\n" \
"ds_read_b64_tr_b16 a[186:187], v1 offset:16928\n" \
"ds_read_b64_tr_b16 v[184:185], v1 offset:16448\n" \
"ds_read_b64_tr_b16 v[186:187], v1 offset:16960\n" \
"ds_read_b64_tr_b16 v[72:73], v1 offset:16480\n" \
"ds_read_b64_tr_b16 v[74:75], v1 offset:16992\n" \
"s_waitcnt lgkmcnt(0)\n" \
"s_add_u32 m0, s52, 16384\n" \
"s_add_u32 s54, s97, 3145728\n" \
"s_lshr_b32 s55, s54, 8\n" \
"s_and_b32 s55, s55, 0x2000\n" \
"s_xor_b32 s54, s54, s55\n" \
"buffer_load_dwordx4 v0, s[40:43], s54 offen sc1 lds\n" \
"s_add_u32 m0, s52, 17408\n" \
"s_add_u32 s54, s97, 3178496\n" \
"s_lshr_b32 s55, s54, 8\n" \
"s_and_b32 s55, s55, 0x2000\n" \
"s_xor_b32 s54, s54, s55\n" \
"buffer_load_dwordx4 v0, s[40:43], s54 offen sc1 lds\n" \
"s_add_u32 m0, s52, 18432\n" \
"s_add_u32 s54, s97, 3211264\n" \
"s_lshr_b32 s55, s54, 8\n" \
"s_and_b32 s55, s55, 0x2000\n" \
"s_xor_b32 s54, s54, s55\n" \
"buffer_load_dwordx4 v0, s[40:43], s54 offen sc1 lds\n" \
"s_add_u32 m0, s52, 19456\n" \
"s_add_u32 s54, s97, 3244032\n" \
"s_lshr_b32 s55, s54, 8\n" \
"s_and_b32 s55, s55, 0x2000\n" \
"s_xor_b32 s54, s54, s55\n" \
"buffer_load_dwordx4 v0, s[40:43], s54 offen sc1 lds\n" \
"s_mov_b32 s54, s99\n" \
".Lsg2_z24_%=:\n" \
"s_sleep 1\n" \
"s_sub_u32 s54, s54, 1\n" \
"s_cbranch_scc0 .Lsg2_z24_%=\n" \
"s_waitcnt vmcnt(36)\n" \
"ds_read_b64_tr_b16 a[60:61], v1 offset:20480\n" \
"ds_read_b64_tr_b16 a[62:63], v1 offset:20992\n" \
"ds_read_b64_tr_b16 a[188:189], v1 offset:20512\n" \
"ds_read_b64_tr_b16 a[190:191], v1 offset:21024\n" \
"ds_read_b64_tr_b16 v[188:189], v1 offset:20544\n" \
"ds_read_b64_tr_b16 v[190:191], v1 offset:21056\n" \
"ds_read_b64_tr_b16 v[76:77], v1 offset:20576\n" \
"ds_read_b64_tr_b16 v[78:79], v1 offset:21088\n" \
"s_waitcnt lgkmcnt(0)\n" \
"s_add_u32 m0, s52, 20480\n" \
"s_add_u32 s54, s97, 3276800\n" \
"s_lshr_b32 s55, s54, 8\n" \
"s_and_b32 s55, s55, 0x2000\n" \
"s_xor_b32 s54, s54, s55\n" \
"buffer_load_dwordx4 v0, s[40:43], s54 offen sc1 lds\n" \
"s_add_u32 m0, s52, 21504\n" \
"s_add_u32 s54, s97, 3309568\n" \
"s_lshr_b32 s55, s54, 8\n" \
"s_and_b32 s55, s55, 0x2000\n" \
"s_xor_b32 s54, s54, s55\n" \
"buffer_load_dwordx4 v0, s[40:43], s54 offen sc1 lds\n" \
"s_add_u32 m0, s52, 22528\n" \
"s_add_u32 s54, s97, 3342336\n" \
"s_lshr_b32 s55, s54, 8\n" \
"s_and_b32 s55, s55, 0x2000\n" \
"s_xor_b32 s54, s54, s55\n" \
"buffer_load_dwordx4 v0, s[40:43], s54 offen sc1 lds\n" \
"s_add_u32 m0, s52, 23552\n" \
"s_add_u32 s54, s97, 3375104\n" \
"s_lshr_b32 s55, s54, 8\n" \
"s_and_b32 s55, s55, 0x2000\n" \
"s_xor_b32 s54, s54, s55\n" \
"buffer_load_dwordx4 v0, s[40:43], s54 offen sc1 lds\n" \
"s_mov_b32 s54, s99\n" \
".Lsg2_z25_%=:\n" \
"s_sleep 1\n" \
"s_sub_u32 s54, s54, 1\n" \
"s_cbranch_scc0 .Lsg2_z25_%=\n" \
"s_waitcnt vmcnt(36)\n" \
"ds_read_b64_tr_b16 a[64:65], v1 offset:24576\n" \
"ds_read_b64_tr_b16 a[66:67], v1 offset:25088\n" \
"ds_read_b64_tr_b16 a[192:193], v1 offset:24608\n" \
"ds_read_b64_tr_b16 a[194:195], v1 offset:25120\n" \
"ds_read_b64_tr_b16 v[192:193], v1 offset:24640\n" \
"ds_read_b64_tr_b16 v[194:195], v1 offset:25152\n" \
"ds_read_b64_tr_b16 v[80:81], v1 offset:24672\n" \
"ds_read_b64_tr_b16 v[82:83], v1 offset:25184\n" \
"s_waitcnt lgkmcnt(0)\n" \
"s_add_u32 m0, s52, 24576\n" \
"s_add_u32 s54, s97, 3407872\n" \
"s_lshr_b32 s55, s54, 8\n" \
"s_and_b32 s55, s55, 0x2000\n" \
"s_xor_b32 s54, s54, s55\n" \
"buffer_load_dwordx4 v0, s[40:43], s54 offen sc1 lds\n" \
"s_add_u32 m0, s52, 25600\n" \
"s_add_u32 s54, s97, 3440640\n" \
"s_lshr_b32 s55, s54, 8\n" \
"s_and_b32 s55, s55, 0x2000\n" \
"s_xor_b32 s54, s54, s55\n" \
"buffer_load_dwordx4 v0, s[40:43], s54 offen sc1 lds\n" \
"s_add_u32 m0, s52, 26624\n" \
"s_add_u32 s54, s97, 3473408\n" \
"s_lshr_b32 s55, s54, 8\n" \
"s_and_b32 s55, s55, 0x2000\n" \
"s_xor_b32 s54, s54, s55\n" \
"buffer_load_dwordx4 v0, s[40:43], s54 offen sc1 lds\n" \
"s_add_u32 m0, s52, 27648\n" \
"s_add_u32 s54, s97, 3506176\n" \
"s_lshr_b32 s55, s54, 8\n" \
"s_and_b32 s55, s55, 0x2000\n" \
"s_xor_b32 s54, s54, s55\n" \
"buffer_load_dwordx4 v0, s[40:43], s54 offen sc1 lds\n" \
"s_mov_b32 s54, s99\n" \
".Lsg2_z26_%=:\n" \
"s_sleep 1\n" \
"s_sub_u32 s54, s54, 1\n" \
"s_cbranch_scc0 .Lsg2_z26_%=\n" \
"s_waitcnt vmcnt(36)\n" \
"ds_read_b64_tr_b16 a[68:69], v1 offset:28672\n" \
"ds_read_b64_tr_b16 a[70:71], v1 offset:29184\n" \
"ds_read_b64_tr_b16 a[196:197], v1 offset:28704\n" \
"ds_read_b64_tr_b16 a[198:199], v1 offset:29216\n" \
"ds_read_b64_tr_b16 v[196:197], v1 offset:28736\n" \
"ds_read_b64_tr_b16 v[198:199], v1 offset:29248\n" \
"ds_read_b64_tr_b16 v[84:85], v1 offset:28768\n" \
"ds_read_b64_tr_b16 v[86:87], v1 offset:29280\n" \
"s_waitcnt lgkmcnt(0)\n" \
"s_add_u32 m0, s52, 28672\n" \
"s_add_u32 s54, s97, 3538944\n" \
"s_lshr_b32 s55, s54, 8\n" \
"s_and_b32 s55, s55, 0x2000\n" \
"s_xor_b32 s54, s54, s55\n" \
"buffer_load_dwordx4 v0, s[40:43], s54 offen sc1 lds\n" \
"s_add_u32 m0, s52, 29696\n" \
"s_add_u32 s54, s97, 3571712\n" \
"s_lshr_b32 s55, s54, 8\n" \
"s_and_b32 s55, s55, 0x2000\n" \
"s_xor_b32 s54, s54, s55\n" \
"buffer_load_dwordx4 v0, s[40:43], s54 offen sc1 lds\n" \
"s_add_u32 m0, s52, 30720\n" \
"s_add_u32 s54, s97, 3604480\n" \
"s_lshr_b32 s55, s54, 8\n" \
"s_and_b32 s55, s55, 0x2000\n" \
"s_xor_b32 s54, s54, s55\n" \
"buffer_load_dwordx4 v0, s[40:43], s54 offen sc1 lds\n" \
"s_add_u32 m0, s52, 31744\n" \
"s_add_u32 s54, s97, 3637248\n" \
"s_lshr_b32 s55, s54, 8\n" \
"s_and_b32 s55, s55, 0x2000\n" \
"s_xor_b32 s54, s54, s55\n" \
"buffer_load_dwordx4 v0, s[40:43], s54 offen sc1 lds\n" \
"s_mov_b32 s54, s99\n" \
".Lsg2_z27_%=:\n" \
"s_sleep 1\n" \
"s_sub_u32 s54, s54, 1\n" \
"s_cbranch_scc0 .Lsg2_z27_%=\n" \
"s_waitcnt vmcnt(36)\n" \
"ds_read_b64_tr_b16 a[72:73], v9 offset:32768\n" \
"ds_read_b64_tr_b16 a[74:75], v9 offset:33280\n" \
"ds_read_b64_tr_b16 a[200:201], v9 offset:32800\n" \
"ds_read_b64_tr_b16 a[202:203], v9 offset:33312\n" \
"ds_read_b64_tr_b16 v[200:201], v9 offset:32832\n" \
"ds_read_b64_tr_b16 v[202:203], v9 offset:33344\n" \
"ds_read_b64_tr_b16 v[88:89], v9 offset:32864\n" \
"ds_read_b64_tr_b16 v[90:91], v9 offset:33376\n" \
"s_waitcnt lgkmcnt(0)\n" \
"s_waitcnt vmcnt(32)\n" \
"ds_read_b64_tr_b16 a[76:77], v9 offset:36864\n" \
"ds_read_b64_tr_b16 a[78:79], v9 offset:37376\n" \
"ds_read_b64_tr_b16 a[204:205], v9 offset:36896\n" \
"ds_read_b64_tr_b16 a[206:207], v9 offset:37408\n" \
"ds_read_b64_tr_b16 v[204:205], v9 offset:36928\n" \
"ds_read_b64_tr_b16 v[206:207], v9 offset:37440\n" \
"ds_read_b64_tr_b16 v[92:93], v9 offset:36960\n" \
"ds_read_b64_tr_b16 v[94:95], v9 offset:37472\n" \
"s_waitcnt lgkmcnt(0)\n" \
"s_waitcnt vmcnt(28)\n" \
"ds_read_b64_tr_b16 a[80:81], v1 offset:0\n" \
"ds_read_b64_tr_b16 a[82:83], v1 offset:512\n" \
"ds_read_b64_tr_b16 a[208:209], v1 offset:32\n" \
"ds_read_b64_tr_b16 a[210:211], v1 offset:544\n" \
"ds_read_b64_tr_b16 v[208:209], v1 offset:64\n" \
"ds_read_b64_tr_b16 v[210:211], v1 offset:576\n" \
"ds_read_b64_tr_b16 v[96:97], v1 offset:96\n" \
"ds_read_b64_tr_b16 v[98:99], v1 offset:608\n" \
"s_waitcnt lgkmcnt(0)\n" \
"ds_write_b128 v2, v[16:19] offset:0\n" \
"ds_write_b128 v2, v[20:23] offset:1024\n" \
"ds_write_b128 v2, v[24:27] offset:2048\n" \
"ds_write_b128 v2, v[28:31] offset:3072\n" \
"s_waitcnt vmcnt(24)\n" \
"ds_read_b64_tr_b16 a[84:85], v1 offset:4096\n" \
"ds_read_b64_tr_b16 a[86:87], v1 offset:4608\n" \
"ds_read_b64_tr_b16 a[212:213], v1 offset:4128\n" \
"ds_read_b64_tr_b16 a[214:215], v1 offset:4640\n" \
"ds_read_b64_tr_b16 v[212:213], v1 offset:4160\n" \
"ds_read_b64_tr_b16 v[214:215], v1 offset:4672\n" \
"ds_read_b64_tr_b16 v[100:101], v1 offset:4192\n" \
"ds_read_b64_tr_b16 v[102:103], v1 offset:4704\n" \
"s_waitcnt lgkmcnt(0)\n" \
"ds_write_b128 v2, v[32:35] offset:4096\n" \
"ds_write_b128 v2, v[36:39] offset:5120\n" \
"ds_write_b128 v2, v[40:43] offset:6144\n" \
"ds_write_b128 v2, v[44:47] offset:7168\n" \
"s_waitcnt vmcnt(20)\n" \
"ds_read_b64_tr_b16 a[88:89], v1 offset:8192\n" \
"ds_read_b64_tr_b16 a[90:91], v1 offset:8704\n" \
"ds_read_b64_tr_b16 a[216:217], v1 offset:8224\n" \
"ds_read_b64_tr_b16 a[218:219], v1 offset:8736\n" \
"ds_read_b64_tr_b16 v[216:217], v1 offset:8256\n" \
"ds_read_b64_tr_b16 v[218:219], v1 offset:8768\n" \
"ds_read_b64_tr_b16 v[104:105], v1 offset:8288\n" \
"ds_read_b64_tr_b16 v[106:107], v1 offset:8800\n" \
"s_waitcnt lgkmcnt(0)\n" \
"ds_write_b128 v2, v[48:51] offset:8192\n" \
"ds_write_b128 v2, v[52:55] offset:9216\n" \
"ds_write_b128 v2, v[56:59] offset:10240\n" \
"ds_write_b128 v2, v[60:63] offset:11264\n" \
"s_waitcnt vmcnt(16)\n" \
"ds_read_b64_tr_b16 a[92:93], v1 offset:12288\n" \
"ds_read_b64_tr_b16 a[94:95], v1 offset:12800\n" \
"ds_read_b64_tr_b16 a[220:221], v1 offset:12320\n" \
"ds_read_b64_tr_b16 a[222:223], v1 offset:12832\n" \
"ds_read_b64_tr_b16 v[220:221], v1 offset:12352\n" \
"ds_read_b64_tr_b16 v[222:223], v1 offset:12864\n" \
"ds_read_b64_tr_b16 v[108:109], v1 offset:12384\n" \
"ds_read_b64_tr_b16 v[110:111], v1 offset:12896\n" \
"s_waitcnt lgkmcnt(0)\n" \
"ds_write_b128 v2, v[64:67] offset:12288\n" \
"ds_write_b128 v2, v[68:71] offset:13312\n" \
"ds_write_b128 v2, v[72:75] offset:14336\n" \
"ds_write_b128 v2, v[76:79] offset:15360\n" \
"s_waitcnt vmcnt(12)\n" \
"ds_read_b64_tr_b16 a[96:97], v1 offset:16384\n" \
"ds_read_b64_tr_b16 a[98:99], v1 offset:16896\n" \
"ds_read_b64_tr_b16 a[224:225], v1 offset:16416\n" \
"ds_read_b64_tr_b16 a[226:227], v1 offset:16928\n" \
"ds_read_b64_tr_b16 v[224:225], v1 offset:16448\n" \
"ds_read_b64_tr_b16 v[226:227], v1 offset:16960\n" \
"ds_read_b64_tr_b16 v[16:17], v1 offset:16480\n" \
"ds_read_b64_tr_b16 v[18:19], v1 offset:16992\n" \
"s_waitcnt lgkmcnt(0)\n" \
"ds_write_b128 v2, v[80:83] offset:16384\n" \
"ds_write_b128 v2, v[84:87] offset:17408\n" \
"ds_write_b128 v2, v[88:91] offset:18432\n" \
"ds_write_b128 v2, v[92:95] offset:19456\n" \
"s_waitcnt vmcnt(8)\n" \
"ds_read_b64_tr_b16 a[100:101], v1 offset:20480\n" \
"ds_read_b64_tr_b16 a[102:103], v1 offset:20992\n" \
"ds_read_b64_tr_b16 a[228:229], v1 offset:20512\n" \
"ds_read_b64_tr_b16 a[230:231], v1 offset:21024\n" \
"ds_read_b64_tr_b16 v[228:229], v1 offset:20544\n" \
"ds_read_b64_tr_b16 v[230:231], v1 offset:21056\n" \
"ds_read_b64_tr_b16 v[20:21], v1 offset:20576\n" \
"ds_read_b64_tr_b16 v[22:23], v1 offset:21088\n" \
"s_waitcnt lgkmcnt(0)\n" \
"ds_write_b128 v2, v[96:99] offset:20480\n" \
"ds_write_b128 v2, v[100:103] offset:21504\n" \
"ds_write_b128 v2, v[104:107] offset:22528\n" \
"ds_write_b128 v2, v[108:111] offset:23552\n" \
"s_waitcnt vmcnt(4)\n" \
"ds_read_b64_tr_b16 a[104:105], v1 offset:24576\n" \
"ds_read_b64_tr_b16 a[106:107], v1 offset:25088\n" \
"ds_read_b64_tr_b16 a[232:233], v1 offset:24608\n" \
"ds_read_b64_tr_b16 a[234:235], v1 offset:25120\n" \
"ds_read_b64_tr_b16 v[232:233], v1 offset:24640\n" \
"ds_read_b64_tr_b16 v[234:235], v1 offset:25152\n" \
"ds_read_b64_tr_b16 v[24:25], v1 offset:24672\n" \
"ds_read_b64_tr_b16 v[26:27], v1 offset:25184\n" \
"s_waitcnt lgkmcnt(0)\n" \
"ds_write_b128 v2, v[16:19] offset:24576\n" \
"ds_write_b128 v2, v[20:23] offset:25600\n" \
"ds_write_b128 v2, v[24:27] offset:26624\n" \
"s_waitcnt vmcnt(0)\n" \
"ds_read_b64_tr_b16 a[108:109], v1 offset:28672\n" \
"ds_read_b64_tr_b16 a[110:111], v1 offset:29184\n" \
"ds_read_b64_tr_b16 a[236:237], v1 offset:28704\n" \
"ds_read_b64_tr_b16 a[238:239], v1 offset:29216\n" \
"ds_read_b64_tr_b16 v[236:237], v1 offset:28736\n" \
"ds_read_b64_tr_b16 v[238:239], v1 offset:29248\n" \
"ds_read_b64_tr_b16 v[28:29], v1 offset:28768\n" \
"ds_read_b64_tr_b16 v[30:31], v1 offset:29280\n" \
"s_waitcnt lgkmcnt(0)\n" \
"ds_write_b128 v2, v[28:31] offset:28672\n" \
"s_waitcnt vmcnt(0) lgkmcnt(0)\n" \
"s_barrier\n" \
"s_mul_i32 s54, s48, 7\n" \
"s_add_u32 s54, s54, 0\n" \
"s_lshr_b32 s55, s54, 1\n" \
"s_lshl_b32 s55, s55, 14\n" \
"s_and_b32 s54, s54, 1\n" \
"s_lshl_b32 s54, s54, 12\n" \
"s_add_u32 s54, s54, s55\n" \
"s_add_u32 s86, s54, s85\n" \
"s_mul_i32 s54, s48, 7\n" \
"s_add_u32 s54, s54, 1\n" \
"s_lshr_b32 s55, s54, 1\n" \
"s_lshl_b32 s55, s55, 14\n" \
"s_and_b32 s54, s54, 1\n" \
"s_lshl_b32 s54, s54, 12\n" \
"s_add_u32 s54, s54, s55\n" \
"s_add_u32 s87, s54, s85\n" \
"s_mul_i32 s54, s48, 7\n" \
"s_add_u32 s54, s54, 2\n" \
"s_lshr_b32 s55, s54, 1\n" \
"s_lshl_b32 s55, s55, 14\n" \
"s_and_b32 s54, s54, 1\n" \
"s_lshl_b32 s54, s54, 12\n" \
"s_add_u32 s54, s54, s55\n" \
"s_add_u32 s88, s54, s85\n" \
"s_mul_i32 s54, s48, 7\n" \
"s_add_u32 s54, s54, 3\n" \
"s_lshr_b32 s55, s54, 1\n" \
"s_lshl_b32 s55, s55, 14\n" \
"s_and_b32 s54, s54, 1\n" \
"s_lshl_b32 s54, s54, 12\n" \
"s_add_u32 s54, s54, s55\n" \
"s_add_u32 s89, s54, s85\n" \
"s_mul_i32 s54, s48, 7\n" \
"s_add_u32 s54, s54, 4\n" \
"s_lshr_b32 s55, s54, 1\n" \
"s_lshl_b32 s55, s55, 14\n" \
"s_and_b32 s54, s54, 1\n" \
"s_lshl_b32 s54, s54, 12\n" \
"s_add_u32 s54, s54, s55\n" \
"s_add_u32 s90, s54, s85\n" \
"s_mul_i32 s54, s48, 7\n" \
"s_add_u32 s54, s54, 5\n" \
"s_lshr_b32 s55, s54, 1\n" \
"s_lshl_b32 s55, s55, 14\n" \
"s_and_b32 s54, s54, 1\n" \
"s_lshl_b32 s54, s54, 12\n" \
"s_add_u32 s54, s54, s55\n" \
"s_add_u32 s91, s54, s85\n" \
"s_mul_i32 s54, s48, 7\n" \
"s_add_u32 s54, s54, 6\n" \
"s_lshr_b32 s55, s54, 1\n" \
"s_lshl_b32 s55, s55, 14\n" \
"s_and_b32 s54, s54, 1\n" \
"s_lshl_b32 s54, s54, 12\n" \
"s_add_u32 s54, s54, s55\n" \
"s_add_u32 s92, s54, s85\n" \
"s_mov_b32 s55, 0\n" \
"s_mov_b32 s60, 0\n" \
"s_mov_b32 s79, s95\n" \
"s_mov_b32 s98, 0x70000000\n" \
".Lsg2_prime_%=:\n" \
"s_lshr_b32 s59, s44, 1\n" \
"s_add_u32 s59, s59, 1\n" \
"s_lshl_b32 s56, s44, 18\n" \
"s_lshr_b32 s58, s56, 8\n" \
"s_and_b32 s58, s58, 0x2000\n" \
"s_xor_b32 s56, s56, s58\n" \
"buffer_load_dword v11, v4, s[36:39], 0 offen sc1\n" \
"s_waitcnt vmcnt(0)\n" \
"v_cmp_gt_u32_e64 s[62:63], s59, v11\n" \
"s_nop 4\n" \
"s_cmp_lg_u64 s[62:63], 0\n" \
"s_cbranch_scc1 .Lsg2_w_%=\n" \
".Lsg2_wk_%=:\n" \
"s_mov_b32 s76, 0x7ffffff0\n" \
"s_mov_b32 s81, 1\n" \
"s_mov_b32 s84, 0\n" \
"s_waitcnt vmcnt(0) lgkmcnt(0)\n" \
"s_xor_b32 s57, s56, s86\n" \
"buffer_load_dwordx4 v[16:19], v3, s[36:39], s57 offen offset:0 sc1\n" \
"buffer_load_dwordx4 v[20:23], v3, s[36:39], s57 offen offset:1024 sc1\n" \
"buffer_load_dwordx4 v[24:27], v3, s[36:39], s57 offen offset:2048 sc1\n" \
"buffer_load_dwordx4 v[28:31], v3, s[36:39], s57 offen offset:3072 sc1\n" \
"s_xor_b32 s57, s56, s87\n" \
"buffer_load_dwordx4 v[32:35], v3, s[36:39], s57 offen offset:0 sc1\n" \
"buffer_load_dwordx4 v[36:39], v3, s[36:39], s57 offen offset:1024 sc1\n" \
"buffer_load_dwordx4 v[40:43], v3, s[36:39], s57 offen offset:2048 sc1\n" \
"buffer_load_dwordx4 v[44:47], v3, s[36:39], s57 offen offset:3072 sc1\n" \
"s_xor_b32 s57, s56, s88\n" \
"buffer_load_dwordx4 v[48:51], v3, s[36:39], s57 offen offset:0 sc1\n" \
"buffer_load_dwordx4 v[52:55], v3, s[36:39], s57 offen offset:1024 sc1\n" \
"buffer_load_dwordx4 v[56:59], v3, s[36:39], s57 offen offset:2048 sc1\n" \
"buffer_load_dwordx4 v[60:63], v3, s[36:39], s57 offen offset:3072 sc1\n" \
"s_xor_b32 s57, s56, s89\n" \
"buffer_load_dwordx4 v[64:67], v3, s[36:39], s57 offen offset:0 sc1\n" \
"buffer_load_dwordx4 v[68:71], v3, s[36:39], s57 offen offset:1024 sc1\n" \
"ds_read_b128 v[80:83], v2 offset:0\n" \
"ds_read_b128 v[84:87], v2 offset:1024\n" \
"ds_read_b128 v[88:91], v2 offset:2048\n" \
"s_waitcnt lgkmcnt(0)\n" \
"v_mov_b32 v9, 0x7ffffff0\n" \
"buffer_store_dword v9, v9, s[36:39], 0 offen\n" \
"buffer_store_dword v9, v9, s[36:39], 0 offen\n" \
"s_waitcnt vmcnt(15)\n" \
"s_nop 7\n" \
"s_nop 7\n" \
".p2align 6\n" \
".Lsg2_loop_%=:\n" \
"v_mfma_f32_16x16x32_bf16 v[96:99], a[0:3], v[16:19], 0\n" \
"ds_write_b128 v7, v[112:115] offset:0\n" \
"v_mfma_f32_16x16x32_bf16 v[100:103], a[128:131], v[16:19], 0\n" \
"ds_read_b128 v[92:95], v2 offset:3072\n" \
"v_xor_b32_e64 v7, 64, v7\n" \
"v_mfma_f32_16x16x32_bf16 v[104:107], v[128:131], v[16:19], 0\n" \
"s_waitcnt vmcnt(14) lgkmcnt(4)\n" \
"v_mfma_f32_16x16x32_bf16 v[108:111], v[80:83], v[16:19], 0\n" \
"buffer_load_dwordx4 v[16:19], v3, s[36:39], s57 offen offset:2048 sc1\n" \
"v_mfma_f32_16x16x32_bf16 v[96:99], a[4:7], v[20:23], v[96:99]\n" \
"ds_write_b128 v7, v[116:119] offset:1024\n" \
"v_mfma_f32_16x16x32_bf16 v[100:103], a[132:135], v[20:23], v[100:103]\n" \
"ds_read_b128 v[80:83], v2 offset:4096\n" \
"s_cmp_lt_i32 s44, s50\n" \
"v_xor_b32 v7, 192, v7\n" \
"v_mfma_f32_16x16x32_bf16 v[104:107], v[132:135], v[20:23], v[104:107]\n" \
"s_waitcnt vmcnt(14) lgkmcnt(5)\n" \
"s_cselect_b32 s82, s45, s47\n" \
"v_mfma_f32_16x16x32_bf16 v[108:111], v[84:87], v[20:23], v[108:111]\n" \
"buffer_load_dwordx4 v[20:23], v3, s[36:39], s57 offen offset:3072 sc1\n" \
"v_mfma_f32_16x16x32_bf16 v[96:99], a[8:11], v[24:27], v[96:99]\n" \
"ds_write_b128 v7, v[120:123] offset:2048\n" \
"v_mfma_f32_16x16x32_bf16 v[100:103], a[136:139], v[24:27], v[100:103]\n" \
"ds_read_b128 v[84:87], v2 offset:5120\n" \
"s_add_u32 s82, s82, s44\n" \
"s_cmp_lt_u32 s82, 128\n" \
"v_xor_b32 v7, 64, v7\n" \
"v_mfma_f32_16x16x32_bf16 v[104:107], v[136:139], v[24:27], v[104:107]\n" \
"s_waitcnt vmcnt(14) lgkmcnt(6)\n" \
"s_cselect_b32 s82, s82, s94\n" \
"v_mfma_f32_16x16x32_bf16 v[108:111], v[88:91], v[24:27], v[108:111]\n" \
"s_xor_b32 s57, s56, s90\n" \
"buffer_load_dwordx4 v[24:27], v3, s[36:39], s57 offen offset:0 sc1\n" \
"v_mfma_f32_16x16x32_bf16 v[96:99], a[12:15], v[28:31], v[96:99]\n" \
"ds_write_b128 v7, v[124:127] offset:3072\n" \
"v_mfma_f32_16x16x32_bf16 v[100:103], a[140:143], v[28:31], v[100:103]\n" \
"ds_read_b128 v[88:91], v2 offset:6144\n" \
"s_lshl_b32 s83, s82, 18\n" \
"v_xor_b32 v7, 192, v7\n" \
"v_mfma_f32_16x16x32_bf16 v[104:107], v[140:143], v[28:31], v[104:107]\n" \
"s_waitcnt vmcnt(14) lgkmcnt(6)\n" \
"s_lshr_b32 s58, s83, 8\n" \
"v_mfma_f32_16x16x32_bf16 v[108:111], v[92:95], v[28:31], v[108:111]\n" \
"buffer_load_dwordx4 v[28:31], v3, s[36:39], s57 offen offset:1024 sc1\n" \
"v_mfma_f32_16x16x32_bf16 v[96:99], a[16:19], v[32:35], v[96:99]\n" \
"v_mfma_f32_16x16x32_bf16 v[100:103], a[144:147], v[32:35], v[100:103]\n" \
"ds_read_b128 v[92:95], v2 offset:7168\n" \
"s_and_b32 s58, s58, 0x2000\n" \
"v_mfma_f32_16x16x32_bf16 v[104:107], v[144:147], v[32:35], v[104:107]\n" \
"s_waitcnt vmcnt(14) lgkmcnt(5)\n" \
"s_xor_b32 s83, s83, s58\n" \
"v_mfma_f32_16x16x32_bf16 v[108:111], v[80:83], v[32:35], v[108:111]\n" \
"buffer_load_dwordx4 v[32:35], v3, s[36:39], s57 offen offset:2048 sc1\n" \
"v_mfma_f32_16x16x32_bf16 v[96:99], a[20:23], v[36:39], v[96:99]\n" \
"s_waitcnt lgkmcnt(2)\n" \
"s_cmp_lg_u32 s81, 0\n" \
"s_cbranch_scc1 .Lsg2_nb_%=\n" \
"s_barrier\n" \
".Lsg2_nb_%=:\n" \
"v_mfma_f32_16x16x32_bf16 v[100:103], a[148:151], v[36:39], v[100:103]\n" \
"ds_read_b128 v[80:83], v2 offset:8192\n" \
"v_xor_b32_e64 v7, s84, v7\n" \
"v_mfma_f32_16x16x32_bf16 v[104:107], v[148:151], v[36:39], v[104:107]\n" \
"s_waitcnt vmcnt(14)\n" \
"v_mfma_f32_16x16x32_bf16 v[108:111], v[84:87], v[36:39], v[108:111]\n" \
"buffer_load_dwordx4 v[36:39], v3, s[36:39], s57 offen offset:3072 sc1\n" \
"v_mfma_f32_16x16x32_bf16 v[96:99], a[24:27], v[40:43], v[96:99]\n" \
"ds_read_b128 v[112:115], v8 offset:0\n" \
"v_mfma_f32_16x16x32_bf16 v[100:103], a[152:155], v[40:43], v[100:103]\n" \
"ds_read_b128 v[84:87], v2 offset:9216\n" \
"v_mfma_f32_16x16x32_bf16 v[104:107], v[152:155], v[40:43], v[104:107]\n" \
"s_waitcnt vmcnt(14) lgkmcnt(4)\n" \
"v_mfma_f32_16x16x32_bf16 v[108:111], v[88:91], v[40:43], v[108:111]\n" \
"s_xor_b32 s57, s56, s91\n" \
"buffer_load_dwordx4 v[40:43], v3, s[36:39], s57 offen offset:0 sc1\n" \
"v_mfma_f32_16x16x32_bf16 v[96:99], a[28:31], v[44:47], v[96:99]\n" \
"ds_read_b128 v[116:119], v8 offset:4096\n" \
"v_mfma_f32_16x16x32_bf16 v[100:103], a[156:159], v[44:47], v[100:103]\n" \
"ds_read_b128 v[88:91], v2 offset:10240\n" \
"v_mfma_f32_16x16x32_bf16 v[104:107], v[156:159], v[44:47], v[104:107]\n" \
"s_waitcnt vmcnt(14) lgkmcnt(5)\n" \
"v_mfma_f32_16x16x32_bf16 v[108:111], v[92:95], v[44:47], v[108:111]\n" \
"buffer_load_dwordx4 v[44:47], v3, s[36:39], s57 offen offset:1024 sc1\n" \
"v_mfma_f32_16x16x32_bf16 v[96:99], a[32:35], v[48:51], v[96:99]\n" \
"ds_read_b128 v[120:123], v8 offset:8192\n" \
"s_lshr_b32 s59, s82, 1\n" \
"s_add_u32 s59, s59, 1\n" \
"v_mfma_f32_16x16x32_bf16 v[100:103], a[160:163], v[48:51], v[100:103]\n" \
"ds_read_b128 v[92:95], v2 offset:11264\n" \
"v_mfma_f32_16x16x32_bf16 v[104:107], v[160:163], v[48:51], v[104:107]\n" \
"s_waitcnt vmcnt(14) lgkmcnt(6)\n" \
"v_mfma_f32_16x16x32_bf16 v[108:111], v[80:83], v[48:51], v[108:111]\n" \
"buffer_load_dwordx4 v[48:51], v3, s[36:39], s57 offen offset:2048 sc1\n" \
"v_mfma_f32_16x16x32_bf16 v[96:99], a[36:39], v[52:55], v[96:99]\n" \
"ds_read_b128 v[124:127], v8 offset:12288\n" \
"v_mfma_f32_16x16x32_bf16 v[100:103], a[164:167], v[52:55], v[100:103]\n" \
"ds_read_b128 v[80:83], v2 offset:12288\n" \
"v_mfma_f32_16x16x32_bf16 v[104:107], v[164:167], v[52:55], v[104:107]\n" \
"s_waitcnt vmcnt(14) lgkmcnt(6)\n" \
"v_mfma_f32_16x16x32_bf16 v[108:111], v[84:87], v[52:55], v[108:111]\n" \
"buffer_load_dwordx4 v[52:55], v3, s[36:39], s57 offen offset:3072 sc1\n" \
"v_mfma_f32_16x16x32_bf16 v[96:99], a[40:43], v[56:59], v[96:99]\n" \
"v_mfma_f32_16x16x32_bf16 v[100:103], a[168:171], v[56:59], v[100:103]\n" \
"ds_read_b128 v[84:87], v2 offset:13312\n" \
"v_mfma_f32_16x16x32_bf16 v[104:107], v[168:171], v[56:59], v[104:107]\n" \
"s_waitcnt vmcnt(12) lgkmcnt(5)\n" \
"v_add_f32 v112, v112, v116\n" \
"v_mfma_f32_16x16x32_bf16 v[108:111], v[88:91], v[56:59], v[108:111]\n" \
"s_xor_b32 s57, s56, s92\n" \
"buffer_load_dwordx4 v[56:59], v3, s[36:39], s57 offen offset:0 sc1\n" \
"v_mfma_f32_16x16x32_bf16 v[96:99], a[44:47], v[60:63], v[96:99]\n" \
"v_cmp_gt_u32_e64 s[62:63], s59, v11\n" \
"v_mfma_f32_16x16x32_bf16 v[100:103], a[172:175], v[60:63], v[100:103]\n" \
"ds_read_b128 v[88:91], v2 offset:14336\n" \
"v_add_f32 v113, v113, v117\n" \
"v_mfma_f32_16x16x32_bf16 v[104:107], v[172:175], v[60:63], v[104:107]\n" \
"s_waitcnt vmcnt(12) lgkmcnt(4)\n" \
"v_add_f32 v114, v114, v118\n" \
"v_mfma_f32_16x16x32_bf16 v[108:111], v[92:95], v[60:63], v[108:111]\n" \
"buffer_load_dwordx4 v[60:63], v3, s[36:39], s57 offen offset:1024 sc1\n" \
"v_add_f32_e64 v115, v115, v119\n" \
"v_mfma_f32_16x16x32_bf16 v[96:99], a[48:51], v[64:67], v[96:99]\n" \
"s_add_u32 s54, s55, 1\n" \
"s_cmp_lt_u32 s54, s79\n" \
"v_mfma_f32_16x16x32_bf16 v[100:103], a[176:179], v[64:67], v[100:103]\n" \
"ds_read_b128 v[92:95], v2 offset:15360\n" \
"s_cselect_b64 s[62:63], s[62:63], -1\n" \
"v_mfma_f32_16x16x32_bf16 v[104:107], v[176:179], v[64:67], v[104:107]\n" \
"s_waitcnt vmcnt(12) lgkmcnt(3)\n" \
"v_add_f32_e64 v120, v120, v124\n" \
"v_mfma_f32_16x16x32_bf16 v[108:111], v[80:83], v[64:67], v[108:111]\n" \
"buffer_load_dwordx4 v[64:67], v3, s[36:39], s57 offen offset:2048 sc1\n" \
"v_add_f32_e64 v121, v121, v125\n" \
"v_mfma_f32_16x16x32_bf16 v[96:99], a[52:55], v[68:71], v[96:99]\n" \
"s_cmp_lg_u64 s[62:63], 0\n" \
"s_cselect_b32 s81, 1, 0\n" \
"v_mfma_f32_16x16x32_bf16 v[100:103], a[180:183], v[68:71], v[100:103]\n" \
"ds_read_b128 v[80:83], v2 offset:16384\n" \
"s_cselect_b32 s83, 0x70000000, s83\n" \
"v_mfma_f32_16x16x32_bf16 v[104:107], v[180:183], v[68:71], v[104:107]\n" \
"s_waitcnt vmcnt(12) lgkmcnt(3)\n" \
"v_add_f32 v122, v122, v126\n" \
"v_mfma_f32_16x16x32_bf16 v[108:111], v[84:87], v[68:71], v[108:111]\n" \
"buffer_load_dwordx4 v[68:71], v3, s[36:39], s57 offen offset:3072 sc1\n" \
"v_add_f32_e64 v123, v123, v127\n" \
"v_mfma_f32_16x16x32_bf16 v[96:99], a[56:59], v[16:19], v[96:99]\n" \
"v_add_f32_e64 v112, v112, v120\n" \
"v_mfma_f32_16x16x32_bf16 v[100:103], a[184:187], v[16:19], v[100:103]\n" \
"ds_read_b128 v[84:87], v2 offset:17408\n" \
"v_add_f32_e64 v113, v113, v121\n" \
"v_mfma_f32_16x16x32_bf16 v[104:107], v[184:187], v[16:19], v[104:107]\n" \
"s_waitcnt vmcnt(12) lgkmcnt(3)\n" \
"s_mov_b32 s56, s83\n" \
"v_mfma_f32_16x16x32_bf16 v[108:111], v[88:91], v[16:19], v[108:111]\n" \
"s_xor_b32 s57, s56, s86\n" \
"buffer_load_dwordx4 v[16:19], v3, s[36:39], s57 offen offset:0 sc1\n" \
"v_mfma_f32_16x16x32_bf16 v[96:99], a[60:63], v[20:23], v[96:99]\n" \
"v_add_f32 v114, v114, v122\n" \
"v_mfma_f32_16x16x32_bf16 v[100:103], a[188:191], v[20:23], v[100:103]\n" \
"ds_read_b128 v[88:91], v2 offset:18432\n" \
"v_add_f32_e64 v115, v115, v123\n" \
"v_mfma_f32_16x16x32_bf16 v[104:107], v[188:191], v[20:23], v[104:107]\n" \
"s_waitcnt vmcnt(12) lgkmcnt(3)\n" \
"v_xor_b32 v8, s84, v8\n" \
"v_mfma_f32_16x16x32_bf16 v[108:111], v[92:95], v[20:23], v[108:111]\n" \
"buffer_load_dwordx4 v[20:23], v3, s[36:39], s57 offen offset:1024 sc1\n" \
"v_mul_f32 v116, 0xbfb8aa3b, v112\n" \
"v_mfma_f32_16x16x32_bf16 v[96:99], a[64:67], v[24:27], v[96:99]\n" \
"v_mul_f32 v117, 0xbfb8aa3b, v113\n" \
"v_mfma_f32_16x16x32_bf16 v[100:103], a[192:195], v[24:27], v[100:103]\n" \
"ds_read_b128 v[92:95], v2 offset:19456\n" \
"v_mul_f32 v118, 0xbfb8aa3b, v114\n" \
"v_mfma_f32_16x16x32_bf16 v[104:107], v[192:195], v[24:27], v[104:107]\n" \
"s_waitcnt vmcnt(12) lgkmcnt(3)\n" \
"v_mul_f32 v119, 0xbfb8aa3b, v115\n" \
"v_mfma_f32_16x16x32_bf16 v[108:111], v[80:83], v[24:27], v[108:111]\n" \
"buffer_load_dwordx4 v[24:27], v3, s[36:39], s57 offen offset:2048 sc1\n" \
"v_mfma_f32_16x16x32_bf16 v[96:99], a[68:71], v[28:31], v[96:99]\n" \
"v_exp_f32 v116, v116\n" \
"v_mfma_f32_16x16x32_bf16 v[100:103], a[196:199], v[28:31], v[100:103]\n" \
"ds_read_b128 v[80:83], v2 offset:20480\n" \
"v_mfma_f32_16x16x32_bf16 v[104:107], v[196:199], v[28:31], v[104:107]\n" \
"s_waitcnt vmcnt(12) lgkmcnt(3)\n" \
"v_exp_f32 v117, v117\n" \
"v_mfma_f32_16x16x32_bf16 v[108:111], v[84:87], v[28:31], v[108:111]\n" \
"buffer_load_dwordx4 v[28:31], v3, s[36:39], s57 offen offset:3072 sc1\n" \
"v_mfma_f32_16x16x32_bf16 v[96:99], a[72:75], v[32:35], v[96:99]\n" \
"v_exp_f32_e64 v118, v118\n" \
"v_mfma_f32_16x16x32_bf16 v[100:103], a[200:203], v[32:35], v[100:103]\n" \
"ds_read_b128 v[84:87], v2 offset:21504\n" \
"v_mfma_f32_16x16x32_bf16 v[104:107], v[200:203], v[32:35], v[104:107]\n" \
"s_waitcnt vmcnt(12) lgkmcnt(3)\n" \
"v_exp_f32 v119, v119\n" \
"v_mfma_f32_16x16x32_bf16 v[108:111], v[88:91], v[32:35], v[108:111]\n" \
"s_xor_b32 s57, s56, s87\n" \
"buffer_load_dwordx4 v[32:35], v3, s[36:39], s57 offen offset:0 sc1\n" \
"v_mfma_f32_16x16x32_bf16 v[96:99], a[76:79], v[36:39], v[96:99]\n" \
"v_add_f32 v116, 1.0, v116\n" \
"v_mfma_f32_16x16x32_bf16 v[100:103], a[204:207], v[36:39], v[100:103]\n" \
"ds_read_b128 v[88:91], v2 offset:22528\n" \
"v_add_f32_e64 v117, 1.0, v117\n" \
"v_mfma_f32_16x16x32_bf16 v[104:107], v[204:207], v[36:39], v[104:107]\n" \
"s_waitcnt vmcnt(12) lgkmcnt(3)\n" \
"v_add_f32 v118, 1.0, v118\n" \
"v_mfma_f32_16x16x32_bf16 v[108:111], v[92:95], v[36:39], v[108:111]\n" \
"buffer_load_dwordx4 v[36:39], v3, s[36:39], s57 offen offset:1024 sc1\n" \
"v_add_f32_e64 v119, 1.0, v119\n" \
"v_mfma_f32_16x16x32_bf16 v[96:99], a[80:83], v[40:43], v[96:99]\n" \
"v_rcp_f32_e64 v116, v116\n" \
"v_mfma_f32_16x16x32_bf16 v[100:103], a[208:211], v[40:43], v[100:103]\n" \
"ds_read_b128 v[92:95], v2 offset:23552\n" \
"v_mfma_f32_16x16x32_bf16 v[104:107], v[208:211], v[40:43], v[104:107]\n" \
"s_waitcnt vmcnt(12) lgkmcnt(3)\n" \
"v_rcp_f32 v117, v117\n" \
"v_mfma_f32_16x16x32_bf16 v[108:111], v[80:83], v[40:43], v[108:111]\n" \
"buffer_load_dwordx4 v[40:43], v3, s[36:39], s57 offen offset:2048 sc1\n" \
"v_mfma_f32_16x16x32_bf16 v[96:99], a[84:87], v[44:47], v[96:99]\n" \
"v_rcp_f32_e64 v118, v118\n" \
"v_mfma_f32_16x16x32_bf16 v[100:103], a[212:215], v[44:47], v[100:103]\n" \
"ds_read_b128 v[80:83], v2 offset:24576\n" \
"v_mfma_f32_16x16x32_bf16 v[104:107], v[212:215], v[44:47], v[104:107]\n" \
"s_waitcnt vmcnt(12) lgkmcnt(3)\n" \
"v_rcp_f32 v119, v119\n" \
"v_mfma_f32_16x16x32_bf16 v[108:111], v[84:87], v[44:47], v[108:111]\n" \
"buffer_load_dwordx4 v[44:47], v3, s[36:39], s57 offen offset:3072 sc1\n" \
"v_mul_f32_e64 v112, v112, v116\n" \
"v_mfma_f32_16x16x32_bf16 v[96:99], a[88:91], v[48:51], v[96:99]\n" \
"v_mul_f32_e64 v113, v113, v117\n" \
"v_mfma_f32_16x16x32_bf16 v[100:103], a[216:219], v[48:51], v[100:103]\n" \
"ds_read_b128 v[84:87], v2 offset:25600\n" \
"s_lshl_b32 s80, s44, 16\n" \
"v_mfma_f32_16x16x32_bf16 v[104:107], v[216:219], v[48:51], v[104:107]\n" \
"s_waitcnt vmcnt(12) lgkmcnt(3)\n" \
"s_add_u32 s80, s80, s78\n" \
"v_mfma_f32_16x16x32_bf16 v[108:111], v[88:91], v[48:51], v[108:111]\n" \
"s_xor_b32 s57, s56, s88\n" \
"buffer_load_dwordx4 v[48:51], v3, s[36:39], s57 offen offset:0 sc1\n" \
"v_mfma_f32_16x16x32_bf16 v[96:99], a[92:95], v[52:55], v[96:99]\n" \
"v_mul_f32_e64 v114, v114, v118\n" \
"v_mfma_f32_16x16x32_bf16 v[100:103], a[220:223], v[52:55], v[100:103]\n" \
"ds_read_b128 v[88:91], v2 offset:26624\n" \
"s_lshr_b32 s58, s80, 8\n" \
"v_mfma_f32_16x16x32_bf16 v[104:107], v[220:223], v[52:55], v[104:107]\n" \
"s_waitcnt vmcnt(12) lgkmcnt(3)\n" \
"s_and_b32 s58, s58, 0x2000\n" \
"v_mfma_f32_16x16x32_bf16 v[108:111], v[92:95], v[52:55], v[108:111]\n" \
"buffer_load_dwordx4 v[52:55], v3, s[36:39], s57 offen offset:1024 sc1\n" \
"v_mul_f32_e64 v115, v115, v119\n" \
"v_mfma_f32_16x16x32_bf16 v[96:99], a[96:99], v[56:59], v[96:99]\n" \
"v_cvt_pk_bf16_f32 v0, v112, v113\n" \
"v_mfma_f32_16x16x32_bf16 v[100:103], a[224:227], v[56:59], v[100:103]\n" \
"ds_read_b128 v[92:95], v2 offset:28672\n" \
"s_xor_b32 s80, s80, s58\n" \
"v_mfma_f32_16x16x32_bf16 v[104:107], v[224:227], v[56:59], v[104:107]\n" \
"s_waitcnt vmcnt(12) lgkmcnt(3)\n" \
"s_add_u32 s80, s80, 8388608\n" \
"v_mfma_f32_16x16x32_bf16 v[108:111], v[80:83], v[56:59], v[108:111]\n" \
"buffer_load_dwordx4 v[56:59], v3, s[36:39], s57 offen offset:2048 sc1\n" \
"v_cvt_pk_bf16_f32 v1, v114, v115\n" \
"v_mfma_f32_16x16x32_bf16 v[96:99], a[100:103], v[60:63], v[96:99]\n" \
"buffer_load_dword v11, v4, s[36:39], 0 offen sc1\n" \
"buffer_store_dwordx2 v[0:1], v6, s[36:39], s76 offen sc1\n" \
"v_mfma_f32_16x16x32_bf16 v[100:103], a[228:231], v[60:63], v[100:103]\n" \
"ds_read_b128 v[80:83], v2 offset:0\n" \
"v_mfma_f32_16x16x32_bf16 v[104:107], v[228:231], v[60:63], v[104:107]\n" \
"s_waitcnt vmcnt(14) lgkmcnt(3)\n" \
"v_mfma_f32_16x16x32_bf16 v[108:111], v[84:87], v[60:63], v[108:111]\n" \
"buffer_load_dwordx4 v[60:63], v3, s[36:39], s57 offen offset:3072 sc1\n" \
"v_mfma_f32_16x16x32_bf16 v[96:99], a[104:107], v[64:67], v[96:99]\n" \
"s_add_u32 s55, s55, 1\n" \
"s_mov_b32 s84, 0x4000\n" \
"v_mfma_f32_16x16x32_bf16 v[100:103], a[232:235], v[64:67], v[100:103]\n" \
"ds_read_b128 v[84:87], v2 offset:1024\n" \
"v_mfma_f32_16x16x32_bf16 v[104:107], v[232:235], v[64:67], v[104:107]\n" \
"s_waitcnt vmcnt(14) lgkmcnt(3)\n" \
"v_mfma_f32_16x16x32_bf16 v[108:111], v[88:91], v[64:67], v[108:111]\n" \
"s_xor_b32 s57, s56, s89\n" \
"buffer_load_dwordx4 v[64:67], v3, s[36:39], s57 offen offset:0 sc1\n" \
"v_mfma_f32_16x16x32_bf16 v[112:115], a[108:111], v[68:71], v[96:99]\n" \
"s_mov_b32 s76, s80\n" \
"v_mfma_f32_16x16x32_bf16 v[116:119], a[236:239], v[68:71], v[100:103]\n" \
"ds_read_b128 v[88:91], v2 offset:2048\n" \
"s_mov_b32 s44, s82\n" \
"v_mfma_f32_16x16x32_bf16 v[120:123], v[236:239], v[68:71], v[104:107]\n" \
"s_waitcnt vmcnt(14) lgkmcnt(3)\n" \
"v_mfma_f32_16x16x32_bf16 v[124:127], v[92:95], v[68:71], v[108:111]\n" \
"buffer_load_dwordx4 v[68:71], v3, s[36:39], s57 offen offset:1024 sc1\n" \
"s_cmp_eq_u32 s81, 0\n" \
"s_cbranch_scc1 .Lsg2_loop_%=\n" \
"s_nop 7\n" \
"s_nop 7\n" \
"s_waitcnt vmcnt(0) lgkmcnt(0)\n" \
"ds_write_b128 v7, v[112:115] offset:0\n" \
"v_xor_b32 v7, 64, v7\n" \
"ds_write_b128 v7, v[116:119] offset:1024\n" \
"v_xor_b32 v7, 192, v7\n" \
"ds_write_b128 v7, v[120:123] offset:2048\n" \
"v_xor_b32 v7, 64, v7\n" \
"ds_write_b128 v7, v[124:127] offset:3072\n" \
"v_xor_b32 v7, 192, v7\n" \
"s_waitcnt lgkmcnt(0)\n" \
"s_barrier\n" \
"ds_read_b128 v[112:115], v8 offset:0\n" \
"ds_read_b128 v[116:119], v8 offset:4096\n" \
"ds_read_b128 v[120:123], v8 offset:8192\n" \
"ds_read_b128 v[124:127], v8 offset:12288\n" \
"s_waitcnt lgkmcnt(2)\n" \
"v_add_f32 v112, v112, v116\n" \
"v_add_f32 v113, v113, v117\n" \
"v_add_f32 v114, v114, v118\n" \
"v_add_f32 v115, v115, v119\n" \
"s_waitcnt lgkmcnt(0)\n" \
"v_add_f32 v120, v120, v124\n" \
"v_add_f32 v121, v121, v125\n" \
"v_add_f32 v122, v122, v126\n" \
"v_add_f32 v123, v123, v127\n" \
"v_add_f32 v112, v112, v120\n" \
"v_add_f32 v113, v113, v121\n" \
"v_add_f32 v114, v114, v122\n" \
"v_add_f32 v115, v115, v123\n" \
"v_xor_b32 v8, s84, v8\n" \
"v_mul_f32 v116, 0xbfb8aa3b, v112\n" \
"v_mul_f32 v117, 0xbfb8aa3b, v113\n" \
"v_mul_f32 v118, 0xbfb8aa3b, v114\n" \
"v_mul_f32 v119, 0xbfb8aa3b, v115\n" \
"v_exp_f32 v116, v116\n" \
"v_exp_f32 v117, v117\n" \
"v_exp_f32 v118, v118\n" \
"v_exp_f32 v119, v119\n" \
"v_add_f32 v116, 1.0, v116\n" \
"v_add_f32 v117, 1.0, v117\n" \
"v_add_f32 v118, 1.0, v118\n" \
"v_add_f32 v119, 1.0, v119\n" \
"v_rcp_f32 v116, v116\n" \
"v_rcp_f32 v117, v117\n" \
"v_rcp_f32 v118, v118\n" \
"v_rcp_f32 v119, v119\n" \
"v_mul_f32 v112, v112, v116\n" \
"v_mul_f32 v113, v113, v117\n" \
"v_mul_f32 v114, v114, v118\n" \
"v_mul_f32 v115, v115, v119\n" \
"v_cvt_pk_bf16_f32 v0, v112, v113\n" \
"v_cvt_pk_bf16_f32 v1, v114, v115\n" \
"v_xor_b32 v7, 0x4000, v7\n" \
"buffer_store_dwordx2 v[0:1], v6, s[36:39], s76 offen sc1\n" \
"s_cmp_lg_u32 s55, s95\n" \
"s_cbranch_scc1 .Lsg2_g_%=\n" \
"s_waitcnt vmcnt(0) lgkmcnt(0)\n" \
"v_mov_b32 v9, 1\n" \
"s_mov_b64 exec, 1\n" \
"buffer_atomic_add v9, off, s[36:39], s49 offset:64\n" \
"s_mov_b64 exec, -1\n" \
"s_mov_b32 s79, s46\n" \
".Lsg2_g_%=:\n" \
"s_cmp_lt_u32 s55, s46\n" \
"s_cbranch_scc1 .Lsg2_prime_%=\n" \
"s_branch .Lsg2_o_%=\n" \
".Lsg2_ok_%=:\n" \
"s_mov_b32 s44, s94\n" \
".Lsg2_c_%=:\n" \
"s_nop 7\n" \
"s_nop 7\n" \
"s_lshl_b32 s54, s44, 16\n" \
"s_add_u32 s54, s54, s78\n" \
"s_lshr_b32 s58, s54, 8\n" \
"s_and_b32 s58, s58, 0x2000\n" \
"s_xor_b32 s54, s54, s58\n" \
"s_lshl_b32 s80, s44, 15\n" \
"s_cmp_lt_u32 s44, s96\n" \
"s_cselect_b32 s80, s80, 0x70000000\n" \
"s_cselect_b32 s54, s54, 0x70000000\n" \
"s_add_u32 s55, s54, 8388608\n" \
"s_cmp_lt_i32 s44, s50\n" \
"s_cselect_b32 s58, s45, s47\n" \
"s_add_u32 s44, s44, s58\n" \
"buffer_load_dwordx2 v[128:129], v6, s[36:39], s54 offen sc1\n" \
"buffer_load_dwordx2 v[130:131], v6, s[36:39], s55 offen sc1\n" \
"buffer_load_dwordx2 v[132:133], v5, s[64:67], s80 offen\n" \
"s_lshl_b32 s54, s44, 16\n" \
"s_add_u32 s54, s54, s78\n" \
"s_lshr_b32 s58, s54, 8\n" \
"s_and_b32 s58, s58, 0x2000\n" \
"s_xor_b32 s54, s54, s58\n" \
"s_lshl_b32 s81, s44, 15\n" \
"s_cmp_lt_u32 s44, s96\n" \
"s_cselect_b32 s81, s81, 0x70000000\n" \
"s_cselect_b32 s54, s54, 0x70000000\n" \
"s_add_u32 s55, s54, 8388608\n" \
"s_cmp_lt_i32 s44, s50\n" \
"s_cselect_b32 s58, s45, s47\n" \
"s_add_u32 s44, s44, s58\n" \
"buffer_load_dwordx2 v[134:135], v6, s[36:39], s54 offen sc1\n" \
"buffer_load_dwordx2 v[136:137], v6, s[36:39], s55 offen sc1\n" \
"buffer_load_dwordx2 v[138:139], v5, s[64:67], s81 offen\n" \
"s_lshl_b32 s54, s44, 16\n" \
"s_add_u32 s54, s54, s78\n" \
"s_lshr_b32 s58, s54, 8\n" \
"s_and_b32 s58, s58, 0x2000\n" \
"s_xor_b32 s54, s54, s58\n" \
"s_lshl_b32 s82, s44, 15\n" \
"s_cmp_lt_u32 s44, s96\n" \
"s_cselect_b32 s82, s82, 0x70000000\n" \
"s_cselect_b32 s54, s54, 0x70000000\n" \
"s_add_u32 s55, s54, 8388608\n" \
"s_cmp_lt_i32 s44, s50\n" \
"s_cselect_b32 s58, s45, s47\n" \
"s_add_u32 s44, s44, s58\n" \
"buffer_load_dwordx2 v[140:141], v6, s[36:39], s54 offen sc1\n" \
"buffer_load_dwordx2 v[142:143], v6, s[36:39], s55 offen sc1\n" \
"buffer_load_dwordx2 v[144:145], v5, s[64:67], s82 offen\n" \
"s_lshl_b32 s54, s44, 16\n" \
"s_add_u32 s54, s54, s78\n" \
"s_lshr_b32 s58, s54, 8\n" \
"s_and_b32 s58, s58, 0x2000\n" \
"s_xor_b32 s54, s54, s58\n" \
"s_lshl_b32 s83, s44, 15\n" \
"s_cmp_lt_u32 s44, s96\n" \
"s_cselect_b32 s83, s83, 0x70000000\n" \
"s_cselect_b32 s54, s54, 0x70000000\n" \
"s_add_u32 s55, s54, 8388608\n" \
"s_cmp_lt_i32 s44, s50\n" \
"s_cselect_b32 s58, s45, s47\n" \
"s_add_u32 s44, s44, s58\n" \
"buffer_load_dwordx2 v[146:147], v6, s[36:39], s54 offen sc1\n" \
"buffer_load_dwordx2 v[148:149], v6, s[36:39], s55 offen sc1\n" \
"buffer_load_dwordx2 v[150:151], v5, s[64:67], s83 offen\n" \
"s_lshl_b32 s54, s44, 16\n" \
"s_add_u32 s54, s54, s78\n" \
"s_lshr_b32 s58, s54, 8\n" \
"s_and_b32 s58, s58, 0x2000\n" \
"s_xor_b32 s54, s54, s58\n" \
"s_lshl_b32 s84, s44, 15\n" \
"s_cmp_lt_u32 s44, s96\n" \
"s_cselect_b32 s84, s84, 0x70000000\n" \
"s_cselect_b32 s54, s54, 0x70000000\n" \
"s_add_u32 s55, s54, 8388608\n" \
"s_cmp_lt_i32 s44, s50\n" \
"s_cselect_b32 s58, s45, s47\n" \
"s_add_u32 s44, s44, s58\n" \
"buffer_load_dwordx2 v[152:153], v6, s[36:39], s54 offen sc1\n" \
"buffer_load_dwordx2 v[154:155], v6, s[36:39], s55 offen sc1\n" \
"buffer_load_dwordx2 v[156:157], v5, s[64:67], s84 offen\n" \
"s_lshl_b32 s54, s44, 16\n" \
"s_add_u32 s54, s54, s78\n" \
"s_lshr_b32 s58, s54, 8\n" \
"s_and_b32 s58, s58, 0x2000\n" \
"s_xor_b32 s54, s54, s58\n" \
"s_lshl_b32 s85, s44, 15\n" \
"s_cmp_lt_u32 s44, s96\n" \
"s_cselect_b32 s85, s85, 0x70000000\n" \
"s_cselect_b32 s54, s54, 0x70000000\n" \
"s_add_u32 s55, s54, 8388608\n" \
"s_cmp_lt_i32 s44, s50\n" \
"s_cselect_b32 s58, s45, s47\n" \
"s_add_u32 s44, s44, s58\n" \
"buffer_load_dwordx2 v[158:159], v6, s[36:39], s54 offen sc1\n" \
"buffer_load_dwordx2 v[160:161], v6, s[36:39], s55 offen sc1\n" \
"buffer_load_dwordx2 v[162:163], v5, s[64:67], s85 offen\n" \
"s_lshl_b32 s54, s44, 16\n" \
"s_add_u32 s54, s54, s78\n" \
"s_lshr_b32 s58, s54, 8\n" \
"s_and_b32 s58, s58, 0x2000\n" \
"s_xor_b32 s54, s54, s58\n" \
"s_lshl_b32 s86, s44, 15\n" \
"s_cmp_lt_u32 s44, s96\n" \
"s_cselect_b32 s86, s86, 0x70000000\n" \
"s_cselect_b32 s54, s54, 0x70000000\n" \
"s_add_u32 s55, s54, 8388608\n" \
"s_cmp_lt_i32 s44, s50\n" \
"s_cselect_b32 s58, s45, s47\n" \
"s_add_u32 s44, s44, s58\n" \
"buffer_load_dwordx2 v[164:165], v6, s[36:39], s54 offen sc1\n" \
"buffer_load_dwordx2 v[166:167], v6, s[36:39], s55 offen sc1\n" \
"buffer_load_dwordx2 v[168:169], v5, s[64:67], s86 offen\n" \
"s_lshl_b32 s54, s44, 16\n" \
"s_add_u32 s54, s54, s78\n" \
"s_lshr_b32 s58, s54, 8\n" \
"s_and_b32 s58, s58, 0x2000\n" \
"s_xor_b32 s54, s54, s58\n" \
"s_lshl_b32 s87, s44, 15\n" \
"s_cmp_lt_u32 s44, s96\n" \
"s_cselect_b32 s87, s87, 0x70000000\n" \
"s_cselect_b32 s54, s54, 0x70000000\n" \
"s_add_u32 s55, s54, 8388608\n" \
"s_cmp_lt_i32 s44, s50\n" \
"s_cselect_b32 s58, s45, s47\n" \
"s_add_u32 s44, s44, s58\n" \
"buffer_load_dwordx2 v[170:171], v6, s[36:39], s54 offen sc1\n" \
"buffer_load_dwordx2 v[172:173], v6, s[36:39], s55 offen sc1\n" \
"buffer_load_dwordx2 v[174:175], v5, s[64:67], s87 offen\n" \
"s_lshl_b32 s54, s44, 16\n" \
"s_add_u32 s54, s54, s78\n" \
"s_lshr_b32 s58, s54, 8\n" \
"s_and_b32 s58, s58, 0x2000\n" \
"s_xor_b32 s54, s54, s58\n" \
"s_lshl_b32 s88, s44, 15\n" \
"s_cmp_lt_u32 s44, s96\n" \
"s_cselect_b32 s88, s88, 0x70000000\n" \
"s_cselect_b32 s54, s54, 0x70000000\n" \
"s_add_u32 s55, s54, 8388608\n" \
"s_cmp_lt_i32 s44, s50\n" \
"s_cselect_b32 s58, s45, s47\n" \
"s_add_u32 s44, s44, s58\n" \
"buffer_load_dwordx2 v[176:177], v6, s[36:39], s54 offen sc1\n" \
"buffer_load_dwordx2 v[178:179], v6, s[36:39], s55 offen sc1\n" \
"buffer_load_dwordx2 v[180:181], v5, s[64:67], s88 offen\n" \
"s_lshl_b32 s54, s44, 16\n" \
"s_add_u32 s54, s54, s78\n" \
"s_lshr_b32 s58, s54, 8\n" \
"s_and_b32 s58, s58, 0x2000\n" \
"s_xor_b32 s54, s54, s58\n" \
"s_lshl_b32 s89, s44, 15\n" \
"s_cmp_lt_u32 s44, s96\n" \
"s_cselect_b32 s89, s89, 0x70000000\n" \
"s_cselect_b32 s54, s54, 0x70000000\n" \
"s_add_u32 s55, s54, 8388608\n" \
"s_cmp_lt_i32 s44, s50\n" \
"s_cselect_b32 s58, s45, s47\n" \
"s_add_u32 s44, s44, s58\n" \
"buffer_load_dwordx2 v[182:183], v6, s[36:39], s54 offen sc1\n" \
"buffer_load_dwordx2 v[184:185], v6, s[36:39], s55 offen sc1\n" \
"buffer_load_dwordx2 v[186:187], v5, s[64:67], s89 offen\n" \
"s_waitcnt vmcnt(29)\n" \
"v_lshlrev_b32 v112, 16, v128\n" \
"v_and_b32 v113, 0xffff0000, v128\n" \
"v_lshlrev_b32 v114, 16, v129\n" \
"v_and_b32 v115, 0xffff0000, v129\n" \
"s_waitcnt vmcnt(28)\n" \
"v_lshlrev_b32 v116, 16, v130\n" \
"v_and_b32 v117, 0xffff0000, v130\n" \
"v_lshlrev_b32 v118, 16, v131\n" \
"v_and_b32 v119, 0xffff0000, v131\n" \
"s_waitcnt vmcnt(27)\n" \
"v_lshlrev_b32 v120, 16, v132\n" \
"v_and_b32 v121, 0xffff0000, v132\n" \
"v_lshlrev_b32 v122, 16, v133\n" \
"v_and_b32 v123, 0xffff0000, v133\n" \
"v_fmac_f32 v120, v112, v116\n" \
"v_fmac_f32 v121, v113, v117\n" \
"v_fmac_f32 v122, v114, v118\n" \
"v_fmac_f32 v123, v115, v119\n" \
"v_cvt_pk_bf16_f32 v132, v120, v121\n" \
"v_cvt_pk_bf16_f32 v133, v122, v123\n" \
"buffer_store_dwordx2 v[132:133], v5, s[68:71], s80 offen\n" \
"s_waitcnt vmcnt(27)\n" \
"v_lshlrev_b32 v112, 16, v134\n" \
"v_and_b32 v113, 0xffff0000, v134\n" \
"v_lshlrev_b32 v114, 16, v135\n" \
"v_and_b32 v115, 0xffff0000, v135\n" \
"s_waitcnt vmcnt(26)\n" \
"v_lshlrev_b32 v116, 16, v136\n" \
"v_and_b32 v117, 0xffff0000, v136\n" \
"v_lshlrev_b32 v118, 16, v137\n" \
"v_and_b32 v119, 0xffff0000, v137\n" \
"s_waitcnt vmcnt(25)\n" \
"v_lshlrev_b32 v120, 16, v138\n" \
"v_and_b32 v121, 0xffff0000, v138\n" \
"v_lshlrev_b32 v122, 16, v139\n" \
"v_and_b32 v123, 0xffff0000, v139\n" \
"v_fmac_f32 v120, v112, v116\n" \
"v_fmac_f32 v121, v113, v117\n" \
"v_fmac_f32 v122, v114, v118\n" \
"v_fmac_f32 v123, v115, v119\n" \
"v_cvt_pk_bf16_f32 v138, v120, v121\n" \
"v_cvt_pk_bf16_f32 v139, v122, v123\n" \
"buffer_store_dwordx2 v[138:139], v5, s[68:71], s81 offen\n" \
"s_waitcnt vmcnt(25)\n" \
"v_lshlrev_b32 v112, 16, v140\n" \
"v_and_b32 v113, 0xffff0000, v140\n" \
"v_lshlrev_b32 v114, 16, v141\n" \
"v_and_b32 v115, 0xffff0000, v141\n" \
"s_waitcnt vmcnt(24)\n" \
"v_lshlrev_b32 v116, 16, v142\n" \
"v_and_b32 v117, 0xffff0000, v142\n" \
"v_lshlrev_b32 v118, 16, v143\n" \
"v_and_b32 v119, 0xffff0000, v143\n" \
"s_waitcnt vmcnt(23)\n" \
"v_lshlrev_b32 v120, 16, v144\n" \
"v_and_b32 v121, 0xffff0000, v144\n" \
"v_lshlrev_b32 v122, 16, v145\n" \
"v_and_b32 v123, 0xffff0000, v145\n" \
"v_fmac_f32 v120, v112, v116\n" \
"v_fmac_f32 v121, v113, v117\n" \
"v_fmac_f32 v122, v114, v118\n" \
"v_fmac_f32 v123, v115, v119\n" \
"v_cvt_pk_bf16_f32 v144, v120, v121\n" \
"v_cvt_pk_bf16_f32 v145, v122, v123\n" \
"buffer_store_dwordx2 v[144:145], v5, s[68:71], s82 offen\n" \
"s_waitcnt vmcnt(23)\n" \
"v_lshlrev_b32 v112, 16, v146\n" \
"v_and_b32 v113, 0xffff0000, v146\n" \
"v_lshlrev_b32 v114, 16, v147\n" \
"v_and_b32 v115, 0xffff0000, v147\n" \
"s_waitcnt vmcnt(22)\n" \
"v_lshlrev_b32 v116, 16, v148\n" \
"v_and_b32 v117, 0xffff0000, v148\n" \
"v_lshlrev_b32 v118, 16, v149\n" \
"v_and_b32 v119, 0xffff0000, v149\n" \
"s_waitcnt vmcnt(21)\n" \
"v_lshlrev_b32 v120, 16, v150\n" \
"v_and_b32 v121, 0xffff0000, v150\n" \
"v_lshlrev_b32 v122, 16, v151\n" \
"v_and_b32 v123, 0xffff0000, v151\n" \
"v_fmac_f32 v120, v112, v116\n" \
"v_fmac_f32 v121, v113, v117\n" \
"v_fmac_f32 v122, v114, v118\n" \
"v_fmac_f32 v123, v115, v119\n" \
"v_cvt_pk_bf16_f32 v150, v120, v121\n" \
"v_cvt_pk_bf16_f32 v151, v122, v123\n" \
"buffer_store_dwordx2 v[150:151], v5, s[68:71], s83 offen\n" \
"s_waitcnt vmcnt(21)\n" \
"v_lshlrev_b32 v112, 16, v152\n" \
"v_and_b32 v113, 0xffff0000, v152\n" \
"v_lshlrev_b32 v114, 16, v153\n" \
"v_and_b32 v115, 0xffff0000, v153\n" \
"s_waitcnt vmcnt(20)\n" \
"v_lshlrev_b32 v116, 16, v154\n" \
"v_and_b32 v117, 0xffff0000, v154\n" \
"v_lshlrev_b32 v118, 16, v155\n" \
"v_and_b32 v119, 0xffff0000, v155\n" \
"s_waitcnt vmcnt(19)\n" \
"v_lshlrev_b32 v120, 16, v156\n" \
"v_and_b32 v121, 0xffff0000, v156\n" \
"v_lshlrev_b32 v122, 16, v157\n" \
"v_and_b32 v123, 0xffff0000, v157\n" \
"v_fmac_f32 v120, v112, v116\n" \
"v_fmac_f32 v121, v113, v117\n" \
"v_fmac_f32 v122, v114, v118\n" \
"v_fmac_f32 v123, v115, v119\n" \
"v_cvt_pk_bf16_f32 v156, v120, v121\n" \
"v_cvt_pk_bf16_f32 v157, v122, v123\n" \
"buffer_store_dwordx2 v[156:157], v5, s[68:71], s84 offen\n" \
"s_waitcnt vmcnt(19)\n" \
"v_lshlrev_b32 v112, 16, v158\n" \
"v_and_b32 v113, 0xffff0000, v158\n" \
"v_lshlrev_b32 v114, 16, v159\n" \
"v_and_b32 v115, 0xffff0000, v159\n" \
"s_waitcnt vmcnt(18)\n" \
"v_lshlrev_b32 v116, 16, v160\n" \
"v_and_b32 v117, 0xffff0000, v160\n" \
"v_lshlrev_b32 v118, 16, v161\n" \
"v_and_b32 v119, 0xffff0000, v161\n" \
"s_waitcnt vmcnt(17)\n" \
"v_lshlrev_b32 v120, 16, v162\n" \
"v_and_b32 v121, 0xffff0000, v162\n" \
"v_lshlrev_b32 v122, 16, v163\n" \
"v_and_b32 v123, 0xffff0000, v163\n" \
"v_fmac_f32 v120, v112, v116\n" \
"v_fmac_f32 v121, v113, v117\n" \
"v_fmac_f32 v122, v114, v118\n" \
"v_fmac_f32 v123, v115, v119\n" \
"v_cvt_pk_bf16_f32 v162, v120, v121\n" \
"v_cvt_pk_bf16_f32 v163, v122, v123\n" \
"buffer_store_dwordx2 v[162:163], v5, s[68:71], s85 offen\n" \
"s_waitcnt vmcnt(17)\n" \
"v_lshlrev_b32 v112, 16, v164\n" \
"v_and_b32 v113, 0xffff0000, v164\n" \
"v_lshlrev_b32 v114, 16, v165\n" \
"v_and_b32 v115, 0xffff0000, v165\n" \
"s_waitcnt vmcnt(16)\n" \
"v_lshlrev_b32 v116, 16, v166\n" \
"v_and_b32 v117, 0xffff0000, v166\n" \
"v_lshlrev_b32 v118, 16, v167\n" \
"v_and_b32 v119, 0xffff0000, v167\n" \
"s_waitcnt vmcnt(15)\n" \
"v_lshlrev_b32 v120, 16, v168\n" \
"v_and_b32 v121, 0xffff0000, v168\n" \
"v_lshlrev_b32 v122, 16, v169\n" \
"v_and_b32 v123, 0xffff0000, v169\n" \
"v_fmac_f32 v120, v112, v116\n" \
"v_fmac_f32 v121, v113, v117\n" \
"v_fmac_f32 v122, v114, v118\n" \
"v_fmac_f32 v123, v115, v119\n" \
"v_cvt_pk_bf16_f32 v168, v120, v121\n" \
"v_cvt_pk_bf16_f32 v169, v122, v123\n" \
"buffer_store_dwordx2 v[168:169], v5, s[68:71], s86 offen\n" \
"s_waitcnt vmcnt(15)\n" \
"v_lshlrev_b32 v112, 16, v170\n" \
"v_and_b32 v113, 0xffff0000, v170\n" \
"v_lshlrev_b32 v114, 16, v171\n" \
"v_and_b32 v115, 0xffff0000, v171\n" \
"s_waitcnt vmcnt(14)\n" \
"v_lshlrev_b32 v116, 16, v172\n" \
"v_and_b32 v117, 0xffff0000, v172\n" \
"v_lshlrev_b32 v118, 16, v173\n" \
"v_and_b32 v119, 0xffff0000, v173\n" \
"s_waitcnt vmcnt(13)\n" \
"v_lshlrev_b32 v120, 16, v174\n" \
"v_and_b32 v121, 0xffff0000, v174\n" \
"v_lshlrev_b32 v122, 16, v175\n" \
"v_and_b32 v123, 0xffff0000, v175\n" \
"v_fmac_f32 v120, v112, v116\n" \
"v_fmac_f32 v121, v113, v117\n" \
"v_fmac_f32 v122, v114, v118\n" \
"v_fmac_f32 v123, v115, v119\n" \
"v_cvt_pk_bf16_f32 v174, v120, v121\n" \
"v_cvt_pk_bf16_f32 v175, v122, v123\n" \
"buffer_store_dwordx2 v[174:175], v5, s[68:71], s87 offen\n" \
"s_waitcnt vmcnt(13)\n" \
"v_lshlrev_b32 v112, 16, v176\n" \
"v_and_b32 v113, 0xffff0000, v176\n" \
"v_lshlrev_b32 v114, 16, v177\n" \
"v_and_b32 v115, 0xffff0000, v177\n" \
"s_waitcnt vmcnt(12)\n" \
"v_lshlrev_b32 v116, 16, v178\n" \
"v_and_b32 v117, 0xffff0000, v178\n" \
"v_lshlrev_b32 v118, 16, v179\n" \
"v_and_b32 v119, 0xffff0000, v179\n" \
"s_waitcnt vmcnt(11)\n" \
"v_lshlrev_b32 v120, 16, v180\n" \
"v_and_b32 v121, 0xffff0000, v180\n" \
"v_lshlrev_b32 v122, 16, v181\n" \
"v_and_b32 v123, 0xffff0000, v181\n" \
"v_fmac_f32 v120, v112, v116\n" \
"v_fmac_f32 v121, v113, v117\n" \
"v_fmac_f32 v122, v114, v118\n" \
"v_fmac_f32 v123, v115, v119\n" \
"v_cvt_pk_bf16_f32 v180, v120, v121\n" \
"v_cvt_pk_bf16_f32 v181, v122, v123\n" \
"buffer_store_dwordx2 v[180:181], v5, s[68:71], s88 offen\n" \
"s_waitcnt vmcnt(11)\n" \
"v_lshlrev_b32 v112, 16, v182\n" \
"v_and_b32 v113, 0xffff0000, v182\n" \
"v_lshlrev_b32 v114, 16, v183\n" \
"v_and_b32 v115, 0xffff0000, v183\n" \
"s_waitcnt vmcnt(10)\n" \
"v_lshlrev_b32 v116, 16, v184\n" \
"v_and_b32 v117, 0xffff0000, v184\n" \
"v_lshlrev_b32 v118, 16, v185\n" \
"v_and_b32 v119, 0xffff0000, v185\n" \
"s_waitcnt vmcnt(9)\n" \
"v_lshlrev_b32 v120, 16, v186\n" \
"v_and_b32 v121, 0xffff0000, v186\n" \
"v_lshlrev_b32 v122, 16, v187\n" \
"v_and_b32 v123, 0xffff0000, v187\n" \
"v_fmac_f32 v120, v112, v116\n" \
"v_fmac_f32 v121, v113, v117\n" \
"v_fmac_f32 v122, v114, v118\n" \
"v_fmac_f32 v123, v115, v119\n" \
"v_cvt_pk_bf16_f32 v186, v120, v121\n" \
"v_cvt_pk_bf16_f32 v187, v122, v123\n" \
"buffer_store_dwordx2 v[186:187], v5, s[68:71], s89 offen\n" \
"s_cmp_lt_u32 s44, s96\n" \
"s_cbranch_scc1 .Lsg2_c_%=\n" \
"s_branch .Lsg2_end_%=\n" \
".Lsg2_w_%=:\n" \
"s_nop 7\n" \
"s_nop 7\n" \
"s_waitcnt vmcnt(0) lgkmcnt(0)\n" \
"s_mov_b32 s61, 0\n" \
".Lsg2_wa_%=:\n" \
"s_cmp_lg_u32 s60, 0\n" \
"s_cbranch_scc1 .Lsg2_wk_%=\n" \
"buffer_load_dword v11, v4, s[36:39], 0 offen sc1\n" \
"s_waitcnt vmcnt(0)\n" \
"v_cmp_gt_u32_e64 s[62:63], s59, v11\n" \
"s_nop 4\n" \
"s_cmp_eq_u64 s[62:63], 0\n" \
"s_cbranch_scc1 .Lsg2_wk_%=\n" \
"s_sleep 1\n" \
"s_add_u32 s61, s61, 1\n" \
"s_cmp_lt_u32 s61, 65536\n" \
"s_cbranch_scc1 .Lsg2_wa_%=\n" \
"s_mov_b32 s60, 1\n" \
"v_mov_b32 v11, 256\n" \
"v_mov_b32 v9, 1\n" \
"buffer_store_dword v9, v11, s[36:39], 0 offen sc1\n" \
"s_waitcnt vmcnt(0) lgkmcnt(0)\n" \
"s_branch .Lsg2_wk_%=\n" \
".Lsg2_o_%=:\n" \
"s_nop 7\n" \
"s_nop 7\n" \
"s_waitcnt vmcnt(0) lgkmcnt(0)\n" \
"s_mov_b32 s61, 0\n" \
".Lsg2_oa_%=:\n" \
"s_cmp_lg_u32 s60, 0\n" \
"s_cbranch_scc1 .Lsg2_ok_%=\n" \
"buffer_load_dword v9, off, s[36:39], s49 offset:0 sc1\n" \
"s_sub_u32 s54, 10, s47\n" \
"s_waitcnt vmcnt(0)\n" \
"v_cmp_gt_u32 vcc, s54, v9\n" \
"s_nop 4\n" \
"s_cbranch_vccz .Lsg2_ok_%=\n" \
"s_sleep 1\n" \
"s_add_u32 s61, s61, 1\n" \
"s_cmp_lt_u32 s61, 65536\n" \
"s_cbranch_scc1 .Lsg2_oa_%=\n" \
"s_mov_b32 s60, 1\n" \
"v_mov_b32 v11, 256\n" \
"v_mov_b32 v9, 1\n" \
"buffer_store_dword v9, v11, s[36:39], 0 offen sc1\n" \
"s_waitcnt vmcnt(0) lgkmcnt(0)\n" \
"s_branch .Lsg2_ok_%=\n" \
".Lsg2_end_%=:\n" \
"s_endpgm\n"
#define OHACK 0
#define TUNEV(i, c) (c)
#define C_MO 5
#define C_MM 6
#define C_FAR 1
#define FARP (TUNEV(11, C_FAR) << 13)
#define C_X 108
#define C_Y 82
#define C_DS 36
#define C_VD 12
#define C_YL 9
#define C_D0 50
#define C_Y0 0
#define C_GD 265
#define C_PW3 4
#define C_TK 0
__device__ __forceinline__ int tkbase(const Params& p, int h, int fp) { const int m = TUNEV(8, C_TK); return WS_TK + h * (4 * MB + 65536) + (fp ^ (m == 2 ? (h & 8) << 10 : (m ^ 1) << 13)); }

typedef u32 v32u __attribute__((ext_vector_type(32)));
typedef float f32x2 __attribute__((ext_vector_type(2)));
__device__ __forceinline__ float xsum(float s) {
    auto a = __builtin_amdgcn_permlane16_swap(__builtin_bit_cast(u32, s), __builtin_bit_cast(u32, s), false, false);
    s = __builtin_bit_cast(float, (u32)a[0]) + __builtin_bit_cast(float, (u32)a[1]);
    auto b = __builtin_amdgcn_permlane32_swap(__builtin_bit_cast(u32, s), __builtin_bit_cast(u32, s), false, false);
    return __builtin_bit_cast(float, (u32)b[0]) + __builtin_bit_cast(float, (u32)b[1]);
}
__device__ __forceinline__ void ln_tile(const Params& p, const v4u (&xv)[8], int rt, int w, int lane, int fp) {
    const int n = lane & 15, g = lane >> 4;
    const rsrc_t rs = __builtin_amdgcn_make_buffer_rsrc(p.ws, 0, WS_END, 0x00020000);
    LAS float* red = (LAS float*)(uintptr_t)(136 * 1024);
    f32x2 xf[32], s2 = {0.f, 0.f};
#pragma unroll
    for (int i = 0; i < 32; ++i) { xf[i] = f32x2{bflo(xv[i >> 2][i & 3]), bfhi(xv[i >> 2][i & 3])}; s2 += xf[i]; }
    const float s = xsum(s2.x + s2.y);
    if (g == 0) red[n * 4 + w] = s;
    BAR();
    f32x4 t = *(LAS f32x4*)(red + n * 4);
    const float mean = ((t.x + t.y) + (t.z + t.w)) * (1.0f / DM);
    f32x2 q2 = {0.f, 0.f};
#pragma unroll
    for (int i = 0; i < 32; ++i) { const f32x2 a = xf[i] - mean; q2 += a * a; }
    const float q = xsum(q2.x + q2.y);
    if (g == 0) red[64 + n * 4 + w] = q;
    BAR();
    t = *(LAS f32x4*)(red + 64 + n * 4);
    const float var = ((t.x + t.y) + (t.z + t.w)) * (1.0f / DM);
    const float mb = rbf(mean);
    const float rd = 1.0f / rbf(sqrtf(rbf(rbf(var) + 1e-5f)));
#pragma unroll
    for (int i = 0; i < 8; ++i) {
        v4u o;
#pragma unroll
        for (int e = 0; e < 4; ++e) { const f32x2 a = xf[4 * i + e] - mb; const u32 d = pack2(a.x, a.y); const f32x2 y = f32x2{bflo(d), bfhi(d)} * rd; o[e] = pack2(y.x, y.y); }
        st128s(o, rs, farx(WS_XF + rt * 65536 + w * 16384 + fp) + i * 1024 + lane * 16);
    }
    WAIT_V0();
    __builtin_amdgcn_s_barrier(); CFENCE();
    if (w == 0 && lane == 0) __hip_atomic_fetch_add((GAS u32*)(p.ws + WS_CT) + CT_LN + (rt >> 4) * 32, 1u, RLXA);
}
__device__ __forceinline__ void ln_role(const Params& p, int c, int w, int fp) {
    const int lane = threadIdx.x & 63;
    const u16* xr = p.x + (size_t)(c * 16 + (lane & 15)) * DM + w * 256 + (lane >> 4) * 8;
    v4u xa[8], xb[8];
#pragma unroll
    for (int i = 0; i < 8; ++i) { xa[i] = *(const v4u*)(xr + i * 32); xb[i] = *(const v4u*)(xr + 128 * DM + i * 32); }
#pragma unroll 1
    for (int k = 0; k < 16; ++k) {
        v4u xc[8];
#pragma unroll
        for (int i = 0; i < 8; ++i) xc[i] = *(const v4u*)(xr + (size_t)min(k + 2, 15) * 128 * DM + i * 32);
        ln_tile(p, xa, k * 8 + c, w, lane, fp);
#pragma unroll
        for (int i = 0; i < 8; ++i) { xa[i] = xb[i]; xb[i] = xc[i]; }
    }
}

__device__ __forceinline__ void p1_role(const Params& p, int b, int s0, int s1, int id, int w, int fp) {
    const int lane = __builtin_amdgcn_mbcnt_hi(~0u, __builtin_amdgcn_mbcnt_lo(~0u, 0u)), n = lane & 15, g = lane >> 4;
    const bool g1 = b >= 96;
    const int h = b & 31, kind = b >> 5;
    const int stride = g1 ? FF * 2 : 8192;
    const u16* src = g1 ? colsrc2(p, w >> 1, b - 96) : colsrc(p, kind == 0 ? 1 : (kind == 1 ? 0 : 2), 2 * h + (w >> 1));
    const v4u rs = mksrd(p.ws, WS_END), rw = mksrd(src, 1023 * stride + 128);
    const int v0 = lane * 16;
    const int v1 = (8 * (w & 1) + (lane >> 3)) * stride + ((lane & 7) ^ ((((lane >> 4) & 1) | ((w & 1) << 1)) << 1)) * 16;
    const int swz = ((n >> 3) | ((g & 1) << 1)), rowo = (8 * g + (n >> 2)) * 128 + (n & 3) * 8;
    const int v2 = (g1 ? 0 : (w >> 1) * 8192) + rowo + (((g1 ? w : 2 * (w & 1)) ^ swz) * 32);
    const int v3 = (g1 ? 8192 : (w >> 1) * 8192) + rowo + (((g1 ? w : 2 * (w & 1) + 1) ^ swz) * 32);
    const int v4 = (w >> 1) * 65536 + (w & 1) * 16384 + lane * 16, ks = (b - 96) * 2 + (w >> 1);
    int v5, ostr, od;
    if (g1) { v5 = (g & 1) * 262144 + (ks >> 3) * 16384 + (ks & 7) * 1024 + (2 * (w & 1) + (g >> 1)) * 256 + n * 16; ostr = 19; od = 0; }
    else if (kind == 2) { v5 = 32768 + w * 2048 + lane * 16; ostr = 16; od = 1024; }
    else { v5 = kind * 16384 + w * 1024 + lane * 16; ostr = 16; od = 4096; }
    const int v6 = (w == 0 && lane == 0) ? WS_CT + (g1 ? CT_G1 + (b - 96) * 32 : CT_P1 + h * 32 + kind * 4) * 4 : OOB;
#define P1_IN "{v0}"(v0), "{v1}"(v1), "{v2}"(v2), "{v3}"(v3), "{v4}"(v4), "{s[36:39]}"(rs), "{s[40:43]}"(rw), "{s44}"(16 * stride), "{s46}"(s0), "{s47}"(s1), "{s48}"(w), "{s49}"(ostr), "{s50}"(od), "{s45}"(WS_XF + fp), "{s64}"(g1 ? WS_HF + fp : tkbase(p, h, fp)), "{s51}"(rfl(kind == 1 ? 0x3e0293ee : 0x3f800000))
    int a5 = v5, a6 = v6; u32 tc, tr;
    if (g1) asm volatile(ASM_P1_G1 : "+{v5}"(a5), "+{v6}"(a6), "={s70}"(tc), "={s72}"(tr) : P1_IN : CLOB_P1);
    else if (kind == 2) asm volatile(ASM_P1_V : "+{v5}"(a5), "+{v6}"(a6), "={s70}"(tc), "={s72}"(tr) : P1_IN : CLOB_P1);
    else asm volatile(ASM_P1_QK : "+{v5}"(a5), "+{v6}"(a6), "={s70}"(tc), "={s72}"(tr) : P1_IN : CLOB_P1);
}

__device__ __forceinline__ int mlane(int n, int g) { return (g >> 1) * 512 + (n >> 3) * 256 + (g & 1) * 128 + (n & 7) * 16; }
__device__ __forceinline__ int mq(int j) { return ((j & 1) ? 0x4800 : 0) ^ ((j & 2) ? 0x800 : 0); }
__device__ __forceinline__ int hword(int i, int cls) { return WS_M + (cls ^ 0x1000 ^ ((i & 32) ? 0x4800 : 0)) + ((i >> 4) & 1) * 32768 + (i & 15) * 128; }
__device__ __forceinline__ int oblk(const Params& p, int rg, int q, int cls) { return rg * 32768 + q * 8192; }
__device__ __forceinline__ int mpiece(int l, int cls) { return WS_M + (cls ^ mq(l >> 4)) + ((l >> 3) & 1) * 32768 + (l & 7) * 256; }
__device__ __forceinline__ void qk_role(const Params& p, int h, int par, int rot) {
    const int lane = __builtin_amdgcn_mbcnt_hi(~0u, __builtin_amdgcn_mbcnt_lo(~0u, 0u)), w = rfl(threadIdx.x >> 6), n = lane & 15, g = lane >> 4;
    const v4u rs = mksrd(p.ws, WS_END), rw = mksrd(V2 ? colsrc(p, 0, 2 * h + (w >> 1)) : colsrc(p, w >> 1, 2 * h + (w & 1)), 1023 * 8192 + 128);
    const int l16 = lane * 16, v3 = l16 + (V2 ? 0 : 112 * 1024), lex = V2 ? 40960 : 152 * 1024;
    int v1 = l16 + w * 4096, v2 = v1 + 65536;
    const int v4 = WS_M + (w >> 1) * 32768 + (w & 1) * 1024 + mlane(n, g);
    const int v5 = lex + (32 * w + n) * 32 + (g ^ (n >> 2)) * 8;
    const int v6 = lex + (4 * g + (n >> 2)) * 32 + ((n & 3) ^ g) * 8;
    const int v15 = w == 0 ? min(l16, 64) : OOB;
    const int v7 = (OHACK ? (g & 1) : (n & 2)) == 0 ? WS_O + amap(oblk(p, n >> 3, h >> 3, par) + (h & 7) * 1024 + w * 256 + (g >> 1) * 128 + ((n >> 2) & 1) * 64 + (OHACK ? (n & 2) * 16 : (g & 1) * 32) + (n & 1) * 16, par) : OOB;
    const int v8 = WS_V + amap(h * 2048 + w * 512 + lane * 4, par);
    const int v9 = (w == 0 && lane < 32) ? hword(lane, par) : OOB;
    const int v10 = (w == 0 && lane == 0) ? 0 : OOB;
    const int v11 = 49152 + (w & 1) * 4096 + w * 1024 + l16;
    const int v12 = (n & 2) ? 0xEEEE : 0x4444;
    const int v14 = 16 * (w & 1) + n - 4 * g;
    const int r8 = lane >> 3;
    int v32 = r8 * 8192 + ((lane & 7) ^ (((r8 >> 1) & 1) << 1)) * 16, v33 = (8 + r8) * 8192 + ((lane & 7) ^ ((((r8 >> 1) & 1) | 2) << 1)) * 16;
    const int swz = (n >> 3) | ((g & 1) << 1), rowo = (w >> 1) * 4096 + (8 * g + (n >> 2)) * 128 + (n & 3) * 8;
    const int v34 = rowo + ((2 * (w & 1)) ^ swz) * 32, v35 = rowo + ((2 * (w & 1) + 1) ^ swz) * 32;
    if (V2) { v1 = (w & 1) ? v33 : v32; v2 = v34 + (w >> 1) * 4096; v32 = v35 + (w >> 1) * 4096; }
    asm volatile(ASM_QK :: "{v0}"(mpiece(lane, par)), "{v1}"(v1), "{v[2:3]}"(v2u{(u32)v2, (u32)v3}), "{v[4:7]}"(v4u{(u32)v4, (u32)v5, (u32)v6, (u32)v7}), "{v8}"(v8), "{v9}"(v9), "{v10}"(v10), "{v11}"(v11), "{v12}"(v12),
        "{v14}"(v14), "{v15}"(v15), "{v[32:35]}"(v4u{(u32)v32, (u32)v33, (u32)v34, (u32)v35}), "{s[36:39]}"(rs), "{s[40:43]}"(rw), "{s[44:45]}"(v2u{(u32)TUNEV(0, C_MO), (u32)TUNEV(1, C_MM)}), "{s46}"(TUNEV(2, C_X)), "{s17}"(TUNEV(3, C_Y)), "{s[12:15]}"(v4u{(u32)TUNEV(7, C_D0), (u32)C_Y0, (u32)TUNEV(4, C_DS), (u32)rot << 21}), "{s10}"(rfl(par ^ mq(rot))), "{s47}"(WS_DBG + 131072 + (h * 4 + w) * 2048), "{s48}"(w),
        "{s63}"(rfl(tkbase(p, h, (par & 8192) ^ FARP))), "{s80}"(WS_CT + (CT_P1 + h * 32) * 4), "{s87}"(WS_CT + (CT_AT + h * 32) * 4), "{s94}"(rfl(hword(h, par))) : "v255", "a255", "s99", "memory");
    __builtin_unreachable();
}
__device__ __forceinline__ void vo_role(const Params& p, int c, int par) {
    const int lane = __builtin_amdgcn_mbcnt_hi(~0u, __builtin_amdgcn_mbcnt_lo(~0u, 0u)), w = rfl(threadIdx.x >> 6), n = lane & 15, g = lane >> 4;
    const int rg = RGX ? c >> 5 : (c >> 4) & 1, cs = RGX ? c & 31 : (c & 15) + 16 * (c >> 5);
    const v4u rs = mksrd(p.ws, WS_END), rw = mksrd(colsrc(p, V2 ? 1 : 2, c) + (size_t)(256 * w) * 4096, 255 * 8192 + 128), rv = mksrd(colsrc(p, 2, c) + (size_t)(256 * w) * 4096, 255 * 8192 + 128), ro = mksrd(colsrc(p, 3, cs >> 1) + (size_t)(1024 * w) * 1024 + (cs & 1) * 32, 1023 * 2048 + 64), rm = mksrd(p.mem0, 16384);
    const int v0 = lane * 16, v1 = (lane >> 3) * 8192 + (lane & 7) * 16, v2 = (lane >> 2) * 2048 + (lane & 3) * 16;
    const int v3 = (8 * g + (n >> 2)) * 128 + w * 32 + (n & 3) * 8;
    const int v4 = w * (V2 ? 32768 : 40960) + (16 * (g & 1) + 8 * (g >> 1) + 4 * (n >> 3) + ((n >> 2) & 1)) * 64 + (n & 3) * 8;
    const int v5 = WS_M + (w >> 1) * 32768 + (w & 1) * 1024 + mlane(n, g);
    const int v6 = WS_O + amap(oblk(p, rg, w, par) + (2 * (n >> 3) + (g >> 1)) * 256 + (g & 1) * 128 + (n & 7) * 16, par);
    const int v7 = WS_V + amap(c * 1024 + w * 256 + lane * 4, par);
    const int v8 = (w == 0 && (lane & 3) == 0) ? WS_M + (par ^ mq(cs >> 3)) + ((cs >> 2) & 1) * 32768 + ((cs & 3) * 2 + rg) * 256 + lane * 4 : OOB;
    const int v9 = (w == 0 && lane < 32) ? hword(lane, par) : OOB;
    const int v12 = (n & 2) ? 0xEEEE : 0x4444;
    const int v13 = (4 * rg + 2 * ((lane >> 4) & 1) + ((lane >> 2) & 1)) * 2048 + (32 * cs + 16 * (lane >> 5) + 2 * ((lane >> 3) & 1)) * 2;
    const int v15 = (V2 ? 131072 : 0) + 16384 + (g * 16 + ((n >> 1) & 1) * 8 + (n >> 2)) * 32 + w * 8 + (n & 1) * 4;
    asm volatile(ASM_VO :: "{v10}"(mpiece(lane, par)), "{v0}"(v0), "{v1}"(v1), "{v2}"(v2), "{v3}"(v3), "{v4}"(v4), "{v5}"(v5), "{v6}"(v6), "{v7}"(v7), "{v8}"(v8), "{v9}"(v9), "{v12}"(v12),
        "{v13}"(v13), "{v15}"(v15), "{v28}"((w == 0 && lane == 0) ? WS_CT + (CT_P1 + (c >> 1) * 32 + 12 + (c & 1) * 4) * 4 : OOB), "{s[36:39]}"(rs), "{s[40:43]}"(rw), "{s[64:67]}"(ro), "{s[68:71]}"(rm), "{s[92:95]}"(rv), "{s[44:45]}"(v2u{(u32)TUNEV(0, C_MO), (u32)TUNEV(1, C_MM)}), "{s46}"(TUNEV(12, C_VD)), "{s17}"(TUNEV(3, C_Y)),
        "{s63}"(par), "{s74}"(cs >= 16 ? TUNEV(15, C_YL) : 0), "{s47}"(WS_DBG + 131072 + ((32 + c) * 4 + w) * 2048), "{s48}"(w) : "v255", "a255", "s99", "memory");
    __builtin_unreachable();
}
__device__ __forceinline__ void plan(const Params& p, u32 t0) {
    GAS u32* ct = (GAS u32*)(p.ws + WS_CT);
    const int wd[5] = {CT_P1 + 8 * 32 + 8, CT_P1 + 8 * 32, CT_P1 + 8 * 32 + 4, CT_G1 + 12 * 32, CT_G1 + 44 * 32};
    const float t = (float)((u32)__builtin_amdgcn_s_memrealtime() - t0) * 0.01f, r0 = (float)TUNEV(10, S64_R0);
    float f[5], v[5];
#pragma unroll
    for (int i = 0; i < 5; ++i) {
        const float s = (float)__hip_atomic_load(ct + wd[i], RLXA) + (i < 3 ? 1.4f : 2.f), d = fmaxf(t - (i < 3 ? 13.f : 17.f), 1.f);
        v[i] = s / d; f[i] = t + (64.f - s) / v[i] + 9.f;
    }
    { const float m = 0.25f * (v[1] + v[2] + v[3] + v[4]);
#pragma unroll
      for (int i = 0; i < 5; ++i) v[i] = 0.4f * v[i] + 0.6f * m; }
    const float cw = 0.01f * (float)TUNEV(14, 92), cg = 0.97f;
    const float w4 = (128.f / cw + 2.f * (v[0] * f[0] + v[1] * f[1])) / (2.f * (v[0] + v[1])), w5 = (128.f / cw + 2.f * (v[0] * f[0] + v[1] * f[1]) + v[2] * f[2]) / (2.f * (v[0] + v[1]) + v[2]);
    const float g6 = (128.f / cg + 2.f * (v[2] * f[2] + v[3] * f[3] + v[4] * f[4])) / (2.f * (v[2] + v[3] + v[4])), g5 = (128.f / cg + v[2] * f[2] + 2.f * (v[3] * f[3] + v[4] * f[4])) / (v[2] + 2.f * (v[3] + v[4]));
    const int force = TUNEV(6, 0);
    const bool five = force ? force == 5 : fmaxf(w5, g5) < fmaxf(w4, g6);
    const int nw = five ? 5 : 4, st = five ? 10 : 4;
    const float tw = five ? w5 : w4, tg = five ? g5 : g6, late = (128.f - r0) / (float)(10 - nw);
    const int ha = 126 % st + st * min(max((int)((2.f * cw * ((tw - f[0]) * v[0] - (tw - f[1]) * v[1]) - (float)(126 % st)) / (float)st + 0.5f), 0), 40 / st);
    const int na = min(max((int)((cg * (tg - f[2]) * v[2] - late) * (five ? 0.5f : 1.f) + 0.5f), 4), 28), nb = min(max((int)(cg * (tg - f[3]) * v[3] - late + 0.5f), 4), (int)r0 / 2 - na - 4);
    if ((threadIdx.x & 63) == 0) __hip_atomic_store(ct + CT_PLAN, 1u | (nw << 1) | (ha << 8) | (na << 16) | ((na + nb) << 24), RLXA);
}
__device__ __forceinline__ u32 await(const Params& p, int word, u32 mask, u32 least) {
    GAS u32* ct = (GAS u32*)(p.ws + WS_CT);
    u32 x;
    for (u32 spins = 0; ((x = rfl(__hip_atomic_load(ct + word, RLXA))) & mask) < least; ++spins) {
        if (spins > (1u << 18)) { __hip_atomic_store(ct + CT_TMO, 1u, RLXA); return 1u | (4 << 1) | (2 << 8) | (16 << 16) | (32 << 24); }
        __builtin_amdgcn_s_sleep(8);
    }
    CFENCE();
    return x;
}
__device__ __forceinline__ void copy_wd(const Params& p, int u, int fp, u32 t0) {
    const int lane = threadIdx.x & 63;
    const rsrc_t rs = __builtin_amdgcn_make_buffer_rsrc(p.ws + WS_WD, 0, 16 * MB, 0x00020000);
#pragma unroll 1
    for (int k = 0; k < 14; ++k) {
        if (u == 0 && k == 6) plan(p, t0);
        const int row = (k * 32 + u) * 8;
        v4u t[16];
#pragma unroll
        for (int i = 0; i < 16; ++i) t[i] = __builtin_nontemporal_load((const v4u*)(p.wd + (size_t)row * 1024 + i * 512 + lane * 8));
#pragma unroll
        for (int i = 0; i < 16; ++i) st128s(t[i], rs, farx(row * 4096 + fp) + (i >> 3) * 16384 + ((i >> 1) & 3) * 2048 + ((((i & 1) * 8 + (lane >> 3) + (i >> 1)) & 15) * 128) + (lane & 7) * 16);
    }
    WAIT_V0();
    if (lane == 0) __hip_atomic_fetch_add((GAS u32*)(p.ws + WS_CT) + CT_WD, 1u, RLXA);
}
__device__ __forceinline__ void s64_role(const Params& p, int rep, int cb, int id, int w, int fp) {
    const int lane = __builtin_amdgcn_mbcnt_hi(~0u, __builtin_amdgcn_mbcnt_lo(~0u, 0u)), n = lane & 15, g = lane >> 4;

    const u32 pl = await(p, CT_PLAN, 1u, 1u);
    const int nw = (pl >> 1) & 7, kind = rep >= nw, r = kind ? rep - nw : rep;
    if (kind) await(p, CT_WD, 255u, 32u);
    const v4u rs = mksrd(p.ws, WS_END), rw = mksrd(kind ? p.ws + WS_WD : (const u8*)(colsrc(p, 3, cb) + (size_t)(1024 * w) * 1024), kind ? 16 * MB : 1023 * 2048 + 128), rx = mksrd(p.x, 2048 * 2048), ro = mksrd(p.out, 2048 * 2048);
    const int v2 = lane * 16 + w * 32768, v0 = (kind ? (lane >> 5) * 16384 + ((lane >> 3) & 3) * 2048 + ((cb + (lane >> 3)) & 15) * 128 : (lane >> 3) * 2048) + (lane & 7) * 16;
    const int v1 = w * 32768 + ((kind ? 8 : 4) * g + (n >> 2)) * 128 + (n & 3) * 8;
    const int v3 = (kind ? 0 : 49152) + lane * 16;
    const int v4 = WS_CT + (kind ? CT_G1 + (14 * w + min(n, 13)) * 32 : CT_AT + (8 * w + (n & 7)) * 32) * 4;
    const int v5 = (4 * w + g) * 2048 + cb * 128 + n * 8, v6 = (cb >> 2) * 16384 + (cb & 3) * 2048 + w * 512 + lane * 8;
    const int v7 = 131072 + w * 4096 + g * 256 + (n ^ g) * 16, v8 = 131072 + n * 256 + ((4 * w + g) ^ n) * 16;
    const int r0 = TUNEV(10, S64_R0), nr = kind ? 10 - nw : nw;
    int ha, nt, first, rt, n1, lim, st = 2;
    if (kind) {
        const int na = (pl >> 16) & 255, nb = pl >> 24, lo = rep < 6 ? 0 : (rep < 8 ? na : nb), hi = rep < 6 ? na : (rep < 8 ? nb : r0 / 2);
        if (rep < 6 && nw == 5) st = 1;
        ha = r0; first = 2 * lo + (st == 2 ? rep & 1 : 0); lim = 2 * hi; rt = r0 + r; n1 = (128 - rt + nr - 1) / nr; nt = n1 + (hi - lo) * (3 - st);
    } else {
        ha = (pl >> 8) & 255;
        const int hd = r < 2 ? ha / 2 : 0;
        first = rt = r < 2 ? r : ha + r; lim = r0; n1 = hd + (r0 - ha - r + nr - 1) / nr; nt = hd + (128 - ha - r + nr - 1) / nr;
    }
#define S64_IO "{s97}"(896 * 4096 * w + fp), "{s78}"(WS_GT + fp), "{v7}"(v7), "{v8}"(v8), "{s44}"(rt), "{v0}"(v0), "{v1}"(v1), "{v2}"(v2), "{v3}"(v3), "{v4}"(v4), "{v5}"(v5), "{v6}"(v6), "{s[36:39]}"(rs), "{s[40:43]}"(rw), "{s[64:67]}"(rx), "{s[68:71]}"(ro), \
        "{s45}"(st), "{s47}"(nr), "{s50}"(ha), "{s46}"(nt), "{s48}"(w), "{s49}"(WS_CT + CT_S * 4 + (cb * 4 + w) * 128), "{s94}"(first), "{s95}"(n1), "{s96}"(lim), "{s51}"(w ? OOB : WS_DBG + id * 128), "{s99}"(kind == 0 ? TUNEV(9, C_PW3) : 0), "{s85}"(kind ? WS_HF + fp : tkbase(p, 8 * w, fp))
    if (kind) asm volatile(ASM_S64_G2 :: S64_IO : "v255", "a255", "memory");
    else asm volatile(ASM_S64_WO :: S64_IO : "v255", "a255", "memory");
    __builtin_unreachable();
}
#define MAIN_BODY \
int gx = (id >> 5) - 4, gr = id & 31; \
    if (id < 128) { \
        const int x = id >> 5, t = id & 31; \
        const int nq = x < 2 ? NQ0 : 32 - 2 * NQ0; \
        if (x < 3) { if (t < nq) qk_role(p, NQ0 * x + t, par, x & 1); else vo_role(p, (32 - NQ0) * x + t - nq, par); } \
        gx = -1; \
        id = 96 + gr; \
        STAMP(0); \
    } \
    const int fp = (par & 8192) ^ FARP; \
    const int u = PMAP == 2 ? gx * 32 + gr - (gx ? 8 : 0) : gx * 30 + gr, sp = PMAP == 2 ? (gx == 0 ? gr - 24 : -1) : (gx >= 0 && gr >= 30 ? gx * 2 + gr - 30 : -1); \
    const int blk = gx < 0 ? 64 + gr : (u < 64 ? u : u + 32); \
    if (sp < 0) { \
        for (int i = blk >= 96 ? (TUNEV(5, C_GD) & 255) - ((blk & 7) == slowpos(p.wg) ? 6 : 0) : TUNEV(5, C_GD) >> 8; i > 0; --i) __builtin_amdgcn_s_sleep(32);     \
        p1_role(p, blk, 0, 64, id, wv_, fp); \
    } else { const u32 t0 = (u32)__builtin_amdgcn_s_memrealtime(); ln_role(p, sp, wv_, fp); copy_wd(p, sp * 4 + wv_, fp, t0); } \
    STAMP(2); \
    s64_role(p, 2 * gx + 2 + (gr >> 4), gr & 15, id, wv_, fp);
constexpr int LDS_BYTES = 160 * 1024;
__device__ __forceinline__ int take_role(const Params& p, LAS u32* L) {
    if (threadIdx.x == 0) {
        GAS u32* tk = (GAS u32*)(p.ws + WS_CT) + CT_TICK;
        const u32 x = SCRAMBLE == 1 ? (blockIdx.x * 37u >> 3) & 7u : (SCRAMBLE == 2 ? (xcc_id() & 3u) : xcc_id() & 7u);
        const u32 t = __hip_atomic_fetch_add(tk + 64 * x, 1u, RLXA);
        u32 id = 32 * x + t;
        if (t >= 32u) {
            u32 o = __hip_atomic_fetch_add(tk + 64 * 8, 1u, RLXA);
            for (u32 spins = 0; spins < (1u << 20); ++spins) {
                u32 tot = 0;
                for (int k = 0; k < 8; ++k) tot += __hip_atomic_load(tk + 64 * k, RLXA);
                if (tot >= 256u) break;
                __builtin_amdgcn_s_sleep(8);
            }
            id = 255u;
            for (int k = 0; k < 8; ++k) {
                const u32 ck = __hip_atomic_load(tk + 64 * k, RLXA);
                const u32 fr = ck < 32u ? 32u - ck : 0u;
                if (o < fr) { id = 32 * k + ck + o; break; }
                o -= fr;
            }
        }
        *L = id;
    }
    BAR();
    const int id = rfl(*L);
    BAR();
    return id;
}
#define STAMP(i) do {} while (0)
__global__ void __launch_bounds__(256, 1) k_main(Params p) {
    extern __shared__ __attribute__((aligned(16))) u8 smem[];
    int par = rfl(*(const GAS u32*)(p.ws + WS_CT + CT_PAR * 4)) & 1;
    par = (par << 13) | ((TUNEV(13, 0) & 3) << 11) | ((TUNEV(13, 0) & 4) << 12);
    int id = take_role(p, (LAS u32*)smem);
    const int wv_ = rfl(threadIdx.x >> 6);
    STAMP(0);
    MAIN_BODY
    STAMP(7);
}
__global__ void __launch_bounds__(256) k_zero(Params p) {
    __shared__ u32 vote[4];
    u8* ws = p.ws;
    v4u z = {0u, 0u, 0u, 0u};
    if (WARM && blockIdx.x != 0 && blockIdx.x <= 256) {
        const int i = (blockIdx.x - 1) * 32 + (threadIdx.x >> 3), m2 = min(i & 7, 2);
        const rsrc_t rc = __builtin_amdgcn_make_buffer_rsrc(ws + WS_WC, 0, 16 * MB, 0x00020000);
        v4u t[7];
#pragma unroll
        for (int m = 0; m < 4; ++m) t[m] = __builtin_nontemporal_load((const v4u*)(wmat(p, m) + (size_t)i * 512 + slowpos(wmat(p, m)) * 64 + (threadIdx.x & 7) * 8));
        if ((i & 7) < 3) t[4] = __builtin_nontemporal_load((const v4u*)(wmat(p, m2) + (size_t)(i >> 3) * 4096 + slowpos2(wmat(p, m2)) * 64 + (threadIdx.x & 7) * 8));
        if (WARM > 1 && i < 7168) {
            t[5] = __builtin_nontemporal_load((const v4u*)(p.wg + (size_t)i * 512 + slowpos(p.wg) * 64 + (threadIdx.x & 7) * 8));
            t[6] = __builtin_nontemporal_load((const v4u*)(p.wu + (size_t)i * 512 + slowpos(p.wu) * 64 + (threadIdx.x & 7) * 8));
        }
#pragma unroll
        for (int m = 0; m < 4; ++m) __builtin_amdgcn_raw_buffer_store_b128(t[m], rc, i * 1024 + (m == 3 ? 4 : m) * 128 + (threadIdx.x & 7) * 16, 0, ZAUX);
        if ((i & 7) < 3) __builtin_amdgcn_raw_buffer_store_b128(t[4], rc, (i >> 3) * 8192 + (5 + m2) * 128 + (threadIdx.x & 7) * 16, 0, ZAUX);
        if (WARM > 1 && i < 7168) {
            __builtin_amdgcn_raw_buffer_store_b128(t[5], rc, WS_WG - WS_WC + i * 1024 + (threadIdx.x & 7) * 16, 0, ZAUX);
            __builtin_amdgcn_raw_buffer_store_b128(t[6], rc, WS_WG - WS_WC + i * 1024 + 128 + (threadIdx.x & 7) * 16, 0, ZAUX);
        }
    }
    if (blockIdx.x == 0) {
        const rsrc_t rs = __builtin_amdgcn_make_buffer_rsrc(ws, 0, 2 * MB, 0x00020000);
        const int lane = threadIdx.x & 63, w = threadIdx.x >> 6, base = 2 * MB - 32768 + (w & 1) * 16384 + (w >> 1) * 2048 + (lane & 31) * 4;
        st32s(0u, rs, base + 1024); st32s(0u, rs, base + 8192 + 1024);
        u32 a = 65535u, b = 65535u;
#pragma unroll 1
        for (int k = 0; k < ZR; ++k) {
            u64 t0, t1, t2;
            asm volatile("s_waitcnt vmcnt(0)\n\ts_memtime %0\n\ts_waitcnt lgkmcnt(0)" : "=s"(t0) :: "memory");
            st32s(0u, rs, base);
            asm volatile("s_waitcnt vmcnt(0)\n\ts_memtime %0\n\ts_waitcnt lgkmcnt(0)" : "=s"(t1) :: "memory");
            st32s(0u, rs, base + 8192);
            asm volatile("s_waitcnt vmcnt(0)\n\ts_memtime %0\n\ts_waitcnt lgkmcnt(0)" : "=s"(t2) :: "memory");
            a = min(a, (u32)(t1 - t0)); b = min(b, (u32)(t2 - t1));
            if (4 * max(a, b) > 5 * min(a, b) && min(a, b) < 600u && max(a, b) < 1000u) break;
        }
        vote[w] = (a << 16) | b;
        __syncthreads();
        if (threadIdx.x == 0) {
            for (int k = 0; k < 4; ++k) { a = min(a, vote[k] >> 16); b = min(b, vote[k] & 65535u); }
            z.x = (b < a ? 1u : 0u) ^ (xcc_id() >= 4 ? 1u : 0u);
        }
    }
    if (blockIdx.x < 80) ((v4u*)(ws + (blockIdx.x < 16 ? 0 : WS_M - 65536)))[blockIdx.x * 256 + threadIdx.x] = z;
}

extern "C" void kernel_launch(void* const* d_in, const int* in_sizes, int n_in, void* d_out, int out_size, void* d_ws, size_t ws_size, hipStream_t stream) {
    Params p;
    p.x = (const u16*)d_in[0]; p.mem0 = (const u16*)d_in[1]; p.wq = (const u16*)d_in[2]; p.wk = (const u16*)d_in[3]; p.wv = (const u16*)d_in[4]; p.wo = (const u16*)d_in[5];
    p.wg = (const u16*)d_in[8]; p.wu = (const u16*)d_in[9]; p.wd = (const u16*)d_in[10];
    p.out = (u16*)d_out;
    p.ws = (u8*)(((uintptr_t)d_ws + (2u * MB - 1)) & ~(uintptr_t)(2u * MB - 1));
    (void)hipFuncSetAttribute((const void*)k_main, hipFuncAttributeMaxDynamicSharedMemorySize, LDS_BYTES);
    hipLaunchKernelGGL(k_zero, dim3(CODEPF ? 263 : 257), dim3(256), 0, stream, p);
    hipLaunchKernelGGL(k_main, dim3(256), dim3(256), LDS_BYTES, stream, p);
}
```
